```python
import torch

def _softmax(x, axis=-1):
    return torch.softmax(x, dim=axis)
def _gelu(x):
    return torch.nn.functional.gelu(x, approximate="tanh")
def _silu(x):
    return torch.nn.functional.silu(x)
def _sigmoid(x):
    return torch.sigmoid(x)
def _layernorm(x, axis=-1, eps=1e-5):
    m = x.mean(dim=axis, keepdim=True)
    v = x.var(dim=axis, keepdim=True, unbiased=False)
    return (x - m) / torch.sqrt(v + eps)
def _scatter_add(tgt, idx, src, axis=0):
    return tgt.clone().index_add_(axis, idx, src)
def _segment_sum(src, seg, n):
    out = torch.zeros((n,) + src.shape[1:], dtype=src.dtype)
    return out.index_add_(0, seg, src)
def _pad(x, pw):
    flat = tuple(v for pair in reversed(pw) for v in pair)
    return torch.nn.functional.pad(x, flat)

kernel_name = 'concat_heads__ssm_conv_h3__sliding_local_dilated__prompt'

def setup_inputs():

    _w = (lambda a: (lambda t: t.to(torch.bfloat16) if t.is_floating_point() else t)(torch.as_tensor(a)))
    def _A_setup():
        B, S, D = 1, 2048, 1024
        ks = 4
        return {
            "q": _w((lambda *s: torch.randn(*s, dtype=torch.bfloat16) * 0.05)(B, S, D)),
            "k": _w((lambda *s: torch.randn(*s, dtype=torch.bfloat16) * 1.6)(B, S, D)),
            "v": _w((lambda *s: torch.randn(*s, dtype=torch.bfloat16) * 0.8)(B, S, D)),
            "shift_k": _w((lambda *s: torch.randn(*s, dtype=torch.bfloat16) * 1.6)(D, ks) * 0.5),
            "ssm_k": _w((lambda *s: torch.randn(*s, dtype=torch.bfloat16) * 1.6)(D, S) * (1.0 / S ** 0.5)),
            "ssm_D": _w((lambda *s: torch.randn(*s, dtype=torch.bfloat16) * 0.8)(D)),
        }
    _A_in = _A_setup()

    def _B_setup():
        B, S, D = 1, 2048, 1024
        return {
            "x": _w((lambda *s: torch.randn(*s, dtype=torch.bfloat16) * 0.2)(B, S, D)),
            "wqkv": _w((lambda *s: torch.randn(*s, dtype=torch.bfloat16) * 0.2)(D, 3 * D)),
            "wo": _w((lambda *s: torch.randn(*s, dtype=torch.bfloat16) * 0.05)(D, D)),
        }
    _B_in = _B_setup()

    return {
        "A_q": _A_in["q"],
        "A_k": _A_in["k"],
        "A_v": _A_in["v"],
        "A_shift_k": _A_in["shift_k"],
        "A_ssm_k": _A_in["ssm_k"],
        "A_ssm_D": _A_in["ssm_D"],
        "B_x": _B_in["x"],
        "B_wqkv": _B_in["wqkv"],
        "B_wo": _B_in["wo"],
    }

def reference(A_q, A_k, A_v, A_shift_k, A_ssm_k, A_ssm_D, B_x, B_wqkv, B_wo):
    def _A_ref(q, k, v, shift_k, ssm_k, ssm_D):
        B, S, D = q.shape
        ks = shift_k.shape[-1]
        k_pad = _pad(k, ((0, 0), (ks - 1, 0), (0, 0)))
        k_shift = torch.zeros_like(k)
        for t in range(ks):
            k_shift = k_shift + k_pad[:, t:t + S, :] * shift_k[:, ks - 1 - t][None, None, :]
        kv = k_shift * v
        L = 2 * S
        kv_pad = _pad(kv, ((0, 0), (0, S), (0, 0)))
        kf_pad = _pad(ssm_k, ((0, 0), (0, S)))
        kv_f = (lambda x, axes=(-2,-1): torch.fft.rfft2(x.float(), dim=axes))(kv_pad[:, None, :, :], axes=(1, 2))
        kf_f = (lambda x, axes=(-2,-1): torch.fft.rfft2(x.float(), dim=axes))(kf_pad[:, None, :], axes=(1, 2))
        kf_f = (lambda x, *d: x.permute(*d))(kf_f, 1, 2, 0)
        y_f = kv_f * kf_f[None, :, :, :]
        y = (lambda x, s, axes=(-2,-1): torch.fft.irfft2(x, s=s, dim=axes).to(torch.bfloat16))(y_f, (1, L), axes=(1, 2))[:, 0, :S, :]
        y = y + kv * ssm_D[None, None, :]
        return q * y

    def _B_ref(x, wqkv, wo):
        B, S, D = x.shape
        H = 32; Dh = D // H
        W, dil = 8, 2
        qkv = (lambda x, *d: x.permute(*d))((x @ wqkv).reshape(B, S, 3, H, Dh), 2, 0, 3, 1, 4)
        q, k, v = qkv[0], qkv[1], qkv[2]
        scale = Dh ** -0.5
        pos = torch.arange(S)
        off = torch.arange(W) * dil
        idx = pos[:, None] - off[None, :]
        mask = idx >= 0
        idx_c = torch.where(mask, idx, 0).reshape(-1)
        kg = k[:, :, idx_c, :].reshape(B, H, S, W, Dh)
        vg = v[:, :, idx_c, :].reshape(B, H, S, W, Dh)
        logits = torch.einsum("bhsd,bhswd->bhsw", q, kg) * scale
        logits = torch.where(mask[None, None, :, :], logits, -1e9)
        attn = _softmax(logits, axis=-1)
        o = torch.einsum("bhsw,bhswd->bhsd", attn, vg)
        return (lambda x, *d: x.permute(*d))(o, 0, 2, 1, 3).reshape(B, S, D) @ wo

    a = _A_ref(A_q, A_k, A_v, A_shift_k, A_ssm_k, A_ssm_D)
    b = _B_ref(B_x, B_wqkv, B_wo)
    return torch.concatenate([a, b], axis=-1)
```

```cpp
#define LDPOL ""
#define OUTPOL " sc1 nt"
#include <hip/hip_runtime.h>
#include <hip/hip_bf16.h>
#include <stdint.h>

typedef unsigned short u16;
typedef short s16x4 __attribute__((ext_vector_type(4)));
typedef float f32x32 __attribute__((ext_vector_type(32)));
typedef unsigned u32x4 __attribute__((ext_vector_type(4)));
typedef unsigned u32x2 __attribute__((ext_vector_type(2)));
#define LDS_AS __attribute__((address_space(3)))

__device__ __forceinline__ float bflo(unsigned x){ return __uint_as_float(x<<16); }
__device__ __forceinline__ float bfhi(unsigned x){ return __uint_as_float(x&0xffff0000u); }
typedef float f32x2 __attribute__((ext_vector_type(2)));
typedef __bf16 bf16x2_t __attribute__((ext_vector_type(2)));
__device__ __forceinline__ unsigned pack2(float lo, float hi){ return __builtin_bit_cast(unsigned, __builtin_convertvector(f32x2{lo,hi}, bf16x2_t)); }

#define GLDS(name,pol) __device__ __forceinline__ void name(const void* sb, unsigned voff, unsigned ldsb, int imm){ \
  asm volatile("s_add_u32 m0, %2, %3\n\ts_nop 0\n\tglobal_load_lds_dwordx4 %0, %1" pol :: "v"(voff), "s"(sb), "s"(ldsb), "n"(imm) : "memory", "scc"); }
GLDS(glds,"") GLDS(gldsn,LDPOL) GLDS(gldsc," sc1")
#define PACE 63
__device__ __forceinline__ void gldsp(const void* sb, unsigned voff, unsigned ldsb, int imm){
  asm volatile("s_add_u32 m0, %2, %3\n\ts_waitcnt vmcnt(%4)\n\tglobal_load_lds_dwordx4 %0, %1" :: "v"(voff), "s"(sb), "s"(ldsb), "n"(imm), "n"(PACE) : "memory", "scc"); }
__device__ __forceinline__ void touch(const void* sb, unsigned voff, unsigned long long mask){
  asm volatile("s_mov_b64 exec, %2\n\tglobal_load_dword v255, %0, %1\n\ts_mov_b64 exec, -1" :: "v"(voff), "s"(sb), "s"(mask) : "memory", "v255"); }
__device__ __forceinline__ void st16c(void* sb, unsigned vo, u32x4 v){ asm volatile("global_store_dwordx4 %0, %1, %2 sc1\n\ts_nop 1" :: "v"(vo), "v"(v), "s"(sb) : "memory"); }
__device__ __forceinline__ void stos(void* sb, unsigned vo, u32x4 v){ asm volatile("global_store_dwordx4 %0, %1, %2" OUTPOL "\n\ts_nop 1" :: "v"(vo), "v"(v), "s"(sb) : "memory"); }
__device__ __forceinline__ void stoa(void* p, u32x4 v){ asm volatile("global_store_dwordx4 %0, %1, off" OUTPOL "\n\ts_nop 1" :: "v"(p), "v"(v) : "memory"); }
__device__ __forceinline__ void st4c(void* p, unsigned v){ asm volatile("global_store_dword %0, %1, off sc1" :: "v"(p), "v"(v) : "memory"); }
__device__ __forceinline__ void wait_flag(const unsigned* p, unsigned tag){
  int spins=0;
  while(true){
    unsigned v=__hip_atomic_load(p, __ATOMIC_RELAXED, __HIP_MEMORY_SCOPE_AGENT);
    if(__all(v==tag)) break;
    __builtin_amdgcn_s_sleep(2);
    if(++spins>(1<<22)) break;
  }
}
#define WAITV(n) asm volatile("s_waitcnt vmcnt(%0)"::"n"(n):"memory")
#define WAITL() asm volatile("s_waitcnt lgkmcnt(0)":::"memory")
#define BAR() do{ asm volatile("s_waitcnt lgkmcnt(0)":::"memory"); __builtin_amdgcn_s_barrier(); asm volatile("":::"memory"); }while(0)
#define LANES() int l; asm volatile("v_mbcnt_lo_u32_b32 %0, -1, 0\n\tv_mbcnt_hi_u32_b32 %0, -1, %0" : "=v"(l)); __builtin_assume(l>=0 && l<64); const int g=l>>4, fr=l&15, tq=(l&15)>>2, tp=l&3; (void)g; (void)fr; (void)tq; (void)tp;

__device__ __forceinline__ s16x4 trread(const char* p){ return __builtin_amdgcn_ds_read_tr16_b64_v4i16((LDS_AS s16x4*)p); }
__device__ __forceinline__ int wswz(int row){ return ((row>>1)&1) | (((row>>3)&1)<<1); }
__device__ __forceinline__ u32x4 widen(u32x2 a, u32x2 b){
  auto r0=__builtin_amdgcn_permlane16_swap(a[0],b[0],false,false);
  auto r1=__builtin_amdgcn_permlane16_swap(a[1],b[1],false,false);
  return u32x4{r0[0],r1[0],r0[1],r1[1]};
}

constexpr int S=2048, DM=1024;
constexpr int K1_XB=144*128, K1_WB=3*64*128, K1_ST=K1_XB+K1_WB;
constexpr int AUX=3*K1_ST, KVP_=1040;
constexpr int WOB=22528, CV_W=8448, CV_CP=4224;
constexpr int CT_OFF=AUX+33*KVP_;
constexpr int ALL_LDS=CT_OFF+176+96;
constexpr int FL_KV=1024, FL_O=FL_KV+8192, FL_Y=FL_O+8192;

#define CPRIO 2
#define CODE_ST 1590
#define LEAD 3
#define HP3 3
#define HP2 3
#define HP4 0
#define QP 1
#define TNX 1
#define HWS 99
#define HP1 0
#define HUNR 15
#define W0 6
#define OPD 8
#define WT0 99
#define W1 4
#define W2 4
#define W3 4
#define OPOLLS 2
#define POLLS 3
#define HPRE 0
#define CPS2 99
#define KR ((((idx>>2)*7)>>2)+(((idx&3)*7)>>1))
#define NOY 0
#define YLDS 1
#define OLDS 0
#define KVSEL (rbb==3? 0 : idx%KSN)
#define KS0 1
#define KSN 3
#define KST 9
#define KVP 5
#define TGS 2
#define CPS 13
#define H0_ASM \
  "s_mul_i32 s76, s52, 0x60000\n\t" \
  "s_add_u32 s76, s42, s76\n\t" \
  "s_addc_u32 s77, s43, 0\n\t" \
  "s_add_u32 s82, s53, 86016\n\t" \
  "s_add_u32 m0, s82, 18432\n\t" \
  "s_lshl_b32 s78, s52, 7\n\t" \
  "global_load_lds_dwordx4 v0, s[76:77]\n\t" \
  "global_load_lds_dwordx4 v1, s[76:77] offset:1024\n\t" \
  "s_add_u32 s78, s40, s78\n\t" \
  "s_addc_u32 s79, s41, 0\n\t" \
  "s_add_u32 m0, s82, 2048\n\t" \
  "s_nop 0\n\t" \
  "global_load_lds_dwordx4 v6, s[78:79]\n\t" \
  "global_load_lds_dwordx4 v7, s[78:79] offset:1024\n\t" \
  "s_add_u32 m0, s82, 26624\n\t" \
  "s_nop 0\n\t" \
  "global_load_lds_dwordx4 v2, s[76:77]\n\t" \
  "global_load_lds_dwordx4 v3, s[76:77] offset:1024\n\t" \
  "s_add_u32 m0, s82, 10240\n\t" \
  "s_nop 0\n\t" \
  "global_load_lds_dwordx4 v8, s[78:79]\n\t" \
  "global_load_lds_dwordx4 v9, s[78:79] offset:1024\n\t" \
  "s_add_u32 m0, s82, 34816\n\t" \
  "s_nop 0\n\t" \
  "global_load_lds_dwordx4 v4, s[76:77]\n\t" \
  "global_load_lds_dwordx4 v5, s[76:77] offset:1024\n\t" \
  "s_cmp_gt_u32 s59, 1\n\t" \
  "s_cbranch_scc1 1f\n\t" \
  "s_lshr_b32 s83, s53, 1\n\t" \
  "s_add_u32 m0, s83, 86016\n\t" \
  "s_nop 0\n\t" \
  "global_load_lds_dwordx4 v10, s[78:79]\n\t" \
  "1:\n\t"
#define H1_ASM \
  "s_mov_b32 s55, 0\n\t" \
  "s_mov_b32 s54, 0\n\t" \
  "s_add_u32 s52, s52, 1\n\t" \
  "s_and_b32 s52, s52, 15\n\t" \
  "s_cmp_eq_u32 s58, 1\n\t" \
  "s_cbranch_scc1 1f\n\t" \
  "s_mul_i32 s76, s52, 0x60000\n\t" \
  "s_add_u32 s76, s42, s76\n\t" \
  "s_addc_u32 s77, s43, 0\n\t" \
  "s_lshl_b32 s78, s52, 7\n\t" \
  "s_add_u32 s78, s40, s78\n\t" \
  "s_addc_u32 s79, s41, 0\n\t" \
  "s_mul_i32 s80, s47, s46\n\t" \
  "s_add_u32 s80, s44, s80\n\t" \
  "s_addc_u32 s81, s45, 0\n\t" \
  "s_mov_b32 s82, s53\n\t" \
  "s_add_u32 m0, s82, 18432\n\t" \
  "s_add_u32 s52, s52, 1\n\t" \
  "global_load_lds_dwordx4 v0, s[76:77]\n\t" \
  "global_load_lds_dwordx4 v1, s[76:77] offset:1024\n\t" \
  "s_add_u32 m0, s82, 2048\n\t" \
  "s_and_b32 s52, s52, 15\n\t" \
  "global_load_lds_dwordx4 v6, s[78:79]\n\t" \
  "global_load_lds_dwordx4 v7, s[78:79] offset:1024\n\t" \
  "s_add_u32 m0, s82, 26624\n\t" \
  "s_add_u32 s47, s47, 1\n\t" \
  "global_load_lds_dwordx4 v2, s[76:77]\n\t" \
  "global_load_lds_dwordx4 v3, s[76:77] offset:1024\n\t" \
  "s_add_u32 m0, s82, 10240\n\t" \
  "s_and_b32 s47, s47, 15\n\t" \
  "global_load_lds_dwordx4 v8, s[78:79]\n\t" \
  "global_load_lds_dwordx4 v9, s[78:79] offset:1024\n\t" \
  "s_add_u32 m0, s82, 34816\n\t" \
  "s_and_b32 s83, s54, s51\n\t" \
  "global_load_lds_dwordx4 v4, s[76:77]\n\t" \
  "global_load_lds_dwordx4 v5, s[76:77] offset:1024\n\t" \
  "s_add_u32 m0, s83, s49\n\t" \
  "s_mov_b64 exec, s[50:51]\n\t" \
  "global_load_lds_dwordx4 v10, s[80:81]\n\t" \
  "s_mov_b64 exec, -1\n\t" \
  "s_mov_b32 s55, 1\n\t" \
  "s_mov_b32 s54, 43008\n\t" \
  "s_waitcnt vmcnt(11)\n\t" \
  "s_branch 2f\n\t" \
  "1:\n\t" \
  "s_mul_i32 s83, s59, 4160\n\t" \
  "v_add_u32 v20, 7152, v11\n\t" \
  "v_add_u32 v21, 14304, v11\n\t" \
  "v_add_u32 v22, 21456, v11\n\t" \
  "s_add_u32 m0, s83, 130064\n\t" \
  "s_nop 0\n\t" \
  "global_load_lds_dwordx4 v11, s[60:61]" LDPOL "\n\t" \
  "global_load_lds_dwordx4 v20, s[60:61] offset:1040" LDPOL "\n\t" \
  "global_load_lds_dwordx4 v21, s[60:61] offset:2080" LDPOL "\n\t" \
  "global_load_lds_dwordx4 v22, s[60:61] offset:3120" LDPOL "\n\t" \
  "s_add_u32 m0, s83, 146704\n\t" \
  "s_nop 0\n\t" \
  "global_load_lds_dwordx4 v11, s[62:63]" LDPOL "\n\t" \
  "global_load_lds_dwordx4 v20, s[62:63] offset:1040" LDPOL "\n\t" \
  "global_load_lds_dwordx4 v21, s[62:63] offset:2080" LDPOL "\n\t" \
  "global_load_lds_dwordx4 v22, s[62:63] offset:3120" LDPOL "\n\t" \
  "v_subrev_u32 v20, s64, v11\n\t" \
  "s_mov_b32 m0, s68\n\t" \
  "s_mov_b64 exec, s[66:67]\n\t" \
  "global_load_lds_dwordx4 v20, s[60:61]" LDPOL "\n\t" \
  "s_mov_b64 exec, -1\n\t" \
  "s_waitcnt vmcnt(9)\n\t" \
  "2:\n\t" \
  "s_waitcnt lgkmcnt(0)\n\t" \
  "s_barrier\n\t"
#define H_ASM \
  "s_mul_i32 s76, s52, 0x60000\n\t" \
  "s_add_u32 s76, s42, s76\n\t" \
  "s_addc_u32 s77, s43, 0\n\t" \
  "s_lshl_b32 s78, s52, 7\n\t" \
  "s_add_u32 s78, s40, s78\n\t" \
  "s_addc_u32 s79, s41, 0\n\t" \
  "s_mul_i32 s80, s47, s46\n\t" \
  "s_add_u32 s80, s44, s80\n\t" \
  "s_addc_u32 s81, s45, 0\n\t" \
  "s_add_u32 s82, s54, s53\n\t" \
  "s_cmp_eq_u32 s58, 2\n\t" \
  "s_cbranch_scc1 5f\n\t" \
  "s_branch 3f\n\t" \
  "1:\n\t" \
  "s_waitcnt vmcnt(11)\n\t" \
  "2:\n\t" \
  "s_barrier\n\t" \
  "3:\n\t" \
  "s_add_u32 s55, s55, 1\n\t" \
  "s_add_u32 m0, s82, 18432\n\t" \
  "s_add_u32 s52, s52, 1\n\t" \
  "global_load_lds_dwordx4 v0, s[76:77]\n\t" \
  "global_load_lds_dwordx4 v1, s[76:77] offset:1024\n\t" \
  "s_add_u32 m0, s82, 2048\n\t" \
  "s_and_b32 s52, s52, 15\n\t" \
  "global_load_lds_dwordx4 v6, s[78:79]\n\t" \
  "global_load_lds_dwordx4 v7, s[78:79] offset:1024\n\t" \
  "s_add_u32 m0, s82, 26624\n\t" \
  "s_add_u32 s47, s47, 1\n\t" \
  "global_load_lds_dwordx4 v2, s[76:77]\n\t" \
  "global_load_lds_dwordx4 v3, s[76:77] offset:1024\n\t" \
  "s_add_u32 m0, s82, 10240\n\t" \
  "s_and_b32 s47, s47, 15\n\t" \
  "global_load_lds_dwordx4 v8, s[78:79]\n\t" \
  "global_load_lds_dwordx4 v9, s[78:79] offset:1024\n\t" \
  "s_add_u32 m0, s82, 34816\n\t" \
  "s_and_b32 s83, s54, s51\n\t" \
  "global_load_lds_dwordx4 v4, s[76:77]\n\t" \
  "global_load_lds_dwordx4 v5, s[76:77] offset:1024\n\t" \
  "s_bitcmp1_b32 s57, s55\n\t" \
  "s_cbranch_scc1 10f\n\t" \
  "8:\n\t" \
  "s_add_u32 m0, s83, s49\n\t" \
  "s_mov_b64 exec, s[50:51]\n\t" \
  "global_load_lds_dwordx4 v10, s[80:81]\n\t" \
  "s_mov_b64 exec, -1\n\t" \
  "4:\n\t" \
  "s_mul_i32 s76, s52, 0x60000\n\t" \
  "s_add_u32 s76, s42, s76\n\t" \
  "s_addc_u32 s77, s43, 0\n\t" \
  "s_lshl_b32 s78, s52, 7\n\t" \
  "s_add_u32 s78, s40, s78\n\t" \
  "s_addc_u32 s79, s41, 0\n\t" \
  "s_mul_i32 s80, s47, s46\n\t" \
  "s_add_u32 s80, s44, s80\n\t" \
  "s_addc_u32 s81, s45, 0\n\t" \
  "s_add_u32 s54, s54, 43008\n\t" \
  "s_cmp_eq_u32 s54, 129024\n\t" \
  "s_cselect_b32 s54, 0, s54\n\t" \
  "s_add_u32 s82, s54, s53\n\t" \
  "s_bitcmp0_b32 s56, s55\n\t" \
  "s_cbranch_scc1 1b\n\t" \
  "s_cmp_eq_u32 s55, 15\n\t" \
  "s_cbranch_scc1 19f\n\t" \
  "s_cmp_eq_u32 s55, s58\n\t" \
  "s_cbranch_scc1 3b\n\t" \
  "s_waitcnt vmcnt(11)\n\t" \
  "s_barrier\n\t" \
  "5:\n\t" \
  "s_mul_i32 s83, s59, 4160\n\t" \
  "v_add_u32 v20, 7152, v11\n\t" \
  "v_add_u32 v21, 14304, v11\n\t" \
  "v_add_u32 v22, 21456, v11\n\t" \
  "s_add_u32 m0, s83, 130064\n\t" \
  "s_nop 0\n\t" \
  "global_load_lds_dwordx4 v11, s[60:61]" LDPOL "\n\t" \
  "global_load_lds_dwordx4 v20, s[60:61] offset:1040" LDPOL "\n\t" \
  "global_load_lds_dwordx4 v21, s[60:61] offset:2080" LDPOL "\n\t" \
  "global_load_lds_dwordx4 v22, s[60:61] offset:3120" LDPOL "\n\t" \
  "s_add_u32 m0, s83, 146704\n\t" \
  "s_nop 0\n\t" \
  "global_load_lds_dwordx4 v11, s[62:63]" LDPOL "\n\t" \
  "global_load_lds_dwordx4 v20, s[62:63] offset:1040" LDPOL "\n\t" \
  "global_load_lds_dwordx4 v21, s[62:63] offset:2080" LDPOL "\n\t" \
  "global_load_lds_dwordx4 v22, s[62:63] offset:3120" LDPOL "\n\t" \
  "v_subrev_u32 v20, s64, v11\n\t" \
  "s_mov_b32 m0, s68\n\t" \
  "s_mov_b64 exec, s[66:67]\n\t" \
  "global_load_lds_dwordx4 v20, s[60:61]" LDPOL "\n\t" \
  "s_mov_b64 exec, -1\n\t" \
  "s_waitcnt vmcnt(9)\n\t" \
  "s_branch 2b\n\t" \
  "10:\n\t" \
  "s_cmp_eq_u32 s59, 2\n\t" \
  "s_cbranch_scc0 11f\n\t" \
  "s_mov_b64 exec, 0xffff\n\t" \
  "global_load_dword v255, v14, s[72:73]\n\t" \
  "s_mov_b64 exec, -1\n\t" \
  "s_branch 4b\n\t" \
  "11:\n\t" \
  "s_cmp_eq_u32 s55, 2\n\t" \
  "s_cbranch_scc0 12f\n\t" \
  "global_load_dword v254, v14, s[70:71] sc1\n\t" \
  "s_branch 4b\n\t" \
  "12:\n\t" \
  "s_cmp_eq_u32 s55, 5\n\t" \
  "s_cbranch_scc0 13f\n\t" \
  "v_readfirstlane_b32 s84, v254\n\t" \
  "v_mov_b32 v253, 163448\n\t" \
  "s_nop 1\n\t" \
  "s_add_u32 s84, s84, 1\n\t" \
  "v_mov_b32 v254, s84\n\t" \
  "ds_write_b32 v253, v254\n\t" \
  "s_waitcnt lgkmcnt(0)\n\t" \
  "s_branch 8b\n\t" \
  "13:\n\t" \
  "s_cmp_eq_u32 s55, 14\n\t" \
  "s_cbranch_scc0 14f\n\t" \
  "global_load_dword v253, v16, s[70:71] sc1\n\t" \
  "s_branch 4b\n\t" \
  "14:\n\t" \
  "s_mov_b32 exec_hi, 0\n\t" \
  "global_load_dword v255, v10, s[44:45]\n\t" \
  "s_mov_b64 exec, -1\n\t" \
  "s_branch 4b\n\t" \
  "19:\n\t" \
  "s_waitcnt vmcnt(11)\n\t" \
  "s_cmp_lg_u32 s59, 3\n\t" \
  "s_cbranch_scc1 18f\n\t" \
  "v_cmp_ne_u32 vcc, v253, v254\n\t" \
  "v_mov_b32 v253, 163364\n\t" \
  "s_nop 1\n\t" \
  "s_cmp_eq_u64 vcc, 0\n\t" \
  "s_cselect_b32 s83, 1, 0\n\t" \
  "v_mov_b32 v254, s83\n\t" \
  "ds_write_b32 v253, v254\n\t" \
  "18:\n\t"
#define C_ASM \
  "s_branch 29f\n\t" \
  "28:\n\t" \
  "s_mov_b64 exec, 0\n\t" \
  "s_cmp_eq_u32 %13, 0\n\t" \
  "s_cbranch_scc1 500f\n\t" \
  "s_cmp_eq_u32 %13, 1\n\t" \
  "s_cbranch_scc1 501f\n\t" \
  "s_cmp_eq_u32 %13, 2\n\t" \
  "s_cbranch_scc1 502f\n\t" \
  "s_branch 503f\n\t" \
  "26:\n\t" \
  "s_mov_b64 exec, -1\n\t" \
  "s_add_u32 s88, s88, 4\n\t" \
  "s_addc_u32 s89, s89, 0\n\t" \
  "s_setpc_b64 s[88:89]\n\t" \
  ".p2align 12\n\t" \
  "29:\n\t" \
  "s_bitcmp0_b32 %13, 0\n\t" \
  "s_cbranch_scc1 8f\n\t" \
  "global_load_dwordx2 v[10:11], v51, %11\n\t" \
  "global_load_dwordx2 v[12:13], v51, %11 offset:128\n\t" \
  "v_xor_b32 v53, 64, v52\n\t" \
  "v_xor_b32 v55, 32, v54\n\t" \
  "v_add_u32 v56, 43008, v52\n\t" \
  "v_add_u32 v58, 43008, v54\n\t" \
  "v_xor_b32 v57, 64, v56\n\t" \
  "v_xor_b32 v59, 32, v58\n\t" \
  "v_add_u32 v60, 86016, v52\n\t" \
  "v_add_u32 v62, 86016, v54\n\t" \
  "v_xor_b32 v61, 64, v60\n\t" \
  "v_xor_b32 v63, 32, v62\n\t" \
  "s_mov_b32 vcc_hi, 0x05040100\n\t" \
  "s_waitcnt vmcnt(2)\n\t" \
  "s_barrier\n\t" \
  "ds_read_b64_tr_b16 v[92:93], v62 offset:26624\n\t" \
  "ds_read_b64_tr_b16 v[94:95], v62 offset:27136\n\t" \
  "ds_read_b128 v[64:67], v60 offset:10240\n\t" \
  "ds_read_b128 v[68:71], v60 offset:12288\n\t" \
  "ds_read_b128 v[72:75], v60 offset:14336\n\t" \
  "ds_read_b128 v[76:79], v60 offset:16384\n\t" \
  "ds_read_b64_tr_b16 v[96:97], v63 offset:26624\n\t" \
  "ds_read_b64_tr_b16 v[98:99], v63 offset:27136\n\t" \
  "ds_read_b64_tr_b16 v[100:101], v62 offset:34816\n\t" \
  "ds_read_b64_tr_b16 v[102:103], v62 offset:35328\n\t" \
  "ds_read_b128 v[80:83], v60 offset:0\n\t" \
  "ds_read_b64_tr_b16 v[104:105], v63 offset:34816\n\t" \
  "ds_read_b64_tr_b16 v[106:107], v63 offset:35328\n\t" \
  "ds_read_b64_tr_b16 v[84:85], v62 offset:18432\n\t" \
  "ds_read_b64_tr_b16 v[86:87], v62 offset:18944\n\t" \
  "ds_read_b64_tr_b16 v[88:89], v63 offset:18432\n\t" \
  "s_waitcnt lgkmcnt(13)\n\t" \
  "v_mfma_f32_16x16x32_bf16 v[184:187], v[92:95], v[64:67], 0\n\t" \
  "ds_read_b64_tr_b16 v[90:91], v63 offset:18944\n\t" \
  "ds_read_b64_tr_b16 v[136:137], v62 offset:30720\n\t" \
  "s_waitcnt lgkmcnt(14)\n\t" \
  "v_mfma_f32_16x16x32_bf16 v[192:195], v[92:95], v[68:71], 0\n\t" \
  "ds_read_b64_tr_b16 v[138:139], v62 offset:31232\n\t" \
  "ds_read_b128 v[108:111], v61 offset:10240\n\t" \
  "s_waitcnt lgkmcnt(15)\n\t" \
  "v_mfma_f32_16x16x32_bf16 v[200:203], v[92:95], v[72:75], 0\n\t" \
  "ds_read_b128 v[112:115], v61 offset:12288\n\t" \
  "ds_read_b128 v[116:119], v61 offset:14336\n\t" \
  "s_waitcnt lgkmcnt(15)\n\t" \
  "v_mfma_f32_16x16x32_bf16 v[208:211], v[92:95], v[76:79], 0\n\t" \
  "ds_read_b128 v[120:123], v61 offset:16384\n\t" \
  "ds_read_b64_tr_b16 v[140:141], v63 offset:30720\n\t" \
  "s_waitcnt lgkmcnt(15)\n\t" \
  "v_mfma_f32_16x16x32_bf16 v[188:191], v[96:99], v[64:67], 0\n\t" \
  "ds_read_b64_tr_b16 v[142:143], v63 offset:31232\n\t" \
  "ds_read_b64_tr_b16 v[144:145], v62 offset:38912\n\t" \
  "v_mfma_f32_16x16x32_bf16 v[196:199], v[96:99], v[68:71], 0\n\t" \
  "ds_read_b64_tr_b16 v[146:147], v62 offset:39424\n\t" \
  "ds_read_b128 v[124:127], v61 offset:0\n\t" \
  "v_mfma_f32_16x16x32_bf16 v[204:207], v[96:99], v[72:75], 0\n\t" \
  "ds_read_b64_tr_b16 v[148:149], v63 offset:38912\n\t" \
  "ds_read_b64_tr_b16 v[150:151], v63 offset:39424\n\t" \
  "v_mfma_f32_16x16x32_bf16 v[212:215], v[96:99], v[76:79], 0\n\t" \
  "ds_read_b64_tr_b16 v[128:129], v62 offset:22528\n\t" \
  "ds_read_b64_tr_b16 v[130:131], v62 offset:23040\n\t" \
  "s_waitcnt lgkmcnt(14)\n\t" \
  "v_mfma_f32_16x16x32_bf16 v[248:251], v[80:83], v[100:103], 0\n\t" \
  "ds_read_b64_tr_b16 v[132:133], v63 offset:22528\n\t" \
  "ds_read_b64_tr_b16 v[134:135], v63 offset:23040\n\t" \
  "v_mfma_f32_16x16x32_bf16 v[216:219], v[64:67], v[100:103], 0\n\t" \
  "v_mfma_f32_16x16x32_bf16 v[224:227], v[68:71], v[100:103], 0\n\t" \
  "v_mfma_f32_16x16x32_bf16 v[232:235], v[72:75], v[100:103], 0\n\t" \
  "v_mfma_f32_16x16x32_bf16 v[240:243], v[76:79], v[100:103], 0\n\t" \
  "s_waitcnt lgkmcnt(0)\n\t" \
  "s_getpc_b64 s[88:89]\n\t" \
  "s_branch 28b\n\t" \
  "s_barrier\n\t" \
  "v_mfma_f32_16x16x32_bf16 v[252:255], v[80:83], v[104:107], 0\n\t" \
  "ds_read_b64_tr_b16 v[92:93], v54 offset:26624\n\t" \
  "v_mfma_f32_16x16x32_bf16 v[220:223], v[64:67], v[104:107], 0\n\t" \
  "ds_read_b64_tr_b16 v[94:95], v54 offset:27136\n\t" \
  "v_mfma_f32_16x16x32_bf16 v[228:231], v[68:71], v[104:107], 0\n\t" \
  "ds_read_b64_tr_b16 v[96:97], v55 offset:26624\n\t" \
  "v_mfma_f32_16x16x32_bf16 v[236:239], v[72:75], v[104:107], 0\n\t" \
  "ds_read_b64_tr_b16 v[98:99], v55 offset:27136\n\t" \
  "v_mfma_f32_16x16x32_bf16 v[244:247], v[76:79], v[104:107], 0\n\t" \
  "ds_read_b64_tr_b16 v[100:101], v54 offset:34816\n\t" \
  "v_mfma_f32_16x16x32_bf16 v[152:155], v[84:87], v[64:67], 0\n\t" \
  "ds_read_b64_tr_b16 v[102:103], v54 offset:35328\n\t" \
  "v_mfma_f32_16x16x32_bf16 v[160:163], v[84:87], v[68:71], 0\n\t" \
  "ds_read_b128 v[80:83], v52 offset:0\n\t" \
  "v_mfma_f32_16x16x32_bf16 v[168:171], v[84:87], v[72:75], 0\n\t" \
  "ds_read_b64_tr_b16 v[104:105], v55 offset:34816\n\t" \
  "v_mfma_f32_16x16x32_bf16 v[176:179], v[84:87], v[76:79], 0\n\t" \
  "ds_read_b64_tr_b16 v[106:107], v55 offset:35328\n\t" \
  "v_mfma_f32_16x16x32_bf16 v[156:159], v[88:91], v[64:67], 0\n\t" \
  "ds_read_b128 v[64:67], v52 offset:10240\n\t" \
  "v_mfma_f32_16x16x32_bf16 v[164:167], v[88:91], v[68:71], 0\n\t" \
  "ds_read_b128 v[68:71], v52 offset:12288\n\t" \
  "v_mfma_f32_16x16x32_bf16 v[172:175], v[88:91], v[72:75], 0\n\t" \
  "ds_read_b128 v[72:75], v52 offset:14336\n\t" \
  "v_mfma_f32_16x16x32_bf16 v[180:183], v[88:91], v[76:79], 0\n\t" \
  "ds_read_b128 v[76:79], v52 offset:16384\n\t" \
  "v_mfma_f32_16x16x32_bf16 v[184:187], v[136:139], v[108:111], v[184:187]\n\t" \
  "ds_read_b64_tr_b16 v[84:85], v54 offset:18432\n\t" \
  "v_mfma_f32_16x16x32_bf16 v[192:195], v[136:139], v[112:115], v[192:195]\n\t" \
  "ds_read_b64_tr_b16 v[86:87], v54 offset:18944\n\t" \
  "v_mfma_f32_16x16x32_bf16 v[200:203], v[136:139], v[116:119], v[200:203]\n\t" \
  "ds_read_b64_tr_b16 v[88:89], v55 offset:18432\n\t" \
  "v_mfma_f32_16x16x32_bf16 v[208:211], v[136:139], v[120:123], v[208:211]\n\t" \
  "ds_read_b64_tr_b16 v[90:91], v55 offset:18944\n\t" \
  "v_mfma_f32_16x16x32_bf16 v[188:191], v[140:143], v[108:111], v[188:191]\n\t" \
  "ds_read_b64_tr_b16 v[136:137], v54 offset:30720\n\t" \
  "v_mfma_f32_16x16x32_bf16 v[196:199], v[140:143], v[112:115], v[196:199]\n\t" \
  "ds_read_b64_tr_b16 v[138:139], v54 offset:31232\n\t" \
  "v_mfma_f32_16x16x32_bf16 v[204:207], v[140:143], v[116:119], v[204:207]\n\t" \
  "v_mfma_f32_16x16x32_bf16 v[212:215], v[140:143], v[120:123], v[212:215]\n\t" \
  "ds_read_b64_tr_b16 v[140:141], v55 offset:30720\n\t" \
  "v_mfma_f32_16x16x32_bf16 v[248:251], v[124:127], v[144:147], v[248:251]\n\t" \
  "ds_read_b64_tr_b16 v[142:143], v55 offset:31232\n\t" \
  "v_mfma_f32_16x16x32_bf16 v[216:219], v[108:111], v[144:147], v[216:219]\n\t" \
  "v_mfma_f32_16x16x32_bf16 v[224:227], v[112:115], v[144:147], v[224:227]\n\t" \
  "v_mfma_f32_16x16x32_bf16 v[232:235], v[116:119], v[144:147], v[232:235]\n\t" \
  "v_mfma_f32_16x16x32_bf16 v[240:243], v[120:123], v[144:147], v[240:243]\n\t" \
  "ds_read_b64_tr_b16 v[144:145], v54 offset:38912\n\t" \
  "v_mfma_f32_16x16x32_bf16 v[252:255], v[124:127], v[148:151], v[252:255]\n\t" \
  "ds_read_b64_tr_b16 v[146:147], v54 offset:39424\n\t" \
  "v_mfma_f32_16x16x32_bf16 v[220:223], v[108:111], v[148:151], v[220:223]\n\t" \
  "ds_read_b128 v[124:127], v53 offset:0\n\t" \
  "v_mfma_f32_16x16x32_bf16 v[228:231], v[112:115], v[148:151], v[228:231]\n\t" \
  "v_mfma_f32_16x16x32_bf16 v[236:239], v[116:119], v[148:151], v[236:239]\n\t" \
  "v_mfma_f32_16x16x32_bf16 v[244:247], v[120:123], v[148:151], v[244:247]\n\t" \
  "ds_read_b64_tr_b16 v[148:149], v55 offset:38912\n\t" \
  "v_mfma_f32_16x16x32_bf16 v[152:155], v[128:131], v[108:111], v[152:155]\n\t" \
  "ds_read_b64_tr_b16 v[150:151], v55 offset:39424\n\t" \
  "v_mfma_f32_16x16x32_bf16 v[160:163], v[128:131], v[112:115], v[160:163]\n\t" \
  "v_mfma_f32_16x16x32_bf16 v[168:171], v[128:131], v[116:119], v[168:171]\n\t" \
  "v_mfma_f32_16x16x32_bf16 v[176:179], v[128:131], v[120:123], v[176:179]\n\t" \
  "ds_read_b64_tr_b16 v[128:129], v54 offset:22528\n\t" \
  "v_mfma_f32_16x16x32_bf16 v[156:159], v[132:135], v[108:111], v[156:159]\n\t" \
  "ds_read_b128 v[108:111], v53 offset:10240\n\t" \
  "v_mfma_f32_16x16x32_bf16 v[164:167], v[132:135], v[112:115], v[164:167]\n\t" \
  "ds_read_b128 v[112:115], v53 offset:12288\n\t" \
  "v_mfma_f32_16x16x32_bf16 v[172:175], v[132:135], v[116:119], v[172:175]\n\t" \
  "ds_read_b128 v[116:119], v53 offset:14336\n\t" \
  "v_mfma_f32_16x16x32_bf16 v[180:183], v[132:135], v[120:123], v[180:183]\n\t" \
  "ds_read_b128 v[120:123], v53 offset:16384\n\t" \
  "s_waitcnt lgkmcnt(5)\n\t" \
  "v_mfma_f32_16x16x32_bf16 v[184:187], v[92:95], v[64:67], v[184:187]\n\t" \
  "ds_read_b64_tr_b16 v[130:131], v54 offset:23040\n\t" \
  "v_mfma_f32_16x16x32_bf16 v[192:195], v[92:95], v[68:71], v[192:195]\n\t" \
  "ds_read_b64_tr_b16 v[132:133], v55 offset:22528\n\t" \
  "v_mfma_f32_16x16x32_bf16 v[200:203], v[92:95], v[72:75], v[200:203]\n\t" \
  "ds_read_b64_tr_b16 v[134:135], v55 offset:23040\n\t" \
  "v_mfma_f32_16x16x32_bf16 v[208:211], v[92:95], v[76:79], v[208:211]\n\t" \
  "v_mfma_f32_16x16x32_bf16 v[188:191], v[96:99], v[64:67], v[188:191]\n\t" \
  "v_mfma_f32_16x16x32_bf16 v[196:199], v[96:99], v[68:71], v[196:199]\n\t" \
  "v_mfma_f32_16x16x32_bf16 v[204:207], v[96:99], v[72:75], v[204:207]\n\t" \
  "v_mfma_f32_16x16x32_bf16 v[212:215], v[96:99], v[76:79], v[212:215]\n\t" \
  "v_mfma_f32_16x16x32_bf16 v[248:251], v[80:83], v[100:103], v[248:251]\n\t" \
  "v_mfma_f32_16x16x32_bf16 v[216:219], v[64:67], v[100:103], v[216:219]\n\t" \
  "v_mfma_f32_16x16x32_bf16 v[224:227], v[68:71], v[100:103], v[224:227]\n\t" \
  "v_mfma_f32_16x16x32_bf16 v[232:235], v[72:75], v[100:103], v[232:235]\n\t" \
  "v_mfma_f32_16x16x32_bf16 v[240:243], v[76:79], v[100:103], v[240:243]\n\t" \
  "s_waitcnt lgkmcnt(0)\n\t" \
  "s_barrier\n\t" \
  "v_mfma_f32_16x16x32_bf16 v[252:255], v[80:83], v[104:107], v[252:255]\n\t" \
  "ds_read_b64_tr_b16 v[92:93], v58 offset:26624\n\t" \
  "v_mfma_f32_16x16x32_bf16 v[220:223], v[64:67], v[104:107], v[220:223]\n\t" \
  "ds_read_b64_tr_b16 v[94:95], v58 offset:27136\n\t" \
  "v_mfma_f32_16x16x32_bf16 v[228:231], v[68:71], v[104:107], v[228:231]\n\t" \
  "ds_read_b64_tr_b16 v[96:97], v59 offset:26624\n\t" \
  "v_mfma_f32_16x16x32_bf16 v[236:239], v[72:75], v[104:107], v[236:239]\n\t" \
  "ds_read_b64_tr_b16 v[98:99], v59 offset:27136\n\t" \
  "v_mfma_f32_16x16x32_bf16 v[244:247], v[76:79], v[104:107], v[244:247]\n\t" \
  "ds_read_b64_tr_b16 v[100:101], v58 offset:34816\n\t" \
  "v_mfma_f32_16x16x32_bf16 v[152:155], v[84:87], v[64:67], v[152:155]\n\t" \
  "ds_read_b64_tr_b16 v[102:103], v58 offset:35328\n\t" \
  "v_mfma_f32_16x16x32_bf16 v[160:163], v[84:87], v[68:71], v[160:163]\n\t" \
  "ds_read_b128 v[80:83], v56 offset:0\n\t" \
  "v_mfma_f32_16x16x32_bf16 v[168:171], v[84:87], v[72:75], v[168:171]\n\t" \
  "ds_read_b64_tr_b16 v[104:105], v59 offset:34816\n\t" \
  "v_mfma_f32_16x16x32_bf16 v[176:179], v[84:87], v[76:79], v[176:179]\n\t" \
  "ds_read_b64_tr_b16 v[106:107], v59 offset:35328\n\t" \
  "v_mfma_f32_16x16x32_bf16 v[156:159], v[88:91], v[64:67], v[156:159]\n\t" \
  "ds_read_b128 v[64:67], v56 offset:10240\n\t" \
  "v_mfma_f32_16x16x32_bf16 v[164:167], v[88:91], v[68:71], v[164:167]\n\t" \
  "ds_read_b128 v[68:71], v56 offset:12288\n\t" \
  "v_mfma_f32_16x16x32_bf16 v[172:175], v[88:91], v[72:75], v[172:175]\n\t" \
  "ds_read_b128 v[72:75], v56 offset:14336\n\t" \
  "v_mfma_f32_16x16x32_bf16 v[180:183], v[88:91], v[76:79], v[180:183]\n\t" \
  "ds_read_b128 v[76:79], v56 offset:16384\n\t" \
  "v_mfma_f32_16x16x32_bf16 v[184:187], v[136:139], v[108:111], v[184:187]\n\t" \
  "ds_read_b64_tr_b16 v[84:85], v58 offset:18432\n\t" \
  "v_mfma_f32_16x16x32_bf16 v[192:195], v[136:139], v[112:115], v[192:195]\n\t" \
  "ds_read_b64_tr_b16 v[86:87], v58 offset:18944\n\t" \
  "v_mfma_f32_16x16x32_bf16 v[200:203], v[136:139], v[116:119], v[200:203]\n\t" \
  "ds_read_b64_tr_b16 v[88:89], v59 offset:18432\n\t" \
  "v_mfma_f32_16x16x32_bf16 v[208:211], v[136:139], v[120:123], v[208:211]\n\t" \
  "ds_read_b64_tr_b16 v[90:91], v59 offset:18944\n\t" \
  "v_mfma_f32_16x16x32_bf16 v[188:191], v[140:143], v[108:111], v[188:191]\n\t" \
  "ds_read_b64_tr_b16 v[136:137], v58 offset:30720\n\t" \
  "v_mfma_f32_16x16x32_bf16 v[196:199], v[140:143], v[112:115], v[196:199]\n\t" \
  "ds_read_b64_tr_b16 v[138:139], v58 offset:31232\n\t" \
  "v_mfma_f32_16x16x32_bf16 v[204:207], v[140:143], v[116:119], v[204:207]\n\t" \
  "v_mfma_f32_16x16x32_bf16 v[212:215], v[140:143], v[120:123], v[212:215]\n\t" \
  "ds_read_b64_tr_b16 v[140:141], v59 offset:30720\n\t" \
  "v_mfma_f32_16x16x32_bf16 v[248:251], v[124:127], v[144:147], v[248:251]\n\t" \
  "ds_read_b64_tr_b16 v[142:143], v59 offset:31232\n\t" \
  "v_mfma_f32_16x16x32_bf16 v[216:219], v[108:111], v[144:147], v[216:219]\n\t" \
  "v_mfma_f32_16x16x32_bf16 v[224:227], v[112:115], v[144:147], v[224:227]\n\t" \
  "v_mfma_f32_16x16x32_bf16 v[232:235], v[116:119], v[144:147], v[232:235]\n\t" \
  "v_mfma_f32_16x16x32_bf16 v[240:243], v[120:123], v[144:147], v[240:243]\n\t" \
  "ds_read_b64_tr_b16 v[144:145], v58 offset:38912\n\t" \
  "v_mfma_f32_16x16x32_bf16 v[252:255], v[124:127], v[148:151], v[252:255]\n\t" \
  "ds_read_b64_tr_b16 v[146:147], v58 offset:39424\n\t" \
  "v_mfma_f32_16x16x32_bf16 v[220:223], v[108:111], v[148:151], v[220:223]\n\t" \
  "ds_read_b128 v[124:127], v57 offset:0\n\t" \
  "v_mfma_f32_16x16x32_bf16 v[228:231], v[112:115], v[148:151], v[228:231]\n\t" \
  "v_mfma_f32_16x16x32_bf16 v[236:239], v[116:119], v[148:151], v[236:239]\n\t" \
  "v_mfma_f32_16x16x32_bf16 v[244:247], v[120:123], v[148:151], v[244:247]\n\t" \
  "ds_read_b64_tr_b16 v[148:149], v59 offset:38912\n\t" \
  "v_mfma_f32_16x16x32_bf16 v[152:155], v[128:131], v[108:111], v[152:155]\n\t" \
  "ds_read_b64_tr_b16 v[150:151], v59 offset:39424\n\t" \
  "v_mfma_f32_16x16x32_bf16 v[160:163], v[128:131], v[112:115], v[160:163]\n\t" \
  "v_mfma_f32_16x16x32_bf16 v[168:171], v[128:131], v[116:119], v[168:171]\n\t" \
  "v_mfma_f32_16x16x32_bf16 v[176:179], v[128:131], v[120:123], v[176:179]\n\t" \
  "ds_read_b64_tr_b16 v[128:129], v58 offset:22528\n\t" \
  "v_mfma_f32_16x16x32_bf16 v[156:159], v[132:135], v[108:111], v[156:159]\n\t" \
  "ds_read_b128 v[108:111], v57 offset:10240\n\t" \
  "v_mfma_f32_16x16x32_bf16 v[164:167], v[132:135], v[112:115], v[164:167]\n\t" \
  "ds_read_b128 v[112:115], v57 offset:12288\n\t" \
  "v_mfma_f32_16x16x32_bf16 v[172:175], v[132:135], v[116:119], v[172:175]\n\t" \
  "ds_read_b128 v[116:119], v57 offset:14336\n\t" \
  "v_mfma_f32_16x16x32_bf16 v[180:183], v[132:135], v[120:123], v[180:183]\n\t" \
  "ds_read_b128 v[120:123], v57 offset:16384\n\t" \
  "s_waitcnt lgkmcnt(5)\n\t" \
  "v_mfma_f32_16x16x32_bf16 v[184:187], v[92:95], v[64:67], v[184:187]\n\t" \
  "ds_read_b64_tr_b16 v[130:131], v58 offset:23040\n\t" \
  "v_mfma_f32_16x16x32_bf16 v[192:195], v[92:95], v[68:71], v[192:195]\n\t" \
  "ds_read_b64_tr_b16 v[132:133], v59 offset:22528\n\t" \
  "v_mfma_f32_16x16x32_bf16 v[200:203], v[92:95], v[72:75], v[200:203]\n\t" \
  "ds_read_b64_tr_b16 v[134:135], v59 offset:23040\n\t" \
  "v_mfma_f32_16x16x32_bf16 v[208:211], v[92:95], v[76:79], v[208:211]\n\t" \
  "v_mfma_f32_16x16x32_bf16 v[188:191], v[96:99], v[64:67], v[188:191]\n\t" \
  "v_mfma_f32_16x16x32_bf16 v[196:199], v[96:99], v[68:71], v[196:199]\n\t" \
  "v_mfma_f32_16x16x32_bf16 v[204:207], v[96:99], v[72:75], v[204:207]\n\t" \
  "v_mfma_f32_16x16x32_bf16 v[212:215], v[96:99], v[76:79], v[212:215]\n\t" \
  "v_mfma_f32_16x16x32_bf16 v[248:251], v[80:83], v[100:103], v[248:251]\n\t" \
  "v_mfma_f32_16x16x32_bf16 v[216:219], v[64:67], v[100:103], v[216:219]\n\t" \
  "v_mfma_f32_16x16x32_bf16 v[224:227], v[68:71], v[100:103], v[224:227]\n\t" \
  "v_mfma_f32_16x16x32_bf16 v[232:235], v[72:75], v[100:103], v[232:235]\n\t" \
  "v_mfma_f32_16x16x32_bf16 v[240:243], v[76:79], v[100:103], v[240:243]\n\t" \
  "s_waitcnt lgkmcnt(0)\n\t" \
  "s_barrier\n\t" \
  "v_mfma_f32_16x16x32_bf16 v[252:255], v[80:83], v[104:107], v[252:255]\n\t" \
  "ds_read_b64_tr_b16 v[92:93], v62 offset:26624\n\t" \
  "v_bfe_u32 v30, v4, 9, 1\n\t" \
  "v_mfma_f32_16x16x32_bf16 v[220:223], v[64:67], v[104:107], v[220:223]\n\t" \
  "ds_read_b64_tr_b16 v[94:95], v62 offset:27136\n\t" \
  "v_mad_i32_i24 v30, v30, -16, v0\n\t" \
  "v_mfma_f32_16x16x32_bf16 v[228:231], v[68:71], v[104:107], v[228:231]\n\t" \
  "ds_read_b64_tr_b16 v[96:97], v63 offset:26624\n\t" \
  "ds_read_b128 v[26:29], v0 offset:1552\n\t" \
  "v_mfma_f32_16x16x32_bf16 v[236:239], v[72:75], v[104:107], v[236:239]\n\t" \
  "ds_read_b64_tr_b16 v[98:99], v63 offset:27136\n\t" \
  "ds_read_b128 v[30:33], v30 offset:1040\n\t" \
  "v_mfma_f32_16x16x32_bf16 v[244:247], v[76:79], v[104:107], v[244:247]\n\t" \
  "ds_read_b64_tr_b16 v[100:101], v62 offset:34816\n\t" \
  "ds_read_b128 v[34:37], v0 offset:512\n\t" \
  "v_mfma_f32_16x16x32_bf16 v[152:155], v[84:87], v[64:67], v[152:155]\n\t" \
  "ds_read_b64_tr_b16 v[102:103], v62 offset:35328\n\t" \
  "ds_read_b64_tr_b16 v[38:39], v1 offset:512\n\t" \
  "v_mfma_f32_16x16x32_bf16 v[160:163], v[84:87], v[68:71], v[160:163]\n\t" \
  "ds_read_b128 v[80:83], v60 offset:0\n\t" \
  "ds_read_b64_tr_b16 v[40:41], v1 offset:0\n\t" \
  "v_mfma_f32_16x16x32_bf16 v[168:171], v[84:87], v[72:75], v[168:171]\n\t" \
  "ds_read_b64_tr_b16 v[104:105], v63 offset:34816\n\t" \
  "s_waitcnt vmcnt(0)\n\t" \
  "v_mfma_f32_16x16x32_bf16 v[176:179], v[84:87], v[76:79], v[176:179]\n\t" \
  "ds_read_b64_tr_b16 v[106:107], v63 offset:35328\n\t" \
  "v_perm_b32 v18, v10, v10, v14\n\t" \
  "v_mfma_f32_16x16x32_bf16 v[156:159], v[88:91], v[64:67], v[156:159]\n\t" \
  "ds_read_b128 v[64:67], v60 offset:10240\n\t" \
  "v_perm_b32 v19, v10, v10, v15\n\t" \
  "v_mfma_f32_16x16x32_bf16 v[164:167], v[88:91], v[68:71], v[164:167]\n\t" \
  "ds_read_b128 v[68:71], v60 offset:12288\n\t" \
  "v_perm_b32 v20, v10, v10, v16\n\t" \
  "v_mfma_f32_16x16x32_bf16 v[172:175], v[88:91], v[72:75], v[172:175]\n\t" \
  "ds_read_b128 v[72:75], v60 offset:14336\n\t" \
  "v_perm_b32 v21, v10, v10, v17\n\t" \
  "v_mfma_f32_16x16x32_bf16 v[180:183], v[88:91], v[76:79], v[180:183]\n\t" \
  "ds_read_b128 v[76:79], v60 offset:16384\n\t" \
  "v_perm_b32 v22, v11, v11, v14\n\t" \
  "v_mfma_f32_16x16x32_bf16 v[184:187], v[136:139], v[108:111], v[184:187]\n\t" \
  "ds_read_b64_tr_b16 v[84:85], v62 offset:18432\n\t" \
  "v_perm_b32 v23, v11, v11, v15\n\t" \
  "v_mfma_f32_16x16x32_bf16 v[192:195], v[136:139], v[112:115], v[192:195]\n\t" \
  "ds_read_b64_tr_b16 v[86:87], v62 offset:18944\n\t" \
  "v_perm_b32 v24, v11, v11, v16\n\t" \
  "v_mfma_f32_16x16x32_bf16 v[200:203], v[136:139], v[116:119], v[200:203]\n\t" \
  "ds_read_b64_tr_b16 v[88:89], v63 offset:18432\n\t" \
  "v_perm_b32 v25, v11, v11, v17\n\t" \
  "v_mfma_f32_16x16x32_bf16 v[208:211], v[136:139], v[120:123], v[208:211]\n\t" \
  "ds_read_b64_tr_b16 v[90:91], v63 offset:18944\n\t" \
  "v_mfma_f32_16x16x32_bf16 v[188:191], v[140:143], v[108:111], v[188:191]\n\t" \
  "ds_read_b64_tr_b16 v[136:137], v62 offset:30720\n\t" \
  "v_mfma_f32_16x16x32_bf16 v[196:199], v[140:143], v[112:115], v[196:199]\n\t" \
  "ds_read_b64_tr_b16 v[138:139], v62 offset:31232\n\t" \
  "s_waitcnt lgkmcnt(6)\n\t" \
  "v_mfma_f32_16x16x32_bf16 v[42:45], v[26:29], v[18:21], 0\n\t" \
  "v_mfma_f32_16x16x32_bf16 v[42:45], v[30:33], v[22:25], v[42:45]\n\t" \
  "v_mfma_f32_16x16x32_bf16 v[46:49], v[30:33], v[18:21], 0\n\t" \
  "v_mfma_f32_16x16x32_bf16 v[46:49], v[34:37], v[22:25], v[46:49]\n\t" \
  "ds_read_b128 v[26:29], v0 offset:1808\n\t" \
  "ds_read_b128 v[30:33], v0 offset:1296\n\t" \
  "v_mfma_f32_16x16x32_bf16 v[204:207], v[140:143], v[116:119], v[204:207]\n\t" \
  "ds_read_b128 v[34:37], v0 offset:768\n\t" \
  "v_mfma_f32_16x16x32_bf16 v[212:215], v[140:143], v[120:123], v[212:215]\n\t" \
  "ds_read_b64_tr_b16 v[140:141], v63 offset:30720\n\t" \
  "v_mfma_f32_16x16x32_bf16 v[248:251], v[124:127], v[144:147], v[248:251]\n\t" \
  "ds_read_b64_tr_b16 v[142:143], v63 offset:31232\n\t" \
  "v_lshlrev_b32 v50, 16, v38\n\t" \
  "v_mfma_f32_16x16x32_bf16 v[216:219], v[108:111], v[144:147], v[216:219]\n\t" \
  "v_lshlrev_b32 v51, 16, v40\n\t" \
  "v_mul_f32 v42, v42, v50\n\t" \
  "v_mfma_f32_16x16x32_bf16 v[224:227], v[112:115], v[144:147], v[224:227]\n\t" \
  "v_mul_f32 v46, v46, v51\n\t" \
  "v_cvt_pk_bf16_f32 v42, v42, v46\n\t" \
  "v_mfma_f32_16x16x32_bf16 v[232:235], v[116:119], v[144:147], v[232:235]\n\t" \
  "v_and_b32 v50, 0xffff0000, v38\n\t" \
  "v_and_b32 v51, 0xffff0000, v40\n\t" \
  "v_mfma_f32_16x16x32_bf16 v[240:243], v[120:123], v[144:147], v[240:243]\n\t" \
  "ds_read_b64_tr_b16 v[144:145], v62 offset:38912\n\t" \
  "v_mul_f32 v43, v43, v50\n\t" \
  "v_mfma_f32_16x16x32_bf16 v[252:255], v[124:127], v[148:151], v[252:255]\n\t" \
  "ds_read_b64_tr_b16 v[146:147], v62 offset:39424\n\t" \
  "v_mul_f32 v47, v47, v51\n\t" \
  "v_mfma_f32_16x16x32_bf16 v[220:223], v[108:111], v[148:151], v[220:223]\n\t" \
  "ds_read_b128 v[124:127], v61 offset:0\n\t" \
  "v_cvt_pk_bf16_f32 v43, v43, v47\n\t" \
  "v_mfma_f32_16x16x32_bf16 v[228:231], v[112:115], v[148:151], v[228:231]\n\t" \
  "v_lshlrev_b32 v50, 16, v39\n\t" \
  "v_lshlrev_b32 v51, 16, v41\n\t" \
  "v_mfma_f32_16x16x32_bf16 v[236:239], v[116:119], v[148:151], v[236:239]\n\t" \
  "v_mul_f32 v44, v44, v50\n\t" \
  "v_mul_f32 v48, v48, v51\n\t" \
  "v_mfma_f32_16x16x32_bf16 v[244:247], v[120:123], v[148:151], v[244:247]\n\t" \
  "ds_read_b64_tr_b16 v[148:149], v63 offset:38912\n\t" \
  "v_cvt_pk_bf16_f32 v44, v44, v48\n\t" \
  "v_mfma_f32_16x16x32_bf16 v[152:155], v[128:131], v[108:111], v[152:155]\n\t" \
  "ds_read_b64_tr_b16 v[150:151], v63 offset:39424\n\t" \
  "v_and_b32 v50, 0xffff0000, v39\n\t" \
  "v_mfma_f32_16x16x32_bf16 v[160:163], v[128:131], v[112:115], v[160:163]\n\t" \
  "v_and_b32 v51, 0xffff0000, v41\n\t" \
  "v_mul_f32 v45, v45, v50\n\t" \
  "v_mfma_f32_16x16x32_bf16 v[168:171], v[128:131], v[116:119], v[168:171]\n\t" \
  "v_mul_f32 v49, v49, v51\n\t" \
  "ds_read_b64_tr_b16 v[38:39], v1 offset:768\n\t" \
  "v_mfma_f32_16x16x32_bf16 v[176:179], v[128:131], v[120:123], v[176:179]\n\t" \
  "ds_read_b64_tr_b16 v[128:129], v62 offset:22528\n\t" \
  "ds_read_b64_tr_b16 v[40:41], v1 offset:256\n\t" \
  "v_mfma_f32_16x16x32_bf16 v[156:159], v[132:135], v[108:111], v[156:159]\n\t" \
  "ds_read_b128 v[108:111], v61 offset:10240\n\t" \
  "v_cvt_pk_bf16_f32 v45, v45, v49\n\t" \
  "v_mfma_f32_16x16x32_bf16 v[164:167], v[132:135], v[112:115], v[164:167]\n\t" \
  "ds_read_b128 v[112:115], v61 offset:12288\n\t" \
  "v_mfma_f32_16x16x32_bf16 v[172:175], v[132:135], v[116:119], v[172:175]\n\t" \
  "ds_read_b128 v[116:119], v61 offset:14336\n\t" \
  "v_mfma_f32_16x16x32_bf16 v[180:183], v[132:135], v[120:123], v[180:183]\n\t" \
  "ds_read_b128 v[120:123], v61 offset:16384\n\t" \
  "v_mfma_f32_16x16x32_bf16 v[184:187], v[92:95], v[64:67], v[184:187]\n\t" \
  "ds_read_b64_tr_b16 v[130:131], v62 offset:23040\n\t" \
  "v_mfma_f32_16x16x32_bf16 v[192:195], v[92:95], v[68:71], v[192:195]\n\t" \
  "ds_read_b64_tr_b16 v[132:133], v63 offset:22528\n\t" \
  "v_mfma_f32_16x16x32_bf16 v[200:203], v[92:95], v[72:75], v[200:203]\n\t" \
  "ds_read_b64_tr_b16 v[134:135], v63 offset:23040\n\t" \
  "v_mfma_f32_16x16x32_bf16 v[208:211], v[92:95], v[76:79], v[208:211]\n\t" \
  "v_mfma_f32_16x16x32_bf16 v[188:191], v[96:99], v[64:67], v[188:191]\n\t" \
  "v_mfma_f32_16x16x32_bf16 v[196:199], v[96:99], v[68:71], v[196:199]\n\t" \
  "v_mfma_f32_16x16x32_bf16 v[204:207], v[96:99], v[72:75], v[204:207]\n\t" \
  "v_mfma_f32_16x16x32_bf16 v[212:215], v[96:99], v[76:79], v[212:215]\n\t" \
  "v_mfma_f32_16x16x32_bf16 v[248:251], v[80:83], v[100:103], v[248:251]\n\t" \
  "v_mfma_f32_16x16x32_bf16 v[216:219], v[64:67], v[100:103], v[216:219]\n\t" \
  "global_store_dwordx4 v2, v[42:45], %24 offset:0 sc1\n\t" \
  "v_mfma_f32_16x16x32_bf16 v[224:227], v[68:71], v[100:103], v[224:227]\n\t" \
  "s_waitcnt lgkmcnt(0)\n\t" \
  "v_mfma_f32_16x16x32_bf16 v[42:45], v[26:29], v[18:21], 0\n\t" \
  "v_mfma_f32_16x16x32_bf16 v[42:45], v[30:33], v[22:25], v[42:45]\n\t" \
  "v_mfma_f32_16x16x32_bf16 v[46:49], v[30:33], v[18:21], 0\n\t" \
  "v_mfma_f32_16x16x32_bf16 v[46:49], v[34:37], v[22:25], v[46:49]\n\t" \
  "v_bfe_u32 v30, v4, 9, 1\n\t" \
  "v_mad_i32_i24 v30, v30, -16, v0\n\t" \
  "v_mfma_f32_16x16x32_bf16 v[232:235], v[72:75], v[100:103], v[232:235]\n\t" \
  "ds_read_b128 v[26:29], v0 offset:1584\n\t" \
  "ds_read_b128 v[30:33], v30 offset:1072\n\t" \
  "v_mfma_f32_16x16x32_bf16 v[240:243], v[76:79], v[100:103], v[240:243]\n\t" \
  "ds_read_b128 v[34:37], v0 offset:544\n\t" \
  "v_perm_b32 v18, v12, v12, v14\n\t" \
  "s_waitcnt lgkmcnt(0)\n\t" \
  "s_barrier\n\t" \
  "v_mfma_f32_16x16x32_bf16 v[252:255], v[80:83], v[104:107], v[252:255]\n\t" \
  "ds_read_b64_tr_b16 v[92:93], v54 offset:26624\n\t" \
  "v_perm_b32 v19, v12, v12, v15\n\t" \
  "v_mfma_f32_16x16x32_bf16 v[220:223], v[64:67], v[104:107], v[220:223]\n\t" \
  "ds_read_b64_tr_b16 v[94:95], v54 offset:27136\n\t" \
  "v_perm_b32 v20, v12, v12, v16\n\t" \
  "v_mfma_f32_16x16x32_bf16 v[228:231], v[68:71], v[104:107], v[228:231]\n\t" \
  "ds_read_b64_tr_b16 v[96:97], v55 offset:26624\n\t" \
  "v_perm_b32 v21, v12, v12, v17\n\t" \
  "v_mfma_f32_16x16x32_bf16 v[236:239], v[72:75], v[104:107], v[236:239]\n\t" \
  "ds_read_b64_tr_b16 v[98:99], v55 offset:27136\n\t" \
  "v_perm_b32 v22, v13, v13, v14\n\t" \
  "v_mfma_f32_16x16x32_bf16 v[244:247], v[76:79], v[104:107], v[244:247]\n\t" \
  "ds_read_b64_tr_b16 v[100:101], v54 offset:34816\n\t" \
  "v_perm_b32 v23, v13, v13, v15\n\t" \
  "v_mfma_f32_16x16x32_bf16 v[152:155], v[84:87], v[64:67], v[152:155]\n\t" \
  "ds_read_b64_tr_b16 v[102:103], v54 offset:35328\n\t" \
  "v_perm_b32 v24, v13, v13, v16\n\t" \
  "v_mfma_f32_16x16x32_bf16 v[160:163], v[84:87], v[68:71], v[160:163]\n\t" \
  "ds_read_b128 v[80:83], v52 offset:0\n\t" \
  "v_perm_b32 v25, v13, v13, v17\n\t" \
  "v_mfma_f32_16x16x32_bf16 v[168:171], v[84:87], v[72:75], v[168:171]\n\t" \
  "ds_read_b64_tr_b16 v[104:105], v55 offset:34816\n\t" \
  "v_lshlrev_b32 v50, 16, v38\n\t" \
  "v_mfma_f32_16x16x32_bf16 v[176:179], v[84:87], v[76:79], v[176:179]\n\t" \
  "ds_read_b64_tr_b16 v[106:107], v55 offset:35328\n\t" \
  "v_lshlrev_b32 v51, 16, v40\n\t" \
  "v_mfma_f32_16x16x32_bf16 v[156:159], v[88:91], v[64:67], v[156:159]\n\t" \
  "ds_read_b128 v[64:67], v52 offset:10240\n\t" \
  "v_mul_f32 v42, v42, v50\n\t" \
  "v_mfma_f32_16x16x32_bf16 v[164:167], v[88:91], v[68:71], v[164:167]\n\t" \
  "ds_read_b128 v[68:71], v52 offset:12288\n\t" \
  "v_mul_f32 v46, v46, v51\n\t" \
  "v_mfma_f32_16x16x32_bf16 v[172:175], v[88:91], v[72:75], v[172:175]\n\t" \
  "ds_read_b128 v[72:75], v52 offset:14336\n\t" \
  "v_cvt_pk_bf16_f32 v42, v42, v46\n\t" \
  "v_mfma_f32_16x16x32_bf16 v[180:183], v[88:91], v[76:79], v[180:183]\n\t" \
  "ds_read_b128 v[76:79], v52 offset:16384\n\t" \
  "v_and_b32 v50, 0xffff0000, v38\n\t" \
  "v_mfma_f32_16x16x32_bf16 v[184:187], v[136:139], v[108:111], v[184:187]\n\t" \
  "ds_read_b64_tr_b16 v[84:85], v54 offset:18432\n\t" \
  "v_and_b32 v51, 0xffff0000, v40\n\t" \
  "v_mfma_f32_16x16x32_bf16 v[192:195], v[136:139], v[112:115], v[192:195]\n\t" \
  "ds_read_b64_tr_b16 v[86:87], v54 offset:18944\n\t" \
  "v_mul_f32 v43, v43, v50\n\t" \
  "v_mfma_f32_16x16x32_bf16 v[200:203], v[136:139], v[116:119], v[200:203]\n\t" \
  "ds_read_b64_tr_b16 v[88:89], v55 offset:18432\n\t" \
  "v_mul_f32 v47, v47, v51\n\t" \
  "v_mfma_f32_16x16x32_bf16 v[208:211], v[136:139], v[120:123], v[208:211]\n\t" \
  "ds_read_b64_tr_b16 v[90:91], v55 offset:18944\n\t" \
  "v_cvt_pk_bf16_f32 v43, v43, v47\n\t" \
  "v_mfma_f32_16x16x32_bf16 v[188:191], v[140:143], v[108:111], v[188:191]\n\t" \
  "ds_read_b64_tr_b16 v[136:137], v54 offset:30720\n\t" \
  "v_lshlrev_b32 v50, 16, v39\n\t" \
  "v_mfma_f32_16x16x32_bf16 v[196:199], v[140:143], v[112:115], v[196:199]\n\t" \
  "ds_read_b64_tr_b16 v[138:139], v54 offset:31232\n\t" \
  "v_lshlrev_b32 v51, 16, v41\n\t" \
  "v_mfma_f32_16x16x32_bf16 v[204:207], v[140:143], v[116:119], v[204:207]\n\t" \
  "v_mul_f32 v44, v44, v50\n\t" \
  "v_mul_f32 v48, v48, v51\n\t" \
  "v_mfma_f32_16x16x32_bf16 v[212:215], v[140:143], v[120:123], v[212:215]\n\t" \
  "ds_read_b64_tr_b16 v[140:141], v55 offset:30720\n\t" \
  "v_cvt_pk_bf16_f32 v44, v44, v48\n\t" \
  "v_mfma_f32_16x16x32_bf16 v[248:251], v[124:127], v[144:147], v[248:251]\n\t" \
  "ds_read_b64_tr_b16 v[142:143], v55 offset:31232\n\t" \
  "v_and_b32 v50, 0xffff0000, v39\n\t" \
  "v_mfma_f32_16x16x32_bf16 v[216:219], v[108:111], v[144:147], v[216:219]\n\t" \
  "v_and_b32 v51, 0xffff0000, v41\n\t" \
  "v_mul_f32 v45, v45, v50\n\t" \
  "v_mfma_f32_16x16x32_bf16 v[224:227], v[112:115], v[144:147], v[224:227]\n\t" \
  "v_mul_f32 v49, v49, v51\n\t" \
  "ds_read_b64_tr_b16 v[38:39], v1 offset:544\n\t" \
  "v_mfma_f32_16x16x32_bf16 v[232:235], v[116:119], v[144:147], v[232:235]\n\t" \
  "ds_read_b64_tr_b16 v[40:41], v1 offset:32\n\t" \
  "v_cvt_pk_bf16_f32 v45, v45, v49\n\t" \
  "v_mfma_f32_16x16x32_bf16 v[240:243], v[120:123], v[144:147], v[240:243]\n\t" \
  "ds_read_b64_tr_b16 v[144:145], v54 offset:38912\n\t" \
  "v_mfma_f32_16x16x32_bf16 v[252:255], v[124:127], v[148:151], v[252:255]\n\t" \
  "ds_read_b64_tr_b16 v[146:147], v54 offset:39424\n\t" \
  "v_mfma_f32_16x16x32_bf16 v[220:223], v[108:111], v[148:151], v[220:223]\n\t" \
  "ds_read_b128 v[124:127], v53 offset:0\n\t" \
  "v_mfma_f32_16x16x32_bf16 v[228:231], v[112:115], v[148:151], v[228:231]\n\t" \
  "v_mfma_f32_16x16x32_bf16 v[236:239], v[116:119], v[148:151], v[236:239]\n\t" \
  "v_mfma_f32_16x16x32_bf16 v[244:247], v[120:123], v[148:151], v[244:247]\n\t" \
  "ds_read_b64_tr_b16 v[148:149], v55 offset:38912\n\t" \
  "v_mfma_f32_16x16x32_bf16 v[152:155], v[128:131], v[108:111], v[152:155]\n\t" \
  "ds_read_b64_tr_b16 v[150:151], v55 offset:39424\n\t" \
  "v_mfma_f32_16x16x32_bf16 v[160:163], v[128:131], v[112:115], v[160:163]\n\t" \
  "v_mfma_f32_16x16x32_bf16 v[168:171], v[128:131], v[116:119], v[168:171]\n\t" \
  "v_mfma_f32_16x16x32_bf16 v[176:179], v[128:131], v[120:123], v[176:179]\n\t" \
  "ds_read_b64_tr_b16 v[128:129], v54 offset:22528\n\t" \
  "v_mfma_f32_16x16x32_bf16 v[156:159], v[132:135], v[108:111], v[156:159]\n\t" \
  "ds_read_b128 v[108:111], v53 offset:10240\n\t" \
  "v_mfma_f32_16x16x32_bf16 v[164:167], v[132:135], v[112:115], v[164:167]\n\t" \
  "ds_read_b128 v[112:115], v53 offset:12288\n\t" \
  "v_mfma_f32_16x16x32_bf16 v[172:175], v[132:135], v[116:119], v[172:175]\n\t" \
  "ds_read_b128 v[116:119], v53 offset:14336\n\t" \
  "v_mfma_f32_16x16x32_bf16 v[180:183], v[132:135], v[120:123], v[180:183]\n\t" \
  "ds_read_b128 v[120:123], v53 offset:16384\n\t" \
  "s_waitcnt lgkmcnt(5)\n\t" \
  "v_mfma_f32_16x16x32_bf16 v[184:187], v[92:95], v[64:67], v[184:187]\n\t" \
  "ds_read_b64_tr_b16 v[130:131], v54 offset:23040\n\t" \
  "v_mfma_f32_16x16x32_bf16 v[192:195], v[92:95], v[68:71], v[192:195]\n\t" \
  "ds_read_b64_tr_b16 v[132:133], v55 offset:22528\n\t" \
  "v_mfma_f32_16x16x32_bf16 v[200:203], v[92:95], v[72:75], v[200:203]\n\t" \
  "ds_read_b64_tr_b16 v[134:135], v55 offset:23040\n\t" \
  "v_mfma_f32_16x16x32_bf16 v[208:211], v[92:95], v[76:79], v[208:211]\n\t" \
  "v_mfma_f32_16x16x32_bf16 v[188:191], v[96:99], v[64:67], v[188:191]\n\t" \
  "v_mfma_f32_16x16x32_bf16 v[196:199], v[96:99], v[68:71], v[196:199]\n\t" \
  "v_mfma_f32_16x16x32_bf16 v[204:207], v[96:99], v[72:75], v[204:207]\n\t" \
  "v_mfma_f32_16x16x32_bf16 v[212:215], v[96:99], v[76:79], v[212:215]\n\t" \
  "v_mfma_f32_16x16x32_bf16 v[248:251], v[80:83], v[100:103], v[248:251]\n\t" \
  "v_mfma_f32_16x16x32_bf16 v[216:219], v[64:67], v[100:103], v[216:219]\n\t" \
  "global_store_dwordx4 v2, v[42:45], %24 offset:1024 sc1\n\t" \
  "v_add_u32 v2, 0x10000, v2\n\t" \
  "v_mfma_f32_16x16x32_bf16 v[224:227], v[68:71], v[100:103], v[224:227]\n\t" \
  "v_mfma_f32_16x16x32_bf16 v[232:235], v[72:75], v[100:103], v[232:235]\n\t" \
  "v_mfma_f32_16x16x32_bf16 v[42:45], v[26:29], v[18:21], 0\n\t" \
  "v_mfma_f32_16x16x32_bf16 v[42:45], v[30:33], v[22:25], v[42:45]\n\t" \
  "v_mfma_f32_16x16x32_bf16 v[46:49], v[30:33], v[18:21], 0\n\t" \
  "v_mfma_f32_16x16x32_bf16 v[46:49], v[34:37], v[22:25], v[46:49]\n\t" \
  "ds_read_b128 v[26:29], v0 offset:1840\n\t" \
  "ds_read_b128 v[30:33], v0 offset:1328\n\t" \
  "v_mfma_f32_16x16x32_bf16 v[240:243], v[76:79], v[100:103], v[240:243]\n\t" \
  "ds_read_b128 v[34:37], v0 offset:800\n\t" \
  "s_waitcnt lgkmcnt(0)\n\t" \
  "s_barrier\n\t" \
  "v_mfma_f32_16x16x32_bf16 v[252:255], v[80:83], v[104:107], v[252:255]\n\t" \
  "ds_read_b64_tr_b16 v[92:93], v58 offset:26624\n\t" \
  "v_mfma_f32_16x16x32_bf16 v[220:223], v[64:67], v[104:107], v[220:223]\n\t" \
  "ds_read_b64_tr_b16 v[94:95], v58 offset:27136\n\t" \
  "v_lshlrev_b32 v50, 16, v38\n\t" \
  "v_mfma_f32_16x16x32_bf16 v[228:231], v[68:71], v[104:107], v[228:231]\n\t" \
  "ds_read_b64_tr_b16 v[96:97], v59 offset:26624\n\t" \
  "v_lshlrev_b32 v51, 16, v40\n\t" \
  "v_mfma_f32_16x16x32_bf16 v[236:239], v[72:75], v[104:107], v[236:239]\n\t" \
  "ds_read_b64_tr_b16 v[98:99], v59 offset:27136\n\t" \
  "v_mul_f32 v42, v42, v50\n\t" \
  "v_mfma_f32_16x16x32_bf16 v[244:247], v[76:79], v[104:107], v[244:247]\n\t" \
  "ds_read_b64_tr_b16 v[100:101], v58 offset:34816\n\t" \
  "v_mul_f32 v46, v46, v51\n\t" \
  "v_mfma_f32_16x16x32_bf16 v[152:155], v[84:87], v[64:67], v[152:155]\n\t" \
  "ds_read_b64_tr_b16 v[102:103], v58 offset:35328\n\t" \
  "v_cvt_pk_bf16_f32 v42, v42, v46\n\t" \
  "v_mfma_f32_16x16x32_bf16 v[160:163], v[84:87], v[68:71], v[160:163]\n\t" \
  "500:\n\t" \
  "s_cbranch_execz 504f\n\t" \
  "ds_read_b128 v[80:83], v56 offset:0\n\t" \
  "v_and_b32 v50, 0xffff0000, v38\n\t" \
  "v_mfma_f32_16x16x32_bf16 v[168:171], v[84:87], v[72:75], v[168:171]\n\t" \
  "ds_read_b64_tr_b16 v[104:105], v59 offset:34816\n\t" \
  "v_and_b32 v51, 0xffff0000, v40\n\t" \
  "v_mfma_f32_16x16x32_bf16 v[176:179], v[84:87], v[76:79], v[176:179]\n\t" \
  "ds_read_b64_tr_b16 v[106:107], v59 offset:35328\n\t" \
  "v_mul_f32 v43, v43, v50\n\t" \
  "v_mfma_f32_16x16x32_bf16 v[156:159], v[88:91], v[64:67], v[156:159]\n\t" \
  "ds_read_b128 v[64:67], v56 offset:10240\n\t" \
  "v_mul_f32 v47, v47, v51\n\t" \
  "v_mfma_f32_16x16x32_bf16 v[164:167], v[88:91], v[68:71], v[164:167]\n\t" \
  "ds_read_b128 v[68:71], v56 offset:12288\n\t" \
  "v_cvt_pk_bf16_f32 v43, v43, v47\n\t" \
  "v_mfma_f32_16x16x32_bf16 v[172:175], v[88:91], v[72:75], v[172:175]\n\t" \
  "ds_read_b128 v[72:75], v56 offset:14336\n\t" \
  "v_lshlrev_b32 v50, 16, v39\n\t" \
  "v_mfma_f32_16x16x32_bf16 v[180:183], v[88:91], v[76:79], v[180:183]\n\t" \
  "ds_read_b128 v[76:79], v56 offset:16384\n\t" \
  "v_lshlrev_b32 v51, 16, v41\n\t" \
  "v_mfma_f32_16x16x32_bf16 v[184:187], v[136:139], v[108:111], v[184:187]\n\t" \
  "ds_read_b64_tr_b16 v[84:85], v58 offset:18432\n\t" \
  "v_mul_f32 v44, v44, v50\n\t" \
  "v_mfma_f32_16x16x32_bf16 v[192:195], v[136:139], v[112:115], v[192:195]\n\t" \
  "ds_read_b64_tr_b16 v[86:87], v58 offset:18944\n\t" \
  "v_mul_f32 v48, v48, v51\n\t" \
  "v_mfma_f32_16x16x32_bf16 v[200:203], v[136:139], v[116:119], v[200:203]\n\t" \
  "ds_read_b64_tr_b16 v[88:89], v59 offset:18432\n\t" \
  "v_cvt_pk_bf16_f32 v44, v44, v48\n\t" \
  "v_mfma_f32_16x16x32_bf16 v[208:211], v[136:139], v[120:123], v[208:211]\n\t" \
  "ds_read_b64_tr_b16 v[90:91], v59 offset:18944\n\t" \
  "v_and_b32 v50, 0xffff0000, v39\n\t" \
  "v_mfma_f32_16x16x32_bf16 v[188:191], v[140:143], v[108:111], v[188:191]\n\t" \
  "ds_read_b64_tr_b16 v[136:137], v58 offset:30720\n\t" \
  "v_and_b32 v51, 0xffff0000, v41\n\t" \
  "v_mfma_f32_16x16x32_bf16 v[196:199], v[140:143], v[112:115], v[196:199]\n\t" \
  "ds_read_b64_tr_b16 v[138:139], v58 offset:31232\n\t" \
  "v_mul_f32 v45, v45, v50\n\t" \
  "v_mfma_f32_16x16x32_bf16 v[204:207], v[140:143], v[116:119], v[204:207]\n\t" \
  "v_mul_f32 v49, v49, v51\n\t" \
  "ds_read_b64_tr_b16 v[38:39], v1 offset:800\n\t" \
  "v_mfma_f32_16x16x32_bf16 v[212:215], v[140:143], v[120:123], v[212:215]\n\t" \
  "ds_read_b64_tr_b16 v[140:141], v59 offset:30720\n\t" \
  "ds_read_b64_tr_b16 v[40:41], v1 offset:288\n\t" \
  "v_mfma_f32_16x16x32_bf16 v[248:251], v[124:127], v[144:147], v[248:251]\n\t" \
  "ds_read_b64_tr_b16 v[142:143], v59 offset:31232\n\t" \
  "v_cvt_pk_bf16_f32 v45, v45, v49\n\t" \
  "v_mfma_f32_16x16x32_bf16 v[216:219], v[108:111], v[144:147], v[216:219]\n\t" \
  "v_mfma_f32_16x16x32_bf16 v[224:227], v[112:115], v[144:147], v[224:227]\n\t" \
  "v_mfma_f32_16x16x32_bf16 v[232:235], v[116:119], v[144:147], v[232:235]\n\t" \
  "v_mfma_f32_16x16x32_bf16 v[240:243], v[120:123], v[144:147], v[240:243]\n\t" \
  "ds_read_b64_tr_b16 v[144:145], v58 offset:38912\n\t" \
  "v_mfma_f32_16x16x32_bf16 v[252:255], v[124:127], v[148:151], v[252:255]\n\t" \
  "ds_read_b64_tr_b16 v[146:147], v58 offset:39424\n\t" \
  "v_mfma_f32_16x16x32_bf16 v[220:223], v[108:111], v[148:151], v[220:223]\n\t" \
  "ds_read_b128 v[124:127], v57 offset:0\n\t" \
  "v_mfma_f32_16x16x32_bf16 v[228:231], v[112:115], v[148:151], v[228:231]\n\t" \
  "v_mfma_f32_16x16x32_bf16 v[236:239], v[116:119], v[148:151], v[236:239]\n\t" \
  "v_mfma_f32_16x16x32_bf16 v[244:247], v[120:123], v[148:151], v[244:247]\n\t" \
  "ds_read_b64_tr_b16 v[148:149], v59 offset:38912\n\t" \
  "v_mfma_f32_16x16x32_bf16 v[152:155], v[128:131], v[108:111], v[152:155]\n\t" \
  "ds_read_b64_tr_b16 v[150:151], v59 offset:39424\n\t" \
  "v_mfma_f32_16x16x32_bf16 v[160:163], v[128:131], v[112:115], v[160:163]\n\t" \
  "v_mfma_f32_16x16x32_bf16 v[168:171], v[128:131], v[116:119], v[168:171]\n\t" \
  "v_mfma_f32_16x16x32_bf16 v[176:179], v[128:131], v[120:123], v[176:179]\n\t" \
  "ds_read_b64_tr_b16 v[128:129], v58 offset:22528\n\t" \
  "v_mfma_f32_16x16x32_bf16 v[156:159], v[132:135], v[108:111], v[156:159]\n\t" \
  "ds_read_b128 v[108:111], v57 offset:10240\n\t" \
  "v_mfma_f32_16x16x32_bf16 v[164:167], v[132:135], v[112:115], v[164:167]\n\t" \
  "ds_read_b128 v[112:115], v57 offset:12288\n\t" \
  "v_mfma_f32_16x16x32_bf16 v[172:175], v[132:135], v[116:119], v[172:175]\n\t" \
  "ds_read_b128 v[116:119], v57 offset:14336\n\t" \
  "v_mfma_f32_16x16x32_bf16 v[180:183], v[132:135], v[120:123], v[180:183]\n\t" \
  "ds_read_b128 v[120:123], v57 offset:16384\n\t" \
  "s_waitcnt lgkmcnt(5)\n\t" \
  "v_mfma_f32_16x16x32_bf16 v[184:187], v[92:95], v[64:67], v[184:187]\n\t" \
  "ds_read_b64_tr_b16 v[130:131], v58 offset:23040\n\t" \
  "v_mfma_f32_16x16x32_bf16 v[192:195], v[92:95], v[68:71], v[192:195]\n\t" \
  "ds_read_b64_tr_b16 v[132:133], v59 offset:22528\n\t" \
  "v_mfma_f32_16x16x32_bf16 v[200:203], v[92:95], v[72:75], v[200:203]\n\t" \
  "ds_read_b64_tr_b16 v[134:135], v59 offset:23040\n\t" \
  "v_mfma_f32_16x16x32_bf16 v[208:211], v[92:95], v[76:79], v[208:211]\n\t" \
  "v_mfma_f32_16x16x32_bf16 v[188:191], v[96:99], v[64:67], v[188:191]\n\t" \
  "v_mfma_f32_16x16x32_bf16 v[196:199], v[96:99], v[68:71], v[196:199]\n\t" \
  "v_mfma_f32_16x16x32_bf16 v[204:207], v[96:99], v[72:75], v[204:207]\n\t" \
  "v_mfma_f32_16x16x32_bf16 v[212:215], v[96:99], v[76:79], v[212:215]\n\t" \
  "v_mfma_f32_16x16x32_bf16 v[248:251], v[80:83], v[100:103], v[248:251]\n\t" \
  "v_mfma_f32_16x16x32_bf16 v[216:219], v[64:67], v[100:103], v[216:219]\n\t" \
  "global_store_dwordx4 v2, v[42:45], %24 offset:0 sc1\n\t" \
  "v_mfma_f32_16x16x32_bf16 v[224:227], v[68:71], v[100:103], v[224:227]\n\t" \
  "s_nop 0\n\t" \
  "v_mfma_f32_16x16x32_bf16 v[42:45], v[26:29], v[18:21], 0\n\t" \
  "v_mfma_f32_16x16x32_bf16 v[42:45], v[30:33], v[22:25], v[42:45]\n\t" \
  "v_mfma_f32_16x16x32_bf16 v[46:49], v[30:33], v[18:21], 0\n\t" \
  "v_mfma_f32_16x16x32_bf16 v[46:49], v[34:37], v[22:25], v[46:49]\n\t" \
  "v_mfma_f32_16x16x32_bf16 v[232:235], v[72:75], v[100:103], v[232:235]\n\t" \
  "v_mfma_f32_16x16x32_bf16 v[240:243], v[76:79], v[100:103], v[240:243]\n\t" \
  "v_lshlrev_b32 v50, 16, v38\n\t" \
  "v_lshlrev_b32 v51, 16, v40\n\t" \
  "s_waitcnt lgkmcnt(0)\n\t" \
  "s_barrier\n\t" \
  "v_mfma_f32_16x16x32_bf16 v[252:255], v[80:83], v[104:107], v[252:255]\n\t" \
  "ds_read_b64_tr_b16 v[92:93], v62 offset:26624\n\t" \
  "v_mul_f32 v42, v42, v50\n\t" \
  "v_mfma_f32_16x16x32_bf16 v[220:223], v[64:67], v[104:107], v[220:223]\n\t" \
  "ds_read_b64_tr_b16 v[94:95], v62 offset:27136\n\t" \
  "v_mul_f32 v46, v46, v51\n\t" \
  "v_mfma_f32_16x16x32_bf16 v[228:231], v[68:71], v[104:107], v[228:231]\n\t" \
  "ds_read_b64_tr_b16 v[96:97], v63 offset:26624\n\t" \
  "v_cvt_pk_bf16_f32 v42, v42, v46\n\t" \
  "v_mfma_f32_16x16x32_bf16 v[236:239], v[72:75], v[104:107], v[236:239]\n\t" \
  "ds_read_b64_tr_b16 v[98:99], v63 offset:27136\n\t" \
  "v_and_b32 v50, 0xffff0000, v38\n\t" \
  "v_mfma_f32_16x16x32_bf16 v[244:247], v[76:79], v[104:107], v[244:247]\n\t" \
  "ds_read_b64_tr_b16 v[100:101], v62 offset:34816\n\t" \
  "v_and_b32 v51, 0xffff0000, v40\n\t" \
  "v_mfma_f32_16x16x32_bf16 v[152:155], v[84:87], v[64:67], v[152:155]\n\t" \
  "ds_read_b64_tr_b16 v[102:103], v62 offset:35328\n\t" \
  "v_mul_f32 v43, v43, v50\n\t" \
  "v_mfma_f32_16x16x32_bf16 v[160:163], v[84:87], v[68:71], v[160:163]\n\t" \
  "ds_read_b128 v[80:83], v60 offset:0\n\t" \
  "v_mul_f32 v47, v47, v51\n\t" \
  "v_mfma_f32_16x16x32_bf16 v[168:171], v[84:87], v[72:75], v[168:171]\n\t" \
  "ds_read_b64_tr_b16 v[104:105], v63 offset:34816\n\t" \
  "v_cvt_pk_bf16_f32 v43, v43, v47\n\t" \
  "v_mfma_f32_16x16x32_bf16 v[176:179], v[84:87], v[76:79], v[176:179]\n\t" \
  "ds_read_b64_tr_b16 v[106:107], v63 offset:35328\n\t" \
  "v_lshlrev_b32 v50, 16, v39\n\t" \
  "v_mfma_f32_16x16x32_bf16 v[156:159], v[88:91], v[64:67], v[156:159]\n\t" \
  "ds_read_b128 v[64:67], v60 offset:10240\n\t" \
  "v_lshlrev_b32 v51, 16, v41\n\t" \
  "v_mfma_f32_16x16x32_bf16 v[164:167], v[88:91], v[68:71], v[164:167]\n\t" \
  "ds_read_b128 v[68:71], v60 offset:12288\n\t" \
  "v_mul_f32 v44, v44, v50\n\t" \
  "v_mfma_f32_16x16x32_bf16 v[172:175], v[88:91], v[72:75], v[172:175]\n\t" \
  "ds_read_b128 v[72:75], v60 offset:14336\n\t" \
  "v_mul_f32 v48, v48, v51\n\t" \
  "v_mfma_f32_16x16x32_bf16 v[180:183], v[88:91], v[76:79], v[180:183]\n\t" \
  "ds_read_b128 v[76:79], v60 offset:16384\n\t" \
  "v_cvt_pk_bf16_f32 v44, v44, v48\n\t" \
  "v_mfma_f32_16x16x32_bf16 v[184:187], v[136:139], v[108:111], v[184:187]\n\t" \
  "ds_read_b64_tr_b16 v[84:85], v62 offset:18432\n\t" \
  "v_and_b32 v50, 0xffff0000, v39\n\t" \
  "v_mfma_f32_16x16x32_bf16 v[192:195], v[136:139], v[112:115], v[192:195]\n\t" \
  "ds_read_b64_tr_b16 v[86:87], v62 offset:18944\n\t" \
  "v_and_b32 v51, 0xffff0000, v41\n\t" \
  "v_mfma_f32_16x16x32_bf16 v[200:203], v[136:139], v[116:119], v[200:203]\n\t" \
  "ds_read_b64_tr_b16 v[88:89], v63 offset:18432\n\t" \
  "v_mul_f32 v45, v45, v50\n\t" \
  "v_mfma_f32_16x16x32_bf16 v[208:211], v[136:139], v[120:123], v[208:211]\n\t" \
  "ds_read_b64_tr_b16 v[90:91], v63 offset:18944\n\t" \
  "v_mul_f32 v49, v49, v51\n\t" \
  "v_mfma_f32_16x16x32_bf16 v[188:191], v[140:143], v[108:111], v[188:191]\n\t" \
  "ds_read_b64_tr_b16 v[136:137], v62 offset:30720\n\t" \
  "v_cvt_pk_bf16_f32 v45, v45, v49\n\t" \
  "v_mfma_f32_16x16x32_bf16 v[196:199], v[140:143], v[112:115], v[196:199]\n\t" \
  "ds_read_b64_tr_b16 v[138:139], v62 offset:31232\n\t" \
  "v_mfma_f32_16x16x32_bf16 v[204:207], v[140:143], v[116:119], v[204:207]\n\t" \
  "v_mfma_f32_16x16x32_bf16 v[212:215], v[140:143], v[120:123], v[212:215]\n\t" \
  "ds_read_b64_tr_b16 v[140:141], v63 offset:30720\n\t" \
  "v_mfma_f32_16x16x32_bf16 v[248:251], v[124:127], v[144:147], v[248:251]\n\t" \
  "ds_read_b64_tr_b16 v[142:143], v63 offset:31232\n\t" \
  "v_mfma_f32_16x16x32_bf16 v[216:219], v[108:111], v[144:147], v[216:219]\n\t" \
  "v_mfma_f32_16x16x32_bf16 v[224:227], v[112:115], v[144:147], v[224:227]\n\t" \
  "v_mfma_f32_16x16x32_bf16 v[232:235], v[116:119], v[144:147], v[232:235]\n\t" \
  "v_mfma_f32_16x16x32_bf16 v[240:243], v[120:123], v[144:147], v[240:243]\n\t" \
  "ds_read_b64_tr_b16 v[144:145], v62 offset:38912\n\t" \
  "v_mfma_f32_16x16x32_bf16 v[252:255], v[124:127], v[148:151], v[252:255]\n\t" \
  "ds_read_b64_tr_b16 v[146:147], v62 offset:39424\n\t" \
  "v_mfma_f32_16x16x32_bf16 v[220:223], v[108:111], v[148:151], v[220:223]\n\t" \
  "ds_read_b128 v[124:127], v61 offset:0\n\t" \
  "v_mfma_f32_16x16x32_bf16 v[228:231], v[112:115], v[148:151], v[228:231]\n\t" \
  "v_mfma_f32_16x16x32_bf16 v[236:239], v[116:119], v[148:151], v[236:239]\n\t" \
  "v_mfma_f32_16x16x32_bf16 v[244:247], v[120:123], v[148:151], v[244:247]\n\t" \
  "ds_read_b64_tr_b16 v[148:149], v63 offset:38912\n\t" \
  "v_mfma_f32_16x16x32_bf16 v[152:155], v[128:131], v[108:111], v[152:155]\n\t" \
  "ds_read_b64_tr_b16 v[150:151], v63 offset:39424\n\t" \
  "v_mfma_f32_16x16x32_bf16 v[160:163], v[128:131], v[112:115], v[160:163]\n\t" \
  "v_mfma_f32_16x16x32_bf16 v[168:171], v[128:131], v[116:119], v[168:171]\n\t" \
  "v_mfma_f32_16x16x32_bf16 v[176:179], v[128:131], v[120:123], v[176:179]\n\t" \
  "ds_read_b64_tr_b16 v[128:129], v62 offset:22528\n\t" \
  "v_mfma_f32_16x16x32_bf16 v[156:159], v[132:135], v[108:111], v[156:159]\n\t" \
  "ds_read_b128 v[108:111], v61 offset:10240\n\t" \
  "v_mfma_f32_16x16x32_bf16 v[164:167], v[132:135], v[112:115], v[164:167]\n\t" \
  "ds_read_b128 v[112:115], v61 offset:12288\n\t" \
  "v_mfma_f32_16x16x32_bf16 v[172:175], v[132:135], v[116:119], v[172:175]\n\t" \
  "ds_read_b128 v[116:119], v61 offset:14336\n\t" \
  "v_mfma_f32_16x16x32_bf16 v[180:183], v[132:135], v[120:123], v[180:183]\n\t" \
  "ds_read_b128 v[120:123], v61 offset:16384\n\t" \
  "s_waitcnt lgkmcnt(5)\n\t" \
  "v_mfma_f32_16x16x32_bf16 v[184:187], v[92:95], v[64:67], v[184:187]\n\t" \
  "ds_read_b64_tr_b16 v[130:131], v62 offset:23040\n\t" \
  "v_mfma_f32_16x16x32_bf16 v[192:195], v[92:95], v[68:71], v[192:195]\n\t" \
  "ds_read_b64_tr_b16 v[132:133], v63 offset:22528\n\t" \
  "v_mfma_f32_16x16x32_bf16 v[200:203], v[92:95], v[72:75], v[200:203]\n\t" \
  "ds_read_b64_tr_b16 v[134:135], v63 offset:23040\n\t" \
  "v_mfma_f32_16x16x32_bf16 v[208:211], v[92:95], v[76:79], v[208:211]\n\t" \
  "v_mfma_f32_16x16x32_bf16 v[188:191], v[96:99], v[64:67], v[188:191]\n\t" \
  "v_mfma_f32_16x16x32_bf16 v[196:199], v[96:99], v[68:71], v[196:199]\n\t" \
  "v_mfma_f32_16x16x32_bf16 v[204:207], v[96:99], v[72:75], v[204:207]\n\t" \
  "v_mfma_f32_16x16x32_bf16 v[212:215], v[96:99], v[76:79], v[212:215]\n\t" \
  "v_mfma_f32_16x16x32_bf16 v[248:251], v[80:83], v[100:103], v[248:251]\n\t" \
  "v_mfma_f32_16x16x32_bf16 v[216:219], v[64:67], v[100:103], v[216:219]\n\t" \
  "global_store_dwordx4 v2, v[42:45], %24 offset:1024 sc1\n\t" \
  "v_mfma_f32_16x16x32_bf16 v[224:227], v[68:71], v[100:103], v[224:227]\n\t" \
  "v_mfma_f32_16x16x32_bf16 v[232:235], v[72:75], v[100:103], v[232:235]\n\t" \
  "v_mfma_f32_16x16x32_bf16 v[240:243], v[76:79], v[100:103], v[240:243]\n\t" \
  "v_mov_b32 v30, 0xff61b1e6\n\t" \
  "v_bfe_i32 v34, v3, 0, 1\n\t" \
  "s_waitcnt lgkmcnt(0)\n\t" \
  "s_barrier\n\t" \
  "v_mfma_f32_16x16x32_bf16 v[252:255], v[80:83], v[104:107], v[252:255]\n\t" \
  "ds_read_b64_tr_b16 v[92:93], v54 offset:26624\n\t" \
  "v_and_b32 v34, v30, v34\n\t" \
  "v_mfma_f32_16x16x32_bf16 v[220:223], v[64:67], v[104:107], v[220:223]\n\t" \
  "ds_read_b64_tr_b16 v[94:95], v54 offset:27136\n\t" \
  "v_bfe_i32 v38, v3, 4, 1\n\t" \
  "v_mfma_f32_16x16x32_bf16 v[228:231], v[68:71], v[104:107], v[228:231]\n\t" \
  "ds_read_b64_tr_b16 v[96:97], v55 offset:26624\n\t" \
  "v_and_b32 v38, v30, v38\n\t" \
  "v_mfma_f32_16x16x32_bf16 v[236:239], v[72:75], v[104:107], v[236:239]\n\t" \
  "ds_read_b64_tr_b16 v[98:99], v55 offset:27136\n\t" \
  "v_bfe_i32 v42, v3, 8, 1\n\t" \
  "v_mfma_f32_16x16x32_bf16 v[244:247], v[76:79], v[104:107], v[244:247]\n\t" \
  "ds_read_b64_tr_b16 v[100:101], v54 offset:34816\n\t" \
  "v_bfi_b32 v42, v42, v30, %15\n\t" \
  "v_mfma_f32_16x16x32_bf16 v[152:155], v[84:87], v[64:67], v[152:155]\n\t" \
  "ds_read_b64_tr_b16 v[102:103], v54 offset:35328\n\t" \
  "v_mov_b32 v48, 0x3f803f80\n\t" \
  "v_mfma_f32_16x16x32_bf16 v[160:163], v[84:87], v[68:71], v[160:163]\n\t" \
  "ds_read_b128 v[80:83], v52 offset:0\n\t" \
  "v_bfe_i32 v35, v3, 1, 1\n\t" \
  "v_mfma_f32_16x16x32_bf16 v[168:171], v[84:87], v[72:75], v[168:171]\n\t" \
  "ds_read_b64_tr_b16 v[104:105], v55 offset:34816\n\t" \
  "v_and_b32 v35, v30, v35\n\t" \
  "v_mfma_f32_16x16x32_bf16 v[176:179], v[84:87], v[76:79], v[176:179]\n\t" \
  "ds_read_b64_tr_b16 v[106:107], v55 offset:35328\n\t" \
  "v_bfe_i32 v39, v3, 5, 1\n\t" \
  "v_mfma_f32_16x16x32_bf16 v[156:159], v[88:91], v[64:67], v[156:159]\n\t" \
  "ds_read_b128 v[64:67], v52 offset:10240\n\t" \
  "v_and_b32 v39, v30, v39\n\t" \
  "v_mfma_f32_16x16x32_bf16 v[164:167], v[88:91], v[68:71], v[164:167]\n\t" \
  "ds_read_b128 v[68:71], v52 offset:12288\n\t" \
  "v_bfe_i32 v43, v3, 9, 1\n\t" \
  "v_mfma_f32_16x16x32_bf16 v[172:175], v[88:91], v[72:75], v[172:175]\n\t" \
  "ds_read_b128 v[72:75], v52 offset:14336\n\t" \
  "v_bfi_b32 v43, v43, v30, %15\n\t" \
  "v_mfma_f32_16x16x32_bf16 v[180:183], v[88:91], v[76:79], v[180:183]\n\t" \
  "ds_read_b128 v[76:79], v52 offset:16384\n\t" \
  "v_mov_b32 v49, 0x3f803f80\n\t" \
  "v_mfma_f32_16x16x32_bf16 v[184:187], v[136:139], v[108:111], v[184:187]\n\t" \
  "ds_read_b64_tr_b16 v[84:85], v54 offset:18432\n\t" \
  "v_bfe_i32 v36, v3, 2, 1\n\t" \
  "v_mfma_f32_16x16x32_bf16 v[192:195], v[136:139], v[112:115], v[192:195]\n\t" \
  "ds_read_b64_tr_b16 v[86:87], v54 offset:18944\n\t" \
  "v_and_b32 v36, v30, v36\n\t" \
  "v_mfma_f32_16x16x32_bf16 v[200:203], v[136:139], v[116:119], v[200:203]\n\t" \
  "ds_read_b64_tr_b16 v[88:89], v55 offset:18432\n\t" \
  "v_bfe_i32 v40, v3, 6, 1\n\t" \
  "v_mfma_f32_16x16x32_bf16 v[208:211], v[136:139], v[120:123], v[208:211]\n\t" \
  "ds_read_b64_tr_b16 v[90:91], v55 offset:18944\n\t" \
  "v_and_b32 v40, v30, v40\n\t" \
  "v_mfma_f32_16x16x32_bf16 v[188:191], v[140:143], v[108:111], v[188:191]\n\t" \
  "ds_read_b64_tr_b16 v[136:137], v54 offset:30720\n\t" \
  "v_bfe_i32 v44, v3, 10, 1\n\t" \
  "v_mfma_f32_16x16x32_bf16 v[196:199], v[140:143], v[112:115], v[196:199]\n\t" \
  "ds_read_b64_tr_b16 v[138:139], v54 offset:31232\n\t" \
  "v_bfi_b32 v44, v44, v30, %15\n\t" \
  "v_mfma_f32_16x16x32_bf16 v[204:207], v[140:143], v[116:119], v[204:207]\n\t" \
  "v_mov_b32 v50, 0x3f803f80\n\t" \
  "v_bfe_i32 v37, v3, 3, 1\n\t" \
  "v_mfma_f32_16x16x32_bf16 v[212:215], v[140:143], v[120:123], v[212:215]\n\t" \
  "ds_read_b64_tr_b16 v[140:141], v55 offset:30720\n\t" \
  "v_and_b32 v37, v30, v37\n\t" \
  "v_mfma_f32_16x16x32_bf16 v[248:251], v[124:127], v[144:147], v[248:251]\n\t" \
  "ds_read_b64_tr_b16 v[142:143], v55 offset:31232\n\t" \
  "v_bfe_i32 v41, v3, 7, 1\n\t" \
  "v_mfma_f32_16x16x32_bf16 v[216:219], v[108:111], v[144:147], v[216:219]\n\t" \
  "v_and_b32 v41, v30, v41\n\t" \
  "v_bfe_i32 v45, v3, 11, 1\n\t" \
  "v_mfma_f32_16x16x32_bf16 v[224:227], v[112:115], v[144:147], v[224:227]\n\t" \
  "v_bfi_b32 v45, v45, v30, %15\n\t" \
  "v_mov_b32 v51, 0x3f803f80\n\t" \
  "v_mfma_f32_16x16x32_bf16 v[232:235], v[116:119], v[144:147], v[232:235]\n\t" \
  "v_bfe_i32 v46, v3, 12, 1\n\t" \
  "v_bfe_i32 v31, v3, 13, 1\n\t" \
  "v_mfma_f32_16x16x32_bf16 v[240:243], v[120:123], v[144:147], v[240:243]\n\t" \
  "ds_read_b64_tr_b16 v[144:145], v54 offset:38912\n\t" \
  "v_perm_b32 v46, v31, v46, vcc_hi\n\t" \
  "v_mfma_f32_16x16x32_bf16 v[252:255], v[124:127], v[148:151], v[252:255]\n\t" \
  "ds_read_b64_tr_b16 v[146:147], v54 offset:39424\n\t" \
  "v_bfe_i32 v47, v3, 14, 1\n\t" \
  "v_mfma_f32_16x16x32_bf16 v[220:223], v[108:111], v[148:151], v[220:223]\n\t" \
  "ds_read_b128 v[124:127], v53 offset:0\n\t" \
  "v_bfe_i32 v31, v3, 15, 1\n\t" \
  "v_mfma_f32_16x16x32_bf16 v[228:231], v[112:115], v[148:151], v[228:231]\n\t" \
  "v_perm_b32 v47, v31, v47, vcc_hi\n\t" \
  "v_add_u32 v30, 18432, v4\n\t" \
  "v_mfma_f32_16x16x32_bf16 v[236:239], v[116:119], v[148:151], v[236:239]\n\t" \
  "v_xor_b32 v31, 1024, v30\n\t" \
  "v_mov_b32 v32, %12\n\t" \
  "v_mfma_f32_16x16x32_bf16 v[244:247], v[120:123], v[148:151], v[244:247]\n\t" \
  "ds_read_b64_tr_b16 v[148:149], v55 offset:38912\n\t" \
  "v_xor_b32 v33, 4, v32\n\t" \
  "v_mfma_f32_16x16x32_bf16 v[152:155], v[128:131], v[108:111], v[152:155]\n\t" \
  "ds_read_b64_tr_b16 v[150:151], v55 offset:39424\n\t" \
  "v_mov_b32 v0, 1\n\t" \
  "v_mfma_f32_16x16x32_bf16 v[160:163], v[128:131], v[112:115], v[160:163]\n\t" \
  "v_and_b32 v4, 1023, v4\n\t" \
  "v_add_u32 v1, 8192, v4\n\t" \
  "v_mfma_f32_16x16x32_bf16 v[168:171], v[128:131], v[116:119], v[168:171]\n\t" \
  "v_mfma_f32_16x16x32_bf16 v[176:179], v[128:131], v[120:123], v[176:179]\n\t" \
  "ds_read_b64_tr_b16 v[128:129], v54 offset:22528\n\t" \
  "v_mfma_f32_16x16x32_bf16 v[156:159], v[132:135], v[108:111], v[156:159]\n\t" \
  "ds_read_b128 v[108:111], v53 offset:10240\n\t" \
  "v_mfma_f32_16x16x32_bf16 v[164:167], v[132:135], v[112:115], v[164:167]\n\t" \
  "ds_read_b128 v[112:115], v53 offset:12288\n\t" \
  "v_mfma_f32_16x16x32_bf16 v[172:175], v[132:135], v[116:119], v[172:175]\n\t" \
  "ds_read_b128 v[116:119], v53 offset:14336\n\t" \
  "v_mfma_f32_16x16x32_bf16 v[180:183], v[132:135], v[120:123], v[180:183]\n\t" \
  "ds_read_b128 v[120:123], v53 offset:16384\n\t" \
  "s_waitcnt lgkmcnt(5)\n\t" \
  "v_mfma_f32_16x16x32_bf16 v[184:187], v[92:95], v[64:67], v[184:187]\n\t" \
  "ds_read_b64_tr_b16 v[130:131], v54 offset:23040\n\t" \
  "v_mfma_f32_16x16x32_bf16 v[192:195], v[92:95], v[68:71], v[192:195]\n\t" \
  "ds_read_b64_tr_b16 v[132:133], v55 offset:22528\n\t" \
  "v_mfma_f32_16x16x32_bf16 v[200:203], v[92:95], v[72:75], v[200:203]\n\t" \
  "ds_read_b64_tr_b16 v[134:135], v55 offset:23040\n\t" \
  "v_mfma_f32_16x16x32_bf16 v[208:211], v[92:95], v[76:79], v[208:211]\n\t" \
  "v_mfma_f32_16x16x32_bf16 v[188:191], v[96:99], v[64:67], v[188:191]\n\t" \
  "v_mfma_f32_16x16x32_bf16 v[196:199], v[96:99], v[68:71], v[196:199]\n\t" \
  "v_mfma_f32_16x16x32_bf16 v[204:207], v[96:99], v[72:75], v[204:207]\n\t" \
  "v_mfma_f32_16x16x32_bf16 v[212:215], v[96:99], v[76:79], v[212:215]\n\t" \
  "v_mfma_f32_16x16x32_bf16 v[248:251], v[80:83], v[100:103], v[248:251]\n\t" \
  "v_mfma_f32_16x16x32_bf16 v[216:219], v[64:67], v[100:103], v[216:219]\n\t" \
  "v_mfma_f32_16x16x32_bf16 v[224:227], v[68:71], v[100:103], v[224:227]\n\t" \
  "v_mfma_f32_16x16x32_bf16 v[232:235], v[72:75], v[100:103], v[232:235]\n\t" \
  "v_mfma_f32_16x16x32_bf16 v[240:243], v[76:79], v[100:103], v[240:243]\n\t" \
  "s_waitcnt lgkmcnt(0)\n\t" \
  "s_barrier\n\t" \
  "v_mfma_f32_16x16x32_bf16 v[252:255], v[80:83], v[104:107], v[252:255]\n\t" \
  "ds_read_b64_tr_b16 v[92:93], v58 offset:26624\n\t" \
  "v_mfma_f32_16x16x32_bf16 v[220:223], v[64:67], v[104:107], v[220:223]\n\t" \
  "ds_read_b64_tr_b16 v[94:95], v58 offset:27136\n\t" \
  "v_mfma_f32_16x16x32_bf16 v[228:231], v[68:71], v[104:107], v[228:231]\n\t" \
  "ds_read_b64_tr_b16 v[96:97], v59 offset:26624\n\t" \
  "v_mfma_f32_16x16x32_bf16 v[236:239], v[72:75], v[104:107], v[236:239]\n\t" \
  "ds_read_b64_tr_b16 v[98:99], v59 offset:27136\n\t" \
  "v_mfma_f32_16x16x32_bf16 v[244:247], v[76:79], v[104:107], v[244:247]\n\t" \
  "ds_read_b64_tr_b16 v[100:101], v58 offset:34816\n\t" \
  "v_mfma_f32_16x16x32_bf16 v[152:155], v[84:87], v[64:67], v[152:155]\n\t" \
  "ds_read_b64_tr_b16 v[102:103], v58 offset:35328\n\t" \
  "v_mfma_f32_16x16x32_bf16 v[160:163], v[84:87], v[68:71], v[160:163]\n\t" \
  "ds_read_b128 v[80:83], v56 offset:0\n\t" \
  "v_mfma_f32_16x16x32_bf16 v[168:171], v[84:87], v[72:75], v[168:171]\n\t" \
  "ds_read_b64_tr_b16 v[104:105], v59 offset:34816\n\t" \
  "v_mfma_f32_16x16x32_bf16 v[176:179], v[84:87], v[76:79], v[176:179]\n\t" \
  "ds_read_b64_tr_b16 v[106:107], v59 offset:35328\n\t" \
  "v_mfma_f32_16x16x32_bf16 v[156:159], v[88:91], v[64:67], v[156:159]\n\t" \
  "ds_read_b128 v[64:67], v56 offset:10240\n\t" \
  "v_mfma_f32_16x16x32_bf16 v[164:167], v[88:91], v[68:71], v[164:167]\n\t" \
  "ds_read_b128 v[68:71], v56 offset:12288\n\t" \
  "v_mfma_f32_16x16x32_bf16 v[172:175], v[88:91], v[72:75], v[172:175]\n\t" \
  "ds_read_b128 v[72:75], v56 offset:14336\n\t" \
  "v_mfma_f32_16x16x32_bf16 v[180:183], v[88:91], v[76:79], v[180:183]\n\t" \
  "ds_read_b128 v[76:79], v56 offset:16384\n\t" \
  "v_mfma_f32_16x16x32_bf16 v[184:187], v[136:139], v[108:111], v[184:187]\n\t" \
  "ds_read_b64_tr_b16 v[84:85], v58 offset:18432\n\t" \
  "v_mfma_f32_16x16x32_bf16 v[192:195], v[136:139], v[112:115], v[192:195]\n\t" \
  "ds_read_b64_tr_b16 v[86:87], v58 offset:18944\n\t" \
  "v_mfma_f32_16x16x32_bf16 v[200:203], v[136:139], v[116:119], v[200:203]\n\t" \
  "ds_read_b64_tr_b16 v[88:89], v59 offset:18432\n\t" \
  "v_mfma_f32_16x16x32_bf16 v[208:211], v[136:139], v[120:123], v[208:211]\n\t" \
  "ds_read_b64_tr_b16 v[90:91], v59 offset:18944\n\t" \
  "v_mfma_f32_16x16x32_bf16 v[188:191], v[140:143], v[108:111], v[188:191]\n\t" \
  "ds_read_b64_tr_b16 v[136:137], v58 offset:30720\n\t" \
  "v_mfma_f32_16x16x32_bf16 v[196:199], v[140:143], v[112:115], v[196:199]\n\t" \
  "ds_read_b64_tr_b16 v[138:139], v58 offset:31232\n\t" \
  "v_mfma_f32_16x16x32_bf16 v[204:207], v[140:143], v[116:119], v[204:207]\n\t" \
  "v_mfma_f32_16x16x32_bf16 v[212:215], v[140:143], v[120:123], v[212:215]\n\t" \
  "ds_read_b64_tr_b16 v[140:141], v59 offset:30720\n\t" \
  "v_mfma_f32_16x16x32_bf16 v[248:251], v[124:127], v[144:147], v[248:251]\n\t" \
  "ds_read_b64_tr_b16 v[142:143], v59 offset:31232\n\t" \
  "v_mfma_f32_16x16x32_bf16 v[216:219], v[108:111], v[144:147], v[216:219]\n\t" \
  "v_mfma_f32_16x16x32_bf16 v[224:227], v[112:115], v[144:147], v[224:227]\n\t" \
  "v_mfma_f32_16x16x32_bf16 v[232:235], v[116:119], v[144:147], v[232:235]\n\t" \
  "v_mfma_f32_16x16x32_bf16 v[240:243], v[120:123], v[144:147], v[240:243]\n\t" \
  "ds_read_b64_tr_b16 v[144:145], v58 offset:38912\n\t" \
  "v_mfma_f32_16x16x32_bf16 v[252:255], v[124:127], v[148:151], v[252:255]\n\t" \
  "ds_read_b64_tr_b16 v[146:147], v58 offset:39424\n\t" \
  "v_mfma_f32_16x16x32_bf16 v[220:223], v[108:111], v[148:151], v[220:223]\n\t" \
  "ds_read_b128 v[124:127], v57 offset:0\n\t" \
  "v_mfma_f32_16x16x32_bf16 v[228:231], v[112:115], v[148:151], v[228:231]\n\t" \
  "v_mfma_f32_16x16x32_bf16 v[236:239], v[116:119], v[148:151], v[236:239]\n\t" \
  "v_mfma_f32_16x16x32_bf16 v[244:247], v[120:123], v[148:151], v[244:247]\n\t" \
  "ds_read_b64_tr_b16 v[148:149], v59 offset:38912\n\t" \
  "v_mfma_f32_16x16x32_bf16 v[152:155], v[128:131], v[108:111], v[152:155]\n\t" \
  "ds_read_b64_tr_b16 v[150:151], v59 offset:39424\n\t" \
  "v_mfma_f32_16x16x32_bf16 v[160:163], v[128:131], v[112:115], v[160:163]\n\t" \
  "v_mfma_f32_16x16x32_bf16 v[168:171], v[128:131], v[116:119], v[168:171]\n\t" \
  "v_mfma_f32_16x16x32_bf16 v[176:179], v[128:131], v[120:123], v[176:179]\n\t" \
  "ds_read_b64_tr_b16 v[128:129], v58 offset:22528\n\t" \
  "v_mfma_f32_16x16x32_bf16 v[156:159], v[132:135], v[108:111], v[156:159]\n\t" \
  "ds_read_b128 v[108:111], v57 offset:10240\n\t" \
  "v_mfma_f32_16x16x32_bf16 v[164:167], v[132:135], v[112:115], v[164:167]\n\t" \
  "ds_read_b128 v[112:115], v57 offset:12288\n\t" \
  "v_mfma_f32_16x16x32_bf16 v[172:175], v[132:135], v[116:119], v[172:175]\n\t" \
  "ds_read_b128 v[116:119], v57 offset:14336\n\t" \
  "v_mfma_f32_16x16x32_bf16 v[180:183], v[132:135], v[120:123], v[180:183]\n\t" \
  "ds_read_b128 v[120:123], v57 offset:16384\n\t" \
  "s_waitcnt lgkmcnt(5)\n\t" \
  "v_mfma_f32_16x16x32_bf16 v[184:187], v[92:95], v[64:67], v[184:187]\n\t" \
  "ds_read_b64_tr_b16 v[130:131], v58 offset:23040\n\t" \
  "v_mfma_f32_16x16x32_bf16 v[192:195], v[92:95], v[68:71], v[192:195]\n\t" \
  "ds_read_b64_tr_b16 v[132:133], v59 offset:22528\n\t" \
  "v_mfma_f32_16x16x32_bf16 v[200:203], v[92:95], v[72:75], v[200:203]\n\t" \
  "ds_read_b64_tr_b16 v[134:135], v59 offset:23040\n\t" \
  "v_mfma_f32_16x16x32_bf16 v[208:211], v[92:95], v[76:79], v[208:211]\n\t" \
  "v_mfma_f32_16x16x32_bf16 v[188:191], v[96:99], v[64:67], v[188:191]\n\t" \
  "v_mfma_f32_16x16x32_bf16 v[196:199], v[96:99], v[68:71], v[196:199]\n\t" \
  "v_mfma_f32_16x16x32_bf16 v[204:207], v[96:99], v[72:75], v[204:207]\n\t" \
  "v_mfma_f32_16x16x32_bf16 v[212:215], v[96:99], v[76:79], v[212:215]\n\t" \
  "v_mfma_f32_16x16x32_bf16 v[248:251], v[80:83], v[100:103], v[248:251]\n\t" \
  "v_mfma_f32_16x16x32_bf16 v[216:219], v[64:67], v[100:103], v[216:219]\n\t" \
  "v_mfma_f32_16x16x32_bf16 v[224:227], v[68:71], v[100:103], v[224:227]\n\t" \
  "v_mfma_f32_16x16x32_bf16 v[232:235], v[72:75], v[100:103], v[232:235]\n\t" \
  "v_mfma_f32_16x16x32_bf16 v[240:243], v[76:79], v[100:103], v[240:243]\n\t" \
  "s_waitcnt lgkmcnt(0)\n\t" \
  "s_waitcnt vmcnt(0)\n\t" \
  "s_barrier\n\t" \
  "v_mfma_f32_16x16x32_bf16 v[252:255], v[80:83], v[104:107], v[252:255]\n\t" \
  "ds_read_b64_tr_b16 v[92:93], v62 offset:26624\n\t" \
  "v_mfma_f32_16x16x32_bf16 v[220:223], v[64:67], v[104:107], v[220:223]\n\t" \
  "ds_read_b64_tr_b16 v[94:95], v62 offset:27136\n\t" \
  "v_mfma_f32_16x16x32_bf16 v[228:231], v[68:71], v[104:107], v[228:231]\n\t" \
  "ds_read_b64_tr_b16 v[96:97], v63 offset:26624\n\t" \
  "v_mfma_f32_16x16x32_bf16 v[236:239], v[72:75], v[104:107], v[236:239]\n\t" \
  "ds_read_b64_tr_b16 v[98:99], v63 offset:27136\n\t" \
  "v_mfma_f32_16x16x32_bf16 v[244:247], v[76:79], v[104:107], v[244:247]\n\t" \
  "ds_read_b64_tr_b16 v[100:101], v62 offset:34816\n\t" \
  "v_mfma_f32_16x16x32_bf16 v[152:155], v[84:87], v[64:67], v[152:155]\n\t" \
  "ds_read_b64_tr_b16 v[102:103], v62 offset:35328\n\t" \
  "v_mfma_f32_16x16x32_bf16 v[160:163], v[84:87], v[68:71], v[160:163]\n\t" \
  "ds_read_b128 v[80:83], v60 offset:0\n\t" \
  "v_mfma_f32_16x16x32_bf16 v[168:171], v[84:87], v[72:75], v[168:171]\n\t" \
  "ds_read_b64_tr_b16 v[104:105], v63 offset:34816\n\t" \
  "v_mfma_f32_16x16x32_bf16 v[176:179], v[84:87], v[76:79], v[176:179]\n\t" \
  "ds_read_b64_tr_b16 v[106:107], v63 offset:35328\n\t" \
  "v_mfma_f32_16x16x32_bf16 v[156:159], v[88:91], v[64:67], v[156:159]\n\t" \
  "ds_read_b128 v[64:67], v60 offset:10240\n\t" \
  "v_mfma_f32_16x16x32_bf16 v[164:167], v[88:91], v[68:71], v[164:167]\n\t" \
  "ds_read_b128 v[68:71], v60 offset:12288\n\t" \
  "v_mfma_f32_16x16x32_bf16 v[172:175], v[88:91], v[72:75], v[172:175]\n\t" \
  "ds_read_b128 v[72:75], v60 offset:14336\n\t" \
  "v_mfma_f32_16x16x32_bf16 v[180:183], v[88:91], v[76:79], v[180:183]\n\t" \
  "ds_read_b128 v[76:79], v60 offset:16384\n\t" \
  "v_mfma_f32_16x16x32_bf16 v[184:187], v[136:139], v[108:111], v[184:187]\n\t" \
  "ds_read_b64_tr_b16 v[84:85], v62 offset:18432\n\t" \
  "v_mfma_f32_16x16x32_bf16 v[192:195], v[136:139], v[112:115], v[192:195]\n\t" \
  "ds_read_b64_tr_b16 v[86:87], v62 offset:18944\n\t" \
  "v_mfma_f32_16x16x32_bf16 v[200:203], v[136:139], v[116:119], v[200:203]\n\t" \
  "ds_read_b64_tr_b16 v[88:89], v63 offset:18432\n\t" \
  "v_mfma_f32_16x16x32_bf16 v[208:211], v[136:139], v[120:123], v[208:211]\n\t" \
  "ds_read_b64_tr_b16 v[90:91], v63 offset:18944\n\t" \
  "v_mfma_f32_16x16x32_bf16 v[188:191], v[140:143], v[108:111], v[188:191]\n\t" \
  "ds_read_b64_tr_b16 v[136:137], v62 offset:30720\n\t" \
  "v_mfma_f32_16x16x32_bf16 v[196:199], v[140:143], v[112:115], v[196:199]\n\t" \
  "ds_read_b64_tr_b16 v[138:139], v62 offset:31232\n\t" \
  "v_mfma_f32_16x16x32_bf16 v[204:207], v[140:143], v[116:119], v[204:207]\n\t" \
  "v_mfma_f32_16x16x32_bf16 v[212:215], v[140:143], v[120:123], v[212:215]\n\t" \
  "ds_read_b64_tr_b16 v[140:141], v63 offset:30720\n\t" \
  "v_mfma_f32_16x16x32_bf16 v[248:251], v[124:127], v[144:147], v[248:251]\n\t" \
  "ds_read_b64_tr_b16 v[142:143], v63 offset:31232\n\t" \
  "v_mfma_f32_16x16x32_bf16 v[216:219], v[108:111], v[144:147], v[216:219]\n\t" \
  "v_mfma_f32_16x16x32_bf16 v[224:227], v[112:115], v[144:147], v[224:227]\n\t" \
  "v_mfma_f32_16x16x32_bf16 v[232:235], v[116:119], v[144:147], v[232:235]\n\t" \
  "v_mfma_f32_16x16x32_bf16 v[240:243], v[120:123], v[144:147], v[240:243]\n\t" \
  "ds_read_b64_tr_b16 v[144:145], v62 offset:38912\n\t" \
  "v_mfma_f32_16x16x32_bf16 v[252:255], v[124:127], v[148:151], v[252:255]\n\t" \
  "ds_read_b64_tr_b16 v[146:147], v62 offset:39424\n\t" \
  "v_mfma_f32_16x16x32_bf16 v[220:223], v[108:111], v[148:151], v[220:223]\n\t" \
  "ds_read_b128 v[124:127], v61 offset:0\n\t" \
  "v_mfma_f32_16x16x32_bf16 v[228:231], v[112:115], v[148:151], v[228:231]\n\t" \
  "v_mfma_f32_16x16x32_bf16 v[236:239], v[116:119], v[148:151], v[236:239]\n\t" \
  "v_mfma_f32_16x16x32_bf16 v[244:247], v[120:123], v[148:151], v[244:247]\n\t" \
  "ds_read_b64_tr_b16 v[148:149], v63 offset:38912\n\t" \
  "v_mfma_f32_16x16x32_bf16 v[152:155], v[128:131], v[108:111], v[152:155]\n\t" \
  "ds_read_b64_tr_b16 v[150:151], v63 offset:39424\n\t" \
  "v_mfma_f32_16x16x32_bf16 v[160:163], v[128:131], v[112:115], v[160:163]\n\t" \
  "v_mfma_f32_16x16x32_bf16 v[168:171], v[128:131], v[116:119], v[168:171]\n\t" \
  "v_mfma_f32_16x16x32_bf16 v[176:179], v[128:131], v[120:123], v[176:179]\n\t" \
  "ds_read_b64_tr_b16 v[128:129], v62 offset:22528\n\t" \
  "v_mfma_f32_16x16x32_bf16 v[156:159], v[132:135], v[108:111], v[156:159]\n\t" \
  "ds_read_b128 v[108:111], v61 offset:10240\n\t" \
  "v_mfma_f32_16x16x32_bf16 v[164:167], v[132:135], v[112:115], v[164:167]\n\t" \
  "ds_read_b128 v[112:115], v61 offset:12288\n\t" \
  "v_mfma_f32_16x16x32_bf16 v[172:175], v[132:135], v[116:119], v[172:175]\n\t" \
  "ds_read_b128 v[116:119], v61 offset:14336\n\t" \
  "v_mfma_f32_16x16x32_bf16 v[180:183], v[132:135], v[120:123], v[180:183]\n\t" \
  "ds_read_b128 v[120:123], v61 offset:16384\n\t" \
  "s_waitcnt lgkmcnt(5)\n\t" \
  "v_mfma_f32_16x16x32_bf16 v[184:187], v[92:95], v[64:67], v[184:187]\n\t" \
  "ds_read_b64_tr_b16 v[130:131], v62 offset:23040\n\t" \
  "v_mfma_f32_16x16x32_bf16 v[192:195], v[92:95], v[68:71], v[192:195]\n\t" \
  "ds_read_b64_tr_b16 v[132:133], v63 offset:22528\n\t" \
  "v_mfma_f32_16x16x32_bf16 v[200:203], v[92:95], v[72:75], v[200:203]\n\t" \
  "ds_read_b64_tr_b16 v[134:135], v63 offset:23040\n\t" \
  "v_mfma_f32_16x16x32_bf16 v[208:211], v[92:95], v[76:79], v[208:211]\n\t" \
  "v_mfma_f32_16x16x32_bf16 v[188:191], v[96:99], v[64:67], v[188:191]\n\t" \
  "v_mfma_f32_16x16x32_bf16 v[196:199], v[96:99], v[68:71], v[196:199]\n\t" \
  "v_mfma_f32_16x16x32_bf16 v[204:207], v[96:99], v[72:75], v[204:207]\n\t" \
  "v_mfma_f32_16x16x32_bf16 v[212:215], v[96:99], v[76:79], v[212:215]\n\t" \
  "v_mfma_f32_16x16x32_bf16 v[248:251], v[80:83], v[100:103], v[248:251]\n\t" \
  "v_mfma_f32_16x16x32_bf16 v[216:219], v[64:67], v[100:103], v[216:219]\n\t" \
  "v_mfma_f32_16x16x32_bf16 v[224:227], v[68:71], v[100:103], v[224:227]\n\t" \
  "v_mfma_f32_16x16x32_bf16 v[232:235], v[72:75], v[100:103], v[232:235]\n\t" \
  "v_mfma_f32_16x16x32_bf16 v[240:243], v[76:79], v[100:103], v[240:243]\n\t" \
  "s_waitcnt lgkmcnt(0)\n\t" \
  "s_barrier\n\t" \
  "v_mfma_f32_16x16x32_bf16 v[252:255], v[80:83], v[104:107], v[252:255]\n\t" \
  "ds_read_b64_tr_b16 v[92:93], v54 offset:26624\n\t" \
  "v_mfma_f32_16x16x32_bf16 v[220:223], v[64:67], v[104:107], v[220:223]\n\t" \
  "ds_read_b64_tr_b16 v[94:95], v54 offset:27136\n\t" \
  "v_mfma_f32_16x16x32_bf16 v[228:231], v[68:71], v[104:107], v[228:231]\n\t" \
  "ds_read_b64_tr_b16 v[96:97], v55 offset:26624\n\t" \
  "v_mfma_f32_16x16x32_bf16 v[236:239], v[72:75], v[104:107], v[236:239]\n\t" \
  "ds_read_b64_tr_b16 v[98:99], v55 offset:27136\n\t" \
  "v_mfma_f32_16x16x32_bf16 v[244:247], v[76:79], v[104:107], v[244:247]\n\t" \
  "ds_read_b64_tr_b16 v[100:101], v54 offset:34816\n\t" \
  "v_mfma_f32_16x16x32_bf16 v[152:155], v[84:87], v[64:67], v[152:155]\n\t" \
  "ds_read_b64_tr_b16 v[102:103], v54 offset:35328\n\t" \
  "v_mfma_f32_16x16x32_bf16 v[160:163], v[84:87], v[68:71], v[160:163]\n\t" \
  "ds_read_b128 v[80:83], v52 offset:0\n\t" \
  "v_mfma_f32_16x16x32_bf16 v[168:171], v[84:87], v[72:75], v[168:171]\n\t" \
  "ds_read_b64_tr_b16 v[104:105], v55 offset:34816\n\t" \
  "v_mfma_f32_16x16x32_bf16 v[176:179], v[84:87], v[76:79], v[176:179]\n\t" \
  "501:\n\t" \
  "s_cbranch_execz 505f\n\t" \
  "ds_read_b64_tr_b16 v[106:107], v55 offset:35328\n\t" \
  "v_mfma_f32_16x16x32_bf16 v[156:159], v[88:91], v[64:67], v[156:159]\n\t" \
  "ds_read_b128 v[64:67], v52 offset:10240\n\t" \
  "v_mfma_f32_16x16x32_bf16 v[164:167], v[88:91], v[68:71], v[164:167]\n\t" \
  "ds_read_b128 v[68:71], v52 offset:12288\n\t" \
  "v_mfma_f32_16x16x32_bf16 v[172:175], v[88:91], v[72:75], v[172:175]\n\t" \
  "ds_read_b128 v[72:75], v52 offset:14336\n\t" \
  "v_mfma_f32_16x16x32_bf16 v[180:183], v[88:91], v[76:79], v[180:183]\n\t" \
  "ds_read_b128 v[76:79], v52 offset:16384\n\t" \
  "v_mfma_f32_16x16x32_bf16 v[184:187], v[136:139], v[108:111], v[184:187]\n\t" \
  "ds_read_b64_tr_b16 v[84:85], v54 offset:18432\n\t" \
  "v_mfma_f32_16x16x32_bf16 v[192:195], v[136:139], v[112:115], v[192:195]\n\t" \
  "ds_read_b64_tr_b16 v[86:87], v54 offset:18944\n\t" \
  "v_mfma_f32_16x16x32_bf16 v[200:203], v[136:139], v[116:119], v[200:203]\n\t" \
  "ds_read_b64_tr_b16 v[88:89], v55 offset:18432\n\t" \
  "v_mfma_f32_16x16x32_bf16 v[208:211], v[136:139], v[120:123], v[208:211]\n\t" \
  "ds_read_b64_tr_b16 v[90:91], v55 offset:18944\n\t" \
  "v_mfma_f32_16x16x32_bf16 v[188:191], v[140:143], v[108:111], v[188:191]\n\t" \
  "ds_read_b64_tr_b16 v[136:137], v54 offset:30720\n\t" \
  "v_mfma_f32_16x16x32_bf16 v[196:199], v[140:143], v[112:115], v[196:199]\n\t" \
  "ds_read_b64_tr_b16 v[138:139], v54 offset:31232\n\t" \
  "v_mfma_f32_16x16x32_bf16 v[204:207], v[140:143], v[116:119], v[204:207]\n\t" \
  "v_mfma_f32_16x16x32_bf16 v[212:215], v[140:143], v[120:123], v[212:215]\n\t" \
  "ds_read_b64_tr_b16 v[140:141], v55 offset:30720\n\t" \
  "v_mfma_f32_16x16x32_bf16 v[248:251], v[124:127], v[144:147], v[248:251]\n\t" \
  "ds_read_b64_tr_b16 v[142:143], v55 offset:31232\n\t" \
  "v_mfma_f32_16x16x32_bf16 v[216:219], v[108:111], v[144:147], v[216:219]\n\t" \
  "v_mfma_f32_16x16x32_bf16 v[224:227], v[112:115], v[144:147], v[224:227]\n\t" \
  "v_mfma_f32_16x16x32_bf16 v[232:235], v[116:119], v[144:147], v[232:235]\n\t" \
  "v_mfma_f32_16x16x32_bf16 v[240:243], v[120:123], v[144:147], v[240:243]\n\t" \
  "ds_read_b64_tr_b16 v[144:145], v54 offset:38912\n\t" \
  "v_mfma_f32_16x16x32_bf16 v[252:255], v[124:127], v[148:151], v[252:255]\n\t" \
  "ds_read_b64_tr_b16 v[146:147], v54 offset:39424\n\t" \
  "v_mfma_f32_16x16x32_bf16 v[220:223], v[108:111], v[148:151], v[220:223]\n\t" \
  "ds_read_b128 v[124:127], v53 offset:0\n\t" \
  "v_mfma_f32_16x16x32_bf16 v[228:231], v[112:115], v[148:151], v[228:231]\n\t" \
  "v_mfma_f32_16x16x32_bf16 v[236:239], v[116:119], v[148:151], v[236:239]\n\t" \
  "v_mfma_f32_16x16x32_bf16 v[244:247], v[120:123], v[148:151], v[244:247]\n\t" \
  "ds_read_b64_tr_b16 v[148:149], v55 offset:38912\n\t" \
  "v_mfma_f32_16x16x32_bf16 v[152:155], v[128:131], v[108:111], v[152:155]\n\t" \
  "ds_read_b64_tr_b16 v[150:151], v55 offset:39424\n\t" \
  "v_mfma_f32_16x16x32_bf16 v[160:163], v[128:131], v[112:115], v[160:163]\n\t" \
  "v_mfma_f32_16x16x32_bf16 v[168:171], v[128:131], v[116:119], v[168:171]\n\t" \
  "v_mfma_f32_16x16x32_bf16 v[176:179], v[128:131], v[120:123], v[176:179]\n\t" \
  "ds_read_b64_tr_b16 v[128:129], v54 offset:22528\n\t" \
  "v_mfma_f32_16x16x32_bf16 v[156:159], v[132:135], v[108:111], v[156:159]\n\t" \
  "ds_read_b128 v[108:111], v53 offset:10240\n\t" \
  "v_mfma_f32_16x16x32_bf16 v[164:167], v[132:135], v[112:115], v[164:167]\n\t" \
  "ds_read_b128 v[112:115], v53 offset:12288\n\t" \
  "v_mfma_f32_16x16x32_bf16 v[172:175], v[132:135], v[116:119], v[172:175]\n\t" \
  "ds_read_b128 v[116:119], v53 offset:14336\n\t" \
  "v_mfma_f32_16x16x32_bf16 v[180:183], v[132:135], v[120:123], v[180:183]\n\t" \
  "ds_read_b128 v[120:123], v53 offset:16384\n\t" \
  "s_waitcnt lgkmcnt(5)\n\t" \
  "v_mfma_f32_16x16x32_bf16 v[184:187], v[92:95], v[64:67], v[184:187]\n\t" \
  "ds_read_b64_tr_b16 v[130:131], v54 offset:23040\n\t" \
  "v_mfma_f32_16x16x32_bf16 v[192:195], v[92:95], v[68:71], v[192:195]\n\t" \
  "ds_read_b64_tr_b16 v[132:133], v55 offset:22528\n\t" \
  "v_mfma_f32_16x16x32_bf16 v[200:203], v[92:95], v[72:75], v[200:203]\n\t" \
  "ds_read_b64_tr_b16 v[134:135], v55 offset:23040\n\t" \
  "v_mfma_f32_16x16x32_bf16 v[208:211], v[92:95], v[76:79], v[208:211]\n\t" \
  "v_mfma_f32_16x16x32_bf16 v[188:191], v[96:99], v[64:67], v[188:191]\n\t" \
  "v_mfma_f32_16x16x32_bf16 v[196:199], v[96:99], v[68:71], v[196:199]\n\t" \
  "v_mfma_f32_16x16x32_bf16 v[204:207], v[96:99], v[72:75], v[204:207]\n\t" \
  "v_mfma_f32_16x16x32_bf16 v[212:215], v[96:99], v[76:79], v[212:215]\n\t" \
  "v_mfma_f32_16x16x32_bf16 v[248:251], v[80:83], v[100:103], v[248:251]\n\t" \
  "v_mfma_f32_16x16x32_bf16 v[216:219], v[64:67], v[100:103], v[216:219]\n\t" \
  "v_mfma_f32_16x16x32_bf16 v[224:227], v[68:71], v[100:103], v[224:227]\n\t" \
  "v_mfma_f32_16x16x32_bf16 v[232:235], v[72:75], v[100:103], v[232:235]\n\t" \
  "v_mfma_f32_16x16x32_bf16 v[240:243], v[76:79], v[100:103], v[240:243]\n\t" \
  "s_waitcnt lgkmcnt(0)\n\t" \
  "s_barrier\n\t" \
  "v_mfma_f32_16x16x32_bf16 v[252:255], v[80:83], v[104:107], v[252:255]\n\t" \
  "ds_read_b64_tr_b16 v[92:93], v58 offset:26624\n\t" \
  "v_mfma_f32_16x16x32_bf16 v[220:223], v[64:67], v[104:107], v[220:223]\n\t" \
  "ds_read_b64_tr_b16 v[94:95], v58 offset:27136\n\t" \
  "v_mfma_f32_16x16x32_bf16 v[228:231], v[68:71], v[104:107], v[228:231]\n\t" \
  "ds_read_b64_tr_b16 v[96:97], v59 offset:26624\n\t" \
  "v_mfma_f32_16x16x32_bf16 v[236:239], v[72:75], v[104:107], v[236:239]\n\t" \
  "ds_read_b64_tr_b16 v[98:99], v59 offset:27136\n\t" \
  "v_mfma_f32_16x16x32_bf16 v[244:247], v[76:79], v[104:107], v[244:247]\n\t" \
  "ds_read_b64_tr_b16 v[100:101], v58 offset:34816\n\t" \
  "v_mfma_f32_16x16x32_bf16 v[152:155], v[84:87], v[64:67], v[152:155]\n\t" \
  "ds_read_b64_tr_b16 v[102:103], v58 offset:35328\n\t" \
  "v_mfma_f32_16x16x32_bf16 v[160:163], v[84:87], v[68:71], v[160:163]\n\t" \
  "ds_read_b128 v[80:83], v56 offset:0\n\t" \
  "v_mfma_f32_16x16x32_bf16 v[168:171], v[84:87], v[72:75], v[168:171]\n\t" \
  "ds_read_b64_tr_b16 v[104:105], v59 offset:34816\n\t" \
  "v_mfma_f32_16x16x32_bf16 v[176:179], v[84:87], v[76:79], v[176:179]\n\t" \
  "ds_read_b64_tr_b16 v[106:107], v59 offset:35328\n\t" \
  "v_mfma_f32_16x16x32_bf16 v[156:159], v[88:91], v[64:67], v[156:159]\n\t" \
  "ds_read_b128 v[64:67], v56 offset:10240\n\t" \
  "v_mfma_f32_16x16x32_bf16 v[164:167], v[88:91], v[68:71], v[164:167]\n\t" \
  "ds_read_b128 v[68:71], v56 offset:12288\n\t" \
  "v_mfma_f32_16x16x32_bf16 v[172:175], v[88:91], v[72:75], v[172:175]\n\t" \
  "ds_read_b128 v[72:75], v56 offset:14336\n\t" \
  "v_mfma_f32_16x16x32_bf16 v[180:183], v[88:91], v[76:79], v[180:183]\n\t" \
  "ds_read_b128 v[76:79], v56 offset:16384\n\t" \
  "v_mfma_f32_16x16x32_bf16 v[184:187], v[136:139], v[108:111], v[184:187]\n\t" \
  "ds_read_b64_tr_b16 v[84:85], v58 offset:18432\n\t" \
  "v_mfma_f32_16x16x32_bf16 v[192:195], v[136:139], v[112:115], v[192:195]\n\t" \
  "ds_read_b64_tr_b16 v[86:87], v58 offset:18944\n\t" \
  "v_mfma_f32_16x16x32_bf16 v[200:203], v[136:139], v[116:119], v[200:203]\n\t" \
  "ds_read_b64_tr_b16 v[88:89], v59 offset:18432\n\t" \
  "v_mfma_f32_16x16x32_bf16 v[208:211], v[136:139], v[120:123], v[208:211]\n\t" \
  "ds_read_b64_tr_b16 v[90:91], v59 offset:18944\n\t" \
  "v_mfma_f32_16x16x32_bf16 v[188:191], v[140:143], v[108:111], v[188:191]\n\t" \
  "ds_read_b64_tr_b16 v[136:137], v58 offset:30720\n\t" \
  "v_mfma_f32_16x16x32_bf16 v[196:199], v[140:143], v[112:115], v[196:199]\n\t" \
  "ds_read_b64_tr_b16 v[138:139], v58 offset:31232\n\t" \
  "v_mfma_f32_16x16x32_bf16 v[204:207], v[140:143], v[116:119], v[204:207]\n\t" \
  "v_mfma_f32_16x16x32_bf16 v[212:215], v[140:143], v[120:123], v[212:215]\n\t" \
  "ds_read_b64_tr_b16 v[140:141], v59 offset:30720\n\t" \
  "v_mfma_f32_16x16x32_bf16 v[248:251], v[124:127], v[144:147], v[248:251]\n\t" \
  "ds_read_b64_tr_b16 v[142:143], v59 offset:31232\n\t" \
  "v_mfma_f32_16x16x32_bf16 v[216:219], v[108:111], v[144:147], v[216:219]\n\t" \
  "v_mfma_f32_16x16x32_bf16 v[224:227], v[112:115], v[144:147], v[224:227]\n\t" \
  "v_mfma_f32_16x16x32_bf16 v[232:235], v[116:119], v[144:147], v[232:235]\n\t" \
  "v_mfma_f32_16x16x32_bf16 v[240:243], v[120:123], v[144:147], v[240:243]\n\t" \
  "ds_read_b64_tr_b16 v[144:145], v58 offset:38912\n\t" \
  "v_mfma_f32_16x16x32_bf16 v[252:255], v[124:127], v[148:151], v[252:255]\n\t" \
  "ds_read_b64_tr_b16 v[146:147], v58 offset:39424\n\t" \
  "v_mfma_f32_16x16x32_bf16 v[220:223], v[108:111], v[148:151], v[220:223]\n\t" \
  "ds_read_b128 v[124:127], v57 offset:0\n\t" \
  "v_mfma_f32_16x16x32_bf16 v[228:231], v[112:115], v[148:151], v[228:231]\n\t" \
  "v_mfma_f32_16x16x32_bf16 v[236:239], v[116:119], v[148:151], v[236:239]\n\t" \
  "v_mfma_f32_16x16x32_bf16 v[244:247], v[120:123], v[148:151], v[244:247]\n\t" \
  "ds_read_b64_tr_b16 v[148:149], v59 offset:38912\n\t" \
  "v_mfma_f32_16x16x32_bf16 v[152:155], v[128:131], v[108:111], v[152:155]\n\t" \
  "ds_read_b64_tr_b16 v[150:151], v59 offset:39424\n\t" \
  "v_mfma_f32_16x16x32_bf16 v[160:163], v[128:131], v[112:115], v[160:163]\n\t" \
  "v_mfma_f32_16x16x32_bf16 v[168:171], v[128:131], v[116:119], v[168:171]\n\t" \
  "v_mfma_f32_16x16x32_bf16 v[176:179], v[128:131], v[120:123], v[176:179]\n\t" \
  "ds_read_b64_tr_b16 v[128:129], v58 offset:22528\n\t" \
  "v_mfma_f32_16x16x32_bf16 v[156:159], v[132:135], v[108:111], v[156:159]\n\t" \
  "ds_read_b128 v[108:111], v57 offset:10240\n\t" \
  "v_mfma_f32_16x16x32_bf16 v[164:167], v[132:135], v[112:115], v[164:167]\n\t" \
  "ds_read_b128 v[112:115], v57 offset:12288\n\t" \
  "v_mfma_f32_16x16x32_bf16 v[172:175], v[132:135], v[116:119], v[172:175]\n\t" \
  "ds_read_b128 v[116:119], v57 offset:14336\n\t" \
  "v_mfma_f32_16x16x32_bf16 v[180:183], v[132:135], v[120:123], v[180:183]\n\t" \
  "ds_read_b128 v[120:123], v57 offset:16384\n\t" \
  "s_waitcnt lgkmcnt(5)\n\t" \
  "v_mfma_f32_16x16x32_bf16 v[184:187], v[92:95], v[64:67], v[184:187]\n\t" \
  "ds_read_b64_tr_b16 v[130:131], v58 offset:23040\n\t" \
  "v_mfma_f32_16x16x32_bf16 v[192:195], v[92:95], v[68:71], v[192:195]\n\t" \
  "ds_read_b64_tr_b16 v[132:133], v59 offset:22528\n\t" \
  "v_mfma_f32_16x16x32_bf16 v[200:203], v[92:95], v[72:75], v[200:203]\n\t" \
  "ds_read_b64_tr_b16 v[134:135], v59 offset:23040\n\t" \
  "v_mfma_f32_16x16x32_bf16 v[208:211], v[92:95], v[76:79], v[208:211]\n\t" \
  "v_mfma_f32_16x16x32_bf16 v[188:191], v[96:99], v[64:67], v[188:191]\n\t" \
  "v_mfma_f32_16x16x32_bf16 v[196:199], v[96:99], v[68:71], v[196:199]\n\t" \
  "v_mfma_f32_16x16x32_bf16 v[204:207], v[96:99], v[72:75], v[204:207]\n\t" \
  "v_mfma_f32_16x16x32_bf16 v[212:215], v[96:99], v[76:79], v[212:215]\n\t" \
  "v_mfma_f32_16x16x32_bf16 v[248:251], v[80:83], v[100:103], v[248:251]\n\t" \
  "v_mfma_f32_16x16x32_bf16 v[216:219], v[64:67], v[100:103], v[216:219]\n\t" \
  "v_mfma_f32_16x16x32_bf16 v[224:227], v[68:71], v[100:103], v[224:227]\n\t" \
  "v_mfma_f32_16x16x32_bf16 v[232:235], v[72:75], v[100:103], v[232:235]\n\t" \
  "v_mfma_f32_16x16x32_bf16 v[240:243], v[76:79], v[100:103], v[240:243]\n\t" \
  "s_waitcnt lgkmcnt(0)\n\t" \
  "s_barrier\n\t" \
  "v_mfma_f32_16x16x32_bf16 v[252:255], v[80:83], v[104:107], v[252:255]\n\t" \
  "ds_read_b64_tr_b16 v[92:93], v62 offset:26624\n\t" \
  "v_mfma_f32_16x16x32_bf16 v[220:223], v[64:67], v[104:107], v[220:223]\n\t" \
  "ds_read_b64_tr_b16 v[94:95], v62 offset:27136\n\t" \
  "v_mfma_f32_16x16x32_bf16 v[228:231], v[68:71], v[104:107], v[228:231]\n\t" \
  "ds_read_b64_tr_b16 v[96:97], v63 offset:26624\n\t" \
  "v_mfma_f32_16x16x32_bf16 v[236:239], v[72:75], v[104:107], v[236:239]\n\t" \
  "ds_read_b64_tr_b16 v[98:99], v63 offset:27136\n\t" \
  "v_mfma_f32_16x16x32_bf16 v[244:247], v[76:79], v[104:107], v[244:247]\n\t" \
  "ds_read_b64_tr_b16 v[100:101], v62 offset:34816\n\t" \
  "v_mfma_f32_16x16x32_bf16 v[152:155], v[84:87], v[64:67], v[152:155]\n\t" \
  "ds_read_b64_tr_b16 v[102:103], v62 offset:35328\n\t" \
  "v_mfma_f32_16x16x32_bf16 v[160:163], v[84:87], v[68:71], v[160:163]\n\t" \
  "ds_read_b128 v[80:83], v60 offset:0\n\t" \
  "v_mfma_f32_16x16x32_bf16 v[168:171], v[84:87], v[72:75], v[168:171]\n\t" \
  "ds_read_b64_tr_b16 v[104:105], v63 offset:34816\n\t" \
  "v_mfma_f32_16x16x32_bf16 v[176:179], v[84:87], v[76:79], v[176:179]\n\t" \
  "ds_read_b64_tr_b16 v[106:107], v63 offset:35328\n\t" \
  "v_mfma_f32_16x16x32_bf16 v[156:159], v[88:91], v[64:67], v[156:159]\n\t" \
  "ds_read_b128 v[64:67], v60 offset:10240\n\t" \
  "v_mfma_f32_16x16x32_bf16 v[164:167], v[88:91], v[68:71], v[164:167]\n\t" \
  "ds_read_b128 v[68:71], v60 offset:12288\n\t" \
  "v_mfma_f32_16x16x32_bf16 v[172:175], v[88:91], v[72:75], v[172:175]\n\t" \
  "ds_read_b128 v[72:75], v60 offset:14336\n\t" \
  "v_mfma_f32_16x16x32_bf16 v[180:183], v[88:91], v[76:79], v[180:183]\n\t" \
  "ds_read_b128 v[76:79], v60 offset:16384\n\t" \
  "v_mfma_f32_16x16x32_bf16 v[184:187], v[136:139], v[108:111], v[184:187]\n\t" \
  "ds_read_b64_tr_b16 v[84:85], v62 offset:18432\n\t" \
  "v_mfma_f32_16x16x32_bf16 v[192:195], v[136:139], v[112:115], v[192:195]\n\t" \
  "ds_read_b64_tr_b16 v[86:87], v62 offset:18944\n\t" \
  "v_mfma_f32_16x16x32_bf16 v[200:203], v[136:139], v[116:119], v[200:203]\n\t" \
  "ds_read_b64_tr_b16 v[88:89], v63 offset:18432\n\t" \
  "v_mfma_f32_16x16x32_bf16 v[208:211], v[136:139], v[120:123], v[208:211]\n\t" \
  "ds_read_b64_tr_b16 v[90:91], v63 offset:18944\n\t" \
  "v_mfma_f32_16x16x32_bf16 v[188:191], v[140:143], v[108:111], v[188:191]\n\t" \
  "ds_read_b64_tr_b16 v[136:137], v62 offset:30720\n\t" \
  "v_mfma_f32_16x16x32_bf16 v[196:199], v[140:143], v[112:115], v[196:199]\n\t" \
  "ds_read_b64_tr_b16 v[138:139], v62 offset:31232\n\t" \
  "v_mfma_f32_16x16x32_bf16 v[204:207], v[140:143], v[116:119], v[204:207]\n\t" \
  "v_mfma_f32_16x16x32_bf16 v[212:215], v[140:143], v[120:123], v[212:215]\n\t" \
  "ds_read_b64_tr_b16 v[140:141], v63 offset:30720\n\t" \
  "v_mfma_f32_16x16x32_bf16 v[248:251], v[124:127], v[144:147], v[248:251]\n\t" \
  "ds_read_b64_tr_b16 v[142:143], v63 offset:31232\n\t" \
  "v_mfma_f32_16x16x32_bf16 v[216:219], v[108:111], v[144:147], v[216:219]\n\t" \
  "v_mfma_f32_16x16x32_bf16 v[224:227], v[112:115], v[144:147], v[224:227]\n\t" \
  "v_mfma_f32_16x16x32_bf16 v[232:235], v[116:119], v[144:147], v[232:235]\n\t" \
  "v_mfma_f32_16x16x32_bf16 v[240:243], v[120:123], v[144:147], v[240:243]\n\t" \
  "ds_read_b64_tr_b16 v[144:145], v62 offset:38912\n\t" \
  "v_mfma_f32_16x16x32_bf16 v[252:255], v[124:127], v[148:151], v[252:255]\n\t" \
  "ds_read_b64_tr_b16 v[146:147], v62 offset:39424\n\t" \
  "v_mfma_f32_16x16x32_bf16 v[220:223], v[108:111], v[148:151], v[220:223]\n\t" \
  "ds_read_b128 v[124:127], v61 offset:0\n\t" \
  "v_mfma_f32_16x16x32_bf16 v[228:231], v[112:115], v[148:151], v[228:231]\n\t" \
  "v_mfma_f32_16x16x32_bf16 v[236:239], v[116:119], v[148:151], v[236:239]\n\t" \
  "v_mfma_f32_16x16x32_bf16 v[244:247], v[120:123], v[148:151], v[244:247]\n\t" \
  "ds_read_b64_tr_b16 v[148:149], v63 offset:38912\n\t" \
  "v_mfma_f32_16x16x32_bf16 v[152:155], v[128:131], v[108:111], v[152:155]\n\t" \
  "ds_read_b64_tr_b16 v[150:151], v63 offset:39424\n\t" \
  "v_mfma_f32_16x16x32_bf16 v[160:163], v[128:131], v[112:115], v[160:163]\n\t" \
  "v_mfma_f32_16x16x32_bf16 v[168:171], v[128:131], v[116:119], v[168:171]\n\t" \
  "v_mfma_f32_16x16x32_bf16 v[176:179], v[128:131], v[120:123], v[176:179]\n\t" \
  "ds_read_b64_tr_b16 v[128:129], v62 offset:22528\n\t" \
  "v_mfma_f32_16x16x32_bf16 v[156:159], v[132:135], v[108:111], v[156:159]\n\t" \
  "ds_read_b128 v[108:111], v61 offset:10240\n\t" \
  "v_mfma_f32_16x16x32_bf16 v[164:167], v[132:135], v[112:115], v[164:167]\n\t" \
  "ds_read_b128 v[112:115], v61 offset:12288\n\t" \
  "v_mfma_f32_16x16x32_bf16 v[172:175], v[132:135], v[116:119], v[172:175]\n\t" \
  "ds_read_b128 v[116:119], v61 offset:14336\n\t" \
  "v_mfma_f32_16x16x32_bf16 v[180:183], v[132:135], v[120:123], v[180:183]\n\t" \
  "ds_read_b128 v[120:123], v61 offset:16384\n\t" \
  "s_waitcnt lgkmcnt(5)\n\t" \
  "v_mfma_f32_16x16x32_bf16 v[184:187], v[92:95], v[64:67], v[184:187]\n\t" \
  "ds_read_b64_tr_b16 v[130:131], v62 offset:23040\n\t" \
  "v_mfma_f32_16x16x32_bf16 v[192:195], v[92:95], v[68:71], v[192:195]\n\t" \
  "ds_read_b64_tr_b16 v[132:133], v63 offset:22528\n\t" \
  "v_mfma_f32_16x16x32_bf16 v[200:203], v[92:95], v[72:75], v[200:203]\n\t" \
  "ds_read_b64_tr_b16 v[134:135], v63 offset:23040\n\t" \
  "v_mfma_f32_16x16x32_bf16 v[208:211], v[92:95], v[76:79], v[208:211]\n\t" \
  "v_mfma_f32_16x16x32_bf16 v[188:191], v[96:99], v[64:67], v[188:191]\n\t" \
  "v_mfma_f32_16x16x32_bf16 v[196:199], v[96:99], v[68:71], v[196:199]\n\t" \
  "v_mfma_f32_16x16x32_bf16 v[204:207], v[96:99], v[72:75], v[204:207]\n\t" \
  "v_mfma_f32_16x16x32_bf16 v[212:215], v[96:99], v[76:79], v[212:215]\n\t" \
  "v_mfma_f32_16x16x32_bf16 v[248:251], v[80:83], v[100:103], v[248:251]\n\t" \
  "v_mfma_f32_16x16x32_bf16 v[216:219], v[64:67], v[100:103], v[216:219]\n\t" \
  "v_mfma_f32_16x16x32_bf16 v[224:227], v[68:71], v[100:103], v[224:227]\n\t" \
  "v_mfma_f32_16x16x32_bf16 v[232:235], v[72:75], v[100:103], v[232:235]\n\t" \
  "v_mfma_f32_16x16x32_bf16 v[240:243], v[76:79], v[100:103], v[240:243]\n\t" \
  "s_waitcnt lgkmcnt(0)\n\t" \
  "s_barrier\n\t" \
  "v_mfma_f32_16x16x32_bf16 v[252:255], v[80:83], v[104:107], v[252:255]\n\t" \
  "ds_read_b64_tr_b16 v[92:93], v54 offset:26624\n\t" \
  "v_mfma_f32_16x16x32_bf16 v[220:223], v[64:67], v[104:107], v[220:223]\n\t" \
  "ds_read_b64_tr_b16 v[94:95], v54 offset:27136\n\t" \
  "v_mfma_f32_16x16x32_bf16 v[228:231], v[68:71], v[104:107], v[228:231]\n\t" \
  "ds_read_b64_tr_b16 v[96:97], v55 offset:26624\n\t" \
  "v_mfma_f32_16x16x32_bf16 v[236:239], v[72:75], v[104:107], v[236:239]\n\t" \
  "ds_read_b64_tr_b16 v[98:99], v55 offset:27136\n\t" \
  "v_mfma_f32_16x16x32_bf16 v[244:247], v[76:79], v[104:107], v[244:247]\n\t" \
  "ds_read_b64_tr_b16 v[100:101], v54 offset:34816\n\t" \
  "v_mfma_f32_16x16x32_bf16 v[152:155], v[84:87], v[64:67], v[152:155]\n\t" \
  "ds_read_b64_tr_b16 v[102:103], v54 offset:35328\n\t" \
  "v_mfma_f32_16x16x32_bf16 v[160:163], v[84:87], v[68:71], v[160:163]\n\t" \
  "ds_read_b128 v[80:83], v52 offset:0\n\t" \
  "v_mfma_f32_16x16x32_bf16 v[168:171], v[84:87], v[72:75], v[168:171]\n\t" \
  "ds_read_b64_tr_b16 v[104:105], v55 offset:34816\n\t" \
  "v_mfma_f32_16x16x32_bf16 v[176:179], v[84:87], v[76:79], v[176:179]\n\t" \
  "ds_read_b64_tr_b16 v[106:107], v55 offset:35328\n\t" \
  "v_mfma_f32_16x16x32_bf16 v[156:159], v[88:91], v[64:67], v[156:159]\n\t" \
  "ds_read_b128 v[64:67], v52 offset:10240\n\t" \
  "v_mfma_f32_16x16x32_bf16 v[164:167], v[88:91], v[68:71], v[164:167]\n\t" \
  "ds_read_b128 v[68:71], v52 offset:12288\n\t" \
  "v_mfma_f32_16x16x32_bf16 v[172:175], v[88:91], v[72:75], v[172:175]\n\t" \
  "ds_read_b128 v[72:75], v52 offset:14336\n\t" \
  "v_mfma_f32_16x16x32_bf16 v[180:183], v[88:91], v[76:79], v[180:183]\n\t" \
  "ds_read_b128 v[76:79], v52 offset:16384\n\t" \
  "v_mfma_f32_16x16x32_bf16 v[184:187], v[136:139], v[108:111], v[184:187]\n\t" \
  "ds_read_b64_tr_b16 v[84:85], v54 offset:18432\n\t" \
  "v_mfma_f32_16x16x32_bf16 v[192:195], v[136:139], v[112:115], v[192:195]\n\t" \
  "ds_read_b64_tr_b16 v[86:87], v54 offset:18944\n\t" \
  "v_mfma_f32_16x16x32_bf16 v[200:203], v[136:139], v[116:119], v[200:203]\n\t" \
  "ds_read_b64_tr_b16 v[88:89], v55 offset:18432\n\t" \
  "v_mfma_f32_16x16x32_bf16 v[208:211], v[136:139], v[120:123], v[208:211]\n\t" \
  "ds_read_b64_tr_b16 v[90:91], v55 offset:18944\n\t" \
  "v_mfma_f32_16x16x32_bf16 v[188:191], v[140:143], v[108:111], v[188:191]\n\t" \
  "ds_read_b64_tr_b16 v[136:137], v54 offset:30720\n\t" \
  "v_mfma_f32_16x16x32_bf16 v[196:199], v[140:143], v[112:115], v[196:199]\n\t" \
  "ds_read_b64_tr_b16 v[138:139], v54 offset:31232\n\t" \
  "v_mfma_f32_16x16x32_bf16 v[204:207], v[140:143], v[116:119], v[204:207]\n\t" \
  "v_mfma_f32_16x16x32_bf16 v[212:215], v[140:143], v[120:123], v[212:215]\n\t" \
  "ds_read_b64_tr_b16 v[140:141], v55 offset:30720\n\t" \
  "v_mfma_f32_16x16x32_bf16 v[248:251], v[124:127], v[144:147], v[248:251]\n\t" \
  "ds_read_b64_tr_b16 v[142:143], v55 offset:31232\n\t" \
  "v_mfma_f32_16x16x32_bf16 v[216:219], v[108:111], v[144:147], v[216:219]\n\t" \
  "v_mfma_f32_16x16x32_bf16 v[224:227], v[112:115], v[144:147], v[224:227]\n\t" \
  "v_mfma_f32_16x16x32_bf16 v[232:235], v[116:119], v[144:147], v[232:235]\n\t" \
  "v_mfma_f32_16x16x32_bf16 v[240:243], v[120:123], v[144:147], v[240:243]\n\t" \
  "ds_read_b64_tr_b16 v[144:145], v54 offset:38912\n\t" \
  "v_mfma_f32_16x16x32_bf16 v[252:255], v[124:127], v[148:151], v[252:255]\n\t" \
  "ds_read_b64_tr_b16 v[146:147], v54 offset:39424\n\t" \
  "v_mfma_f32_16x16x32_bf16 v[220:223], v[108:111], v[148:151], v[220:223]\n\t" \
  "ds_read_b128 v[124:127], v53 offset:0\n\t" \
  "v_mfma_f32_16x16x32_bf16 v[228:231], v[112:115], v[148:151], v[228:231]\n\t" \
  "v_mfma_f32_16x16x32_bf16 v[236:239], v[116:119], v[148:151], v[236:239]\n\t" \
  "v_mfma_f32_16x16x32_bf16 v[244:247], v[120:123], v[148:151], v[244:247]\n\t" \
  "ds_read_b64_tr_b16 v[148:149], v55 offset:38912\n\t" \
  "v_mfma_f32_16x16x32_bf16 v[152:155], v[128:131], v[108:111], v[152:155]\n\t" \
  "ds_read_b64_tr_b16 v[150:151], v55 offset:39424\n\t" \
  "v_mfma_f32_16x16x32_bf16 v[160:163], v[128:131], v[112:115], v[160:163]\n\t" \
  "v_mfma_f32_16x16x32_bf16 v[168:171], v[128:131], v[116:119], v[168:171]\n\t" \
  "v_mfma_f32_16x16x32_bf16 v[176:179], v[128:131], v[120:123], v[176:179]\n\t" \
  "ds_read_b64_tr_b16 v[128:129], v54 offset:22528\n\t" \
  "v_mfma_f32_16x16x32_bf16 v[156:159], v[132:135], v[108:111], v[156:159]\n\t" \
  "ds_read_b128 v[108:111], v53 offset:10240\n\t" \
  "v_mfma_f32_16x16x32_bf16 v[164:167], v[132:135], v[112:115], v[164:167]\n\t" \
  "ds_read_b128 v[112:115], v53 offset:12288\n\t" \
  "v_mfma_f32_16x16x32_bf16 v[172:175], v[132:135], v[116:119], v[172:175]\n\t" \
  "ds_read_b128 v[116:119], v53 offset:14336\n\t" \
  "v_mfma_f32_16x16x32_bf16 v[180:183], v[132:135], v[120:123], v[180:183]\n\t" \
  "ds_read_b128 v[120:123], v53 offset:16384\n\t" \
  "s_waitcnt lgkmcnt(5)\n\t" \
  "v_mfma_f32_16x16x32_bf16 v[184:187], v[92:95], v[64:67], v[184:187]\n\t" \
  "ds_read_b64_tr_b16 v[130:131], v54 offset:23040\n\t" \
  "v_mfma_f32_16x16x32_bf16 v[192:195], v[92:95], v[68:71], v[192:195]\n\t" \
  "ds_read_b64_tr_b16 v[132:133], v55 offset:22528\n\t" \
  "v_mfma_f32_16x16x32_bf16 v[200:203], v[92:95], v[72:75], v[200:203]\n\t" \
  "ds_read_b64_tr_b16 v[134:135], v55 offset:23040\n\t" \
  "v_mfma_f32_16x16x32_bf16 v[208:211], v[92:95], v[76:79], v[208:211]\n\t" \
  "v_mfma_f32_16x16x32_bf16 v[188:191], v[96:99], v[64:67], v[188:191]\n\t" \
  "v_mfma_f32_16x16x32_bf16 v[196:199], v[96:99], v[68:71], v[196:199]\n\t" \
  "v_mfma_f32_16x16x32_bf16 v[204:207], v[96:99], v[72:75], v[204:207]\n\t" \
  "v_mfma_f32_16x16x32_bf16 v[212:215], v[96:99], v[76:79], v[212:215]\n\t" \
  "v_mfma_f32_16x16x32_bf16 v[248:251], v[80:83], v[100:103], v[248:251]\n\t" \
  "v_mfma_f32_16x16x32_bf16 v[216:219], v[64:67], v[100:103], v[216:219]\n\t" \
  "v_mfma_f32_16x16x32_bf16 v[224:227], v[68:71], v[100:103], v[224:227]\n\t" \
  "v_mfma_f32_16x16x32_bf16 v[232:235], v[72:75], v[100:103], v[232:235]\n\t" \
  "v_mfma_f32_16x16x32_bf16 v[240:243], v[76:79], v[100:103], v[240:243]\n\t" \
  "s_waitcnt lgkmcnt(0)\n\t" \
  "s_barrier\n\t" \
  "v_mfma_f32_16x16x32_bf16 v[252:255], v[80:83], v[104:107], v[252:255]\n\t" \
  "ds_read_b64_tr_b16 v[92:93], v58 offset:26624\n\t" \
  "v_mfma_f32_16x16x32_bf16 v[220:223], v[64:67], v[104:107], v[220:223]\n\t" \
  "ds_read_b64_tr_b16 v[94:95], v58 offset:27136\n\t" \
  "v_mfma_f32_16x16x32_bf16 v[228:231], v[68:71], v[104:107], v[228:231]\n\t" \
  "ds_read_b64_tr_b16 v[96:97], v59 offset:26624\n\t" \
  "v_mfma_f32_16x16x32_bf16 v[236:239], v[72:75], v[104:107], v[236:239]\n\t" \
  "ds_read_b64_tr_b16 v[98:99], v59 offset:27136\n\t" \
  "v_mfma_f32_16x16x32_bf16 v[244:247], v[76:79], v[104:107], v[244:247]\n\t" \
  "ds_read_b64_tr_b16 v[100:101], v58 offset:34816\n\t" \
  "v_mfma_f32_16x16x32_bf16 v[152:155], v[84:87], v[64:67], v[152:155]\n\t" \
  "ds_read_b64_tr_b16 v[102:103], v58 offset:35328\n\t" \
  "v_mfma_f32_16x16x32_bf16 v[160:163], v[84:87], v[68:71], v[160:163]\n\t" \
  "ds_read_b128 v[80:83], v56 offset:0\n\t" \
  "v_mfma_f32_16x16x32_bf16 v[168:171], v[84:87], v[72:75], v[168:171]\n\t" \
  "ds_read_b64_tr_b16 v[104:105], v59 offset:34816\n\t" \
  "v_mfma_f32_16x16x32_bf16 v[176:179], v[84:87], v[76:79], v[176:179]\n\t" \
  "ds_read_b64_tr_b16 v[106:107], v59 offset:35328\n\t" \
  "v_mfma_f32_16x16x32_bf16 v[156:159], v[88:91], v[64:67], v[156:159]\n\t" \
  "ds_read_b128 v[64:67], v56 offset:10240\n\t" \
  "v_mfma_f32_16x16x32_bf16 v[164:167], v[88:91], v[68:71], v[164:167]\n\t" \
  "ds_read_b128 v[68:71], v56 offset:12288\n\t" \
  "v_mfma_f32_16x16x32_bf16 v[172:175], v[88:91], v[72:75], v[172:175]\n\t" \
  "ds_read_b128 v[72:75], v56 offset:14336\n\t" \
  "v_mfma_f32_16x16x32_bf16 v[180:183], v[88:91], v[76:79], v[180:183]\n\t" \
  "ds_read_b128 v[76:79], v56 offset:16384\n\t" \
  "v_mfma_f32_16x16x32_bf16 v[184:187], v[136:139], v[108:111], v[184:187]\n\t" \
  "ds_read_b64_tr_b16 v[84:85], v58 offset:18432\n\t" \
  "v_mfma_f32_16x16x32_bf16 v[192:195], v[136:139], v[112:115], v[192:195]\n\t" \
  "ds_read_b64_tr_b16 v[86:87], v58 offset:18944\n\t" \
  "v_mfma_f32_16x16x32_bf16 v[200:203], v[136:139], v[116:119], v[200:203]\n\t" \
  "ds_read_b64_tr_b16 v[88:89], v59 offset:18432\n\t" \
  "v_mfma_f32_16x16x32_bf16 v[208:211], v[136:139], v[120:123], v[208:211]\n\t" \
  "ds_read_b64_tr_b16 v[90:91], v59 offset:18944\n\t" \
  "v_mfma_f32_16x16x32_bf16 v[188:191], v[140:143], v[108:111], v[188:191]\n\t" \
  "ds_read_b64_tr_b16 v[136:137], v58 offset:30720\n\t" \
  "v_mfma_f32_16x16x32_bf16 v[196:199], v[140:143], v[112:115], v[196:199]\n\t" \
  "ds_read_b64_tr_b16 v[138:139], v58 offset:31232\n\t" \
  "v_mfma_f32_16x16x32_bf16 v[204:207], v[140:143], v[116:119], v[204:207]\n\t" \
  "v_mfma_f32_16x16x32_bf16 v[212:215], v[140:143], v[120:123], v[212:215]\n\t" \
  "ds_read_b64_tr_b16 v[140:141], v59 offset:30720\n\t" \
  "v_mfma_f32_16x16x32_bf16 v[248:251], v[124:127], v[144:147], v[248:251]\n\t" \
  "ds_read_b64_tr_b16 v[142:143], v59 offset:31232\n\t" \
  "v_mfma_f32_16x16x32_bf16 v[216:219], v[108:111], v[144:147], v[216:219]\n\t" \
  "v_mfma_f32_16x16x32_bf16 v[224:227], v[112:115], v[144:147], v[224:227]\n\t" \
  "v_mfma_f32_16x16x32_bf16 v[232:235], v[116:119], v[144:147], v[232:235]\n\t" \
  "v_mfma_f32_16x16x32_bf16 v[240:243], v[120:123], v[144:147], v[240:243]\n\t" \
  "ds_read_b64_tr_b16 v[144:145], v58 offset:38912\n\t" \
  "v_mfma_f32_16x16x32_bf16 v[252:255], v[124:127], v[148:151], v[252:255]\n\t" \
  "ds_read_b64_tr_b16 v[146:147], v58 offset:39424\n\t" \
  "v_mfma_f32_16x16x32_bf16 v[220:223], v[108:111], v[148:151], v[220:223]\n\t" \
  "ds_read_b128 v[124:127], v57 offset:0\n\t" \
  "v_mfma_f32_16x16x32_bf16 v[228:231], v[112:115], v[148:151], v[228:231]\n\t" \
  "v_mfma_f32_16x16x32_bf16 v[236:239], v[116:119], v[148:151], v[236:239]\n\t" \
  "v_mfma_f32_16x16x32_bf16 v[244:247], v[120:123], v[148:151], v[244:247]\n\t" \
  "ds_read_b64_tr_b16 v[148:149], v59 offset:38912\n\t" \
  "v_mfma_f32_16x16x32_bf16 v[152:155], v[128:131], v[108:111], v[152:155]\n\t" \
  "ds_read_b64_tr_b16 v[150:151], v59 offset:39424\n\t" \
  "v_mfma_f32_16x16x32_bf16 v[160:163], v[128:131], v[112:115], v[160:163]\n\t" \
  "v_mfma_f32_16x16x32_bf16 v[168:171], v[128:131], v[116:119], v[168:171]\n\t" \
  "v_mfma_f32_16x16x32_bf16 v[176:179], v[128:131], v[120:123], v[176:179]\n\t" \
  "ds_read_b64_tr_b16 v[128:129], v58 offset:22528\n\t" \
  "v_mfma_f32_16x16x32_bf16 v[156:159], v[132:135], v[108:111], v[156:159]\n\t" \
  "ds_read_b128 v[108:111], v57 offset:10240\n\t" \
  "v_mfma_f32_16x16x32_bf16 v[164:167], v[132:135], v[112:115], v[164:167]\n\t" \
  "ds_read_b128 v[112:115], v57 offset:12288\n\t" \
  "v_mfma_f32_16x16x32_bf16 v[172:175], v[132:135], v[116:119], v[172:175]\n\t" \
  "ds_read_b128 v[116:119], v57 offset:14336\n\t" \
  "v_mfma_f32_16x16x32_bf16 v[180:183], v[132:135], v[120:123], v[180:183]\n\t" \
  "ds_read_b128 v[120:123], v57 offset:16384\n\t" \
  "s_waitcnt lgkmcnt(5)\n\t" \
  "v_mfma_f32_16x16x32_bf16 v[184:187], v[92:95], v[64:67], v[184:187]\n\t" \
  "ds_read_b64_tr_b16 v[130:131], v58 offset:23040\n\t" \
  "v_mfma_f32_16x16x32_bf16 v[192:195], v[92:95], v[68:71], v[192:195]\n\t" \
  "v_and_b32 v14, -16, v32\n\t" \
  "ds_read_b64_tr_b16 v[132:133], v59 offset:22528\n\t" \
  "v_mfma_f32_16x16x32_bf16 v[200:203], v[92:95], v[72:75], v[200:203]\n\t" \
  "ds_read_b64_tr_b16 v[134:135], v59 offset:23040\n\t" \
  "v_mfma_f32_16x16x32_bf16 v[208:211], v[92:95], v[76:79], v[208:211]\n\t" \
  "ds_read_b128 v[10:13], v14 offset:128\n\t" \
  "v_mfma_f32_16x16x32_bf16 v[188:191], v[96:99], v[64:67], v[188:191]\n\t" \
  "v_mfma_f32_16x16x32_bf16 v[196:199], v[96:99], v[68:71], v[196:199]\n\t" \
  "v_mfma_f32_16x16x32_bf16 v[204:207], v[96:99], v[72:75], v[204:207]\n\t" \
  "v_mfma_f32_16x16x32_bf16 v[212:215], v[96:99], v[76:79], v[212:215]\n\t" \
  "v_mfma_f32_16x16x32_bf16 v[248:251], v[80:83], v[100:103], v[248:251]\n\t" \
  "v_mfma_f32_16x16x32_bf16 v[216:219], v[64:67], v[100:103], v[216:219]\n\t" \
  "s_waitcnt lgkmcnt(0)\n\t" \
  "v_and_b32 v10, v10, v11\n\t" \
  "v_and_b32 v12, v12, v13\n\t" \
  "v_mfma_f32_16x16x32_bf16 v[224:227], v[68:71], v[100:103], v[224:227]\n\t" \
  "v_and_b32 v10, v10, v12\n\t" \
  "v_mfma_f32_16x16x32_bf16 v[232:235], v[72:75], v[100:103], v[232:235]\n\t" \
  "v_readfirstlane_b32 vcc_lo, v10\n\t" \
  "v_mfma_f32_16x16x32_bf16 v[240:243], v[76:79], v[100:103], v[240:243]\n\t" \
  "ds_write_b32 v32, v0 offset:144\n\t" \
  "s_cmp_eq_u32 vcc_lo, 0\n\t" \
  "s_cbranch_scc1 15f\n\t" \
  "14:\n\t" \
  "v_mfma_f32_16x16x32_bf16 v[252:255], v[80:83], v[104:107], v[252:255]\n\t" \
  "ds_read_b64_tr_b16 v[92:93], v62 offset:26624\n\t" \
  "v_mfma_f32_16x16x32_bf16 v[220:223], v[64:67], v[104:107], v[220:223]\n\t" \
  "ds_read_b64_tr_b16 v[94:95], v62 offset:27136\n\t" \
  "v_mfma_f32_16x16x32_bf16 v[228:231], v[68:71], v[104:107], v[228:231]\n\t" \
  "ds_read_b64_tr_b16 v[96:97], v63 offset:26624\n\t" \
  "v_mfma_f32_16x16x32_bf16 v[236:239], v[72:75], v[104:107], v[236:239]\n\t" \
  "ds_read_b64_tr_b16 v[98:99], v63 offset:27136\n\t" \
  "v_mfma_f32_16x16x32_bf16 v[244:247], v[76:79], v[104:107], v[244:247]\n\t" \
  "ds_read_b64_tr_b16 v[100:101], v62 offset:34816\n\t" \
  "v_mfma_f32_16x16x32_bf16 v[152:155], v[84:87], v[64:67], v[152:155]\n\t" \
  "ds_read_b64_tr_b16 v[102:103], v62 offset:35328\n\t" \
  "v_mfma_f32_16x16x32_bf16 v[160:163], v[84:87], v[68:71], v[160:163]\n\t" \
  "ds_read_b128 v[80:83], v60 offset:0\n\t" \
  "v_mfma_f32_16x16x32_bf16 v[168:171], v[84:87], v[72:75], v[168:171]\n\t" \
  "ds_read_b64_tr_b16 v[104:105], v63 offset:34816\n\t" \
  "v_mfma_f32_16x16x32_bf16 v[176:179], v[84:87], v[76:79], v[176:179]\n\t" \
  "ds_read_b64_tr_b16 v[106:107], v63 offset:35328\n\t" \
  "v_mfma_f32_16x16x32_bf16 v[156:159], v[88:91], v[64:67], v[156:159]\n\t" \
  "ds_read_b128 v[64:67], v60 offset:10240\n\t" \
  "v_mfma_f32_16x16x32_bf16 v[164:167], v[88:91], v[68:71], v[164:167]\n\t" \
  "ds_read_b128 v[68:71], v60 offset:12288\n\t" \
  "v_mfma_f32_16x16x32_bf16 v[172:175], v[88:91], v[72:75], v[172:175]\n\t" \
  "ds_read_b128 v[72:75], v60 offset:14336\n\t" \
  "v_mfma_f32_16x16x32_bf16 v[180:183], v[88:91], v[76:79], v[180:183]\n\t" \
  "ds_read_b128 v[76:79], v60 offset:16384\n\t" \
  "v_mfma_f32_16x16x32_bf16 v[184:187], v[136:139], v[108:111], v[184:187]\n\t" \
  "ds_read_b64_tr_b16 v[84:85], v62 offset:18432\n\t" \
  "v_mfma_f32_16x16x32_bf16 v[192:195], v[136:139], v[112:115], v[192:195]\n\t" \
  "ds_read_b64_tr_b16 v[86:87], v62 offset:18944\n\t" \
  "v_mfma_f32_16x16x32_bf16 v[200:203], v[136:139], v[116:119], v[200:203]\n\t" \
  "ds_read_b64_tr_b16 v[88:89], v63 offset:18432\n\t" \
  "v_mfma_f32_16x16x32_bf16 v[208:211], v[136:139], v[120:123], v[208:211]\n\t" \
  "ds_read_b64_tr_b16 v[90:91], v63 offset:18944\n\t" \
  "v_mfma_f32_16x16x32_bf16 v[188:191], v[140:143], v[108:111], v[188:191]\n\t" \
  "ds_read_b64_tr_b16 v[136:137], v62 offset:30720\n\t" \
  "v_mfma_f32_16x16x32_bf16 v[196:199], v[140:143], v[112:115], v[196:199]\n\t" \
  "ds_read_b64_tr_b16 v[138:139], v62 offset:31232\n\t" \
  "v_mfma_f32_16x16x32_bf16 v[204:207], v[140:143], v[116:119], v[204:207]\n\t" \
  "v_mfma_f32_16x16x32_bf16 v[212:215], v[140:143], v[120:123], v[212:215]\n\t" \
  "ds_read_b64_tr_b16 v[140:141], v63 offset:30720\n\t" \
  "v_mfma_f32_16x16x32_bf16 v[248:251], v[124:127], v[144:147], v[248:251]\n\t" \
  "ds_read_b64_tr_b16 v[142:143], v63 offset:31232\n\t" \
  "v_mfma_f32_16x16x32_bf16 v[216:219], v[108:111], v[144:147], v[216:219]\n\t" \
  "v_mfma_f32_16x16x32_bf16 v[224:227], v[112:115], v[144:147], v[224:227]\n\t" \
  "v_mfma_f32_16x16x32_bf16 v[232:235], v[116:119], v[144:147], v[232:235]\n\t" \
  "v_mfma_f32_16x16x32_bf16 v[240:243], v[120:123], v[144:147], v[240:243]\n\t" \
  "ds_read_b64_tr_b16 v[144:145], v62 offset:38912\n\t" \
  "v_mfma_f32_16x16x32_bf16 v[252:255], v[124:127], v[148:151], v[252:255]\n\t" \
  "ds_read_b64_tr_b16 v[146:147], v62 offset:39424\n\t" \
  "v_mfma_f32_16x16x32_bf16 v[220:223], v[108:111], v[148:151], v[220:223]\n\t" \
  "ds_read_b128 v[124:127], v61 offset:0\n\t" \
  "v_mfma_f32_16x16x32_bf16 v[228:231], v[112:115], v[148:151], v[228:231]\n\t" \
  "v_mfma_f32_16x16x32_bf16 v[236:239], v[116:119], v[148:151], v[236:239]\n\t" \
  "v_mfma_f32_16x16x32_bf16 v[244:247], v[120:123], v[148:151], v[244:247]\n\t" \
  "ds_read_b64_tr_b16 v[148:149], v63 offset:38912\n\t" \
  "v_mfma_f32_16x16x32_bf16 v[152:155], v[128:131], v[108:111], v[152:155]\n\t" \
  "ds_read_b64_tr_b16 v[150:151], v63 offset:39424\n\t" \
  "v_mfma_f32_16x16x32_bf16 v[160:163], v[128:131], v[112:115], v[160:163]\n\t" \
  "v_mfma_f32_16x16x32_bf16 v[168:171], v[128:131], v[116:119], v[168:171]\n\t" \
  "v_mfma_f32_16x16x32_bf16 v[176:179], v[128:131], v[120:123], v[176:179]\n\t" \
  "ds_read_b64_tr_b16 v[128:129], v62 offset:22528\n\t" \
  "v_mfma_f32_16x16x32_bf16 v[156:159], v[132:135], v[108:111], v[156:159]\n\t" \
  "ds_read_b128 v[108:111], v61 offset:10240\n\t" \
  "v_mfma_f32_16x16x32_bf16 v[164:167], v[132:135], v[112:115], v[164:167]\n\t" \
  "ds_read_b128 v[112:115], v61 offset:12288\n\t" \
  "v_mfma_f32_16x16x32_bf16 v[172:175], v[132:135], v[116:119], v[172:175]\n\t" \
  "ds_read_b128 v[116:119], v61 offset:14336\n\t" \
  "v_mfma_f32_16x16x32_bf16 v[180:183], v[132:135], v[120:123], v[180:183]\n\t" \
  "ds_read_b128 v[120:123], v61 offset:16384\n\t" \
  "s_waitcnt lgkmcnt(5)\n\t" \
  "v_mfma_f32_16x16x32_bf16 v[184:187], v[92:95], v[64:67], v[184:187]\n\t" \
  "ds_read_b64_tr_b16 v[130:131], v62 offset:23040\n\t" \
  "v_mfma_f32_16x16x32_bf16 v[192:195], v[92:95], v[68:71], v[192:195]\n\t" \
  "ds_read_b64_tr_b16 v[132:133], v63 offset:22528\n\t" \
  "v_mfma_f32_16x16x32_bf16 v[200:203], v[92:95], v[72:75], v[200:203]\n\t" \
  "ds_read_b64_tr_b16 v[134:135], v63 offset:23040\n\t" \
  "v_mfma_f32_16x16x32_bf16 v[208:211], v[92:95], v[76:79], v[208:211]\n\t" \
  "v_mfma_f32_16x16x32_bf16 v[188:191], v[96:99], v[64:67], v[188:191]\n\t" \
  "v_mfma_f32_16x16x32_bf16 v[196:199], v[96:99], v[68:71], v[196:199]\n\t" \
  "v_mfma_f32_16x16x32_bf16 v[204:207], v[96:99], v[72:75], v[204:207]\n\t" \
  "v_mfma_f32_16x16x32_bf16 v[212:215], v[96:99], v[76:79], v[212:215]\n\t" \
  "v_mfma_f32_16x16x32_bf16 v[248:251], v[80:83], v[100:103], v[248:251]\n\t" \
  "v_mfma_f32_16x16x32_bf16 v[216:219], v[64:67], v[100:103], v[216:219]\n\t" \
  "v_mfma_f32_16x16x32_bf16 v[224:227], v[68:71], v[100:103], v[224:227]\n\t" \
  "v_mfma_f32_16x16x32_bf16 v[232:235], v[72:75], v[100:103], v[232:235]\n\t" \
  "502:\n\t" \
  "s_cbranch_execz 506f\n\t" \
  "v_mfma_f32_16x16x32_bf16 v[240:243], v[76:79], v[100:103], v[240:243]\n\t" \
  "s_waitcnt lgkmcnt(0)\n\t" \
  "ds_write_b32 v32, v0\n\t" \
  "v_mfma_f32_16x16x32_bf16 v[252:255], v[80:83], v[104:107], v[252:255]\n\t" \
  "v_mfma_f32_16x16x32_bf16 v[220:223], v[64:67], v[104:107], v[220:223]\n\t" \
  "v_mfma_f32_16x16x32_bf16 v[228:231], v[68:71], v[104:107], v[228:231]\n\t" \
  "v_mfma_f32_16x16x32_bf16 v[236:239], v[72:75], v[104:107], v[236:239]\n\t" \
  "v_mfma_f32_16x16x32_bf16 v[244:247], v[76:79], v[104:107], v[244:247]\n\t" \
  "v_mfma_f32_16x16x32_bf16 v[152:155], v[84:87], v[64:67], v[152:155]\n\t" \
  "v_mfma_f32_16x16x32_bf16 v[160:163], v[84:87], v[68:71], v[160:163]\n\t" \
  "v_mfma_f32_16x16x32_bf16 v[168:171], v[84:87], v[72:75], v[168:171]\n\t" \
  "v_mfma_f32_16x16x32_bf16 v[176:179], v[84:87], v[76:79], v[176:179]\n\t" \
  "v_mfma_f32_16x16x32_bf16 v[156:159], v[88:91], v[64:67], v[156:159]\n\t" \
  "v_mfma_f32_16x16x32_bf16 v[164:167], v[88:91], v[68:71], v[164:167]\n\t" \
  "v_mfma_f32_16x16x32_bf16 v[172:175], v[88:91], v[72:75], v[172:175]\n\t" \
  "v_mfma_f32_16x16x32_bf16 v[180:183], v[88:91], v[76:79], v[180:183]\n\t" \
  "v_mfma_f32_16x16x32_bf16 v[184:187], v[136:139], v[108:111], v[184:187]\n\t" \
  "v_and_b32 v56, -16, v32\n\t" \
  "v_mfma_f32_16x16x32_bf16 v[192:195], v[136:139], v[112:115], v[192:195]\n\t" \
  "v_mfma_f32_16x16x32_bf16 v[200:203], v[136:139], v[116:119], v[200:203]\n\t" \
  "v_mfma_f32_16x16x32_bf16 v[208:211], v[136:139], v[120:123], v[208:211]\n\t" \
  "v_mfma_f32_16x16x32_bf16 v[188:191], v[140:143], v[108:111], v[188:191]\n\t" \
  "v_mfma_f32_16x16x32_bf16 v[196:199], v[140:143], v[112:115], v[196:199]\n\t" \
  "v_mfma_f32_16x16x32_bf16 v[204:207], v[140:143], v[116:119], v[204:207]\n\t" \
  "v_mfma_f32_16x16x32_bf16 v[212:215], v[140:143], v[120:123], v[212:215]\n\t" \
  "v_cvt_pk_bf16_f32 v14, v184, v185\n\t" \
  "v_cvt_pk_bf16_f32 v15, v186, v187\n\t" \
  "v_mfma_f32_16x16x32_bf16 v[152:155], v[128:131], v[108:111], v[152:155]\n\t" \
  "v_cvt_pk_bf16_f32 v18, v192, v193\n\t" \
  "v_cvt_pk_bf16_f32 v19, v194, v195\n\t" \
  "v_mfma_f32_16x16x32_bf16 v[160:163], v[128:131], v[112:115], v[160:163]\n\t" \
  "v_cvt_pk_bf16_f32 v16, v188, v189\n\t" \
  "v_cvt_pk_bf16_f32 v17, v190, v191\n\t" \
  "v_mfma_f32_16x16x32_bf16 v[168:171], v[128:131], v[116:119], v[168:171]\n\t" \
  "v_cvt_pk_bf16_f32 v20, v196, v197\n\t" \
  "v_cvt_pk_bf16_f32 v21, v198, v199\n\t" \
  "v_mfma_f32_16x16x32_bf16 v[176:179], v[128:131], v[120:123], v[176:179]\n\t" \
  "v_cvt_pk_bf16_f32 v22, v200, v201\n\t" \
  "v_cvt_pk_bf16_f32 v23, v202, v203\n\t" \
  "v_mfma_f32_16x16x32_bf16 v[156:159], v[132:135], v[108:111], v[156:159]\n\t" \
  "v_cvt_pk_bf16_f32 v24, v204, v205\n\t" \
  "v_cvt_pk_bf16_f32 v25, v206, v207\n\t" \
  "v_mfma_f32_16x16x32_bf16 v[164:167], v[132:135], v[112:115], v[164:167]\n\t" \
  "v_cvt_pk_bf16_f32 v26, v208, v209\n\t" \
  "v_cvt_pk_bf16_f32 v27, v210, v211\n\t" \
  "v_mfma_f32_16x16x32_bf16 v[172:175], v[132:135], v[116:119], v[172:175]\n\t" \
  "v_cvt_pk_bf16_f32 v28, v212, v213\n\t" \
  "v_cvt_pk_bf16_f32 v29, v214, v215\n\t" \
  "v_mfma_f32_16x16x32_bf16 v[180:183], v[132:135], v[120:123], v[180:183]\n\t" \
  "v_cvt_pk_bf16_f32 v84, v152, v153\n\t" \
  "v_cvt_pk_bf16_f32 v85, v154, v155\n\t" \
  "v_mfma_f32_16x16x32_bf16 v[248:251], v[124:127], v[144:147], v[248:251]\n\t" \
  "v_cvt_pk_bf16_f32 v86, v156, v157\n\t" \
  "v_cvt_pk_bf16_f32 v87, v158, v159\n\t" \
  "v_mfma_f32_16x16x32_bf16 v[216:219], v[108:111], v[144:147], v[216:219]\n\t" \
  "v_cvt_pk_bf16_f32 v88, v160, v161\n\t" \
  "v_cvt_pk_bf16_f32 v89, v162, v163\n\t" \
  "v_mfma_f32_16x16x32_bf16 v[224:227], v[112:115], v[144:147], v[224:227]\n\t" \
  "v_cvt_pk_bf16_f32 v90, v164, v165\n\t" \
  "v_cvt_pk_bf16_f32 v91, v166, v167\n\t" \
  "v_mfma_f32_16x16x32_bf16 v[232:235], v[116:119], v[144:147], v[232:235]\n\t" \
  "v_cvt_pk_bf16_f32 v72, v248, v249\n\t" \
  "v_cvt_pk_bf16_f32 v73, v250, v251\n\t" \
  "v_mfma_f32_16x16x32_bf16 v[240:243], v[120:123], v[144:147], v[240:243]\n\t" \
  "ds_read_b32 v52, v33 offset:48\n\t" \
  "ds_read_b128 v[10:13], v31\n\t" \
  "v_cvt_pk_bf16_f32 v92, v168, v169\n\t" \
  "v_mfma_f32_16x16x32_bf16 v[252:255], v[124:127], v[148:151], v[252:255]\n\t" \
  "v_cvt_pk_bf16_f32 v93, v170, v171\n\t" \
  "v_cvt_pk_bf16_f32 v94, v172, v173\n\t" \
  "v_mfma_f32_16x16x32_bf16 v[220:223], v[108:111], v[148:151], v[220:223]\n\t" \
  "v_cvt_pk_bf16_f32 v95, v174, v175\n\t" \
  "v_cvt_pk_bf16_f32 v96, v176, v177\n\t" \
  "v_mfma_f32_16x16x32_bf16 v[228:231], v[112:115], v[148:151], v[228:231]\n\t" \
  "v_cvt_pk_bf16_f32 v97, v178, v179\n\t" \
  "v_cvt_pk_bf16_f32 v98, v180, v181\n\t" \
  "v_mfma_f32_16x16x32_bf16 v[236:239], v[116:119], v[148:151], v[236:239]\n\t" \
  "v_cvt_pk_bf16_f32 v82, v252, v253\n\t" \
  "v_cvt_pk_bf16_f32 v83, v254, v255\n\t" \
  "v_mfma_f32_16x16x32_bf16 v[244:247], v[120:123], v[148:151], v[244:247]\n\t" \
  "ds_write_b64 v30, v[72:73]\n\t" \
  "ds_write_b64 v30, v[82:83] offset:8\n\t" \
  "ds_write_b32 v32, v0 offset:48\n\t" \
  "v_mfma_f32_16x16x32_bf16 v[200:203], v[22:25], v[92:95], v[34:37]\n\t" \
  "v_cvt_pk_bf16_f32 v99, v182, v183\n\t" \
  "s_waitcnt lgkmcnt(0)\n\t" \
  "v_readfirstlane_b32 vcc_lo, v52\n\t" \
  "s_nop 1\n\t" \
  "s_cmp_eq_u32 vcc_lo, 0\n\t" \
  "s_cbranch_scc1 5f\n\t" \
  "4:\n\t" \
  "v_mfma_f32_16x16x32_bf16 v[204:207], v[18:21], v[92:95], v[38:41]\n\t" \
  "v_cvt_pk_bf16_f32 v66, v232, v233\n\t" \
  "v_cvt_pk_bf16_f32 v67, v234, v235\n\t" \
  "v_mfma_f32_16x16x32_bf16 v[208:211], v[26:29], v[96:99], v[34:37]\n\t" \
  "v_cvt_pk_bf16_f32 v76, v236, v237\n\t" \
  "v_cvt_pk_bf16_f32 v77, v238, v239\n\t" \
  "v_mfma_f32_16x16x32_bf16 v[212:215], v[22:25], v[96:99], v[38:41]\n\t" \
  "v_cvt_pk_bf16_f32 v64, v240, v241\n\t" \
  "v_cvt_pk_bf16_f32 v65, v242, v243\n\t" \
  "v_mfma_f32_16x16x32_bf16 v[192:195], v[18:21], v[88:91], v[34:37]\n\t" \
  "v_max_f32 v200, v200, v204\n\t" \
  "v_max_f32 v201, v201, v205\n\t" \
  "v_mfma_f32_16x16x32_bf16 v[196:199], v[14:17], v[88:91], v[38:41]\n\t" \
  "v_max_f32 v202, v202, v206\n\t" \
  "v_max_f32 v203, v203, v207\n\t" \
  "v_mfma_f32_16x16x32_bf16 v[184:187], v[14:17], v[84:87], v[34:37]\n\t" \
  "v_max_f32 v208, v208, v212\n\t" \
  "v_max_f32 v209, v209, v213\n\t" \
  "v_mfma_f32_16x16x32_bf16 v[188:191], v[10:13], v[84:87], v[42:45]\n\t" \
  "v_max_f32 v210, v210, v214\n\t" \
  "v_max_f32 v211, v211, v215\n\t" \
  "v_max_f32 v192, v192, v196\n\t" \
  "v_max_f32 v193, v193, v197\n\t" \
  "v_max_f32 v194, v194, v198\n\t" \
  "v_max3_f32 v106, v208, v209, v210\n\t" \
  "v_max3_f32 v104, v200, v201, v202\n\t" \
  "v_max_f32 v195, v195, v199\n\t" \
  "v_max_f32 v184, v184, v188\n\t" \
  "v_max_f32 v185, v185, v189\n\t" \
  "v_max_f32 v186, v186, v190\n\t" \
  "v_max3_f32 v102, v192, v193, v194\n\t" \
  "v_max_f32 v187, v187, v191\n\t" \
  "v_max3_f32 v100, v184, v185, v186\n\t" \
  "v_max_f32 v106, v106, v211\n\t" \
  "v_max_f32 v104, v104, v203\n\t" \
  "v_max_f32 v102, v102, v195\n\t" \
  "v_max_f32 v100, v100, v187\n\t" \
  "v_permlane16_swap_b32 v104, v106\n\t" \
  "v_cvt_pk_bf16_f32 v74, v244, v245\n\t" \
  "v_permlane16_swap_b32 v100, v102\n\t" \
  "v_max_f32 v104, v104, v106\n\t" \
  "v_max_f32 v100, v100, v102\n\t" \
  "v_cvt_pk_bf16_f32 v75, v246, v247\n\t" \
  "v_cvt_pk_bf16_f32 v68, v224, v225\n\t" \
  "v_permlane32_swap_b32 v100, v104\n\t" \
  "v_cvt_pk_bf16_f32 v69, v226, v227\n\t" \
  "v_max_f32 v100, v100, v104\n\t" \
  "v_mul_f32 v100, 0xbe8293ee, v100\n\t" \
  "v_mov_b32 v104, v100\n\t" \
  "v_cvt_pk_bf16_f32 v78, v228, v229\n\t" \
  "v_cvt_pk_bf16_f32 v79, v230, v231\n\t" \
  "v_permlane32_swap_b32 v100, v104\n\t" \
  "v_cvt_pk_bf16_f32 v70, v216, v217\n\t" \
  "v_mov_b32 v106, v104\n\t" \
  "v_mov_b32 v102, v100\n\t" \
  "v_cvt_pk_bf16_f32 v71, v218, v219\n\t" \
  "v_permlane16_swap_b32 v104, v106\n\t" \
  "v_permlane16_swap_b32 v100, v102\n\t" \
  "v_fma_f32 v208, v208, %16, v106\n\t" \
  "v_fma_f32 v209, v209, %16, v106\n\t" \
  "v_fma_f32 v210, v210, %16, v106\n\t" \
  "v_fma_f32 v211, v211, %16, v106\n\t" \
  "v_exp_f32 v208, v208\n\t" \
  "v_exp_f32 v209, v209\n\t" \
  "v_exp_f32 v210, v210\n\t" \
  "v_exp_f32 v211, v211\n\t" \
  "v_cvt_pk_bf16_f32 v212, v208, v209\n\t" \
  "v_cvt_pk_bf16_f32 v213, v210, v211\n\t" \
  "v_bfi_b32 v214, v46, 0, v212\n\t" \
  "v_bfi_b32 v215, v47, 0, v213\n\t" \
  "v_and_b32 v212, v46, v212\n\t" \
  "v_and_b32 v213, v47, v213\n\t" \
  "v_fma_f32 v200, v200, %16, v104\n\t" \
  "v_fma_f32 v201, v201, %16, v104\n\t" \
  "v_mfma_f32_16x16x32_bf16 v[208:211], v[48:51], v[212:215], 0\n\t" \
  "v_fma_f32 v202, v202, %16, v104\n\t" \
  "v_fma_f32 v203, v203, %16, v104\n\t" \
  "v_mfma_f32_16x16x32_bf16 v[176:179], v[64:67], v[212:215], 0\n\t" \
  "v_exp_f32 v200, v200\n\t" \
  "v_mfma_f32_16x16x32_bf16 v[180:183], v[74:77], v[212:215], 0\n\t" \
  "v_exp_f32 v201, v201\n\t" \
  "v_exp_f32 v202, v202\n\t" \
  "v_exp_f32 v203, v203\n\t" \
  "v_cvt_pk_bf16_f32 v204, v200, v201\n\t" \
  "v_cvt_pk_bf16_f32 v205, v202, v203\n\t" \
  "v_bfi_b32 v206, v46, 0, v204\n\t" \
  "v_bfi_b32 v207, v47, 0, v205\n\t" \
  "v_rcp_f32 v106, v208\n\t" \
  "v_and_b32 v204, v46, v204\n\t" \
  "v_and_b32 v205, v47, v205\n\t" \
  "v_pk_mul_f32 v[176:177], v[176:177], v[106:107] op_sel_hi:[1,0]\n\t" \
  "v_pk_mul_f32 v[178:179], v[178:179], v[106:107] op_sel_hi:[1,0]\n\t" \
  "v_mfma_f32_16x16x32_bf16 v[200:203], v[48:51], v[204:207], 0\n\t" \
  "v_pk_mul_f32 v[180:181], v[180:181], v[106:107] op_sel_hi:[1,0]\n\t" \
  "v_pk_mul_f32 v[182:183], v[182:183], v[106:107] op_sel_hi:[1,0]\n\t" \
  "v_mfma_f32_16x16x32_bf16 v[168:171], v[66:69], v[204:207], 0\n\t" \
  "v_cvt_pk_bf16_f32 v240, v176, v177\n\t" \
  "v_cvt_pk_bf16_f32 v241, v178, v179\n\t" \
  "v_mfma_f32_16x16x32_bf16 v[172:175], v[76:79], v[204:207], 0\n\t" \
  "v_cvt_pk_bf16_f32 v242, v180, v181\n\t" \
  "v_cvt_pk_bf16_f32 v243, v182, v183\n\t" \
  "global_store_dwordx4 v1, v[240:243], %14 offset:0 sc1\n\t" \
  "v_fma_f32 v192, v192, %16, v102\n\t" \
  "v_fma_f32 v193, v193, %16, v102\n\t" \
  "v_fma_f32 v194, v194, %16, v102\n\t" \
  "v_fma_f32 v195, v195, %16, v102\n\t" \
  "v_exp_f32 v192, v192\n\t" \
  "v_exp_f32 v193, v193\n\t" \
  "v_exp_f32 v194, v194\n\t" \
  "v_exp_f32 v195, v195\n\t" \
  "v_cvt_pk_bf16_f32 v196, v192, v193\n\t" \
  "v_cvt_pk_bf16_f32 v197, v194, v195\n\t" \
  "v_bfi_b32 v198, v46, 0, v196\n\t" \
  "v_bfi_b32 v199, v47, 0, v197\n\t" \
  "v_rcp_f32 v104, v200\n\t" \
  "v_and_b32 v196, v46, v196\n\t" \
  "v_and_b32 v197, v47, v197\n\t" \
  "v_cvt_pk_bf16_f32 v80, v220, v221\n\t" \
  "v_cvt_pk_bf16_f32 v81, v222, v223\n\t" \
  "v_mfma_f32_16x16x32_bf16 v[192:195], v[48:51], v[196:199], 0\n\t" \
  "v_pk_mul_f32 v[168:169], v[168:169], v[104:105] op_sel_hi:[1,0]\n\t" \
  "v_pk_mul_f32 v[170:171], v[170:171], v[104:105] op_sel_hi:[1,0]\n\t" \
  "v_mfma_f32_16x16x32_bf16 v[160:163], v[68:71], v[196:199], 0\n\t" \
  "v_pk_mul_f32 v[172:173], v[172:173], v[104:105] op_sel_hi:[1,0]\n\t" \
  "v_pk_mul_f32 v[174:175], v[174:175], v[104:105] op_sel_hi:[1,0]\n\t" \
  "v_mfma_f32_16x16x32_bf16 v[164:167], v[78:81], v[196:199], 0\n\t" \
  "v_cvt_pk_bf16_f32 v232, v168, v169\n\t" \
  "v_cvt_pk_bf16_f32 v233, v170, v171\n\t" \
  "v_cvt_pk_bf16_f32 v234, v172, v173\n\t" \
  "v_cvt_pk_bf16_f32 v235, v174, v175\n\t" \
  "global_store_dwordx4 v1, v[232:235], %14 offset:-4096 sc1\n\t" \
  "v_fma_f32 v184, v184, %16, v100\n\t" \
  "v_fma_f32 v185, v185, %16, v100\n\t" \
  "v_fma_f32 v186, v186, %16, v100\n\t" \
  "v_fma_f32 v187, v187, %16, v100\n\t" \
  "v_exp_f32 v184, v184\n\t" \
  "v_exp_f32 v185, v185\n\t" \
  "v_exp_f32 v186, v186\n\t" \
  "v_exp_f32 v187, v187\n\t" \
  "v_cvt_pk_bf16_f32 v188, v184, v185\n\t" \
  "v_cvt_pk_bf16_f32 v189, v186, v187\n\t" \
  "v_bfi_b32 v190, v46, 0, v188\n\t" \
  "v_bfi_b32 v191, v47, 0, v189\n\t" \
  "v_rcp_f32 v102, v192\n\t" \
  "v_and_b32 v188, v46, v188\n\t" \
  "v_and_b32 v189, v47, v189\n\t" \
  "v_pk_mul_f32 v[160:161], v[160:161], v[102:103] op_sel_hi:[1,0]\n\t" \
  "v_pk_mul_f32 v[162:163], v[162:163], v[102:103] op_sel_hi:[1,0]\n\t" \
  "v_mfma_f32_16x16x32_bf16 v[184:187], v[48:51], v[188:191], 0\n\t" \
  "v_pk_mul_f32 v[164:165], v[164:165], v[102:103] op_sel_hi:[1,0]\n\t" \
  "v_pk_mul_f32 v[166:167], v[166:167], v[102:103] op_sel_hi:[1,0]\n\t" \
  "v_mfma_f32_16x16x32_bf16 v[152:155], v[70:73], v[188:191], 0\n\t" \
  "v_cvt_pk_bf16_f32 v224, v160, v161\n\t" \
  "v_cvt_pk_bf16_f32 v225, v162, v163\n\t" \
  "v_mfma_f32_16x16x32_bf16 v[156:159], v[80:83], v[188:191], 0\n\t" \
  "v_cvt_pk_bf16_f32 v226, v164, v165\n\t" \
  "v_cvt_pk_bf16_f32 v227, v166, v167\n\t" \
  "global_store_dwordx4 v4, v[224:227], %14 offset:0 sc1\n\t" \
  "v_rcp_f32 v100, v184\n\t" \
  "ds_read_b128 v[60:63], v56 offset:64\n\t" \
  "ds_read_b64_tr_b16 v[108:109], v5 offset:0\n\t" \
  "ds_read_b64_tr_b16 v[110:111], v5 offset:16\n\t" \
  "ds_read_b128 v[112:115], v6 offset:16384\n\t" \
  "ds_read_b128 v[116:119], v6 offset:15360\n\t" \
  "ds_read_b64_tr_b16 v[120:121], v5 offset:512\n\t" \
  "ds_read_b64_tr_b16 v[122:123], v5 offset:528\n\t" \
  "ds_read_b128 v[124:127], v6 offset:14336\n\t" \
  "ds_read_b64_tr_b16 v[128:129], v5 offset:1024\n\t" \
  "ds_read_b64_tr_b16 v[130:131], v5 offset:1040\n\t" \
  "ds_read_b128 v[132:135], v6 offset:13312\n\t" \
  "ds_read_b64_tr_b16 v[136:137], v5 offset:1536\n\t" \
  "ds_read_b64_tr_b16 v[138:139], v5 offset:1552\n\t" \
  "ds_read_b128 v[140:143], v6 offset:12288\n\t" \
  "ds_read_b64_tr_b16 v[144:145], v5 offset:2048\n\t" \
  "v_pk_mul_f32 v[152:153], v[152:153], v[100:101] op_sel_hi:[1,0]\n\t" \
  "v_pk_mul_f32 v[154:155], v[154:155], v[100:101] op_sel_hi:[1,0]\n\t" \
  "v_cvt_pk_bf16_f32 v216, v152, v153\n\t" \
  "v_pk_mul_f32 v[156:157], v[156:157], v[100:101] op_sel_hi:[1,0]\n\t" \
  "v_pk_mul_f32 v[158:159], v[158:159], v[100:101] op_sel_hi:[1,0]\n\t" \
  "v_cvt_pk_bf16_f32 v217, v154, v155\n\t" \
  "v_cvt_pk_bf16_f32 v218, v156, v157\n\t" \
  "v_cvt_pk_bf16_f32 v219, v158, v159\n\t" \
  "global_store_dwordx4 v4, v[216:219], %14 offset:-4096 sc1\n\t" \
  "ds_write_b32 v32, v0 offset:112\n\t" \
  "s_branch 9f\n\t" \
  "5:\n\t" \
  "ds_read_b32 v52, v33 offset:48\n\t" \
  "ds_read_b128 v[10:13], v31\n\t" \
  "s_waitcnt lgkmcnt(0)\n\t" \
  "v_readfirstlane_b32 vcc_lo, v52\n\t" \
  "s_nop 1\n\t" \
  "s_cmp_eq_u32 vcc_lo, 0\n\t" \
  "s_cbranch_scc1 5b\n\t" \
  "s_branch 4b\n\t" \
  "15:\n\t" \
  "ds_read_b128 v[10:13], v14 offset:128\n\t" \
  "s_waitcnt lgkmcnt(0)\n\t" \
  "v_and_b32 v10, v10, v11\n\t" \
  "v_and_b32 v12, v12, v13\n\t" \
  "v_and_b32 v10, v10, v12\n\t" \
  "s_nop 1\n\t" \
  "v_readfirstlane_b32 vcc_lo, v10\n\t" \
  "s_nop 1\n\t" \
  "s_cmp_eq_u32 vcc_lo, 0\n\t" \
  "s_cbranch_scc1 15b\n\t" \
  "s_branch 14b\n\t" \
  "8:\n\t" \
  "global_load_dwordx2 v[10:11], v51, %11\n\t" \
  "global_load_dwordx2 v[12:13], v51, %11 offset:128\n\t" \
  "v_xor_b32 v53, 64, v52\n\t" \
  "v_xor_b32 v55, 32, v54\n\t" \
  "v_add_u32 v56, 43008, v52\n\t" \
  "v_add_u32 v58, 43008, v54\n\t" \
  "v_xor_b32 v57, 64, v56\n\t" \
  "v_xor_b32 v59, 32, v58\n\t" \
  "v_add_u32 v60, 86016, v52\n\t" \
  "v_add_u32 v62, 86016, v54\n\t" \
  "v_xor_b32 v61, 64, v60\n\t" \
  "v_xor_b32 v63, 32, v62\n\t" \
  "s_mov_b32 vcc_hi, 0x05040100\n\t" \
  "s_waitcnt vmcnt(2)\n\t" \
  "s_barrier\n\t" \
  "ds_read_b64_tr_b16 v[92:93], v62 offset:26624\n\t" \
  "ds_read_b64_tr_b16 v[94:95], v62 offset:27136\n\t" \
  "ds_read_b128 v[80:83], v60 offset:0\n\t" \
  "ds_read_b128 v[64:67], v60 offset:2048\n\t" \
  "ds_read_b128 v[68:71], v60 offset:4096\n\t" \
  "ds_read_b128 v[72:75], v60 offset:6144\n\t" \
  "ds_read_b128 v[76:79], v60 offset:8192\n\t" \
  "ds_read_b64_tr_b16 v[96:97], v63 offset:26624\n\t" \
  "ds_read_b64_tr_b16 v[98:99], v63 offset:27136\n\t" \
  "ds_read_b64_tr_b16 v[100:101], v62 offset:34816\n\t" \
  "ds_read_b64_tr_b16 v[102:103], v62 offset:35328\n\t" \
  "ds_read_b64_tr_b16 v[104:105], v63 offset:34816\n\t" \
  "ds_read_b64_tr_b16 v[106:107], v63 offset:35328\n\t" \
  "ds_read_b64_tr_b16 v[84:85], v62 offset:18432\n\t" \
  "ds_read_b64_tr_b16 v[86:87], v62 offset:18944\n\t" \
  "ds_read_b64_tr_b16 v[88:89], v63 offset:18432\n\t" \
  "s_waitcnt lgkmcnt(13)\n\t" \
  "v_mfma_f32_16x16x32_bf16 v[248:251], v[92:95], v[80:83], 0\n\t" \
  "ds_read_b64_tr_b16 v[90:91], v63 offset:18944\n\t" \
  "ds_read_b64_tr_b16 v[136:137], v62 offset:30720\n\t" \
  "s_waitcnt lgkmcnt(14)\n\t" \
  "v_mfma_f32_16x16x32_bf16 v[184:187], v[92:95], v[64:67], 0\n\t" \
  "ds_read_b64_tr_b16 v[138:139], v62 offset:31232\n\t" \
  "ds_read_b128 v[124:127], v61 offset:0\n\t" \
  "s_waitcnt lgkmcnt(15)\n\t" \
  "v_mfma_f32_16x16x32_bf16 v[192:195], v[92:95], v[68:71], 0\n\t" \
  "ds_read_b128 v[108:111], v61 offset:2048\n\t" \
  "ds_read_b128 v[112:115], v61 offset:4096\n\t" \
  "s_waitcnt lgkmcnt(15)\n\t" \
  "v_mfma_f32_16x16x32_bf16 v[200:203], v[92:95], v[72:75], 0\n\t" \
  "ds_read_b128 v[116:119], v61 offset:6144\n\t" \
  "ds_read_b128 v[120:123], v61 offset:8192\n\t" \
  "v_mfma_f32_16x16x32_bf16 v[208:211], v[92:95], v[76:79], 0\n\t" \
  "ds_read_b64_tr_b16 v[140:141], v63 offset:30720\n\t" \
  "ds_read_b64_tr_b16 v[142:143], v63 offset:31232\n\t" \
  "s_waitcnt lgkmcnt(15)\n\t" \
  "v_mfma_f32_16x16x32_bf16 v[252:255], v[96:99], v[80:83], 0\n\t" \
  "ds_read_b64_tr_b16 v[144:145], v62 offset:38912\n\t" \
  "ds_read_b64_tr_b16 v[146:147], v62 offset:39424\n\t" \
  "v_mfma_f32_16x16x32_bf16 v[188:191], v[96:99], v[64:67], 0\n\t" \
  "ds_read_b64_tr_b16 v[148:149], v63 offset:38912\n\t" \
  "ds_read_b64_tr_b16 v[150:151], v63 offset:39424\n\t" \
  "v_mfma_f32_16x16x32_bf16 v[196:199], v[96:99], v[68:71], 0\n\t" \
  "ds_read_b64_tr_b16 v[128:129], v62 offset:22528\n\t" \
  "ds_read_b64_tr_b16 v[130:131], v62 offset:23040\n\t" \
  "v_mfma_f32_16x16x32_bf16 v[204:207], v[96:99], v[72:75], 0\n\t" \
  "ds_read_b64_tr_b16 v[132:133], v63 offset:22528\n\t" \
  "ds_read_b64_tr_b16 v[134:135], v63 offset:23040\n\t" \
  "v_mfma_f32_16x16x32_bf16 v[212:215], v[96:99], v[76:79], 0\n\t" \
  "v_mfma_f32_16x16x32_bf16 v[216:219], v[64:67], v[100:103], 0\n\t" \
  "v_mfma_f32_16x16x32_bf16 v[224:227], v[68:71], v[100:103], 0\n\t" \
  "v_mfma_f32_16x16x32_bf16 v[232:235], v[72:75], v[100:103], 0\n\t" \
  "s_waitcnt lgkmcnt(0)\n\t" \
  "s_getpc_b64 s[88:89]\n\t" \
  "s_branch 28b\n\t" \
  "s_barrier\n\t" \
  "v_mfma_f32_16x16x32_bf16 v[240:243], v[76:79], v[100:103], 0\n\t" \
  "ds_read_b64_tr_b16 v[92:93], v54 offset:26624\n\t" \
  "v_mfma_f32_16x16x32_bf16 v[220:223], v[64:67], v[104:107], 0\n\t" \
  "ds_read_b64_tr_b16 v[94:95], v54 offset:27136\n\t" \
  "v_mfma_f32_16x16x32_bf16 v[228:231], v[68:71], v[104:107], 0\n\t" \
  "ds_read_b128 v[80:83], v52 offset:0\n\t" \
  "v_mfma_f32_16x16x32_bf16 v[236:239], v[72:75], v[104:107], 0\n\t" \
  "ds_read_b64_tr_b16 v[96:97], v55 offset:26624\n\t" \
  "v_mfma_f32_16x16x32_bf16 v[244:247], v[76:79], v[104:107], 0\n\t" \
  "ds_read_b64_tr_b16 v[98:99], v55 offset:27136\n\t" \
  "v_mfma_f32_16x16x32_bf16 v[152:155], v[84:87], v[64:67], 0\n\t" \
  "ds_read_b64_tr_b16 v[100:101], v54 offset:34816\n\t" \
  "v_mfma_f32_16x16x32_bf16 v[160:163], v[84:87], v[68:71], 0\n\t" \
  "ds_read_b64_tr_b16 v[102:103], v54 offset:35328\n\t" \
  "v_mfma_f32_16x16x32_bf16 v[168:171], v[84:87], v[72:75], 0\n\t" \
  "ds_read_b64_tr_b16 v[104:105], v55 offset:34816\n\t" \
  "v_mfma_f32_16x16x32_bf16 v[176:179], v[84:87], v[76:79], 0\n\t" \
  "ds_read_b64_tr_b16 v[106:107], v55 offset:35328\n\t" \
  "v_mfma_f32_16x16x32_bf16 v[156:159], v[88:91], v[64:67], 0\n\t" \
  "ds_read_b128 v[64:67], v52 offset:2048\n\t" \
  "v_mfma_f32_16x16x32_bf16 v[164:167], v[88:91], v[68:71], 0\n\t" \
  "ds_read_b128 v[68:71], v52 offset:4096\n\t" \
  "v_mfma_f32_16x16x32_bf16 v[172:175], v[88:91], v[72:75], 0\n\t" \
  "ds_read_b128 v[72:75], v52 offset:6144\n\t" \
  "v_mfma_f32_16x16x32_bf16 v[180:183], v[88:91], v[76:79], 0\n\t" \
  "ds_read_b128 v[76:79], v52 offset:8192\n\t" \
  "v_mfma_f32_16x16x32_bf16 v[248:251], v[136:139], v[124:127], v[248:251]\n\t" \
  "ds_read_b64_tr_b16 v[84:85], v54 offset:18432\n\t" \
  "v_mfma_f32_16x16x32_bf16 v[184:187], v[136:139], v[108:111], v[184:187]\n\t" \
  "ds_read_b64_tr_b16 v[86:87], v54 offset:18944\n\t" \
  "v_mfma_f32_16x16x32_bf16 v[192:195], v[136:139], v[112:115], v[192:195]\n\t" \
  "ds_read_b64_tr_b16 v[88:89], v55 offset:18432\n\t" \
  "v_mfma_f32_16x16x32_bf16 v[200:203], v[136:139], v[116:119], v[200:203]\n\t" \
  "ds_read_b64_tr_b16 v[90:91], v55 offset:18944\n\t" \
  "v_mfma_f32_16x16x32_bf16 v[208:211], v[136:139], v[120:123], v[208:211]\n\t" \
  "ds_read_b64_tr_b16 v[136:137], v54 offset:30720\n\t" \
  "v_mfma_f32_16x16x32_bf16 v[252:255], v[140:143], v[124:127], v[252:255]\n\t" \
  "ds_read_b64_tr_b16 v[138:139], v54 offset:31232\n\t" \
  "v_mfma_f32_16x16x32_bf16 v[188:191], v[140:143], v[108:111], v[188:191]\n\t" \
  "ds_read_b128 v[124:127], v53 offset:0\n\t" \
  "v_mfma_f32_16x16x32_bf16 v[196:199], v[140:143], v[112:115], v[196:199]\n\t" \
  "v_mfma_f32_16x16x32_bf16 v[204:207], v[140:143], v[116:119], v[204:207]\n\t" \
  "v_mfma_f32_16x16x32_bf16 v[212:215], v[140:143], v[120:123], v[212:215]\n\t" \
  "ds_read_b64_tr_b16 v[140:141], v55 offset:30720\n\t" \
  "v_mfma_f32_16x16x32_bf16 v[216:219], v[108:111], v[144:147], v[216:219]\n\t" \
  "ds_read_b64_tr_b16 v[142:143], v55 offset:31232\n\t" \
  "v_mfma_f32_16x16x32_bf16 v[224:227], v[112:115], v[144:147], v[224:227]\n\t" \
  "v_mfma_f32_16x16x32_bf16 v[232:235], v[116:119], v[144:147], v[232:235]\n\t" \
  "v_mfma_f32_16x16x32_bf16 v[240:243], v[120:123], v[144:147], v[240:243]\n\t" \
  "ds_read_b64_tr_b16 v[144:145], v54 offset:38912\n\t" \
  "v_mfma_f32_16x16x32_bf16 v[220:223], v[108:111], v[148:151], v[220:223]\n\t" \
  "ds_read_b64_tr_b16 v[146:147], v54 offset:39424\n\t" \
  "v_mfma_f32_16x16x32_bf16 v[228:231], v[112:115], v[148:151], v[228:231]\n\t" \
  "v_mfma_f32_16x16x32_bf16 v[236:239], v[116:119], v[148:151], v[236:239]\n\t" \
  "v_mfma_f32_16x16x32_bf16 v[244:247], v[120:123], v[148:151], v[244:247]\n\t" \
  "ds_read_b64_tr_b16 v[148:149], v55 offset:38912\n\t" \
  "v_mfma_f32_16x16x32_bf16 v[152:155], v[128:131], v[108:111], v[152:155]\n\t" \
  "ds_read_b64_tr_b16 v[150:151], v55 offset:39424\n\t" \
  "v_mfma_f32_16x16x32_bf16 v[160:163], v[128:131], v[112:115], v[160:163]\n\t" \
  "v_mfma_f32_16x16x32_bf16 v[168:171], v[128:131], v[116:119], v[168:171]\n\t" \
  "v_mfma_f32_16x16x32_bf16 v[176:179], v[128:131], v[120:123], v[176:179]\n\t" \
  "ds_read_b64_tr_b16 v[128:129], v54 offset:22528\n\t" \
  "v_mfma_f32_16x16x32_bf16 v[156:159], v[132:135], v[108:111], v[156:159]\n\t" \
  "ds_read_b128 v[108:111], v53 offset:2048\n\t" \
  "v_mfma_f32_16x16x32_bf16 v[164:167], v[132:135], v[112:115], v[164:167]\n\t" \
  "ds_read_b128 v[112:115], v53 offset:4096\n\t" \
  "v_mfma_f32_16x16x32_bf16 v[172:175], v[132:135], v[116:119], v[172:175]\n\t" \
  "ds_read_b128 v[116:119], v53 offset:6144\n\t" \
  "v_mfma_f32_16x16x32_bf16 v[180:183], v[132:135], v[120:123], v[180:183]\n\t" \
  "ds_read_b128 v[120:123], v53 offset:8192\n\t" \
  "s_waitcnt lgkmcnt(5)\n\t" \
  "v_mfma_f32_16x16x32_bf16 v[248:251], v[92:95], v[80:83], v[248:251]\n\t" \
  "ds_read_b64_tr_b16 v[130:131], v54 offset:23040\n\t" \
  "v_mfma_f32_16x16x32_bf16 v[184:187], v[92:95], v[64:67], v[184:187]\n\t" \
  "ds_read_b64_tr_b16 v[132:133], v55 offset:22528\n\t" \
  "v_mfma_f32_16x16x32_bf16 v[192:195], v[92:95], v[68:71], v[192:195]\n\t" \
  "ds_read_b64_tr_b16 v[134:135], v55 offset:23040\n\t" \
  "v_mfma_f32_16x16x32_bf16 v[200:203], v[92:95], v[72:75], v[200:203]\n\t" \
  "v_mfma_f32_16x16x32_bf16 v[208:211], v[92:95], v[76:79], v[208:211]\n\t" \
  "v_mfma_f32_16x16x32_bf16 v[252:255], v[96:99], v[80:83], v[252:255]\n\t" \
  "v_mfma_f32_16x16x32_bf16 v[188:191], v[96:99], v[64:67], v[188:191]\n\t" \
  "v_mfma_f32_16x16x32_bf16 v[196:199], v[96:99], v[68:71], v[196:199]\n\t" \
  "v_mfma_f32_16x16x32_bf16 v[204:207], v[96:99], v[72:75], v[204:207]\n\t" \
  "v_mfma_f32_16x16x32_bf16 v[212:215], v[96:99], v[76:79], v[212:215]\n\t" \
  "v_mfma_f32_16x16x32_bf16 v[216:219], v[64:67], v[100:103], v[216:219]\n\t" \
  "v_mfma_f32_16x16x32_bf16 v[224:227], v[68:71], v[100:103], v[224:227]\n\t" \
  "v_mfma_f32_16x16x32_bf16 v[232:235], v[72:75], v[100:103], v[232:235]\n\t" \
  "s_waitcnt lgkmcnt(0)\n\t" \
  "s_barrier\n\t" \
  "v_mfma_f32_16x16x32_bf16 v[240:243], v[76:79], v[100:103], v[240:243]\n\t" \
  "ds_read_b64_tr_b16 v[92:93], v58 offset:26624\n\t" \
  "v_mfma_f32_16x16x32_bf16 v[220:223], v[64:67], v[104:107], v[220:223]\n\t" \
  "ds_read_b64_tr_b16 v[94:95], v58 offset:27136\n\t" \
  "v_mfma_f32_16x16x32_bf16 v[228:231], v[68:71], v[104:107], v[228:231]\n\t" \
  "ds_read_b128 v[80:83], v56 offset:0\n\t" \
  "v_mfma_f32_16x16x32_bf16 v[236:239], v[72:75], v[104:107], v[236:239]\n\t" \
  "ds_read_b64_tr_b16 v[96:97], v59 offset:26624\n\t" \
  "v_mfma_f32_16x16x32_bf16 v[244:247], v[76:79], v[104:107], v[244:247]\n\t" \
  "ds_read_b64_tr_b16 v[98:99], v59 offset:27136\n\t" \
  "v_mfma_f32_16x16x32_bf16 v[152:155], v[84:87], v[64:67], v[152:155]\n\t" \
  "ds_read_b64_tr_b16 v[100:101], v58 offset:34816\n\t" \
  "v_mfma_f32_16x16x32_bf16 v[160:163], v[84:87], v[68:71], v[160:163]\n\t" \
  "ds_read_b64_tr_b16 v[102:103], v58 offset:35328\n\t" \
  "v_mfma_f32_16x16x32_bf16 v[168:171], v[84:87], v[72:75], v[168:171]\n\t" \
  "ds_read_b64_tr_b16 v[104:105], v59 offset:34816\n\t" \
  "v_mfma_f32_16x16x32_bf16 v[176:179], v[84:87], v[76:79], v[176:179]\n\t" \
  "ds_read_b64_tr_b16 v[106:107], v59 offset:35328\n\t" \
  "v_mfma_f32_16x16x32_bf16 v[156:159], v[88:91], v[64:67], v[156:159]\n\t" \
  "ds_read_b128 v[64:67], v56 offset:2048\n\t" \
  "v_mfma_f32_16x16x32_bf16 v[164:167], v[88:91], v[68:71], v[164:167]\n\t" \
  "ds_read_b128 v[68:71], v56 offset:4096\n\t" \
  "v_mfma_f32_16x16x32_bf16 v[172:175], v[88:91], v[72:75], v[172:175]\n\t" \
  "ds_read_b128 v[72:75], v56 offset:6144\n\t" \
  "v_mfma_f32_16x16x32_bf16 v[180:183], v[88:91], v[76:79], v[180:183]\n\t" \
  "ds_read_b128 v[76:79], v56 offset:8192\n\t" \
  "v_mfma_f32_16x16x32_bf16 v[248:251], v[136:139], v[124:127], v[248:251]\n\t" \
  "ds_read_b64_tr_b16 v[84:85], v58 offset:18432\n\t" \
  "v_mfma_f32_16x16x32_bf16 v[184:187], v[136:139], v[108:111], v[184:187]\n\t" \
  "ds_read_b64_tr_b16 v[86:87], v58 offset:18944\n\t" \
  "v_mfma_f32_16x16x32_bf16 v[192:195], v[136:139], v[112:115], v[192:195]\n\t" \
  "ds_read_b64_tr_b16 v[88:89], v59 offset:18432\n\t" \
  "v_mfma_f32_16x16x32_bf16 v[200:203], v[136:139], v[116:119], v[200:203]\n\t" \
  "ds_read_b64_tr_b16 v[90:91], v59 offset:18944\n\t" \
  "v_mfma_f32_16x16x32_bf16 v[208:211], v[136:139], v[120:123], v[208:211]\n\t" \
  "ds_read_b64_tr_b16 v[136:137], v58 offset:30720\n\t" \
  "v_mfma_f32_16x16x32_bf16 v[252:255], v[140:143], v[124:127], v[252:255]\n\t" \
  "ds_read_b64_tr_b16 v[138:139], v58 offset:31232\n\t" \
  "v_mfma_f32_16x16x32_bf16 v[188:191], v[140:143], v[108:111], v[188:191]\n\t" \
  "ds_read_b128 v[124:127], v57 offset:0\n\t" \
  "v_mfma_f32_16x16x32_bf16 v[196:199], v[140:143], v[112:115], v[196:199]\n\t" \
  "v_mfma_f32_16x16x32_bf16 v[204:207], v[140:143], v[116:119], v[204:207]\n\t" \
  "v_mfma_f32_16x16x32_bf16 v[212:215], v[140:143], v[120:123], v[212:215]\n\t" \
  "ds_read_b64_tr_b16 v[140:141], v59 offset:30720\n\t" \
  "v_mfma_f32_16x16x32_bf16 v[216:219], v[108:111], v[144:147], v[216:219]\n\t" \
  "ds_read_b64_tr_b16 v[142:143], v59 offset:31232\n\t" \
  "v_mfma_f32_16x16x32_bf16 v[224:227], v[112:115], v[144:147], v[224:227]\n\t" \
  "v_mfma_f32_16x16x32_bf16 v[232:235], v[116:119], v[144:147], v[232:235]\n\t" \
  "v_mfma_f32_16x16x32_bf16 v[240:243], v[120:123], v[144:147], v[240:243]\n\t" \
  "ds_read_b64_tr_b16 v[144:145], v58 offset:38912\n\t" \
  "v_mfma_f32_16x16x32_bf16 v[220:223], v[108:111], v[148:151], v[220:223]\n\t" \
  "ds_read_b64_tr_b16 v[146:147], v58 offset:39424\n\t" \
  "v_mfma_f32_16x16x32_bf16 v[228:231], v[112:115], v[148:151], v[228:231]\n\t" \
  "v_mfma_f32_16x16x32_bf16 v[236:239], v[116:119], v[148:151], v[236:239]\n\t" \
  "v_mfma_f32_16x16x32_bf16 v[244:247], v[120:123], v[148:151], v[244:247]\n\t" \
  "ds_read_b64_tr_b16 v[148:149], v59 offset:38912\n\t" \
  "v_mfma_f32_16x16x32_bf16 v[152:155], v[128:131], v[108:111], v[152:155]\n\t" \
  "ds_read_b64_tr_b16 v[150:151], v59 offset:39424\n\t" \
  "v_mfma_f32_16x16x32_bf16 v[160:163], v[128:131], v[112:115], v[160:163]\n\t" \
  "v_mfma_f32_16x16x32_bf16 v[168:171], v[128:131], v[116:119], v[168:171]\n\t" \
  "v_mfma_f32_16x16x32_bf16 v[176:179], v[128:131], v[120:123], v[176:179]\n\t" \
  "ds_read_b64_tr_b16 v[128:129], v58 offset:22528\n\t" \
  "v_mfma_f32_16x16x32_bf16 v[156:159], v[132:135], v[108:111], v[156:159]\n\t" \
  "ds_read_b128 v[108:111], v57 offset:2048\n\t" \
  "v_mfma_f32_16x16x32_bf16 v[164:167], v[132:135], v[112:115], v[164:167]\n\t" \
  "ds_read_b128 v[112:115], v57 offset:4096\n\t" \
  "v_mfma_f32_16x16x32_bf16 v[172:175], v[132:135], v[116:119], v[172:175]\n\t" \
  "ds_read_b128 v[116:119], v57 offset:6144\n\t" \
  "v_mfma_f32_16x16x32_bf16 v[180:183], v[132:135], v[120:123], v[180:183]\n\t" \
  "ds_read_b128 v[120:123], v57 offset:8192\n\t" \
  "s_waitcnt lgkmcnt(5)\n\t" \
  "v_mfma_f32_16x16x32_bf16 v[248:251], v[92:95], v[80:83], v[248:251]\n\t" \
  "ds_read_b64_tr_b16 v[130:131], v58 offset:23040\n\t" \
  "v_mfma_f32_16x16x32_bf16 v[184:187], v[92:95], v[64:67], v[184:187]\n\t" \
  "ds_read_b64_tr_b16 v[132:133], v59 offset:22528\n\t" \
  "v_mfma_f32_16x16x32_bf16 v[192:195], v[92:95], v[68:71], v[192:195]\n\t" \
  "ds_read_b64_tr_b16 v[134:135], v59 offset:23040\n\t" \
  "v_mfma_f32_16x16x32_bf16 v[200:203], v[92:95], v[72:75], v[200:203]\n\t" \
  "v_mfma_f32_16x16x32_bf16 v[208:211], v[92:95], v[76:79], v[208:211]\n\t" \
  "v_mfma_f32_16x16x32_bf16 v[252:255], v[96:99], v[80:83], v[252:255]\n\t" \
  "v_mfma_f32_16x16x32_bf16 v[188:191], v[96:99], v[64:67], v[188:191]\n\t" \
  "v_mfma_f32_16x16x32_bf16 v[196:199], v[96:99], v[68:71], v[196:199]\n\t" \
  "v_mfma_f32_16x16x32_bf16 v[204:207], v[96:99], v[72:75], v[204:207]\n\t" \
  "v_mfma_f32_16x16x32_bf16 v[212:215], v[96:99], v[76:79], v[212:215]\n\t" \
  "v_mfma_f32_16x16x32_bf16 v[216:219], v[64:67], v[100:103], v[216:219]\n\t" \
  "v_mfma_f32_16x16x32_bf16 v[224:227], v[68:71], v[100:103], v[224:227]\n\t" \
  "v_mfma_f32_16x16x32_bf16 v[232:235], v[72:75], v[100:103], v[232:235]\n\t" \
  "s_waitcnt lgkmcnt(0)\n\t" \
  "s_barrier\n\t" \
  "v_mfma_f32_16x16x32_bf16 v[240:243], v[76:79], v[100:103], v[240:243]\n\t" \
  "ds_read_b64_tr_b16 v[92:93], v62 offset:26624\n\t" \
  "v_bfe_u32 v30, v4, 9, 1\n\t" \
  "v_mfma_f32_16x16x32_bf16 v[220:223], v[64:67], v[104:107], v[220:223]\n\t" \
  "ds_read_b64_tr_b16 v[94:95], v62 offset:27136\n\t" \
  "v_mad_i32_i24 v30, v30, -16, v0\n\t" \
  "v_mfma_f32_16x16x32_bf16 v[228:231], v[68:71], v[104:107], v[228:231]\n\t" \
  "ds_read_b128 v[80:83], v60 offset:0\n\t" \
  "ds_read_b128 v[26:29], v0 offset:1552\n\t" \
  "v_mfma_f32_16x16x32_bf16 v[236:239], v[72:75], v[104:107], v[236:239]\n\t" \
  "ds_read_b64_tr_b16 v[96:97], v63 offset:26624\n\t" \
  "ds_read_b128 v[30:33], v30 offset:1040\n\t" \
  "v_mfma_f32_16x16x32_bf16 v[244:247], v[76:79], v[104:107], v[244:247]\n\t" \
  "ds_read_b64_tr_b16 v[98:99], v63 offset:27136\n\t" \
  "ds_read_b128 v[34:37], v0 offset:512\n\t" \
  "v_mfma_f32_16x16x32_bf16 v[152:155], v[84:87], v[64:67], v[152:155]\n\t" \
  "ds_read_b64_tr_b16 v[100:101], v62 offset:34816\n\t" \
  "ds_read_b64_tr_b16 v[38:39], v1 offset:512\n\t" \
  "v_mfma_f32_16x16x32_bf16 v[160:163], v[84:87], v[68:71], v[160:163]\n\t" \
  "ds_read_b64_tr_b16 v[102:103], v62 offset:35328\n\t" \
  "ds_read_b64_tr_b16 v[40:41], v1 offset:0\n\t" \
  "v_mfma_f32_16x16x32_bf16 v[168:171], v[84:87], v[72:75], v[168:171]\n\t" \
  "503:\n\t" \
  "s_cbranch_execz 507f\n\t" \
  "ds_read_b64_tr_b16 v[104:105], v63 offset:34816\n\t" \
  "s_waitcnt vmcnt(0)\n\t" \
  "v_mfma_f32_16x16x32_bf16 v[176:179], v[84:87], v[76:79], v[176:179]\n\t" \
  "ds_read_b64_tr_b16 v[106:107], v63 offset:35328\n\t" \
  "v_perm_b32 v18, v10, v10, v14\n\t" \
  "v_mfma_f32_16x16x32_bf16 v[156:159], v[88:91], v[64:67], v[156:159]\n\t" \
  "ds_read_b128 v[64:67], v60 offset:2048\n\t" \
  "v_perm_b32 v19, v10, v10, v15\n\t" \
  "v_mfma_f32_16x16x32_bf16 v[164:167], v[88:91], v[68:71], v[164:167]\n\t" \
  "ds_read_b128 v[68:71], v60 offset:4096\n\t" \
  "v_perm_b32 v20, v10, v10, v16\n\t" \
  "v_mfma_f32_16x16x32_bf16 v[172:175], v[88:91], v[72:75], v[172:175]\n\t" \
  "ds_read_b128 v[72:75], v60 offset:6144\n\t" \
  "v_perm_b32 v21, v10, v10, v17\n\t" \
  "v_mfma_f32_16x16x32_bf16 v[180:183], v[88:91], v[76:79], v[180:183]\n\t" \
  "ds_read_b128 v[76:79], v60 offset:8192\n\t" \
  "v_perm_b32 v22, v11, v11, v14\n\t" \
  "v_mfma_f32_16x16x32_bf16 v[248:251], v[136:139], v[124:127], v[248:251]\n\t" \
  "ds_read_b64_tr_b16 v[84:85], v62 offset:18432\n\t" \
  "v_perm_b32 v23, v11, v11, v15\n\t" \
  "v_mfma_f32_16x16x32_bf16 v[184:187], v[136:139], v[108:111], v[184:187]\n\t" \
  "ds_read_b64_tr_b16 v[86:87], v62 offset:18944\n\t" \
  "v_perm_b32 v24, v11, v11, v16\n\t" \
  "v_mfma_f32_16x16x32_bf16 v[192:195], v[136:139], v[112:115], v[192:195]\n\t" \
  "ds_read_b64_tr_b16 v[88:89], v63 offset:18432\n\t" \
  "v_perm_b32 v25, v11, v11, v17\n\t" \
  "v_mfma_f32_16x16x32_bf16 v[200:203], v[136:139], v[116:119], v[200:203]\n\t" \
  "ds_read_b64_tr_b16 v[90:91], v63 offset:18944\n\t" \
  "v_mfma_f32_16x16x32_bf16 v[208:211], v[136:139], v[120:123], v[208:211]\n\t" \
  "ds_read_b64_tr_b16 v[136:137], v62 offset:30720\n\t" \
  "v_mfma_f32_16x16x32_bf16 v[252:255], v[140:143], v[124:127], v[252:255]\n\t" \
  "ds_read_b64_tr_b16 v[138:139], v62 offset:31232\n\t" \
  "s_waitcnt lgkmcnt(6)\n\t" \
  "v_mfma_f32_16x16x32_bf16 v[42:45], v[26:29], v[18:21], 0\n\t" \
  "v_mfma_f32_16x16x32_bf16 v[42:45], v[30:33], v[22:25], v[42:45]\n\t" \
  "v_mfma_f32_16x16x32_bf16 v[46:49], v[30:33], v[18:21], 0\n\t" \
  "v_mfma_f32_16x16x32_bf16 v[46:49], v[34:37], v[22:25], v[46:49]\n\t" \
  "ds_read_b128 v[26:29], v0 offset:1808\n\t" \
  "ds_read_b128 v[30:33], v0 offset:1296\n\t" \
  "v_mfma_f32_16x16x32_bf16 v[188:191], v[140:143], v[108:111], v[188:191]\n\t" \
  "ds_read_b128 v[124:127], v61 offset:0\n\t" \
  "ds_read_b128 v[34:37], v0 offset:768\n\t" \
  "v_mfma_f32_16x16x32_bf16 v[196:199], v[140:143], v[112:115], v[196:199]\n\t" \
  "v_mfma_f32_16x16x32_bf16 v[204:207], v[140:143], v[116:119], v[204:207]\n\t" \
  "v_mfma_f32_16x16x32_bf16 v[212:215], v[140:143], v[120:123], v[212:215]\n\t" \
  "ds_read_b64_tr_b16 v[140:141], v63 offset:30720\n\t" \
  "v_lshlrev_b32 v50, 16, v38\n\t" \
  "v_mfma_f32_16x16x32_bf16 v[216:219], v[108:111], v[144:147], v[216:219]\n\t" \
  "ds_read_b64_tr_b16 v[142:143], v63 offset:31232\n\t" \
  "v_lshlrev_b32 v51, 16, v40\n\t" \
  "v_mfma_f32_16x16x32_bf16 v[224:227], v[112:115], v[144:147], v[224:227]\n\t" \
  "v_mul_f32 v42, v42, v50\n\t" \
  "v_mul_f32 v46, v46, v51\n\t" \
  "v_mfma_f32_16x16x32_bf16 v[232:235], v[116:119], v[144:147], v[232:235]\n\t" \
  "v_cvt_pk_bf16_f32 v42, v42, v46\n\t" \
  "v_and_b32 v50, 0xffff0000, v38\n\t" \
  "v_mfma_f32_16x16x32_bf16 v[240:243], v[120:123], v[144:147], v[240:243]\n\t" \
  "ds_read_b64_tr_b16 v[144:145], v62 offset:38912\n\t" \
  "v_and_b32 v51, 0xffff0000, v40\n\t" \
  "v_mfma_f32_16x16x32_bf16 v[220:223], v[108:111], v[148:151], v[220:223]\n\t" \
  "ds_read_b64_tr_b16 v[146:147], v62 offset:39424\n\t" \
  "v_mul_f32 v43, v43, v50\n\t" \
  "v_mfma_f32_16x16x32_bf16 v[228:231], v[112:115], v[148:151], v[228:231]\n\t" \
  "v_mul_f32 v47, v47, v51\n\t" \
  "v_cvt_pk_bf16_f32 v43, v43, v47\n\t" \
  "v_mfma_f32_16x16x32_bf16 v[236:239], v[116:119], v[148:151], v[236:239]\n\t" \
  "v_lshlrev_b32 v50, 16, v39\n\t" \
  "v_lshlrev_b32 v51, 16, v41\n\t" \
  "v_mfma_f32_16x16x32_bf16 v[244:247], v[120:123], v[148:151], v[244:247]\n\t" \
  "ds_read_b64_tr_b16 v[148:149], v63 offset:38912\n\t" \
  "v_mul_f32 v44, v44, v50\n\t" \
  "v_mfma_f32_16x16x32_bf16 v[152:155], v[128:131], v[108:111], v[152:155]\n\t" \
  "ds_read_b64_tr_b16 v[150:151], v63 offset:39424\n\t" \
  "v_mul_f32 v48, v48, v51\n\t" \
  "v_mfma_f32_16x16x32_bf16 v[160:163], v[128:131], v[112:115], v[160:163]\n\t" \
  "v_cvt_pk_bf16_f32 v44, v44, v48\n\t" \
  "v_and_b32 v50, 0xffff0000, v39\n\t" \
  "v_mfma_f32_16x16x32_bf16 v[168:171], v[128:131], v[116:119], v[168:171]\n\t" \
  "v_and_b32 v51, 0xffff0000, v41\n\t" \
  "v_mul_f32 v45, v45, v50\n\t" \
  "v_mfma_f32_16x16x32_bf16 v[176:179], v[128:131], v[120:123], v[176:179]\n\t" \
  "ds_read_b64_tr_b16 v[128:129], v62 offset:22528\n\t" \
  "v_mul_f32 v49, v49, v51\n\t" \
  "v_mfma_f32_16x16x32_bf16 v[156:159], v[132:135], v[108:111], v[156:159]\n\t" \
  "ds_read_b128 v[108:111], v61 offset:2048\n\t" \
  "ds_read_b64_tr_b16 v[38:39], v1 offset:768\n\t" \
  "v_mfma_f32_16x16x32_bf16 v[164:167], v[132:135], v[112:115], v[164:167]\n\t" \
  "ds_read_b128 v[112:115], v61 offset:4096\n\t" \
  "ds_read_b64_tr_b16 v[40:41], v1 offset:256\n\t" \
  "v_mfma_f32_16x16x32_bf16 v[172:175], v[132:135], v[116:119], v[172:175]\n\t" \
  "ds_read_b128 v[116:119], v61 offset:6144\n\t" \
  "v_cvt_pk_bf16_f32 v45, v45, v49\n\t" \
  "v_mfma_f32_16x16x32_bf16 v[180:183], v[132:135], v[120:123], v[180:183]\n\t" \
  "ds_read_b128 v[120:123], v61 offset:8192\n\t" \
  "v_mfma_f32_16x16x32_bf16 v[248:251], v[92:95], v[80:83], v[248:251]\n\t" \
  "ds_read_b64_tr_b16 v[130:131], v62 offset:23040\n\t" \
  "v_mfma_f32_16x16x32_bf16 v[184:187], v[92:95], v[64:67], v[184:187]\n\t" \
  "ds_read_b64_tr_b16 v[132:133], v63 offset:22528\n\t" \
  "v_mfma_f32_16x16x32_bf16 v[192:195], v[92:95], v[68:71], v[192:195]\n\t" \
  "ds_read_b64_tr_b16 v[134:135], v63 offset:23040\n\t" \
  "v_mfma_f32_16x16x32_bf16 v[200:203], v[92:95], v[72:75], v[200:203]\n\t" \
  "v_mfma_f32_16x16x32_bf16 v[208:211], v[92:95], v[76:79], v[208:211]\n\t" \
  "v_mfma_f32_16x16x32_bf16 v[252:255], v[96:99], v[80:83], v[252:255]\n\t" \
  "v_mfma_f32_16x16x32_bf16 v[188:191], v[96:99], v[64:67], v[188:191]\n\t" \
  "v_mfma_f32_16x16x32_bf16 v[196:199], v[96:99], v[68:71], v[196:199]\n\t" \
  "v_mfma_f32_16x16x32_bf16 v[204:207], v[96:99], v[72:75], v[204:207]\n\t" \
  "v_mfma_f32_16x16x32_bf16 v[212:215], v[96:99], v[76:79], v[212:215]\n\t" \
  "global_store_dwordx4 v2, v[42:45], %24 offset:0 sc1\n\t" \
  "v_mfma_f32_16x16x32_bf16 v[216:219], v[64:67], v[100:103], v[216:219]\n\t" \
  "s_waitcnt lgkmcnt(0)\n\t" \
  "v_mfma_f32_16x16x32_bf16 v[42:45], v[26:29], v[18:21], 0\n\t" \
  "v_mfma_f32_16x16x32_bf16 v[42:45], v[30:33], v[22:25], v[42:45]\n\t" \
  "v_mfma_f32_16x16x32_bf16 v[46:49], v[30:33], v[18:21], 0\n\t" \
  "v_mfma_f32_16x16x32_bf16 v[46:49], v[34:37], v[22:25], v[46:49]\n\t" \
  "v_bfe_u32 v30, v4, 9, 1\n\t" \
  "v_mad_i32_i24 v30, v30, -16, v0\n\t" \
  "v_mfma_f32_16x16x32_bf16 v[224:227], v[68:71], v[100:103], v[224:227]\n\t" \
  "ds_read_b128 v[26:29], v0 offset:1584\n\t" \
  "ds_read_b128 v[30:33], v30 offset:1072\n\t" \
  "v_mfma_f32_16x16x32_bf16 v[232:235], v[72:75], v[100:103], v[232:235]\n\t" \
  "ds_read_b128 v[34:37], v0 offset:544\n\t" \
  "v_perm_b32 v18, v12, v12, v14\n\t" \
  "s_waitcnt lgkmcnt(0)\n\t" \
  "s_barrier\n\t" \
  "v_mfma_f32_16x16x32_bf16 v[240:243], v[76:79], v[100:103], v[240:243]\n\t" \
  "ds_read_b64_tr_b16 v[92:93], v54 offset:26624\n\t" \
  "v_perm_b32 v19, v12, v12, v15\n\t" \
  "v_mfma_f32_16x16x32_bf16 v[220:223], v[64:67], v[104:107], v[220:223]\n\t" \
  "ds_read_b64_tr_b16 v[94:95], v54 offset:27136\n\t" \
  "v_perm_b32 v20, v12, v12, v16\n\t" \
  "v_mfma_f32_16x16x32_bf16 v[228:231], v[68:71], v[104:107], v[228:231]\n\t" \
  "ds_read_b128 v[80:83], v52 offset:0\n\t" \
  "v_perm_b32 v21, v12, v12, v17\n\t" \
  "v_mfma_f32_16x16x32_bf16 v[236:239], v[72:75], v[104:107], v[236:239]\n\t" \
  "ds_read_b64_tr_b16 v[96:97], v55 offset:26624\n\t" \
  "v_perm_b32 v22, v13, v13, v14\n\t" \
  "v_mfma_f32_16x16x32_bf16 v[244:247], v[76:79], v[104:107], v[244:247]\n\t" \
  "ds_read_b64_tr_b16 v[98:99], v55 offset:27136\n\t" \
  "v_perm_b32 v23, v13, v13, v15\n\t" \
  "v_mfma_f32_16x16x32_bf16 v[152:155], v[84:87], v[64:67], v[152:155]\n\t" \
  "ds_read_b64_tr_b16 v[100:101], v54 offset:34816\n\t" \
  "v_perm_b32 v24, v13, v13, v16\n\t" \
  "v_mfma_f32_16x16x32_bf16 v[160:163], v[84:87], v[68:71], v[160:163]\n\t" \
  "ds_read_b64_tr_b16 v[102:103], v54 offset:35328\n\t" \
  "v_perm_b32 v25, v13, v13, v17\n\t" \
  "v_mfma_f32_16x16x32_bf16 v[168:171], v[84:87], v[72:75], v[168:171]\n\t" \
  "ds_read_b64_tr_b16 v[104:105], v55 offset:34816\n\t" \
  "v_lshlrev_b32 v50, 16, v38\n\t" \
  "v_mfma_f32_16x16x32_bf16 v[176:179], v[84:87], v[76:79], v[176:179]\n\t" \
  "ds_read_b64_tr_b16 v[106:107], v55 offset:35328\n\t" \
  "v_lshlrev_b32 v51, 16, v40\n\t" \
  "v_mfma_f32_16x16x32_bf16 v[156:159], v[88:91], v[64:67], v[156:159]\n\t" \
  "ds_read_b128 v[64:67], v52 offset:2048\n\t" \
  "v_mul_f32 v42, v42, v50\n\t" \
  "v_mfma_f32_16x16x32_bf16 v[164:167], v[88:91], v[68:71], v[164:167]\n\t" \
  "ds_read_b128 v[68:71], v52 offset:4096\n\t" \
  "v_mul_f32 v46, v46, v51\n\t" \
  "v_mfma_f32_16x16x32_bf16 v[172:175], v[88:91], v[72:75], v[172:175]\n\t" \
  "ds_read_b128 v[72:75], v52 offset:6144\n\t" \
  "v_cvt_pk_bf16_f32 v42, v42, v46\n\t" \
  "v_mfma_f32_16x16x32_bf16 v[180:183], v[88:91], v[76:79], v[180:183]\n\t" \
  "ds_read_b128 v[76:79], v52 offset:8192\n\t" \
  "v_and_b32 v50, 0xffff0000, v38\n\t" \
  "v_mfma_f32_16x16x32_bf16 v[248:251], v[136:139], v[124:127], v[248:251]\n\t" \
  "ds_read_b64_tr_b16 v[84:85], v54 offset:18432\n\t" \
  "v_and_b32 v51, 0xffff0000, v40\n\t" \
  "v_mfma_f32_16x16x32_bf16 v[184:187], v[136:139], v[108:111], v[184:187]\n\t" \
  "ds_read_b64_tr_b16 v[86:87], v54 offset:18944\n\t" \
  "v_mul_f32 v43, v43, v50\n\t" \
  "v_mfma_f32_16x16x32_bf16 v[192:195], v[136:139], v[112:115], v[192:195]\n\t" \
  "ds_read_b64_tr_b16 v[88:89], v55 offset:18432\n\t" \
  "v_mul_f32 v47, v47, v51\n\t" \
  "v_mfma_f32_16x16x32_bf16 v[200:203], v[136:139], v[116:119], v[200:203]\n\t" \
  "ds_read_b64_tr_b16 v[90:91], v55 offset:18944\n\t" \
  "v_cvt_pk_bf16_f32 v43, v43, v47\n\t" \
  "v_mfma_f32_16x16x32_bf16 v[208:211], v[136:139], v[120:123], v[208:211]\n\t" \
  "ds_read_b64_tr_b16 v[136:137], v54 offset:30720\n\t" \
  "v_lshlrev_b32 v50, 16, v39\n\t" \
  "v_mfma_f32_16x16x32_bf16 v[252:255], v[140:143], v[124:127], v[252:255]\n\t" \
  "ds_read_b64_tr_b16 v[138:139], v54 offset:31232\n\t" \
  "v_lshlrev_b32 v51, 16, v41\n\t" \
  "v_mfma_f32_16x16x32_bf16 v[188:191], v[140:143], v[108:111], v[188:191]\n\t" \
  "ds_read_b128 v[124:127], v53 offset:0\n\t" \
  "v_mul_f32 v44, v44, v50\n\t" \
  "v_mfma_f32_16x16x32_bf16 v[196:199], v[140:143], v[112:115], v[196:199]\n\t" \
  "v_mul_f32 v48, v48, v51\n\t" \
  "v_cvt_pk_bf16_f32 v44, v44, v48\n\t" \
  "v_mfma_f32_16x16x32_bf16 v[204:207], v[140:143], v[116:119], v[204:207]\n\t" \
  "v_and_b32 v50, 0xffff0000, v39\n\t" \
  "v_and_b32 v51, 0xffff0000, v41\n\t" \
  "v_mfma_f32_16x16x32_bf16 v[212:215], v[140:143], v[120:123], v[212:215]\n\t" \
  "ds_read_b64_tr_b16 v[140:141], v55 offset:30720\n\t" \
  "v_mul_f32 v45, v45, v50\n\t" \
  "v_mfma_f32_16x16x32_bf16 v[216:219], v[108:111], v[144:147], v[216:219]\n\t" \
  "ds_read_b64_tr_b16 v[142:143], v55 offset:31232\n\t" \
  "v_mul_f32 v49, v49, v51\n\t" \
  "v_mfma_f32_16x16x32_bf16 v[224:227], v[112:115], v[144:147], v[224:227]\n\t" \
  "ds_read_b64_tr_b16 v[38:39], v1 offset:544\n\t" \
  "ds_read_b64_tr_b16 v[40:41], v1 offset:32\n\t" \
  "v_mfma_f32_16x16x32_bf16 v[232:235], v[116:119], v[144:147], v[232:235]\n\t" \
  "v_cvt_pk_bf16_f32 v45, v45, v49\n\t" \
  "v_mfma_f32_16x16x32_bf16 v[240:243], v[120:123], v[144:147], v[240:243]\n\t" \
  "ds_read_b64_tr_b16 v[144:145], v54 offset:38912\n\t" \
  "v_mfma_f32_16x16x32_bf16 v[220:223], v[108:111], v[148:151], v[220:223]\n\t" \
  "ds_read_b64_tr_b16 v[146:147], v54 offset:39424\n\t" \
  "v_mfma_f32_16x16x32_bf16 v[228:231], v[112:115], v[148:151], v[228:231]\n\t" \
  "v_mfma_f32_16x16x32_bf16 v[236:239], v[116:119], v[148:151], v[236:239]\n\t" \
  "v_mfma_f32_16x16x32_bf16 v[244:247], v[120:123], v[148:151], v[244:247]\n\t" \
  "ds_read_b64_tr_b16 v[148:149], v55 offset:38912\n\t" \
  "v_mfma_f32_16x16x32_bf16 v[152:155], v[128:131], v[108:111], v[152:155]\n\t" \
  "ds_read_b64_tr_b16 v[150:151], v55 offset:39424\n\t" \
  "v_mfma_f32_16x16x32_bf16 v[160:163], v[128:131], v[112:115], v[160:163]\n\t" \
  "v_mfma_f32_16x16x32_bf16 v[168:171], v[128:131], v[116:119], v[168:171]\n\t" \
  "v_mfma_f32_16x16x32_bf16 v[176:179], v[128:131], v[120:123], v[176:179]\n\t" \
  "ds_read_b64_tr_b16 v[128:129], v54 offset:22528\n\t" \
  "v_mfma_f32_16x16x32_bf16 v[156:159], v[132:135], v[108:111], v[156:159]\n\t" \
  "ds_read_b128 v[108:111], v53 offset:2048\n\t" \
  "v_mfma_f32_16x16x32_bf16 v[164:167], v[132:135], v[112:115], v[164:167]\n\t" \
  "ds_read_b128 v[112:115], v53 offset:4096\n\t" \
  "v_mfma_f32_16x16x32_bf16 v[172:175], v[132:135], v[116:119], v[172:175]\n\t" \
  "ds_read_b128 v[116:119], v53 offset:6144\n\t" \
  "v_mfma_f32_16x16x32_bf16 v[180:183], v[132:135], v[120:123], v[180:183]\n\t" \
  "ds_read_b128 v[120:123], v53 offset:8192\n\t" \
  "s_waitcnt lgkmcnt(5)\n\t" \
  "v_mfma_f32_16x16x32_bf16 v[248:251], v[92:95], v[80:83], v[248:251]\n\t" \
  "ds_read_b64_tr_b16 v[130:131], v54 offset:23040\n\t" \
  "v_mfma_f32_16x16x32_bf16 v[184:187], v[92:95], v[64:67], v[184:187]\n\t" \
  "ds_read_b64_tr_b16 v[132:133], v55 offset:22528\n\t" \
  "v_mfma_f32_16x16x32_bf16 v[192:195], v[92:95], v[68:71], v[192:195]\n\t" \
  "ds_read_b64_tr_b16 v[134:135], v55 offset:23040\n\t" \
  "v_mfma_f32_16x16x32_bf16 v[200:203], v[92:95], v[72:75], v[200:203]\n\t" \
  "v_mfma_f32_16x16x32_bf16 v[208:211], v[92:95], v[76:79], v[208:211]\n\t" \
  "v_mfma_f32_16x16x32_bf16 v[252:255], v[96:99], v[80:83], v[252:255]\n\t" \
  "v_mfma_f32_16x16x32_bf16 v[188:191], v[96:99], v[64:67], v[188:191]\n\t" \
  "v_mfma_f32_16x16x32_bf16 v[196:199], v[96:99], v[68:71], v[196:199]\n\t" \
  "v_mfma_f32_16x16x32_bf16 v[204:207], v[96:99], v[72:75], v[204:207]\n\t" \
  "v_mfma_f32_16x16x32_bf16 v[212:215], v[96:99], v[76:79], v[212:215]\n\t" \
  "global_store_dwordx4 v2, v[42:45], %24 offset:1024 sc1\n\t" \
  "v_add_u32 v2, 0x10000, v2\n\t" \
  "v_mfma_f32_16x16x32_bf16 v[216:219], v[64:67], v[100:103], v[216:219]\n\t" \
  "v_mfma_f32_16x16x32_bf16 v[224:227], v[68:71], v[100:103], v[224:227]\n\t" \
  "v_mfma_f32_16x16x32_bf16 v[42:45], v[26:29], v[18:21], 0\n\t" \
  "v_mfma_f32_16x16x32_bf16 v[42:45], v[30:33], v[22:25], v[42:45]\n\t" \
  "v_mfma_f32_16x16x32_bf16 v[46:49], v[30:33], v[18:21], 0\n\t" \
  "v_mfma_f32_16x16x32_bf16 v[46:49], v[34:37], v[22:25], v[46:49]\n\t" \
  "ds_read_b128 v[26:29], v0 offset:1840\n\t" \
  "ds_read_b128 v[30:33], v0 offset:1328\n\t" \
  "v_mfma_f32_16x16x32_bf16 v[232:235], v[72:75], v[100:103], v[232:235]\n\t" \
  "ds_read_b128 v[34:37], v0 offset:800\n\t" \
  "s_waitcnt lgkmcnt(0)\n\t" \
  "s_barrier\n\t" \
  "v_mfma_f32_16x16x32_bf16 v[240:243], v[76:79], v[100:103], v[240:243]\n\t" \
  "ds_read_b64_tr_b16 v[92:93], v58 offset:26624\n\t" \
  "v_mfma_f32_16x16x32_bf16 v[220:223], v[64:67], v[104:107], v[220:223]\n\t" \
  "ds_read_b64_tr_b16 v[94:95], v58 offset:27136\n\t" \
  "v_lshlrev_b32 v50, 16, v38\n\t" \
  "v_mfma_f32_16x16x32_bf16 v[228:231], v[68:71], v[104:107], v[228:231]\n\t" \
  "ds_read_b128 v[80:83], v56 offset:0\n\t" \
  "v_lshlrev_b32 v51, 16, v40\n\t" \
  "v_mfma_f32_16x16x32_bf16 v[236:239], v[72:75], v[104:107], v[236:239]\n\t" \
  "ds_read_b64_tr_b16 v[96:97], v59 offset:26624\n\t" \
  "v_mul_f32 v42, v42, v50\n\t" \
  "v_mfma_f32_16x16x32_bf16 v[244:247], v[76:79], v[104:107], v[244:247]\n\t" \
  "ds_read_b64_tr_b16 v[98:99], v59 offset:27136\n\t" \
  "v_mul_f32 v46, v46, v51\n\t" \
  "v_mfma_f32_16x16x32_bf16 v[152:155], v[84:87], v[64:67], v[152:155]\n\t" \
  "ds_read_b64_tr_b16 v[100:101], v58 offset:34816\n\t" \
  "v_cvt_pk_bf16_f32 v42, v42, v46\n\t" \
  "v_mfma_f32_16x16x32_bf16 v[160:163], v[84:87], v[68:71], v[160:163]\n\t" \
  "ds_read_b64_tr_b16 v[102:103], v58 offset:35328\n\t" \
  "v_and_b32 v50, 0xffff0000, v38\n\t" \
  "v_mfma_f32_16x16x32_bf16 v[168:171], v[84:87], v[72:75], v[168:171]\n\t" \
  "ds_read_b64_tr_b16 v[104:105], v59 offset:34816\n\t" \
  "v_and_b32 v51, 0xffff0000, v40\n\t" \
  "v_mfma_f32_16x16x32_bf16 v[176:179], v[84:87], v[76:79], v[176:179]\n\t" \
  "ds_read_b64_tr_b16 v[106:107], v59 offset:35328\n\t" \
  "v_mul_f32 v43, v43, v50\n\t" \
  "v_mfma_f32_16x16x32_bf16 v[156:159], v[88:91], v[64:67], v[156:159]\n\t" \
  "ds_read_b128 v[64:67], v56 offset:2048\n\t" \
  "v_mul_f32 v47, v47, v51\n\t" \
  "v_mfma_f32_16x16x32_bf16 v[164:167], v[88:91], v[68:71], v[164:167]\n\t" \
  "ds_read_b128 v[68:71], v56 offset:4096\n\t" \
  "v_cvt_pk_bf16_f32 v43, v43, v47\n\t" \
  "v_mfma_f32_16x16x32_bf16 v[172:175], v[88:91], v[72:75], v[172:175]\n\t" \
  "ds_read_b128 v[72:75], v56 offset:6144\n\t" \
  "v_lshlrev_b32 v50, 16, v39\n\t" \
  "v_mfma_f32_16x16x32_bf16 v[180:183], v[88:91], v[76:79], v[180:183]\n\t" \
  "ds_read_b128 v[76:79], v56 offset:8192\n\t" \
  "v_lshlrev_b32 v51, 16, v41\n\t" \
  "v_mfma_f32_16x16x32_bf16 v[248:251], v[136:139], v[124:127], v[248:251]\n\t" \
  "ds_read_b64_tr_b16 v[84:85], v58 offset:18432\n\t" \
  "v_mul_f32 v44, v44, v50\n\t" \
  "v_mfma_f32_16x16x32_bf16 v[184:187], v[136:139], v[108:111], v[184:187]\n\t" \
  "ds_read_b64_tr_b16 v[86:87], v58 offset:18944\n\t" \
  "v_mul_f32 v48, v48, v51\n\t" \
  "v_mfma_f32_16x16x32_bf16 v[192:195], v[136:139], v[112:115], v[192:195]\n\t" \
  "ds_read_b64_tr_b16 v[88:89], v59 offset:18432\n\t" \
  "v_cvt_pk_bf16_f32 v44, v44, v48\n\t" \
  "v_mfma_f32_16x16x32_bf16 v[200:203], v[136:139], v[116:119], v[200:203]\n\t" \
  "ds_read_b64_tr_b16 v[90:91], v59 offset:18944\n\t" \
  "v_and_b32 v50, 0xffff0000, v39\n\t" \
  "v_mfma_f32_16x16x32_bf16 v[208:211], v[136:139], v[120:123], v[208:211]\n\t" \
  "ds_read_b64_tr_b16 v[136:137], v58 offset:30720\n\t" \
  "v_and_b32 v51, 0xffff0000, v41\n\t" \
  "v_mfma_f32_16x16x32_bf16 v[252:255], v[140:143], v[124:127], v[252:255]\n\t" \
  "ds_read_b64_tr_b16 v[138:139], v58 offset:31232\n\t" \
  "v_mul_f32 v45, v45, v50\n\t" \
  "v_mfma_f32_16x16x32_bf16 v[188:191], v[140:143], v[108:111], v[188:191]\n\t" \
  "ds_read_b128 v[124:127], v57 offset:0\n\t" \
  "v_mul_f32 v49, v49, v51\n\t" \
  "v_mfma_f32_16x16x32_bf16 v[196:199], v[140:143], v[112:115], v[196:199]\n\t" \
  "ds_read_b64_tr_b16 v[38:39], v1 offset:800\n\t" \
  "ds_read_b64_tr_b16 v[40:41], v1 offset:288\n\t" \
  "v_mfma_f32_16x16x32_bf16 v[204:207], v[140:143], v[116:119], v[204:207]\n\t" \
  "v_cvt_pk_bf16_f32 v45, v45, v49\n\t" \
  "v_mfma_f32_16x16x32_bf16 v[212:215], v[140:143], v[120:123], v[212:215]\n\t" \
  "ds_read_b64_tr_b16 v[140:141], v59 offset:30720\n\t" \
  "v_mfma_f32_16x16x32_bf16 v[216:219], v[108:111], v[144:147], v[216:219]\n\t" \
  "ds_read_b64_tr_b16 v[142:143], v59 offset:31232\n\t" \
  "v_mfma_f32_16x16x32_bf16 v[224:227], v[112:115], v[144:147], v[224:227]\n\t" \
  "v_mfma_f32_16x16x32_bf16 v[232:235], v[116:119], v[144:147], v[232:235]\n\t" \
  "v_mfma_f32_16x16x32_bf16 v[240:243], v[120:123], v[144:147], v[240:243]\n\t" \
  "ds_read_b64_tr_b16 v[144:145], v58 offset:38912\n\t" \
  "v_mfma_f32_16x16x32_bf16 v[220:223], v[108:111], v[148:151], v[220:223]\n\t" \
  "ds_read_b64_tr_b16 v[146:147], v58 offset:39424\n\t" \
  "v_mfma_f32_16x16x32_bf16 v[228:231], v[112:115], v[148:151], v[228:231]\n\t" \
  "v_mfma_f32_16x16x32_bf16 v[236:239], v[116:119], v[148:151], v[236:239]\n\t" \
  "v_mfma_f32_16x16x32_bf16 v[244:247], v[120:123], v[148:151], v[244:247]\n\t" \
  "ds_read_b64_tr_b16 v[148:149], v59 offset:38912\n\t" \
  "v_mfma_f32_16x16x32_bf16 v[152:155], v[128:131], v[108:111], v[152:155]\n\t" \
  "ds_read_b64_tr_b16 v[150:151], v59 offset:39424\n\t" \
  "v_mfma_f32_16x16x32_bf16 v[160:163], v[128:131], v[112:115], v[160:163]\n\t" \
  "v_mfma_f32_16x16x32_bf16 v[168:171], v[128:131], v[116:119], v[168:171]\n\t" \
  "v_mfma_f32_16x16x32_bf16 v[176:179], v[128:131], v[120:123], v[176:179]\n\t" \
  "ds_read_b64_tr_b16 v[128:129], v58 offset:22528\n\t" \
  "v_mfma_f32_16x16x32_bf16 v[156:159], v[132:135], v[108:111], v[156:159]\n\t" \
  "ds_read_b128 v[108:111], v57 offset:2048\n\t" \
  "v_mfma_f32_16x16x32_bf16 v[164:167], v[132:135], v[112:115], v[164:167]\n\t" \
  "ds_read_b128 v[112:115], v57 offset:4096\n\t" \
  "v_mfma_f32_16x16x32_bf16 v[172:175], v[132:135], v[116:119], v[172:175]\n\t" \
  "ds_read_b128 v[116:119], v57 offset:6144\n\t" \
  "v_mfma_f32_16x16x32_bf16 v[180:183], v[132:135], v[120:123], v[180:183]\n\t" \
  "ds_read_b128 v[120:123], v57 offset:8192\n\t" \
  "s_waitcnt lgkmcnt(5)\n\t" \
  "v_mfma_f32_16x16x32_bf16 v[248:251], v[92:95], v[80:83], v[248:251]\n\t" \
  "ds_read_b64_tr_b16 v[130:131], v58 offset:23040\n\t" \
  "v_mfma_f32_16x16x32_bf16 v[184:187], v[92:95], v[64:67], v[184:187]\n\t" \
  "ds_read_b64_tr_b16 v[132:133], v59 offset:22528\n\t" \
  "v_mfma_f32_16x16x32_bf16 v[192:195], v[92:95], v[68:71], v[192:195]\n\t" \
  "ds_read_b64_tr_b16 v[134:135], v59 offset:23040\n\t" \
  "v_mfma_f32_16x16x32_bf16 v[200:203], v[92:95], v[72:75], v[200:203]\n\t" \
  "v_mfma_f32_16x16x32_bf16 v[208:211], v[92:95], v[76:79], v[208:211]\n\t" \
  "v_mfma_f32_16x16x32_bf16 v[252:255], v[96:99], v[80:83], v[252:255]\n\t" \
  "v_mfma_f32_16x16x32_bf16 v[188:191], v[96:99], v[64:67], v[188:191]\n\t" \
  "v_mfma_f32_16x16x32_bf16 v[196:199], v[96:99], v[68:71], v[196:199]\n\t" \
  "v_mfma_f32_16x16x32_bf16 v[204:207], v[96:99], v[72:75], v[204:207]\n\t" \
  "v_mfma_f32_16x16x32_bf16 v[212:215], v[96:99], v[76:79], v[212:215]\n\t" \
  "global_store_dwordx4 v2, v[42:45], %24 offset:0 sc1\n\t" \
  "v_mfma_f32_16x16x32_bf16 v[216:219], v[64:67], v[100:103], v[216:219]\n\t" \
  "s_nop 0\n\t" \
  "v_mfma_f32_16x16x32_bf16 v[42:45], v[26:29], v[18:21], 0\n\t" \
  "v_mfma_f32_16x16x32_bf16 v[42:45], v[30:33], v[22:25], v[42:45]\n\t" \
  "v_mfma_f32_16x16x32_bf16 v[46:49], v[30:33], v[18:21], 0\n\t" \
  "v_mfma_f32_16x16x32_bf16 v[46:49], v[34:37], v[22:25], v[46:49]\n\t" \
  "v_mfma_f32_16x16x32_bf16 v[224:227], v[68:71], v[100:103], v[224:227]\n\t" \
  "v_mfma_f32_16x16x32_bf16 v[232:235], v[72:75], v[100:103], v[232:235]\n\t" \
  "v_lshlrev_b32 v50, 16, v38\n\t" \
  "v_lshlrev_b32 v51, 16, v40\n\t" \
  "s_waitcnt lgkmcnt(0)\n\t" \
  "s_barrier\n\t" \
  "v_mfma_f32_16x16x32_bf16 v[240:243], v[76:79], v[100:103], v[240:243]\n\t" \
  "ds_read_b64_tr_b16 v[92:93], v62 offset:26624\n\t" \
  "v_mul_f32 v42, v42, v50\n\t" \
  "v_mfma_f32_16x16x32_bf16 v[220:223], v[64:67], v[104:107], v[220:223]\n\t" \
  "ds_read_b64_tr_b16 v[94:95], v62 offset:27136\n\t" \
  "v_mul_f32 v46, v46, v51\n\t" \
  "v_mfma_f32_16x16x32_bf16 v[228:231], v[68:71], v[104:107], v[228:231]\n\t" \
  "ds_read_b128 v[80:83], v60 offset:0\n\t" \
  "v_cvt_pk_bf16_f32 v42, v42, v46\n\t" \
  "v_mfma_f32_16x16x32_bf16 v[236:239], v[72:75], v[104:107], v[236:239]\n\t" \
  "ds_read_b64_tr_b16 v[96:97], v63 offset:26624\n\t" \
  "v_and_b32 v50, 0xffff0000, v38\n\t" \
  "v_mfma_f32_16x16x32_bf16 v[244:247], v[76:79], v[104:107], v[244:247]\n\t" \
  "ds_read_b64_tr_b16 v[98:99], v63 offset:27136\n\t" \
  "v_and_b32 v51, 0xffff0000, v40\n\t" \
  "v_mfma_f32_16x16x32_bf16 v[152:155], v[84:87], v[64:67], v[152:155]\n\t" \
  "ds_read_b64_tr_b16 v[100:101], v62 offset:34816\n\t" \
  "v_mul_f32 v43, v43, v50\n\t" \
  "v_mfma_f32_16x16x32_bf16 v[160:163], v[84:87], v[68:71], v[160:163]\n\t" \
  "ds_read_b64_tr_b16 v[102:103], v62 offset:35328\n\t" \
  "v_mul_f32 v47, v47, v51\n\t" \
  "v_mfma_f32_16x16x32_bf16 v[168:171], v[84:87], v[72:75], v[168:171]\n\t" \
  "ds_read_b64_tr_b16 v[104:105], v63 offset:34816\n\t" \
  "v_cvt_pk_bf16_f32 v43, v43, v47\n\t" \
  "v_mfma_f32_16x16x32_bf16 v[176:179], v[84:87], v[76:79], v[176:179]\n\t" \
  "ds_read_b64_tr_b16 v[106:107], v63 offset:35328\n\t" \
  "v_lshlrev_b32 v50, 16, v39\n\t" \
  "v_mfma_f32_16x16x32_bf16 v[156:159], v[88:91], v[64:67], v[156:159]\n\t" \
  "ds_read_b128 v[64:67], v60 offset:2048\n\t" \
  "v_lshlrev_b32 v51, 16, v41\n\t" \
  "v_mfma_f32_16x16x32_bf16 v[164:167], v[88:91], v[68:71], v[164:167]\n\t" \
  "ds_read_b128 v[68:71], v60 offset:4096\n\t" \
  "v_mul_f32 v44, v44, v50\n\t" \
  "v_mfma_f32_16x16x32_bf16 v[172:175], v[88:91], v[72:75], v[172:175]\n\t" \
  "ds_read_b128 v[72:75], v60 offset:6144\n\t" \
  "v_mul_f32 v48, v48, v51\n\t" \
  "v_mfma_f32_16x16x32_bf16 v[180:183], v[88:91], v[76:79], v[180:183]\n\t" \
  "ds_read_b128 v[76:79], v60 offset:8192\n\t" \
  "v_cvt_pk_bf16_f32 v44, v44, v48\n\t" \
  "v_mfma_f32_16x16x32_bf16 v[248:251], v[136:139], v[124:127], v[248:251]\n\t" \
  "ds_read_b64_tr_b16 v[84:85], v62 offset:18432\n\t" \
  "v_and_b32 v50, 0xffff0000, v39\n\t" \
  "v_mfma_f32_16x16x32_bf16 v[184:187], v[136:139], v[108:111], v[184:187]\n\t" \
  "ds_read_b64_tr_b16 v[86:87], v62 offset:18944\n\t" \
  "v_and_b32 v51, 0xffff0000, v41\n\t" \
  "v_mfma_f32_16x16x32_bf16 v[192:195], v[136:139], v[112:115], v[192:195]\n\t" \
  "ds_read_b64_tr_b16 v[88:89], v63 offset:18432\n\t" \
  "v_mul_f32 v45, v45, v50\n\t" \
  "v_mfma_f32_16x16x32_bf16 v[200:203], v[136:139], v[116:119], v[200:203]\n\t" \
  "ds_read_b64_tr_b16 v[90:91], v63 offset:18944\n\t" \
  "v_mul_f32 v49, v49, v51\n\t" \
  "v_mfma_f32_16x16x32_bf16 v[208:211], v[136:139], v[120:123], v[208:211]\n\t" \
  "ds_read_b64_tr_b16 v[136:137], v62 offset:30720\n\t" \
  "v_cvt_pk_bf16_f32 v45, v45, v49\n\t" \
  "v_mfma_f32_16x16x32_bf16 v[252:255], v[140:143], v[124:127], v[252:255]\n\t" \
  "ds_read_b64_tr_b16 v[138:139], v62 offset:31232\n\t" \
  "v_mfma_f32_16x16x32_bf16 v[188:191], v[140:143], v[108:111], v[188:191]\n\t" \
  "ds_read_b128 v[124:127], v61 offset:0\n\t" \
  "v_mfma_f32_16x16x32_bf16 v[196:199], v[140:143], v[112:115], v[196:199]\n\t" \
  "v_mfma_f32_16x16x32_bf16 v[204:207], v[140:143], v[116:119], v[204:207]\n\t" \
  "v_mfma_f32_16x16x32_bf16 v[212:215], v[140:143], v[120:123], v[212:215]\n\t" \
  "ds_read_b64_tr_b16 v[140:141], v63 offset:30720\n\t" \
  "v_mfma_f32_16x16x32_bf16 v[216:219], v[108:111], v[144:147], v[216:219]\n\t" \
  "ds_read_b64_tr_b16 v[142:143], v63 offset:31232\n\t" \
  "v_mfma_f32_16x16x32_bf16 v[224:227], v[112:115], v[144:147], v[224:227]\n\t" \
  "v_mfma_f32_16x16x32_bf16 v[232:235], v[116:119], v[144:147], v[232:235]\n\t" \
  "v_mfma_f32_16x16x32_bf16 v[240:243], v[120:123], v[144:147], v[240:243]\n\t" \
  "ds_read_b64_tr_b16 v[144:145], v62 offset:38912\n\t" \
  "v_mfma_f32_16x16x32_bf16 v[220:223], v[108:111], v[148:151], v[220:223]\n\t" \
  "ds_read_b64_tr_b16 v[146:147], v62 offset:39424\n\t" \
  "v_mfma_f32_16x16x32_bf16 v[228:231], v[112:115], v[148:151], v[228:231]\n\t" \
  "v_mfma_f32_16x16x32_bf16 v[236:239], v[116:119], v[148:151], v[236:239]\n\t" \
  "v_mfma_f32_16x16x32_bf16 v[244:247], v[120:123], v[148:151], v[244:247]\n\t" \
  "ds_read_b64_tr_b16 v[148:149], v63 offset:38912\n\t" \
  "v_mfma_f32_16x16x32_bf16 v[152:155], v[128:131], v[108:111], v[152:155]\n\t" \
  "ds_read_b64_tr_b16 v[150:151], v63 offset:39424\n\t" \
  "v_mfma_f32_16x16x32_bf16 v[160:163], v[128:131], v[112:115], v[160:163]\n\t" \
  "v_mfma_f32_16x16x32_bf16 v[168:171], v[128:131], v[116:119], v[168:171]\n\t" \
  "v_mfma_f32_16x16x32_bf16 v[176:179], v[128:131], v[120:123], v[176:179]\n\t" \
  "ds_read_b64_tr_b16 v[128:129], v62 offset:22528\n\t" \
  "v_mfma_f32_16x16x32_bf16 v[156:159], v[132:135], v[108:111], v[156:159]\n\t" \
  "ds_read_b128 v[108:111], v61 offset:2048\n\t" \
  "v_mfma_f32_16x16x32_bf16 v[164:167], v[132:135], v[112:115], v[164:167]\n\t" \
  "ds_read_b128 v[112:115], v61 offset:4096\n\t" \
  "v_mfma_f32_16x16x32_bf16 v[172:175], v[132:135], v[116:119], v[172:175]\n\t" \
  "ds_read_b128 v[116:119], v61 offset:6144\n\t" \
  "v_mfma_f32_16x16x32_bf16 v[180:183], v[132:135], v[120:123], v[180:183]\n\t" \
  "ds_read_b128 v[120:123], v61 offset:8192\n\t" \
  "s_waitcnt lgkmcnt(5)\n\t" \
  "v_mfma_f32_16x16x32_bf16 v[248:251], v[92:95], v[80:83], v[248:251]\n\t" \
  "ds_read_b64_tr_b16 v[130:131], v62 offset:23040\n\t" \
  "v_mfma_f32_16x16x32_bf16 v[184:187], v[92:95], v[64:67], v[184:187]\n\t" \
  "ds_read_b64_tr_b16 v[132:133], v63 offset:22528\n\t" \
  "v_mfma_f32_16x16x32_bf16 v[192:195], v[92:95], v[68:71], v[192:195]\n\t" \
  "ds_read_b64_tr_b16 v[134:135], v63 offset:23040\n\t" \
  "v_mfma_f32_16x16x32_bf16 v[200:203], v[92:95], v[72:75], v[200:203]\n\t" \
  "v_mfma_f32_16x16x32_bf16 v[208:211], v[92:95], v[76:79], v[208:211]\n\t" \
  "v_mfma_f32_16x16x32_bf16 v[252:255], v[96:99], v[80:83], v[252:255]\n\t" \
  "v_mfma_f32_16x16x32_bf16 v[188:191], v[96:99], v[64:67], v[188:191]\n\t" \
  "v_mfma_f32_16x16x32_bf16 v[196:199], v[96:99], v[68:71], v[196:199]\n\t" \
  "v_mfma_f32_16x16x32_bf16 v[204:207], v[96:99], v[72:75], v[204:207]\n\t" \
  "v_mfma_f32_16x16x32_bf16 v[212:215], v[96:99], v[76:79], v[212:215]\n\t" \
  "global_store_dwordx4 v2, v[42:45], %24 offset:1024 sc1\n\t" \
  "v_mfma_f32_16x16x32_bf16 v[216:219], v[64:67], v[100:103], v[216:219]\n\t" \
  "v_mfma_f32_16x16x32_bf16 v[224:227], v[68:71], v[100:103], v[224:227]\n\t" \
  "v_mfma_f32_16x16x32_bf16 v[232:235], v[72:75], v[100:103], v[232:235]\n\t" \
  "v_mov_b32 v30, 0xff61b1e6\n\t" \
  "v_bfe_i32 v34, v3, 0, 1\n\t" \
  "s_waitcnt lgkmcnt(0)\n\t" \
  "s_barrier\n\t" \
  "v_mfma_f32_16x16x32_bf16 v[240:243], v[76:79], v[100:103], v[240:243]\n\t" \
  "ds_read_b64_tr_b16 v[92:93], v54 offset:26624\n\t" \
  "v_and_b32 v34, v30, v34\n\t" \
  "v_mfma_f32_16x16x32_bf16 v[220:223], v[64:67], v[104:107], v[220:223]\n\t" \
  "ds_read_b64_tr_b16 v[94:95], v54 offset:27136\n\t" \
  "v_bfe_i32 v38, v3, 4, 1\n\t" \
  "v_mfma_f32_16x16x32_bf16 v[228:231], v[68:71], v[104:107], v[228:231]\n\t" \
  "ds_read_b128 v[80:83], v52 offset:0\n\t" \
  "v_and_b32 v38, v30, v38\n\t" \
  "v_mfma_f32_16x16x32_bf16 v[236:239], v[72:75], v[104:107], v[236:239]\n\t" \
  "ds_read_b64_tr_b16 v[96:97], v55 offset:26624\n\t" \
  "v_bfe_i32 v42, v3, 8, 1\n\t" \
  "v_mfma_f32_16x16x32_bf16 v[244:247], v[76:79], v[104:107], v[244:247]\n\t" \
  "ds_read_b64_tr_b16 v[98:99], v55 offset:27136\n\t" \
  "v_bfi_b32 v42, v42, v30, %15\n\t" \
  "v_mfma_f32_16x16x32_bf16 v[152:155], v[84:87], v[64:67], v[152:155]\n\t" \
  "ds_read_b64_tr_b16 v[100:101], v54 offset:34816\n\t" \
  "v_mov_b32 v48, 0x3f803f80\n\t" \
  "v_mfma_f32_16x16x32_bf16 v[160:163], v[84:87], v[68:71], v[160:163]\n\t" \
  "ds_read_b64_tr_b16 v[102:103], v54 offset:35328\n\t" \
  "v_bfe_i32 v35, v3, 1, 1\n\t" \
  "v_mfma_f32_16x16x32_bf16 v[168:171], v[84:87], v[72:75], v[168:171]\n\t" \
  "ds_read_b64_tr_b16 v[104:105], v55 offset:34816\n\t" \
  "v_and_b32 v35, v30, v35\n\t" \
  "v_mfma_f32_16x16x32_bf16 v[176:179], v[84:87], v[76:79], v[176:179]\n\t" \
  "ds_read_b64_tr_b16 v[106:107], v55 offset:35328\n\t" \
  "v_bfe_i32 v39, v3, 5, 1\n\t" \
  "v_mfma_f32_16x16x32_bf16 v[156:159], v[88:91], v[64:67], v[156:159]\n\t" \
  "ds_read_b128 v[64:67], v52 offset:2048\n\t" \
  "v_and_b32 v39, v30, v39\n\t" \
  "v_mfma_f32_16x16x32_bf16 v[164:167], v[88:91], v[68:71], v[164:167]\n\t" \
  "ds_read_b128 v[68:71], v52 offset:4096\n\t" \
  "v_bfe_i32 v43, v3, 9, 1\n\t" \
  "v_mfma_f32_16x16x32_bf16 v[172:175], v[88:91], v[72:75], v[172:175]\n\t" \
  "ds_read_b128 v[72:75], v52 offset:6144\n\t" \
  "v_bfi_b32 v43, v43, v30, %15\n\t" \
  "v_mfma_f32_16x16x32_bf16 v[180:183], v[88:91], v[76:79], v[180:183]\n\t" \
  "ds_read_b128 v[76:79], v52 offset:8192\n\t" \
  "v_mov_b32 v49, 0x3f803f80\n\t" \
  "v_mfma_f32_16x16x32_bf16 v[248:251], v[136:139], v[124:127], v[248:251]\n\t" \
  "ds_read_b64_tr_b16 v[84:85], v54 offset:18432\n\t" \
  "v_bfe_i32 v36, v3, 2, 1\n\t" \
  "v_mfma_f32_16x16x32_bf16 v[184:187], v[136:139], v[108:111], v[184:187]\n\t" \
  "ds_read_b64_tr_b16 v[86:87], v54 offset:18944\n\t" \
  "v_and_b32 v36, v30, v36\n\t" \
  "v_mfma_f32_16x16x32_bf16 v[192:195], v[136:139], v[112:115], v[192:195]\n\t" \
  "ds_read_b64_tr_b16 v[88:89], v55 offset:18432\n\t" \
  "v_bfe_i32 v40, v3, 6, 1\n\t" \
  "v_mfma_f32_16x16x32_bf16 v[200:203], v[136:139], v[116:119], v[200:203]\n\t" \
  "ds_read_b64_tr_b16 v[90:91], v55 offset:18944\n\t" \
  "v_and_b32 v40, v30, v40\n\t" \
  "v_mfma_f32_16x16x32_bf16 v[208:211], v[136:139], v[120:123], v[208:211]\n\t" \
  "ds_read_b64_tr_b16 v[136:137], v54 offset:30720\n\t" \
  "v_bfe_i32 v44, v3, 10, 1\n\t" \
  "v_mfma_f32_16x16x32_bf16 v[252:255], v[140:143], v[124:127], v[252:255]\n\t" \
  "ds_read_b64_tr_b16 v[138:139], v54 offset:31232\n\t" \
  "v_bfi_b32 v44, v44, v30, %15\n\t" \
  "v_mfma_f32_16x16x32_bf16 v[188:191], v[140:143], v[108:111], v[188:191]\n\t" \
  "ds_read_b128 v[124:127], v53 offset:0\n\t" \
  "v_mov_b32 v50, 0x3f803f80\n\t" \
  "v_mfma_f32_16x16x32_bf16 v[196:199], v[140:143], v[112:115], v[196:199]\n\t" \
  "v_bfe_i32 v37, v3, 3, 1\n\t" \
  "v_and_b32 v37, v30, v37\n\t" \
  "v_mfma_f32_16x16x32_bf16 v[204:207], v[140:143], v[116:119], v[204:207]\n\t" \
  "v_bfe_i32 v41, v3, 7, 1\n\t" \
  "v_and_b32 v41, v30, v41\n\t" \
  "v_mfma_f32_16x16x32_bf16 v[212:215], v[140:143], v[120:123], v[212:215]\n\t" \
  "504:\n\t" \
  "s_cbranch_execz 508f\n\t" \
  "ds_read_b64_tr_b16 v[140:141], v55 offset:30720\n\t" \
  "v_bfe_i32 v45, v3, 11, 1\n\t" \
  "v_mfma_f32_16x16x32_bf16 v[216:219], v[108:111], v[144:147], v[216:219]\n\t" \
  "ds_read_b64_tr_b16 v[142:143], v55 offset:31232\n\t" \
  "v_bfi_b32 v45, v45, v30, %15\n\t" \
  "v_mfma_f32_16x16x32_bf16 v[224:227], v[112:115], v[144:147], v[224:227]\n\t" \
  "v_mov_b32 v51, 0x3f803f80\n\t" \
  "v_bfe_i32 v46, v3, 12, 1\n\t" \
  "v_mfma_f32_16x16x32_bf16 v[232:235], v[116:119], v[144:147], v[232:235]\n\t" \
  "v_bfe_i32 v31, v3, 13, 1\n\t" \
  "v_perm_b32 v46, v31, v46, vcc_hi\n\t" \
  "v_mfma_f32_16x16x32_bf16 v[240:243], v[120:123], v[144:147], v[240:243]\n\t" \
  "ds_read_b64_tr_b16 v[144:145], v54 offset:38912\n\t" \
  "v_bfe_i32 v47, v3, 14, 1\n\t" \
  "v_mfma_f32_16x16x32_bf16 v[220:223], v[108:111], v[148:151], v[220:223]\n\t" \
  "ds_read_b64_tr_b16 v[146:147], v54 offset:39424\n\t" \
  "v_bfe_i32 v31, v3, 15, 1\n\t" \
  "v_mfma_f32_16x16x32_bf16 v[228:231], v[112:115], v[148:151], v[228:231]\n\t" \
  "v_perm_b32 v47, v31, v47, vcc_hi\n\t" \
  "v_add_u32 v30, 18432, v4\n\t" \
  "v_mfma_f32_16x16x32_bf16 v[236:239], v[116:119], v[148:151], v[236:239]\n\t" \
  "v_xor_b32 v31, 1024, v30\n\t" \
  "v_mov_b32 v32, %12\n\t" \
  "v_mfma_f32_16x16x32_bf16 v[244:247], v[120:123], v[148:151], v[244:247]\n\t" \
  "ds_read_b64_tr_b16 v[148:149], v55 offset:38912\n\t" \
  "v_xor_b32 v33, 4, v32\n\t" \
  "v_mfma_f32_16x16x32_bf16 v[152:155], v[128:131], v[108:111], v[152:155]\n\t" \
  "ds_read_b64_tr_b16 v[150:151], v55 offset:39424\n\t" \
  "v_mov_b32 v0, 1\n\t" \
  "v_mfma_f32_16x16x32_bf16 v[160:163], v[128:131], v[112:115], v[160:163]\n\t" \
  "v_and_b32 v4, 1023, v4\n\t" \
  "v_add_u32 v1, 8192, v4\n\t" \
  "v_mfma_f32_16x16x32_bf16 v[168:171], v[128:131], v[116:119], v[168:171]\n\t" \
  "v_mfma_f32_16x16x32_bf16 v[176:179], v[128:131], v[120:123], v[176:179]\n\t" \
  "ds_read_b64_tr_b16 v[128:129], v54 offset:22528\n\t" \
  "v_mfma_f32_16x16x32_bf16 v[156:159], v[132:135], v[108:111], v[156:159]\n\t" \
  "ds_read_b128 v[108:111], v53 offset:2048\n\t" \
  "v_mfma_f32_16x16x32_bf16 v[164:167], v[132:135], v[112:115], v[164:167]\n\t" \
  "ds_read_b128 v[112:115], v53 offset:4096\n\t" \
  "v_mfma_f32_16x16x32_bf16 v[172:175], v[132:135], v[116:119], v[172:175]\n\t" \
  "ds_read_b128 v[116:119], v53 offset:6144\n\t" \
  "v_mfma_f32_16x16x32_bf16 v[180:183], v[132:135], v[120:123], v[180:183]\n\t" \
  "ds_read_b128 v[120:123], v53 offset:8192\n\t" \
  "s_waitcnt lgkmcnt(5)\n\t" \
  "v_mfma_f32_16x16x32_bf16 v[248:251], v[92:95], v[80:83], v[248:251]\n\t" \
  "ds_read_b64_tr_b16 v[130:131], v54 offset:23040\n\t" \
  "v_mfma_f32_16x16x32_bf16 v[184:187], v[92:95], v[64:67], v[184:187]\n\t" \
  "ds_read_b64_tr_b16 v[132:133], v55 offset:22528\n\t" \
  "v_mfma_f32_16x16x32_bf16 v[192:195], v[92:95], v[68:71], v[192:195]\n\t" \
  "ds_read_b64_tr_b16 v[134:135], v55 offset:23040\n\t" \
  "v_mfma_f32_16x16x32_bf16 v[200:203], v[92:95], v[72:75], v[200:203]\n\t" \
  "v_mfma_f32_16x16x32_bf16 v[208:211], v[92:95], v[76:79], v[208:211]\n\t" \
  "v_mfma_f32_16x16x32_bf16 v[252:255], v[96:99], v[80:83], v[252:255]\n\t" \
  "v_mfma_f32_16x16x32_bf16 v[188:191], v[96:99], v[64:67], v[188:191]\n\t" \
  "v_mfma_f32_16x16x32_bf16 v[196:199], v[96:99], v[68:71], v[196:199]\n\t" \
  "v_mfma_f32_16x16x32_bf16 v[204:207], v[96:99], v[72:75], v[204:207]\n\t" \
  "v_mfma_f32_16x16x32_bf16 v[212:215], v[96:99], v[76:79], v[212:215]\n\t" \
  "v_mfma_f32_16x16x32_bf16 v[216:219], v[64:67], v[100:103], v[216:219]\n\t" \
  "v_mfma_f32_16x16x32_bf16 v[224:227], v[68:71], v[100:103], v[224:227]\n\t" \
  "v_mfma_f32_16x16x32_bf16 v[232:235], v[72:75], v[100:103], v[232:235]\n\t" \
  "s_waitcnt lgkmcnt(0)\n\t" \
  "s_barrier\n\t" \
  "v_mfma_f32_16x16x32_bf16 v[240:243], v[76:79], v[100:103], v[240:243]\n\t" \
  "ds_read_b64_tr_b16 v[92:93], v58 offset:26624\n\t" \
  "v_mfma_f32_16x16x32_bf16 v[220:223], v[64:67], v[104:107], v[220:223]\n\t" \
  "ds_read_b64_tr_b16 v[94:95], v58 offset:27136\n\t" \
  "v_mfma_f32_16x16x32_bf16 v[228:231], v[68:71], v[104:107], v[228:231]\n\t" \
  "ds_read_b128 v[80:83], v56 offset:0\n\t" \
  "v_mfma_f32_16x16x32_bf16 v[236:239], v[72:75], v[104:107], v[236:239]\n\t" \
  "ds_read_b64_tr_b16 v[96:97], v59 offset:26624\n\t" \
  "v_mfma_f32_16x16x32_bf16 v[244:247], v[76:79], v[104:107], v[244:247]\n\t" \
  "ds_read_b64_tr_b16 v[98:99], v59 offset:27136\n\t" \
  "v_mfma_f32_16x16x32_bf16 v[152:155], v[84:87], v[64:67], v[152:155]\n\t" \
  "ds_read_b64_tr_b16 v[100:101], v58 offset:34816\n\t" \
  "v_mfma_f32_16x16x32_bf16 v[160:163], v[84:87], v[68:71], v[160:163]\n\t" \
  "ds_read_b64_tr_b16 v[102:103], v58 offset:35328\n\t" \
  "v_mfma_f32_16x16x32_bf16 v[168:171], v[84:87], v[72:75], v[168:171]\n\t" \
  "ds_read_b64_tr_b16 v[104:105], v59 offset:34816\n\t" \
  "v_mfma_f32_16x16x32_bf16 v[176:179], v[84:87], v[76:79], v[176:179]\n\t" \
  "ds_read_b64_tr_b16 v[106:107], v59 offset:35328\n\t" \
  "v_mfma_f32_16x16x32_bf16 v[156:159], v[88:91], v[64:67], v[156:159]\n\t" \
  "ds_read_b128 v[64:67], v56 offset:2048\n\t" \
  "v_mfma_f32_16x16x32_bf16 v[164:167], v[88:91], v[68:71], v[164:167]\n\t" \
  "ds_read_b128 v[68:71], v56 offset:4096\n\t" \
  "v_mfma_f32_16x16x32_bf16 v[172:175], v[88:91], v[72:75], v[172:175]\n\t" \
  "ds_read_b128 v[72:75], v56 offset:6144\n\t" \
  "v_mfma_f32_16x16x32_bf16 v[180:183], v[88:91], v[76:79], v[180:183]\n\t" \
  "ds_read_b128 v[76:79], v56 offset:8192\n\t" \
  "v_mfma_f32_16x16x32_bf16 v[248:251], v[136:139], v[124:127], v[248:251]\n\t" \
  "ds_read_b64_tr_b16 v[84:85], v58 offset:18432\n\t" \
  "v_mfma_f32_16x16x32_bf16 v[184:187], v[136:139], v[108:111], v[184:187]\n\t" \
  "ds_read_b64_tr_b16 v[86:87], v58 offset:18944\n\t" \
  "v_mfma_f32_16x16x32_bf16 v[192:195], v[136:139], v[112:115], v[192:195]\n\t" \
  "ds_read_b64_tr_b16 v[88:89], v59 offset:18432\n\t" \
  "v_mfma_f32_16x16x32_bf16 v[200:203], v[136:139], v[116:119], v[200:203]\n\t" \
  "ds_read_b64_tr_b16 v[90:91], v59 offset:18944\n\t" \
  "v_mfma_f32_16x16x32_bf16 v[208:211], v[136:139], v[120:123], v[208:211]\n\t" \
  "ds_read_b64_tr_b16 v[136:137], v58 offset:30720\n\t" \
  "v_mfma_f32_16x16x32_bf16 v[252:255], v[140:143], v[124:127], v[252:255]\n\t" \
  "ds_read_b64_tr_b16 v[138:139], v58 offset:31232\n\t" \
  "v_mfma_f32_16x16x32_bf16 v[188:191], v[140:143], v[108:111], v[188:191]\n\t" \
  "ds_read_b128 v[124:127], v57 offset:0\n\t" \
  "v_mfma_f32_16x16x32_bf16 v[196:199], v[140:143], v[112:115], v[196:199]\n\t" \
  "v_mfma_f32_16x16x32_bf16 v[204:207], v[140:143], v[116:119], v[204:207]\n\t" \
  "v_mfma_f32_16x16x32_bf16 v[212:215], v[140:143], v[120:123], v[212:215]\n\t" \
  "ds_read_b64_tr_b16 v[140:141], v59 offset:30720\n\t" \
  "v_mfma_f32_16x16x32_bf16 v[216:219], v[108:111], v[144:147], v[216:219]\n\t" \
  "ds_read_b64_tr_b16 v[142:143], v59 offset:31232\n\t" \
  "v_mfma_f32_16x16x32_bf16 v[224:227], v[112:115], v[144:147], v[224:227]\n\t" \
  "v_mfma_f32_16x16x32_bf16 v[232:235], v[116:119], v[144:147], v[232:235]\n\t" \
  "v_mfma_f32_16x16x32_bf16 v[240:243], v[120:123], v[144:147], v[240:243]\n\t" \
  "ds_read_b64_tr_b16 v[144:145], v58 offset:38912\n\t" \
  "v_mfma_f32_16x16x32_bf16 v[220:223], v[108:111], v[148:151], v[220:223]\n\t" \
  "ds_read_b64_tr_b16 v[146:147], v58 offset:39424\n\t" \
  "v_mfma_f32_16x16x32_bf16 v[228:231], v[112:115], v[148:151], v[228:231]\n\t" \
  "v_mfma_f32_16x16x32_bf16 v[236:239], v[116:119], v[148:151], v[236:239]\n\t" \
  "v_mfma_f32_16x16x32_bf16 v[244:247], v[120:123], v[148:151], v[244:247]\n\t" \
  "ds_read_b64_tr_b16 v[148:149], v59 offset:38912\n\t" \
  "v_mfma_f32_16x16x32_bf16 v[152:155], v[128:131], v[108:111], v[152:155]\n\t" \
  "ds_read_b64_tr_b16 v[150:151], v59 offset:39424\n\t" \
  "v_mfma_f32_16x16x32_bf16 v[160:163], v[128:131], v[112:115], v[160:163]\n\t" \
  "v_mfma_f32_16x16x32_bf16 v[168:171], v[128:131], v[116:119], v[168:171]\n\t" \
  "v_mfma_f32_16x16x32_bf16 v[176:179], v[128:131], v[120:123], v[176:179]\n\t" \
  "ds_read_b64_tr_b16 v[128:129], v58 offset:22528\n\t" \
  "v_mfma_f32_16x16x32_bf16 v[156:159], v[132:135], v[108:111], v[156:159]\n\t" \
  "ds_read_b128 v[108:111], v57 offset:2048\n\t" \
  "v_mfma_f32_16x16x32_bf16 v[164:167], v[132:135], v[112:115], v[164:167]\n\t" \
  "ds_read_b128 v[112:115], v57 offset:4096\n\t" \
  "v_mfma_f32_16x16x32_bf16 v[172:175], v[132:135], v[116:119], v[172:175]\n\t" \
  "ds_read_b128 v[116:119], v57 offset:6144\n\t" \
  "v_mfma_f32_16x16x32_bf16 v[180:183], v[132:135], v[120:123], v[180:183]\n\t" \
  "ds_read_b128 v[120:123], v57 offset:8192\n\t" \
  "s_waitcnt lgkmcnt(5)\n\t" \
  "v_mfma_f32_16x16x32_bf16 v[248:251], v[92:95], v[80:83], v[248:251]\n\t" \
  "ds_read_b64_tr_b16 v[130:131], v58 offset:23040\n\t" \
  "v_mfma_f32_16x16x32_bf16 v[184:187], v[92:95], v[64:67], v[184:187]\n\t" \
  "ds_read_b64_tr_b16 v[132:133], v59 offset:22528\n\t" \
  "v_mfma_f32_16x16x32_bf16 v[192:195], v[92:95], v[68:71], v[192:195]\n\t" \
  "ds_read_b64_tr_b16 v[134:135], v59 offset:23040\n\t" \
  "v_mfma_f32_16x16x32_bf16 v[200:203], v[92:95], v[72:75], v[200:203]\n\t" \
  "v_mfma_f32_16x16x32_bf16 v[208:211], v[92:95], v[76:79], v[208:211]\n\t" \
  "v_mfma_f32_16x16x32_bf16 v[252:255], v[96:99], v[80:83], v[252:255]\n\t" \
  "v_mfma_f32_16x16x32_bf16 v[188:191], v[96:99], v[64:67], v[188:191]\n\t" \
  "v_mfma_f32_16x16x32_bf16 v[196:199], v[96:99], v[68:71], v[196:199]\n\t" \
  "v_mfma_f32_16x16x32_bf16 v[204:207], v[96:99], v[72:75], v[204:207]\n\t" \
  "v_mfma_f32_16x16x32_bf16 v[212:215], v[96:99], v[76:79], v[212:215]\n\t" \
  "v_mfma_f32_16x16x32_bf16 v[216:219], v[64:67], v[100:103], v[216:219]\n\t" \
  "v_mfma_f32_16x16x32_bf16 v[224:227], v[68:71], v[100:103], v[224:227]\n\t" \
  "v_mfma_f32_16x16x32_bf16 v[232:235], v[72:75], v[100:103], v[232:235]\n\t" \
  "s_waitcnt lgkmcnt(0)\n\t" \
  "s_waitcnt vmcnt(0)\n\t" \
  "s_barrier\n\t" \
  "v_mfma_f32_16x16x32_bf16 v[240:243], v[76:79], v[100:103], v[240:243]\n\t" \
  "ds_read_b64_tr_b16 v[92:93], v62 offset:26624\n\t" \
  "v_mfma_f32_16x16x32_bf16 v[220:223], v[64:67], v[104:107], v[220:223]\n\t" \
  "ds_read_b64_tr_b16 v[94:95], v62 offset:27136\n\t" \
  "v_mfma_f32_16x16x32_bf16 v[228:231], v[68:71], v[104:107], v[228:231]\n\t" \
  "ds_read_b128 v[80:83], v60 offset:0\n\t" \
  "v_mfma_f32_16x16x32_bf16 v[236:239], v[72:75], v[104:107], v[236:239]\n\t" \
  "ds_read_b64_tr_b16 v[96:97], v63 offset:26624\n\t" \
  "v_mfma_f32_16x16x32_bf16 v[244:247], v[76:79], v[104:107], v[244:247]\n\t" \
  "ds_read_b64_tr_b16 v[98:99], v63 offset:27136\n\t" \
  "v_mfma_f32_16x16x32_bf16 v[152:155], v[84:87], v[64:67], v[152:155]\n\t" \
  "ds_read_b64_tr_b16 v[100:101], v62 offset:34816\n\t" \
  "v_mfma_f32_16x16x32_bf16 v[160:163], v[84:87], v[68:71], v[160:163]\n\t" \
  "ds_read_b64_tr_b16 v[102:103], v62 offset:35328\n\t" \
  "v_mfma_f32_16x16x32_bf16 v[168:171], v[84:87], v[72:75], v[168:171]\n\t" \
  "ds_read_b64_tr_b16 v[104:105], v63 offset:34816\n\t" \
  "v_mfma_f32_16x16x32_bf16 v[176:179], v[84:87], v[76:79], v[176:179]\n\t" \
  "ds_read_b64_tr_b16 v[106:107], v63 offset:35328\n\t" \
  "v_mfma_f32_16x16x32_bf16 v[156:159], v[88:91], v[64:67], v[156:159]\n\t" \
  "ds_read_b128 v[64:67], v60 offset:2048\n\t" \
  "v_mfma_f32_16x16x32_bf16 v[164:167], v[88:91], v[68:71], v[164:167]\n\t" \
  "ds_read_b128 v[68:71], v60 offset:4096\n\t" \
  "v_mfma_f32_16x16x32_bf16 v[172:175], v[88:91], v[72:75], v[172:175]\n\t" \
  "ds_read_b128 v[72:75], v60 offset:6144\n\t" \
  "v_mfma_f32_16x16x32_bf16 v[180:183], v[88:91], v[76:79], v[180:183]\n\t" \
  "ds_read_b128 v[76:79], v60 offset:8192\n\t" \
  "v_mfma_f32_16x16x32_bf16 v[248:251], v[136:139], v[124:127], v[248:251]\n\t" \
  "ds_read_b64_tr_b16 v[84:85], v62 offset:18432\n\t" \
  "v_mfma_f32_16x16x32_bf16 v[184:187], v[136:139], v[108:111], v[184:187]\n\t" \
  "ds_read_b64_tr_b16 v[86:87], v62 offset:18944\n\t" \
  "v_mfma_f32_16x16x32_bf16 v[192:195], v[136:139], v[112:115], v[192:195]\n\t" \
  "ds_read_b64_tr_b16 v[88:89], v63 offset:18432\n\t" \
  "v_mfma_f32_16x16x32_bf16 v[200:203], v[136:139], v[116:119], v[200:203]\n\t" \
  "ds_read_b64_tr_b16 v[90:91], v63 offset:18944\n\t" \
  "v_mfma_f32_16x16x32_bf16 v[208:211], v[136:139], v[120:123], v[208:211]\n\t" \
  "ds_read_b64_tr_b16 v[136:137], v62 offset:30720\n\t" \
  "v_mfma_f32_16x16x32_bf16 v[252:255], v[140:143], v[124:127], v[252:255]\n\t" \
  "ds_read_b64_tr_b16 v[138:139], v62 offset:31232\n\t" \
  "v_mfma_f32_16x16x32_bf16 v[188:191], v[140:143], v[108:111], v[188:191]\n\t" \
  "ds_read_b128 v[124:127], v61 offset:0\n\t" \
  "v_mfma_f32_16x16x32_bf16 v[196:199], v[140:143], v[112:115], v[196:199]\n\t" \
  "v_mfma_f32_16x16x32_bf16 v[204:207], v[140:143], v[116:119], v[204:207]\n\t" \
  "v_mfma_f32_16x16x32_bf16 v[212:215], v[140:143], v[120:123], v[212:215]\n\t" \
  "ds_read_b64_tr_b16 v[140:141], v63 offset:30720\n\t" \
  "v_mfma_f32_16x16x32_bf16 v[216:219], v[108:111], v[144:147], v[216:219]\n\t" \
  "ds_read_b64_tr_b16 v[142:143], v63 offset:31232\n\t" \
  "v_mfma_f32_16x16x32_bf16 v[224:227], v[112:115], v[144:147], v[224:227]\n\t" \
  "v_mfma_f32_16x16x32_bf16 v[232:235], v[116:119], v[144:147], v[232:235]\n\t" \
  "v_mfma_f32_16x16x32_bf16 v[240:243], v[120:123], v[144:147], v[240:243]\n\t" \
  "ds_read_b64_tr_b16 v[144:145], v62 offset:38912\n\t" \
  "v_mfma_f32_16x16x32_bf16 v[220:223], v[108:111], v[148:151], v[220:223]\n\t" \
  "ds_read_b64_tr_b16 v[146:147], v62 offset:39424\n\t" \
  "v_mfma_f32_16x16x32_bf16 v[228:231], v[112:115], v[148:151], v[228:231]\n\t" \
  "v_mfma_f32_16x16x32_bf16 v[236:239], v[116:119], v[148:151], v[236:239]\n\t" \
  "v_mfma_f32_16x16x32_bf16 v[244:247], v[120:123], v[148:151], v[244:247]\n\t" \
  "ds_read_b64_tr_b16 v[148:149], v63 offset:38912\n\t" \
  "v_mfma_f32_16x16x32_bf16 v[152:155], v[128:131], v[108:111], v[152:155]\n\t" \
  "ds_read_b64_tr_b16 v[150:151], v63 offset:39424\n\t" \
  "v_mfma_f32_16x16x32_bf16 v[160:163], v[128:131], v[112:115], v[160:163]\n\t" \
  "v_mfma_f32_16x16x32_bf16 v[168:171], v[128:131], v[116:119], v[168:171]\n\t" \
  "v_mfma_f32_16x16x32_bf16 v[176:179], v[128:131], v[120:123], v[176:179]\n\t" \
  "ds_read_b64_tr_b16 v[128:129], v62 offset:22528\n\t" \
  "v_mfma_f32_16x16x32_bf16 v[156:159], v[132:135], v[108:111], v[156:159]\n\t" \
  "ds_read_b128 v[108:111], v61 offset:2048\n\t" \
  "v_mfma_f32_16x16x32_bf16 v[164:167], v[132:135], v[112:115], v[164:167]\n\t" \
  "ds_read_b128 v[112:115], v61 offset:4096\n\t" \
  "v_mfma_f32_16x16x32_bf16 v[172:175], v[132:135], v[116:119], v[172:175]\n\t" \
  "ds_read_b128 v[116:119], v61 offset:6144\n\t" \
  "v_mfma_f32_16x16x32_bf16 v[180:183], v[132:135], v[120:123], v[180:183]\n\t" \
  "ds_read_b32 v10, v32 offset:104\n\t" \
  "ds_read_b128 v[120:123], v61 offset:8192\n\t" \
  "s_waitcnt lgkmcnt(6)\n\t" \
  "v_mfma_f32_16x16x32_bf16 v[248:251], v[92:95], v[80:83], v[248:251]\n\t" \
  "v_lshlrev_b32 v11, 3, v4\n\t" \
  "ds_read_b64_tr_b16 v[130:131], v62 offset:23040\n\t" \
  "v_mfma_f32_16x16x32_bf16 v[184:187], v[92:95], v[64:67], v[184:187]\n\t" \
  "ds_read_b64_tr_b16 v[132:133], v63 offset:22528\n\t" \
  "v_mfma_f32_16x16x32_bf16 v[192:195], v[92:95], v[68:71], v[192:195]\n\t" \
  "ds_read_b64_tr_b16 v[134:135], v63 offset:23040\n\t" \
  "v_mfma_f32_16x16x32_bf16 v[200:203], v[92:95], v[72:75], v[200:203]\n\t" \
  "v_mfma_f32_16x16x32_bf16 v[208:211], v[92:95], v[76:79], v[208:211]\n\t" \
  "v_mfma_f32_16x16x32_bf16 v[252:255], v[96:99], v[80:83], v[252:255]\n\t" \
  "v_mfma_f32_16x16x32_bf16 v[188:191], v[96:99], v[64:67], v[188:191]\n\t" \
  "v_mfma_f32_16x16x32_bf16 v[196:199], v[96:99], v[68:71], v[196:199]\n\t" \
  "v_mfma_f32_16x16x32_bf16 v[204:207], v[96:99], v[72:75], v[204:207]\n\t" \
  "v_mfma_f32_16x16x32_bf16 v[212:215], v[96:99], v[76:79], v[212:215]\n\t" \
  "s_waitcnt lgkmcnt(4)\n\t" \
  "s_cmp_lg_u32 %13, 0\n\t" \
  "s_cbranch_scc1 27f\n\t" \
  "s_mov_b32 exec_hi, 0\n\t" \
  "global_store_dword v11, v10, %25 sc1\n\t" \
  "s_mov_b32 exec_hi, -1\n\t" \
  "27:\n\t" \
  "v_mfma_f32_16x16x32_bf16 v[216:219], v[64:67], v[100:103], v[216:219]\n\t" \
  "v_mfma_f32_16x16x32_bf16 v[224:227], v[68:71], v[100:103], v[224:227]\n\t" \
  "v_mfma_f32_16x16x32_bf16 v[232:235], v[72:75], v[100:103], v[232:235]\n\t" \
  "s_waitcnt lgkmcnt(0)\n\t" \
  "s_barrier\n\t" \
  "v_mfma_f32_16x16x32_bf16 v[240:243], v[76:79], v[100:103], v[240:243]\n\t" \
  "ds_read_b64_tr_b16 v[92:93], v54 offset:26624\n\t" \
  "v_mfma_f32_16x16x32_bf16 v[220:223], v[64:67], v[104:107], v[220:223]\n\t" \
  "ds_read_b64_tr_b16 v[94:95], v54 offset:27136\n\t" \
  "v_mfma_f32_16x16x32_bf16 v[228:231], v[68:71], v[104:107], v[228:231]\n\t" \
  "ds_read_b128 v[80:83], v52 offset:0\n\t" \
  "v_mfma_f32_16x16x32_bf16 v[236:239], v[72:75], v[104:107], v[236:239]\n\t" \
  "ds_read_b64_tr_b16 v[96:97], v55 offset:26624\n\t" \
  "v_mfma_f32_16x16x32_bf16 v[244:247], v[76:79], v[104:107], v[244:247]\n\t" \
  "ds_read_b64_tr_b16 v[98:99], v55 offset:27136\n\t" \
  "v_mfma_f32_16x16x32_bf16 v[152:155], v[84:87], v[64:67], v[152:155]\n\t" \
  "ds_read_b64_tr_b16 v[100:101], v54 offset:34816\n\t" \
  "v_mfma_f32_16x16x32_bf16 v[160:163], v[84:87], v[68:71], v[160:163]\n\t" \
  "ds_read_b64_tr_b16 v[102:103], v54 offset:35328\n\t" \
  "v_mfma_f32_16x16x32_bf16 v[168:171], v[84:87], v[72:75], v[168:171]\n\t" \
  "ds_read_b64_tr_b16 v[104:105], v55 offset:34816\n\t" \
  "v_mfma_f32_16x16x32_bf16 v[176:179], v[84:87], v[76:79], v[176:179]\n\t" \
  "ds_read_b64_tr_b16 v[106:107], v55 offset:35328\n\t" \
  "v_mfma_f32_16x16x32_bf16 v[156:159], v[88:91], v[64:67], v[156:159]\n\t" \
  "ds_read_b128 v[64:67], v52 offset:2048\n\t" \
  "v_mfma_f32_16x16x32_bf16 v[164:167], v[88:91], v[68:71], v[164:167]\n\t" \
  "ds_read_b128 v[68:71], v52 offset:4096\n\t" \
  "v_mfma_f32_16x16x32_bf16 v[172:175], v[88:91], v[72:75], v[172:175]\n\t" \
  "ds_read_b128 v[72:75], v52 offset:6144\n\t" \
  "v_mfma_f32_16x16x32_bf16 v[180:183], v[88:91], v[76:79], v[180:183]\n\t" \
  "ds_read_b128 v[76:79], v52 offset:8192\n\t" \
  "v_mfma_f32_16x16x32_bf16 v[248:251], v[136:139], v[124:127], v[248:251]\n\t" \
  "ds_read_b64_tr_b16 v[84:85], v54 offset:18432\n\t" \
  "v_mfma_f32_16x16x32_bf16 v[184:187], v[136:139], v[108:111], v[184:187]\n\t" \
  "ds_read_b64_tr_b16 v[86:87], v54 offset:18944\n\t" \
  "v_mfma_f32_16x16x32_bf16 v[192:195], v[136:139], v[112:115], v[192:195]\n\t" \
  "ds_read_b64_tr_b16 v[88:89], v55 offset:18432\n\t" \
  "v_mfma_f32_16x16x32_bf16 v[200:203], v[136:139], v[116:119], v[200:203]\n\t" \
  "ds_read_b64_tr_b16 v[90:91], v55 offset:18944\n\t" \
  "v_mfma_f32_16x16x32_bf16 v[208:211], v[136:139], v[120:123], v[208:211]\n\t" \
  "ds_read_b64_tr_b16 v[136:137], v54 offset:30720\n\t" \
  "v_mfma_f32_16x16x32_bf16 v[252:255], v[140:143], v[124:127], v[252:255]\n\t" \
  "ds_read_b64_tr_b16 v[138:139], v54 offset:31232\n\t" \
  "v_mfma_f32_16x16x32_bf16 v[188:191], v[140:143], v[108:111], v[188:191]\n\t" \
  "ds_read_b128 v[124:127], v53 offset:0\n\t" \
  "v_mfma_f32_16x16x32_bf16 v[196:199], v[140:143], v[112:115], v[196:199]\n\t" \
  "v_mfma_f32_16x16x32_bf16 v[204:207], v[140:143], v[116:119], v[204:207]\n\t" \
  "v_mfma_f32_16x16x32_bf16 v[212:215], v[140:143], v[120:123], v[212:215]\n\t" \
  "ds_read_b64_tr_b16 v[140:141], v55 offset:30720\n\t" \
  "v_mfma_f32_16x16x32_bf16 v[216:219], v[108:111], v[144:147], v[216:219]\n\t" \
  "ds_read_b64_tr_b16 v[142:143], v55 offset:31232\n\t" \
  "v_mfma_f32_16x16x32_bf16 v[224:227], v[112:115], v[144:147], v[224:227]\n\t" \
  "v_mfma_f32_16x16x32_bf16 v[232:235], v[116:119], v[144:147], v[232:235]\n\t" \
  "v_mfma_f32_16x16x32_bf16 v[240:243], v[120:123], v[144:147], v[240:243]\n\t" \
  "ds_read_b64_tr_b16 v[144:145], v54 offset:38912\n\t" \
  "v_mfma_f32_16x16x32_bf16 v[220:223], v[108:111], v[148:151], v[220:223]\n\t" \
  "ds_read_b64_tr_b16 v[146:147], v54 offset:39424\n\t" \
  "v_mfma_f32_16x16x32_bf16 v[228:231], v[112:115], v[148:151], v[228:231]\n\t" \
  "v_mfma_f32_16x16x32_bf16 v[236:239], v[116:119], v[148:151], v[236:239]\n\t" \
  "v_mfma_f32_16x16x32_bf16 v[244:247], v[120:123], v[148:151], v[244:247]\n\t" \
  "ds_read_b64_tr_b16 v[148:149], v55 offset:38912\n\t" \
  "v_mfma_f32_16x16x32_bf16 v[152:155], v[128:131], v[108:111], v[152:155]\n\t" \
  "ds_read_b64_tr_b16 v[150:151], v55 offset:39424\n\t" \
  "v_mfma_f32_16x16x32_bf16 v[160:163], v[128:131], v[112:115], v[160:163]\n\t" \
  "v_mfma_f32_16x16x32_bf16 v[168:171], v[128:131], v[116:119], v[168:171]\n\t" \
  "v_mfma_f32_16x16x32_bf16 v[176:179], v[128:131], v[120:123], v[176:179]\n\t" \
  "ds_read_b64_tr_b16 v[128:129], v54 offset:22528\n\t" \
  "v_mfma_f32_16x16x32_bf16 v[156:159], v[132:135], v[108:111], v[156:159]\n\t" \
  "ds_read_b128 v[108:111], v53 offset:2048\n\t" \
  "v_mfma_f32_16x16x32_bf16 v[164:167], v[132:135], v[112:115], v[164:167]\n\t" \
  "ds_read_b128 v[112:115], v53 offset:4096\n\t" \
  "v_mfma_f32_16x16x32_bf16 v[172:175], v[132:135], v[116:119], v[172:175]\n\t" \
  "ds_read_b128 v[116:119], v53 offset:6144\n\t" \
  "v_mfma_f32_16x16x32_bf16 v[180:183], v[132:135], v[120:123], v[180:183]\n\t" \
  "ds_read_b128 v[120:123], v53 offset:8192\n\t" \
  "s_waitcnt lgkmcnt(5)\n\t" \
  "v_mfma_f32_16x16x32_bf16 v[248:251], v[92:95], v[80:83], v[248:251]\n\t" \
  "ds_read_b64_tr_b16 v[130:131], v54 offset:23040\n\t" \
  "v_mfma_f32_16x16x32_bf16 v[184:187], v[92:95], v[64:67], v[184:187]\n\t" \
  "ds_read_b64_tr_b16 v[132:133], v55 offset:22528\n\t" \
  "v_mfma_f32_16x16x32_bf16 v[192:195], v[92:95], v[68:71], v[192:195]\n\t" \
  "ds_read_b64_tr_b16 v[134:135], v55 offset:23040\n\t" \
  "v_mfma_f32_16x16x32_bf16 v[200:203], v[92:95], v[72:75], v[200:203]\n\t" \
  "v_mfma_f32_16x16x32_bf16 v[208:211], v[92:95], v[76:79], v[208:211]\n\t" \
  "v_mfma_f32_16x16x32_bf16 v[252:255], v[96:99], v[80:83], v[252:255]\n\t" \
  "v_mfma_f32_16x16x32_bf16 v[188:191], v[96:99], v[64:67], v[188:191]\n\t" \
  "v_mfma_f32_16x16x32_bf16 v[196:199], v[96:99], v[68:71], v[196:199]\n\t" \
  "v_mfma_f32_16x16x32_bf16 v[204:207], v[96:99], v[72:75], v[204:207]\n\t" \
  "v_mfma_f32_16x16x32_bf16 v[212:215], v[96:99], v[76:79], v[212:215]\n\t" \
  "v_mfma_f32_16x16x32_bf16 v[216:219], v[64:67], v[100:103], v[216:219]\n\t" \
  "v_mfma_f32_16x16x32_bf16 v[224:227], v[68:71], v[100:103], v[224:227]\n\t" \
  "v_mfma_f32_16x16x32_bf16 v[232:235], v[72:75], v[100:103], v[232:235]\n\t" \
  "s_waitcnt lgkmcnt(0)\n\t" \
  "s_barrier\n\t" \
  "v_mfma_f32_16x16x32_bf16 v[240:243], v[76:79], v[100:103], v[240:243]\n\t" \
  "ds_read_b64_tr_b16 v[92:93], v58 offset:26624\n\t" \
  "v_mfma_f32_16x16x32_bf16 v[220:223], v[64:67], v[104:107], v[220:223]\n\t" \
  "ds_read_b64_tr_b16 v[94:95], v58 offset:27136\n\t" \
  "v_mfma_f32_16x16x32_bf16 v[228:231], v[68:71], v[104:107], v[228:231]\n\t" \
  "ds_read_b128 v[80:83], v56 offset:0\n\t" \
  "v_mfma_f32_16x16x32_bf16 v[236:239], v[72:75], v[104:107], v[236:239]\n\t" \
  "ds_read_b64_tr_b16 v[96:97], v59 offset:26624\n\t" \
  "v_mfma_f32_16x16x32_bf16 v[244:247], v[76:79], v[104:107], v[244:247]\n\t" \
  "ds_read_b64_tr_b16 v[98:99], v59 offset:27136\n\t" \
  "v_mfma_f32_16x16x32_bf16 v[152:155], v[84:87], v[64:67], v[152:155]\n\t" \
  "ds_read_b64_tr_b16 v[100:101], v58 offset:34816\n\t" \
  "v_mfma_f32_16x16x32_bf16 v[160:163], v[84:87], v[68:71], v[160:163]\n\t" \
  "ds_read_b64_tr_b16 v[102:103], v58 offset:35328\n\t" \
  "v_mfma_f32_16x16x32_bf16 v[168:171], v[84:87], v[72:75], v[168:171]\n\t" \
  "ds_read_b64_tr_b16 v[104:105], v59 offset:34816\n\t" \
  "v_mfma_f32_16x16x32_bf16 v[176:179], v[84:87], v[76:79], v[176:179]\n\t" \
  "ds_read_b64_tr_b16 v[106:107], v59 offset:35328\n\t" \
  "v_mfma_f32_16x16x32_bf16 v[156:159], v[88:91], v[64:67], v[156:159]\n\t" \
  "ds_read_b128 v[64:67], v56 offset:2048\n\t" \
  "v_mfma_f32_16x16x32_bf16 v[164:167], v[88:91], v[68:71], v[164:167]\n\t" \
  "ds_read_b128 v[68:71], v56 offset:4096\n\t" \
  "v_mfma_f32_16x16x32_bf16 v[172:175], v[88:91], v[72:75], v[172:175]\n\t" \
  "ds_read_b128 v[72:75], v56 offset:6144\n\t" \
  "v_mfma_f32_16x16x32_bf16 v[180:183], v[88:91], v[76:79], v[180:183]\n\t" \
  "ds_read_b128 v[76:79], v56 offset:8192\n\t" \
  "v_mfma_f32_16x16x32_bf16 v[248:251], v[136:139], v[124:127], v[248:251]\n\t" \
  "ds_read_b64_tr_b16 v[84:85], v58 offset:18432\n\t" \
  "v_mfma_f32_16x16x32_bf16 v[184:187], v[136:139], v[108:111], v[184:187]\n\t" \
  "ds_read_b64_tr_b16 v[86:87], v58 offset:18944\n\t" \
  "v_mfma_f32_16x16x32_bf16 v[192:195], v[136:139], v[112:115], v[192:195]\n\t" \
  "ds_read_b64_tr_b16 v[88:89], v59 offset:18432\n\t" \
  "v_mfma_f32_16x16x32_bf16 v[200:203], v[136:139], v[116:119], v[200:203]\n\t" \
  "ds_read_b64_tr_b16 v[90:91], v59 offset:18944\n\t" \
  "v_mfma_f32_16x16x32_bf16 v[208:211], v[136:139], v[120:123], v[208:211]\n\t" \
  "ds_read_b64_tr_b16 v[136:137], v58 offset:30720\n\t" \
  "v_mfma_f32_16x16x32_bf16 v[252:255], v[140:143], v[124:127], v[252:255]\n\t" \
  "ds_read_b64_tr_b16 v[138:139], v58 offset:31232\n\t" \
  "v_mfma_f32_16x16x32_bf16 v[188:191], v[140:143], v[108:111], v[188:191]\n\t" \
  "ds_read_b128 v[124:127], v57 offset:0\n\t" \
  "v_mfma_f32_16x16x32_bf16 v[196:199], v[140:143], v[112:115], v[196:199]\n\t" \
  "v_mfma_f32_16x16x32_bf16 v[204:207], v[140:143], v[116:119], v[204:207]\n\t" \
  "v_mfma_f32_16x16x32_bf16 v[212:215], v[140:143], v[120:123], v[212:215]\n\t" \
  "ds_read_b64_tr_b16 v[140:141], v59 offset:30720\n\t" \
  "v_mfma_f32_16x16x32_bf16 v[216:219], v[108:111], v[144:147], v[216:219]\n\t" \
  "ds_read_b64_tr_b16 v[142:143], v59 offset:31232\n\t" \
  "v_mfma_f32_16x16x32_bf16 v[224:227], v[112:115], v[144:147], v[224:227]\n\t" \
  "v_mfma_f32_16x16x32_bf16 v[232:235], v[116:119], v[144:147], v[232:235]\n\t" \
  "v_mfma_f32_16x16x32_bf16 v[240:243], v[120:123], v[144:147], v[240:243]\n\t" \
  "ds_read_b64_tr_b16 v[144:145], v58 offset:38912\n\t" \
  "v_mfma_f32_16x16x32_bf16 v[220:223], v[108:111], v[148:151], v[220:223]\n\t" \
  "ds_read_b64_tr_b16 v[146:147], v58 offset:39424\n\t" \
  "v_mfma_f32_16x16x32_bf16 v[228:231], v[112:115], v[148:151], v[228:231]\n\t" \
  "v_mfma_f32_16x16x32_bf16 v[236:239], v[116:119], v[148:151], v[236:239]\n\t" \
  "v_mfma_f32_16x16x32_bf16 v[244:247], v[120:123], v[148:151], v[244:247]\n\t" \
  "ds_read_b64_tr_b16 v[148:149], v59 offset:38912\n\t" \
  "v_mfma_f32_16x16x32_bf16 v[152:155], v[128:131], v[108:111], v[152:155]\n\t" \
  "ds_read_b64_tr_b16 v[150:151], v59 offset:39424\n\t" \
  "v_mfma_f32_16x16x32_bf16 v[160:163], v[128:131], v[112:115], v[160:163]\n\t" \
  "v_mfma_f32_16x16x32_bf16 v[168:171], v[128:131], v[116:119], v[168:171]\n\t" \
  "v_mfma_f32_16x16x32_bf16 v[176:179], v[128:131], v[120:123], v[176:179]\n\t" \
  "ds_read_b64_tr_b16 v[128:129], v58 offset:22528\n\t" \
  "v_mfma_f32_16x16x32_bf16 v[156:159], v[132:135], v[108:111], v[156:159]\n\t" \
  "ds_read_b128 v[108:111], v57 offset:2048\n\t" \
  "v_mfma_f32_16x16x32_bf16 v[164:167], v[132:135], v[112:115], v[164:167]\n\t" \
  "ds_read_b128 v[112:115], v57 offset:4096\n\t" \
  "v_mfma_f32_16x16x32_bf16 v[172:175], v[132:135], v[116:119], v[172:175]\n\t" \
  "ds_read_b128 v[116:119], v57 offset:6144\n\t" \
  "v_mfma_f32_16x16x32_bf16 v[180:183], v[132:135], v[120:123], v[180:183]\n\t" \
  "ds_read_b128 v[120:123], v57 offset:8192\n\t" \
  "s_waitcnt lgkmcnt(5)\n\t" \
  "v_mfma_f32_16x16x32_bf16 v[248:251], v[92:95], v[80:83], v[248:251]\n\t" \
  "ds_read_b64_tr_b16 v[130:131], v58 offset:23040\n\t" \
  "v_mfma_f32_16x16x32_bf16 v[184:187], v[92:95], v[64:67], v[184:187]\n\t" \
  "ds_read_b64_tr_b16 v[132:133], v59 offset:22528\n\t" \
  "v_mfma_f32_16x16x32_bf16 v[192:195], v[92:95], v[68:71], v[192:195]\n\t" \
  "ds_read_b64_tr_b16 v[134:135], v59 offset:23040\n\t" \
  "v_mfma_f32_16x16x32_bf16 v[200:203], v[92:95], v[72:75], v[200:203]\n\t" \
  "v_mfma_f32_16x16x32_bf16 v[208:211], v[92:95], v[76:79], v[208:211]\n\t" \
  "v_mfma_f32_16x16x32_bf16 v[252:255], v[96:99], v[80:83], v[252:255]\n\t" \
  "v_mfma_f32_16x16x32_bf16 v[188:191], v[96:99], v[64:67], v[188:191]\n\t" \
  "v_mfma_f32_16x16x32_bf16 v[196:199], v[96:99], v[68:71], v[196:199]\n\t" \
  "v_mfma_f32_16x16x32_bf16 v[204:207], v[96:99], v[72:75], v[204:207]\n\t" \
  "v_mfma_f32_16x16x32_bf16 v[212:215], v[96:99], v[76:79], v[212:215]\n\t" \
  "v_mfma_f32_16x16x32_bf16 v[216:219], v[64:67], v[100:103], v[216:219]\n\t" \
  "v_mfma_f32_16x16x32_bf16 v[224:227], v[68:71], v[100:103], v[224:227]\n\t" \
  "v_mfma_f32_16x16x32_bf16 v[232:235], v[72:75], v[100:103], v[232:235]\n\t" \
  "s_waitcnt lgkmcnt(0)\n\t" \
  "s_barrier\n\t" \
  "v_mfma_f32_16x16x32_bf16 v[240:243], v[76:79], v[100:103], v[240:243]\n\t" \
  "ds_read_b64_tr_b16 v[92:93], v62 offset:26624\n\t" \
  "v_mfma_f32_16x16x32_bf16 v[220:223], v[64:67], v[104:107], v[220:223]\n\t" \
  "ds_read_b64_tr_b16 v[94:95], v62 offset:27136\n\t" \
  "v_mfma_f32_16x16x32_bf16 v[228:231], v[68:71], v[104:107], v[228:231]\n\t" \
  "ds_read_b128 v[80:83], v60 offset:0\n\t" \
  "v_mfma_f32_16x16x32_bf16 v[236:239], v[72:75], v[104:107], v[236:239]\n\t" \
  "ds_read_b64_tr_b16 v[96:97], v63 offset:26624\n\t" \
  "v_mfma_f32_16x16x32_bf16 v[244:247], v[76:79], v[104:107], v[244:247]\n\t" \
  "ds_read_b64_tr_b16 v[98:99], v63 offset:27136\n\t" \
  "v_mfma_f32_16x16x32_bf16 v[152:155], v[84:87], v[64:67], v[152:155]\n\t" \
  "ds_read_b64_tr_b16 v[100:101], v62 offset:34816\n\t" \
  "v_mfma_f32_16x16x32_bf16 v[160:163], v[84:87], v[68:71], v[160:163]\n\t" \
  "ds_read_b64_tr_b16 v[102:103], v62 offset:35328\n\t" \
  "v_mfma_f32_16x16x32_bf16 v[168:171], v[84:87], v[72:75], v[168:171]\n\t" \
  "ds_read_b64_tr_b16 v[104:105], v63 offset:34816\n\t" \
  "v_mfma_f32_16x16x32_bf16 v[176:179], v[84:87], v[76:79], v[176:179]\n\t" \
  "ds_read_b64_tr_b16 v[106:107], v63 offset:35328\n\t" \
  "v_mfma_f32_16x16x32_bf16 v[156:159], v[88:91], v[64:67], v[156:159]\n\t" \
  "ds_read_b128 v[64:67], v60 offset:2048\n\t" \
  "v_mfma_f32_16x16x32_bf16 v[164:167], v[88:91], v[68:71], v[164:167]\n\t" \
  "ds_read_b128 v[68:71], v60 offset:4096\n\t" \
  "v_mfma_f32_16x16x32_bf16 v[172:175], v[88:91], v[72:75], v[172:175]\n\t" \
  "ds_read_b128 v[72:75], v60 offset:6144\n\t" \
  "v_mfma_f32_16x16x32_bf16 v[180:183], v[88:91], v[76:79], v[180:183]\n\t" \
  "ds_read_b128 v[76:79], v60 offset:8192\n\t" \
  "v_mfma_f32_16x16x32_bf16 v[248:251], v[136:139], v[124:127], v[248:251]\n\t" \
  "ds_read_b64_tr_b16 v[84:85], v62 offset:18432\n\t" \
  "v_mfma_f32_16x16x32_bf16 v[184:187], v[136:139], v[108:111], v[184:187]\n\t" \
  "ds_read_b64_tr_b16 v[86:87], v62 offset:18944\n\t" \
  "v_mfma_f32_16x16x32_bf16 v[192:195], v[136:139], v[112:115], v[192:195]\n\t" \
  "ds_read_b64_tr_b16 v[88:89], v63 offset:18432\n\t" \
  "v_mfma_f32_16x16x32_bf16 v[200:203], v[136:139], v[116:119], v[200:203]\n\t" \
  "ds_read_b64_tr_b16 v[90:91], v63 offset:18944\n\t" \
  "v_mfma_f32_16x16x32_bf16 v[208:211], v[136:139], v[120:123], v[208:211]\n\t" \
  "ds_read_b64_tr_b16 v[136:137], v62 offset:30720\n\t" \
  "v_mfma_f32_16x16x32_bf16 v[252:255], v[140:143], v[124:127], v[252:255]\n\t" \
  "ds_read_b64_tr_b16 v[138:139], v62 offset:31232\n\t" \
  "v_mfma_f32_16x16x32_bf16 v[188:191], v[140:143], v[108:111], v[188:191]\n\t" \
  "ds_read_b128 v[124:127], v61 offset:0\n\t" \
  "v_mfma_f32_16x16x32_bf16 v[196:199], v[140:143], v[112:115], v[196:199]\n\t" \
  "v_mfma_f32_16x16x32_bf16 v[204:207], v[140:143], v[116:119], v[204:207]\n\t" \
  "v_mfma_f32_16x16x32_bf16 v[212:215], v[140:143], v[120:123], v[212:215]\n\t" \
  "ds_read_b64_tr_b16 v[140:141], v63 offset:30720\n\t" \
  "v_mfma_f32_16x16x32_bf16 v[216:219], v[108:111], v[144:147], v[216:219]\n\t" \
  "ds_read_b64_tr_b16 v[142:143], v63 offset:31232\n\t" \
  "v_mfma_f32_16x16x32_bf16 v[224:227], v[112:115], v[144:147], v[224:227]\n\t" \
  "v_mfma_f32_16x16x32_bf16 v[232:235], v[116:119], v[144:147], v[232:235]\n\t" \
  "v_mfma_f32_16x16x32_bf16 v[240:243], v[120:123], v[144:147], v[240:243]\n\t" \
  "ds_read_b64_tr_b16 v[144:145], v62 offset:38912\n\t" \
  "v_mfma_f32_16x16x32_bf16 v[220:223], v[108:111], v[148:151], v[220:223]\n\t" \
  "ds_read_b64_tr_b16 v[146:147], v62 offset:39424\n\t" \
  "v_mfma_f32_16x16x32_bf16 v[228:231], v[112:115], v[148:151], v[228:231]\n\t" \
  "v_mfma_f32_16x16x32_bf16 v[236:239], v[116:119], v[148:151], v[236:239]\n\t" \
  "v_mfma_f32_16x16x32_bf16 v[244:247], v[120:123], v[148:151], v[244:247]\n\t" \
  "ds_read_b64_tr_b16 v[148:149], v63 offset:38912\n\t" \
  "v_mfma_f32_16x16x32_bf16 v[152:155], v[128:131], v[108:111], v[152:155]\n\t" \
  "ds_read_b64_tr_b16 v[150:151], v63 offset:39424\n\t" \
  "v_mfma_f32_16x16x32_bf16 v[160:163], v[128:131], v[112:115], v[160:163]\n\t" \
  "v_mfma_f32_16x16x32_bf16 v[168:171], v[128:131], v[116:119], v[168:171]\n\t" \
  "v_mfma_f32_16x16x32_bf16 v[176:179], v[128:131], v[120:123], v[176:179]\n\t" \
  "ds_read_b64_tr_b16 v[128:129], v62 offset:22528\n\t" \
  "v_mfma_f32_16x16x32_bf16 v[156:159], v[132:135], v[108:111], v[156:159]\n\t" \
  "ds_read_b128 v[108:111], v61 offset:2048\n\t" \
  "v_mfma_f32_16x16x32_bf16 v[164:167], v[132:135], v[112:115], v[164:167]\n\t" \
  "ds_read_b128 v[112:115], v61 offset:4096\n\t" \
  "v_mfma_f32_16x16x32_bf16 v[172:175], v[132:135], v[116:119], v[172:175]\n\t" \
  "ds_read_b128 v[116:119], v61 offset:6144\n\t" \
  "v_mfma_f32_16x16x32_bf16 v[180:183], v[132:135], v[120:123], v[180:183]\n\t" \
  "ds_read_b128 v[120:123], v61 offset:8192\n\t" \
  "s_waitcnt lgkmcnt(5)\n\t" \
  "v_mfma_f32_16x16x32_bf16 v[248:251], v[92:95], v[80:83], v[248:251]\n\t" \
  "ds_read_b64_tr_b16 v[130:131], v62 offset:23040\n\t" \
  "v_mfma_f32_16x16x32_bf16 v[184:187], v[92:95], v[64:67], v[184:187]\n\t" \
  "ds_read_b64_tr_b16 v[132:133], v63 offset:22528\n\t" \
  "v_mfma_f32_16x16x32_bf16 v[192:195], v[92:95], v[68:71], v[192:195]\n\t" \
  "ds_read_b64_tr_b16 v[134:135], v63 offset:23040\n\t" \
  "v_mfma_f32_16x16x32_bf16 v[200:203], v[92:95], v[72:75], v[200:203]\n\t" \
  "v_mfma_f32_16x16x32_bf16 v[208:211], v[92:95], v[76:79], v[208:211]\n\t" \
  "v_mfma_f32_16x16x32_bf16 v[252:255], v[96:99], v[80:83], v[252:255]\n\t" \
  "v_mfma_f32_16x16x32_bf16 v[188:191], v[96:99], v[64:67], v[188:191]\n\t" \
  "v_mfma_f32_16x16x32_bf16 v[196:199], v[96:99], v[68:71], v[196:199]\n\t" \
  "v_mfma_f32_16x16x32_bf16 v[204:207], v[96:99], v[72:75], v[204:207]\n\t" \
  "v_mfma_f32_16x16x32_bf16 v[212:215], v[96:99], v[76:79], v[212:215]\n\t" \
  "v_mfma_f32_16x16x32_bf16 v[216:219], v[64:67], v[100:103], v[216:219]\n\t" \
  "v_mfma_f32_16x16x32_bf16 v[224:227], v[68:71], v[100:103], v[224:227]\n\t" \
  "v_mfma_f32_16x16x32_bf16 v[232:235], v[72:75], v[100:103], v[232:235]\n\t" \
  "s_waitcnt lgkmcnt(0)\n\t" \
  "s_barrier\n\t" \
  "v_mfma_f32_16x16x32_bf16 v[240:243], v[76:79], v[100:103], v[240:243]\n\t" \
  "ds_read_b64_tr_b16 v[92:93], v54 offset:26624\n\t" \
  "v_mfma_f32_16x16x32_bf16 v[220:223], v[64:67], v[104:107], v[220:223]\n\t" \
  "ds_read_b64_tr_b16 v[94:95], v54 offset:27136\n\t" \
  "v_mfma_f32_16x16x32_bf16 v[228:231], v[68:71], v[104:107], v[228:231]\n\t" \
  "ds_read_b128 v[80:83], v52 offset:0\n\t" \
  "v_mfma_f32_16x16x32_bf16 v[236:239], v[72:75], v[104:107], v[236:239]\n\t" \
  "ds_read_b64_tr_b16 v[96:97], v55 offset:26624\n\t" \
  "v_mfma_f32_16x16x32_bf16 v[244:247], v[76:79], v[104:107], v[244:247]\n\t" \
  "ds_read_b64_tr_b16 v[98:99], v55 offset:27136\n\t" \
  "505:\n\t" \
  "s_cbranch_execz 509f\n\t" \
  "v_mfma_f32_16x16x32_bf16 v[152:155], v[84:87], v[64:67], v[152:155]\n\t" \
  "ds_read_b64_tr_b16 v[100:101], v54 offset:34816\n\t" \
  "v_mfma_f32_16x16x32_bf16 v[160:163], v[84:87], v[68:71], v[160:163]\n\t" \
  "ds_read_b64_tr_b16 v[102:103], v54 offset:35328\n\t" \
  "v_mfma_f32_16x16x32_bf16 v[168:171], v[84:87], v[72:75], v[168:171]\n\t" \
  "ds_read_b64_tr_b16 v[104:105], v55 offset:34816\n\t" \
  "v_mfma_f32_16x16x32_bf16 v[176:179], v[84:87], v[76:79], v[176:179]\n\t" \
  "ds_read_b64_tr_b16 v[106:107], v55 offset:35328\n\t" \
  "v_mfma_f32_16x16x32_bf16 v[156:159], v[88:91], v[64:67], v[156:159]\n\t" \
  "ds_read_b128 v[64:67], v52 offset:2048\n\t" \
  "v_mfma_f32_16x16x32_bf16 v[164:167], v[88:91], v[68:71], v[164:167]\n\t" \
  "ds_read_b128 v[68:71], v52 offset:4096\n\t" \
  "v_mfma_f32_16x16x32_bf16 v[172:175], v[88:91], v[72:75], v[172:175]\n\t" \
  "ds_read_b128 v[72:75], v52 offset:6144\n\t" \
  "v_mfma_f32_16x16x32_bf16 v[180:183], v[88:91], v[76:79], v[180:183]\n\t" \
  "ds_read_b128 v[76:79], v52 offset:8192\n\t" \
  "v_mfma_f32_16x16x32_bf16 v[248:251], v[136:139], v[124:127], v[248:251]\n\t" \
  "ds_read_b64_tr_b16 v[84:85], v54 offset:18432\n\t" \
  "v_mfma_f32_16x16x32_bf16 v[184:187], v[136:139], v[108:111], v[184:187]\n\t" \
  "ds_read_b64_tr_b16 v[86:87], v54 offset:18944\n\t" \
  "v_mfma_f32_16x16x32_bf16 v[192:195], v[136:139], v[112:115], v[192:195]\n\t" \
  "ds_read_b64_tr_b16 v[88:89], v55 offset:18432\n\t" \
  "v_mfma_f32_16x16x32_bf16 v[200:203], v[136:139], v[116:119], v[200:203]\n\t" \
  "ds_read_b64_tr_b16 v[90:91], v55 offset:18944\n\t" \
  "v_mfma_f32_16x16x32_bf16 v[208:211], v[136:139], v[120:123], v[208:211]\n\t" \
  "ds_read_b64_tr_b16 v[136:137], v54 offset:30720\n\t" \
  "v_mfma_f32_16x16x32_bf16 v[252:255], v[140:143], v[124:127], v[252:255]\n\t" \
  "ds_read_b64_tr_b16 v[138:139], v54 offset:31232\n\t" \
  "v_mfma_f32_16x16x32_bf16 v[188:191], v[140:143], v[108:111], v[188:191]\n\t" \
  "ds_read_b128 v[124:127], v53 offset:0\n\t" \
  "v_mfma_f32_16x16x32_bf16 v[196:199], v[140:143], v[112:115], v[196:199]\n\t" \
  "v_mfma_f32_16x16x32_bf16 v[204:207], v[140:143], v[116:119], v[204:207]\n\t" \
  "v_mfma_f32_16x16x32_bf16 v[212:215], v[140:143], v[120:123], v[212:215]\n\t" \
  "ds_read_b64_tr_b16 v[140:141], v55 offset:30720\n\t" \
  "v_mfma_f32_16x16x32_bf16 v[216:219], v[108:111], v[144:147], v[216:219]\n\t" \
  "ds_read_b64_tr_b16 v[142:143], v55 offset:31232\n\t" \
  "v_mfma_f32_16x16x32_bf16 v[224:227], v[112:115], v[144:147], v[224:227]\n\t" \
  "v_mfma_f32_16x16x32_bf16 v[232:235], v[116:119], v[144:147], v[232:235]\n\t" \
  "v_mfma_f32_16x16x32_bf16 v[240:243], v[120:123], v[144:147], v[240:243]\n\t" \
  "ds_read_b64_tr_b16 v[144:145], v54 offset:38912\n\t" \
  "v_mfma_f32_16x16x32_bf16 v[220:223], v[108:111], v[148:151], v[220:223]\n\t" \
  "ds_read_b64_tr_b16 v[146:147], v54 offset:39424\n\t" \
  "v_mfma_f32_16x16x32_bf16 v[228:231], v[112:115], v[148:151], v[228:231]\n\t" \
  "v_mfma_f32_16x16x32_bf16 v[236:239], v[116:119], v[148:151], v[236:239]\n\t" \
  "v_mfma_f32_16x16x32_bf16 v[244:247], v[120:123], v[148:151], v[244:247]\n\t" \
  "ds_read_b64_tr_b16 v[148:149], v55 offset:38912\n\t" \
  "v_mfma_f32_16x16x32_bf16 v[152:155], v[128:131], v[108:111], v[152:155]\n\t" \
  "ds_read_b64_tr_b16 v[150:151], v55 offset:39424\n\t" \
  "v_mfma_f32_16x16x32_bf16 v[160:163], v[128:131], v[112:115], v[160:163]\n\t" \
  "v_mfma_f32_16x16x32_bf16 v[168:171], v[128:131], v[116:119], v[168:171]\n\t" \
  "v_mfma_f32_16x16x32_bf16 v[176:179], v[128:131], v[120:123], v[176:179]\n\t" \
  "ds_read_b64_tr_b16 v[128:129], v54 offset:22528\n\t" \
  "v_mfma_f32_16x16x32_bf16 v[156:159], v[132:135], v[108:111], v[156:159]\n\t" \
  "ds_read_b128 v[108:111], v53 offset:2048\n\t" \
  "v_mfma_f32_16x16x32_bf16 v[164:167], v[132:135], v[112:115], v[164:167]\n\t" \
  "ds_read_b128 v[112:115], v53 offset:4096\n\t" \
  "v_mfma_f32_16x16x32_bf16 v[172:175], v[132:135], v[116:119], v[172:175]\n\t" \
  "ds_read_b128 v[116:119], v53 offset:6144\n\t" \
  "v_mfma_f32_16x16x32_bf16 v[180:183], v[132:135], v[120:123], v[180:183]\n\t" \
  "ds_read_b128 v[120:123], v53 offset:8192\n\t" \
  "s_waitcnt lgkmcnt(5)\n\t" \
  "v_mfma_f32_16x16x32_bf16 v[248:251], v[92:95], v[80:83], v[248:251]\n\t" \
  "ds_read_b64_tr_b16 v[130:131], v54 offset:23040\n\t" \
  "v_mfma_f32_16x16x32_bf16 v[184:187], v[92:95], v[64:67], v[184:187]\n\t" \
  "ds_read_b64_tr_b16 v[132:133], v55 offset:22528\n\t" \
  "v_mfma_f32_16x16x32_bf16 v[192:195], v[92:95], v[68:71], v[192:195]\n\t" \
  "ds_read_b64_tr_b16 v[134:135], v55 offset:23040\n\t" \
  "v_mfma_f32_16x16x32_bf16 v[200:203], v[92:95], v[72:75], v[200:203]\n\t" \
  "v_mfma_f32_16x16x32_bf16 v[208:211], v[92:95], v[76:79], v[208:211]\n\t" \
  "v_mfma_f32_16x16x32_bf16 v[252:255], v[96:99], v[80:83], v[252:255]\n\t" \
  "v_mfma_f32_16x16x32_bf16 v[188:191], v[96:99], v[64:67], v[188:191]\n\t" \
  "v_mfma_f32_16x16x32_bf16 v[196:199], v[96:99], v[68:71], v[196:199]\n\t" \
  "v_mfma_f32_16x16x32_bf16 v[204:207], v[96:99], v[72:75], v[204:207]\n\t" \
  "v_mfma_f32_16x16x32_bf16 v[212:215], v[96:99], v[76:79], v[212:215]\n\t" \
  "v_mfma_f32_16x16x32_bf16 v[216:219], v[64:67], v[100:103], v[216:219]\n\t" \
  "v_mfma_f32_16x16x32_bf16 v[224:227], v[68:71], v[100:103], v[224:227]\n\t" \
  "v_mfma_f32_16x16x32_bf16 v[232:235], v[72:75], v[100:103], v[232:235]\n\t" \
  "s_waitcnt lgkmcnt(0)\n\t" \
  "s_barrier\n\t" \
  "v_mfma_f32_16x16x32_bf16 v[240:243], v[76:79], v[100:103], v[240:243]\n\t" \
  "ds_read_b64_tr_b16 v[92:93], v58 offset:26624\n\t" \
  "v_mfma_f32_16x16x32_bf16 v[220:223], v[64:67], v[104:107], v[220:223]\n\t" \
  "ds_read_b64_tr_b16 v[94:95], v58 offset:27136\n\t" \
  "v_mfma_f32_16x16x32_bf16 v[228:231], v[68:71], v[104:107], v[228:231]\n\t" \
  "ds_read_b128 v[80:83], v56 offset:0\n\t" \
  "v_mfma_f32_16x16x32_bf16 v[236:239], v[72:75], v[104:107], v[236:239]\n\t" \
  "ds_read_b64_tr_b16 v[96:97], v59 offset:26624\n\t" \
  "v_mfma_f32_16x16x32_bf16 v[244:247], v[76:79], v[104:107], v[244:247]\n\t" \
  "ds_read_b64_tr_b16 v[98:99], v59 offset:27136\n\t" \
  "v_mfma_f32_16x16x32_bf16 v[152:155], v[84:87], v[64:67], v[152:155]\n\t" \
  "ds_read_b64_tr_b16 v[100:101], v58 offset:34816\n\t" \
  "v_mfma_f32_16x16x32_bf16 v[160:163], v[84:87], v[68:71], v[160:163]\n\t" \
  "ds_read_b64_tr_b16 v[102:103], v58 offset:35328\n\t" \
  "v_mfma_f32_16x16x32_bf16 v[168:171], v[84:87], v[72:75], v[168:171]\n\t" \
  "ds_read_b64_tr_b16 v[104:105], v59 offset:34816\n\t" \
  "v_mfma_f32_16x16x32_bf16 v[176:179], v[84:87], v[76:79], v[176:179]\n\t" \
  "ds_read_b64_tr_b16 v[106:107], v59 offset:35328\n\t" \
  "v_mfma_f32_16x16x32_bf16 v[156:159], v[88:91], v[64:67], v[156:159]\n\t" \
  "ds_read_b128 v[64:67], v56 offset:2048\n\t" \
  "v_mfma_f32_16x16x32_bf16 v[164:167], v[88:91], v[68:71], v[164:167]\n\t" \
  "ds_read_b128 v[68:71], v56 offset:4096\n\t" \
  "v_mfma_f32_16x16x32_bf16 v[172:175], v[88:91], v[72:75], v[172:175]\n\t" \
  "ds_read_b128 v[72:75], v56 offset:6144\n\t" \
  "v_mfma_f32_16x16x32_bf16 v[180:183], v[88:91], v[76:79], v[180:183]\n\t" \
  "ds_read_b128 v[76:79], v56 offset:8192\n\t" \
  "v_mfma_f32_16x16x32_bf16 v[248:251], v[136:139], v[124:127], v[248:251]\n\t" \
  "ds_read_b64_tr_b16 v[84:85], v58 offset:18432\n\t" \
  "v_mfma_f32_16x16x32_bf16 v[184:187], v[136:139], v[108:111], v[184:187]\n\t" \
  "ds_read_b64_tr_b16 v[86:87], v58 offset:18944\n\t" \
  "v_mfma_f32_16x16x32_bf16 v[192:195], v[136:139], v[112:115], v[192:195]\n\t" \
  "ds_read_b64_tr_b16 v[88:89], v59 offset:18432\n\t" \
  "v_mfma_f32_16x16x32_bf16 v[200:203], v[136:139], v[116:119], v[200:203]\n\t" \
  "ds_read_b64_tr_b16 v[90:91], v59 offset:18944\n\t" \
  "v_mfma_f32_16x16x32_bf16 v[208:211], v[136:139], v[120:123], v[208:211]\n\t" \
  "ds_read_b64_tr_b16 v[136:137], v58 offset:30720\n\t" \
  "v_mfma_f32_16x16x32_bf16 v[252:255], v[140:143], v[124:127], v[252:255]\n\t" \
  "ds_read_b64_tr_b16 v[138:139], v58 offset:31232\n\t" \
  "v_mfma_f32_16x16x32_bf16 v[188:191], v[140:143], v[108:111], v[188:191]\n\t" \
  "ds_read_b128 v[124:127], v57 offset:0\n\t" \
  "v_mfma_f32_16x16x32_bf16 v[196:199], v[140:143], v[112:115], v[196:199]\n\t" \
  "v_mfma_f32_16x16x32_bf16 v[204:207], v[140:143], v[116:119], v[204:207]\n\t" \
  "v_mfma_f32_16x16x32_bf16 v[212:215], v[140:143], v[120:123], v[212:215]\n\t" \
  "ds_read_b64_tr_b16 v[140:141], v59 offset:30720\n\t" \
  "v_mfma_f32_16x16x32_bf16 v[216:219], v[108:111], v[144:147], v[216:219]\n\t" \
  "ds_read_b64_tr_b16 v[142:143], v59 offset:31232\n\t" \
  "v_mfma_f32_16x16x32_bf16 v[224:227], v[112:115], v[144:147], v[224:227]\n\t" \
  "v_mfma_f32_16x16x32_bf16 v[232:235], v[116:119], v[144:147], v[232:235]\n\t" \
  "v_mfma_f32_16x16x32_bf16 v[240:243], v[120:123], v[144:147], v[240:243]\n\t" \
  "ds_read_b64_tr_b16 v[144:145], v58 offset:38912\n\t" \
  "v_mfma_f32_16x16x32_bf16 v[220:223], v[108:111], v[148:151], v[220:223]\n\t" \
  "ds_read_b64_tr_b16 v[146:147], v58 offset:39424\n\t" \
  "v_mfma_f32_16x16x32_bf16 v[228:231], v[112:115], v[148:151], v[228:231]\n\t" \
  "v_mfma_f32_16x16x32_bf16 v[236:239], v[116:119], v[148:151], v[236:239]\n\t" \
  "v_mfma_f32_16x16x32_bf16 v[244:247], v[120:123], v[148:151], v[244:247]\n\t" \
  "ds_read_b64_tr_b16 v[148:149], v59 offset:38912\n\t" \
  "v_mfma_f32_16x16x32_bf16 v[152:155], v[128:131], v[108:111], v[152:155]\n\t" \
  "ds_read_b64_tr_b16 v[150:151], v59 offset:39424\n\t" \
  "v_mfma_f32_16x16x32_bf16 v[160:163], v[128:131], v[112:115], v[160:163]\n\t" \
  "v_mfma_f32_16x16x32_bf16 v[168:171], v[128:131], v[116:119], v[168:171]\n\t" \
  "v_mfma_f32_16x16x32_bf16 v[176:179], v[128:131], v[120:123], v[176:179]\n\t" \
  "ds_read_b64_tr_b16 v[128:129], v58 offset:22528\n\t" \
  "v_mfma_f32_16x16x32_bf16 v[156:159], v[132:135], v[108:111], v[156:159]\n\t" \
  "ds_read_b128 v[108:111], v57 offset:2048\n\t" \
  "v_mfma_f32_16x16x32_bf16 v[164:167], v[132:135], v[112:115], v[164:167]\n\t" \
  "ds_read_b128 v[112:115], v57 offset:4096\n\t" \
  "v_mfma_f32_16x16x32_bf16 v[172:175], v[132:135], v[116:119], v[172:175]\n\t" \
  "ds_read_b128 v[116:119], v57 offset:6144\n\t" \
  "v_mfma_f32_16x16x32_bf16 v[180:183], v[132:135], v[120:123], v[180:183]\n\t" \
  "ds_read_b128 v[120:123], v57 offset:8192\n\t" \
  "s_waitcnt lgkmcnt(5)\n\t" \
  "v_mfma_f32_16x16x32_bf16 v[248:251], v[92:95], v[80:83], v[248:251]\n\t" \
  "ds_read_b64_tr_b16 v[130:131], v58 offset:23040\n\t" \
  "v_mfma_f32_16x16x32_bf16 v[184:187], v[92:95], v[64:67], v[184:187]\n\t" \
  "v_and_b32 v14, -16, v32\n\t" \
  "ds_read_b64_tr_b16 v[132:133], v59 offset:22528\n\t" \
  "v_mfma_f32_16x16x32_bf16 v[192:195], v[92:95], v[68:71], v[192:195]\n\t" \
  "ds_read_b64_tr_b16 v[134:135], v59 offset:23040\n\t" \
  "v_mfma_f32_16x16x32_bf16 v[200:203], v[92:95], v[72:75], v[200:203]\n\t" \
  "ds_read_b128 v[10:13], v14 offset:128\n\t" \
  "v_mfma_f32_16x16x32_bf16 v[208:211], v[92:95], v[76:79], v[208:211]\n\t" \
  "v_mfma_f32_16x16x32_bf16 v[252:255], v[96:99], v[80:83], v[252:255]\n\t" \
  "v_mfma_f32_16x16x32_bf16 v[188:191], v[96:99], v[64:67], v[188:191]\n\t" \
  "v_mfma_f32_16x16x32_bf16 v[196:199], v[96:99], v[68:71], v[196:199]\n\t" \
  "v_mfma_f32_16x16x32_bf16 v[204:207], v[96:99], v[72:75], v[204:207]\n\t" \
  "v_mfma_f32_16x16x32_bf16 v[212:215], v[96:99], v[76:79], v[212:215]\n\t" \
  "s_waitcnt lgkmcnt(0)\n\t" \
  "v_and_b32 v10, v10, v11\n\t" \
  "v_and_b32 v12, v12, v13\n\t" \
  "v_mfma_f32_16x16x32_bf16 v[216:219], v[64:67], v[100:103], v[216:219]\n\t" \
  "v_and_b32 v10, v10, v12\n\t" \
  "v_mfma_f32_16x16x32_bf16 v[224:227], v[68:71], v[100:103], v[224:227]\n\t" \
  "v_readfirstlane_b32 vcc_lo, v10\n\t" \
  "v_mfma_f32_16x16x32_bf16 v[232:235], v[72:75], v[100:103], v[232:235]\n\t" \
  "ds_write_b32 v32, v0 offset:144\n\t" \
  "s_cmp_eq_u32 vcc_lo, 0\n\t" \
  "s_cbranch_scc1 15f\n\t" \
  "14:\n\t" \
  "v_mfma_f32_16x16x32_bf16 v[240:243], v[76:79], v[100:103], v[240:243]\n\t" \
  "ds_read_b64_tr_b16 v[92:93], v62 offset:26624\n\t" \
  "v_mfma_f32_16x16x32_bf16 v[220:223], v[64:67], v[104:107], v[220:223]\n\t" \
  "ds_read_b64_tr_b16 v[94:95], v62 offset:27136\n\t" \
  "v_mfma_f32_16x16x32_bf16 v[228:231], v[68:71], v[104:107], v[228:231]\n\t" \
  "ds_read_b128 v[80:83], v60 offset:0\n\t" \
  "v_mfma_f32_16x16x32_bf16 v[236:239], v[72:75], v[104:107], v[236:239]\n\t" \
  "ds_read_b64_tr_b16 v[96:97], v63 offset:26624\n\t" \
  "v_mfma_f32_16x16x32_bf16 v[244:247], v[76:79], v[104:107], v[244:247]\n\t" \
  "ds_read_b64_tr_b16 v[98:99], v63 offset:27136\n\t" \
  "v_mfma_f32_16x16x32_bf16 v[152:155], v[84:87], v[64:67], v[152:155]\n\t" \
  "ds_read_b64_tr_b16 v[100:101], v62 offset:34816\n\t" \
  "v_mfma_f32_16x16x32_bf16 v[160:163], v[84:87], v[68:71], v[160:163]\n\t" \
  "ds_read_b64_tr_b16 v[102:103], v62 offset:35328\n\t" \
  "v_mfma_f32_16x16x32_bf16 v[168:171], v[84:87], v[72:75], v[168:171]\n\t" \
  "ds_read_b64_tr_b16 v[104:105], v63 offset:34816\n\t" \
  "v_mfma_f32_16x16x32_bf16 v[176:179], v[84:87], v[76:79], v[176:179]\n\t" \
  "ds_read_b64_tr_b16 v[106:107], v63 offset:35328\n\t" \
  "v_mfma_f32_16x16x32_bf16 v[156:159], v[88:91], v[64:67], v[156:159]\n\t" \
  "ds_read_b128 v[64:67], v60 offset:2048\n\t" \
  "v_mfma_f32_16x16x32_bf16 v[164:167], v[88:91], v[68:71], v[164:167]\n\t" \
  "ds_read_b128 v[68:71], v60 offset:4096\n\t" \
  "v_mfma_f32_16x16x32_bf16 v[172:175], v[88:91], v[72:75], v[172:175]\n\t" \
  "ds_read_b128 v[72:75], v60 offset:6144\n\t" \
  "v_mfma_f32_16x16x32_bf16 v[180:183], v[88:91], v[76:79], v[180:183]\n\t" \
  "ds_read_b128 v[76:79], v60 offset:8192\n\t" \
  "v_mfma_f32_16x16x32_bf16 v[248:251], v[136:139], v[124:127], v[248:251]\n\t" \
  "ds_read_b64_tr_b16 v[84:85], v62 offset:18432\n\t" \
  "v_mfma_f32_16x16x32_bf16 v[184:187], v[136:139], v[108:111], v[184:187]\n\t" \
  "ds_read_b64_tr_b16 v[86:87], v62 offset:18944\n\t" \
  "v_mfma_f32_16x16x32_bf16 v[192:195], v[136:139], v[112:115], v[192:195]\n\t" \
  "ds_read_b64_tr_b16 v[88:89], v63 offset:18432\n\t" \
  "v_mfma_f32_16x16x32_bf16 v[200:203], v[136:139], v[116:119], v[200:203]\n\t" \
  "ds_read_b64_tr_b16 v[90:91], v63 offset:18944\n\t" \
  "v_mfma_f32_16x16x32_bf16 v[208:211], v[136:139], v[120:123], v[208:211]\n\t" \
  "ds_read_b64_tr_b16 v[136:137], v62 offset:30720\n\t" \
  "v_mfma_f32_16x16x32_bf16 v[252:255], v[140:143], v[124:127], v[252:255]\n\t" \
  "ds_read_b64_tr_b16 v[138:139], v62 offset:31232\n\t" \
  "v_mfma_f32_16x16x32_bf16 v[188:191], v[140:143], v[108:111], v[188:191]\n\t" \
  "ds_read_b128 v[124:127], v61 offset:0\n\t" \
  "v_mfma_f32_16x16x32_bf16 v[196:199], v[140:143], v[112:115], v[196:199]\n\t" \
  "v_mfma_f32_16x16x32_bf16 v[204:207], v[140:143], v[116:119], v[204:207]\n\t" \
  "v_mfma_f32_16x16x32_bf16 v[212:215], v[140:143], v[120:123], v[212:215]\n\t" \
  "ds_read_b64_tr_b16 v[140:141], v63 offset:30720\n\t" \
  "v_mfma_f32_16x16x32_bf16 v[216:219], v[108:111], v[144:147], v[216:219]\n\t" \
  "ds_read_b64_tr_b16 v[142:143], v63 offset:31232\n\t" \
  "v_mfma_f32_16x16x32_bf16 v[224:227], v[112:115], v[144:147], v[224:227]\n\t" \
  "v_mfma_f32_16x16x32_bf16 v[232:235], v[116:119], v[144:147], v[232:235]\n\t" \
  "v_mfma_f32_16x16x32_bf16 v[240:243], v[120:123], v[144:147], v[240:243]\n\t" \
  "ds_read_b64_tr_b16 v[144:145], v62 offset:38912\n\t" \
  "v_mfma_f32_16x16x32_bf16 v[220:223], v[108:111], v[148:151], v[220:223]\n\t" \
  "ds_read_b64_tr_b16 v[146:147], v62 offset:39424\n\t" \
  "v_mfma_f32_16x16x32_bf16 v[228:231], v[112:115], v[148:151], v[228:231]\n\t" \
  "v_mfma_f32_16x16x32_bf16 v[236:239], v[116:119], v[148:151], v[236:239]\n\t" \
  "v_mfma_f32_16x16x32_bf16 v[244:247], v[120:123], v[148:151], v[244:247]\n\t" \
  "ds_read_b64_tr_b16 v[148:149], v63 offset:38912\n\t" \
  "v_mfma_f32_16x16x32_bf16 v[152:155], v[128:131], v[108:111], v[152:155]\n\t" \
  "ds_read_b64_tr_b16 v[150:151], v63 offset:39424\n\t" \
  "v_mfma_f32_16x16x32_bf16 v[160:163], v[128:131], v[112:115], v[160:163]\n\t" \
  "v_mfma_f32_16x16x32_bf16 v[168:171], v[128:131], v[116:119], v[168:171]\n\t" \
  "v_mfma_f32_16x16x32_bf16 v[176:179], v[128:131], v[120:123], v[176:179]\n\t" \
  "ds_read_b64_tr_b16 v[128:129], v62 offset:22528\n\t" \
  "v_mfma_f32_16x16x32_bf16 v[156:159], v[132:135], v[108:111], v[156:159]\n\t" \
  "ds_read_b128 v[108:111], v61 offset:2048\n\t" \
  "v_mfma_f32_16x16x32_bf16 v[164:167], v[132:135], v[112:115], v[164:167]\n\t" \
  "ds_read_b128 v[112:115], v61 offset:4096\n\t" \
  "v_mfma_f32_16x16x32_bf16 v[172:175], v[132:135], v[116:119], v[172:175]\n\t" \
  "ds_read_b128 v[116:119], v61 offset:6144\n\t" \
  "v_mfma_f32_16x16x32_bf16 v[180:183], v[132:135], v[120:123], v[180:183]\n\t" \
  "ds_read_b128 v[120:123], v61 offset:8192\n\t" \
  "s_waitcnt lgkmcnt(5)\n\t" \
  "v_mfma_f32_16x16x32_bf16 v[248:251], v[92:95], v[80:83], v[248:251]\n\t" \
  "ds_read_b64_tr_b16 v[130:131], v62 offset:23040\n\t" \
  "v_mfma_f32_16x16x32_bf16 v[184:187], v[92:95], v[64:67], v[184:187]\n\t" \
  "ds_read_b64_tr_b16 v[132:133], v63 offset:22528\n\t" \
  "v_mfma_f32_16x16x32_bf16 v[192:195], v[92:95], v[68:71], v[192:195]\n\t" \
  "ds_read_b64_tr_b16 v[134:135], v63 offset:23040\n\t" \
  "v_mfma_f32_16x16x32_bf16 v[200:203], v[92:95], v[72:75], v[200:203]\n\t" \
  "v_mfma_f32_16x16x32_bf16 v[208:211], v[92:95], v[76:79], v[208:211]\n\t" \
  "v_mfma_f32_16x16x32_bf16 v[252:255], v[96:99], v[80:83], v[252:255]\n\t" \
  "v_mfma_f32_16x16x32_bf16 v[188:191], v[96:99], v[64:67], v[188:191]\n\t" \
  "v_mfma_f32_16x16x32_bf16 v[196:199], v[96:99], v[68:71], v[196:199]\n\t" \
  "v_mfma_f32_16x16x32_bf16 v[204:207], v[96:99], v[72:75], v[204:207]\n\t" \
  "v_mfma_f32_16x16x32_bf16 v[212:215], v[96:99], v[76:79], v[212:215]\n\t" \
  "v_mfma_f32_16x16x32_bf16 v[216:219], v[64:67], v[100:103], v[216:219]\n\t" \
  "v_mfma_f32_16x16x32_bf16 v[224:227], v[68:71], v[100:103], v[224:227]\n\t" \
  "v_mfma_f32_16x16x32_bf16 v[232:235], v[72:75], v[100:103], v[232:235]\n\t" \
  "s_waitcnt lgkmcnt(0)\n\t" \
  "ds_write_b32 v32, v0\n\t" \
  "v_mfma_f32_16x16x32_bf16 v[240:243], v[76:79], v[100:103], v[240:243]\n\t" \
  "v_mfma_f32_16x16x32_bf16 v[220:223], v[64:67], v[104:107], v[220:223]\n\t" \
  "v_mfma_f32_16x16x32_bf16 v[228:231], v[68:71], v[104:107], v[228:231]\n\t" \
  "v_mfma_f32_16x16x32_bf16 v[236:239], v[72:75], v[104:107], v[236:239]\n\t" \
  "v_mfma_f32_16x16x32_bf16 v[244:247], v[76:79], v[104:107], v[244:247]\n\t" \
  "v_mfma_f32_16x16x32_bf16 v[152:155], v[84:87], v[64:67], v[152:155]\n\t" \
  "v_mfma_f32_16x16x32_bf16 v[160:163], v[84:87], v[68:71], v[160:163]\n\t" \
  "v_mfma_f32_16x16x32_bf16 v[168:171], v[84:87], v[72:75], v[168:171]\n\t" \
  "v_mfma_f32_16x16x32_bf16 v[176:179], v[84:87], v[76:79], v[176:179]\n\t" \
  "v_mfma_f32_16x16x32_bf16 v[156:159], v[88:91], v[64:67], v[156:159]\n\t" \
  "v_mfma_f32_16x16x32_bf16 v[164:167], v[88:91], v[68:71], v[164:167]\n\t" \
  "v_mfma_f32_16x16x32_bf16 v[172:175], v[88:91], v[72:75], v[172:175]\n\t" \
  "v_mfma_f32_16x16x32_bf16 v[180:183], v[88:91], v[76:79], v[180:183]\n\t" \
  "v_mfma_f32_16x16x32_bf16 v[248:251], v[136:139], v[124:127], v[248:251]\n\t" \
  "v_and_b32 v56, -16, v32\n\t" \
  "v_mfma_f32_16x16x32_bf16 v[184:187], v[136:139], v[108:111], v[184:187]\n\t" \
  "v_mfma_f32_16x16x32_bf16 v[192:195], v[136:139], v[112:115], v[192:195]\n\t" \
  "v_mfma_f32_16x16x32_bf16 v[200:203], v[136:139], v[116:119], v[200:203]\n\t" \
  "v_mfma_f32_16x16x32_bf16 v[208:211], v[136:139], v[120:123], v[208:211]\n\t" \
  "v_mfma_f32_16x16x32_bf16 v[252:255], v[140:143], v[124:127], v[252:255]\n\t" \
  "v_mfma_f32_16x16x32_bf16 v[188:191], v[140:143], v[108:111], v[188:191]\n\t" \
  "v_mfma_f32_16x16x32_bf16 v[196:199], v[140:143], v[112:115], v[196:199]\n\t" \
  "v_cvt_pk_bf16_f32 v10, v248, v249\n\t" \
  "v_cvt_pk_bf16_f32 v11, v250, v251\n\t" \
  "v_mfma_f32_16x16x32_bf16 v[204:207], v[140:143], v[116:119], v[204:207]\n\t" \
  "v_cvt_pk_bf16_f32 v14, v184, v185\n\t" \
  "v_cvt_pk_bf16_f32 v15, v186, v187\n\t" \
  "v_mfma_f32_16x16x32_bf16 v[212:215], v[140:143], v[120:123], v[212:215]\n\t" \
  "v_cvt_pk_bf16_f32 v12, v252, v253\n\t" \
  "v_cvt_pk_bf16_f32 v13, v254, v255\n\t" \
  "v_mfma_f32_16x16x32_bf16 v[152:155], v[128:131], v[108:111], v[152:155]\n\t" \
  "ds_write_b128 v30, v[10:13]\n\t" \
  "ds_write_b32 v32, v0 offset:48\n\t" \
  "v_mfma_f32_16x16x32_bf16 v[160:163], v[128:131], v[112:115], v[160:163]\n\t" \
  "v_cvt_pk_bf16_f32 v16, v188, v189\n\t" \
  "v_cvt_pk_bf16_f32 v17, v190, v191\n\t" \
  "v_mfma_f32_16x16x32_bf16 v[168:171], v[128:131], v[116:119], v[168:171]\n\t" \
  "v_cvt_pk_bf16_f32 v18, v192, v193\n\t" \
  "v_cvt_pk_bf16_f32 v19, v194, v195\n\t" \
  "v_mfma_f32_16x16x32_bf16 v[176:179], v[128:131], v[120:123], v[176:179]\n\t" \
  "v_cvt_pk_bf16_f32 v20, v196, v197\n\t" \
  "v_cvt_pk_bf16_f32 v21, v198, v199\n\t" \
  "v_mfma_f32_16x16x32_bf16 v[156:159], v[132:135], v[108:111], v[156:159]\n\t" \
  "v_cvt_pk_bf16_f32 v22, v200, v201\n\t" \
  "v_cvt_pk_bf16_f32 v23, v202, v203\n\t" \
  "v_mfma_f32_16x16x32_bf16 v[164:167], v[132:135], v[112:115], v[164:167]\n\t" \
  "v_cvt_pk_bf16_f32 v24, v204, v205\n\t" \
  "v_cvt_pk_bf16_f32 v25, v206, v207\n\t" \
  "v_mfma_f32_16x16x32_bf16 v[172:175], v[132:135], v[116:119], v[172:175]\n\t" \
  "v_cvt_pk_bf16_f32 v26, v208, v209\n\t" \
  "v_cvt_pk_bf16_f32 v27, v210, v211\n\t" \
  "v_mfma_f32_16x16x32_bf16 v[180:183], v[132:135], v[120:123], v[180:183]\n\t" \
  "v_cvt_pk_bf16_f32 v28, v212, v213\n\t" \
  "v_cvt_pk_bf16_f32 v29, v214, v215\n\t" \
  "v_mfma_f32_16x16x32_bf16 v[216:219], v[108:111], v[144:147], v[216:219]\n\t" \
  "v_cvt_pk_bf16_f32 v84, v152, v153\n\t" \
  "v_cvt_pk_bf16_f32 v85, v154, v155\n\t" \
  "v_mfma_f32_16x16x32_bf16 v[224:227], v[112:115], v[144:147], v[224:227]\n\t" \
  "v_cvt_pk_bf16_f32 v86, v156, v157\n\t" \
  "v_cvt_pk_bf16_f32 v87, v158, v159\n\t" \
  "v_mfma_f32_16x16x32_bf16 v[232:235], v[116:119], v[144:147], v[232:235]\n\t" \
  "v_cvt_pk_bf16_f32 v88, v160, v161\n\t" \
  "v_cvt_pk_bf16_f32 v89, v162, v163\n\t" \
  "v_mfma_f32_16x16x32_bf16 v[240:243], v[120:123], v[144:147], v[240:243]\n\t" \
  "v_cvt_pk_bf16_f32 v90, v164, v165\n\t" \
  "v_cvt_pk_bf16_f32 v91, v166, v167\n\t" \
  "v_mfma_f32_16x16x32_bf16 v[220:223], v[108:111], v[148:151], v[220:223]\n\t" \
  "v_cvt_pk_bf16_f32 v92, v168, v169\n\t" \
  "v_cvt_pk_bf16_f32 v93, v170, v171\n\t" \
  "v_mfma_f32_16x16x32_bf16 v[228:231], v[112:115], v[148:151], v[228:231]\n\t" \
  "v_cvt_pk_bf16_f32 v94, v172, v173\n\t" \
  "v_cvt_pk_bf16_f32 v95, v174, v175\n\t" \
  "v_mfma_f32_16x16x32_bf16 v[236:239], v[116:119], v[148:151], v[236:239]\n\t" \
  "v_cvt_pk_bf16_f32 v96, v176, v177\n\t" \
  "v_cvt_pk_bf16_f32 v97, v178, v179\n\t" \
  "v_mfma_f32_16x16x32_bf16 v[244:247], v[120:123], v[148:151], v[244:247]\n\t" \
  "v_cvt_pk_bf16_f32 v98, v180, v181\n\t" \
  "v_cvt_pk_bf16_f32 v99, v182, v183\n\t" \
  "v_mfma_f32_16x16x32_bf16 v[200:203], v[22:25], v[92:95], v[34:37]\n\t" \
  "v_cvt_pk_bf16_f32 v66, v232, v233\n\t" \
  "v_cvt_pk_bf16_f32 v67, v234, v235\n\t" \
  "v_mfma_f32_16x16x32_bf16 v[208:211], v[26:29], v[96:99], v[34:37]\n\t" \
  "v_cvt_pk_bf16_f32 v76, v236, v237\n\t" \
  "v_cvt_pk_bf16_f32 v77, v238, v239\n\t" \
  "v_mfma_f32_16x16x32_bf16 v[212:215], v[22:25], v[96:99], v[38:41]\n\t" \
  "v_cvt_pk_bf16_f32 v64, v240, v241\n\t" \
  "v_cvt_pk_bf16_f32 v65, v242, v243\n\t" \
  "v_mfma_f32_16x16x32_bf16 v[204:207], v[18:21], v[92:95], v[38:41]\n\t" \
  "v_cvt_pk_bf16_f32 v74, v244, v245\n\t" \
  "v_cvt_pk_bf16_f32 v75, v246, v247\n\t" \
  "v_mfma_f32_16x16x32_bf16 v[192:195], v[18:21], v[88:91], v[34:37]\n\t" \
  "v_cvt_pk_bf16_f32 v68, v224, v225\n\t" \
  "v_cvt_pk_bf16_f32 v69, v226, v227\n\t" \
  "v_mfma_f32_16x16x32_bf16 v[196:199], v[14:17], v[88:91], v[38:41]\n\t" \
  "v_max_f32 v208, v208, v212\n\t" \
  "v_max_f32 v209, v209, v213\n\t" \
  "v_mfma_f32_16x16x32_bf16 v[184:187], v[14:17], v[84:87], v[34:37]\n\t" \
  "v_max_f32 v210, v210, v214\n\t" \
  "v_max_f32 v200, v200, v204\n\t" \
  "v_mfma_f32_16x16x32_bf16 v[188:191], v[10:13], v[84:87], v[42:45]\n\t" \
  "v_max_f32 v201, v201, v205\n\t" \
  "v_max_f32 v202, v202, v206\n\t" \
  "v_max_f32 v192, v192, v196\n\t" \
  "v_max_f32 v193, v193, v197\n\t" \
  "v_max_f32 v194, v194, v198\n\t" \
  "v_max_f32 v211, v211, v215\n\t" \
  "ds_read_b32 v52, v33 offset:48\n\t" \
  "ds_read_b64 v[72:73], v31\n\t" \
  "ds_read_b64 v[82:83], v31 offset:8\n\t" \
  "v_max3_f32 v106, v208, v209, v210\n\t" \
  "v_max_f32 v184, v184, v188\n\t" \
  "v_max_f32 v185, v185, v189\n\t" \
  "v_max_f32 v186, v186, v190\n\t" \
  "v_max_f32 v203, v203, v207\n\t" \
  "v_max3_f32 v104, v200, v201, v202\n\t" \
  "v_max_f32 v195, v195, v199\n\t" \
  "v_max3_f32 v102, v192, v193, v194\n\t" \
  "v_max_f32 v187, v187, v191\n\t" \
  "v_max3_f32 v100, v184, v185, v186\n\t" \
  "v_max_f32 v106, v106, v211\n\t" \
  "v_max_f32 v104, v104, v203\n\t" \
  "v_max_f32 v102, v102, v195\n\t" \
  "v_max_f32 v100, v100, v187\n\t" \
  "v_permlane16_swap_b32 v104, v106\n\t" \
  "v_cvt_pk_bf16_f32 v78, v228, v229\n\t" \
  "v_permlane16_swap_b32 v100, v102\n\t" \
  "v_max_f32 v104, v104, v106\n\t" \
  "v_max_f32 v100, v100, v102\n\t" \
  "v_cvt_pk_bf16_f32 v79, v230, v231\n\t" \
  "v_cvt_pk_bf16_f32 v70, v216, v217\n\t" \
  "v_permlane32_swap_b32 v100, v104\n\t" \
  "v_cvt_pk_bf16_f32 v71, v218, v219\n\t" \
  "v_max_f32 v100, v100, v104\n\t" \
  "v_mul_f32 v100, 0xbe8293ee, v100\n\t" \
  "v_mov_b32 v104, v100\n\t" \
  "v_cvt_pk_bf16_f32 v80, v220, v221\n\t" \
  "v_cvt_pk_bf16_f32 v81, v222, v223\n\t" \
  "v_permlane32_swap_b32 v100, v104\n\t" \
  "s_waitcnt lgkmcnt(0)\n\t" \
  "v_readfirstlane_b32 vcc_lo, v52\n\t" \
  "s_nop 1\n\t" \
  "s_cmp_eq_u32 vcc_lo, 0\n\t" \
  "s_cbranch_scc1 5f\n\t" \
  "4:\n\t" \
  "v_mov_b32 v106, v104\n\t" \
  "v_mov_b32 v102, v100\n\t" \
  "s_nop 0\n\t" \
  "v_permlane16_swap_b32 v104, v106\n\t" \
  "v_permlane16_swap_b32 v100, v102\n\t" \
  "v_fma_f32 v208, v208, %16, v106\n\t" \
  "v_fma_f32 v209, v209, %16, v106\n\t" \
  "v_fma_f32 v210, v210, %16, v106\n\t" \
  "v_fma_f32 v211, v211, %16, v106\n\t" \
  "v_exp_f32 v208, v208\n\t" \
  "v_exp_f32 v209, v209\n\t" \
  "v_exp_f32 v210, v210\n\t" \
  "v_exp_f32 v211, v211\n\t" \
  "v_cvt_pk_bf16_f32 v212, v208, v209\n\t" \
  "v_cvt_pk_bf16_f32 v213, v210, v211\n\t" \
  "v_bfi_b32 v214, v46, 0, v212\n\t" \
  "v_bfi_b32 v215, v47, 0, v213\n\t" \
  "v_and_b32 v212, v46, v212\n\t" \
  "v_and_b32 v213, v47, v213\n\t" \
  "v_fma_f32 v200, v200, %16, v104\n\t" \
  "v_fma_f32 v201, v201, %16, v104\n\t" \
  "v_mfma_f32_16x16x32_bf16 v[208:211], v[48:51], v[212:215], 0\n\t" \
  "v_fma_f32 v202, v202, %16, v104\n\t" \
  "v_fma_f32 v203, v203, %16, v104\n\t" \
  "v_mfma_f32_16x16x32_bf16 v[176:179], v[64:67], v[212:215], 0\n\t" \
  "v_exp_f32 v200, v200\n\t" \
  "v_mfma_f32_16x16x32_bf16 v[180:183], v[74:77], v[212:215], 0\n\t" \
  "v_exp_f32 v201, v201\n\t" \
  "v_exp_f32 v202, v202\n\t" \
  "v_exp_f32 v203, v203\n\t" \
  "v_cvt_pk_bf16_f32 v204, v200, v201\n\t" \
  "v_cvt_pk_bf16_f32 v205, v202, v203\n\t" \
  "v_bfi_b32 v206, v46, 0, v204\n\t" \
  "v_bfi_b32 v207, v47, 0, v205\n\t" \
  "v_rcp_f32 v106, v208\n\t" \
  "v_and_b32 v204, v46, v204\n\t" \
  "v_and_b32 v205, v47, v205\n\t" \
  "v_pk_mul_f32 v[176:177], v[176:177], v[106:107] op_sel_hi:[1,0]\n\t" \
  "v_pk_mul_f32 v[178:179], v[178:179], v[106:107] op_sel_hi:[1,0]\n\t" \
  "v_mfma_f32_16x16x32_bf16 v[200:203], v[48:51], v[204:207], 0\n\t" \
  "v_pk_mul_f32 v[180:181], v[180:181], v[106:107] op_sel_hi:[1,0]\n\t" \
  "v_pk_mul_f32 v[182:183], v[182:183], v[106:107] op_sel_hi:[1,0]\n\t" \
  "v_mfma_f32_16x16x32_bf16 v[168:171], v[66:69], v[204:207], 0\n\t" \
  "v_cvt_pk_bf16_f32 v240, v176, v177\n\t" \
  "v_cvt_pk_bf16_f32 v241, v178, v179\n\t" \
  "v_mfma_f32_16x16x32_bf16 v[172:175], v[76:79], v[204:207], 0\n\t" \
  "v_cvt_pk_bf16_f32 v242, v180, v181\n\t" \
  "v_cvt_pk_bf16_f32 v243, v182, v183\n\t" \
  "global_store_dwordx4 v1, v[240:243], %14 offset:0 sc1\n\t" \
  "v_fma_f32 v192, v192, %16, v102\n\t" \
  "v_fma_f32 v193, v193, %16, v102\n\t" \
  "v_fma_f32 v194, v194, %16, v102\n\t" \
  "v_fma_f32 v195, v195, %16, v102\n\t" \
  "v_exp_f32 v192, v192\n\t" \
  "v_exp_f32 v193, v193\n\t" \
  "v_exp_f32 v194, v194\n\t" \
  "v_exp_f32 v195, v195\n\t" \
  "v_cvt_pk_bf16_f32 v196, v192, v193\n\t" \
  "v_cvt_pk_bf16_f32 v197, v194, v195\n\t" \
  "v_bfi_b32 v198, v46, 0, v196\n\t" \
  "v_bfi_b32 v199, v47, 0, v197\n\t" \
  "v_rcp_f32 v104, v200\n\t" \
  "v_and_b32 v196, v46, v196\n\t" \
  "v_and_b32 v197, v47, v197\n\t" \
  "v_pk_mul_f32 v[168:169], v[168:169], v[104:105] op_sel_hi:[1,0]\n\t" \
  "v_pk_mul_f32 v[170:171], v[170:171], v[104:105] op_sel_hi:[1,0]\n\t" \
  "v_mfma_f32_16x16x32_bf16 v[192:195], v[48:51], v[196:199], 0\n\t" \
  "v_pk_mul_f32 v[172:173], v[172:173], v[104:105] op_sel_hi:[1,0]\n\t" \
  "v_pk_mul_f32 v[174:175], v[174:175], v[104:105] op_sel_hi:[1,0]\n\t" \
  "v_mfma_f32_16x16x32_bf16 v[160:163], v[68:71], v[196:199], 0\n\t" \
  "v_cvt_pk_bf16_f32 v232, v168, v169\n\t" \
  "v_cvt_pk_bf16_f32 v233, v170, v171\n\t" \
  "v_mfma_f32_16x16x32_bf16 v[164:167], v[78:81], v[196:199], 0\n\t" \
  "v_cvt_pk_bf16_f32 v234, v172, v173\n\t" \
  "v_cvt_pk_bf16_f32 v235, v174, v175\n\t" \
  "global_store_dwordx4 v1, v[232:235], %14 offset:-4096 sc1\n\t" \
  "v_fma_f32 v184, v184, %16, v100\n\t" \
  "v_fma_f32 v185, v185, %16, v100\n\t" \
  "v_fma_f32 v186, v186, %16, v100\n\t" \
  "v_fma_f32 v187, v187, %16, v100\n\t" \
  "v_exp_f32 v184, v184\n\t" \
  "v_exp_f32 v185, v185\n\t" \
  "v_exp_f32 v186, v186\n\t" \
  "v_exp_f32 v187, v187\n\t" \
  "v_cvt_pk_bf16_f32 v188, v184, v185\n\t" \
  "v_cvt_pk_bf16_f32 v189, v186, v187\n\t" \
  "v_bfi_b32 v190, v46, 0, v188\n\t" \
  "v_bfi_b32 v191, v47, 0, v189\n\t" \
  "v_rcp_f32 v102, v192\n\t" \
  "v_and_b32 v188, v46, v188\n\t" \
  "v_and_b32 v189, v47, v189\n\t" \
  "v_pk_mul_f32 v[160:161], v[160:161], v[102:103] op_sel_hi:[1,0]\n\t" \
  "v_pk_mul_f32 v[162:163], v[162:163], v[102:103] op_sel_hi:[1,0]\n\t" \
  "v_mfma_f32_16x16x32_bf16 v[184:187], v[48:51], v[188:191], 0\n\t" \
  "v_pk_mul_f32 v[164:165], v[164:165], v[102:103] op_sel_hi:[1,0]\n\t" \
  "v_pk_mul_f32 v[166:167], v[166:167], v[102:103] op_sel_hi:[1,0]\n\t" \
  "v_mfma_f32_16x16x32_bf16 v[152:155], v[70:73], v[188:191], 0\n\t" \
  "v_cvt_pk_bf16_f32 v224, v160, v161\n\t" \
  "v_cvt_pk_bf16_f32 v225, v162, v163\n\t" \
  "v_mfma_f32_16x16x32_bf16 v[156:159], v[80:83], v[188:191], 0\n\t" \
  "v_cvt_pk_bf16_f32 v226, v164, v165\n\t" \
  "v_cvt_pk_bf16_f32 v227, v166, v167\n\t" \
  "global_store_dwordx4 v4, v[224:227], %14 offset:0 sc1\n\t" \
  "v_rcp_f32 v100, v184\n\t" \
  "ds_read_b128 v[60:63], v56 offset:64\n\t" \
  "ds_read_b64_tr_b16 v[108:109], v5 offset:0\n\t" \
  "ds_read_b64_tr_b16 v[110:111], v5 offset:16\n\t" \
  "ds_read_b128 v[112:115], v6 offset:16384\n\t" \
  "ds_read_b128 v[116:119], v6 offset:15360\n\t" \
  "ds_read_b64_tr_b16 v[120:121], v5 offset:512\n\t" \
  "ds_read_b64_tr_b16 v[122:123], v5 offset:528\n\t" \
  "ds_read_b128 v[124:127], v6 offset:14336\n\t" \
  "ds_read_b64_tr_b16 v[128:129], v5 offset:1024\n\t" \
  "ds_read_b64_tr_b16 v[130:131], v5 offset:1040\n\t" \
  "ds_read_b128 v[132:135], v6 offset:13312\n\t" \
  "ds_read_b64_tr_b16 v[136:137], v5 offset:1536\n\t" \
  "ds_read_b64_tr_b16 v[138:139], v5 offset:1552\n\t" \
  "ds_read_b128 v[140:143], v6 offset:12288\n\t" \
  "ds_read_b64_tr_b16 v[144:145], v5 offset:2048\n\t" \
  "v_pk_mul_f32 v[152:153], v[152:153], v[100:101] op_sel_hi:[1,0]\n\t" \
  "v_pk_mul_f32 v[154:155], v[154:155], v[100:101] op_sel_hi:[1,0]\n\t" \
  "v_cvt_pk_bf16_f32 v216, v152, v153\n\t" \
  "v_pk_mul_f32 v[156:157], v[156:157], v[100:101] op_sel_hi:[1,0]\n\t" \
  "v_pk_mul_f32 v[158:159], v[158:159], v[100:101] op_sel_hi:[1,0]\n\t" \
  "v_cvt_pk_bf16_f32 v217, v154, v155\n\t" \
  "v_cvt_pk_bf16_f32 v218, v156, v157\n\t" \
  "v_cvt_pk_bf16_f32 v219, v158, v159\n\t" \
  "global_store_dwordx4 v4, v[216:219], %14 offset:-4096 sc1\n\t" \
  "ds_write_b32 v32, v0 offset:112\n\t" \
  "s_branch 9f\n\t" \
  "5:\n\t" \
  "ds_read_b32 v52, v33 offset:48\n\t" \
  "ds_read_b64 v[72:73], v31\n\t" \
  "ds_read_b64 v[82:83], v31 offset:8\n\t" \
  "s_waitcnt lgkmcnt(0)\n\t" \
  "v_readfirstlane_b32 vcc_lo, v52\n\t" \
  "s_nop 1\n\t" \
  "s_cmp_eq_u32 vcc_lo, 0\n\t" \
  "s_cbranch_scc1 5b\n\t" \
  "s_branch 4b\n\t" \
  "15:\n\t" \
  "ds_read_b128 v[10:13], v14 offset:128\n\t" \
  "s_waitcnt lgkmcnt(0)\n\t" \
  "v_and_b32 v10, v10, v11\n\t" \
  "v_and_b32 v12, v12, v13\n\t" \
  "v_and_b32 v10, v10, v12\n\t" \
  "s_nop 1\n\t" \
  "v_readfirstlane_b32 vcc_lo, v10\n\t" \
  "s_nop 1\n\t" \
  "s_cmp_eq_u32 vcc_lo, 0\n\t" \
  "s_cbranch_scc1 15b\n\t" \
  "s_branch 14b\n\t" \
  "9:\n\t" \
  "506:\n\t" \
  "s_cbranch_execz 510f\n\t" \
  "s_waitcnt lgkmcnt(14)\n\t" \
  "v_and_b32 v60, v60, v61\n\t" \
  "v_and_b32 v62, v62, v63\n\t" \
  "507:\n\t" \
  "s_cbranch_execz 26b\n\t" \
  "v_mov_b32 v12, %13\n\t" \
  "v_and_b32 v60, v60, v62\n\t" \
  "s_nop 0\n\t" \
  "v_readfirstlane_b32 vcc_lo, v60\n\t" \
  "s_nop 1\n\t" \
  "s_cmp_eq_u32 vcc_lo, 0\n\t" \
  "s_cbranch_scc1 7f\n\t" \
  "6:\n\t" \
  "s_waitcnt lgkmcnt(11)\n\t" \
  "v_mfma_f32_16x16x32_bf16 v[192:195], v[108:111], v[112:115], 0\n\t" \
  "ds_read_b64_tr_b16 v[146:147], v5 offset:2064\n\t" \
  "ds_read_b128 v[148:151], v6 offset:11264\n\t" \
  "s_waitcnt lgkmcnt(12)\n\t" \
  "v_mfma_f32_16x16x32_bf16 v[196:199], v[108:111], v[116:119], 0\n\t" \
  "ds_read_b64_tr_b16 v[64:65], v5 offset:2560\n\t" \
  "ds_read_b64_tr_b16 v[66:67], v5 offset:2576\n\t" \
  "s_waitcnt lgkmcnt(12)\n\t" \
  "v_mfma_f32_16x16x32_bf16 v[196:199], v[120:123], v[112:115], v[196:199]\n\t" \
  "ds_read_b128 v[68:71], v6 offset:10240\n\t" \
  "ds_read_b64_tr_b16 v[72:73], v5 offset:3072\n\t" \
  "s_waitcnt lgkmcnt(13)\n\t" \
  "v_mfma_f32_16x16x32_bf16 v[200:203], v[108:111], v[124:127], 0\n\t" \
  "ds_read_b64_tr_b16 v[74:75], v5 offset:3088\n\t" \
  "ds_read_b128 v[76:79], v6 offset:9216\n\t" \
  "v_mfma_f32_16x16x32_bf16 v[200:203], v[120:123], v[116:119], v[200:203]\n\t" \
  "ds_read_b64_tr_b16 v[80:81], v5 offset:3584\n\t" \
  "ds_read_b64_tr_b16 v[82:83], v5 offset:3600\n\t" \
  "s_waitcnt lgkmcnt(15)\n\t" \
  "v_mfma_f32_16x16x32_bf16 v[200:203], v[128:131], v[112:115], v[200:203]\n\t" \
  "ds_read_b128 v[84:87], v6 offset:0\n\t" \
  "ds_read_b128 v[88:91], v6 offset:1024\n\t" \
  "s_waitcnt lgkmcnt(15)\n\t" \
  "v_mfma_f32_16x16x32_bf16 v[204:207], v[108:111], v[132:135], 0\n\t" \
  "ds_read_b128 v[92:95], v6 offset:2048\n\t" \
  "ds_read_b128 v[96:99], v6 offset:3072\n\t" \
  "v_mfma_f32_16x16x32_bf16 v[204:207], v[120:123], v[124:127], v[204:207]\n\t" \
  "ds_read_b128 v[100:103], v6 offset:4096\n\t" \
  "ds_read_b128 v[104:107], v6 offset:5120\n\t" \
  "v_mfma_f32_16x16x32_bf16 v[204:207], v[128:131], v[116:119], v[204:207]\n\t" \
  "ds_read_b128 v[152:155], v6 offset:6144\n\t" \
  "ds_read_b128 v[156:159], v6 offset:7168\n\t" \
  "s_waitcnt lgkmcnt(14)\n\t" \
  "v_mfma_f32_16x16x32_bf16 v[204:207], v[136:139], v[112:115], v[204:207]\n\t" \
  "v_lshl_add_u32 v12, v12, 12, v4\n\t" \
  "s_lshl_b32 s88, %13, 5\n\t" \
  "v_mfma_f32_16x16x32_bf16 v[208:211], v[108:111], v[140:143], 0\n\t" \
  "v_mfma_f32_16x16x32_bf16 v[208:211], v[120:123], v[132:135], v[208:211]\n\t" \
  "v_mfma_f32_16x16x32_bf16 v[208:211], v[128:131], v[124:127], v[208:211]\n\t" \
  "s_and_b32 s88, s88, 64\n\t" \
  "v_xor_b32 v13, s88, v54\n\t" \
  "v_mfma_f32_16x16x32_bf16 v[208:211], v[136:139], v[116:119], v[208:211]\n\t" \
  "v_mfma_f32_16x16x32_bf16 v[208:211], v[144:147], v[112:115], v[208:211]\n\t" \
  "v_mfma_f32_16x16x32_bf16 v[212:215], v[108:111], v[148:151], 0\n\t" \
  "ds_read_b32 v55, v56 offset:104\n\t" \
  "v_mfma_f32_16x16x32_bf16 v[212:215], v[120:123], v[140:143], v[212:215]\n\t" \
  "v_mfma_f32_16x16x32_bf16 v[212:215], v[128:131], v[132:135], v[212:215]\n\t" \
  "v_mfma_f32_16x16x32_bf16 v[212:215], v[136:139], v[124:127], v[212:215]\n\t" \
  "v_mfma_f32_16x16x32_bf16 v[212:215], v[144:147], v[116:119], v[212:215]\n\t" \
  "v_mfma_f32_16x16x32_bf16 v[212:215], v[64:67], v[112:115], v[212:215]\n\t" \
  "s_waitcnt lgkmcnt(1)\n\t" \
  "v_mfma_f32_16x16x32_bf16 v[216:219], v[108:111], v[68:71], 0\n\t" \
  "v_mfma_f32_16x16x32_bf16 v[216:219], v[120:123], v[148:151], v[216:219]\n\t" \
  "v_mfma_f32_16x16x32_bf16 v[216:219], v[128:131], v[140:143], v[216:219]\n\t" \
  "v_mfma_f32_16x16x32_bf16 v[216:219], v[136:139], v[132:135], v[216:219]\n\t" \
  "v_mfma_f32_16x16x32_bf16 v[216:219], v[144:147], v[124:127], v[216:219]\n\t" \
  "v_mfma_f32_16x16x32_bf16 v[216:219], v[64:67], v[116:119], v[216:219]\n\t" \
  "v_mfma_f32_16x16x32_bf16 v[216:219], v[72:75], v[112:115], v[216:219]\n\t" \
  "v_mfma_f32_16x16x32_bf16 v[220:223], v[108:111], v[76:79], 0\n\t" \
  "v_mfma_f32_16x16x32_bf16 v[220:223], v[120:123], v[68:71], v[220:223]\n\t" \
  "v_mfma_f32_16x16x32_bf16 v[220:223], v[128:131], v[148:151], v[220:223]\n\t" \
  "v_mfma_f32_16x16x32_bf16 v[220:223], v[136:139], v[140:143], v[220:223]\n\t" \
  "v_mfma_f32_16x16x32_bf16 v[220:223], v[144:147], v[132:135], v[220:223]\n\t" \
  "ds_read_b64_tr_b16 v[160:161], v5 offset:128\n\t" \
  "v_mfma_f32_16x16x32_bf16 v[220:223], v[64:67], v[124:127], v[220:223]\n\t" \
  "ds_read_b64_tr_b16 v[162:163], v5 offset:144\n\t" \
  "v_mfma_f32_16x16x32_bf16 v[220:223], v[72:75], v[116:119], v[220:223]\n\t" \
  "ds_read_b128 v[164:167], v7 offset:16384\n\t" \
  "v_mfma_f32_16x16x32_bf16 v[220:223], v[80:83], v[112:115], v[220:223]\n\t" \
  "ds_read_b128 v[168:171], v7 offset:15360\n\t" \
  "v_mfma_f32_16x16x32_bf16 v[252:255], v[108:111], v[84:87], 0\n\t" \
  "ds_read_b64_tr_b16 v[172:173], v5 offset:640\n\t" \
  "v_mfma_f32_16x16x32_bf16 v[252:255], v[120:123], v[88:91], v[252:255]\n\t" \
  "ds_read_b64_tr_b16 v[174:175], v5 offset:656\n\t" \
  "v_mfma_f32_16x16x32_bf16 v[252:255], v[128:131], v[92:95], v[252:255]\n\t" \
  "ds_read_b128 v[176:179], v7 offset:14336\n\t" \
  "v_mfma_f32_16x16x32_bf16 v[252:255], v[136:139], v[96:99], v[252:255]\n\t" \
  "ds_read_b64_tr_b16 v[180:181], v5 offset:1152\n\t" \
  "v_mfma_f32_16x16x32_bf16 v[252:255], v[144:147], v[100:103], v[252:255]\n\t" \
  "ds_read_b64_tr_b16 v[182:183], v5 offset:1168\n\t" \
  "v_mfma_f32_16x16x32_bf16 v[252:255], v[64:67], v[104:107], v[252:255]\n\t" \
  "ds_read_b128 v[184:187], v7 offset:13312\n\t" \
  "v_mfma_f32_16x16x32_bf16 v[252:255], v[72:75], v[152:155], v[252:255]\n\t" \
  "ds_read_b64_tr_b16 v[188:189], v5 offset:1664\n\t" \
  "v_mfma_f32_16x16x32_bf16 v[252:255], v[80:83], v[156:159], v[252:255]\n\t" \
  "ds_read_b64_tr_b16 v[190:191], v5 offset:1680\n\t" \
  "v_mfma_f32_16x16x32_bf16 v[248:251], v[108:111], v[88:91], 0\n\t" \
  "ds_read_b128 v[76:79], v7 offset:12288\n\t" \
  "v_mfma_f32_16x16x32_bf16 v[248:251], v[120:123], v[92:95], v[248:251]\n\t" \
  "v_mfma_f32_16x16x32_bf16 v[248:251], v[128:131], v[96:99], v[248:251]\n\t" \
  "ds_read_b64_tr_b16 v[68:69], v5 offset:2176\n\t" \
  "v_mfma_f32_16x16x32_bf16 v[248:251], v[136:139], v[100:103], v[248:251]\n\t" \
  "ds_read_b64_tr_b16 v[70:71], v5 offset:2192\n\t" \
  "v_mfma_f32_16x16x32_bf16 v[248:251], v[144:147], v[104:107], v[248:251]\n\t" \
  "ds_read_b128 v[148:151], v7 offset:11264\n\t" \
  "v_mfma_f32_16x16x32_bf16 v[248:251], v[64:67], v[152:155], v[248:251]\n\t" \
  "v_mfma_f32_16x16x32_bf16 v[248:251], v[72:75], v[156:159], v[248:251]\n\t" \
  "v_mfma_f32_16x16x32_bf16 v[244:247], v[108:111], v[92:95], 0\n\t" \
  "v_mfma_f32_16x16x32_bf16 v[244:247], v[120:123], v[96:99], v[244:247]\n\t" \
  "ds_read_b64_tr_b16 v[140:141], v5 offset:2688\n\t" \
  "v_mfma_f32_16x16x32_bf16 v[244:247], v[128:131], v[100:103], v[244:247]\n\t" \
  "ds_read_b64_tr_b16 v[142:143], v5 offset:2704\n\t" \
  "v_mfma_f32_16x16x32_bf16 v[244:247], v[136:139], v[104:107], v[244:247]\n\t" \
  "ds_read_b128 v[132:135], v7 offset:10240\n\t" \
  "v_mfma_f32_16x16x32_bf16 v[244:247], v[144:147], v[152:155], v[244:247]\n\t" \
  "v_mfma_f32_16x16x32_bf16 v[244:247], v[64:67], v[156:159], v[244:247]\n\t" \
  "v_mfma_f32_16x16x32_bf16 v[240:243], v[108:111], v[96:99], 0\n\t" \
  "v_mfma_f32_16x16x32_bf16 v[240:243], v[120:123], v[100:103], v[240:243]\n\t" \
  "v_mov_b32 v11, 1\n\t" \
  "v_mfma_f32_16x16x32_bf16 v[240:243], v[128:131], v[104:107], v[240:243]\n\t" \
  "ds_read_b64_tr_b16 v[124:125], v5 offset:3200\n\t" \
  "v_mfma_f32_16x16x32_bf16 v[240:243], v[136:139], v[152:155], v[240:243]\n\t" \
  "s_waitcnt vmcnt(0)\n\t" \
  "s_mov_b64 exec, 1\n\t" \
  "ds_add_rtn_u32 v10, v56, v11 offset:16\n\t" \
  "s_mov_b64 exec, -1\n\t" \
  "v_mfma_f32_16x16x32_bf16 v[240:243], v[144:147], v[156:159], v[240:243]\n\t" \
  "ds_read_b64_tr_b16 v[126:127], v5 offset:3216\n\t" \
  "v_mfma_f32_16x16x32_bf16 v[236:239], v[108:111], v[100:103], 0\n\t" \
  "ds_read_b128 v[116:119], v7 offset:9216\n\t" \
  "v_mfma_f32_16x16x32_bf16 v[236:239], v[120:123], v[104:107], v[236:239]\n\t" \
  "v_bfe_u32 v11, v12, 4, 4\n\t" \
  "v_mfma_f32_16x16x32_bf16 v[236:239], v[128:131], v[152:155], v[236:239]\n\t" \
  "v_lshlrev_b32 v11, 7, v11\n\t" \
  "v_mfma_f32_16x16x32_bf16 v[236:239], v[136:139], v[156:159], v[236:239]\n\t" \
  "v_mfma_f32_16x16x32_bf16 v[232:235], v[108:111], v[104:107], 0\n\t" \
  "v_mfma_f32_16x16x32_bf16 v[232:235], v[120:123], v[152:155], v[232:235]\n\t" \
  "s_waitcnt lgkmcnt(2)\n\t" \
  "v_readfirstlane_b32 vcc_lo, v10\n\t" \
  "v_mfma_f32_16x16x32_bf16 v[232:235], v[128:131], v[156:159], v[232:235]\n\t" \
  "s_cmp_eq_u32 vcc_lo, 3\n\t" \
  "s_cbranch_scc0 23f\n\t" \
  "s_mov_b64 exec, 0xffff\n\t" \
  "global_store_dword v11, v55, %19 sc1\n\t" \
  "s_mov_b64 exec, -1\n\t" \
  "23:\n\t" \
  "v_mfma_f32_16x16x32_bf16 v[228:231], v[108:111], v[152:155], 0\n\t" \
  "ds_read_b64_tr_b16 v[64:65], v5 offset:3712\n\t" \
  "v_mfma_f32_16x16x32_bf16 v[228:231], v[120:123], v[156:159], v[228:231]\n\t" \
  "ds_read_b64_tr_b16 v[66:67], v5 offset:3728\n\t" \
  "v_mfma_f32_16x16x32_bf16 v[224:227], v[108:111], v[156:159], 0\n\t" \
  "ds_read_b128 v[72:75], v7 offset:0\n\t" \
  "v_mfma_f32_16x16x32_bf16 v[192:195], v[160:163], v[164:167], v[192:195]\n\t" \
  "ds_read_b128 v[80:83], v7 offset:1024\n\t" \
  "v_mfma_f32_16x16x32_bf16 v[196:199], v[160:163], v[168:171], v[196:199]\n\t" \
  "ds_read_b128 v[84:87], v7 offset:2048\n\t" \
  "v_mfma_f32_16x16x32_bf16 v[196:199], v[172:175], v[164:167], v[196:199]\n\t" \
  "ds_read_b128 v[88:91], v7 offset:3072\n\t" \
  "v_mfma_f32_16x16x32_bf16 v[200:203], v[160:163], v[176:179], v[200:203]\n\t" \
  "ds_read_b128 v[92:95], v7 offset:4096\n\t" \
  "v_mfma_f32_16x16x32_bf16 v[200:203], v[172:175], v[168:171], v[200:203]\n\t" \
  "ds_read_b128 v[108:111], v7 offset:5120\n\t" \
  "v_mfma_f32_16x16x32_bf16 v[200:203], v[180:183], v[164:167], v[200:203]\n\t" \
  "ds_read_b128 v[96:99], v7 offset:6144\n\t" \
  "v_mfma_f32_16x16x32_bf16 v[204:207], v[160:163], v[184:187], v[204:207]\n\t" \
  "ds_read_b128 v[100:103], v7 offset:7168\n\t" \
  "v_mfma_f32_16x16x32_bf16 v[204:207], v[172:175], v[176:179], v[204:207]\n\t" \
  "v_mfma_f32_16x16x32_bf16 v[204:207], v[180:183], v[168:171], v[204:207]\n\t" \
  "v_mfma_f32_16x16x32_bf16 v[204:207], v[188:191], v[164:167], v[204:207]\n\t" \
  "v_mfma_f32_16x16x32_bf16 v[208:211], v[160:163], v[76:79], v[208:211]\n\t" \
  "v_mfma_f32_16x16x32_bf16 v[208:211], v[172:175], v[184:187], v[208:211]\n\t" \
  "v_mfma_f32_16x16x32_bf16 v[208:211], v[180:183], v[176:179], v[208:211]\n\t" \
  "v_mfma_f32_16x16x32_bf16 v[208:211], v[188:191], v[168:171], v[208:211]\n\t" \
  "v_mfma_f32_16x16x32_bf16 v[208:211], v[68:71], v[164:167], v[208:211]\n\t" \
  "s_getpc_b64 s[70:71]\n\t" \
  "24:\n\t" \
  "s_add_u32 s70, s70, 25f-24b\n\t" \
  "s_addc_u32 s71, s71, 0\n\t" \
  "v_mfma_f32_16x16x32_bf16 v[212:215], v[160:163], v[148:151], v[212:215]\n\t" \
  "v_mfma_f32_16x16x32_bf16 v[212:215], v[172:175], v[76:79], v[212:215]\n\t" \
  "v_mfma_f32_16x16x32_bf16 v[212:215], v[180:183], v[184:187], v[212:215]\n\t" \
  "v_mfma_f32_16x16x32_bf16 v[212:215], v[188:191], v[176:179], v[212:215]\n\t" \
  "v_lshrrev_b32 v14, 1, v4\n\t" \
  "v_mfma_f32_16x16x32_bf16 v[212:215], v[68:71], v[168:171], v[212:215]\n\t" \
  "v_mfma_f32_16x16x32_bf16 v[212:215], v[140:143], v[164:167], v[212:215]\n\t" \
  "v_lshrrev_b32 v15, 24, v3\n\t" \
  "v_mfma_f32_16x16x32_bf16 v[216:219], v[160:163], v[132:135], v[216:219]\n\t" \
  "v_mfma_f32_16x16x32_bf16 v[216:219], v[172:175], v[148:151], v[216:219]\n\t" \
  "v_add_u32 v14, %20, v14\n\t" \
  "v_mfma_f32_16x16x32_bf16 v[216:219], v[180:183], v[76:79], v[216:219]\n\t" \
  "v_mfma_f32_16x16x32_bf16 v[216:219], v[188:191], v[184:187], v[216:219]\n\t" \
  "v_lshrrev_b32 v18, 10, v12\n\t" \
  "v_mfma_f32_16x16x32_bf16 v[216:219], v[68:71], v[176:179], v[216:219]\n\t" \
  "v_mfma_f32_16x16x32_bf16 v[216:219], v[140:143], v[168:171], v[216:219]\n\t" \
  "v_and_b32 v18, 12, v18\n\t" \
  "s_waitcnt lgkmcnt(0)\n\t" \
  "v_mfma_f32_16x16x32_bf16 v[216:219], v[124:127], v[164:167], v[216:219]\n\t" \
  "v_mfma_f32_16x16x32_bf16 v[220:223], v[160:163], v[116:119], v[220:223]\n\t" \
  "v_mov_b32 v17, 16\n\t" \
  "v_mfma_f32_16x16x32_bf16 v[220:223], v[172:175], v[132:135], v[220:223]\n\t" \
  "v_mfma_f32_16x16x32_bf16 v[220:223], v[180:183], v[148:151], v[220:223]\n\t" \
  "v_add_u32 v18, v56, v18\n\t" \
  "v_mfma_f32_16x16x32_bf16 v[220:223], v[188:191], v[76:79], v[220:223]\n\t" \
  "v_mfma_f32_16x16x32_bf16 v[220:223], v[68:71], v[184:187], v[220:223]\n\t" \
  "s_mov_b32 s76, 0\n\t" \
  "ds_read_b64_tr_b16 v[76:77], v5 offset:256\n\t" \
  "v_mfma_f32_16x16x32_bf16 v[220:223], v[140:143], v[176:179], v[220:223]\n\t" \
  "ds_read_b64_tr_b16 v[78:79], v5 offset:272\n\t" \
  "v_mfma_f32_16x16x32_bf16 v[220:223], v[124:127], v[168:171], v[220:223]\n\t" \
  "s_brev_b32 s77, 1\n\t" \
  "ds_read_b128 v[104:107], v8 offset:16384\n\t" \
  "v_mfma_f32_16x16x32_bf16 v[220:223], v[64:67], v[164:167], v[220:223]\n\t" \
  "ds_read_b128 v[112:115], v8 offset:15360\n\t" \
  "v_mfma_f32_16x16x32_bf16 v[252:255], v[160:163], v[72:75], v[252:255]\n\t" \
  "s_mov_b32 s75, 0\n\t" \
  "ds_read_b64_tr_b16 v[120:121], v5 offset:768\n\t" \
  "v_mfma_f32_16x16x32_bf16 v[252:255], v[172:175], v[80:83], v[252:255]\n\t" \
  "ds_read_b64_tr_b16 v[122:123], v5 offset:784\n\t" \
  "v_mfma_f32_16x16x32_bf16 v[252:255], v[180:183], v[84:87], v[252:255]\n\t" \
  "v_add_u32 v28, 22528, v13\n\t" \
  "ds_read_b128 v[116:119], v8 offset:14336\n\t" \
  "v_mfma_f32_16x16x32_bf16 v[252:255], v[188:191], v[88:91], v[252:255]\n\t" \
  "ds_read_b64_tr_b16 v[72:73], v5 offset:1280\n\t" \
  "v_mfma_f32_16x16x32_bf16 v[252:255], v[68:71], v[92:95], v[252:255]\n\t" \
  "v_xor_b32 v29, 32, v28\n\t" \
  "ds_read_b64_tr_b16 v[74:75], v5 offset:1296\n\t" \
  "v_mfma_f32_16x16x32_bf16 v[252:255], v[140:143], v[108:111], v[252:255]\n\t" \
  "ds_read_b128 v[128:131], v8 offset:13312\n\t" \
  "v_mfma_f32_16x16x32_bf16 v[252:255], v[124:127], v[96:99], v[252:255]\n\t" \
  "v_xor_b32 v30, 64, v28\n\t" \
  "ds_read_b64_tr_b16 v[136:137], v5 offset:1792\n\t" \
  "v_mfma_f32_16x16x32_bf16 v[252:255], v[64:67], v[100:103], v[252:255]\n\t" \
  "ds_read_b64_tr_b16 v[138:139], v5 offset:1808\n\t" \
  "v_mfma_f32_16x16x32_bf16 v[248:251], v[160:163], v[80:83], v[248:251]\n\t" \
  "v_xor_b32 v31, 96, v28\n\t" \
  "ds_read_b128 v[144:147], v8 offset:12288\n\t" \
  "v_mfma_f32_16x16x32_bf16 v[248:251], v[172:175], v[84:87], v[248:251]\n\t" \
  "v_mfma_f32_16x16x32_bf16 v[248:251], v[180:183], v[88:91], v[248:251]\n\t" \
  "v_add_u32 v32, 88064, v13\n\t" \
  "ds_read_b64_tr_b16 v[64:65], v5 offset:2304\n\t" \
  "v_mfma_f32_16x16x32_bf16 v[248:251], v[188:191], v[92:95], v[248:251]\n\t" \
  "ds_read_b64_tr_b16 v[66:67], v5 offset:2320\n\t" \
  "v_mfma_f32_16x16x32_bf16 v[248:251], v[68:71], v[108:111], v[248:251]\n\t" \
  "v_xor_b32 v33, 32, v32\n\t" \
  "ds_read_b128 v[80:83], v8 offset:11264\n\t" \
  "v_mfma_f32_16x16x32_bf16 v[248:251], v[140:143], v[96:99], v[248:251]\n\t" \
  "v_mfma_f32_16x16x32_bf16 v[248:251], v[124:127], v[100:103], v[248:251]\n\t" \
  "v_xor_b32 v34, 64, v32\n\t" \
  "v_mfma_f32_16x16x32_bf16 v[244:247], v[160:163], v[84:87], v[244:247]\n\t" \
  "v_mfma_f32_16x16x32_bf16 v[244:247], v[172:175], v[88:91], v[244:247]\n\t" \
  "v_xor_b32 v35, 96, v32\n\t" \
  "ds_read_b64_tr_b16 v[84:85], v5 offset:2816\n\t" \
  "v_mfma_f32_16x16x32_bf16 v[244:247], v[180:183], v[92:95], v[244:247]\n\t" \
  "ds_read_b64_tr_b16 v[86:87], v5 offset:2832\n\t" \
  "v_mfma_f32_16x16x32_bf16 v[244:247], v[188:191], v[108:111], v[244:247]\n\t" \
  "v_add_u32 v36, 0, v13\n\t" \
  "ds_read_b128 v[124:127], v8 offset:10240\n\t" \
  "v_mfma_f32_16x16x32_bf16 v[244:247], v[68:71], v[96:99], v[244:247]\n\t" \
  "v_mfma_f32_16x16x32_bf16 v[244:247], v[140:143], v[100:103], v[244:247]\n\t" \
  "v_xor_b32 v37, 32, v36\n\t" \
  "v_mfma_f32_16x16x32_bf16 v[240:243], v[160:163], v[88:91], v[240:243]\n\t" \
  "v_mfma_f32_16x16x32_bf16 v[240:243], v[172:175], v[92:95], v[240:243]\n\t" \
  "v_xor_b32 v38, 64, v36\n\t" \
  "v_mfma_f32_16x16x32_bf16 v[240:243], v[180:183], v[108:111], v[240:243]\n\t" \
  "ds_read_b64_tr_b16 v[88:89], v5 offset:3328\n\t" \
  "v_mfma_f32_16x16x32_bf16 v[240:243], v[188:191], v[96:99], v[240:243]\n\t" \
  "v_xor_b32 v39, 96, v36\n\t" \
  "ds_read_b64_tr_b16 v[90:91], v5 offset:3344\n\t" \
  "v_mfma_f32_16x16x32_bf16 v[240:243], v[68:71], v[100:103], v[240:243]\n\t" \
  "ds_read_b128 v[152:155], v8 offset:9216\n\t" \
  "v_mfma_f32_16x16x32_bf16 v[236:239], v[160:163], v[92:95], v[236:239]\n\t" \
  "v_lshrrev_b32 v57, 5, v4\n\t" \
  "v_mfma_f32_16x16x32_bf16 v[236:239], v[172:175], v[108:111], v[236:239]\n\t" \
  "v_mfma_f32_16x16x32_bf16 v[236:239], v[180:183], v[96:99], v[236:239]\n\t" \
  "v_and_b32 v57, 0x18, v57\n\t" \
  "v_mfma_f32_16x16x32_bf16 v[236:239], v[188:191], v[100:103], v[236:239]\n\t" \
  "v_mfma_f32_16x16x32_bf16 v[232:235], v[160:163], v[108:111], v[232:235]\n\t" \
  "v_and_b32 v62, 0x70, v4\n\t" \
  "v_mfma_f32_16x16x32_bf16 v[232:235], v[172:175], v[96:99], v[232:235]\n\t" \
  "ds_read_b64_tr_b16 v[68:69], v5 offset:3840\n\t" \
  "v_mfma_f32_16x16x32_bf16 v[232:235], v[180:183], v[100:103], v[232:235]\n\t" \
  "v_xor_b32 v57, v57, v62\n\t" \
  "ds_read_b64_tr_b16 v[70:71], v5 offset:3856\n\t" \
  "v_mfma_f32_16x16x32_bf16 v[228:231], v[160:163], v[96:99], v[228:231]\n\t" \
  "ds_read_b128 v[92:95], v8 offset:0\n\t" \
  "v_mfma_f32_16x16x32_bf16 v[228:231], v[172:175], v[100:103], v[228:231]\n\t" \
  "v_and_b32 v58, 0xf0, v4\n\t" \
  "ds_read_b128 v[108:111], v8 offset:1024\n\t" \
  "v_mfma_f32_16x16x32_bf16 v[224:227], v[160:163], v[100:103], v[224:227]\n\t" \
  "ds_read_b128 v[96:99], v8 offset:2048\n\t" \
  "s_waitcnt lgkmcnt(5)\n\t" \
  "v_mfma_f32_16x16x32_bf16 v[192:195], v[76:79], v[104:107], v[192:195]\n\t" \
  "v_lshl_add_u32 v57, v58, 3, v57\n\t" \
  "ds_read_b128 v[100:103], v8 offset:3072\n\t" \
  "v_mfma_f32_16x16x32_bf16 v[196:199], v[76:79], v[112:115], v[196:199]\n\t" \
  "ds_read_b128 v[156:159], v8 offset:4096\n\t" \
  "v_mfma_f32_16x16x32_bf16 v[196:199], v[120:123], v[104:107], v[196:199]\n\t" \
  "v_add_u32 v57, %23, v57\n\t" \
  "ds_read_b128 v[160:163], v8 offset:5120\n\t" \
  "v_mfma_f32_16x16x32_bf16 v[200:203], v[76:79], v[116:119], v[200:203]\n\t" \
  "ds_read_b128 v[164:167], v8 offset:6144\n\t" \
  "v_mfma_f32_16x16x32_bf16 v[200:203], v[120:123], v[112:115], v[200:203]\n\t" \
  "v_xor_b32 v58, 32, v57\n\t" \
  "ds_read_b128 v[168:171], v8 offset:7168\n\t" \
  "v_mfma_f32_16x16x32_bf16 v[200:203], v[72:75], v[104:107], v[200:203]\n\t" \
  "v_mfma_f32_16x16x32_bf16 v[204:207], v[76:79], v[128:131], v[204:207]\n\t" \
  "v_xor_b32 v59, 64, v57\n\t" \
  "v_mfma_f32_16x16x32_bf16 v[204:207], v[120:123], v[116:119], v[204:207]\n\t" \
  "v_mfma_f32_16x16x32_bf16 v[204:207], v[72:75], v[112:115], v[204:207]\n\t" \
  "v_xor_b32 v60, 96, v57\n\t" \
  "v_mfma_f32_16x16x32_bf16 v[204:207], v[136:139], v[104:107], v[204:207]\n\t" \
  "v_mfma_f32_16x16x32_bf16 v[208:211], v[76:79], v[144:147], v[208:211]\n\t" \
  "v_lshrrev_b32 v61, 3, v4\n\t" \
  "v_mfma_f32_16x16x32_bf16 v[208:211], v[120:123], v[128:131], v[208:211]\n\t" \
  "v_mfma_f32_16x16x32_bf16 v[208:211], v[72:75], v[116:119], v[208:211]\n\t" \
  "v_and_b32 v61, 0x70, v61\n\t" \
  "v_mfma_f32_16x16x32_bf16 v[208:211], v[136:139], v[112:115], v[208:211]\n\t" \
  "v_mfma_f32_16x16x32_bf16 v[208:211], v[64:67], v[104:107], v[208:211]\n\t" \
  "v_xor_b32 v61, v61, v4\n\t" \
  "v_mfma_f32_16x16x32_bf16 v[212:215], v[76:79], v[80:83], v[212:215]\n\t" \
  "v_mfma_f32_16x16x32_bf16 v[212:215], v[120:123], v[144:147], v[212:215]\n\t" \
  "v_add_u32 v61, %23, v61\n\t" \
  "v_mfma_f32_16x16x32_bf16 v[212:215], v[72:75], v[128:131], v[212:215]\n\t" \
  "v_mfma_f32_16x16x32_bf16 v[212:215], v[136:139], v[116:119], v[212:215]\n\t" \
  "v_and_b32 v63, 0x380, v4\n\t" \
  "v_mfma_f32_16x16x32_bf16 v[212:215], v[64:67], v[112:115], v[212:215]\n\t" \
  "v_mfma_f32_16x16x32_bf16 v[212:215], v[84:87], v[104:107], v[212:215]\n\t" \
  "v_lshl_add_u32 v62, v63, 6, v62\n\t" \
  "v_mfma_f32_16x16x32_bf16 v[216:219], v[76:79], v[124:127], v[216:219]\n\t" \
  "v_mfma_f32_16x16x32_bf16 v[216:219], v[120:123], v[80:83], v[216:219]\n\t" \
  "v_add_u32 v63, 0x10000, v62\n\t" \
  "v_mfma_f32_16x16x32_bf16 v[216:219], v[72:75], v[144:147], v[216:219]\n\t" \
  "v_mfma_f32_16x16x32_bf16 v[216:219], v[136:139], v[128:131], v[216:219]\n\t" \
  "v_mfma_f32_16x16x32_bf16 v[216:219], v[64:67], v[116:119], v[216:219]\n\t" \
  "v_mfma_f32_16x16x32_bf16 v[216:219], v[84:87], v[112:115], v[216:219]\n\t" \
  "v_mfma_f32_16x16x32_bf16 v[216:219], v[88:91], v[104:107], v[216:219]\n\t" \
  "v_mfma_f32_16x16x32_bf16 v[220:223], v[76:79], v[152:155], v[220:223]\n\t" \
  "v_mfma_f32_16x16x32_bf16 v[220:223], v[120:123], v[124:127], v[220:223]\n\t" \
  "v_mfma_f32_16x16x32_bf16 v[220:223], v[72:75], v[80:83], v[220:223]\n\t" \
  "v_mfma_f32_16x16x32_bf16 v[220:223], v[136:139], v[144:147], v[220:223]\n\t" \
  "v_mfma_f32_16x16x32_bf16 v[220:223], v[64:67], v[128:131], v[220:223]\n\t" \
  "ds_read_b64_tr_b16 v[172:173], v5 offset:384\n\t" \
  "v_mfma_f32_16x16x32_bf16 v[220:223], v[84:87], v[116:119], v[220:223]\n\t" \
  "ds_read_b64_tr_b16 v[174:175], v5 offset:400\n\t" \
  "v_mfma_f32_16x16x32_bf16 v[220:223], v[88:91], v[112:115], v[220:223]\n\t" \
  "ds_read_b128 v[80:83], v9 offset:9216\n\t" \
  "s_waitcnt lgkmcnt(3)\n\t" \
  "v_mfma_f32_16x16x32_bf16 v[220:223], v[68:71], v[104:107], v[220:223]\n\t" \
  "ds_read_b64_tr_b16 v[176:177], v5 offset:896\n\t" \
  "v_mfma_f32_16x16x32_bf16 v[252:255], v[76:79], v[92:95], v[252:255]\n\t" \
  "ds_read_b64_tr_b16 v[178:179], v5 offset:912\n\t" \
  "v_mfma_f32_16x16x32_bf16 v[252:255], v[120:123], v[108:111], v[252:255]\n\t" \
  "ds_read_b128 v[116:119], v9 offset:10240\n\t" \
  "v_mfma_f32_16x16x32_bf16 v[252:255], v[72:75], v[96:99], v[252:255]\n\t" \
  "ds_read_b64_tr_b16 v[180:181], v5 offset:1408\n\t" \
  "v_mfma_f32_16x16x32_bf16 v[252:255], v[136:139], v[100:103], v[252:255]\n\t" \
  "ds_read_b64_tr_b16 v[182:183], v5 offset:1424\n\t" \
  "v_mfma_f32_16x16x32_bf16 v[252:255], v[64:67], v[156:159], v[252:255]\n\t" \
  "ds_read_b128 v[112:115], v9 offset:11264\n\t" \
  "v_mfma_f32_16x16x32_bf16 v[252:255], v[84:87], v[160:163], v[252:255]\n\t" \
  "ds_read_b64_tr_b16 v[184:185], v5 offset:1920\n\t" \
  "v_mfma_f32_16x16x32_bf16 v[252:255], v[88:91], v[164:167], v[252:255]\n\t" \
  "ds_read_b64_tr_b16 v[186:187], v5 offset:1936\n\t" \
  "v_mfma_f32_16x16x32_bf16 v[252:255], v[68:71], v[168:171], v[252:255]\n\t" \
  "ds_read_b128 v[104:107], v9 offset:12288\n\t" \
  "v_mfma_f32_16x16x32_bf16 v[248:251], v[76:79], v[108:111], v[248:251]\n\t" \
  "ds_read_b64_tr_b16 v[188:189], v5 offset:2432\n\t" \
  "v_mfma_f32_16x16x32_bf16 v[248:251], v[120:123], v[96:99], v[248:251]\n\t" \
  "ds_read_b64_tr_b16 v[190:191], v5 offset:2448\n\t" \
  "v_mfma_f32_16x16x32_bf16 v[248:251], v[72:75], v[100:103], v[248:251]\n\t" \
  "ds_read_b128 v[124:127], v9 offset:13312\n\t" \
  "v_mfma_f32_16x16x32_bf16 v[248:251], v[136:139], v[156:159], v[248:251]\n\t" \
  "ds_read_b64_tr_b16 v[148:149], v5 offset:2944\n\t" \
  "v_mfma_f32_16x16x32_bf16 v[248:251], v[64:67], v[160:163], v[248:251]\n\t" \
  "ds_read_b64_tr_b16 v[150:151], v5 offset:2960\n\t" \
  "v_mfma_f32_16x16x32_bf16 v[248:251], v[84:87], v[164:167], v[248:251]\n\t" \
  "ds_read_b128 v[140:143], v9 offset:14336\n\t" \
  "v_mfma_f32_16x16x32_bf16 v[248:251], v[88:91], v[168:171], v[248:251]\n\t" \
  "ds_read_b64_tr_b16 v[132:133], v5 offset:3456\n\t" \
  "v_mfma_f32_16x16x32_bf16 v[244:247], v[76:79], v[96:99], v[244:247]\n\t" \
  "ds_read_b64_tr_b16 v[134:135], v5 offset:3472\n\t" \
  "v_mfma_f32_16x16x32_bf16 v[244:247], v[120:123], v[100:103], v[244:247]\n\t" \
  "ds_read_b128 v[128:131], v9 offset:15360\n\t" \
  "v_mfma_f32_16x16x32_bf16 v[244:247], v[72:75], v[156:159], v[244:247]\n\t" \
  "ds_read_b64_tr_b16 v[144:145], v5 offset:3968\n\t" \
  "v_mfma_f32_16x16x32_bf16 v[244:247], v[136:139], v[160:163], v[244:247]\n\t" \
  "ds_read_b64_tr_b16 v[146:147], v5 offset:3984\n\t" \
  "v_mfma_f32_16x16x32_bf16 v[244:247], v[64:67], v[164:167], v[244:247]\n\t" \
  "ds_read_b128 v[152:155], v9 offset:16384\n\t" \
  "v_mfma_f32_16x16x32_bf16 v[244:247], v[84:87], v[168:171], v[244:247]\n\t" \
  "v_mfma_f32_16x16x32_bf16 v[240:243], v[76:79], v[100:103], v[240:243]\n\t" \
  "v_mfma_f32_16x16x32_bf16 v[240:243], v[120:123], v[156:159], v[240:243]\n\t" \
  "v_mfma_f32_16x16x32_bf16 v[240:243], v[72:75], v[160:163], v[240:243]\n\t" \
  "v_mfma_f32_16x16x32_bf16 v[240:243], v[136:139], v[164:167], v[240:243]\n\t" \
  "v_mfma_f32_16x16x32_bf16 v[240:243], v[64:67], v[168:171], v[240:243]\n\t" \
  "v_mfma_f32_16x16x32_bf16 v[236:239], v[76:79], v[156:159], v[236:239]\n\t" \
  "v_mfma_f32_16x16x32_bf16 v[236:239], v[120:123], v[160:163], v[236:239]\n\t" \
  "v_mfma_f32_16x16x32_bf16 v[236:239], v[72:75], v[164:167], v[236:239]\n\t" \
  "v_mfma_f32_16x16x32_bf16 v[236:239], v[136:139], v[168:171], v[236:239]\n\t" \
  "v_mfma_f32_16x16x32_bf16 v[232:235], v[76:79], v[160:163], v[232:235]\n\t" \
  "v_mfma_f32_16x16x32_bf16 v[232:235], v[120:123], v[164:167], v[232:235]\n\t" \
  "v_mfma_f32_16x16x32_bf16 v[232:235], v[72:75], v[168:171], v[232:235]\n\t" \
  "ds_read_b32 v16, v56 offset:96\n\t" \
  "ds_read_b128 v[136:139], v9 offset:7168\n\t" \
  "v_mfma_f32_16x16x32_bf16 v[228:231], v[76:79], v[164:167], v[228:231]\n\t" \
  "ds_read_b128 v[156:159], v9 offset:6144\n\t" \
  "v_mfma_f32_16x16x32_bf16 v[228:231], v[120:123], v[168:171], v[228:231]\n\t" \
  "v_mfma_f32_16x16x32_bf16 v[224:227], v[76:79], v[168:171], v[224:227]\n\t" \
  "ds_read_b128 v[160:163], v9 offset:5120\n\t" \
  "s_waitcnt lgkmcnt(4)\n\t" \
  "v_mfma_f32_16x16x32_bf16 v[220:223], v[172:175], v[80:83], v[220:223]\n\t" \
  "v_mfma_f32_16x16x32_bf16 v[220:223], v[176:179], v[116:119], v[220:223]\n\t" \
  "v_mfma_f32_16x16x32_bf16 v[220:223], v[180:183], v[112:115], v[220:223]\n\t" \
  "ds_read_b128 v[164:167], v9 offset:4096\n\t" \
  "v_mfma_f32_16x16x32_bf16 v[220:223], v[184:187], v[104:107], v[220:223]\n\t" \
  "s_waitcnt lgkmcnt(4)\n\t" \
  "v_readfirstlane_b32 vcc_lo, v16\n\t" \
  "v_mfma_f32_16x16x32_bf16 v[220:223], v[188:191], v[124:127], v[220:223]\n\t" \
  "s_cmp_eq_u32 vcc_lo, 0\n\t" \
  "s_cbranch_scc1 30f\n\t" \
  "s_lshr_b64 s[90:91], %18, s75\n\t" \
  "s_and_b32 s90, s90, 15\n\t" \
  "s_lshl_b32 s90, s90, 14\n\t" \
  "s_add_u32 s90, s90, s92\n\t" \
  "s_addc_u32 s91, s93, 0\n\t" \
  "s_add_u32 s75, s75, 4\n\t" \
  "s_swappc_b64 s[68:69], s[70:71]\n\t" \
  "30:\n\t" \
  "v_mfma_f32_16x16x32_bf16 v[220:223], v[148:151], v[140:143], v[220:223]\n\t" \
  "v_mfma_f32_16x16x32_bf16 v[220:223], v[132:135], v[128:131], v[220:223]\n\t" \
  "ds_read_b128 v[168:171], v9 offset:3072\n\t" \
  "v_mfma_f32_16x16x32_bf16 v[220:223], v[144:147], v[152:155], v[220:223]\n\t" \
  "v_mfma_f32_16x16x32_bf16 v[216:219], v[172:175], v[116:119], v[216:219]\n\t" \
  "v_mfma_f32_16x16x32_bf16 v[216:219], v[176:179], v[112:115], v[216:219]\n\t" \
  "v_mfma_f32_16x16x32_bf16 v[216:219], v[180:183], v[104:107], v[216:219]\n\t" \
  "ds_read_b32 v16, v56 offset:96\n\t" \
  "v_mfma_f32_16x16x32_bf16 v[216:219], v[184:187], v[124:127], v[216:219]\n\t" \
  "s_nop 2\n\t" \
  "v_add_f32 v253, v253, v220\n\t" \
  "v_add_f32 v254, v254, v221\n\t" \
  "v_mfma_f32_16x16x32_bf16 v[216:219], v[188:191], v[140:143], v[216:219]\n\t" \
  "v_add_f32 v255, v255, v222\n\t" \
  "v_mfma_f32_16x16x32_bf16 v[216:219], v[148:151], v[128:131], v[216:219]\n\t" \
  "v_mfma_f32_16x16x32_bf16 v[216:219], v[132:135], v[152:155], v[216:219]\n\t" \
  "v_mfma_f32_16x16x32_bf16 v[212:215], v[172:175], v[112:115], v[212:215]\n\t" \
  "v_mfma_f32_16x16x32_bf16 v[212:215], v[176:179], v[104:107], v[212:215]\n\t" \
  "v_mfma_f32_16x16x32_bf16 v[212:215], v[180:183], v[124:127], v[212:215]\n\t" \
  "s_waitcnt lgkmcnt(0)\n\t" \
  "v_readfirstlane_b32 vcc_lo, v16\n\t" \
  "v_mfma_f32_16x16x32_bf16 v[212:215], v[184:187], v[140:143], v[212:215]\n\t" \
  "s_cmp_eq_u32 vcc_lo, 0\n\t" \
  "s_cbranch_scc1 31f\n\t" \
  "s_lshr_b64 s[90:91], %18, s75\n\t" \
  "s_and_b32 s90, s90, 15\n\t" \
  "s_lshl_b32 s90, s90, 14\n\t" \
  "s_add_u32 s90, s90, s92\n\t" \
  "s_addc_u32 s91, s93, 0\n\t" \
  "s_add_u32 s75, s75, 4\n\t" \
  "s_swappc_b64 s[68:69], s[70:71]\n\t" \
  "31:\n\t" \
  "v_mfma_f32_16x16x32_bf16 v[212:215], v[188:191], v[128:131], v[212:215]\n\t" \
  "v_add_f32 v249, v249, v216\n\t" \
  "v_add_f32 v250, v250, v217\n\t" \
  "v_mfma_f32_16x16x32_bf16 v[212:215], v[148:151], v[152:155], v[212:215]\n\t" \
  "v_add_f32 v251, v251, v218\n\t" \
  "v_cndmask_b32 v27, v223, v219, s[76:77]\n\t" \
  "v_mfma_f32_16x16x32_bf16 v[208:211], v[172:175], v[104:107], v[208:211]\n\t" \
  "ds_bpermute_b32 v27, v15, v27\n\t" \
  "v_mfma_f32_16x16x32_bf16 v[208:211], v[176:179], v[124:127], v[208:211]\n\t" \
  "v_mfma_f32_16x16x32_bf16 v[208:211], v[180:183], v[140:143], v[208:211]\n\t" \
  "v_mfma_f32_16x16x32_bf16 v[208:211], v[184:187], v[128:131], v[208:211]\n\t" \
  "s_nop 0\n\t" \
  "v_add_f32 v245, v245, v212\n\t" \
  "v_add_f32 v246, v246, v213\n\t" \
  "v_mfma_f32_16x16x32_bf16 v[208:211], v[188:191], v[152:155], v[208:211]\n\t" \
  "v_add_f32 v247, v247, v214\n\t" \
  "v_cndmask_b32 v26, v219, v215, s[76:77]\n\t" \
  "v_mfma_f32_16x16x32_bf16 v[204:207], v[172:175], v[124:127], v[204:207]\n\t" \
  "ds_read_b32 v16, v56 offset:96\n\t" \
  "ds_bpermute_b32 v26, v15, v26\n\t" \
  "s_waitcnt lgkmcnt(2)\n\t" \
  "v_add_f32 v252, v252, v27\n\t" \
  "v_mfma_f32_16x16x32_bf16 v[204:207], v[176:179], v[140:143], v[204:207]\n\t" \
  "v_mfma_f32_16x16x32_bf16 v[204:207], v[180:183], v[128:131], v[204:207]\n\t" \
  "v_mfma_f32_16x16x32_bf16 v[204:207], v[184:187], v[152:155], v[204:207]\n\t" \
  "v_cndmask_b32 v25, v215, v211, s[76:77]\n\t" \
  "ds_bpermute_b32 v25, v15, v25\n\t" \
  "v_mfma_f32_16x16x32_bf16 v[200:203], v[172:175], v[140:143], v[200:203]\n\t" \
  "ds_read_b128 v[140:143], v9 offset:2048\n\t" \
  "s_waitcnt lgkmcnt(2)\n\t" \
  "v_add_f32 v248, v248, v26\n\t" \
  "v_mfma_f32_16x16x32_bf16 v[200:203], v[176:179], v[128:131], v[200:203]\n\t" \
  "v_mfma_f32_16x16x32_bf16 v[200:203], v[180:183], v[152:155], v[200:203]\n\t" \
  "v_mfma_f32_16x16x32_bf16 v[196:199], v[172:175], v[128:131], v[196:199]\n\t" \
  "v_readfirstlane_b32 vcc_lo, v16\n\t" \
  "ds_read_b128 v[128:131], v9 offset:1024\n\t" \
  "v_cndmask_b32 v24, v211, v207, s[76:77]\n\t" \
  "v_mfma_f32_16x16x32_bf16 v[196:199], v[176:179], v[152:155], v[196:199]\n\t" \
  "s_cmp_eq_u32 vcc_lo, 0\n\t" \
  "s_cbranch_scc1 32f\n\t" \
  "s_lshr_b64 s[90:91], %18, s75\n\t" \
  "s_and_b32 s90, s90, 15\n\t" \
  "s_lshl_b32 s90, s90, 14\n\t" \
  "s_add_u32 s90, s90, s92\n\t" \
  "s_addc_u32 s91, s93, 0\n\t" \
  "s_add_u32 s75, s75, 4\n\t" \
  "s_swappc_b64 s[68:69], s[70:71]\n\t" \
  "32:\n\t" \
  "v_mfma_f32_16x16x32_bf16 v[192:195], v[172:175], v[152:155], v[192:195]\n\t" \
  "ds_read_b128 v[152:155], v9 offset:0\n\t" \
  "ds_bpermute_b32 v24, v15, v24\n\t" \
  "s_waitcnt lgkmcnt(4)\n\t" \
  "v_add_f32 v244, v244, v25\n\t" \
  "v_mfma_f32_16x16x32_bf16 v[224:227], v[172:175], v[136:139], v[224:227]\n\t" \
  "v_cndmask_b32 v23, v207, v203, s[76:77]\n\t" \
  "ds_bpermute_b32 v23, v15, v23\n\t" \
  "v_mfma_f32_16x16x32_bf16 v[228:231], v[172:175], v[156:159], v[228:231]\n\t" \
  "v_mfma_f32_16x16x32_bf16 v[228:231], v[176:179], v[136:139], v[228:231]\n\t" \
  "v_cndmask_b32 v22, v203, v199, s[76:77]\n\t" \
  "ds_bpermute_b32 v22, v15, v22\n\t" \
  "v_mfma_f32_16x16x32_bf16 v[232:235], v[172:175], v[160:163], v[232:235]\n\t" \
  "v_cndmask_b32 v20, v195, 0, s[76:77]\n\t" \
  "ds_bpermute_b32 v20, v15, v20\n\t" \
  "v_mfma_f32_16x16x32_bf16 v[232:235], v[176:179], v[156:159], v[232:235]\n\t" \
  "v_cndmask_b32 v21, v199, v195, s[76:77]\n\t" \
  "ds_bpermute_b32 v21, v15, v21\n\t" \
  "v_mfma_f32_16x16x32_bf16 v[232:235], v[180:183], v[136:139], v[232:235]\n\t" \
  "v_add_f32 v225, v225, v192\n\t" \
  "v_add_f32 v226, v226, v193\n\t" \
  "v_mfma_f32_16x16x32_bf16 v[236:239], v[172:175], v[164:167], v[236:239]\n\t" \
  "ds_read_b32 v16, v56 offset:96\n\t" \
  "v_add_f32 v227, v227, v194\n\t" \
  "v_add_f32 v229, v229, v196\n\t" \
  "v_mfma_f32_16x16x32_bf16 v[236:239], v[176:179], v[160:163], v[236:239]\n\t" \
  "v_add_f32 v230, v230, v197\n\t" \
  "v_add_f32 v231, v231, v198\n\t" \
  "v_mfma_f32_16x16x32_bf16 v[236:239], v[180:183], v[156:159], v[236:239]\n\t" \
  "v_mfma_f32_16x16x32_bf16 v[236:239], v[184:187], v[136:139], v[236:239]\n\t" \
  "s_waitcnt lgkmcnt(2)\n\t" \
  "v_add_f32 v224, v224, v20\n\t" \
  "v_cvt_pk_bf16_f32 v45, v226, v227\n\t" \
  "v_mfma_f32_16x16x32_bf16 v[240:243], v[172:175], v[168:171], v[240:243]\n\t" \
  "v_cvt_pk_bf16_f32 v44, v224, v225\n\t" \
  "ds_write_b64 v14, v[44:45] offset:0\n\t" \
  "v_mfma_f32_16x16x32_bf16 v[240:243], v[176:179], v[164:167], v[240:243]\n\t" \
  "s_waitcnt lgkmcnt(2)\n\t" \
  "v_add_f32 v228, v228, v21\n\t" \
  "v_cvt_pk_bf16_f32 v47, v230, v231\n\t" \
  "v_mfma_f32_16x16x32_bf16 v[240:243], v[180:183], v[160:163], v[240:243]\n\t" \
  "v_cvt_pk_bf16_f32 v46, v228, v229\n\t" \
  "ds_write_b64 v14, v[46:47] offset:512\n\t" \
  "v_mfma_f32_16x16x32_bf16 v[240:243], v[184:187], v[156:159], v[240:243]\n\t" \
  "s_waitcnt lgkmcnt(2)\n\t" \
  "v_readfirstlane_b32 vcc_lo, v16\n\t" \
  "v_add_f32 v233, v233, v200\n\t" \
  "v_mfma_f32_16x16x32_bf16 v[240:243], v[188:191], v[136:139], v[240:243]\n\t" \
  "s_cmp_eq_u32 vcc_lo, 0\n\t" \
  "s_cbranch_scc1 33f\n\t" \
  "s_lshr_b64 s[90:91], %18, s75\n\t" \
  "s_and_b32 s90, s90, 15\n\t" \
  "s_lshl_b32 s90, s90, 14\n\t" \
  "s_add_u32 s90, s90, s92\n\t" \
  "s_addc_u32 s91, s93, 0\n\t" \
  "s_add_u32 s75, s75, 4\n\t" \
  "s_swappc_b64 s[68:69], s[70:71]\n\t" \
  "33:\n\t" \
  "v_mfma_f32_16x16x32_bf16 v[244:247], v[172:175], v[140:143], v[244:247]\n\t" \
  "ds_read_b128 v[40:43], v56 offset:80\n\t" \
  "v_add_f32 v234, v234, v201\n\t" \
  "v_add_f32 v235, v235, v202\n\t" \
  "v_mfma_f32_16x16x32_bf16 v[244:247], v[176:179], v[168:171], v[244:247]\n\t" \
  "v_add_f32 v232, v232, v22\n\t" \
  "v_cvt_pk_bf16_f32 v49, v234, v235\n\t" \
  "v_mfma_f32_16x16x32_bf16 v[244:247], v[180:183], v[164:167], v[244:247]\n\t" \
  "v_cvt_pk_bf16_f32 v48, v232, v233\n\t" \
  "ds_write_b64 v14, v[48:49] offset:1024\n\t" \
  "v_mfma_f32_16x16x32_bf16 v[244:247], v[184:187], v[160:163], v[244:247]\n\t" \
  "v_add_f32 v237, v237, v204\n\t" \
  "v_add_f32 v238, v238, v205\n\t" \
  "v_mfma_f32_16x16x32_bf16 v[244:247], v[188:191], v[156:159], v[244:247]\n\t" \
  "v_add_f32 v239, v239, v206\n\t" \
  "v_add_f32 v236, v236, v23\n\t" \
  "v_mfma_f32_16x16x32_bf16 v[244:247], v[148:151], v[136:139], v[244:247]\n\t" \
  "ds_read_b32 v16, v56 offset:96\n\t" \
  "v_cvt_pk_bf16_f32 v51, v238, v239\n\t" \
  "v_cvt_pk_bf16_f32 v50, v236, v237\n\t" \
  "v_mfma_f32_16x16x32_bf16 v[248:251], v[172:175], v[128:131], v[248:251]\n\t" \
  "ds_write_b64 v14, v[50:51] offset:1536\n\t" \
  "v_add_f32 v241, v241, v208\n\t" \
  "508:\n\t" \
  "s_cbranch_execz 26b\n\t" \
  "v_mfma_f32_16x16x32_bf16 v[248:251], v[176:179], v[140:143], v[248:251]\n\t" \
  "s_waitcnt lgkmcnt(3)\n\t" \
  "v_min_u32 v40, v40, v41\n\t" \
  "v_min3_u32 v40, v40, v42, v43\n\t" \
  "v_mfma_f32_16x16x32_bf16 v[248:251], v[180:183], v[168:171], v[248:251]\n\t" \
  "v_readfirstlane_b32 vcc_lo, v40\n\t" \
  "v_add_f32 v242, v242, v209\n\t" \
  "v_add_f32 v243, v243, v210\n\t" \
  "v_mfma_f32_16x16x32_bf16 v[248:251], v[184:187], v[164:167], v[248:251]\n\t" \
  "s_cmp_ge_u32 vcc_lo, 13\n\t" \
  "s_cbranch_scc0 35f\n\t" \
  "36:\n\t" \
  "v_mfma_f32_16x16x32_bf16 v[248:251], v[188:191], v[160:163], v[248:251]\n\t" \
  "v_add_f32 v240, v240, v24\n\t" \
  "v_cvt_pk_bf16_f32 v45, v242, v243\n\t" \
  "ds_read_b64_tr_b16 v[192:193], v28 offset:0\n\t" \
  "v_mfma_f32_16x16x32_bf16 v[248:251], v[148:151], v[156:159], v[248:251]\n\t" \
  "v_cvt_pk_bf16_f32 v44, v240, v241\n\t" \
  "ds_write_b64 v14, v[44:45] offset:2048\n\t" \
  "ds_read_b64_tr_b16 v[194:195], v28 offset:512\n\t" \
  "v_mfma_f32_16x16x32_bf16 v[248:251], v[132:135], v[136:139], v[248:251]\n\t" \
  "s_waitcnt lgkmcnt(4)\n\t" \
  "v_readfirstlane_b32 vcc_lo, v16\n\t" \
  "v_cvt_pk_bf16_f32 v47, v246, v247\n\t" \
  "v_mfma_f32_16x16x32_bf16 v[252:255], v[172:175], v[152:155], v[252:255]\n\t" \
  "s_cmp_eq_u32 vcc_lo, 0\n\t" \
  "s_cbranch_scc1 34f\n\t" \
  "s_cmp_ge_u32 s75, 16\n\t" \
  "s_cbranch_scc1 34f\n\t" \
  "s_lshr_b64 s[90:91], %18, s75\n\t" \
  "s_and_b32 s90, s90, 15\n\t" \
  "s_lshl_b32 s90, s90, 14\n\t" \
  "s_add_u32 s90, s90, s92\n\t" \
  "s_addc_u32 s91, s93, 0\n\t" \
  "s_add_u32 s75, s75, 4\n\t" \
  "s_swappc_b64 s[68:69], s[70:71]\n\t" \
  "34:\n\t" \
  "v_mfma_f32_16x16x32_bf16 v[252:255], v[176:179], v[128:131], v[252:255]\n\t" \
  "v_cvt_pk_bf16_f32 v46, v244, v245\n\t" \
  "ds_write_b64 v14, v[46:47] offset:2560\n\t" \
  "ds_read_b64_tr_b16 v[196:197], v28 offset:4096\n\t" \
  "v_mfma_f32_16x16x32_bf16 v[252:255], v[180:183], v[140:143], v[252:255]\n\t" \
  "ds_read_b64_tr_b16 v[198:199], v28 offset:4608\n\t" \
  "v_mfma_f32_16x16x32_bf16 v[252:255], v[184:187], v[168:171], v[252:255]\n\t" \
  "v_cvt_pk_bf16_f32 v49, v250, v251\n\t" \
  "v_cvt_pk_bf16_f32 v48, v248, v249\n\t" \
  "ds_read_b64_tr_b16 v[200:201], v29 offset:0\n\t" \
  "v_mfma_f32_16x16x32_bf16 v[252:255], v[188:191], v[164:167], v[252:255]\n\t" \
  "ds_write_b64 v14, v[48:49] offset:3072\n\t" \
  "ds_read_b64_tr_b16 v[202:203], v29 offset:512\n\t" \
  "v_mfma_f32_16x16x32_bf16 v[252:255], v[148:151], v[160:163], v[252:255]\n\t" \
  "ds_read_b64_tr_b16 v[204:205], v29 offset:4096\n\t" \
  "v_mfma_f32_16x16x32_bf16 v[252:255], v[132:135], v[156:159], v[252:255]\n\t" \
  "ds_read_b64_tr_b16 v[206:207], v29 offset:4608\n\t" \
  "v_mfma_f32_16x16x32_bf16 v[252:255], v[144:147], v[136:139], v[252:255]\n\t" \
  "ds_read_b64_tr_b16 v[208:209], v30 offset:0\n\t" \
  "ds_read_b32 v16, v56 offset:96\n\t" \
  "ds_read_b64_tr_b16 v[210:211], v30 offset:512\n\t" \
  "ds_read_b64_tr_b16 v[212:213], v30 offset:4096\n\t" \
  "ds_read_b64_tr_b16 v[214:215], v30 offset:4608\n\t" \
  "ds_read_b64_tr_b16 v[216:217], v31 offset:0\n\t" \
  "ds_read_b64_tr_b16 v[218:219], v31 offset:512\n\t" \
  "ds_read_b64_tr_b16 v[220:221], v31 offset:4096\n\t" \
  "ds_read_b64_tr_b16 v[222:223], v31 offset:4608\n\t" \
  "v_cvt_pk_bf16_f32 v51, v254, v255\n\t" \
  "v_cvt_pk_bf16_f32 v50, v252, v253\n\t" \
  "ds_write_b64 v14, v[50:51] offset:3584\n\t" \
  "ds_write_b32 v18, v17 offset:32\n\t" \
  "s_cmp_lg_u32 s75, 0\n\t" \
  "s_cbranch_scc1 41f\n\t" \
  "s_waitcnt lgkmcnt(9)\n\t" \
  "v_readfirstlane_b32 vcc_lo, v16\n\t" \
  "s_nop 1\n\t" \
  "s_cmp_ge_u32 vcc_lo, 1\n\t" \
  "s_cbranch_scc0 43f\n\t" \
  "44:\n\t" \
  "41:\n\t" \
  "s_cmp_ge_u32 s75, 16\n\t" \
  "s_cbranch_scc1 42f\n\t" \
  "s_lshr_b64 s[90:91], %18, s75\n\t" \
  "s_and_b32 s90, s90, 15\n\t" \
  "s_lshl_b32 s90, s90, 14\n\t" \
  "s_add_u32 s90, s90, s92\n\t" \
  "s_addc_u32 s91, s93, 0\n\t" \
  "s_add_u32 s75, s75, 4\n\t" \
  "s_swappc_b64 s[68:69], s[70:71]\n\t" \
  "s_branch 41b\n\t" \
  "42:\n\t" \
  "s_waitcnt lgkmcnt(15)\n\t" \
  "s_waitcnt vmcnt(14)\n\t" \
  "v_mfma_f32_16x16x32_bf16 v[224:227], v[192:195], v[64:67], 0\n\t" \
  "v_mfma_f32_16x16x32_bf16 v[224:227], v[196:199], v[68:71], v[224:227]\n\t" \
  "s_waitcnt vmcnt(12)\n\t" \
  "v_mfma_f32_16x16x32_bf16 v[228:231], v[196:199], v[76:79], 0\n\t" \
  "s_lshr_b64 s[90:91], %18, 16\n\t" \
  "s_and_b32 s90, s90, 15\n\t" \
  "s_lshl_b32 s90, s90, 14\n\t" \
  "s_add_u32 s90, s90, s92\n\t" \
  "s_addc_u32 s91, s93, 0\n\t" \
  "global_load_dwordx4 v[128:131], v12, s[90:91] offset:0 sc1\n\t" \
  "v_mfma_f32_16x16x32_bf16 v[228:231], v[192:195], v[72:75], v[228:231]\n\t" \
  "s_waitcnt lgkmcnt(12)\n\t" \
  "v_mfma_f32_16x16x32_bf16 v[232:235], v[200:203], v[64:67], 0\n\t" \
  "ds_read_b64_tr_b16 v[192:193], v28 offset:8192\n\t" \
  "s_waitcnt lgkmcnt(12)\n\t" \
  "v_mfma_f32_16x16x32_bf16 v[232:235], v[204:207], v[68:71], v[232:235]\n\t" \
  "ds_read_b64_tr_b16 v[194:195], v28 offset:8704\n\t" \
  "v_mfma_f32_16x16x32_bf16 v[236:239], v[204:207], v[76:79], 0\n\t" \
  "ds_read_b64_tr_b16 v[196:197], v28 offset:12288\n\t" \
  "global_load_dwordx4 v[132:135], v12, s[90:91] offset:1024 sc1\n\t" \
  "v_mfma_f32_16x16x32_bf16 v[236:239], v[200:203], v[72:75], v[236:239]\n\t" \
  "ds_read_b64_tr_b16 v[198:199], v28 offset:12800\n\t" \
  "s_waitcnt lgkmcnt(4)\n\t" \
  "v_mfma_f32_16x16x32_bf16 v[240:243], v[208:211], v[64:67], 0\n\t" \
  "ds_read_b64_tr_b16 v[200:201], v29 offset:8192\n\t" \
  "v_mfma_f32_16x16x32_bf16 v[240:243], v[212:215], v[68:71], v[240:243]\n\t" \
  "ds_read_b64_tr_b16 v[202:203], v29 offset:8704\n\t" \
  "v_mfma_f32_16x16x32_bf16 v[244:247], v[212:215], v[76:79], 0\n\t" \
  "ds_read_b64_tr_b16 v[204:205], v29 offset:12288\n\t" \
  "global_load_dwordx4 v[136:139], v12, s[90:91] offset:2048 sc1\n\t" \
  "v_mfma_f32_16x16x32_bf16 v[244:247], v[208:211], v[72:75], v[244:247]\n\t" \
  "ds_read_b64_tr_b16 v[206:207], v29 offset:12800\n\t" \
  "v_mfma_f32_16x16x32_bf16 v[248:251], v[216:219], v[64:67], 0\n\t" \
  "ds_read_b64_tr_b16 v[208:209], v30 offset:8192\n\t" \
  "v_mfma_f32_16x16x32_bf16 v[248:251], v[220:223], v[68:71], v[248:251]\n\t" \
  "ds_read_b64_tr_b16 v[210:211], v30 offset:8704\n\t" \
  "v_mfma_f32_16x16x32_bf16 v[252:255], v[220:223], v[76:79], 0\n\t" \
  "ds_read_b64_tr_b16 v[212:213], v30 offset:12288\n\t" \
  "global_load_dwordx4 v[140:143], v12, s[90:91] offset:3072 sc1\n\t" \
  "v_mfma_f32_16x16x32_bf16 v[252:255], v[216:219], v[72:75], v[252:255]\n\t" \
  "ds_read_b64_tr_b16 v[214:215], v30 offset:12800\n\t" \
  "s_waitcnt lgkmcnt(6)\n\t" \
  "s_waitcnt vmcnt(14)\n\t" \
  "v_mfma_f32_16x16x32_bf16 v[224:227], v[192:195], v[80:83], v[224:227]\n\t" \
  "ds_read_b64_tr_b16 v[216:217], v31 offset:8192\n\t" \
  "v_mfma_f32_16x16x32_bf16 v[224:227], v[196:199], v[84:87], v[224:227]\n\t" \
  "ds_read_b64_tr_b16 v[218:219], v31 offset:8704\n\t" \
  "s_waitcnt vmcnt(12)\n\t" \
  "v_mfma_f32_16x16x32_bf16 v[228:231], v[196:199], v[92:95], v[228:231]\n\t" \
  "ds_read_b64_tr_b16 v[220:221], v31 offset:12288\n\t" \
  "s_lshr_b64 s[90:91], %18, 20\n\t" \
  "s_and_b32 s90, s90, 15\n\t" \
  "s_lshl_b32 s90, s90, 14\n\t" \
  "s_add_u32 s90, s90, s92\n\t" \
  "s_addc_u32 s91, s93, 0\n\t" \
  "global_load_dwordx4 v[144:147], v12, s[90:91] offset:0 sc1\n\t" \
  "v_mfma_f32_16x16x32_bf16 v[228:231], v[192:195], v[88:91], v[228:231]\n\t" \
  "ds_read_b64_tr_b16 v[222:223], v31 offset:12800\n\t" \
  "v_mfma_f32_16x16x32_bf16 v[232:235], v[200:203], v[80:83], v[232:235]\n\t" \
  "ds_read_b64_tr_b16 v[192:193], v28 offset:16384\n\t" \
  "s_waitcnt lgkmcnt(6)\n\t" \
  "v_mfma_f32_16x16x32_bf16 v[232:235], v[204:207], v[84:87], v[232:235]\n\t" \
  "ds_read_b64_tr_b16 v[194:195], v28 offset:16896\n\t" \
  "ds_write_b32 v18, v17 offset:160\n\t" \
  "v_mfma_f32_16x16x32_bf16 v[236:239], v[204:207], v[92:95], v[236:239]\n\t" \
  "ds_read_b64_tr_b16 v[196:197], v28 offset:20480\n\t" \
  "global_load_dwordx4 v[148:151], v12, s[90:91] offset:1024 sc1\n\t" \
  "v_mfma_f32_16x16x32_bf16 v[236:239], v[200:203], v[88:91], v[236:239]\n\t" \
  "ds_read_b64_tr_b16 v[198:199], v28 offset:20992\n\t" \
  "v_mfma_f32_16x16x32_bf16 v[240:243], v[208:211], v[80:83], v[240:243]\n\t" \
  "ds_read_b64_tr_b16 v[200:201], v29 offset:16384\n\t" \
  "s_waitcnt lgkmcnt(7)\n\t" \
  "v_mfma_f32_16x16x32_bf16 v[240:243], v[212:215], v[84:87], v[240:243]\n\t" \
  "ds_read_b64_tr_b16 v[202:203], v29 offset:16896\n\t" \
  "v_mfma_f32_16x16x32_bf16 v[244:247], v[212:215], v[92:95], v[244:247]\n\t" \
  "ds_read_b64_tr_b16 v[204:205], v29 offset:20480\n\t" \
  "global_load_dwordx4 v[152:155], v12, s[90:91] offset:2048 sc1\n\t" \
  "v_mfma_f32_16x16x32_bf16 v[244:247], v[208:211], v[88:91], v[244:247]\n\t" \
  "ds_read_b64_tr_b16 v[206:207], v29 offset:20992\n\t" \
  "v_mfma_f32_16x16x32_bf16 v[248:251], v[216:219], v[80:83], v[248:251]\n\t" \
  "ds_read_b64_tr_b16 v[208:209], v30 offset:16384\n\t" \
  "s_waitcnt lgkmcnt(6)\n\t" \
  "v_mfma_f32_16x16x32_bf16 v[248:251], v[220:223], v[84:87], v[248:251]\n\t" \
  "ds_read_b64_tr_b16 v[210:211], v30 offset:16896\n\t" \
  "v_mfma_f32_16x16x32_bf16 v[252:255], v[220:223], v[92:95], v[252:255]\n\t" \
  "ds_read_b64_tr_b16 v[212:213], v30 offset:20480\n\t" \
  "global_load_dwordx4 v[156:159], v12, s[90:91] offset:3072 sc1\n\t" \
  "v_mfma_f32_16x16x32_bf16 v[252:255], v[216:219], v[88:91], v[252:255]\n\t" \
  "ds_read_b64_tr_b16 v[214:215], v30 offset:20992\n\t" \
  "s_waitcnt vmcnt(14)\n\t" \
  "v_mfma_f32_16x16x32_bf16 v[224:227], v[192:195], v[96:99], v[224:227]\n\t" \
  "ds_read_b64_tr_b16 v[216:217], v31 offset:16384\n\t" \
  "s_waitcnt lgkmcnt(6)\n\t" \
  "v_mfma_f32_16x16x32_bf16 v[224:227], v[196:199], v[100:103], v[224:227]\n\t" \
  "ds_read_b64_tr_b16 v[218:219], v31 offset:16896\n\t" \
  "s_lshr_b64 s[90:91], %18, 24\n\t" \
  "s_and_b32 s90, s90, 15\n\t" \
  "s_lshl_b32 s90, s90, 14\n\t" \
  "s_add_u32 s90, s90, s92\n\t" \
  "s_addc_u32 s91, s93, 0\n\t" \
  "global_load_dwordx4 v[160:163], v12, s[90:91] offset:0 sc1\n\t" \
  "s_waitcnt vmcnt(13)\n\t" \
  "v_mfma_f32_16x16x32_bf16 v[228:231], v[196:199], v[108:111], v[228:231]\n\t" \
  "ds_read_b64_tr_b16 v[220:221], v31 offset:20480\n\t" \
  "v_mfma_f32_16x16x32_bf16 v[228:231], v[192:195], v[104:107], v[228:231]\n\t" \
  "ds_read_b64_tr_b16 v[222:223], v31 offset:20992\n\t" \
  "global_load_dwordx4 v[164:167], v12, s[90:91] offset:1024 sc1\n\t" \
  "v_mfma_f32_16x16x32_bf16 v[232:235], v[200:203], v[96:99], v[232:235]\n\t" \
  "ds_read_b64_tr_b16 v[192:193], v28 offset:24576\n\t" \
  "s_waitcnt lgkmcnt(6)\n\t" \
  "v_mfma_f32_16x16x32_bf16 v[232:235], v[204:207], v[100:103], v[232:235]\n\t" \
  "ds_read_b64_tr_b16 v[194:195], v28 offset:25088\n\t" \
  "global_load_dwordx4 v[168:171], v12, s[90:91] offset:2048 sc1\n\t" \
  "v_mfma_f32_16x16x32_bf16 v[236:239], v[204:207], v[108:111], v[236:239]\n\t" \
  "ds_read_b64_tr_b16 v[196:197], v28 offset:28672\n\t" \
  "v_mfma_f32_16x16x32_bf16 v[236:239], v[200:203], v[104:107], v[236:239]\n\t" \
  "ds_read_b64_tr_b16 v[198:199], v28 offset:29184\n\t" \
  "global_load_dwordx4 v[172:175], v12, s[90:91] offset:3072 sc1\n\t" \
  "v_mfma_f32_16x16x32_bf16 v[240:243], v[208:211], v[96:99], v[240:243]\n\t" \
  "ds_read_b64_tr_b16 v[200:201], v29 offset:24576\n\t" \
  "s_waitcnt lgkmcnt(6)\n\t" \
  "v_mfma_f32_16x16x32_bf16 v[240:243], v[212:215], v[100:103], v[240:243]\n\t" \
  "ds_read_b64_tr_b16 v[202:203], v29 offset:25088\n\t" \
  "s_lshr_b64 s[90:91], %18, 28\n\t" \
  "s_and_b32 s90, s90, 15\n\t" \
  "s_lshl_b32 s90, s90, 14\n\t" \
  "s_add_u32 s90, s90, s92\n\t" \
  "s_addc_u32 s91, s93, 0\n\t" \
  "global_load_dwordx4 v[176:179], v12, s[90:91] offset:0 sc1\n\t" \
  "v_mfma_f32_16x16x32_bf16 v[244:247], v[212:215], v[108:111], v[244:247]\n\t" \
  "ds_read_b64_tr_b16 v[204:205], v29 offset:28672\n\t" \
  "v_mfma_f32_16x16x32_bf16 v[244:247], v[208:211], v[104:107], v[244:247]\n\t" \
  "ds_read_b64_tr_b16 v[206:207], v29 offset:29184\n\t" \
  "global_load_dwordx4 v[180:183], v12, s[90:91] offset:1024 sc1\n\t" \
  "v_mfma_f32_16x16x32_bf16 v[248:251], v[216:219], v[96:99], v[248:251]\n\t" \
  "ds_read_b64_tr_b16 v[208:209], v30 offset:24576\n\t" \
  "s_waitcnt lgkmcnt(6)\n\t" \
  "v_mfma_f32_16x16x32_bf16 v[248:251], v[220:223], v[100:103], v[248:251]\n\t" \
  "ds_read_b64_tr_b16 v[210:211], v30 offset:25088\n\t" \
  "global_load_dwordx4 v[184:187], v12, s[90:91] offset:2048 sc1\n\t" \
  "v_mfma_f32_16x16x32_bf16 v[252:255], v[220:223], v[108:111], v[252:255]\n\t" \
  "ds_read_b64_tr_b16 v[212:213], v30 offset:28672\n\t" \
  "v_mfma_f32_16x16x32_bf16 v[252:255], v[216:219], v[104:107], v[252:255]\n\t" \
  "ds_read_b64_tr_b16 v[214:215], v30 offset:29184\n\t" \
  "global_load_dwordx4 v[188:191], v12, s[90:91] offset:3072 sc1\n\t" \
  "s_waitcnt vmcnt(18)\n\t" \
  "v_mfma_f32_16x16x32_bf16 v[224:227], v[192:195], v[112:115], v[224:227]\n\t" \
  "ds_read_b64_tr_b16 v[216:217], v31 offset:24576\n\t" \
  "s_waitcnt lgkmcnt(6)\n\t" \
  "v_mfma_f32_16x16x32_bf16 v[224:227], v[196:199], v[116:119], v[224:227]\n\t" \
  "ds_read_b64_tr_b16 v[218:219], v31 offset:25088\n\t" \
  "s_waitcnt vmcnt(16)\n\t" \
  "v_mfma_f32_16x16x32_bf16 v[228:231], v[196:199], v[124:127], v[228:231]\n\t" \
  "ds_read_b64_tr_b16 v[220:221], v31 offset:28672\n\t" \
  "s_lshr_b64 s[90:91], %18, 32\n\t" \
  "s_and_b32 s90, s90, 15\n\t" \
  "s_lshl_b32 s90, s90, 14\n\t" \
  "s_add_u32 s90, s90, s92\n\t" \
  "s_addc_u32 s91, s93, 0\n\t" \
  "global_load_dwordx4 v[64:67], v12, s[90:91] offset:0 sc1\n\t" \
  "v_mfma_f32_16x16x32_bf16 v[228:231], v[192:195], v[120:123], v[228:231]\n\t" \
  "ds_read_b64_tr_b16 v[222:223], v31 offset:29184\n\t" \
  "v_mfma_f32_16x16x32_bf16 v[232:235], v[200:203], v[112:115], v[232:235]\n\t" \
  "ds_read_b64_tr_b16 v[192:193], v28 offset:32768\n\t" \
  "s_waitcnt lgkmcnt(6)\n\t" \
  "v_mfma_f32_16x16x32_bf16 v[232:235], v[204:207], v[116:119], v[232:235]\n\t" \
  "ds_read_b64_tr_b16 v[194:195], v28 offset:33280\n\t" \
  "v_mfma_f32_16x16x32_bf16 v[236:239], v[204:207], v[124:127], v[236:239]\n\t" \
  "ds_read_b64_tr_b16 v[196:197], v28 offset:36864\n\t" \
  "global_load_dwordx4 v[68:71], v12, s[90:91] offset:1024 sc1\n\t" \
  "v_mfma_f32_16x16x32_bf16 v[236:239], v[200:203], v[120:123], v[236:239]\n\t" \
  "ds_read_b64_tr_b16 v[198:199], v28 offset:37376\n\t" \
  "v_mfma_f32_16x16x32_bf16 v[240:243], v[208:211], v[112:115], v[240:243]\n\t" \
  "ds_read_b64_tr_b16 v[200:201], v29 offset:32768\n\t" \
  "s_waitcnt lgkmcnt(6)\n\t" \
  "v_mfma_f32_16x16x32_bf16 v[240:243], v[212:215], v[116:119], v[240:243]\n\t" \
  "ds_read_b64_tr_b16 v[202:203], v29 offset:33280\n\t" \
  "v_mfma_f32_16x16x32_bf16 v[244:247], v[212:215], v[124:127], v[244:247]\n\t" \
  "ds_read_b64_tr_b16 v[204:205], v29 offset:36864\n\t" \
  "global_load_dwordx4 v[72:75], v12, s[90:91] offset:2048 sc1\n\t" \
  "v_mfma_f32_16x16x32_bf16 v[244:247], v[208:211], v[120:123], v[244:247]\n\t" \
  "ds_read_b64_tr_b16 v[206:207], v29 offset:37376\n\t" \
  "v_mfma_f32_16x16x32_bf16 v[248:251], v[216:219], v[112:115], v[248:251]\n\t" \
  "ds_read_b64_tr_b16 v[208:209], v30 offset:32768\n\t" \
  "s_waitcnt lgkmcnt(6)\n\t" \
  "v_mfma_f32_16x16x32_bf16 v[248:251], v[220:223], v[116:119], v[248:251]\n\t" \
  "ds_read_b64_tr_b16 v[210:211], v30 offset:33280\n\t" \
  "v_mfma_f32_16x16x32_bf16 v[252:255], v[220:223], v[124:127], v[252:255]\n\t" \
  "ds_read_b64_tr_b16 v[212:213], v30 offset:36864\n\t" \
  "global_load_dwordx4 v[76:79], v12, s[90:91] offset:3072 sc1\n\t" \
  "v_mfma_f32_16x16x32_bf16 v[252:255], v[216:219], v[120:123], v[252:255]\n\t" \
  "ds_read_b64_tr_b16 v[214:215], v30 offset:37376\n\t" \
  "s_waitcnt vmcnt(18)\n\t" \
  "v_mfma_f32_16x16x32_bf16 v[224:227], v[192:195], v[128:131], v[224:227]\n\t" \
  "ds_read_b64_tr_b16 v[216:217], v31 offset:32768\n\t" \
  "s_waitcnt lgkmcnt(6)\n\t" \
  "v_mfma_f32_16x16x32_bf16 v[224:227], v[196:199], v[132:135], v[224:227]\n\t" \
  "ds_read_b64_tr_b16 v[218:219], v31 offset:33280\n\t" \
  "s_waitcnt vmcnt(16)\n\t" \
  "v_mfma_f32_16x16x32_bf16 v[228:231], v[196:199], v[140:143], v[228:231]\n\t" \
  "ds_read_b64_tr_b16 v[220:221], v31 offset:36864\n\t" \
  "s_lshr_b64 s[90:91], %18, 36\n\t" \
  "s_and_b32 s90, s90, 15\n\t" \
  "s_lshl_b32 s90, s90, 14\n\t" \
  "s_add_u32 s90, s90, s92\n\t" \
  "s_addc_u32 s91, s93, 0\n\t" \
  "global_load_dwordx4 v[80:83], v12, s[90:91] offset:0 sc1\n\t" \
  "v_mfma_f32_16x16x32_bf16 v[228:231], v[192:195], v[136:139], v[228:231]\n\t" \
  "ds_read_b64_tr_b16 v[222:223], v31 offset:37376\n\t" \
  "v_mfma_f32_16x16x32_bf16 v[232:235], v[200:203], v[128:131], v[232:235]\n\t" \
  "ds_read_b64_tr_b16 v[192:193], v28 offset:40960\n\t" \
  "s_waitcnt lgkmcnt(6)\n\t" \
  "v_mfma_f32_16x16x32_bf16 v[232:235], v[204:207], v[132:135], v[232:235]\n\t" \
  "ds_read_b64_tr_b16 v[194:195], v28 offset:41472\n\t" \
  "v_mfma_f32_16x16x32_bf16 v[236:239], v[204:207], v[140:143], v[236:239]\n\t" \
  "ds_read_b64_tr_b16 v[196:197], v28 offset:45056\n\t" \
  "global_load_dwordx4 v[84:87], v12, s[90:91] offset:1024 sc1\n\t" \
  "v_mfma_f32_16x16x32_bf16 v[236:239], v[200:203], v[136:139], v[236:239]\n\t" \
  "ds_read_b64_tr_b16 v[198:199], v28 offset:45568\n\t" \
  "v_mfma_f32_16x16x32_bf16 v[240:243], v[208:211], v[128:131], v[240:243]\n\t" \
  "ds_read_b64_tr_b16 v[200:201], v29 offset:40960\n\t" \
  "s_waitcnt lgkmcnt(6)\n\t" \
  "v_mfma_f32_16x16x32_bf16 v[240:243], v[212:215], v[132:135], v[240:243]\n\t" \
  "ds_read_b64_tr_b16 v[202:203], v29 offset:41472\n\t" \
  "v_mfma_f32_16x16x32_bf16 v[244:247], v[212:215], v[140:143], v[244:247]\n\t" \
  "ds_read_b64_tr_b16 v[204:205], v29 offset:45056\n\t" \
  "global_load_dwordx4 v[88:91], v12, s[90:91] offset:2048 sc1\n\t" \
  "v_mfma_f32_16x16x32_bf16 v[244:247], v[208:211], v[136:139], v[244:247]\n\t" \
  "ds_read_b64_tr_b16 v[206:207], v29 offset:45568\n\t" \
  "v_mfma_f32_16x16x32_bf16 v[248:251], v[216:219], v[128:131], v[248:251]\n\t" \
  "ds_read_b64_tr_b16 v[208:209], v30 offset:40960\n\t" \
  "s_waitcnt lgkmcnt(6)\n\t" \
  "v_mfma_f32_16x16x32_bf16 v[248:251], v[220:223], v[132:135], v[248:251]\n\t" \
  "ds_read_b64_tr_b16 v[210:211], v30 offset:41472\n\t" \
  "v_mfma_f32_16x16x32_bf16 v[252:255], v[220:223], v[140:143], v[252:255]\n\t" \
  "ds_read_b64_tr_b16 v[212:213], v30 offset:45056\n\t" \
  "global_load_dwordx4 v[92:95], v12, s[90:91] offset:3072 sc1\n\t" \
  "v_mfma_f32_16x16x32_bf16 v[252:255], v[216:219], v[136:139], v[252:255]\n\t" \
  "ds_read_b64_tr_b16 v[214:215], v30 offset:45568\n\t" \
  "s_waitcnt vmcnt(18)\n\t" \
  "v_mfma_f32_16x16x32_bf16 v[224:227], v[192:195], v[144:147], v[224:227]\n\t" \
  "ds_read_b64_tr_b16 v[216:217], v31 offset:40960\n\t" \
  "s_waitcnt lgkmcnt(6)\n\t" \
  "v_mfma_f32_16x16x32_bf16 v[224:227], v[196:199], v[148:151], v[224:227]\n\t" \
  "ds_read_b64_tr_b16 v[218:219], v31 offset:41472\n\t" \
  "s_waitcnt vmcnt(16)\n\t" \
  "v_mfma_f32_16x16x32_bf16 v[228:231], v[196:199], v[156:159], v[228:231]\n\t" \
  "ds_read_b64_tr_b16 v[220:221], v31 offset:45056\n\t" \
  "s_lshr_b64 s[90:91], %18, 40\n\t" \
  "s_and_b32 s90, s90, 15\n\t" \
  "s_lshl_b32 s90, s90, 14\n\t" \
  "s_add_u32 s90, s90, s92\n\t" \
  "s_addc_u32 s91, s93, 0\n\t" \
  "global_load_dwordx4 v[96:99], v12, s[90:91] offset:0 sc1\n\t" \
  "v_mfma_f32_16x16x32_bf16 v[228:231], v[192:195], v[152:155], v[228:231]\n\t" \
  "ds_read_b64_tr_b16 v[222:223], v31 offset:45568\n\t" \
  "v_mfma_f32_16x16x32_bf16 v[232:235], v[200:203], v[144:147], v[232:235]\n\t" \
  "ds_read_b64_tr_b16 v[192:193], v28 offset:49152\n\t" \
  "s_waitcnt lgkmcnt(6)\n\t" \
  "v_mfma_f32_16x16x32_bf16 v[232:235], v[204:207], v[148:151], v[232:235]\n\t" \
  "ds_read_b64_tr_b16 v[194:195], v28 offset:49664\n\t" \
  "v_mfma_f32_16x16x32_bf16 v[236:239], v[204:207], v[156:159], v[236:239]\n\t" \
  "ds_read_b64_tr_b16 v[196:197], v28 offset:53248\n\t" \
  "global_load_dwordx4 v[100:103], v12, s[90:91] offset:1024 sc1\n\t" \
  "v_mfma_f32_16x16x32_bf16 v[236:239], v[200:203], v[152:155], v[236:239]\n\t" \
  "ds_read_b64_tr_b16 v[198:199], v28 offset:53760\n\t" \
  "v_mfma_f32_16x16x32_bf16 v[240:243], v[208:211], v[144:147], v[240:243]\n\t" \
  "ds_read_b64_tr_b16 v[200:201], v29 offset:49152\n\t" \
  "s_waitcnt lgkmcnt(6)\n\t" \
  "v_mfma_f32_16x16x32_bf16 v[240:243], v[212:215], v[148:151], v[240:243]\n\t" \
  "ds_read_b64_tr_b16 v[202:203], v29 offset:49664\n\t" \
  "v_mfma_f32_16x16x32_bf16 v[244:247], v[212:215], v[156:159], v[244:247]\n\t" \
  "ds_read_b64_tr_b16 v[204:205], v29 offset:53248\n\t" \
  "global_load_dwordx4 v[104:107], v12, s[90:91] offset:2048 sc1\n\t" \
  "v_mfma_f32_16x16x32_bf16 v[244:247], v[208:211], v[152:155], v[244:247]\n\t" \
  "ds_read_b64_tr_b16 v[206:207], v29 offset:53760\n\t" \
  "v_mfma_f32_16x16x32_bf16 v[248:251], v[216:219], v[144:147], v[248:251]\n\t" \
  "ds_read_b64_tr_b16 v[208:209], v30 offset:49152\n\t" \
  "s_waitcnt lgkmcnt(6)\n\t" \
  "v_mfma_f32_16x16x32_bf16 v[248:251], v[220:223], v[148:151], v[248:251]\n\t" \
  "ds_read_b64_tr_b16 v[210:211], v30 offset:49664\n\t" \
  "v_mfma_f32_16x16x32_bf16 v[252:255], v[220:223], v[156:159], v[252:255]\n\t" \
  "ds_read_b64_tr_b16 v[212:213], v30 offset:53248\n\t" \
  "global_load_dwordx4 v[108:111], v12, s[90:91] offset:3072 sc1\n\t" \
  "ds_write_b32 v56, v56 offset:108\n\t" \
  "v_mfma_f32_16x16x32_bf16 v[252:255], v[216:219], v[152:155], v[252:255]\n\t" \
  "ds_read_b64_tr_b16 v[214:215], v30 offset:53760\n\t" \
  "s_waitcnt vmcnt(18)\n\t" \
  "v_mfma_f32_16x16x32_bf16 v[224:227], v[192:195], v[160:163], v[224:227]\n\t" \
  "ds_read_b64_tr_b16 v[216:217], v31 offset:49152\n\t" \
  "s_waitcnt lgkmcnt(7)\n\t" \
  "v_mfma_f32_16x16x32_bf16 v[224:227], v[196:199], v[164:167], v[224:227]\n\t" \
  "ds_read_b64_tr_b16 v[218:219], v31 offset:49664\n\t" \
  "s_waitcnt vmcnt(16)\n\t" \
  "v_mfma_f32_16x16x32_bf16 v[228:231], v[196:199], v[172:175], v[228:231]\n\t" \
  "ds_read_b64_tr_b16 v[220:221], v31 offset:53248\n\t" \
  "s_lshr_b64 s[90:91], %18, 44\n\t" \
  "s_and_b32 s90, s90, 15\n\t" \
  "s_lshl_b32 s90, s90, 14\n\t" \
  "s_add_u32 s90, s90, s92\n\t" \
  "s_addc_u32 s91, s93, 0\n\t" \
  "global_load_dwordx4 v[112:115], v12, s[90:91] offset:0 sc1\n\t" \
  "v_mfma_f32_16x16x32_bf16 v[228:231], v[192:195], v[168:171], v[228:231]\n\t" \
  "ds_read_b64_tr_b16 v[222:223], v31 offset:53760\n\t" \
  "v_mfma_f32_16x16x32_bf16 v[232:235], v[200:203], v[160:163], v[232:235]\n\t" \
  "ds_read_b64_tr_b16 v[192:193], v28 offset:57344\n\t" \
  "s_waitcnt lgkmcnt(6)\n\t" \
  "v_mfma_f32_16x16x32_bf16 v[232:235], v[204:207], v[164:167], v[232:235]\n\t" \
  "ds_read_b64_tr_b16 v[194:195], v28 offset:57856\n\t" \
  "v_mfma_f32_16x16x32_bf16 v[236:239], v[204:207], v[172:175], v[236:239]\n\t" \
  "ds_read_b64_tr_b16 v[196:197], v28 offset:61440\n\t" \
  "global_load_dwordx4 v[116:119], v12, s[90:91] offset:1024 sc1\n\t" \
  "v_mfma_f32_16x16x32_bf16 v[236:239], v[200:203], v[168:171], v[236:239]\n\t" \
  "ds_read_b64_tr_b16 v[198:199], v28 offset:61952\n\t" \
  "v_mfma_f32_16x16x32_bf16 v[240:243], v[208:211], v[160:163], v[240:243]\n\t" \
  "ds_read_b64_tr_b16 v[200:201], v29 offset:57344\n\t" \
  "s_waitcnt lgkmcnt(6)\n\t" \
  "v_mfma_f32_16x16x32_bf16 v[240:243], v[212:215], v[164:167], v[240:243]\n\t" \
  "ds_read_b64_tr_b16 v[202:203], v29 offset:57856\n\t" \
  "v_mfma_f32_16x16x32_bf16 v[244:247], v[212:215], v[172:175], v[244:247]\n\t" \
  "ds_read_b64_tr_b16 v[204:205], v29 offset:61440\n\t" \
  "global_load_dwordx4 v[120:123], v12, s[90:91] offset:2048 sc1\n\t" \
  "v_mfma_f32_16x16x32_bf16 v[244:247], v[208:211], v[168:171], v[244:247]\n\t" \
  "ds_read_b64_tr_b16 v[206:207], v29 offset:61952\n\t" \
  "v_mfma_f32_16x16x32_bf16 v[248:251], v[216:219], v[160:163], v[248:251]\n\t" \
  "ds_read_b64_tr_b16 v[208:209], v30 offset:57344\n\t" \
  "s_waitcnt lgkmcnt(6)\n\t" \
  "v_mfma_f32_16x16x32_bf16 v[248:251], v[220:223], v[164:167], v[248:251]\n\t" \
  "ds_read_b64_tr_b16 v[210:211], v30 offset:57856\n\t" \
  "v_mfma_f32_16x16x32_bf16 v[252:255], v[220:223], v[172:175], v[252:255]\n\t" \
  "ds_read_b64_tr_b16 v[212:213], v30 offset:61440\n\t" \
  "global_load_dwordx4 v[124:127], v12, s[90:91] offset:3072 sc1\n\t" \
  "v_mfma_f32_16x16x32_bf16 v[252:255], v[216:219], v[168:171], v[252:255]\n\t" \
  "ds_read_b64_tr_b16 v[214:215], v30 offset:61952\n\t" \
  "s_waitcnt vmcnt(18)\n\t" \
  "v_mfma_f32_16x16x32_bf16 v[224:227], v[192:195], v[176:179], v[224:227]\n\t" \
  "ds_read_b64_tr_b16 v[216:217], v31 offset:57344\n\t" \
  "s_waitcnt lgkmcnt(6)\n\t" \
  "v_mfma_f32_16x16x32_bf16 v[224:227], v[196:199], v[180:183], v[224:227]\n\t" \
  "ds_read_b64_tr_b16 v[218:219], v31 offset:57856\n\t" \
  "s_waitcnt vmcnt(16)\n\t" \
  "v_mfma_f32_16x16x32_bf16 v[228:231], v[196:199], v[188:191], v[228:231]\n\t" \
  "ds_read_b64_tr_b16 v[220:221], v31 offset:61440\n\t" \
  "s_lshr_b64 s[90:91], %18, 48\n\t" \
  "s_and_b32 s90, s90, 15\n\t" \
  "s_lshl_b32 s90, s90, 14\n\t" \
  "s_add_u32 s90, s90, s92\n\t" \
  "s_addc_u32 s91, s93, 0\n\t" \
  "global_load_dwordx4 v[128:131], v12, s[90:91] offset:0 sc1\n\t" \
  "v_mfma_f32_16x16x32_bf16 v[228:231], v[192:195], v[184:187], v[228:231]\n\t" \
  "ds_read_b64_tr_b16 v[222:223], v31 offset:61952\n\t" \
  "v_mfma_f32_16x16x32_bf16 v[232:235], v[200:203], v[176:179], v[232:235]\n\t" \
  "ds_read_b64_tr_b16 v[192:193], v32 offset:0\n\t" \
  "s_waitcnt lgkmcnt(6)\n\t" \
  "v_mfma_f32_16x16x32_bf16 v[232:235], v[204:207], v[180:183], v[232:235]\n\t" \
  "ds_read_b64_tr_b16 v[194:195], v32 offset:512\n\t" \
  "v_mfma_f32_16x16x32_bf16 v[236:239], v[204:207], v[188:191], v[236:239]\n\t" \
  "ds_read_b64_tr_b16 v[196:197], v32 offset:4096\n\t" \
  "global_load_dwordx4 v[132:135], v12, s[90:91] offset:1024 sc1\n\t" \
  "v_mfma_f32_16x16x32_bf16 v[236:239], v[200:203], v[184:187], v[236:239]\n\t" \
  "ds_read_b64_tr_b16 v[198:199], v32 offset:4608\n\t" \
  "v_mfma_f32_16x16x32_bf16 v[240:243], v[208:211], v[176:179], v[240:243]\n\t" \
  "ds_read_b64_tr_b16 v[200:201], v33 offset:0\n\t" \
  "s_waitcnt lgkmcnt(6)\n\t" \
  "v_mfma_f32_16x16x32_bf16 v[240:243], v[212:215], v[180:183], v[240:243]\n\t" \
  "ds_read_b64_tr_b16 v[202:203], v33 offset:512\n\t" \
  "v_mfma_f32_16x16x32_bf16 v[244:247], v[212:215], v[188:191], v[244:247]\n\t" \
  "ds_read_b64_tr_b16 v[204:205], v33 offset:4096\n\t" \
  "global_load_dwordx4 v[136:139], v12, s[90:91] offset:2048 sc1\n\t" \
  "v_mfma_f32_16x16x32_bf16 v[244:247], v[208:211], v[184:187], v[244:247]\n\t" \
  "ds_read_b64_tr_b16 v[206:207], v33 offset:4608\n\t" \
  "v_mfma_f32_16x16x32_bf16 v[248:251], v[216:219], v[176:179], v[248:251]\n\t" \
  "ds_read_b64_tr_b16 v[208:209], v34 offset:0\n\t" \
  "s_waitcnt lgkmcnt(6)\n\t" \
  "v_mfma_f32_16x16x32_bf16 v[248:251], v[220:223], v[180:183], v[248:251]\n\t" \
  "ds_read_b64_tr_b16 v[210:211], v34 offset:512\n\t" \
  "v_mfma_f32_16x16x32_bf16 v[252:255], v[220:223], v[188:191], v[252:255]\n\t" \
  "ds_read_b64_tr_b16 v[212:213], v34 offset:4096\n\t" \
  "global_load_dwordx4 v[140:143], v12, s[90:91] offset:3072 sc1\n\t" \
  "v_mfma_f32_16x16x32_bf16 v[252:255], v[216:219], v[184:187], v[252:255]\n\t" \
  "ds_read_b64_tr_b16 v[214:215], v34 offset:4608\n\t" \
  "s_waitcnt vmcnt(18)\n\t" \
  "v_mfma_f32_16x16x32_bf16 v[224:227], v[192:195], v[64:67], v[224:227]\n\t" \
  "ds_read_b64_tr_b16 v[216:217], v35 offset:0\n\t" \
  "s_waitcnt lgkmcnt(6)\n\t" \
  "v_mfma_f32_16x16x32_bf16 v[224:227], v[196:199], v[68:71], v[224:227]\n\t" \
  "ds_read_b64_tr_b16 v[218:219], v35 offset:512\n\t" \
  "s_waitcnt vmcnt(16)\n\t" \
  "v_mfma_f32_16x16x32_bf16 v[228:231], v[196:199], v[76:79], v[228:231]\n\t" \
  "ds_read_b64_tr_b16 v[220:221], v35 offset:4096\n\t" \
  "s_lshr_b64 s[90:91], %18, 52\n\t" \
  "s_and_b32 s90, s90, 15\n\t" \
  "s_lshl_b32 s90, s90, 14\n\t" \
  "s_add_u32 s90, s90, s92\n\t" \
  "s_addc_u32 s91, s93, 0\n\t" \
  "global_load_dwordx4 v[144:147], v12, s[90:91] offset:0 sc1\n\t" \
  "v_mfma_f32_16x16x32_bf16 v[228:231], v[192:195], v[72:75], v[228:231]\n\t" \
  "ds_read_b64_tr_b16 v[222:223], v35 offset:4608\n\t" \
  "v_mfma_f32_16x16x32_bf16 v[232:235], v[200:203], v[64:67], v[232:235]\n\t" \
  "ds_read_b64_tr_b16 v[192:193], v32 offset:8192\n\t" \
  "s_waitcnt lgkmcnt(6)\n\t" \
  "v_mfma_f32_16x16x32_bf16 v[232:235], v[204:207], v[68:71], v[232:235]\n\t" \
  "ds_read_b64_tr_b16 v[194:195], v32 offset:8704\n\t" \
  "v_mfma_f32_16x16x32_bf16 v[236:239], v[204:207], v[76:79], v[236:239]\n\t" \
  "ds_read_b64_tr_b16 v[196:197], v32 offset:12288\n\t" \
  "global_load_dwordx4 v[148:151], v12, s[90:91] offset:1024 sc1\n\t" \
  "ds_read_b32 v16, v56 offset:96\n\t" \
  "v_mfma_f32_16x16x32_bf16 v[236:239], v[200:203], v[72:75], v[236:239]\n\t" \
  "ds_read_b64_tr_b16 v[198:199], v32 offset:12800\n\t" \
  "v_mfma_f32_16x16x32_bf16 v[240:243], v[208:211], v[64:67], v[240:243]\n\t" \
  "ds_read_b64_tr_b16 v[200:201], v33 offset:8192\n\t" \
  "s_waitcnt lgkmcnt(7)\n\t" \
  "v_mfma_f32_16x16x32_bf16 v[240:243], v[212:215], v[68:71], v[240:243]\n\t" \
  "ds_read_b64_tr_b16 v[202:203], v33 offset:8704\n\t" \
  "v_mfma_f32_16x16x32_bf16 v[244:247], v[212:215], v[76:79], v[244:247]\n\t" \
  "ds_read_b64_tr_b16 v[204:205], v33 offset:12288\n\t" \
  "global_load_dwordx4 v[152:155], v12, s[90:91] offset:2048 sc1\n\t" \
  "v_mfma_f32_16x16x32_bf16 v[244:247], v[208:211], v[72:75], v[244:247]\n\t" \
  "ds_read_b64_tr_b16 v[206:207], v33 offset:12800\n\t" \
  "v_mfma_f32_16x16x32_bf16 v[248:251], v[216:219], v[64:67], v[248:251]\n\t" \
  "ds_read_b64_tr_b16 v[208:209], v34 offset:8192\n\t" \
  "s_waitcnt lgkmcnt(6)\n\t" \
  "v_mfma_f32_16x16x32_bf16 v[248:251], v[220:223], v[68:71], v[248:251]\n\t" \
  "ds_read_b64_tr_b16 v[210:211], v34 offset:8704\n\t" \
  "v_mfma_f32_16x16x32_bf16 v[252:255], v[220:223], v[76:79], v[252:255]\n\t" \
  "ds_read_b64_tr_b16 v[212:213], v34 offset:12288\n\t" \
  "global_load_dwordx4 v[156:159], v12, s[90:91] offset:3072 sc1\n\t" \
  "v_readfirstlane_b32 vcc_lo, v16\n\t" \
  "v_mfma_f32_16x16x32_bf16 v[252:255], v[216:219], v[72:75], v[252:255]\n\t" \
  "ds_read_b64_tr_b16 v[214:215], v34 offset:12800\n\t" \
  "s_cmp_ge_u32 vcc_lo, 2\n\t" \
  "s_cbranch_scc0 46f\n\t" \
  "47:\n\t" \
  "s_waitcnt vmcnt(18)\n\t" \
  "v_mfma_f32_16x16x32_bf16 v[224:227], v[192:195], v[80:83], v[224:227]\n\t" \
  "ds_read_b64_tr_b16 v[216:217], v35 offset:8192\n\t" \
  "s_waitcnt lgkmcnt(6)\n\t" \
  "v_mfma_f32_16x16x32_bf16 v[224:227], v[196:199], v[84:87], v[224:227]\n\t" \
  "ds_read_b64_tr_b16 v[218:219], v35 offset:8704\n\t" \
  "s_waitcnt vmcnt(16)\n\t" \
  "v_mfma_f32_16x16x32_bf16 v[228:231], v[196:199], v[92:95], v[228:231]\n\t" \
  "ds_read_b64_tr_b16 v[220:221], v35 offset:12288\n\t" \
  "s_lshr_b64 s[90:91], %18, 56\n\t" \
  "s_and_b32 s90, s90, 15\n\t" \
  "s_lshl_b32 s90, s90, 14\n\t" \
  "s_add_u32 s90, s90, s92\n\t" \
  "s_addc_u32 s91, s93, 0\n\t" \
  "global_load_dwordx4 v[160:163], v12, s[90:91] offset:0 sc1\n\t" \
  "v_mfma_f32_16x16x32_bf16 v[228:231], v[192:195], v[88:91], v[228:231]\n\t" \
  "ds_read_b64_tr_b16 v[222:223], v35 offset:12800\n\t" \
  "v_mfma_f32_16x16x32_bf16 v[232:235], v[200:203], v[80:83], v[232:235]\n\t" \
  "ds_read_b64_tr_b16 v[192:193], v32 offset:16384\n\t" \
  "s_waitcnt lgkmcnt(6)\n\t" \
  "v_mfma_f32_16x16x32_bf16 v[232:235], v[204:207], v[84:87], v[232:235]\n\t" \
  "ds_read_b64_tr_b16 v[194:195], v32 offset:16896\n\t" \
  "v_mfma_f32_16x16x32_bf16 v[236:239], v[204:207], v[92:95], v[236:239]\n\t" \
  "ds_read_b64_tr_b16 v[196:197], v32 offset:20480\n\t" \
  "global_load_dwordx4 v[164:167], v12, s[90:91] offset:1024 sc1\n\t" \
  "v_mfma_f32_16x16x32_bf16 v[236:239], v[200:203], v[88:91], v[236:239]\n\t" \
  "ds_read_b64_tr_b16 v[198:199], v32 offset:20992\n\t" \
  "v_mfma_f32_16x16x32_bf16 v[240:243], v[208:211], v[80:83], v[240:243]\n\t" \
  "ds_read_b64_tr_b16 v[200:201], v33 offset:16384\n\t" \
  "s_waitcnt lgkmcnt(6)\n\t" \
  "v_mfma_f32_16x16x32_bf16 v[240:243], v[212:215], v[84:87], v[240:243]\n\t" \
  "ds_read_b64_tr_b16 v[202:203], v33 offset:16896\n\t" \
  "v_mfma_f32_16x16x32_bf16 v[244:247], v[212:215], v[92:95], v[244:247]\n\t" \
  "ds_read_b64_tr_b16 v[204:205], v33 offset:20480\n\t" \
  "global_load_dwordx4 v[168:171], v12, s[90:91] offset:2048 sc1\n\t" \
  "v_mfma_f32_16x16x32_bf16 v[244:247], v[208:211], v[88:91], v[244:247]\n\t" \
  "ds_read_b64_tr_b16 v[206:207], v33 offset:20992\n\t" \
  "v_mfma_f32_16x16x32_bf16 v[248:251], v[216:219], v[80:83], v[248:251]\n\t" \
  "ds_read_b64_tr_b16 v[208:209], v34 offset:16384\n\t" \
  "s_waitcnt lgkmcnt(6)\n\t" \
  "v_mfma_f32_16x16x32_bf16 v[248:251], v[220:223], v[84:87], v[248:251]\n\t" \
  "ds_read_b64_tr_b16 v[210:211], v34 offset:16896\n\t" \
  "v_mfma_f32_16x16x32_bf16 v[252:255], v[220:223], v[92:95], v[252:255]\n\t" \
  "ds_read_b64_tr_b16 v[212:213], v34 offset:20480\n\t" \
  "global_load_dwordx4 v[172:175], v12, s[90:91] offset:3072 sc1\n\t" \
  "v_mfma_f32_16x16x32_bf16 v[252:255], v[216:219], v[88:91], v[252:255]\n\t" \
  "ds_read_b64_tr_b16 v[214:215], v34 offset:20992\n\t" \
  "s_waitcnt vmcnt(18)\n\t" \
  "v_mfma_f32_16x16x32_bf16 v[224:227], v[192:195], v[96:99], v[224:227]\n\t" \
  "ds_read_b64_tr_b16 v[216:217], v35 offset:16384\n\t" \
  "s_waitcnt lgkmcnt(6)\n\t" \
  "v_mfma_f32_16x16x32_bf16 v[224:227], v[196:199], v[100:103], v[224:227]\n\t" \
  "ds_read_b64_tr_b16 v[218:219], v35 offset:16896\n\t" \
  "s_waitcnt vmcnt(16)\n\t" \
  "v_mfma_f32_16x16x32_bf16 v[228:231], v[196:199], v[108:111], v[228:231]\n\t" \
  "ds_read_b64_tr_b16 v[220:221], v35 offset:20480\n\t" \
  "s_lshr_b64 s[90:91], %18, 60\n\t" \
  "s_and_b32 s90, s90, 15\n\t" \
  "s_lshl_b32 s90, s90, 14\n\t" \
  "s_add_u32 s90, s90, s92\n\t" \
  "s_addc_u32 s91, s93, 0\n\t" \
  "global_load_dwordx4 v[176:179], v12, s[90:91] offset:0 sc1\n\t" \
  "v_mfma_f32_16x16x32_bf16 v[228:231], v[192:195], v[104:107], v[228:231]\n\t" \
  "ds_read_b64_tr_b16 v[222:223], v35 offset:20992\n\t" \
  "v_mfma_f32_16x16x32_bf16 v[232:235], v[200:203], v[96:99], v[232:235]\n\t" \
  "ds_read_b64_tr_b16 v[192:193], v32 offset:24576\n\t" \
  "s_waitcnt lgkmcnt(6)\n\t" \
  "v_mfma_f32_16x16x32_bf16 v[232:235], v[204:207], v[100:103], v[232:235]\n\t" \
  "ds_read_b64_tr_b16 v[194:195], v32 offset:25088\n\t" \
  "v_mfma_f32_16x16x32_bf16 v[236:239], v[204:207], v[108:111], v[236:239]\n\t" \
  "ds_read_b64_tr_b16 v[196:197], v32 offset:28672\n\t" \
  "global_load_dwordx4 v[180:183], v12, s[90:91] offset:1024 sc1\n\t" \
  "v_mfma_f32_16x16x32_bf16 v[236:239], v[200:203], v[104:107], v[236:239]\n\t" \
  "ds_read_b64_tr_b16 v[198:199], v32 offset:29184\n\t" \
  "v_mfma_f32_16x16x32_bf16 v[240:243], v[208:211], v[96:99], v[240:243]\n\t" \
  "ds_read_b64_tr_b16 v[200:201], v33 offset:24576\n\t" \
  "s_waitcnt lgkmcnt(6)\n\t" \
  "v_mfma_f32_16x16x32_bf16 v[240:243], v[212:215], v[100:103], v[240:243]\n\t" \
  "509:\n\t" \
  "s_cbranch_execz 26b\n\t" \
  "ds_read_b64_tr_b16 v[202:203], v33 offset:25088\n\t" \
  "v_mfma_f32_16x16x32_bf16 v[244:247], v[212:215], v[108:111], v[244:247]\n\t" \
  "ds_read_b64_tr_b16 v[204:205], v33 offset:28672\n\t" \
  "global_load_dwordx4 v[184:187], v12, s[90:91] offset:2048 sc1\n\t" \
  "v_mfma_f32_16x16x32_bf16 v[244:247], v[208:211], v[104:107], v[244:247]\n\t" \
  "ds_read_b64_tr_b16 v[206:207], v33 offset:29184\n\t" \
  "v_mfma_f32_16x16x32_bf16 v[248:251], v[216:219], v[96:99], v[248:251]\n\t" \
  "ds_read_b64_tr_b16 v[208:209], v34 offset:24576\n\t" \
  "s_waitcnt lgkmcnt(6)\n\t" \
  "v_mfma_f32_16x16x32_bf16 v[248:251], v[220:223], v[100:103], v[248:251]\n\t" \
  "ds_read_b64_tr_b16 v[210:211], v34 offset:25088\n\t" \
  "v_mfma_f32_16x16x32_bf16 v[252:255], v[220:223], v[108:111], v[252:255]\n\t" \
  "ds_read_b64_tr_b16 v[212:213], v34 offset:28672\n\t" \
  "global_load_dwordx4 v[188:191], v12, s[90:91] offset:3072 sc1\n\t" \
  "v_mfma_f32_16x16x32_bf16 v[252:255], v[216:219], v[104:107], v[252:255]\n\t" \
  "ds_read_b64_tr_b16 v[214:215], v34 offset:29184\n\t" \
  "s_waitcnt vmcnt(18)\n\t" \
  "v_mfma_f32_16x16x32_bf16 v[224:227], v[192:195], v[112:115], v[224:227]\n\t" \
  "ds_read_b64_tr_b16 v[216:217], v35 offset:24576\n\t" \
  "s_waitcnt lgkmcnt(6)\n\t" \
  "v_mfma_f32_16x16x32_bf16 v[224:227], v[196:199], v[116:119], v[224:227]\n\t" \
  "ds_read_b64_tr_b16 v[218:219], v35 offset:25088\n\t" \
  "s_waitcnt vmcnt(16)\n\t" \
  "v_mfma_f32_16x16x32_bf16 v[228:231], v[196:199], v[124:127], v[228:231]\n\t" \
  "ds_read_b64_tr_b16 v[220:221], v35 offset:28672\n\t" \
  "v_mfma_f32_16x16x32_bf16 v[228:231], v[192:195], v[120:123], v[228:231]\n\t" \
  "ds_read_b64_tr_b16 v[222:223], v35 offset:29184\n\t" \
  "v_mfma_f32_16x16x32_bf16 v[232:235], v[200:203], v[112:115], v[232:235]\n\t" \
  "ds_read_b64_tr_b16 v[192:193], v32 offset:32768\n\t" \
  "s_waitcnt lgkmcnt(6)\n\t" \
  "v_mfma_f32_16x16x32_bf16 v[232:235], v[204:207], v[116:119], v[232:235]\n\t" \
  "ds_read_b64_tr_b16 v[194:195], v32 offset:33280\n\t" \
  "v_mfma_f32_16x16x32_bf16 v[236:239], v[204:207], v[124:127], v[236:239]\n\t" \
  "ds_read_b64_tr_b16 v[196:197], v32 offset:36864\n\t" \
  "v_mfma_f32_16x16x32_bf16 v[236:239], v[200:203], v[120:123], v[236:239]\n\t" \
  "ds_read_b64_tr_b16 v[198:199], v32 offset:37376\n\t" \
  "v_mfma_f32_16x16x32_bf16 v[240:243], v[208:211], v[112:115], v[240:243]\n\t" \
  "ds_read_b64_tr_b16 v[200:201], v33 offset:32768\n\t" \
  "s_waitcnt lgkmcnt(6)\n\t" \
  "v_mfma_f32_16x16x32_bf16 v[240:243], v[212:215], v[116:119], v[240:243]\n\t" \
  "ds_read_b128 v[40:43], v56 offset:80\n\t" \
  "ds_read_b64_tr_b16 v[202:203], v33 offset:33280\n\t" \
  "v_mfma_f32_16x16x32_bf16 v[244:247], v[212:215], v[124:127], v[244:247]\n\t" \
  "ds_read_b64_tr_b16 v[204:205], v33 offset:36864\n\t" \
  "v_mfma_f32_16x16x32_bf16 v[244:247], v[208:211], v[120:123], v[244:247]\n\t" \
  "ds_read_b64_tr_b16 v[206:207], v33 offset:37376\n\t" \
  "v_mfma_f32_16x16x32_bf16 v[248:251], v[216:219], v[112:115], v[248:251]\n\t" \
  "ds_read_b64_tr_b16 v[208:209], v34 offset:32768\n\t" \
  "s_waitcnt lgkmcnt(7)\n\t" \
  "v_mfma_f32_16x16x32_bf16 v[248:251], v[220:223], v[116:119], v[248:251]\n\t" \
  "ds_read_b64_tr_b16 v[210:211], v34 offset:33280\n\t" \
  "v_mfma_f32_16x16x32_bf16 v[252:255], v[220:223], v[124:127], v[252:255]\n\t" \
  "ds_read_b64_tr_b16 v[212:213], v34 offset:36864\n\t" \
  "v_mfma_f32_16x16x32_bf16 v[252:255], v[216:219], v[120:123], v[252:255]\n\t" \
  "ds_read_b64_tr_b16 v[214:215], v34 offset:37376\n\t" \
  "s_waitcnt vmcnt(14)\n\t" \
  "v_mfma_f32_16x16x32_bf16 v[224:227], v[192:195], v[128:131], v[224:227]\n\t" \
  "s_waitcnt lgkmcnt(7)\n\t" \
  "v_min_u32 v40, v40, v41\n\t" \
  "v_min3_u32 v40, v40, v42, v43\n\t" \
  "ds_read_b64_tr_b16 v[216:217], v35 offset:32768\n\t" \
  "v_mfma_f32_16x16x32_bf16 v[224:227], v[196:199], v[132:135], v[224:227]\n\t" \
  "v_readfirstlane_b32 vcc_lo, v40\n\t" \
  "ds_read_b64_tr_b16 v[218:219], v35 offset:33280\n\t" \
  "s_waitcnt vmcnt(12)\n\t" \
  "v_mfma_f32_16x16x32_bf16 v[228:231], v[196:199], v[140:143], v[228:231]\n\t" \
  "s_cmp_ge_u32 vcc_lo, 16\n\t" \
  "s_cbranch_scc0 37f\n\t" \
  "38:\n\t" \
  "ds_read_b64_tr_b16 v[220:221], v35 offset:36864\n\t" \
  "v_mfma_f32_16x16x32_bf16 v[228:231], v[192:195], v[136:139], v[228:231]\n\t" \
  "ds_read_b64_tr_b16 v[222:223], v35 offset:37376\n\t" \
  "s_waitcnt lgkmcnt(6)\n\t" \
  "v_mfma_f32_16x16x32_bf16 v[232:235], v[200:203], v[128:131], v[232:235]\n\t" \
  "ds_read_b64_tr_b16 v[192:193], v36 offset:0\n\t" \
  "v_mfma_f32_16x16x32_bf16 v[232:235], v[204:207], v[132:135], v[232:235]\n\t" \
  "ds_read_b64_tr_b16 v[194:195], v36 offset:512\n\t" \
  "v_mfma_f32_16x16x32_bf16 v[236:239], v[204:207], v[140:143], v[236:239]\n\t" \
  "ds_read_b64_tr_b16 v[196:197], v36 offset:4096\n\t" \
  "v_mfma_f32_16x16x32_bf16 v[236:239], v[200:203], v[136:139], v[236:239]\n\t" \
  "ds_read_b64_tr_b16 v[198:199], v36 offset:4608\n\t" \
  "v_mfma_f32_16x16x32_bf16 v[240:243], v[208:211], v[128:131], v[240:243]\n\t" \
  "ds_read_b64_tr_b16 v[200:201], v37 offset:0\n\t" \
  "s_waitcnt lgkmcnt(6)\n\t" \
  "v_mfma_f32_16x16x32_bf16 v[240:243], v[212:215], v[132:135], v[240:243]\n\t" \
  "ds_read_b64_tr_b16 v[202:203], v37 offset:512\n\t" \
  "v_mfma_f32_16x16x32_bf16 v[244:247], v[212:215], v[140:143], v[244:247]\n\t" \
  "ds_read_b64_tr_b16 v[204:205], v37 offset:4096\n\t" \
  "v_mfma_f32_16x16x32_bf16 v[244:247], v[208:211], v[136:139], v[244:247]\n\t" \
  "ds_read_b64_tr_b16 v[206:207], v37 offset:4608\n\t" \
  "v_mfma_f32_16x16x32_bf16 v[248:251], v[216:219], v[128:131], v[248:251]\n\t" \
  "ds_read_b64_tr_b16 v[208:209], v38 offset:0\n\t" \
  "s_waitcnt lgkmcnt(6)\n\t" \
  "v_mfma_f32_16x16x32_bf16 v[248:251], v[220:223], v[132:135], v[248:251]\n\t" \
  "ds_read_b64_tr_b16 v[210:211], v38 offset:512\n\t" \
  "v_mfma_f32_16x16x32_bf16 v[252:255], v[220:223], v[140:143], v[252:255]\n\t" \
  "ds_read_b64_tr_b16 v[212:213], v38 offset:4096\n\t" \
  "v_mfma_f32_16x16x32_bf16 v[252:255], v[216:219], v[136:139], v[252:255]\n\t" \
  "ds_read_b64_tr_b16 v[214:215], v38 offset:4608\n\t" \
  "s_waitcnt vmcnt(10)\n\t" \
  "v_mfma_f32_16x16x32_bf16 v[224:227], v[192:195], v[144:147], v[224:227]\n\t" \
  "ds_read_b64_tr_b16 v[216:217], v39 offset:0\n\t" \
  "s_waitcnt lgkmcnt(6)\n\t" \
  "v_mfma_f32_16x16x32_bf16 v[224:227], v[196:199], v[148:151], v[224:227]\n\t" \
  "ds_read_b64_tr_b16 v[218:219], v39 offset:512\n\t" \
  "s_waitcnt vmcnt(8)\n\t" \
  "v_mfma_f32_16x16x32_bf16 v[228:231], v[196:199], v[156:159], v[228:231]\n\t" \
  "ds_read_b64_tr_b16 v[220:221], v39 offset:4096\n\t" \
  "v_mfma_f32_16x16x32_bf16 v[228:231], v[192:195], v[152:155], v[228:231]\n\t" \
  "ds_read_b64_tr_b16 v[222:223], v39 offset:4608\n\t" \
  "v_mfma_f32_16x16x32_bf16 v[232:235], v[200:203], v[144:147], v[232:235]\n\t" \
  "ds_read_b64_tr_b16 v[192:193], v36 offset:8192\n\t" \
  "s_waitcnt lgkmcnt(6)\n\t" \
  "v_mfma_f32_16x16x32_bf16 v[232:235], v[204:207], v[148:151], v[232:235]\n\t" \
  "ds_read_b64_tr_b16 v[194:195], v36 offset:8704\n\t" \
  "v_mfma_f32_16x16x32_bf16 v[236:239], v[204:207], v[156:159], v[236:239]\n\t" \
  "ds_read_b64_tr_b16 v[196:197], v36 offset:12288\n\t" \
  "v_mfma_f32_16x16x32_bf16 v[236:239], v[200:203], v[152:155], v[236:239]\n\t" \
  "ds_read_b64_tr_b16 v[198:199], v36 offset:12800\n\t" \
  "v_mfma_f32_16x16x32_bf16 v[240:243], v[208:211], v[144:147], v[240:243]\n\t" \
  "ds_read_b64_tr_b16 v[200:201], v37 offset:8192\n\t" \
  "s_waitcnt lgkmcnt(6)\n\t" \
  "v_mfma_f32_16x16x32_bf16 v[240:243], v[212:215], v[148:151], v[240:243]\n\t" \
  "ds_read_b128 v[40:43], v56 offset:160\n\t" \
  "ds_read_b64_tr_b16 v[202:203], v37 offset:8704\n\t" \
  "v_mfma_f32_16x16x32_bf16 v[244:247], v[212:215], v[156:159], v[244:247]\n\t" \
  "ds_read_b64_tr_b16 v[204:205], v37 offset:12288\n\t" \
  "v_mfma_f32_16x16x32_bf16 v[244:247], v[208:211], v[152:155], v[244:247]\n\t" \
  "ds_read_b64_tr_b16 v[206:207], v37 offset:12800\n\t" \
  "v_mfma_f32_16x16x32_bf16 v[248:251], v[216:219], v[144:147], v[248:251]\n\t" \
  "ds_read_b64_tr_b16 v[208:209], v38 offset:8192\n\t" \
  "s_waitcnt lgkmcnt(7)\n\t" \
  "v_mfma_f32_16x16x32_bf16 v[248:251], v[220:223], v[148:151], v[248:251]\n\t" \
  "ds_read_b64_tr_b16 v[210:211], v38 offset:8704\n\t" \
  "v_mfma_f32_16x16x32_bf16 v[252:255], v[220:223], v[156:159], v[252:255]\n\t" \
  "ds_read_b64_tr_b16 v[212:213], v38 offset:12288\n\t" \
  "v_mfma_f32_16x16x32_bf16 v[252:255], v[216:219], v[152:155], v[252:255]\n\t" \
  "ds_read_b64_tr_b16 v[214:215], v38 offset:12800\n\t" \
  "s_waitcnt vmcnt(6)\n\t" \
  "v_mfma_f32_16x16x32_bf16 v[224:227], v[192:195], v[160:163], v[224:227]\n\t" \
  "s_waitcnt lgkmcnt(7)\n\t" \
  "v_min_u32 v40, v40, v41\n\t" \
  "v_min3_u32 v40, v40, v42, v43\n\t" \
  "ds_read_b64_tr_b16 v[216:217], v39 offset:8192\n\t" \
  "v_mfma_f32_16x16x32_bf16 v[224:227], v[196:199], v[164:167], v[224:227]\n\t" \
  "v_readfirstlane_b32 vcc_lo, v40\n\t" \
  "ds_read_b64_tr_b16 v[218:219], v39 offset:8704\n\t" \
  "s_waitcnt vmcnt(4)\n\t" \
  "v_mfma_f32_16x16x32_bf16 v[228:231], v[196:199], v[172:175], v[228:231]\n\t" \
  "s_cmp_ge_u32 vcc_lo, 16\n\t" \
  "s_cbranch_scc0 39f\n\t" \
  "40:\n\t" \
  "ds_read_b64_tr_b16 v[220:221], v39 offset:12288\n\t" \
  "v_mfma_f32_16x16x32_bf16 v[228:231], v[192:195], v[168:171], v[228:231]\n\t" \
  "ds_read_b64_tr_b16 v[222:223], v39 offset:12800\n\t" \
  "s_waitcnt lgkmcnt(6)\n\t" \
  "v_mfma_f32_16x16x32_bf16 v[232:235], v[200:203], v[160:163], v[232:235]\n\t" \
  "ds_read_b64_tr_b16 v[192:193], v32 offset:45312\n\t" \
  "v_mfma_f32_16x16x32_bf16 v[232:235], v[204:207], v[164:167], v[232:235]\n\t" \
  "ds_read_b64_tr_b16 v[194:195], v32 offset:45824\n\t" \
  "v_mfma_f32_16x16x32_bf16 v[236:239], v[204:207], v[172:175], v[236:239]\n\t" \
  "ds_read_b64_tr_b16 v[196:197], v32 offset:53760\n\t" \
  "v_mfma_f32_16x16x32_bf16 v[236:239], v[200:203], v[168:171], v[236:239]\n\t" \
  "ds_read_b64_tr_b16 v[198:199], v32 offset:54272\n\t" \
  "v_mfma_f32_16x16x32_bf16 v[240:243], v[208:211], v[160:163], v[240:243]\n\t" \
  "ds_read_b64_tr_b16 v[200:201], v33 offset:45312\n\t" \
  "s_waitcnt lgkmcnt(6)\n\t" \
  "v_mfma_f32_16x16x32_bf16 v[240:243], v[212:215], v[164:167], v[240:243]\n\t" \
  "ds_read_b64_tr_b16 v[202:203], v33 offset:45824\n\t" \
  "v_mfma_f32_16x16x32_bf16 v[244:247], v[212:215], v[172:175], v[244:247]\n\t" \
  "ds_read_b64_tr_b16 v[204:205], v33 offset:53760\n\t" \
  "v_mfma_f32_16x16x32_bf16 v[244:247], v[208:211], v[168:171], v[244:247]\n\t" \
  "ds_read_b64_tr_b16 v[206:207], v33 offset:54272\n\t" \
  "v_mfma_f32_16x16x32_bf16 v[248:251], v[216:219], v[160:163], v[248:251]\n\t" \
  "ds_read_b64_tr_b16 v[208:209], v34 offset:45312\n\t" \
  "s_waitcnt lgkmcnt(6)\n\t" \
  "v_mfma_f32_16x16x32_bf16 v[248:251], v[220:223], v[164:167], v[248:251]\n\t" \
  "ds_read_b64_tr_b16 v[210:211], v34 offset:45824\n\t" \
  "v_mfma_f32_16x16x32_bf16 v[252:255], v[220:223], v[172:175], v[252:255]\n\t" \
  "ds_read_b64_tr_b16 v[212:213], v34 offset:53760\n\t" \
  "v_mfma_f32_16x16x32_bf16 v[252:255], v[216:219], v[168:171], v[252:255]\n\t" \
  "ds_read_b64_tr_b16 v[214:215], v34 offset:54272\n\t" \
  "s_waitcnt vmcnt(2)\n\t" \
  "v_mfma_f32_16x16x32_bf16 v[224:227], v[192:195], v[176:179], v[224:227]\n\t" \
  "ds_read_b64_tr_b16 v[216:217], v35 offset:45312\n\t" \
  "s_waitcnt lgkmcnt(6)\n\t" \
  "v_mfma_f32_16x16x32_bf16 v[224:227], v[196:199], v[180:183], v[224:227]\n\t" \
  "ds_read_b64_tr_b16 v[218:219], v35 offset:45824\n\t" \
  "s_waitcnt vmcnt(0)\n\t" \
  "v_mfma_f32_16x16x32_bf16 v[228:231], v[196:199], v[188:191], v[228:231]\n\t" \
  "ds_read_b64_tr_b16 v[220:221], v35 offset:53760\n\t" \
  "v_mfma_f32_16x16x32_bf16 v[228:231], v[192:195], v[184:187], v[228:231]\n\t" \
  "ds_read_b64_tr_b16 v[222:223], v35 offset:54272\n\t" \
  "v_mfma_f32_16x16x32_bf16 v[232:235], v[200:203], v[176:179], v[232:235]\n\t" \
  "s_waitcnt lgkmcnt(5)\n\t" \
  "v_mfma_f32_16x16x32_bf16 v[232:235], v[204:207], v[180:183], v[232:235]\n\t" \
  "v_mfma_f32_16x16x32_bf16 v[236:239], v[204:207], v[188:191], v[236:239]\n\t" \
  "v_cvt_pk_bf16_f32 v224, v224, v225\n\t" \
  "v_cvt_pk_bf16_f32 v225, v226, v227\n\t" \
  "v_mfma_f32_16x16x32_bf16 v[236:239], v[200:203], v[184:187], v[236:239]\n\t" \
  "ds_write_b64 v57, v[224:225] offset:0\n\t" \
  "v_mfma_f32_16x16x32_bf16 v[240:243], v[208:211], v[176:179], v[240:243]\n\t" \
  "v_cvt_pk_bf16_f32 v228, v228, v229\n\t" \
  "v_cvt_pk_bf16_f32 v229, v230, v231\n\t" \
  "s_waitcnt lgkmcnt(2)\n\t" \
  "v_mfma_f32_16x16x32_bf16 v[240:243], v[212:215], v[180:183], v[240:243]\n\t" \
  "ds_write_b64 v57, v[228:229] offset:2048\n\t" \
  "v_mfma_f32_16x16x32_bf16 v[244:247], v[212:215], v[188:191], v[244:247]\n\t" \
  "v_cvt_pk_bf16_f32 v232, v232, v233\n\t" \
  "v_cvt_pk_bf16_f32 v233, v234, v235\n\t" \
  "v_mfma_f32_16x16x32_bf16 v[244:247], v[208:211], v[184:187], v[244:247]\n\t" \
  "ds_write_b64 v58, v[232:233] offset:0\n\t" \
  "v_mfma_f32_16x16x32_bf16 v[248:251], v[216:219], v[176:179], v[248:251]\n\t" \
  "v_cvt_pk_bf16_f32 v236, v236, v237\n\t" \
  "v_cvt_pk_bf16_f32 v237, v238, v239\n\t" \
  "s_waitcnt lgkmcnt(3)\n\t" \
  "v_mfma_f32_16x16x32_bf16 v[248:251], v[220:223], v[180:183], v[248:251]\n\t" \
  "ds_write_b64 v58, v[236:237] offset:2048\n\t" \
  "v_mfma_f32_16x16x32_bf16 v[252:255], v[220:223], v[188:191], v[252:255]\n\t" \
  "v_cvt_pk_bf16_f32 v240, v240, v241\n\t" \
  "v_cvt_pk_bf16_f32 v241, v242, v243\n\t" \
  "v_mfma_f32_16x16x32_bf16 v[252:255], v[216:219], v[184:187], v[252:255]\n\t" \
  "ds_write_b64 v59, v[240:241] offset:0\n\t" \
  "v_cvt_pk_bf16_f32 v244, v244, v245\n\t" \
  "v_cvt_pk_bf16_f32 v245, v246, v247\n\t" \
  "ds_write_b64 v59, v[244:245] offset:2048\n\t" \
  "v_cvt_pk_bf16_f32 v248, v248, v249\n\t" \
  "v_cvt_pk_bf16_f32 v249, v250, v251\n\t" \
  "ds_write_b64 v60, v[248:249] offset:0\n\t" \
  "s_nop 0\n\t" \
  "v_cvt_pk_bf16_f32 v252, v252, v253\n\t" \
  "v_cvt_pk_bf16_f32 v253, v254, v255\n\t" \
  "ds_write_b64 v60, v[252:253] offset:2048\n\t" \
  "ds_read_b128 v[64:67], v61 offset:0\n\t" \
  "ds_read_b128 v[68:71], v61 offset:1024\n\t" \
  "ds_read_b128 v[72:75], v61 offset:2048\n\t" \
  "ds_read_b128 v[76:79], v61 offset:3072\n\t" \
  "s_waitcnt lgkmcnt(3)\n\t" \
  "global_store_dwordx4 v62, v[64:67], %22 offset:-4096" OUTPOL "\n\t" \
  "s_waitcnt lgkmcnt(2)\n\t" \
  "global_store_dwordx4 v63, v[68:71], %22 offset:-4096" OUTPOL "\n\t" \
  "s_waitcnt lgkmcnt(1)\n\t" \
  "global_store_dwordx4 v62, v[72:75], %22 offset:0" OUTPOL "\n\t" \
  "s_waitcnt lgkmcnt(0)\n\t" \
  "global_store_dwordx4 v63, v[76:79], %22 offset:0" OUTPOL "\n\t" \
  "s_branch 19f\n\t" \
  "7:\n\t" \
  "s_sleep 1\n\t" \
  "ds_read_b128 v[60:63], v56 offset:64\n\t" \
  "ds_read_b64_tr_b16 v[108:109], v5 offset:0\n\t" \
  "ds_read_b64_tr_b16 v[110:111], v5 offset:16\n\t" \
  "ds_read_b128 v[112:115], v6 offset:16384\n\t" \
  "ds_read_b128 v[116:119], v6 offset:15360\n\t" \
  "ds_read_b64_tr_b16 v[120:121], v5 offset:512\n\t" \
  "ds_read_b64_tr_b16 v[122:123], v5 offset:528\n\t" \
  "ds_read_b128 v[124:127], v6 offset:14336\n\t" \
  "ds_read_b64_tr_b16 v[128:129], v5 offset:1024\n\t" \
  "ds_read_b64_tr_b16 v[130:131], v5 offset:1040\n\t" \
  "ds_read_b128 v[132:135], v6 offset:13312\n\t" \
  "ds_read_b64_tr_b16 v[136:137], v5 offset:1536\n\t" \
  "ds_read_b64_tr_b16 v[138:139], v5 offset:1552\n\t" \
  "ds_read_b128 v[140:143], v6 offset:12288\n\t" \
  "ds_read_b64_tr_b16 v[144:145], v5 offset:2048\n\t" \
  "s_waitcnt lgkmcnt(0)\n\t" \
  "v_and_b32 v60, v60, v61\n\t" \
  "v_and_b32 v62, v62, v63\n\t" \
  "v_mov_b32 v12, %13\n\t" \
  "v_and_b32 v60, v60, v62\n\t" \
  "s_nop 0\n\t" \
  "v_readfirstlane_b32 vcc_lo, v60\n\t" \
  "s_nop 1\n\t" \
  "s_cmp_eq_u32 vcc_lo, 0\n\t" \
  "s_cbranch_scc1 7b\n\t" \
  "s_branch 6b\n\t" \
  "25:\n\t" \
  "510:\n\t" \
  "s_cbranch_execz 26b\n\t" \
  "global_load_dwordx4 v[64:67], v12, s[90:91] offset:0 sc1\n\t" \
  "global_load_dwordx4 v[68:71], v12, s[90:91] offset:1024 sc1\n\t" \
  "global_load_dwordx4 v[72:75], v12, s[90:91] offset:2048 sc1\n\t" \
  "global_load_dwordx4 v[76:79], v12, s[90:91] offset:3072 sc1\n\t" \
  "s_swappc_b64 s[70:71], s[68:69]\n\t" \
  "global_load_dwordx4 v[80:83], v12, s[90:91] offset:0 sc1\n\t" \
  "global_load_dwordx4 v[84:87], v12, s[90:91] offset:1024 sc1\n\t" \
  "global_load_dwordx4 v[88:91], v12, s[90:91] offset:2048 sc1\n\t" \
  "global_load_dwordx4 v[92:95], v12, s[90:91] offset:3072 sc1\n\t" \
  "s_swappc_b64 s[70:71], s[68:69]\n\t" \
  "global_load_dwordx4 v[96:99], v12, s[90:91] offset:0 sc1\n\t" \
  "global_load_dwordx4 v[100:103], v12, s[90:91] offset:1024 sc1\n\t" \
  "global_load_dwordx4 v[104:107], v12, s[90:91] offset:2048 sc1\n\t" \
  "global_load_dwordx4 v[108:111], v12, s[90:91] offset:3072 sc1\n\t" \
  "s_swappc_b64 s[70:71], s[68:69]\n\t" \
  "global_load_dwordx4 v[112:115], v12, s[90:91] offset:0 sc1\n\t" \
  "global_load_dwordx4 v[116:119], v12, s[90:91] offset:1024 sc1\n\t" \
  "global_load_dwordx4 v[120:123], v12, s[90:91] offset:2048 sc1\n\t" \
  "global_load_dwordx4 v[124:127], v12, s[90:91] offset:3072 sc1\n\t" \
  "s_swappc_b64 s[70:71], s[68:69]\n\t" \
  "35:\n\t" \
  "ds_read_b128 v[40:43], v56 offset:80\n\t" \
  "s_waitcnt lgkmcnt(0)\n\t" \
  "v_min_u32 v40, v40, v41\n\t" \
  "v_min3_u32 v40, v40, v42, v43\n\t" \
  "s_nop 1\n\t" \
  "v_readfirstlane_b32 vcc_lo, v40\n\t" \
  "s_nop 1\n\t" \
  "s_cmp_ge_u32 vcc_lo, 13\n\t" \
  "s_cbranch_scc0 35b\n\t" \
  "s_branch 36b\n\t" \
  "37:\n\t" \
  "ds_read_b128 v[40:43], v56 offset:80\n\t" \
  "s_waitcnt lgkmcnt(0)\n\t" \
  "v_min_u32 v40, v40, v41\n\t" \
  "v_min3_u32 v40, v40, v42, v43\n\t" \
  "s_nop 1\n\t" \
  "v_readfirstlane_b32 vcc_lo, v40\n\t" \
  "s_nop 1\n\t" \
  "s_cmp_ge_u32 vcc_lo, 16\n\t" \
  "s_cbranch_scc0 37b\n\t" \
  "s_branch 38b\n\t" \
  "39:\n\t" \
  "ds_read_b128 v[40:43], v56 offset:160\n\t" \
  "s_waitcnt lgkmcnt(0)\n\t" \
  "v_min_u32 v40, v40, v41\n\t" \
  "v_min3_u32 v40, v40, v42, v43\n\t" \
  "s_nop 1\n\t" \
  "v_readfirstlane_b32 vcc_lo, v40\n\t" \
  "s_nop 1\n\t" \
  "s_cmp_ge_u32 vcc_lo, 16\n\t" \
  "s_cbranch_scc0 39b\n\t" \
  "s_branch 40b\n\t" \
  "43:\n\t" \
  "s_cmp_eq_u32 %13, 0\n\t" \
  "s_cbranch_scc1 45f\n\t" \
  "s_sleep 1\n\t" \
  "ds_read_b32 v16, v56 offset:96\n\t" \
  "s_waitcnt lgkmcnt(0)\n\t" \
  "v_readfirstlane_b32 vcc_lo, v16\n\t" \
  "s_nop 1\n\t" \
  "s_cmp_ge_u32 vcc_lo, 1\n\t" \
  "s_cbranch_scc0 43b\n\t" \
  "s_branch 44b\n\t" \
  "45:\n\t" \
  "v_bfe_u32 v19, v12, 4, 4\n\t" \
  "v_lshlrev_b32 v19, 2, v19\n\t" \
  "s_mov_b32 s90, 0\n\t" \
  "145:\n\t" \
  "global_load_dword v16, v19, %21 sc1\n\t" \
  "s_add_u32 s90, s90, 1\n\t" \
  "s_waitcnt vmcnt(0)\n\t" \
  "v_cmp_ne_u32 vcc, v55, v16\n\t" \
  "s_mov_b32 s88, 0xf7f7f7f7\n\t" \
  "s_mov_b32 s89, s88\n\t" \
  "s_and_b64 vcc, vcc, s[88:89]\n\t" \
  "s_cbranch_scc0 245f\n\t" \
  "s_sleep 2\n\t" \
  "s_cmp_lt_u32 s90, 0x400000\n\t" \
  "s_cbranch_scc1 145b\n\t" \
  "245:\n\t" \
  "v_mov_b32 v16, 1\n\t" \
  "ds_max_u32 v56, v16 offset:96\n\t" \
  "s_branch 44b\n\t" \
  "46:\n\t" \
  "s_cmp_eq_u32 %13, 0\n\t" \
  "s_cbranch_scc1 48f\n\t" \
  "s_sleep 1\n\t" \
  "ds_read_b32 v16, v56 offset:96\n\t" \
  "s_waitcnt lgkmcnt(0)\n\t" \
  "v_readfirstlane_b32 vcc_lo, v16\n\t" \
  "s_nop 1\n\t" \
  "s_cmp_ge_u32 vcc_lo, 2\n\t" \
  "s_cbranch_scc0 46b\n\t" \
  "s_branch 47b\n\t" \
  "48:\n\t" \
  "v_bfe_u32 v19, v12, 4, 4\n\t" \
  "v_lshlrev_b32 v19, 2, v19\n\t" \
  "s_mov_b32 s90, 0\n\t" \
  "148:\n\t" \
  "global_load_dword v16, v19, %21 sc1\n\t" \
  "s_add_u32 s90, s90, 1\n\t" \
  "s_waitcnt vmcnt(0)\n\t" \
  "v_cmp_ne_u32 vcc, v55, v16\n\t" \
  "s_mov_b32 s88, 0xf7f7f7f7\n\t" \
  "s_mov_b32 s89, s88\n\t" \
  "s_and_b64 vcc, vcc, -1\n\t" \
  "s_cbranch_scc0 248f\n\t" \
  "s_sleep 2\n\t" \
  "s_cmp_lt_u32 s90, 0x400000\n\t" \
  "s_cbranch_scc1 148b\n\t" \
  "248:\n\t" \
  "v_mov_b32 v16, 2\n\t" \
  "ds_max_u32 v56, v16 offset:96\n\t" \
  "s_branch 47b\n\t" \
  "19:\n\t"
#define C_CLOB "v10","v11","v12","v13","v18","v19","v20","v21","v22","v23","v24","v25","v26","v27","v28","v29","v30","v31","v32","v33","v34","v35","v36","v37","v38","v39","v40","v41","v42","v43","v44","v45","v46","v47","v48","v49","v50","v53","v55","v56","v57","v58","v59","v60","v61","v62","v63","v64","v65","v66","v67","v68","v69","v70","v71","v72","v73","v74","v75","v76","v77","v78","v79","v80","v81","v82","v83","v84","v85","v86","v87","v88","v89","v90","v91","v92","v93","v94","v95","v96","v97","v98","v99","v100","v101","v102","v103","v104","v105","v106","v107","v108","v109","v110","v111","v112","v113","v114","v115","v116","v117","v118","v119","v120","v121","v122","v123","v124","v125","v126","v127","v128","v129","v130","v131","v132","v133","v134","v135","v136","v137","v138","v139","v140","v141","v142","v143","v144","v145","v146","v147","v148","v149","v150","v151","v152","v153","v154","v155","v156","v157","v158","v159","v160","v161","v162","v163","v164","v165","v166","v167","v168","v169","v170","v171","v172","v173","v174","v175","v176","v177","v178","v179","v180","v181","v182","v183","v184","v185","v186","v187","v188","v189","v190","v191","v192","v193","v194","v195","v196","v197","v198","v199","v200","v201","v202","v203","v204","v205","v206","v207","v208","v209","v210","v211","v212","v213","v214","v215","v216","v217","v218","v219","v220","v221","v222","v223","v224","v225","v226","v227","v228","v229","v230","v231","v232","v233","v234","v235","v236","v237","v238","v239","v240","v241","v242","v243","v244","v245","v246","v247","v248","v249","v250","v251","v252","v253","v254","v255"

static_assert(KVP<13 && KVP>TGS+1, "");

#define TS(i)
#define TSDUMP()
#define TSB(i)
#define TSR(i)
#define TSH(i)
#define TSDECL()

typedef unsigned long long u64;
typedef u64 u64x8 __attribute__((ext_vector_type(8)));
typedef u64 u64x4 __attribute__((ext_vector_type(4)));
typedef unsigned u32x8 __attribute__((ext_vector_type(8)));
typedef unsigned u32x16 __attribute__((ext_vector_type(16)));
template<class T> __device__ __forceinline__ T sg(T v){
  if constexpr(sizeof(T)==4) return (T)__builtin_amdgcn_readfirstlane((unsigned)v); else return v; }
#define GP(T,v) ((T*)(__attribute__((address_space(1))) T*)(v))
__global__ __launch_bounds__(512,2) __attribute__((aligned(4096))) void fused_kernel(const u16* x_, const u16* wqkv_, const u16* kin_, const u16* vin_, const u16* shk_, const u16* hk_,
    const u16* wo_, const u16* qin_, const u16* sD_, u16* out_, u16* ws_, u16* unused_)
{
#define ST0()
#define ST1()
#define ARGS0() u64x8 ka; u64x4 kb; asm volatile("s_load_dwordx16 %0, %2, 0\n\ts_load_dwordx8 %1, %2, 64" : "=&s"(ka), "=&s"(kb) : "s"(__builtin_amdgcn_kernarg_segment_ptr()) : "memory")
#define ARGS() asm volatile("s_waitcnt lgkmcnt(0)" : "+s"(ka), "+s"(kb) :: "memory"); ST1() \
  const u16 *x=GP(const u16,ka[0]), *wqkv=GP(const u16,ka[1]), *kin=GP(const u16,ka[2]), *vin=GP(const u16,ka[3]), *shk=GP(const u16,ka[4]), *hk=GP(const u16,ka[5]), *wo=GP(const u16,ka[6]), *qin=GP(const u16,ka[7]), *sD=GP(const u16,kb[0]); \
  u16 *out=GP(u16,kb[1]), *o=GP(u16,kb[2]), *xrT=o+(size_t)S*DM, *yT=o+(size_t)2*S*DM; unsigned* flags=(unsigned*)(o+(size_t)6*S*DM); \
  (void)x; (void)wqkv; (void)kin; (void)vin; (void)shk; (void)hk; (void)wo; (void)qin; (void)sD; (void)out; (void)xrT; (void)yT; (void)flags
  ST0()
  __shared__ __attribute__((aligned(1024))) char smem[ALL_LDS];
  const int tid=threadIdx.x;
  const int w8=__builtin_amdgcn_readfirstlane(tid>>6);
  asm volatile("s_cmp_lg_u32 %0, 7\n\ts_cbranch_scc1 1f\n\ts_lshr_b32 vcc_lo, %2, 3\n\ts_mul_i32 vcc_lo, vcc_lo, %3\n\tv_and_b32 v254, 15, %1\n\tv_lshl_add_u32 v254, v254, 7, vcc_lo\n\ts_getpc_b64 vcc\n\tglobal_load_dword v255, v254, vcc\n1:"
      :: "s"(w8), "v"(tid), "s"(blockIdx.x), "n"(CODE_ST) : "memory", "scc", "vcc", "v254", "v255");
  LDS_AS unsigned* const ct=(LDS_AS unsigned*)(LDS_AS char*)(smem+CT_OFF);
  volatile LDS_AS unsigned* const vct=ct;
  auto all4=[&](int i)->bool{ u32x4 v=*(volatile LDS_AS u32x4*)(ct+i); return __builtin_amdgcn_readfirstlane(v[0]&v[1]&v[2]&v[3])!=0; };
#define ROTF(b) ((4+4*xr+4*xc+8*(b))&15)
#define BLOCK() int bid=blockIdx.x, w=w8&3; asm volatile("" : "+s"(bid), "+s"(w)); __builtin_assume(bid>=0 && bid<256 && w>=0 && w<4); \
  const int xcd=bid&7, idx=bid>>3, ra=idx&3, rbb=idx>>2, xr=xcd>>1, xc=xcd&1, rb=xr*4+ra, hp=xc*8+rbb, s0=rb*128, tm=rb, tn=hp^TNX; (void)s0; \
  auto korder=[&]()->unsigned long long{ const int r=KR; const unsigned long long N=0xfedca987654210ull, m=(1ull<<56)-1; const int sh=4*(r>=14? r-14 : r); \
    return (((N>>sh)|(N<<(56-sh)))&m) | ((idx&4)? 0xb3ull<<56 : 0x3bull<<56); }

  if(__builtin_expect(w8>=4,0)){
  BLOCK(); constexpr int WB=4; (void)WB;
  const int hd=w>>1, par=w&1;
  __builtin_amdgcn_s_setprio(CPRIO);
  {
    ARGS0(); LANES();
    if(w==0){ if(l<44) ct[l]=0u; if(idx==0) *(u32x4*)(smem+AUX+l*16)=u32x4{0,0,0,0}; }
    unsigned a0b=fr*128+((g^(fr&7))*16);
    const int r0=8*g+tq;
    unsigned bbb=r0*128+(((2*hd)^wswz(r0))*32)+8*tp;
    unsigned kA=AUX+(fr^3)*KVP_+w*64+(g&1)*16-(g>>1)*256, kB=AUX+(20+4*g-tq)*KVP_+w*64+8*tp;
    const int cch=127-4*idx-g;
    unsigned kvo=(xcd*32+8*w+tq)*16384+(cch>>4)*2048+(cch&15)*64+((tp^((cch>>2)&3))*16);
    unsigned sho=(xcd*128+32*w+fr)*8; const unsigned ctl=CT_OFF+4*w;
    u32x4 sel;
    { const unsigned b2=(g>>1)? 0x0302u : 0x0100u, sv=(fr&1)? (b2<<16|0x0c0cu) : (0x0c0c0000u|b2);
      #pragma unroll
      for(int i=0;i<4;i++) sel[i]= (fr>>1)==(g&1)*4+i ? sv : 0x0c0c0c0cu; }
    unsigned msk=0, lx=w*1024+l*16;
    #pragma unroll
    for(int e=0;e<4;e++){ const int d=4*g+e-fr; const unsigned pv=d<9, hv=par? pv : (unsigned)(d<1 || 4*g+e>7);
      msk|=((unsigned)(d+7)>7u?1u:0u)<<e | pv<<(4+e) | hv<<(8+e) | (d<=0?1u:0u)<<(12+e); }
    msk|=(unsigned)(g? l-16 : 48+((l-1)&15))<<26;
    const unsigned hb=__builtin_amdgcn_readfirstlane(rb==0 ? 0xcfa8965fu : 0u);
    unsigned ap=AUX+w*CV_W+(tq&1)*(CV_CP-4)+8+32*g+4*tq+8*tp;
    u32x4 bp;
    #pragma unroll
    for(int b=0;b<4;b++){ const int c=15+4*b-fr+g; bp[b]=c*64+((w^((c>>2)&3))*16); }
    asm volatile("" : "+v"(a0b), "+v"(bbb), "+v"(kA), "+v"(kB), "+v"(kvo), "+v"(sho), "+v"(msk), "+v"(lx), "+v"(ap), "+v"(bp), "+v"(sel));
    ARGS();
    const u16* ow=o+((size_t)(rb*16+hp)*16+par*2+hd)*512+2048;
    asm volatile("" : "+s"(shk));
    asm volatile(C_ASM : "+{v52}"(a0b), "+{v54}"(bbb), "+{v0}"(kA), "+{v1}"(kB), "+{v2}"(kvo), "+{v51}"(sho), "+{v3}"(msk), "+{v4}"(lx), "+{v5}"(ap), "+{v[6:9]}"(bp), "+{v[14:17]}"(sel)
       : "s"(shk), "s"(ctl), "s"(w), "s"(ow), "s"(hb), "s"(0.17677669529663687f*1.44269504088896341f), "{s[92:93]}"(o+(size_t)tm*16*8192), "s"(korder()),
         "s"(flags+FL_O+rb*512+hp), "s"(AUX+w*(CV_W+64)), "s"(flags+FL_O+(tm*16+tn)*32),
         "s"(out+(size_t)(tm*128+w*32+1)*2048+1024+tn*64), "s"(w<2? 16384+w*4096 : AUX+4352+w*CV_W), "s"(xrT), "s"(flags+FL_KV+xcd*1024+idx) : "memory", "scc", "vcc", "s68", "s69", "s70", "s71", "s75", "s76", "s77", "s88", "s89", "s90", "s91",
       C_CLOB);
    TSDECL();
    TS(20); TSDUMP();
  }
  return;
  }
  {
    ARGS0(); BLOCK(); constexpr int WB=0; (void)WB; LANES(); TSDECL();
    const unsigned vx=(l>>3)*4096+(((l&7)^(l>>3))*16), vxs=vx+(s0+32*w)*2048, vw=(l>>3)*6144+((((l&7)>>1)^((l>>4)&1))*32)+(l&1)*16+hp*128+w*98304, vw1=(vw^64)+49152-1024;
    u32x8 va; u32x2 vb; unsigned vsp;
    #pragma unroll
    for(int i=0;i<6;i++) va[i]=((i&1)? vw1 : vw)+(i>>1)*2048;
    va[6]=vxs; va[7]=vxs+32768-1024; vb[0]=vxs+2048; vb[1]=vxs+2048+32768-1024;
    vsp=vx+(rb? s0-16+w : 0)*2048;
    const unsigned sw1=w*1024, sw2=w*2048;
    unsigned rot=ROTF(rbb);
    asm volatile("" : "+v"(va), "+v"(vb), "+v"(vsp));
    asm volatile("" : "+s"(rot));
    unsigned vt=(w&1)? (xr*512+idx*16+(l>>1))*2048+(l&1)*1024 : (2*idx+(l&1))*6144+((l>>1)&3)*2048+xc*1024;
    const int kt0=idx*64; int kvs=KS0+KVSEL;
    unsigned kvl=(kt0+16*w+g)*2048+xcd*256+fr*16, l16=l*16;
    const bool hl= w==0 && kt0;
    unsigned c46=sg(w<2? 128u : w==2? 393216u : 0u), c49=sg(w<2? sw1 : (unsigned)ALL_LDS-96), c64=sg(hl? 8192u : 0u), c68=sg(hl? AUX : ALL_LDS-96);
    unsigned long long c50= w<2? ~0ull : w==2? 63ull : 1ull, c66= hl? ~0ull : 1ull;
    asm volatile("" : "+v"(vt), "+v"(kvl), "+v"(l16));
    asm volatile("" : "+s"(c46), "+s"(c49), "+s"(c64), "+s"(c68), "+s"(c50), "+s"(c66), "+s"(kvs));
    ARGS();
    TSB(16);
    asm volatile(H0_ASM : "+{s[40:41]}"(x), "+{s[42:43]}"(wqkv) : "{v[0:7]}"(va), "{v[8:9]}"(vb), "{v10}"(vsp), "{s52}"(rot), "{s53}"(sw2), "{s59}"(w)
      : "memory", "scc", "s76", "s77", "s78", "s79", "s82", "s83");
    const u16* spb= w==2? wqkv : x;
    const int hl3=(3-(int)((size_t)spb>>7))&7, roth=ROTF(hl3), xs=((hl3-ROTF(0))&7)-LEAD;
    unsigned i11=sg((w==2? roth+LEAD+1 : rot+1)&15), sbo, sst;
    if(w>=2){
      vsp=vt+hl3*128;
      touch(spb, vsp+((roth+(l>>3))&15)*c46, w==2? 0x3f3f3f3f3f3f3f3full>>(56-8*LEAD) : xs<=1? 0xffffffffull : 1ull);
    }
    TSB(17);
    unsigned gof=(((bid>>4)*128+64*(w&1)+4*(w>>1)+16*((l>>5)&1)+((l>>3)&3))*DM+(bid&15)*64)*2+(l16&127);
    unsigned wv[2];
    wv[0]=(((l>>5)*16+w*4+((l>>3)&3))*DM+tn*64)*2+((l16&127)^((l&16)*2)^((w&1)*64)); wv[1]=wv[0]+32*DM*2;
    unsigned zpa=(w>>1)*9216+(w&1)*512+l16, hda=AUX+w*CV_W+(l&1)*CV_CP+(l>>1)*16, yra=AUX+tq*(CV_W+64)+(g>>1)*128+(g&1)*32+tp*8+w*1024;
    constexpr unsigned FLB=12*S*DM;
    unsigned vc=(w&1)? FLB+(g? (1024+(g-1)*8192+(g==2? tm*16+tn : bid)*32)*4 : 0) : idx*CODE_ST+fr*128u;
    unsigned kfo=FLB+(FL_KV+bid*32+(l&31))*4, ofo= w==0? FLB+(FL_O+(tm*16+tn)*32+fr)*4 : FLB;
    asm volatile("" : "+v"(kfo), "+v"(ofo), "+v"(gof), "+v"(wv[0]), "+v"(wv[1]), "+v"(hda), "+v"(yra), "+v"(zpa), "+v"(vc));
    unsigned long long kt=korder(); asm volatile("" : "+s"(kt));
#define HDBG_O
#define HDBG_I
#define H_IN "{v[0:7]}"(va), "{v[8:9]}"(vb), "{v10}"(vsp), "{v11}"(kvl), "{s[40:41]}"(x), "{s[42:43]}"(wqkv), "{s[44:45]}"(spb), "{s46}"(c46), "{s49}"(c49), "{s[50:51]}"(c50), "{s53}"(sw2), \
      "{s58}"(kvs), "{s59}"(w), "{s[60:61]}"(kin), "{s[62:63]}"(vin), "{s64}"(c64), "{s[66:67]}"(c66), "{s68}"(c68) HDBG_I
#define H_CLOB "memory", "scc", "vcc", "s76", "s77", "s78", "s79", "s80", "s81", "s82", "s83", "s84", "s85", "v20", "v21", "v22", "v253", "v254", "v255"
    asm volatile(H1_ASM : "+{s47}"(i11), "+{s52}"(rot), "={s54}"(sbo), "={s55}"(sst) HDBG_O : H_IN : H_CLOB);
    const u16* hrow=hk+(size_t)(bid*4+w)*S;
    u32x4 h0; unsigned dvu;
    asm volatile("s_load_dwordx4 %0, %2, 0\n\ts_load_dword %1, %3, %4" : "=&s"(h0), "=&s"(dvu) : "s"(hrow), "s"(sD), "s"(bid*8+(w&2)*2) : "memory");
    unsigned long long pc; asm volatile("s_getpc_b64 %0" : "=s"(pc)); pc&=~4095ull;
    __builtin_amdgcn_s_setprio(HP1);
    asm volatile(H_ASM : "+{s47}"(i11), "+{s52}"(rot), "+{s54}"(sbo), "+{s55}"(sst) HDBG_O
      : H_IN, "{v14}"(vc), "{v16}"(kfo), "{s56}"(sg(3u<<(kvs-1) | 1u<<15)),
        "{s57}"(sg(((w&1)? (1u<<TGS | 1u<<KVP | 1u<<14 | ((1u<<(xs+8)>>8)&~3u)) : 1u<<CPS)&~(unsigned)(c50>>32))), "{s[70:71]}"(o), "{s[72:73]}"(pc) : H_CLOB);
    asm volatile("s_waitcnt lgkmcnt(0)" : "+s"(h0), "+s"(dvu) :: "memory");
    u32x4 qv[4], wr[6];
    BAR(); TSB(14);
    __builtin_amdgcn_s_setprio(HP2);
    { const unsigned fa=AUX+w*CV_W+32;
      asm volatile("s_mov_b64 exec, -2\n\ts_add_u32 m0, %3, %4\n\ts_nop 0\n\tglobal_load_lds_dwordx4 %0, %1" LDPOL "\n\ts_mov_b32 m0, %3\n\ts_nop 0\n\tglobal_load_lds_dwordx4 %0, %2" LDPOL "\n\ts_mov_b64 exec, -1"
        :: "v"(l16), "s"(hrow), "s"(hrow-2), "s"(fa), "n"(CV_CP) : "memory", "scc");
      #pragma unroll
      for(int i=1;i<4;i++){ gldsn(hrow+i*512, l16, fa, CV_CP+i*1024); gldsn(hrow-2+i*512, l16, fa, i*1024); } }
    const unsigned tag=__builtin_amdgcn_readfirstlane(vct[26]); const bool kvok=__builtin_amdgcn_readfirstlane(vct[5])!=0;
    u32x4 hv;
    { const unsigned d0=(h0[0]&0xffff0000u)|(pack2(bflo(h0[0])+((w&1)? bfhi(dvu) : bflo(dvu)),0.f)&0xffffu);
      hv= l==4? u32x4{0,d0,h0[1],h0[2]} : l==5? u32x4{d0,h0[1],h0[2],h0[3]} : u32x4{0,0,0,0}; }
    asm volatile("s_mov_b64 exec, 63\n\tds_write_b128 %0, %1\n\ts_mov_b64 exec, %3\n\tds_write_b128 %2, %1 offset:7680\n\ts_mov_b64 exec, -1"
      :: "v"(hda), "v"(hv), "v"(zpa), "s"(0xffffffff00000000ull) : "memory");
#define QLD() asm volatile("global_load_dwordx4 %0, %4, %5" LDPOL "\n\tglobal_load_dwordx4 %1, %4, %6" LDPOL "\n\tglobal_load_dwordx4 %2, %4, %7" LDPOL "\n\tglobal_load_dwordx4 %3, %4, %8" LDPOL \
        : "=&v"(qv[0]), "=&v"(qv[1]), "=&v"(qv[2]), "=&v"(qv[3]) : "v"(gof), "s"(qin), "s"(qin+8*DM), "s"(qin+32*DM), "s"(qin+40*DM) : "memory")
    auto kvt=[&](){
      #pragma unroll
      for(int i=0;i<4;i++) gldsc(xrT+(size_t)bid*8192+w*2048+i*512, l16, sw2, (i&1)*9216+(i>>1)*1024); };
    WAITV(8);
    vct[32+w]=1u;
    TSH(6);
    if(!kvok) wait_flag((const unsigned*)((const char*)o+kfo), tag);
    kvt();
    TSH(7);
    auto wos=[&](int st){ const u16* b=wo+(size_t)((kt>>(4*st))&15)*64*DM; glds(b,wv[0],sw1,WOB+st*8192); glds(b,wv[1],sw1,WOB+st*8192+4096); };
    wos(0); wos(1);
    if(QP==0) QLD();
    WAITV(QP==0? 8:4); WAITL();
    vct[16+w]=1u;
    TSH(8);
    __builtin_amdgcn_s_setprio(HP4);
    if(QP==1) QLD();
    while(!all4(36)) __builtin_amdgcn_s_sleep(1);
    #pragma unroll
    for(int st=2;st<7;st++){ wos(st); WAITV(W1); }
    while(!all4(0)) __builtin_amdgcn_s_sleep(1);
    #pragma unroll
    for(int st=7;st<13;st++){ wos(st); WAITV(W2); }
    #pragma unroll
    for(int st=0;st<3;st++){
      asm volatile("global_load_dwordx4 %0, %2, %4\n\tglobal_load_dwordx4 %1, %3, %4" : "=&v"(wr[2*st]), "=&v"(wr[2*st+1]) : "v"(wv[0]), "v"(wv[1]), "s"(wo+(size_t)((kt>>(52+4*st))&15)*64*DM) : "memory");
      WAITV(W3); }
    unsigned op;
    asm volatile("global_load_dword %0, %1, %2 sc1" : "=v"(op) : "v"(ofo), "s"(o) : "memory");
    if(QP==2) QLD();
    WAITV(QP==2? 11:7); vct[20+w]=13u;
    TSH(9);
    unsigned oseen=0;
    auto olevel=[&](unsigned v){ const unsigned long long ne=__ballot(v!=tag); const unsigned lv= ne==0 ? 2u : (ne&0xf7f7f7f7f7f7f7f7ull)==0 ? 1u : 0u;
      if(lv>oseen){ if(oseen==0){ TSH(10); } oseen=lv; if(l==0) __hip_atomic_fetch_max(ct+24, lv, __ATOMIC_RELAXED, __HIP_MEMORY_SCOPE_WORKGROUP); } };
    asm volatile("s_waitcnt vmcnt(0)" : "+v"(wr[0]), "+v"(wr[1]), "+v"(wr[2]), "+v"(wr[3]), "+v"(wr[4]), "+v"(wr[5]),
                                        "+v"(qv[0]), "+v"(qv[1]), "+v"(qv[2]), "+v"(qv[3]), "+v"(op) :: "memory");
    if(w==0) olevel(op);
    __builtin_amdgcn_s_setprio(HP3);
    bool gen= !(bid==0 && w==1);
    auto trygen=[&](){
      unsigned v[4];
      #pragma unroll
      for(int i=0;i<4;i++) v[i]=__hip_atomic_load(flags+FL_KV+(i*2+(l>>5))*1024+(l&31), __ATOMIC_RELAXED, __HIP_MEMORY_SCOPE_AGENT);
      if(__all(v[0]==tag && v[1]==tag && v[2]==tag && v[3]==tag)){ if(l==0) st4c(flags, tag); gen=true; } };
    if(!gen) trygen();
    float qf[32];
    #pragma unroll
    for(int i=0;i<16;i++){ qf[2*i]=bflo(qv[i>>2][i&3]); qf[2*i+1]=bfhi(qv[i>>2][i&3]); }
    #pragma unroll
    for(int i=0;i<32;i+=8) asm volatile("" : "+v"(qf[i]), "+v"(qf[i+1]), "+v"(qf[i+2]), "+v"(qf[i+3]), "+v"(qf[i+4]), "+v"(qf[i+5]), "+v"(qf[i+6]), "+v"(qf[i+7]));
    while(!all4(8)){
      if(w==0 && oseen<2) olevel(__hip_atomic_load((const unsigned*)((const char*)o+ofo), __ATOMIC_RELAXED, __HIP_MEMORY_SCOPE_AGENT));
      __builtin_amdgcn_s_sleep(OPOLLS);
    }
    TSH(11);
    __builtin_amdgcn_s_setprio(3);
    {
      const char* yb=smem+yra;
      s16x4 ya[4], yc[4];
      #pragma unroll
      for(int i=0;i<4;i++){ ya[i]=trread(yb+(i>>1)*512+(i&1)*64); yc[i]=trread(yb+(i>>1)*512+(i&1)*64+256); }
      #pragma unroll
      for(int i=0;i<4;i++) if(NOY!=1) st16c(yT+((size_t)(w*4+i)*256+bid)*512, l16, __builtin_bit_cast(u32x4,__builtin_shufflevector(ya[i],yc[i],0,1,2,3,4,5,6,7)));
    }
    #pragma unroll
    for(int i=0;i<6;i++) *(u32x4*)(smem+(i<4? i*4096 : AUX+4352+(i&1)*CV_W)+w*1024+l16)=wr[i];
    WAITL();
    vct[20+w]=16u;
    TSH(12);
    if(NOY){ for(int sp_=0; !gen && sp_<(1<<22); sp_++) trygen(); TS(20); TSDUMP(); return; }
    unsigned po=gof+(gof&~2047u);
    const u16* p0=yT+((size_t)(bid>>4)*256+(bid&15)*16)*512;
    unsigned y0=g*1024+w*256+((fr^(g&2))*16);
    asm volatile("" : "+v"(y0), "+v"(po));
    WAITV(0);
    if(l<4) st4c(flags+FL_Y+((w*4+l)*16+(bid>>4))*32+(bid&15), tag);
    TSH(13);
    if(w==0){ int sp_=0; while(oseen<2 && vct[24]<2u && ++sp_<(1<<22)){ olevel(__hip_atomic_load((const unsigned*)((const char*)o+ofo), __ATOMIC_RELAXED, __HIP_MEMORY_SCOPE_AGENT)); __builtin_amdgcn_s_sleep(8); } }
    if(w==0){
      const unsigned* fp=flags+FL_Y+bid*32+(l&15); int spins=0;
      while(vct[27]==0u) __builtin_amdgcn_s_sleep(2);
      while(true){
        unsigned v=__hip_atomic_load(fp, __ATOMIC_RELAXED, __HIP_MEMORY_SCOPE_AGENT);
        if(__all(v==tag) || ++spins>(1<<20)) break;
        __builtin_amdgcn_s_sleep(POLLS);
      }
      { u32x4 v; do{ v=*(volatile LDS_AS u32x4*)(ct+20); }while(__builtin_amdgcn_readfirstlane(v[0]&v[1]&v[2]&v[3]&16u)==0); }
      vct[25]=1u;
    } else { while(vct[25]==0u) __builtin_amdgcn_s_sleep(2); }
    TSH(14);
    {
      u32x4 yv[4];
      #pragma unroll
      for(int i=0;i<4;i++) gldsc(p0+i*2048, y0^(i*64), w*CV_W, AUX+i*1024);
      const char* ys=smem+AUX+w*CV_W+(l&7)*512;
      const int yi=((l>>3)^((l&7)*2))*16;
      WAITV(0);
      yv[0]=*(const u32x4*)(ys+yi); yv[1]=*(const u32x4*)(ys+yi+256); yv[2]=*(const u32x4*)(ys+(yi^128)); yv[3]=*(const u32x4*)(ys+(yi^128)+256);
      TSH(15);
      #pragma unroll
      for(int r=0;r<4;r++){
        u32x4 ov;
        #pragma unroll
        for(int e=0;e<4;e++){ const unsigned y2=yv[(r>>1)*2+(e>>1)][(r&1)*2+(e&1)]; ov[e]=pack2(qf[r*8+2*e]*bflo(y2), qf[r*8+2*e+1]*bfhi(y2)); }
        stos(out+((r&1)*8+(r>>1)*32)*2048, po, ov);
      }
    }
    for(int sp_=0; !gen && sp_<(1<<22); sp_++) trygen();
    TS(20); TSDUMP();
  }

}

extern "C" void kernel_launch(void* const* d_in, const int* in_sizes, int n_in, void* d_out, int out_size, void* d_ws, size_t ws_size, hipStream_t stream){
  const u16* q=(const u16*)d_in[0]; const u16* k=(const u16*)d_in[1]; const u16* v=(const u16*)d_in[2];
  const u16* shk=(const u16*)d_in[3]; const u16* hk=(const u16*)d_in[4]; const u16* sD=(const u16*)d_in[5];
  const u16* x=(const u16*)d_in[6]; const u16* wqkv=(const u16*)d_in[7]; const u16* wo=(const u16*)d_in[8];
  fused_kernel<<<256,512,0,stream>>>(x,wqkv,k,v,shk,hk,wo,q,sD,(u16*)d_out,(u16*)d_ws,(u16*)d_ws);
}
```

```cpp
#define STAMP
#define PWAVE 9
#define PIDX 0
#define PSEL 1
#include <hip/hip_runtime.h>
#include <hip/hip_bf16.h>
#include <stdint.h>

typedef unsigned short u16;
typedef short s16x4 __attribute__((ext_vector_type(4)));
typedef float f32x32 __attribute__((ext_vector_type(32)));
typedef unsigned u32x4 __attribute__((ext_vector_type(4)));
typedef unsigned u32x2 __attribute__((ext_vector_type(2)));
#define LDS_AS __attribute__((address_space(3)))

__device__ __forceinline__ float bflo(unsigned x){ return __uint_as_float(x<<16); }
__device__ __forceinline__ float bfhi(unsigned x){ return __uint_as_float(x&0xffff0000u); }
typedef float f32x2 __attribute__((ext_vector_type(2)));
typedef __bf16 bf16x2_t __attribute__((ext_vector_type(2)));
__device__ __forceinline__ unsigned pack2(float lo, float hi){ return __builtin_bit_cast(unsigned, __builtin_convertvector(f32x2{lo,hi}, bf16x2_t)); }

#define GLDS(name,pol) __device__ __forceinline__ void name(const void* sb, unsigned voff, unsigned ldsb, int imm){ \
  asm volatile("s_add_u32 m0, %2, %3\n\ts_nop 0\n\tglobal_load_lds_dwordx4 %0, %1" pol :: "v"(voff), "s"(sb), "s"(ldsb), "n"(imm) : "memory", "scc"); }
#define LDPOL " nt"
GLDS(glds,"") GLDS(gldsn,LDPOL) GLDS(gldsc," sc1")
#define PACE 63
__device__ __forceinline__ void gldsp(const void* sb, unsigned voff, unsigned ldsb, int imm){
  asm volatile("s_add_u32 m0, %2, %3\n\ts_waitcnt vmcnt(%4)\n\tglobal_load_lds_dwordx4 %0, %1" :: "v"(voff), "s"(sb), "s"(ldsb), "n"(imm), "n"(PACE) : "memory", "scc"); }
__device__ __forceinline__ void touch(const void* sb, unsigned voff, unsigned long long mask){
  asm volatile("s_mov_b64 exec, %2\n\tglobal_load_dword v255, %0, %1\n\ts_mov_b64 exec, -1" :: "v"(voff), "s"(sb), "s"(mask) : "memory", "v255"); }
__device__ __forceinline__ void st16c(void* sb, unsigned vo, u32x4 v){ asm volatile("global_store_dwordx4 %0, %1, %2 sc1\n\ts_nop 1" :: "v"(vo), "v"(v), "s"(sb) : "memory"); }
#define OUTPOL " nt"
__device__ __forceinline__ void stos(void* sb, unsigned vo, u32x4 v){ asm volatile("global_store_dwordx4 %0, %1, %2" OUTPOL "\n\ts_nop 1" :: "v"(vo), "v"(v), "s"(sb) : "memory"); }
__device__ __forceinline__ void stoa(void* p, u32x4 v){ asm volatile("global_store_dwordx4 %0, %1, off" OUTPOL "\n\ts_nop 1" :: "v"(p), "v"(v) : "memory"); }
__device__ __forceinline__ void st4c(void* p, unsigned v){ asm volatile("global_store_dword %0, %1, off sc1" :: "v"(p), "v"(v) : "memory"); }
__device__ __forceinline__ void wait_flag(const unsigned* p, unsigned tag){
  int spins=0;
  while(true){
    unsigned v=__hip_atomic_load(p, __ATOMIC_RELAXED, __HIP_MEMORY_SCOPE_AGENT);
    if(__all(v==tag)) break;
    __builtin_amdgcn_s_sleep(2);
    if(++spins>(1<<22)) break;
  }
}
#define WAITV(n) asm volatile("s_waitcnt vmcnt(%0)"::"n"(n):"memory")
#define WAITL() asm volatile("s_waitcnt lgkmcnt(0)":::"memory")
#define BAR() do{ asm volatile("s_waitcnt lgkmcnt(0)":::"memory"); __builtin_amdgcn_s_barrier(); asm volatile("":::"memory"); }while(0)
#define LANES() int l; asm volatile("v_mbcnt_lo_u32_b32 %0, -1, 0\n\tv_mbcnt_hi_u32_b32 %0, -1, %0" : "=v"(l)); __builtin_assume(l>=0 && l<64); const int g=l>>4, fr=l&15, tq=(l&15)>>2, tp=l&3; (void)g; (void)fr; (void)tq; (void)tp;

__device__ __forceinline__ s16x4 trread(const char* p){ return __builtin_amdgcn_ds_read_tr16_b64_v4i16((LDS_AS s16x4*)p); }
__device__ __forceinline__ int wswz(int row){ return ((row>>1)&1) | (((row>>3)&1)<<1); }
__device__ __forceinline__ u32x4 widen(u32x2 a, u32x2 b){
  auto r0=__builtin_amdgcn_permlane16_swap(a[0],b[0],false,false);
  auto r1=__builtin_amdgcn_permlane16_swap(a[1],b[1],false,false);
  return u32x4{r0[0],r1[0],r0[1],r1[1]};
}

constexpr int S=2048, DM=1024;
constexpr int K1_XB=144*128, K1_WB=3*64*128, K1_ST=K1_XB+K1_WB;
constexpr int AUX=3*K1_ST, KVP_=1040;
constexpr int WOB=22528, CV_W=8448, CV_CP=4224;
constexpr int CT_OFF=AUX+33*KVP_;
constexpr int ALL_LDS=CT_OFF+176+96;
constexpr int FL_KV=1024, FL_O=FL_KV+8192, FL_Y=FL_O+8192;

#define CPRIO 2
#define CODE_ST 1590
#define LEAD 3
#define HP3 3
#define HP2 3
#define HP4 0
#define QP 1
#define TNX 1
#define HWS 99
#define HP1 0
#define HUNR 15
#define W0 6
#define OPD 8
#define WT0 99
#define W1 4
#define W2 4
#define W3 4
#define OPOLLS 2
#define POLLS 3
#define HPRE 0
#define CPS2 99
#define KR ((((idx>>2)*7)>>2)+(((idx&3)*7)>>1))
#define NOY 0
#define YLDS 1
#define OLDS 0
#define KVSEL (rbb==3? 0 : idx%KSN)
#define G2DBG
#define KS0 1
#define KSN 3
#define KST 9
#define KVP 5
#define TGS 2
#define CPS 13
#define H0_ASM \
  "s_mul_i32 s76, s52, 0x60000\n\t" \
  "s_add_u32 s76, s42, s76\n\t" \
  "s_addc_u32 s77, s43, 0\n\t" \
  "s_add_u32 s82, s53, 86016\n\t" \
  "s_add_u32 m0, s82, 18432\n\t" \
  "s_lshl_b32 s78, s52, 7\n\t" \
  "global_load_lds_dwordx4 v0, s[76:77]\n\t" \
  "global_load_lds_dwordx4 v1, s[76:77] offset:1024\n\t" \
  "s_add_u32 s78, s40, s78\n\t" \
  "s_addc_u32 s79, s41, 0\n\t" \
  "s_add_u32 m0, s82, 2048\n\t" \
  "s_nop 0\n\t" \
  "global_load_lds_dwordx4 v6, s[78:79]\n\t" \
  "global_load_lds_dwordx4 v7, s[78:79] offset:1024\n\t" \
  "s_add_u32 m0, s82, 26624\n\t" \
  "s_nop 0\n\t" \
  "global_load_lds_dwordx4 v2, s[76:77]\n\t" \
  "global_load_lds_dwordx4 v3, s[76:77] offset:1024\n\t" \
  "s_add_u32 m0, s82, 10240\n\t" \
  "s_nop 0\n\t" \
  "global_load_lds_dwordx4 v8, s[78:79]\n\t" \
  "global_load_lds_dwordx4 v9, s[78:79] offset:1024\n\t" \
  "s_add_u32 m0, s82, 34816\n\t" \
  "s_nop 0\n\t" \
  "global_load_lds_dwordx4 v4, s[76:77]\n\t" \
  "global_load_lds_dwordx4 v5, s[76:77] offset:1024\n\t" \
  "s_cmp_gt_u32 s59, 1\n\t" \
  "s_cbranch_scc1 1f\n\t" \
  "s_lshr_b32 s83, s53, 1\n\t" \
  "s_add_u32 m0, s83, 86016\n\t" \
  "s_nop 0\n\t" \
  "global_load_lds_dwordx4 v10, s[78:79]\n\t" \
  "1:\n\t"
#define H1_ASM \
  "s_mov_b32 s55, 0\n\t" \
  "s_mov_b32 s54, 0\n\t" \
  "s_add_u32 s52, s52, 1\n\t" \
  "s_and_b32 s52, s52, 15\n\t" \
  "s_cmp_eq_u32 s58, 1\n\t" \
  "s_cbranch_scc1 1f\n\t" \
  "s_mul_i32 s76, s52, 0x60000\n\t" \
  "s_add_u32 s76, s42, s76\n\t" \
  "s_addc_u32 s77, s43, 0\n\t" \
  "s_lshl_b32 s78, s52, 7\n\t" \
  "s_add_u32 s78, s40, s78\n\t" \
  "s_addc_u32 s79, s41, 0\n\t" \
  "s_mul_i32 s80, s47, s46\n\t" \
  "s_add_u32 s80, s44, s80\n\t" \
  "s_addc_u32 s81, s45, 0\n\t" \
  "s_mov_b32 s82, s53\n\t" \
  "s_add_u32 m0, s82, 18432\n\t" \
  "s_add_u32 s52, s52, 1\n\t" \
  "global_load_lds_dwordx4 v0, s[76:77]\n\t" \
  "global_load_lds_dwordx4 v1, s[76:77] offset:1024\n\t" \
  "s_add_u32 m0, s82, 2048\n\t" \
  "s_and_b32 s52, s52, 15\n\t" \
  "global_load_lds_dwordx4 v6, s[78:79]\n\t" \
  "global_load_lds_dwordx4 v7, s[78:79] offset:1024\n\t" \
  "s_add_u32 m0, s82, 26624\n\t" \
  "s_add_u32 s47, s47, 1\n\t" \
  "global_load_lds_dwordx4 v2, s[76:77]\n\t" \
  "global_load_lds_dwordx4 v3, s[76:77] offset:1024\n\t" \
  "s_add_u32 m0, s82, 10240\n\t" \
  "s_and_b32 s47, s47, 15\n\t" \
  "global_load_lds_dwordx4 v8, s[78:79]\n\t" \
  "global_load_lds_dwordx4 v9, s[78:79] offset:1024\n\t" \
  "s_add_u32 m0, s82, 34816\n\t" \
  "s_and_b32 s83, s54, s51\n\t" \
  "global_load_lds_dwordx4 v4, s[76:77]\n\t" \
  "global_load_lds_dwordx4 v5, s[76:77] offset:1024\n\t" \
  "s_add_u32 m0, s83, s49\n\t" \
  "s_mov_b64 exec, s[50:51]\n\t" \
  "global_load_lds_dwordx4 v10, s[80:81]\n\t" \
  "s_mov_b64 exec, -1\n\t" \
  "s_mov_b32 s55, 1\n\t" \
  "s_mov_b32 s54, 43008\n\t" \
  "s_waitcnt vmcnt(11)\n\t" \
  "s_branch 2f\n\t" \
  "1:\n\t" \
  "s_mul_i32 s83, s59, 4160\n\t" \
  "v_add_u32 v20, 7152, v11\n\t" \
  "v_add_u32 v21, 14304, v11\n\t" \
  "v_add_u32 v22, 21456, v11\n\t" \
  "s_add_u32 m0, s83, 130064\n\t" \
  "s_nop 0\n\t" \
  "global_load_lds_dwordx4 v11, s[60:61]" LDPOL "\n\t" \
  "global_load_lds_dwordx4 v20, s[60:61] offset:1040" LDPOL "\n\t" \
  "global_load_lds_dwordx4 v21, s[60:61] offset:2080" LDPOL "\n\t" \
  "global_load_lds_dwordx4 v22, s[60:61] offset:3120" LDPOL "\n\t" \
  "s_add_u32 m0, s83, 146704\n\t" \
  "s_nop 0\n\t" \
  "global_load_lds_dwordx4 v11, s[62:63]" LDPOL "\n\t" \
  "global_load_lds_dwordx4 v20, s[62:63] offset:1040" LDPOL "\n\t" \
  "global_load_lds_dwordx4 v21, s[62:63] offset:2080" LDPOL "\n\t" \
  "global_load_lds_dwordx4 v22, s[62:63] offset:3120" LDPOL "\n\t" \
  "v_subrev_u32 v20, s64, v11\n\t" \
  "s_mov_b32 m0, s68\n\t" \
  "s_mov_b64 exec, s[66:67]\n\t" \
  "global_load_lds_dwordx4 v20, s[60:61]" LDPOL "\n\t" \
  "s_mov_b64 exec, -1\n\t" \
  "s_waitcnt vmcnt(9)\n\t" \
  "2:\n\t" \
  "s_waitcnt lgkmcnt(0)\n\t" \
  "s_barrier\n\t" \
  "s_cmp_lg_u32 s59, 3\n\t" \
  "s_cbranch_scc1 9f\n\t" \
  "s_memrealtime s[84:85]\n\t" \
  "s_waitcnt lgkmcnt(0)\n\t" \
  "s_sub_u32 s84, s84, s87\n\t" \
  "s_mov_b32 m0, s86\n\t" \
  "s_add_u32 s86, s86, 1\n\t" \
  "v_writelane_b32 v40, s84, m0\n\t" \
  "9:\n\t"
#define H_ASM \
  "s_mul_i32 s76, s52, 0x60000\n\t" \
  "s_add_u32 s76, s42, s76\n\t" \
  "s_addc_u32 s77, s43, 0\n\t" \
  "s_lshl_b32 s78, s52, 7\n\t" \
  "s_add_u32 s78, s40, s78\n\t" \
  "s_addc_u32 s79, s41, 0\n\t" \
  "s_mul_i32 s80, s47, s46\n\t" \
  "s_add_u32 s80, s44, s80\n\t" \
  "s_addc_u32 s81, s45, 0\n\t" \
  "s_add_u32 s82, s54, s53\n\t" \
  "s_cmp_eq_u32 s58, 2\n\t" \
  "s_cbranch_scc1 5f\n\t" \
  "s_branch 3f\n\t" \
  "1:\n\t" \
  "s_waitcnt vmcnt(11)\n\t" \
  "2:\n\t" \
  "s_barrier\n\t" \
  "s_cmp_lg_u32 s59, 3\n\t" \
  "s_cbranch_scc1 9f\n\t" \
  "s_memrealtime s[84:85]\n\t" \
  "s_waitcnt lgkmcnt(0)\n\t" \
  "s_sub_u32 s84, s84, s87\n\t" \
  "s_mov_b32 m0, s86\n\t" \
  "s_add_u32 s86, s86, 1\n\t" \
  "v_writelane_b32 v40, s84, m0\n\t" \
  "9:\n\t" \
  "3:\n\t" \
  "s_add_u32 s55, s55, 1\n\t" \
  "s_add_u32 m0, s82, 18432\n\t" \
  "s_add_u32 s52, s52, 1\n\t" \
  "global_load_lds_dwordx4 v0, s[76:77]\n\t" \
  "global_load_lds_dwordx4 v1, s[76:77] offset:1024\n\t" \
  "s_add_u32 m0, s82, 2048\n\t" \
  "s_and_b32 s52, s52, 15\n\t" \
  "global_load_lds_dwordx4 v6, s[78:79]\n\t" \
  "global_load_lds_dwordx4 v7, s[78:79] offset:1024\n\t" \
  "s_add_u32 m0, s82, 26624\n\t" \
  "s_add_u32 s47, s47, 1\n\t" \
  "global_load_lds_dwordx4 v2, s[76:77]\n\t" \
  "global_load_lds_dwordx4 v3, s[76:77] offset:1024\n\t" \
  "s_add_u32 m0, s82, 10240\n\t" \
  "s_and_b32 s47, s47, 15\n\t" \
  "global_load_lds_dwordx4 v8, s[78:79]\n\t" \
  "global_load_lds_dwordx4 v9, s[78:79] offset:1024\n\t" \
  "s_add_u32 m0, s82, 34816\n\t" \
  "s_and_b32 s83, s54, s51\n\t" \
  "global_load_lds_dwordx4 v4, s[76:77]\n\t" \
  "global_load_lds_dwordx4 v5, s[76:77] offset:1024\n\t" \
  "s_bitcmp1_b32 s57, s55\n\t" \
  "s_cbranch_scc1 10f\n\t" \
  "8:\n\t" \
  "s_add_u32 m0, s83, s49\n\t" \
  "s_mov_b64 exec, s[50:51]\n\t" \
  "global_load_lds_dwordx4 v10, s[80:81]\n\t" \
  "s_mov_b64 exec, -1\n\t" \
  "4:\n\t" \
  "s_mul_i32 s76, s52, 0x60000\n\t" \
  "s_add_u32 s76, s42, s76\n\t" \
  "s_addc_u32 s77, s43, 0\n\t" \
  "s_lshl_b32 s78, s52, 7\n\t" \
  "s_add_u32 s78, s40, s78\n\t" \
  "s_addc_u32 s79, s41, 0\n\t" \
  "s_mul_i32 s80, s47, s46\n\t" \
  "s_add_u32 s80, s44, s80\n\t" \
  "s_addc_u32 s81, s45, 0\n\t" \
  "s_add_u32 s54, s54, 43008\n\t" \
  "s_cmp_eq_u32 s54, 129024\n\t" \
  "s_cselect_b32 s54, 0, s54\n\t" \
  "s_add_u32 s82, s54, s53\n\t" \
  "s_bitcmp0_b32 s56, s55\n\t" \
  "s_cbranch_scc1 1b\n\t" \
  "s_cmp_eq_u32 s55, 15\n\t" \
  "s_cbranch_scc1 19f\n\t" \
  "s_cmp_eq_u32 s55, s58\n\t" \
  "s_cbranch_scc1 3b\n\t" \
  "s_waitcnt vmcnt(11)\n\t" \
  "s_barrier\n\t" \
  "s_cmp_lg_u32 s59, 3\n\t" \
  "s_cbranch_scc1 9f\n\t" \
  "s_memrealtime s[84:85]\n\t" \
  "s_waitcnt lgkmcnt(0)\n\t" \
  "s_sub_u32 s84, s84, s87\n\t" \
  "s_mov_b32 m0, s86\n\t" \
  "s_add_u32 s86, s86, 1\n\t" \
  "v_writelane_b32 v40, s84, m0\n\t" \
  "9:\n\t" \
  "5:\n\t" \
  "s_mul_i32 s83, s59, 4160\n\t" \
  "v_add_u32 v20, 7152, v11\n\t" \
  "v_add_u32 v21, 14304, v11\n\t" \
  "v_add_u32 v22, 21456, v11\n\t" \
  "s_add_u32 m0, s83, 130064\n\t" \
  "s_nop 0\n\t" \
  "global_load_lds_dwordx4 v11, s[60:61]" LDPOL "\n\t" \
  "global_load_lds_dwordx4 v20, s[60:61] offset:1040" LDPOL "\n\t" \
  "global_load_lds_dwordx4 v21, s[60:61] offset:2080" LDPOL "\n\t" \
  "global_load_lds_dwordx4 v22, s[60:61] offset:3120" LDPOL "\n\t" \
  "s_add_u32 m0, s83, 146704\n\t" \
  "s_nop 0\n\t" \
  "global_load_lds_dwordx4 v11, s[62:63]" LDPOL "\n\t" \
  "global_load_lds_dwordx4 v20, s[62:63] offset:1040" LDPOL "\n\t" \
  "global_load_lds_dwordx4 v21, s[62:63] offset:2080" LDPOL "\n\t" \
  "global_load_lds_dwordx4 v22, s[62:63] offset:3120" LDPOL "\n\t" \
  "v_subrev_u32 v20, s64, v11\n\t" \
  "s_mov_b32 m0, s68\n\t" \
  "s_mov_b64 exec, s[66:67]\n\t" \
  "global_load_lds_dwordx4 v20, s[60:61]" LDPOL "\n\t" \
  "s_mov_b64 exec, -1\n\t" \
  "s_waitcnt vmcnt(9)\n\t" \
  "s_branch 2b\n\t" \
  "10:\n\t" \
  "s_cmp_eq_u32 s59, 2\n\t" \
  "s_cbranch_scc0 11f\n\t" \
  "s_mov_b64 exec, 0xffff\n\t" \
  "global_load_dword v255, v14, s[72:73]\n\t" \
  "s_mov_b64 exec, -1\n\t" \
  "s_branch 4b\n\t" \
  "11:\n\t" \
  "s_cmp_eq_u32 s55, 2\n\t" \
  "s_cbranch_scc0 12f\n\t" \
  "global_load_dword v254, v14, s[70:71] sc1\n\t" \
  "s_branch 4b\n\t" \
  "12:\n\t" \
  "s_cmp_eq_u32 s55, 5\n\t" \
  "s_cbranch_scc0 13f\n\t" \
  "v_readfirstlane_b32 s84, v254\n\t" \
  "v_mov_b32 v253, 163448\n\t" \
  "s_nop 1\n\t" \
  "s_add_u32 s84, s84, 1\n\t" \
  "v_mov_b32 v254, s84\n\t" \
  "ds_write_b32 v253, v254\n\t" \
  "s_waitcnt lgkmcnt(0)\n\t" \
  "s_branch 8b\n\t" \
  "13:\n\t" \
  "s_cmp_eq_u32 s55, 14\n\t" \
  "s_cbranch_scc0 14f\n\t" \
  "global_load_dword v253, v16, s[70:71] sc1\n\t" \
  "s_branch 4b\n\t" \
  "14:\n\t" \
  "s_mov_b32 exec_hi, 0\n\t" \
  "global_load_dword v255, v10, s[44:45]\n\t" \
  "s_mov_b64 exec, -1\n\t" \
  "s_branch 4b\n\t" \
  "19:\n\t" \
  "s_waitcnt vmcnt(11)\n\t" \
  "s_cmp_lg_u32 s59, 3\n\t" \
  "s_cbranch_scc1 18f\n\t" \
  "v_cmp_ne_u32 vcc, v253, v254\n\t" \
  "v_mov_b32 v253, 163364\n\t" \
  "s_nop 1\n\t" \
  "s_cmp_eq_u64 vcc, 0\n\t" \
  "s_cselect_b32 s83, 1, 0\n\t" \
  "v_mov_b32 v254, s83\n\t" \
  "ds_write_b32 v253, v254\n\t" \
  "18:\n\t"
#define C_ASM \
  "s_branch 29f\n\t" \
  "28:\n\t" \
  "s_mov_b64 exec, 0\n\t" \
  "s_cmp_eq_u32 %13, 0\n\t" \
  "s_cbranch_scc1 500f\n\t" \
  "s_cmp_eq_u32 %13, 1\n\t" \
  "s_cbranch_scc1 501f\n\t" \
  "s_cmp_eq_u32 %13, 2\n\t" \
  "s_cbranch_scc1 502f\n\t" \
  "s_branch 503f\n\t" \
  "26:\n\t" \
  "s_mov_b64 exec, -1\n\t" \
  "s_add_u32 s88, s88, 4\n\t" \
  "s_addc_u32 s89, s89, 0\n\t" \
  "s_setpc_b64 s[88:89]\n\t" \
  ".p2align 12\n\t" \
  "29:\n\t" \
  "s_bitcmp0_b32 %13, 0\n\t" \
  "s_cbranch_scc1 8f\n\t" \
  "global_load_dwordx2 v[10:11], v51, %11\n\t" \
  "global_load_dwordx2 v[12:13], v51, %11 offset:128\n\t" \
  "v_xor_b32 v53, 64, v52\n\t" \
  "v_xor_b32 v55, 32, v54\n\t" \
  "v_add_u32 v56, 43008, v52\n\t" \
  "v_add_u32 v58, 43008, v54\n\t" \
  "v_xor_b32 v57, 64, v56\n\t" \
  "v_xor_b32 v59, 32, v58\n\t" \
  "v_add_u32 v60, 86016, v52\n\t" \
  "v_add_u32 v62, 86016, v54\n\t" \
  "v_xor_b32 v61, 64, v60\n\t" \
  "v_xor_b32 v63, 32, v62\n\t" \
  "s_mov_b32 vcc_hi, 0x05040100\n\t" \
  "s_waitcnt vmcnt(2)\n\t" \
  "s_barrier\n\t" \
  "ds_read_b64_tr_b16 v[92:93], v62 offset:26624\n\t" \
  "ds_read_b64_tr_b16 v[94:95], v62 offset:27136\n\t" \
  "ds_read_b128 v[64:67], v60 offset:10240\n\t" \
  "ds_read_b128 v[68:71], v60 offset:12288\n\t" \
  "ds_read_b128 v[72:75], v60 offset:14336\n\t" \
  "ds_read_b128 v[76:79], v60 offset:16384\n\t" \
  "ds_read_b64_tr_b16 v[96:97], v63 offset:26624\n\t" \
  "ds_read_b64_tr_b16 v[98:99], v63 offset:27136\n\t" \
  "ds_read_b64_tr_b16 v[100:101], v62 offset:34816\n\t" \
  "ds_read_b64_tr_b16 v[102:103], v62 offset:35328\n\t" \
  "ds_read_b128 v[80:83], v60 offset:0\n\t" \
  "ds_read_b64_tr_b16 v[104:105], v63 offset:34816\n\t" \
  "ds_read_b64_tr_b16 v[106:107], v63 offset:35328\n\t" \
  "ds_read_b64_tr_b16 v[84:85], v62 offset:18432\n\t" \
  "ds_read_b64_tr_b16 v[86:87], v62 offset:18944\n\t" \
  "ds_read_b64_tr_b16 v[88:89], v63 offset:18432\n\t" \
  "s_waitcnt lgkmcnt(13)\n\t" \
  "v_mfma_f32_16x16x32_bf16 v[184:187], v[92:95], v[64:67], 0\n\t" \
  "ds_read_b64_tr_b16 v[90:91], v63 offset:18944\n\t" \
  "ds_read_b64_tr_b16 v[136:137], v62 offset:30720\n\t" \
  "s_waitcnt lgkmcnt(14)\n\t" \
  "v_mfma_f32_16x16x32_bf16 v[192:195], v[92:95], v[68:71], 0\n\t" \
  "ds_read_b64_tr_b16 v[138:139], v62 offset:31232\n\t" \
  "ds_read_b128 v[108:111], v61 offset:10240\n\t" \
  "s_waitcnt lgkmcnt(15)\n\t" \
  "v_mfma_f32_16x16x32_bf16 v[200:203], v[92:95], v[72:75], 0\n\t" \
  "ds_read_b128 v[112:115], v61 offset:12288\n\t" \
  "ds_read_b128 v[116:119], v61 offset:14336\n\t" \
  "s_waitcnt lgkmcnt(15)\n\t" \
  "v_mfma_f32_16x16x32_bf16 v[208:211], v[92:95], v[76:79], 0\n\t" \
  "ds_read_b128 v[120:123], v61 offset:16384\n\t" \
  "ds_read_b64_tr_b16 v[140:141], v63 offset:30720\n\t" \
  "s_waitcnt lgkmcnt(15)\n\t" \
  "v_mfma_f32_16x16x32_bf16 v[188:191], v[96:99], v[64:67], 0\n\t" \
  "ds_read_b64_tr_b16 v[142:143], v63 offset:31232\n\t" \
  "ds_read_b64_tr_b16 v[144:145], v62 offset:38912\n\t" \
  "v_mfma_f32_16x16x32_bf16 v[196:199], v[96:99], v[68:71], 0\n\t" \
  "ds_read_b64_tr_b16 v[146:147], v62 offset:39424\n\t" \
  "ds_read_b128 v[124:127], v61 offset:0\n\t" \
  "v_mfma_f32_16x16x32_bf16 v[204:207], v[96:99], v[72:75], 0\n\t" \
  "ds_read_b64_tr_b16 v[148:149], v63 offset:38912\n\t" \
  "ds_read_b64_tr_b16 v[150:151], v63 offset:39424\n\t" \
  "v_mfma_f32_16x16x32_bf16 v[212:215], v[96:99], v[76:79], 0\n\t" \
  "ds_read_b64_tr_b16 v[128:129], v62 offset:22528\n\t" \
  "ds_read_b64_tr_b16 v[130:131], v62 offset:23040\n\t" \
  "s_waitcnt lgkmcnt(14)\n\t" \
  "v_mfma_f32_16x16x32_bf16 v[248:251], v[80:83], v[100:103], 0\n\t" \
  "ds_read_b64_tr_b16 v[132:133], v63 offset:22528\n\t" \
  "ds_read_b64_tr_b16 v[134:135], v63 offset:23040\n\t" \
  "v_mfma_f32_16x16x32_bf16 v[216:219], v[64:67], v[100:103], 0\n\t" \
  "v_mfma_f32_16x16x32_bf16 v[224:227], v[68:71], v[100:103], 0\n\t" \
  "v_mfma_f32_16x16x32_bf16 v[232:235], v[72:75], v[100:103], 0\n\t" \
  "v_mfma_f32_16x16x32_bf16 v[240:243], v[76:79], v[100:103], 0\n\t" \
  "s_waitcnt lgkmcnt(0)\n\t" \
  "s_getpc_b64 s[88:89]\n\t" \
  "s_branch 28b\n\t" \
  "s_barrier\n\t" \
  "v_mfma_f32_16x16x32_bf16 v[252:255], v[80:83], v[104:107], 0\n\t" \
  "ds_read_b64_tr_b16 v[92:93], v54 offset:26624\n\t" \
  "v_mfma_f32_16x16x32_bf16 v[220:223], v[64:67], v[104:107], 0\n\t" \
  "ds_read_b64_tr_b16 v[94:95], v54 offset:27136\n\t" \
  "v_mfma_f32_16x16x32_bf16 v[228:231], v[68:71], v[104:107], 0\n\t" \
  "ds_read_b64_tr_b16 v[96:97], v55 offset:26624\n\t" \
  "v_mfma_f32_16x16x32_bf16 v[236:239], v[72:75], v[104:107], 0\n\t" \
  "ds_read_b64_tr_b16 v[98:99], v55 offset:27136\n\t" \
  "v_mfma_f32_16x16x32_bf16 v[244:247], v[76:79], v[104:107], 0\n\t" \
  "ds_read_b64_tr_b16 v[100:101], v54 offset:34816\n\t" \
  "v_mfma_f32_16x16x32_bf16 v[152:155], v[84:87], v[64:67], 0\n\t" \
  "ds_read_b64_tr_b16 v[102:103], v54 offset:35328\n\t" \
  "v_mfma_f32_16x16x32_bf16 v[160:163], v[84:87], v[68:71], 0\n\t" \
  "ds_read_b128 v[80:83], v52 offset:0\n\t" \
  "v_mfma_f32_16x16x32_bf16 v[168:171], v[84:87], v[72:75], 0\n\t" \
  "ds_read_b64_tr_b16 v[104:105], v55 offset:34816\n\t" \
  "v_mfma_f32_16x16x32_bf16 v[176:179], v[84:87], v[76:79], 0\n\t" \
  "ds_read_b64_tr_b16 v[106:107], v55 offset:35328\n\t" \
  "v_mfma_f32_16x16x32_bf16 v[156:159], v[88:91], v[64:67], 0\n\t" \
  "ds_read_b128 v[64:67], v52 offset:10240\n\t" \
  "v_mfma_f32_16x16x32_bf16 v[164:167], v[88:91], v[68:71], 0\n\t" \
  "ds_read_b128 v[68:71], v52 offset:12288\n\t" \
  "v_mfma_f32_16x16x32_bf16 v[172:175], v[88:91], v[72:75], 0\n\t" \
  "ds_read_b128 v[72:75], v52 offset:14336\n\t" \
  "v_mfma_f32_16x16x32_bf16 v[180:183], v[88:91], v[76:79], 0\n\t" \
  "ds_read_b128 v[76:79], v52 offset:16384\n\t" \
  "v_mfma_f32_16x16x32_bf16 v[184:187], v[136:139], v[108:111], v[184:187]\n\t" \
  "ds_read_b64_tr_b16 v[84:85], v54 offset:18432\n\t" \
  "v_mfma_f32_16x16x32_bf16 v[192:195], v[136:139], v[112:115], v[192:195]\n\t" \
  "ds_read_b64_tr_b16 v[86:87], v54 offset:18944\n\t" \
  "v_mfma_f32_16x16x32_bf16 v[200:203], v[136:139], v[116:119], v[200:203]\n\t" \
  "ds_read_b64_tr_b16 v[88:89], v55 offset:18432\n\t" \
  "v_mfma_f32_16x16x32_bf16 v[208:211], v[136:139], v[120:123], v[208:211]\n\t" \
  "ds_read_b64_tr_b16 v[90:91], v55 offset:18944\n\t" \
  "v_mfma_f32_16x16x32_bf16 v[188:191], v[140:143], v[108:111], v[188:191]\n\t" \
  "ds_read_b64_tr_b16 v[136:137], v54 offset:30720\n\t" \
  "v_mfma_f32_16x16x32_bf16 v[196:199], v[140:143], v[112:115], v[196:199]\n\t" \
  "ds_read_b64_tr_b16 v[138:139], v54 offset:31232\n\t" \
  "v_mfma_f32_16x16x32_bf16 v[204:207], v[140:143], v[116:119], v[204:207]\n\t" \
  "v_mfma_f32_16x16x32_bf16 v[212:215], v[140:143], v[120:123], v[212:215]\n\t" \
  "ds_read_b64_tr_b16 v[140:141], v55 offset:30720\n\t" \
  "v_mfma_f32_16x16x32_bf16 v[248:251], v[124:127], v[144:147], v[248:251]\n\t" \
  "ds_read_b64_tr_b16 v[142:143], v55 offset:31232\n\t" \
  "v_mfma_f32_16x16x32_bf16 v[216:219], v[108:111], v[144:147], v[216:219]\n\t" \
  "v_mfma_f32_16x16x32_bf16 v[224:227], v[112:115], v[144:147], v[224:227]\n\t" \
  "v_mfma_f32_16x16x32_bf16 v[232:235], v[116:119], v[144:147], v[232:235]\n\t" \
  "v_mfma_f32_16x16x32_bf16 v[240:243], v[120:123], v[144:147], v[240:243]\n\t" \
  "ds_read_b64_tr_b16 v[144:145], v54 offset:38912\n\t" \
  "v_mfma_f32_16x16x32_bf16 v[252:255], v[124:127], v[148:151], v[252:255]\n\t" \
  "ds_read_b64_tr_b16 v[146:147], v54 offset:39424\n\t" \
  "v_mfma_f32_16x16x32_bf16 v[220:223], v[108:111], v[148:151], v[220:223]\n\t" \
  "ds_read_b128 v[124:127], v53 offset:0\n\t" \
  "v_mfma_f32_16x16x32_bf16 v[228:231], v[112:115], v[148:151], v[228:231]\n\t" \
  "v_mfma_f32_16x16x32_bf16 v[236:239], v[116:119], v[148:151], v[236:239]\n\t" \
  "v_mfma_f32_16x16x32_bf16 v[244:247], v[120:123], v[148:151], v[244:247]\n\t" \
  "ds_read_b64_tr_b16 v[148:149], v55 offset:38912\n\t" \
  "v_mfma_f32_16x16x32_bf16 v[152:155], v[128:131], v[108:111], v[152:155]\n\t" \
  "ds_read_b64_tr_b16 v[150:151], v55 offset:39424\n\t" \
  "v_mfma_f32_16x16x32_bf16 v[160:163], v[128:131], v[112:115], v[160:163]\n\t" \
  "v_mfma_f32_16x16x32_bf16 v[168:171], v[128:131], v[116:119], v[168:171]\n\t" \
  "v_mfma_f32_16x16x32_bf16 v[176:179], v[128:131], v[120:123], v[176:179]\n\t" \
  "ds_read_b64_tr_b16 v[128:129], v54 offset:22528\n\t" \
  "v_mfma_f32_16x16x32_bf16 v[156:159], v[132:135], v[108:111], v[156:159]\n\t" \
  "ds_read_b128 v[108:111], v53 offset:10240\n\t" \
  "v_mfma_f32_16x16x32_bf16 v[164:167], v[132:135], v[112:115], v[164:167]\n\t" \
  "ds_read_b128 v[112:115], v53 offset:12288\n\t" \
  "v_mfma_f32_16x16x32_bf16 v[172:175], v[132:135], v[116:119], v[172:175]\n\t" \
  "ds_read_b128 v[116:119], v53 offset:14336\n\t" \
  "v_mfma_f32_16x16x32_bf16 v[180:183], v[132:135], v[120:123], v[180:183]\n\t" \
  "ds_read_b128 v[120:123], v53 offset:16384\n\t" \
  "s_waitcnt lgkmcnt(5)\n\t" \
  "v_mfma_f32_16x16x32_bf16 v[184:187], v[92:95], v[64:67], v[184:187]\n\t" \
  "ds_read_b64_tr_b16 v[130:131], v54 offset:23040\n\t" \
  "v_mfma_f32_16x16x32_bf16 v[192:195], v[92:95], v[68:71], v[192:195]\n\t" \
  "ds_read_b64_tr_b16 v[132:133], v55 offset:22528\n\t" \
  "v_mfma_f32_16x16x32_bf16 v[200:203], v[92:95], v[72:75], v[200:203]\n\t" \
  "ds_read_b64_tr_b16 v[134:135], v55 offset:23040\n\t" \
  "v_mfma_f32_16x16x32_bf16 v[208:211], v[92:95], v[76:79], v[208:211]\n\t" \
  "v_mfma_f32_16x16x32_bf16 v[188:191], v[96:99], v[64:67], v[188:191]\n\t" \
  "v_mfma_f32_16x16x32_bf16 v[196:199], v[96:99], v[68:71], v[196:199]\n\t" \
  "v_mfma_f32_16x16x32_bf16 v[204:207], v[96:99], v[72:75], v[204:207]\n\t" \
  "v_mfma_f32_16x16x32_bf16 v[212:215], v[96:99], v[76:79], v[212:215]\n\t" \
  "v_mfma_f32_16x16x32_bf16 v[248:251], v[80:83], v[100:103], v[248:251]\n\t" \
  "v_mfma_f32_16x16x32_bf16 v[216:219], v[64:67], v[100:103], v[216:219]\n\t" \
  "v_mfma_f32_16x16x32_bf16 v[224:227], v[68:71], v[100:103], v[224:227]\n\t" \
  "v_mfma_f32_16x16x32_bf16 v[232:235], v[72:75], v[100:103], v[232:235]\n\t" \
  "v_mfma_f32_16x16x32_bf16 v[240:243], v[76:79], v[100:103], v[240:243]\n\t" \
  "s_waitcnt lgkmcnt(0)\n\t" \
  "s_barrier\n\t" \
  "v_mfma_f32_16x16x32_bf16 v[252:255], v[80:83], v[104:107], v[252:255]\n\t" \
  "ds_read_b64_tr_b16 v[92:93], v58 offset:26624\n\t" \
  "v_mfma_f32_16x16x32_bf16 v[220:223], v[64:67], v[104:107], v[220:223]\n\t" \
  "ds_read_b64_tr_b16 v[94:95], v58 offset:27136\n\t" \
  "v_mfma_f32_16x16x32_bf16 v[228:231], v[68:71], v[104:107], v[228:231]\n\t" \
  "ds_read_b64_tr_b16 v[96:97], v59 offset:26624\n\t" \
  "v_mfma_f32_16x16x32_bf16 v[236:239], v[72:75], v[104:107], v[236:239]\n\t" \
  "ds_read_b64_tr_b16 v[98:99], v59 offset:27136\n\t" \
  "v_mfma_f32_16x16x32_bf16 v[244:247], v[76:79], v[104:107], v[244:247]\n\t" \
  "ds_read_b64_tr_b16 v[100:101], v58 offset:34816\n\t" \
  "v_mfma_f32_16x16x32_bf16 v[152:155], v[84:87], v[64:67], v[152:155]\n\t" \
  "ds_read_b64_tr_b16 v[102:103], v58 offset:35328\n\t" \
  "v_mfma_f32_16x16x32_bf16 v[160:163], v[84:87], v[68:71], v[160:163]\n\t" \
  "ds_read_b128 v[80:83], v56 offset:0\n\t" \
  "v_mfma_f32_16x16x32_bf16 v[168:171], v[84:87], v[72:75], v[168:171]\n\t" \
  "ds_read_b64_tr_b16 v[104:105], v59 offset:34816\n\t" \
  "v_mfma_f32_16x16x32_bf16 v[176:179], v[84:87], v[76:79], v[176:179]\n\t" \
  "ds_read_b64_tr_b16 v[106:107], v59 offset:35328\n\t" \
  "v_mfma_f32_16x16x32_bf16 v[156:159], v[88:91], v[64:67], v[156:159]\n\t" \
  "ds_read_b128 v[64:67], v56 offset:10240\n\t" \
  "v_mfma_f32_16x16x32_bf16 v[164:167], v[88:91], v[68:71], v[164:167]\n\t" \
  "ds_read_b128 v[68:71], v56 offset:12288\n\t" \
  "v_mfma_f32_16x16x32_bf16 v[172:175], v[88:91], v[72:75], v[172:175]\n\t" \
  "ds_read_b128 v[72:75], v56 offset:14336\n\t" \
  "v_mfma_f32_16x16x32_bf16 v[180:183], v[88:91], v[76:79], v[180:183]\n\t" \
  "ds_read_b128 v[76:79], v56 offset:16384\n\t" \
  "v_mfma_f32_16x16x32_bf16 v[184:187], v[136:139], v[108:111], v[184:187]\n\t" \
  "ds_read_b64_tr_b16 v[84:85], v58 offset:18432\n\t" \
  "v_mfma_f32_16x16x32_bf16 v[192:195], v[136:139], v[112:115], v[192:195]\n\t" \
  "ds_read_b64_tr_b16 v[86:87], v58 offset:18944\n\t" \
  "v_mfma_f32_16x16x32_bf16 v[200:203], v[136:139], v[116:119], v[200:203]\n\t" \
  "ds_read_b64_tr_b16 v[88:89], v59 offset:18432\n\t" \
  "v_mfma_f32_16x16x32_bf16 v[208:211], v[136:139], v[120:123], v[208:211]\n\t" \
  "ds_read_b64_tr_b16 v[90:91], v59 offset:18944\n\t" \
  "v_mfma_f32_16x16x32_bf16 v[188:191], v[140:143], v[108:111], v[188:191]\n\t" \
  "ds_read_b64_tr_b16 v[136:137], v58 offset:30720\n\t" \
  "v_mfma_f32_16x16x32_bf16 v[196:199], v[140:143], v[112:115], v[196:199]\n\t" \
  "ds_read_b64_tr_b16 v[138:139], v58 offset:31232\n\t" \
  "v_mfma_f32_16x16x32_bf16 v[204:207], v[140:143], v[116:119], v[204:207]\n\t" \
  "v_mfma_f32_16x16x32_bf16 v[212:215], v[140:143], v[120:123], v[212:215]\n\t" \
  "ds_read_b64_tr_b16 v[140:141], v59 offset:30720\n\t" \
  "v_mfma_f32_16x16x32_bf16 v[248:251], v[124:127], v[144:147], v[248:251]\n\t" \
  "ds_read_b64_tr_b16 v[142:143], v59 offset:31232\n\t" \
  "v_mfma_f32_16x16x32_bf16 v[216:219], v[108:111], v[144:147], v[216:219]\n\t" \
  "v_mfma_f32_16x16x32_bf16 v[224:227], v[112:115], v[144:147], v[224:227]\n\t" \
  "v_mfma_f32_16x16x32_bf16 v[232:235], v[116:119], v[144:147], v[232:235]\n\t" \
  "v_mfma_f32_16x16x32_bf16 v[240:243], v[120:123], v[144:147], v[240:243]\n\t" \
  "ds_read_b64_tr_b16 v[144:145], v58 offset:38912\n\t" \
  "v_mfma_f32_16x16x32_bf16 v[252:255], v[124:127], v[148:151], v[252:255]\n\t" \
  "ds_read_b64_tr_b16 v[146:147], v58 offset:39424\n\t" \
  "v_mfma_f32_16x16x32_bf16 v[220:223], v[108:111], v[148:151], v[220:223]\n\t" \
  "ds_read_b128 v[124:127], v57 offset:0\n\t" \
  "v_mfma_f32_16x16x32_bf16 v[228:231], v[112:115], v[148:151], v[228:231]\n\t" \
  "v_mfma_f32_16x16x32_bf16 v[236:239], v[116:119], v[148:151], v[236:239]\n\t" \
  "v_mfma_f32_16x16x32_bf16 v[244:247], v[120:123], v[148:151], v[244:247]\n\t" \
  "ds_read_b64_tr_b16 v[148:149], v59 offset:38912\n\t" \
  "v_mfma_f32_16x16x32_bf16 v[152:155], v[128:131], v[108:111], v[152:155]\n\t" \
  "ds_read_b64_tr_b16 v[150:151], v59 offset:39424\n\t" \
  "v_mfma_f32_16x16x32_bf16 v[160:163], v[128:131], v[112:115], v[160:163]\n\t" \
  "v_mfma_f32_16x16x32_bf16 v[168:171], v[128:131], v[116:119], v[168:171]\n\t" \
  "v_mfma_f32_16x16x32_bf16 v[176:179], v[128:131], v[120:123], v[176:179]\n\t" \
  "ds_read_b64_tr_b16 v[128:129], v58 offset:22528\n\t" \
  "v_mfma_f32_16x16x32_bf16 v[156:159], v[132:135], v[108:111], v[156:159]\n\t" \
  "ds_read_b128 v[108:111], v57 offset:10240\n\t" \
  "v_mfma_f32_16x16x32_bf16 v[164:167], v[132:135], v[112:115], v[164:167]\n\t" \
  "ds_read_b128 v[112:115], v57 offset:12288\n\t" \
  "v_mfma_f32_16x16x32_bf16 v[172:175], v[132:135], v[116:119], v[172:175]\n\t" \
  "ds_read_b128 v[116:119], v57 offset:14336\n\t" \
  "v_mfma_f32_16x16x32_bf16 v[180:183], v[132:135], v[120:123], v[180:183]\n\t" \
  "ds_read_b128 v[120:123], v57 offset:16384\n\t" \
  "s_waitcnt lgkmcnt(5)\n\t" \
  "v_mfma_f32_16x16x32_bf16 v[184:187], v[92:95], v[64:67], v[184:187]\n\t" \
  "ds_read_b64_tr_b16 v[130:131], v58 offset:23040\n\t" \
  "v_mfma_f32_16x16x32_bf16 v[192:195], v[92:95], v[68:71], v[192:195]\n\t" \
  "ds_read_b64_tr_b16 v[132:133], v59 offset:22528\n\t" \
  "v_mfma_f32_16x16x32_bf16 v[200:203], v[92:95], v[72:75], v[200:203]\n\t" \
  "ds_read_b64_tr_b16 v[134:135], v59 offset:23040\n\t" \
  "v_mfma_f32_16x16x32_bf16 v[208:211], v[92:95], v[76:79], v[208:211]\n\t" \
  "v_mfma_f32_16x16x32_bf16 v[188:191], v[96:99], v[64:67], v[188:191]\n\t" \
  "v_mfma_f32_16x16x32_bf16 v[196:199], v[96:99], v[68:71], v[196:199]\n\t" \
  "v_mfma_f32_16x16x32_bf16 v[204:207], v[96:99], v[72:75], v[204:207]\n\t" \
  "v_mfma_f32_16x16x32_bf16 v[212:215], v[96:99], v[76:79], v[212:215]\n\t" \
  "v_mfma_f32_16x16x32_bf16 v[248:251], v[80:83], v[100:103], v[248:251]\n\t" \
  "v_mfma_f32_16x16x32_bf16 v[216:219], v[64:67], v[100:103], v[216:219]\n\t" \
  "v_mfma_f32_16x16x32_bf16 v[224:227], v[68:71], v[100:103], v[224:227]\n\t" \
  "v_mfma_f32_16x16x32_bf16 v[232:235], v[72:75], v[100:103], v[232:235]\n\t" \
  "v_mfma_f32_16x16x32_bf16 v[240:243], v[76:79], v[100:103], v[240:243]\n\t" \
  "s_waitcnt lgkmcnt(0)\n\t" \
  "s_barrier\n\t" \
  "v_mfma_f32_16x16x32_bf16 v[252:255], v[80:83], v[104:107], v[252:255]\n\t" \
  "ds_read_b64_tr_b16 v[92:93], v62 offset:26624\n\t" \
  "v_bfe_u32 v30, v4, 9, 1\n\t" \
  "v_mfma_f32_16x16x32_bf16 v[220:223], v[64:67], v[104:107], v[220:223]\n\t" \
  "ds_read_b64_tr_b16 v[94:95], v62 offset:27136\n\t" \
  "v_mad_i32_i24 v30, v30, -16, v0\n\t" \
  "v_mfma_f32_16x16x32_bf16 v[228:231], v[68:71], v[104:107], v[228:231]\n\t" \
  "ds_read_b64_tr_b16 v[96:97], v63 offset:26624\n\t" \
  "ds_read_b128 v[26:29], v0 offset:1552\n\t" \
  "v_mfma_f32_16x16x32_bf16 v[236:239], v[72:75], v[104:107], v[236:239]\n\t" \
  "ds_read_b64_tr_b16 v[98:99], v63 offset:27136\n\t" \
  "ds_read_b128 v[30:33], v30 offset:1040\n\t" \
  "v_mfma_f32_16x16x32_bf16 v[244:247], v[76:79], v[104:107], v[244:247]\n\t" \
  "ds_read_b64_tr_b16 v[100:101], v62 offset:34816\n\t" \
  "ds_read_b128 v[34:37], v0 offset:512\n\t" \
  "v_mfma_f32_16x16x32_bf16 v[152:155], v[84:87], v[64:67], v[152:155]\n\t" \
  "ds_read_b64_tr_b16 v[102:103], v62 offset:35328\n\t" \
  "ds_read_b64_tr_b16 v[38:39], v1 offset:512\n\t" \
  "v_mfma_f32_16x16x32_bf16 v[160:163], v[84:87], v[68:71], v[160:163]\n\t" \
  "ds_read_b128 v[80:83], v60 offset:0\n\t" \
  "ds_read_b64_tr_b16 v[40:41], v1 offset:0\n\t" \
  "v_mfma_f32_16x16x32_bf16 v[168:171], v[84:87], v[72:75], v[168:171]\n\t" \
  "ds_read_b64_tr_b16 v[104:105], v63 offset:34816\n\t" \
  "s_waitcnt vmcnt(0)\n\t" \
  "v_mfma_f32_16x16x32_bf16 v[176:179], v[84:87], v[76:79], v[176:179]\n\t" \
  "ds_read_b64_tr_b16 v[106:107], v63 offset:35328\n\t" \
  "v_perm_b32 v18, v10, v10, v14\n\t" \
  "v_mfma_f32_16x16x32_bf16 v[156:159], v[88:91], v[64:67], v[156:159]\n\t" \
  "ds_read_b128 v[64:67], v60 offset:10240\n\t" \
  "v_perm_b32 v19, v10, v10, v15\n\t" \
  "v_mfma_f32_16x16x32_bf16 v[164:167], v[88:91], v[68:71], v[164:167]\n\t" \
  "ds_read_b128 v[68:71], v60 offset:12288\n\t" \
  "v_perm_b32 v20, v10, v10, v16\n\t" \
  "v_mfma_f32_16x16x32_bf16 v[172:175], v[88:91], v[72:75], v[172:175]\n\t" \
  "ds_read_b128 v[72:75], v60 offset:14336\n\t" \
  "v_perm_b32 v21, v10, v10, v17\n\t" \
  "v_mfma_f32_16x16x32_bf16 v[180:183], v[88:91], v[76:79], v[180:183]\n\t" \
  "ds_read_b128 v[76:79], v60 offset:16384\n\t" \
  "v_perm_b32 v22, v11, v11, v14\n\t" \
  "v_mfma_f32_16x16x32_bf16 v[184:187], v[136:139], v[108:111], v[184:187]\n\t" \
  "ds_read_b64_tr_b16 v[84:85], v62 offset:18432\n\t" \
  "v_perm_b32 v23, v11, v11, v15\n\t" \
  "v_mfma_f32_16x16x32_bf16 v[192:195], v[136:139], v[112:115], v[192:195]\n\t" \
  "ds_read_b64_tr_b16 v[86:87], v62 offset:18944\n\t" \
  "v_perm_b32 v24, v11, v11, v16\n\t" \
  "v_mfma_f32_16x16x32_bf16 v[200:203], v[136:139], v[116:119], v[200:203]\n\t" \
  "ds_read_b64_tr_b16 v[88:89], v63 offset:18432\n\t" \
  "v_perm_b32 v25, v11, v11, v17\n\t" \
  "v_mfma_f32_16x16x32_bf16 v[208:211], v[136:139], v[120:123], v[208:211]\n\t" \
  "ds_read_b64_tr_b16 v[90:91], v63 offset:18944\n\t" \
  "v_mfma_f32_16x16x32_bf16 v[188:191], v[140:143], v[108:111], v[188:191]\n\t" \
  "ds_read_b64_tr_b16 v[136:137], v62 offset:30720\n\t" \
  "v_mfma_f32_16x16x32_bf16 v[196:199], v[140:143], v[112:115], v[196:199]\n\t" \
  "ds_read_b64_tr_b16 v[138:139], v62 offset:31232\n\t" \
  "s_waitcnt lgkmcnt(6)\n\t" \
  "v_mfma_f32_16x16x32_bf16 v[42:45], v[26:29], v[18:21], 0\n\t" \
  "v_mfma_f32_16x16x32_bf16 v[42:45], v[30:33], v[22:25], v[42:45]\n\t" \
  "v_mfma_f32_16x16x32_bf16 v[46:49], v[30:33], v[18:21], 0\n\t" \
  "v_mfma_f32_16x16x32_bf16 v[46:49], v[34:37], v[22:25], v[46:49]\n\t" \
  "ds_read_b128 v[26:29], v0 offset:1808\n\t" \
  "ds_read_b128 v[30:33], v0 offset:1296\n\t" \
  "v_mfma_f32_16x16x32_bf16 v[204:207], v[140:143], v[116:119], v[204:207]\n\t" \
  "ds_read_b128 v[34:37], v0 offset:768\n\t" \
  "v_mfma_f32_16x16x32_bf16 v[212:215], v[140:143], v[120:123], v[212:215]\n\t" \
  "ds_read_b64_tr_b16 v[140:141], v63 offset:30720\n\t" \
  "v_mfma_f32_16x16x32_bf16 v[248:251], v[124:127], v[144:147], v[248:251]\n\t" \
  "ds_read_b64_tr_b16 v[142:143], v63 offset:31232\n\t" \
  "v_lshlrev_b32 v50, 16, v38\n\t" \
  "v_mfma_f32_16x16x32_bf16 v[216:219], v[108:111], v[144:147], v[216:219]\n\t" \
  "v_lshlrev_b32 v51, 16, v40\n\t" \
  "v_mul_f32 v42, v42, v50\n\t" \
  "v_mfma_f32_16x16x32_bf16 v[224:227], v[112:115], v[144:147], v[224:227]\n\t" \
  "v_mul_f32 v46, v46, v51\n\t" \
  "v_cvt_pk_bf16_f32 v42, v42, v46\n\t" \
  "v_mfma_f32_16x16x32_bf16 v[232:235], v[116:119], v[144:147], v[232:235]\n\t" \
  "v_and_b32 v50, 0xffff0000, v38\n\t" \
  "v_and_b32 v51, 0xffff0000, v40\n\t" \
  "v_mfma_f32_16x16x32_bf16 v[240:243], v[120:123], v[144:147], v[240:243]\n\t" \
  "ds_read_b64_tr_b16 v[144:145], v62 offset:38912\n\t" \
  "v_mul_f32 v43, v43, v50\n\t" \
  "v_mfma_f32_16x16x32_bf16 v[252:255], v[124:127], v[148:151], v[252:255]\n\t" \
  "ds_read_b64_tr_b16 v[146:147], v62 offset:39424\n\t" \
  "v_mul_f32 v47, v47, v51\n\t" \
  "v_mfma_f32_16x16x32_bf16 v[220:223], v[108:111], v[148:151], v[220:223]\n\t" \
  "ds_read_b128 v[124:127], v61 offset:0\n\t" \
  "v_cvt_pk_bf16_f32 v43, v43, v47\n\t" \
  "v_mfma_f32_16x16x32_bf16 v[228:231], v[112:115], v[148:151], v[228:231]\n\t" \
  "v_lshlrev_b32 v50, 16, v39\n\t" \
  "v_lshlrev_b32 v51, 16, v41\n\t" \
  "v_mfma_f32_16x16x32_bf16 v[236:239], v[116:119], v[148:151], v[236:239]\n\t" \
  "v_mul_f32 v44, v44, v50\n\t" \
  "v_mul_f32 v48, v48, v51\n\t" \
  "v_mfma_f32_16x16x32_bf16 v[244:247], v[120:123], v[148:151], v[244:247]\n\t" \
  "ds_read_b64_tr_b16 v[148:149], v63 offset:38912\n\t" \
  "v_cvt_pk_bf16_f32 v44, v44, v48\n\t" \
  "v_mfma_f32_16x16x32_bf16 v[152:155], v[128:131], v[108:111], v[152:155]\n\t" \
  "ds_read_b64_tr_b16 v[150:151], v63 offset:39424\n\t" \
  "v_and_b32 v50, 0xffff0000, v39\n\t" \
  "v_mfma_f32_16x16x32_bf16 v[160:163], v[128:131], v[112:115], v[160:163]\n\t" \
  "v_and_b32 v51, 0xffff0000, v41\n\t" \
  "v_mul_f32 v45, v45, v50\n\t" \
  "v_mfma_f32_16x16x32_bf16 v[168:171], v[128:131], v[116:119], v[168:171]\n\t" \
  "v_mul_f32 v49, v49, v51\n\t" \
  "ds_read_b64_tr_b16 v[38:39], v1 offset:768\n\t" \
  "v_mfma_f32_16x16x32_bf16 v[176:179], v[128:131], v[120:123], v[176:179]\n\t" \
  "ds_read_b64_tr_b16 v[128:129], v62 offset:22528\n\t" \
  "ds_read_b64_tr_b16 v[40:41], v1 offset:256\n\t" \
  "v_mfma_f32_16x16x32_bf16 v[156:159], v[132:135], v[108:111], v[156:159]\n\t" \
  "ds_read_b128 v[108:111], v61 offset:10240\n\t" \
  "v_cvt_pk_bf16_f32 v45, v45, v49\n\t" \
  "v_mfma_f32_16x16x32_bf16 v[164:167], v[132:135], v[112:115], v[164:167]\n\t" \
  "ds_read_b128 v[112:115], v61 offset:12288\n\t" \
  "v_mfma_f32_16x16x32_bf16 v[172:175], v[132:135], v[116:119], v[172:175]\n\t" \
  "ds_read_b128 v[116:119], v61 offset:14336\n\t" \
  "v_mfma_f32_16x16x32_bf16 v[180:183], v[132:135], v[120:123], v[180:183]\n\t" \
  "ds_read_b128 v[120:123], v61 offset:16384\n\t" \
  "v_mfma_f32_16x16x32_bf16 v[184:187], v[92:95], v[64:67], v[184:187]\n\t" \
  "ds_read_b64_tr_b16 v[130:131], v62 offset:23040\n\t" \
  "v_mfma_f32_16x16x32_bf16 v[192:195], v[92:95], v[68:71], v[192:195]\n\t" \
  "ds_read_b64_tr_b16 v[132:133], v63 offset:22528\n\t" \
  "v_mfma_f32_16x16x32_bf16 v[200:203], v[92:95], v[72:75], v[200:203]\n\t" \
  "ds_read_b64_tr_b16 v[134:135], v63 offset:23040\n\t" \
  "v_mfma_f32_16x16x32_bf16 v[208:211], v[92:95], v[76:79], v[208:211]\n\t" \
  "v_mfma_f32_16x16x32_bf16 v[188:191], v[96:99], v[64:67], v[188:191]\n\t" \
  "v_mfma_f32_16x16x32_bf16 v[196:199], v[96:99], v[68:71], v[196:199]\n\t" \
  "v_mfma_f32_16x16x32_bf16 v[204:207], v[96:99], v[72:75], v[204:207]\n\t" \
  "v_mfma_f32_16x16x32_bf16 v[212:215], v[96:99], v[76:79], v[212:215]\n\t" \
  "v_mfma_f32_16x16x32_bf16 v[248:251], v[80:83], v[100:103], v[248:251]\n\t" \
  "v_mfma_f32_16x16x32_bf16 v[216:219], v[64:67], v[100:103], v[216:219]\n\t" \
  "global_store_dwordx4 v2, v[42:45], %24 offset:0 sc1\n\t" \
  "v_mfma_f32_16x16x32_bf16 v[224:227], v[68:71], v[100:103], v[224:227]\n\t" \
  "s_waitcnt lgkmcnt(0)\n\t" \
  "v_mfma_f32_16x16x32_bf16 v[42:45], v[26:29], v[18:21], 0\n\t" \
  "v_mfma_f32_16x16x32_bf16 v[42:45], v[30:33], v[22:25], v[42:45]\n\t" \
  "v_mfma_f32_16x16x32_bf16 v[46:49], v[30:33], v[18:21], 0\n\t" \
  "v_mfma_f32_16x16x32_bf16 v[46:49], v[34:37], v[22:25], v[46:49]\n\t" \
  "v_bfe_u32 v30, v4, 9, 1\n\t" \
  "v_mad_i32_i24 v30, v30, -16, v0\n\t" \
  "v_mfma_f32_16x16x32_bf16 v[232:235], v[72:75], v[100:103], v[232:235]\n\t" \
  "ds_read_b128 v[26:29], v0 offset:1584\n\t" \
  "ds_read_b128 v[30:33], v30 offset:1072\n\t" \
  "v_mfma_f32_16x16x32_bf16 v[240:243], v[76:79], v[100:103], v[240:243]\n\t" \
  "ds_read_b128 v[34:37], v0 offset:544\n\t" \
  "v_perm_b32 v18, v12, v12, v14\n\t" \
  "s_waitcnt lgkmcnt(0)\n\t" \
  "s_barrier\n\t" \
  "v_mfma_f32_16x16x32_bf16 v[252:255], v[80:83], v[104:107], v[252:255]\n\t" \
  "ds_read_b64_tr_b16 v[92:93], v54 offset:26624\n\t" \
  "v_perm_b32 v19, v12, v12, v15\n\t" \
  "v_mfma_f32_16x16x32_bf16 v[220:223], v[64:67], v[104:107], v[220:223]\n\t" \
  "ds_read_b64_tr_b16 v[94:95], v54 offset:27136\n\t" \
  "v_perm_b32 v20, v12, v12, v16\n\t" \
  "v_mfma_f32_16x16x32_bf16 v[228:231], v[68:71], v[104:107], v[228:231]\n\t" \
  "ds_read_b64_tr_b16 v[96:97], v55 offset:26624\n\t" \
  "v_perm_b32 v21, v12, v12, v17\n\t" \
  "v_mfma_f32_16x16x32_bf16 v[236:239], v[72:75], v[104:107], v[236:239]\n\t" \
  "ds_read_b64_tr_b16 v[98:99], v55 offset:27136\n\t" \
  "v_perm_b32 v22, v13, v13, v14\n\t" \
  "v_mfma_f32_16x16x32_bf16 v[244:247], v[76:79], v[104:107], v[244:247]\n\t" \
  "ds_read_b64_tr_b16 v[100:101], v54 offset:34816\n\t" \
  "v_perm_b32 v23, v13, v13, v15\n\t" \
  "v_mfma_f32_16x16x32_bf16 v[152:155], v[84:87], v[64:67], v[152:155]\n\t" \
  "ds_read_b64_tr_b16 v[102:103], v54 offset:35328\n\t" \
  "v_perm_b32 v24, v13, v13, v16\n\t" \
  "v_mfma_f32_16x16x32_bf16 v[160:163], v[84:87], v[68:71], v[160:163]\n\t" \
  "ds_read_b128 v[80:83], v52 offset:0\n\t" \
  "v_perm_b32 v25, v13, v13, v17\n\t" \
  "v_mfma_f32_16x16x32_bf16 v[168:171], v[84:87], v[72:75], v[168:171]\n\t" \
  "ds_read_b64_tr_b16 v[104:105], v55 offset:34816\n\t" \
  "v_lshlrev_b32 v50, 16, v38\n\t" \
  "v_mfma_f32_16x16x32_bf16 v[176:179], v[84:87], v[76:79], v[176:179]\n\t" \
  "ds_read_b64_tr_b16 v[106:107], v55 offset:35328\n\t" \
  "v_lshlrev_b32 v51, 16, v40\n\t" \
  "v_mfma_f32_16x16x32_bf16 v[156:159], v[88:91], v[64:67], v[156:159]\n\t" \
  "ds_read_b128 v[64:67], v52 offset:10240\n\t" \
  "v_mul_f32 v42, v42, v50\n\t" \
  "v_mfma_f32_16x16x32_bf16 v[164:167], v[88:91], v[68:71], v[164:167]\n\t" \
  "ds_read_b128 v[68:71], v52 offset:12288\n\t" \
  "v_mul_f32 v46, v46, v51\n\t" \
  "v_mfma_f32_16x16x32_bf16 v[172:175], v[88:91], v[72:75], v[172:175]\n\t" \
  "ds_read_b128 v[72:75], v52 offset:14336\n\t" \
  "v_cvt_pk_bf16_f32 v42, v42, v46\n\t" \
  "v_mfma_f32_16x16x32_bf16 v[180:183], v[88:91], v[76:79], v[180:183]\n\t" \
  "ds_read_b128 v[76:79], v52 offset:16384\n\t" \
  "v_and_b32 v50, 0xffff0000, v38\n\t" \
  "v_mfma_f32_16x16x32_bf16 v[184:187], v[136:139], v[108:111], v[184:187]\n\t" \
  "ds_read_b64_tr_b16 v[84:85], v54 offset:18432\n\t" \
  "v_and_b32 v51, 0xffff0000, v40\n\t" \
  "v_mfma_f32_16x16x32_bf16 v[192:195], v[136:139], v[112:115], v[192:195]\n\t" \
  "ds_read_b64_tr_b16 v[86:87], v54 offset:18944\n\t" \
  "v_mul_f32 v43, v43, v50\n\t" \
  "v_mfma_f32_16x16x32_bf16 v[200:203], v[136:139], v[116:119], v[200:203]\n\t" \
  "ds_read_b64_tr_b16 v[88:89], v55 offset:18432\n\t" \
  "v_mul_f32 v47, v47, v51\n\t" \
  "v_mfma_f32_16x16x32_bf16 v[208:211], v[136:139], v[120:123], v[208:211]\n\t" \
  "ds_read_b64_tr_b16 v[90:91], v55 offset:18944\n\t" \
  "v_cvt_pk_bf16_f32 v43, v43, v47\n\t" \
  "v_mfma_f32_16x16x32_bf16 v[188:191], v[140:143], v[108:111], v[188:191]\n\t" \
  "ds_read_b64_tr_b16 v[136:137], v54 offset:30720\n\t" \
  "v_lshlrev_b32 v50, 16, v39\n\t" \
  "v_mfma_f32_16x16x32_bf16 v[196:199], v[140:143], v[112:115], v[196:199]\n\t" \
  "ds_read_b64_tr_b16 v[138:139], v54 offset:31232\n\t" \
  "v_lshlrev_b32 v51, 16, v41\n\t" \
  "v_mfma_f32_16x16x32_bf16 v[204:207], v[140:143], v[116:119], v[204:207]\n\t" \
  "v_mul_f32 v44, v44, v50\n\t" \
  "v_mul_f32 v48, v48, v51\n\t" \
  "v_mfma_f32_16x16x32_bf16 v[212:215], v[140:143], v[120:123], v[212:215]\n\t" \
  "ds_read_b64_tr_b16 v[140:141], v55 offset:30720\n\t" \
  "v_cvt_pk_bf16_f32 v44, v44, v48\n\t" \
  "v_mfma_f32_16x16x32_bf16 v[248:251], v[124:127], v[144:147], v[248:251]\n\t" \
  "ds_read_b64_tr_b16 v[142:143], v55 offset:31232\n\t" \
  "v_and_b32 v50, 0xffff0000, v39\n\t" \
  "v_mfma_f32_16x16x32_bf16 v[216:219], v[108:111], v[144:147], v[216:219]\n\t" \
  "v_and_b32 v51, 0xffff0000, v41\n\t" \
  "v_mul_f32 v45, v45, v50\n\t" \
  "v_mfma_f32_16x16x32_bf16 v[224:227], v[112:115], v[144:147], v[224:227]\n\t" \
  "v_mul_f32 v49, v49, v51\n\t" \
  "ds_read_b64_tr_b16 v[38:39], v1 offset:544\n\t" \
  "v_mfma_f32_16x16x32_bf16 v[232:235], v[116:119], v[144:147], v[232:235]\n\t" \
  "ds_read_b64_tr_b16 v[40:41], v1 offset:32\n\t" \
  "v_cvt_pk_bf16_f32 v45, v45, v49\n\t" \
  "v_mfma_f32_16x16x32_bf16 v[240:243], v[120:123], v[144:147], v[240:243]\n\t" \
  "ds_read_b64_tr_b16 v[144:145], v54 offset:38912\n\t" \
  "v_mfma_f32_16x16x32_bf16 v[252:255], v[124:127], v[148:151], v[252:255]\n\t" \
  "ds_read_b64_tr_b16 v[146:147], v54 offset:39424\n\t" \
  "v_mfma_f32_16x16x32_bf16 v[220:223], v[108:111], v[148:151], v[220:223]\n\t" \
  "ds_read_b128 v[124:127], v53 offset:0\n\t" \
  "v_mfma_f32_16x16x32_bf16 v[228:231], v[112:115], v[148:151], v[228:231]\n\t" \
  "v_mfma_f32_16x16x32_bf16 v[236:239], v[116:119], v[148:151], v[236:239]\n\t" \
  "v_mfma_f32_16x16x32_bf16 v[244:247], v[120:123], v[148:151], v[244:247]\n\t" \
  "ds_read_b64_tr_b16 v[148:149], v55 offset:38912\n\t" \
  "v_mfma_f32_16x16x32_bf16 v[152:155], v[128:131], v[108:111], v[152:155]\n\t" \
  "ds_read_b64_tr_b16 v[150:151], v55 offset:39424\n\t" \
  "v_mfma_f32_16x16x32_bf16 v[160:163], v[128:131], v[112:115], v[160:163]\n\t" \
  "v_mfma_f32_16x16x32_bf16 v[168:171], v[128:131], v[116:119], v[168:171]\n\t" \
  "v_mfma_f32_16x16x32_bf16 v[176:179], v[128:131], v[120:123], v[176:179]\n\t" \
  "ds_read_b64_tr_b16 v[128:129], v54 offset:22528\n\t" \
  "v_mfma_f32_16x16x32_bf16 v[156:159], v[132:135], v[108:111], v[156:159]\n\t" \
  "ds_read_b128 v[108:111], v53 offset:10240\n\t" \
  "v_mfma_f32_16x16x32_bf16 v[164:167], v[132:135], v[112:115], v[164:167]\n\t" \
  "ds_read_b128 v[112:115], v53 offset:12288\n\t" \
  "v_mfma_f32_16x16x32_bf16 v[172:175], v[132:135], v[116:119], v[172:175]\n\t" \
  "ds_read_b128 v[116:119], v53 offset:14336\n\t" \
  "v_mfma_f32_16x16x32_bf16 v[180:183], v[132:135], v[120:123], v[180:183]\n\t" \
  "ds_read_b128 v[120:123], v53 offset:16384\n\t" \
  "s_waitcnt lgkmcnt(5)\n\t" \
  "v_mfma_f32_16x16x32_bf16 v[184:187], v[92:95], v[64:67], v[184:187]\n\t" \
  "ds_read_b64_tr_b16 v[130:131], v54 offset:23040\n\t" \
  "v_mfma_f32_16x16x32_bf16 v[192:195], v[92:95], v[68:71], v[192:195]\n\t" \
  "ds_read_b64_tr_b16 v[132:133], v55 offset:22528\n\t" \
  "v_mfma_f32_16x16x32_bf16 v[200:203], v[92:95], v[72:75], v[200:203]\n\t" \
  "ds_read_b64_tr_b16 v[134:135], v55 offset:23040\n\t" \
  "v_mfma_f32_16x16x32_bf16 v[208:211], v[92:95], v[76:79], v[208:211]\n\t" \
  "v_mfma_f32_16x16x32_bf16 v[188:191], v[96:99], v[64:67], v[188:191]\n\t" \
  "v_mfma_f32_16x16x32_bf16 v[196:199], v[96:99], v[68:71], v[196:199]\n\t" \
  "v_mfma_f32_16x16x32_bf16 v[204:207], v[96:99], v[72:75], v[204:207]\n\t" \
  "v_mfma_f32_16x16x32_bf16 v[212:215], v[96:99], v[76:79], v[212:215]\n\t" \
  "v_mfma_f32_16x16x32_bf16 v[248:251], v[80:83], v[100:103], v[248:251]\n\t" \
  "v_mfma_f32_16x16x32_bf16 v[216:219], v[64:67], v[100:103], v[216:219]\n\t" \
  "global_store_dwordx4 v2, v[42:45], %24 offset:1024 sc1\n\t" \
  "v_add_u32 v2, 0x10000, v2\n\t" \
  "v_mfma_f32_16x16x32_bf16 v[224:227], v[68:71], v[100:103], v[224:227]\n\t" \
  "v_mfma_f32_16x16x32_bf16 v[232:235], v[72:75], v[100:103], v[232:235]\n\t" \
  "v_mfma_f32_16x16x32_bf16 v[42:45], v[26:29], v[18:21], 0\n\t" \
  "v_mfma_f32_16x16x32_bf16 v[42:45], v[30:33], v[22:25], v[42:45]\n\t" \
  "v_mfma_f32_16x16x32_bf16 v[46:49], v[30:33], v[18:21], 0\n\t" \
  "v_mfma_f32_16x16x32_bf16 v[46:49], v[34:37], v[22:25], v[46:49]\n\t" \
  "ds_read_b128 v[26:29], v0 offset:1840\n\t" \
  "ds_read_b128 v[30:33], v0 offset:1328\n\t" \
  "v_mfma_f32_16x16x32_bf16 v[240:243], v[76:79], v[100:103], v[240:243]\n\t" \
  "ds_read_b128 v[34:37], v0 offset:800\n\t" \
  "s_waitcnt lgkmcnt(0)\n\t" \
  "s_barrier\n\t" \
  "v_mfma_f32_16x16x32_bf16 v[252:255], v[80:83], v[104:107], v[252:255]\n\t" \
  "ds_read_b64_tr_b16 v[92:93], v58 offset:26624\n\t" \
  "v_mfma_f32_16x16x32_bf16 v[220:223], v[64:67], v[104:107], v[220:223]\n\t" \
  "ds_read_b64_tr_b16 v[94:95], v58 offset:27136\n\t" \
  "v_lshlrev_b32 v50, 16, v38\n\t" \
  "v_mfma_f32_16x16x32_bf16 v[228:231], v[68:71], v[104:107], v[228:231]\n\t" \
  "ds_read_b64_tr_b16 v[96:97], v59 offset:26624\n\t" \
  "v_lshlrev_b32 v51, 16, v40\n\t" \
  "v_mfma_f32_16x16x32_bf16 v[236:239], v[72:75], v[104:107], v[236:239]\n\t" \
  "ds_read_b64_tr_b16 v[98:99], v59 offset:27136\n\t" \
  "v_mul_f32 v42, v42, v50\n\t" \
  "v_mfma_f32_16x16x32_bf16 v[244:247], v[76:79], v[104:107], v[244:247]\n\t" \
  "ds_read_b64_tr_b16 v[100:101], v58 offset:34816\n\t" \
  "v_mul_f32 v46, v46, v51\n\t" \
  "v_mfma_f32_16x16x32_bf16 v[152:155], v[84:87], v[64:67], v[152:155]\n\t" \
  "ds_read_b64_tr_b16 v[102:103], v58 offset:35328\n\t" \
  "v_cvt_pk_bf16_f32 v42, v42, v46\n\t" \
  "v_mfma_f32_16x16x32_bf16 v[160:163], v[84:87], v[68:71], v[160:163]\n\t" \
  "500:\n\t" \
  "s_cbranch_execz 504f\n\t" \
  "ds_read_b128 v[80:83], v56 offset:0\n\t" \
  "v_and_b32 v50, 0xffff0000, v38\n\t" \
  "v_mfma_f32_16x16x32_bf16 v[168:171], v[84:87], v[72:75], v[168:171]\n\t" \
  "ds_read_b64_tr_b16 v[104:105], v59 offset:34816\n\t" \
  "v_and_b32 v51, 0xffff0000, v40\n\t" \
  "v_mfma_f32_16x16x32_bf16 v[176:179], v[84:87], v[76:79], v[176:179]\n\t" \
  "ds_read_b64_tr_b16 v[106:107], v59 offset:35328\n\t" \
  "v_mul_f32 v43, v43, v50\n\t" \
  "v_mfma_f32_16x16x32_bf16 v[156:159], v[88:91], v[64:67], v[156:159]\n\t" \
  "ds_read_b128 v[64:67], v56 offset:10240\n\t" \
  "v_mul_f32 v47, v47, v51\n\t" \
  "v_mfma_f32_16x16x32_bf16 v[164:167], v[88:91], v[68:71], v[164:167]\n\t" \
  "ds_read_b128 v[68:71], v56 offset:12288\n\t" \
  "v_cvt_pk_bf16_f32 v43, v43, v47\n\t" \
  "v_mfma_f32_16x16x32_bf16 v[172:175], v[88:91], v[72:75], v[172:175]\n\t" \
  "ds_read_b128 v[72:75], v56 offset:14336\n\t" \
  "v_lshlrev_b32 v50, 16, v39\n\t" \
  "v_mfma_f32_16x16x32_bf16 v[180:183], v[88:91], v[76:79], v[180:183]\n\t" \
  "ds_read_b128 v[76:79], v56 offset:16384\n\t" \
  "v_lshlrev_b32 v51, 16, v41\n\t" \
  "v_mfma_f32_16x16x32_bf16 v[184:187], v[136:139], v[108:111], v[184:187]\n\t" \
  "ds_read_b64_tr_b16 v[84:85], v58 offset:18432\n\t" \
  "v_mul_f32 v44, v44, v50\n\t" \
  "v_mfma_f32_16x16x32_bf16 v[192:195], v[136:139], v[112:115], v[192:195]\n\t" \
  "ds_read_b64_tr_b16 v[86:87], v58 offset:18944\n\t" \
  "v_mul_f32 v48, v48, v51\n\t" \
  "v_mfma_f32_16x16x32_bf16 v[200:203], v[136:139], v[116:119], v[200:203]\n\t" \
  "ds_read_b64_tr_b16 v[88:89], v59 offset:18432\n\t" \
  "v_cvt_pk_bf16_f32 v44, v44, v48\n\t" \
  "v_mfma_f32_16x16x32_bf16 v[208:211], v[136:139], v[120:123], v[208:211]\n\t" \
  "ds_read_b64_tr_b16 v[90:91], v59 offset:18944\n\t" \
  "v_and_b32 v50, 0xffff0000, v39\n\t" \
  "v_mfma_f32_16x16x32_bf16 v[188:191], v[140:143], v[108:111], v[188:191]\n\t" \
  "ds_read_b64_tr_b16 v[136:137], v58 offset:30720\n\t" \
  "v_and_b32 v51, 0xffff0000, v41\n\t" \
  "v_mfma_f32_16x16x32_bf16 v[196:199], v[140:143], v[112:115], v[196:199]\n\t" \
  "ds_read_b64_tr_b16 v[138:139], v58 offset:31232\n\t" \
  "v_mul_f32 v45, v45, v50\n\t" \
  "v_mfma_f32_16x16x32_bf16 v[204:207], v[140:143], v[116:119], v[204:207]\n\t" \
  "v_mul_f32 v49, v49, v51\n\t" \
  "ds_read_b64_tr_b16 v[38:39], v1 offset:800\n\t" \
  "v_mfma_f32_16x16x32_bf16 v[212:215], v[140:143], v[120:123], v[212:215]\n\t" \
  "ds_read_b64_tr_b16 v[140:141], v59 offset:30720\n\t" \
  "ds_read_b64_tr_b16 v[40:41], v1 offset:288\n\t" \
  "v_mfma_f32_16x16x32_bf16 v[248:251], v[124:127], v[144:147], v[248:251]\n\t" \
  "ds_read_b64_tr_b16 v[142:143], v59 offset:31232\n\t" \
  "v_cvt_pk_bf16_f32 v45, v45, v49\n\t" \
  "v_mfma_f32_16x16x32_bf16 v[216:219], v[108:111], v[144:147], v[216:219]\n\t" \
  "v_mfma_f32_16x16x32_bf16 v[224:227], v[112:115], v[144:147], v[224:227]\n\t" \
  "v_mfma_f32_16x16x32_bf16 v[232:235], v[116:119], v[144:147], v[232:235]\n\t" \
  "v_mfma_f32_16x16x32_bf16 v[240:243], v[120:123], v[144:147], v[240:243]\n\t" \
  "ds_read_b64_tr_b16 v[144:145], v58 offset:38912\n\t" \
  "v_mfma_f32_16x16x32_bf16 v[252:255], v[124:127], v[148:151], v[252:255]\n\t" \
  "ds_read_b64_tr_b16 v[146:147], v58 offset:39424\n\t" \
  "v_mfma_f32_16x16x32_bf16 v[220:223], v[108:111], v[148:151], v[220:223]\n\t" \
  "ds_read_b128 v[124:127], v57 offset:0\n\t" \
  "v_mfma_f32_16x16x32_bf16 v[228:231], v[112:115], v[148:151], v[228:231]\n\t" \
  "v_mfma_f32_16x16x32_bf16 v[236:239], v[116:119], v[148:151], v[236:239]\n\t" \
  "v_mfma_f32_16x16x32_bf16 v[244:247], v[120:123], v[148:151], v[244:247]\n\t" \
  "ds_read_b64_tr_b16 v[148:149], v59 offset:38912\n\t" \
  "v_mfma_f32_16x16x32_bf16 v[152:155], v[128:131], v[108:111], v[152:155]\n\t" \
  "ds_read_b64_tr_b16 v[150:151], v59 offset:39424\n\t" \
  "v_mfma_f32_16x16x32_bf16 v[160:163], v[128:131], v[112:115], v[160:163]\n\t" \
  "v_mfma_f32_16x16x32_bf16 v[168:171], v[128:131], v[116:119], v[168:171]\n\t" \
  "v_mfma_f32_16x16x32_bf16 v[176:179], v[128:131], v[120:123], v[176:179]\n\t" \
  "ds_read_b64_tr_b16 v[128:129], v58 offset:22528\n\t" \
  "v_mfma_f32_16x16x32_bf16 v[156:159], v[132:135], v[108:111], v[156:159]\n\t" \
  "ds_read_b128 v[108:111], v57 offset:10240\n\t" \
  "v_mfma_f32_16x16x32_bf16 v[164:167], v[132:135], v[112:115], v[164:167]\n\t" \
  "ds_read_b128 v[112:115], v57 offset:12288\n\t" \
  "v_mfma_f32_16x16x32_bf16 v[172:175], v[132:135], v[116:119], v[172:175]\n\t" \
  "ds_read_b128 v[116:119], v57 offset:14336\n\t" \
  "v_mfma_f32_16x16x32_bf16 v[180:183], v[132:135], v[120:123], v[180:183]\n\t" \
  "ds_read_b128 v[120:123], v57 offset:16384\n\t" \
  "s_waitcnt lgkmcnt(5)\n\t" \
  "v_mfma_f32_16x16x32_bf16 v[184:187], v[92:95], v[64:67], v[184:187]\n\t" \
  "ds_read_b64_tr_b16 v[130:131], v58 offset:23040\n\t" \
  "v_mfma_f32_16x16x32_bf16 v[192:195], v[92:95], v[68:71], v[192:195]\n\t" \
  "ds_read_b64_tr_b16 v[132:133], v59 offset:22528\n\t" \
  "v_mfma_f32_16x16x32_bf16 v[200:203], v[92:95], v[72:75], v[200:203]\n\t" \
  "ds_read_b64_tr_b16 v[134:135], v59 offset:23040\n\t" \
  "v_mfma_f32_16x16x32_bf16 v[208:211], v[92:95], v[76:79], v[208:211]\n\t" \
  "v_mfma_f32_16x16x32_bf16 v[188:191], v[96:99], v[64:67], v[188:191]\n\t" \
  "v_mfma_f32_16x16x32_bf16 v[196:199], v[96:99], v[68:71], v[196:199]\n\t" \
  "v_mfma_f32_16x16x32_bf16 v[204:207], v[96:99], v[72:75], v[204:207]\n\t" \
  "v_mfma_f32_16x16x32_bf16 v[212:215], v[96:99], v[76:79], v[212:215]\n\t" \
  "v_mfma_f32_16x16x32_bf16 v[248:251], v[80:83], v[100:103], v[248:251]\n\t" \
  "v_mfma_f32_16x16x32_bf16 v[216:219], v[64:67], v[100:103], v[216:219]\n\t" \
  "global_store_dwordx4 v2, v[42:45], %24 offset:0 sc1\n\t" \
  "v_mfma_f32_16x16x32_bf16 v[224:227], v[68:71], v[100:103], v[224:227]\n\t" \
  "s_nop 0\n\t" \
  "v_mfma_f32_16x16x32_bf16 v[42:45], v[26:29], v[18:21], 0\n\t" \
  "v_mfma_f32_16x16x32_bf16 v[42:45], v[30:33], v[22:25], v[42:45]\n\t" \
  "v_mfma_f32_16x16x32_bf16 v[46:49], v[30:33], v[18:21], 0\n\t" \
  "v_mfma_f32_16x16x32_bf16 v[46:49], v[34:37], v[22:25], v[46:49]\n\t" \
  "v_mfma_f32_16x16x32_bf16 v[232:235], v[72:75], v[100:103], v[232:235]\n\t" \
  "v_mfma_f32_16x16x32_bf16 v[240:243], v[76:79], v[100:103], v[240:243]\n\t" \
  "v_lshlrev_b32 v50, 16, v38\n\t" \
  "v_lshlrev_b32 v51, 16, v40\n\t" \
  "s_waitcnt lgkmcnt(0)\n\t" \
  "s_barrier\n\t" \
  "v_mfma_f32_16x16x32_bf16 v[252:255], v[80:83], v[104:107], v[252:255]\n\t" \
  "ds_read_b64_tr_b16 v[92:93], v62 offset:26624\n\t" \
  "v_mul_f32 v42, v42, v50\n\t" \
  "v_mfma_f32_16x16x32_bf16 v[220:223], v[64:67], v[104:107], v[220:223]\n\t" \
  "ds_read_b64_tr_b16 v[94:95], v62 offset:27136\n\t" \
  "v_mul_f32 v46, v46, v51\n\t" \
  "v_mfma_f32_16x16x32_bf16 v[228:231], v[68:71], v[104:107], v[228:231]\n\t" \
  "ds_read_b64_tr_b16 v[96:97], v63 offset:26624\n\t" \
  "v_cvt_pk_bf16_f32 v42, v42, v46\n\t" \
  "v_mfma_f32_16x16x32_bf16 v[236:239], v[72:75], v[104:107], v[236:239]\n\t" \
  "ds_read_b64_tr_b16 v[98:99], v63 offset:27136\n\t" \
  "v_and_b32 v50, 0xffff0000, v38\n\t" \
  "v_mfma_f32_16x16x32_bf16 v[244:247], v[76:79], v[104:107], v[244:247]\n\t" \
  "ds_read_b64_tr_b16 v[100:101], v62 offset:34816\n\t" \
  "v_and_b32 v51, 0xffff0000, v40\n\t" \
  "v_mfma_f32_16x16x32_bf16 v[152:155], v[84:87], v[64:67], v[152:155]\n\t" \
  "ds_read_b64_tr_b16 v[102:103], v62 offset:35328\n\t" \
  "v_mul_f32 v43, v43, v50\n\t" \
  "v_mfma_f32_16x16x32_bf16 v[160:163], v[84:87], v[68:71], v[160:163]\n\t" \
  "ds_read_b128 v[80:83], v60 offset:0\n\t" \
  "v_mul_f32 v47, v47, v51\n\t" \
  "v_mfma_f32_16x16x32_bf16 v[168:171], v[84:87], v[72:75], v[168:171]\n\t" \
  "ds_read_b64_tr_b16 v[104:105], v63 offset:34816\n\t" \
  "v_cvt_pk_bf16_f32 v43, v43, v47\n\t" \
  "v_mfma_f32_16x16x32_bf16 v[176:179], v[84:87], v[76:79], v[176:179]\n\t" \
  "ds_read_b64_tr_b16 v[106:107], v63 offset:35328\n\t" \
  "v_lshlrev_b32 v50, 16, v39\n\t" \
  "v_mfma_f32_16x16x32_bf16 v[156:159], v[88:91], v[64:67], v[156:159]\n\t" \
  "ds_read_b128 v[64:67], v60 offset:10240\n\t" \
  "v_lshlrev_b32 v51, 16, v41\n\t" \
  "v_mfma_f32_16x16x32_bf16 v[164:167], v[88:91], v[68:71], v[164:167]\n\t" \
  "ds_read_b128 v[68:71], v60 offset:12288\n\t" \
  "v_mul_f32 v44, v44, v50\n\t" \
  "v_mfma_f32_16x16x32_bf16 v[172:175], v[88:91], v[72:75], v[172:175]\n\t" \
  "ds_read_b128 v[72:75], v60 offset:14336\n\t" \
  "v_mul_f32 v48, v48, v51\n\t" \
  "v_mfma_f32_16x16x32_bf16 v[180:183], v[88:91], v[76:79], v[180:183]\n\t" \
  "ds_read_b128 v[76:79], v60 offset:16384\n\t" \
  "v_cvt_pk_bf16_f32 v44, v44, v48\n\t" \
  "v_mfma_f32_16x16x32_bf16 v[184:187], v[136:139], v[108:111], v[184:187]\n\t" \
  "ds_read_b64_tr_b16 v[84:85], v62 offset:18432\n\t" \
  "v_and_b32 v50, 0xffff0000, v39\n\t" \
  "v_mfma_f32_16x16x32_bf16 v[192:195], v[136:139], v[112:115], v[192:195]\n\t" \
  "ds_read_b64_tr_b16 v[86:87], v62 offset:18944\n\t" \
  "v_and_b32 v51, 0xffff0000, v41\n\t" \
  "v_mfma_f32_16x16x32_bf16 v[200:203], v[136:139], v[116:119], v[200:203]\n\t" \
  "ds_read_b64_tr_b16 v[88:89], v63 offset:18432\n\t" \
  "v_mul_f32 v45, v45, v50\n\t" \
  "v_mfma_f32_16x16x32_bf16 v[208:211], v[136:139], v[120:123], v[208:211]\n\t" \
  "ds_read_b64_tr_b16 v[90:91], v63 offset:18944\n\t" \
  "v_mul_f32 v49, v49, v51\n\t" \
  "v_mfma_f32_16x16x32_bf16 v[188:191], v[140:143], v[108:111], v[188:191]\n\t" \
  "ds_read_b64_tr_b16 v[136:137], v62 offset:30720\n\t" \
  "v_cvt_pk_bf16_f32 v45, v45, v49\n\t" \
  "v_mfma_f32_16x16x32_bf16 v[196:199], v[140:143], v[112:115], v[196:199]\n\t" \
  "ds_read_b64_tr_b16 v[138:139], v62 offset:31232\n\t" \
  "v_mfma_f32_16x16x32_bf16 v[204:207], v[140:143], v[116:119], v[204:207]\n\t" \
  "v_mfma_f32_16x16x32_bf16 v[212:215], v[140:143], v[120:123], v[212:215]\n\t" \
  "ds_read_b64_tr_b16 v[140:141], v63 offset:30720\n\t" \
  "v_mfma_f32_16x16x32_bf16 v[248:251], v[124:127], v[144:147], v[248:251]\n\t" \
  "ds_read_b64_tr_b16 v[142:143], v63 offset:31232\n\t" \
  "v_mfma_f32_16x16x32_bf16 v[216:219], v[108:111], v[144:147], v[216:219]\n\t" \
  "v_mfma_f32_16x16x32_bf16 v[224:227], v[112:115], v[144:147], v[224:227]\n\t" \
  "v_mfma_f32_16x16x32_bf16 v[232:235], v[116:119], v[144:147], v[232:235]\n\t" \
  "v_mfma_f32_16x16x32_bf16 v[240:243], v[120:123], v[144:147], v[240:243]\n\t" \
  "ds_read_b64_tr_b16 v[144:145], v62 offset:38912\n\t" \
  "v_mfma_f32_16x16x32_bf16 v[252:255], v[124:127], v[148:151], v[252:255]\n\t" \
  "ds_read_b64_tr_b16 v[146:147], v62 offset:39424\n\t" \
  "v_mfma_f32_16x16x32_bf16 v[220:223], v[108:111], v[148:151], v[220:223]\n\t" \
  "ds_read_b128 v[124:127], v61 offset:0\n\t" \
  "v_mfma_f32_16x16x32_bf16 v[228:231], v[112:115], v[148:151], v[228:231]\n\t" \
  "v_mfma_f32_16x16x32_bf16 v[236:239], v[116:119], v[148:151], v[236:239]\n\t" \
  "v_mfma_f32_16x16x32_bf16 v[244:247], v[120:123], v[148:151], v[244:247]\n\t" \
  "ds_read_b64_tr_b16 v[148:149], v63 offset:38912\n\t" \
  "v_mfma_f32_16x16x32_bf16 v[152:155], v[128:131], v[108:111], v[152:155]\n\t" \
  "ds_read_b64_tr_b16 v[150:151], v63 offset:39424\n\t" \
  "v_mfma_f32_16x16x32_bf16 v[160:163], v[128:131], v[112:115], v[160:163]\n\t" \
  "v_mfma_f32_16x16x32_bf16 v[168:171], v[128:131], v[116:119], v[168:171]\n\t" \
  "v_mfma_f32_16x16x32_bf16 v[176:179], v[128:131], v[120:123], v[176:179]\n\t" \
  "ds_read_b64_tr_b16 v[128:129], v62 offset:22528\n\t" \
  "v_mfma_f32_16x16x32_bf16 v[156:159], v[132:135], v[108:111], v[156:159]\n\t" \
  "ds_read_b128 v[108:111], v61 offset:10240\n\t" \
  "v_mfma_f32_16x16x32_bf16 v[164:167], v[132:135], v[112:115], v[164:167]\n\t" \
  "ds_read_b128 v[112:115], v61 offset:12288\n\t" \
  "v_mfma_f32_16x16x32_bf16 v[172:175], v[132:135], v[116:119], v[172:175]\n\t" \
  "ds_read_b128 v[116:119], v61 offset:14336\n\t" \
  "v_mfma_f32_16x16x32_bf16 v[180:183], v[132:135], v[120:123], v[180:183]\n\t" \
  "ds_read_b128 v[120:123], v61 offset:16384\n\t" \
  "s_waitcnt lgkmcnt(5)\n\t" \
  "v_mfma_f32_16x16x32_bf16 v[184:187], v[92:95], v[64:67], v[184:187]\n\t" \
  "ds_read_b64_tr_b16 v[130:131], v62 offset:23040\n\t" \
  "v_mfma_f32_16x16x32_bf16 v[192:195], v[92:95], v[68:71], v[192:195]\n\t" \
  "ds_read_b64_tr_b16 v[132:133], v63 offset:22528\n\t" \
  "v_mfma_f32_16x16x32_bf16 v[200:203], v[92:95], v[72:75], v[200:203]\n\t" \
  "ds_read_b64_tr_b16 v[134:135], v63 offset:23040\n\t" \
  "v_mfma_f32_16x16x32_bf16 v[208:211], v[92:95], v[76:79], v[208:211]\n\t" \
  "v_mfma_f32_16x16x32_bf16 v[188:191], v[96:99], v[64:67], v[188:191]\n\t" \
  "v_mfma_f32_16x16x32_bf16 v[196:199], v[96:99], v[68:71], v[196:199]\n\t" \
  "v_mfma_f32_16x16x32_bf16 v[204:207], v[96:99], v[72:75], v[204:207]\n\t" \
  "v_mfma_f32_16x16x32_bf16 v[212:215], v[96:99], v[76:79], v[212:215]\n\t" \
  "v_mfma_f32_16x16x32_bf16 v[248:251], v[80:83], v[100:103], v[248:251]\n\t" \
  "v_mfma_f32_16x16x32_bf16 v[216:219], v[64:67], v[100:103], v[216:219]\n\t" \
  "global_store_dwordx4 v2, v[42:45], %24 offset:1024 sc1\n\t" \
  "v_mfma_f32_16x16x32_bf16 v[224:227], v[68:71], v[100:103], v[224:227]\n\t" \
  "v_mfma_f32_16x16x32_bf16 v[232:235], v[72:75], v[100:103], v[232:235]\n\t" \
  "v_mfma_f32_16x16x32_bf16 v[240:243], v[76:79], v[100:103], v[240:243]\n\t" \
  "v_mov_b32 v30, 0xff61b1e6\n\t" \
  "v_bfe_i32 v34, v3, 0, 1\n\t" \
  "s_waitcnt lgkmcnt(0)\n\t" \
  "s_barrier\n\t" \
  "v_mfma_f32_16x16x32_bf16 v[252:255], v[80:83], v[104:107], v[252:255]\n\t" \
  "ds_read_b64_tr_b16 v[92:93], v54 offset:26624\n\t" \
  "v_and_b32 v34, v30, v34\n\t" \
  "v_mfma_f32_16x16x32_bf16 v[220:223], v[64:67], v[104:107], v[220:223]\n\t" \
  "ds_read_b64_tr_b16 v[94:95], v54 offset:27136\n\t" \
  "v_bfe_i32 v38, v3, 4, 1\n\t" \
  "v_mfma_f32_16x16x32_bf16 v[228:231], v[68:71], v[104:107], v[228:231]\n\t" \
  "ds_read_b64_tr_b16 v[96:97], v55 offset:26624\n\t" \
  "v_and_b32 v38, v30, v38\n\t" \
  "v_mfma_f32_16x16x32_bf16 v[236:239], v[72:75], v[104:107], v[236:239]\n\t" \
  "ds_read_b64_tr_b16 v[98:99], v55 offset:27136\n\t" \
  "v_bfe_i32 v42, v3, 8, 1\n\t" \
  "v_mfma_f32_16x16x32_bf16 v[244:247], v[76:79], v[104:107], v[244:247]\n\t" \
  "ds_read_b64_tr_b16 v[100:101], v54 offset:34816\n\t" \
  "v_bfi_b32 v42, v42, v30, %15\n\t" \
  "v_mfma_f32_16x16x32_bf16 v[152:155], v[84:87], v[64:67], v[152:155]\n\t" \
  "ds_read_b64_tr_b16 v[102:103], v54 offset:35328\n\t" \
  "v_mov_b32 v48, 0x3f803f80\n\t" \
  "v_mfma_f32_16x16x32_bf16 v[160:163], v[84:87], v[68:71], v[160:163]\n\t" \
  "ds_read_b128 v[80:83], v52 offset:0\n\t" \
  "v_bfe_i32 v35, v3, 1, 1\n\t" \
  "v_mfma_f32_16x16x32_bf16 v[168:171], v[84:87], v[72:75], v[168:171]\n\t" \
  "ds_read_b64_tr_b16 v[104:105], v55 offset:34816\n\t" \
  "v_and_b32 v35, v30, v35\n\t" \
  "v_mfma_f32_16x16x32_bf16 v[176:179], v[84:87], v[76:79], v[176:179]\n\t" \
  "ds_read_b64_tr_b16 v[106:107], v55 offset:35328\n\t" \
  "v_bfe_i32 v39, v3, 5, 1\n\t" \
  "v_mfma_f32_16x16x32_bf16 v[156:159], v[88:91], v[64:67], v[156:159]\n\t" \
  "ds_read_b128 v[64:67], v52 offset:10240\n\t" \
  "v_and_b32 v39, v30, v39\n\t" \
  "v_mfma_f32_16x16x32_bf16 v[164:167], v[88:91], v[68:71], v[164:167]\n\t" \
  "ds_read_b128 v[68:71], v52 offset:12288\n\t" \
  "v_bfe_i32 v43, v3, 9, 1\n\t" \
  "v_mfma_f32_16x16x32_bf16 v[172:175], v[88:91], v[72:75], v[172:175]\n\t" \
  "ds_read_b128 v[72:75], v52 offset:14336\n\t" \
  "v_bfi_b32 v43, v43, v30, %15\n\t" \
  "v_mfma_f32_16x16x32_bf16 v[180:183], v[88:91], v[76:79], v[180:183]\n\t" \
  "ds_read_b128 v[76:79], v52 offset:16384\n\t" \
  "v_mov_b32 v49, 0x3f803f80\n\t" \
  "v_mfma_f32_16x16x32_bf16 v[184:187], v[136:139], v[108:111], v[184:187]\n\t" \
  "ds_read_b64_tr_b16 v[84:85], v54 offset:18432\n\t" \
  "v_bfe_i32 v36, v3, 2, 1\n\t" \
  "v_mfma_f32_16x16x32_bf16 v[192:195], v[136:139], v[112:115], v[192:195]\n\t" \
  "ds_read_b64_tr_b16 v[86:87], v54 offset:18944\n\t" \
  "v_and_b32 v36, v30, v36\n\t" \
  "v_mfma_f32_16x16x32_bf16 v[200:203], v[136:139], v[116:119], v[200:203]\n\t" \
  "ds_read_b64_tr_b16 v[88:89], v55 offset:18432\n\t" \
  "v_bfe_i32 v40, v3, 6, 1\n\t" \
  "v_mfma_f32_16x16x32_bf16 v[208:211], v[136:139], v[120:123], v[208:211]\n\t" \
  "ds_read_b64_tr_b16 v[90:91], v55 offset:18944\n\t" \
  "v_and_b32 v40, v30, v40\n\t" \
  "v_mfma_f32_16x16x32_bf16 v[188:191], v[140:143], v[108:111], v[188:191]\n\t" \
  "ds_read_b64_tr_b16 v[136:137], v54 offset:30720\n\t" \
  "v_bfe_i32 v44, v3, 10, 1\n\t" \
  "v_mfma_f32_16x16x32_bf16 v[196:199], v[140:143], v[112:115], v[196:199]\n\t" \
  "ds_read_b64_tr_b16 v[138:139], v54 offset:31232\n\t" \
  "v_bfi_b32 v44, v44, v30, %15\n\t" \
  "v_mfma_f32_16x16x32_bf16 v[204:207], v[140:143], v[116:119], v[204:207]\n\t" \
  "v_mov_b32 v50, 0x3f803f80\n\t" \
  "v_bfe_i32 v37, v3, 3, 1\n\t" \
  "v_mfma_f32_16x16x32_bf16 v[212:215], v[140:143], v[120:123], v[212:215]\n\t" \
  "ds_read_b64_tr_b16 v[140:141], v55 offset:30720\n\t" \
  "v_and_b32 v37, v30, v37\n\t" \
  "v_mfma_f32_16x16x32_bf16 v[248:251], v[124:127], v[144:147], v[248:251]\n\t" \
  "ds_read_b64_tr_b16 v[142:143], v55 offset:31232\n\t" \
  "v_bfe_i32 v41, v3, 7, 1\n\t" \
  "v_mfma_f32_16x16x32_bf16 v[216:219], v[108:111], v[144:147], v[216:219]\n\t" \
  "v_and_b32 v41, v30, v41\n\t" \
  "v_bfe_i32 v45, v3, 11, 1\n\t" \
  "v_mfma_f32_16x16x32_bf16 v[224:227], v[112:115], v[144:147], v[224:227]\n\t" \
  "v_bfi_b32 v45, v45, v30, %15\n\t" \
  "v_mov_b32 v51, 0x3f803f80\n\t" \
  "v_mfma_f32_16x16x32_bf16 v[232:235], v[116:119], v[144:147], v[232:235]\n\t" \
  "v_bfe_i32 v46, v3, 12, 1\n\t" \
  "v_bfe_i32 v31, v3, 13, 1\n\t" \
  "v_mfma_f32_16x16x32_bf16 v[240:243], v[120:123], v[144:147], v[240:243]\n\t" \
  "ds_read_b64_tr_b16 v[144:145], v54 offset:38912\n\t" \
  "v_perm_b32 v46, v31, v46, vcc_hi\n\t" \
  "v_mfma_f32_16x16x32_bf16 v[252:255], v[124:127], v[148:151], v[252:255]\n\t" \
  "ds_read_b64_tr_b16 v[146:147], v54 offset:39424\n\t" \
  "v_bfe_i32 v47, v3, 14, 1\n\t" \
  "v_mfma_f32_16x16x32_bf16 v[220:223], v[108:111], v[148:151], v[220:223]\n\t" \
  "ds_read_b128 v[124:127], v53 offset:0\n\t" \
  "v_bfe_i32 v31, v3, 15, 1\n\t" \
  "v_mfma_f32_16x16x32_bf16 v[228:231], v[112:115], v[148:151], v[228:231]\n\t" \
  "v_perm_b32 v47, v31, v47, vcc_hi\n\t" \
  "v_add_u32 v30, 18432, v4\n\t" \
  "v_mfma_f32_16x16x32_bf16 v[236:239], v[116:119], v[148:151], v[236:239]\n\t" \
  "v_xor_b32 v31, 1024, v30\n\t" \
  "v_mov_b32 v32, %12\n\t" \
  "v_mfma_f32_16x16x32_bf16 v[244:247], v[120:123], v[148:151], v[244:247]\n\t" \
  "ds_read_b64_tr_b16 v[148:149], v55 offset:38912\n\t" \
  "v_xor_b32 v33, 4, v32\n\t" \
  "v_mfma_f32_16x16x32_bf16 v[152:155], v[128:131], v[108:111], v[152:155]\n\t" \
  "ds_read_b64_tr_b16 v[150:151], v55 offset:39424\n\t" \
  "v_mov_b32 v0, 1\n\t" \
  "v_mfma_f32_16x16x32_bf16 v[160:163], v[128:131], v[112:115], v[160:163]\n\t" \
  "v_and_b32 v4, 1023, v4\n\t" \
  "v_add_u32 v1, 8192, v4\n\t" \
  "v_mfma_f32_16x16x32_bf16 v[168:171], v[128:131], v[116:119], v[168:171]\n\t" \
  "v_mfma_f32_16x16x32_bf16 v[176:179], v[128:131], v[120:123], v[176:179]\n\t" \
  "ds_read_b64_tr_b16 v[128:129], v54 offset:22528\n\t" \
  "v_mfma_f32_16x16x32_bf16 v[156:159], v[132:135], v[108:111], v[156:159]\n\t" \
  "ds_read_b128 v[108:111], v53 offset:10240\n\t" \
  "v_mfma_f32_16x16x32_bf16 v[164:167], v[132:135], v[112:115], v[164:167]\n\t" \
  "ds_read_b128 v[112:115], v53 offset:12288\n\t" \
  "v_mfma_f32_16x16x32_bf16 v[172:175], v[132:135], v[116:119], v[172:175]\n\t" \
  "ds_read_b128 v[116:119], v53 offset:14336\n\t" \
  "v_mfma_f32_16x16x32_bf16 v[180:183], v[132:135], v[120:123], v[180:183]\n\t" \
  "ds_read_b128 v[120:123], v53 offset:16384\n\t" \
  "s_waitcnt lgkmcnt(5)\n\t" \
  "v_mfma_f32_16x16x32_bf16 v[184:187], v[92:95], v[64:67], v[184:187]\n\t" \
  "ds_read_b64_tr_b16 v[130:131], v54 offset:23040\n\t" \
  "v_mfma_f32_16x16x32_bf16 v[192:195], v[92:95], v[68:71], v[192:195]\n\t" \
  "ds_read_b64_tr_b16 v[132:133], v55 offset:22528\n\t" \
  "v_mfma_f32_16x16x32_bf16 v[200:203], v[92:95], v[72:75], v[200:203]\n\t" \
  "ds_read_b64_tr_b16 v[134:135], v55 offset:23040\n\t" \
  "v_mfma_f32_16x16x32_bf16 v[208:211], v[92:95], v[76:79], v[208:211]\n\t" \
  "v_mfma_f32_16x16x32_bf16 v[188:191], v[96:99], v[64:67], v[188:191]\n\t" \
  "v_mfma_f32_16x16x32_bf16 v[196:199], v[96:99], v[68:71], v[196:199]\n\t" \
  "v_mfma_f32_16x16x32_bf16 v[204:207], v[96:99], v[72:75], v[204:207]\n\t" \
  "v_mfma_f32_16x16x32_bf16 v[212:215], v[96:99], v[76:79], v[212:215]\n\t" \
  "v_mfma_f32_16x16x32_bf16 v[248:251], v[80:83], v[100:103], v[248:251]\n\t" \
  "v_mfma_f32_16x16x32_bf16 v[216:219], v[64:67], v[100:103], v[216:219]\n\t" \
  "v_mfma_f32_16x16x32_bf16 v[224:227], v[68:71], v[100:103], v[224:227]\n\t" \
  "v_mfma_f32_16x16x32_bf16 v[232:235], v[72:75], v[100:103], v[232:235]\n\t" \
  "v_mfma_f32_16x16x32_bf16 v[240:243], v[76:79], v[100:103], v[240:243]\n\t" \
  "s_waitcnt lgkmcnt(0)\n\t" \
  "s_barrier\n\t" \
  "v_mfma_f32_16x16x32_bf16 v[252:255], v[80:83], v[104:107], v[252:255]\n\t" \
  "ds_read_b64_tr_b16 v[92:93], v58 offset:26624\n\t" \
  "v_mfma_f32_16x16x32_bf16 v[220:223], v[64:67], v[104:107], v[220:223]\n\t" \
  "ds_read_b64_tr_b16 v[94:95], v58 offset:27136\n\t" \
  "v_mfma_f32_16x16x32_bf16 v[228:231], v[68:71], v[104:107], v[228:231]\n\t" \
  "ds_read_b64_tr_b16 v[96:97], v59 offset:26624\n\t" \
  "v_mfma_f32_16x16x32_bf16 v[236:239], v[72:75], v[104:107], v[236:239]\n\t" \
  "ds_read_b64_tr_b16 v[98:99], v59 offset:27136\n\t" \
  "v_mfma_f32_16x16x32_bf16 v[244:247], v[76:79], v[104:107], v[244:247]\n\t" \
  "ds_read_b64_tr_b16 v[100:101], v58 offset:34816\n\t" \
  "v_mfma_f32_16x16x32_bf16 v[152:155], v[84:87], v[64:67], v[152:155]\n\t" \
  "ds_read_b64_tr_b16 v[102:103], v58 offset:35328\n\t" \
  "v_mfma_f32_16x16x32_bf16 v[160:163], v[84:87], v[68:71], v[160:163]\n\t" \
  "ds_read_b128 v[80:83], v56 offset:0\n\t" \
  "v_mfma_f32_16x16x32_bf16 v[168:171], v[84:87], v[72:75], v[168:171]\n\t" \
  "ds_read_b64_tr_b16 v[104:105], v59 offset:34816\n\t" \
  "v_mfma_f32_16x16x32_bf16 v[176:179], v[84:87], v[76:79], v[176:179]\n\t" \
  "ds_read_b64_tr_b16 v[106:107], v59 offset:35328\n\t" \
  "v_mfma_f32_16x16x32_bf16 v[156:159], v[88:91], v[64:67], v[156:159]\n\t" \
  "ds_read_b128 v[64:67], v56 offset:10240\n\t" \
  "v_mfma_f32_16x16x32_bf16 v[164:167], v[88:91], v[68:71], v[164:167]\n\t" \
  "ds_read_b128 v[68:71], v56 offset:12288\n\t" \
  "v_mfma_f32_16x16x32_bf16 v[172:175], v[88:91], v[72:75], v[172:175]\n\t" \
  "ds_read_b128 v[72:75], v56 offset:14336\n\t" \
  "v_mfma_f32_16x16x32_bf16 v[180:183], v[88:91], v[76:79], v[180:183]\n\t" \
  "ds_read_b128 v[76:79], v56 offset:16384\n\t" \
  "v_mfma_f32_16x16x32_bf16 v[184:187], v[136:139], v[108:111], v[184:187]\n\t" \
  "ds_read_b64_tr_b16 v[84:85], v58 offset:18432\n\t" \
  "v_mfma_f32_16x16x32_bf16 v[192:195], v[136:139], v[112:115], v[192:195]\n\t" \
  "ds_read_b64_tr_b16 v[86:87], v58 offset:18944\n\t" \
  "v_mfma_f32_16x16x32_bf16 v[200:203], v[136:139], v[116:119], v[200:203]\n\t" \
  "ds_read_b64_tr_b16 v[88:89], v59 offset:18432\n\t" \
  "v_mfma_f32_16x16x32_bf16 v[208:211], v[136:139], v[120:123], v[208:211]\n\t" \
  "ds_read_b64_tr_b16 v[90:91], v59 offset:18944\n\t" \
  "v_mfma_f32_16x16x32_bf16 v[188:191], v[140:143], v[108:111], v[188:191]\n\t" \
  "ds_read_b64_tr_b16 v[136:137], v58 offset:30720\n\t" \
  "v_mfma_f32_16x16x32_bf16 v[196:199], v[140:143], v[112:115], v[196:199]\n\t" \
  "ds_read_b64_tr_b16 v[138:139], v58 offset:31232\n\t" \
  "v_mfma_f32_16x16x32_bf16 v[204:207], v[140:143], v[116:119], v[204:207]\n\t" \
  "v_mfma_f32_16x16x32_bf16 v[212:215], v[140:143], v[120:123], v[212:215]\n\t" \
  "ds_read_b64_tr_b16 v[140:141], v59 offset:30720\n\t" \
  "v_mfma_f32_16x16x32_bf16 v[248:251], v[124:127], v[144:147], v[248:251]\n\t" \
  "ds_read_b64_tr_b16 v[142:143], v59 offset:31232\n\t" \
  "v_mfma_f32_16x16x32_bf16 v[216:219], v[108:111], v[144:147], v[216:219]\n\t" \
  "v_mfma_f32_16x16x32_bf16 v[224:227], v[112:115], v[144:147], v[224:227]\n\t" \
  "v_mfma_f32_16x16x32_bf16 v[232:235], v[116:119], v[144:147], v[232:235]\n\t" \
  "v_mfma_f32_16x16x32_bf16 v[240:243], v[120:123], v[144:147], v[240:243]\n\t" \
  "ds_read_b64_tr_b16 v[144:145], v58 offset:38912\n\t" \
  "v_mfma_f32_16x16x32_bf16 v[252:255], v[124:127], v[148:151], v[252:255]\n\t" \
  "ds_read_b64_tr_b16 v[146:147], v58 offset:39424\n\t" \
  "v_mfma_f32_16x16x32_bf16 v[220:223], v[108:111], v[148:151], v[220:223]\n\t" \
  "ds_read_b128 v[124:127], v57 offset:0\n\t" \
  "v_mfma_f32_16x16x32_bf16 v[228:231], v[112:115], v[148:151], v[228:231]\n\t" \
  "v_mfma_f32_16x16x32_bf16 v[236:239], v[116:119], v[148:151], v[236:239]\n\t" \
  "v_mfma_f32_16x16x32_bf16 v[244:247], v[120:123], v[148:151], v[244:247]\n\t" \
  "ds_read_b64_tr_b16 v[148:149], v59 offset:38912\n\t" \
  "v_mfma_f32_16x16x32_bf16 v[152:155], v[128:131], v[108:111], v[152:155]\n\t" \
  "ds_read_b64_tr_b16 v[150:151], v59 offset:39424\n\t" \
  "v_mfma_f32_16x16x32_bf16 v[160:163], v[128:131], v[112:115], v[160:163]\n\t" \
  "v_mfma_f32_16x16x32_bf16 v[168:171], v[128:131], v[116:119], v[168:171]\n\t" \
  "v_mfma_f32_16x16x32_bf16 v[176:179], v[128:131], v[120:123], v[176:179]\n\t" \
  "ds_read_b64_tr_b16 v[128:129], v58 offset:22528\n\t" \
  "v_mfma_f32_16x16x32_bf16 v[156:159], v[132:135], v[108:111], v[156:159]\n\t" \
  "ds_read_b128 v[108:111], v57 offset:10240\n\t" \
  "v_mfma_f32_16x16x32_bf16 v[164:167], v[132:135], v[112:115], v[164:167]\n\t" \
  "ds_read_b128 v[112:115], v57 offset:12288\n\t" \
  "v_mfma_f32_16x16x32_bf16 v[172:175], v[132:135], v[116:119], v[172:175]\n\t" \
  "ds_read_b128 v[116:119], v57 offset:14336\n\t" \
  "v_mfma_f32_16x16x32_bf16 v[180:183], v[132:135], v[120:123], v[180:183]\n\t" \
  "ds_read_b128 v[120:123], v57 offset:16384\n\t" \
  "s_waitcnt lgkmcnt(5)\n\t" \
  "v_mfma_f32_16x16x32_bf16 v[184:187], v[92:95], v[64:67], v[184:187]\n\t" \
  "ds_read_b64_tr_b16 v[130:131], v58 offset:23040\n\t" \
  "v_mfma_f32_16x16x32_bf16 v[192:195], v[92:95], v[68:71], v[192:195]\n\t" \
  "ds_read_b64_tr_b16 v[132:133], v59 offset:22528\n\t" \
  "v_mfma_f32_16x16x32_bf16 v[200:203], v[92:95], v[72:75], v[200:203]\n\t" \
  "ds_read_b64_tr_b16 v[134:135], v59 offset:23040\n\t" \
  "v_mfma_f32_16x16x32_bf16 v[208:211], v[92:95], v[76:79], v[208:211]\n\t" \
  "v_mfma_f32_16x16x32_bf16 v[188:191], v[96:99], v[64:67], v[188:191]\n\t" \
  "v_mfma_f32_16x16x32_bf16 v[196:199], v[96:99], v[68:71], v[196:199]\n\t" \
  "v_mfma_f32_16x16x32_bf16 v[204:207], v[96:99], v[72:75], v[204:207]\n\t" \
  "v_mfma_f32_16x16x32_bf16 v[212:215], v[96:99], v[76:79], v[212:215]\n\t" \
  "v_mfma_f32_16x16x32_bf16 v[248:251], v[80:83], v[100:103], v[248:251]\n\t" \
  "v_mfma_f32_16x16x32_bf16 v[216:219], v[64:67], v[100:103], v[216:219]\n\t" \
  "v_mfma_f32_16x16x32_bf16 v[224:227], v[68:71], v[100:103], v[224:227]\n\t" \
  "v_mfma_f32_16x16x32_bf16 v[232:235], v[72:75], v[100:103], v[232:235]\n\t" \
  "v_mfma_f32_16x16x32_bf16 v[240:243], v[76:79], v[100:103], v[240:243]\n\t" \
  "s_waitcnt lgkmcnt(0)\n\t" \
  "s_waitcnt vmcnt(0)\n\t" \
  "s_barrier\n\t" \
  "v_mfma_f32_16x16x32_bf16 v[252:255], v[80:83], v[104:107], v[252:255]\n\t" \
  "ds_read_b64_tr_b16 v[92:93], v62 offset:26624\n\t" \
  "v_mfma_f32_16x16x32_bf16 v[220:223], v[64:67], v[104:107], v[220:223]\n\t" \
  "ds_read_b64_tr_b16 v[94:95], v62 offset:27136\n\t" \
  "v_mfma_f32_16x16x32_bf16 v[228:231], v[68:71], v[104:107], v[228:231]\n\t" \
  "ds_read_b64_tr_b16 v[96:97], v63 offset:26624\n\t" \
  "v_mfma_f32_16x16x32_bf16 v[236:239], v[72:75], v[104:107], v[236:239]\n\t" \
  "ds_read_b64_tr_b16 v[98:99], v63 offset:27136\n\t" \
  "v_mfma_f32_16x16x32_bf16 v[244:247], v[76:79], v[104:107], v[244:247]\n\t" \
  "ds_read_b64_tr_b16 v[100:101], v62 offset:34816\n\t" \
  "v_mfma_f32_16x16x32_bf16 v[152:155], v[84:87], v[64:67], v[152:155]\n\t" \
  "ds_read_b64_tr_b16 v[102:103], v62 offset:35328\n\t" \
  "v_mfma_f32_16x16x32_bf16 v[160:163], v[84:87], v[68:71], v[160:163]\n\t" \
  "ds_read_b128 v[80:83], v60 offset:0\n\t" \
  "v_mfma_f32_16x16x32_bf16 v[168:171], v[84:87], v[72:75], v[168:171]\n\t" \
  "ds_read_b64_tr_b16 v[104:105], v63 offset:34816\n\t" \
  "v_mfma_f32_16x16x32_bf16 v[176:179], v[84:87], v[76:79], v[176:179]\n\t" \
  "ds_read_b64_tr_b16 v[106:107], v63 offset:35328\n\t" \
  "v_mfma_f32_16x16x32_bf16 v[156:159], v[88:91], v[64:67], v[156:159]\n\t" \
  "ds_read_b128 v[64:67], v60 offset:10240\n\t" \
  "v_mfma_f32_16x16x32_bf16 v[164:167], v[88:91], v[68:71], v[164:167]\n\t" \
  "ds_read_b128 v[68:71], v60 offset:12288\n\t" \
  "v_mfma_f32_16x16x32_bf16 v[172:175], v[88:91], v[72:75], v[172:175]\n\t" \
  "ds_read_b128 v[72:75], v60 offset:14336\n\t" \
  "v_mfma_f32_16x16x32_bf16 v[180:183], v[88:91], v[76:79], v[180:183]\n\t" \
  "ds_read_b128 v[76:79], v60 offset:16384\n\t" \
  "v_mfma_f32_16x16x32_bf16 v[184:187], v[136:139], v[108:111], v[184:187]\n\t" \
  "ds_read_b64_tr_b16 v[84:85], v62 offset:18432\n\t" \
  "v_mfma_f32_16x16x32_bf16 v[192:195], v[136:139], v[112:115], v[192:195]\n\t" \
  "ds_read_b64_tr_b16 v[86:87], v62 offset:18944\n\t" \
  "v_mfma_f32_16x16x32_bf16 v[200:203], v[136:139], v[116:119], v[200:203]\n\t" \
  "ds_read_b64_tr_b16 v[88:89], v63 offset:18432\n\t" \
  "v_mfma_f32_16x16x32_bf16 v[208:211], v[136:139], v[120:123], v[208:211]\n\t" \
  "ds_read_b64_tr_b16 v[90:91], v63 offset:18944\n\t" \
  "v_mfma_f32_16x16x32_bf16 v[188:191], v[140:143], v[108:111], v[188:191]\n\t" \
  "ds_read_b64_tr_b16 v[136:137], v62 offset:30720\n\t" \
  "v_mfma_f32_16x16x32_bf16 v[196:199], v[140:143], v[112:115], v[196:199]\n\t" \
  "ds_read_b64_tr_b16 v[138:139], v62 offset:31232\n\t" \
  "v_mfma_f32_16x16x32_bf16 v[204:207], v[140:143], v[116:119], v[204:207]\n\t" \
  "v_mfma_f32_16x16x32_bf16 v[212:215], v[140:143], v[120:123], v[212:215]\n\t" \
  "ds_read_b64_tr_b16 v[140:141], v63 offset:30720\n\t" \
  "v_mfma_f32_16x16x32_bf16 v[248:251], v[124:127], v[144:147], v[248:251]\n\t" \
  "ds_read_b64_tr_b16 v[142:143], v63 offset:31232\n\t" \
  "v_mfma_f32_16x16x32_bf16 v[216:219], v[108:111], v[144:147], v[216:219]\n\t" \
  "v_mfma_f32_16x16x32_bf16 v[224:227], v[112:115], v[144:147], v[224:227]\n\t" \
  "v_mfma_f32_16x16x32_bf16 v[232:235], v[116:119], v[144:147], v[232:235]\n\t" \
  "v_mfma_f32_16x16x32_bf16 v[240:243], v[120:123], v[144:147], v[240:243]\n\t" \
  "ds_read_b64_tr_b16 v[144:145], v62 offset:38912\n\t" \
  "v_mfma_f32_16x16x32_bf16 v[252:255], v[124:127], v[148:151], v[252:255]\n\t" \
  "ds_read_b64_tr_b16 v[146:147], v62 offset:39424\n\t" \
  "v_mfma_f32_16x16x32_bf16 v[220:223], v[108:111], v[148:151], v[220:223]\n\t" \
  "ds_read_b128 v[124:127], v61 offset:0\n\t" \
  "v_mfma_f32_16x16x32_bf16 v[228:231], v[112:115], v[148:151], v[228:231]\n\t" \
  "v_mfma_f32_16x16x32_bf16 v[236:239], v[116:119], v[148:151], v[236:239]\n\t" \
  "v_mfma_f32_16x16x32_bf16 v[244:247], v[120:123], v[148:151], v[244:247]\n\t" \
  "ds_read_b64_tr_b16 v[148:149], v63 offset:38912\n\t" \
  "v_mfma_f32_16x16x32_bf16 v[152:155], v[128:131], v[108:111], v[152:155]\n\t" \
  "ds_read_b64_tr_b16 v[150:151], v63 offset:39424\n\t" \
  "v_mfma_f32_16x16x32_bf16 v[160:163], v[128:131], v[112:115], v[160:163]\n\t" \
  "v_mfma_f32_16x16x32_bf16 v[168:171], v[128:131], v[116:119], v[168:171]\n\t" \
  "v_mfma_f32_16x16x32_bf16 v[176:179], v[128:131], v[120:123], v[176:179]\n\t" \
  "ds_read_b64_tr_b16 v[128:129], v62 offset:22528\n\t" \
  "v_mfma_f32_16x16x32_bf16 v[156:159], v[132:135], v[108:111], v[156:159]\n\t" \
  "ds_read_b128 v[108:111], v61 offset:10240\n\t" \
  "v_mfma_f32_16x16x32_bf16 v[164:167], v[132:135], v[112:115], v[164:167]\n\t" \
  "ds_read_b128 v[112:115], v61 offset:12288\n\t" \
  "v_mfma_f32_16x16x32_bf16 v[172:175], v[132:135], v[116:119], v[172:175]\n\t" \
  "ds_read_b128 v[116:119], v61 offset:14336\n\t" \
  "v_mfma_f32_16x16x32_bf16 v[180:183], v[132:135], v[120:123], v[180:183]\n\t" \
  "ds_read_b128 v[120:123], v61 offset:16384\n\t" \
  "s_waitcnt lgkmcnt(5)\n\t" \
  "v_mfma_f32_16x16x32_bf16 v[184:187], v[92:95], v[64:67], v[184:187]\n\t" \
  "ds_read_b64_tr_b16 v[130:131], v62 offset:23040\n\t" \
  "v_mfma_f32_16x16x32_bf16 v[192:195], v[92:95], v[68:71], v[192:195]\n\t" \
  "ds_read_b64_tr_b16 v[132:133], v63 offset:22528\n\t" \
  "v_mfma_f32_16x16x32_bf16 v[200:203], v[92:95], v[72:75], v[200:203]\n\t" \
  "ds_read_b64_tr_b16 v[134:135], v63 offset:23040\n\t" \
  "v_mfma_f32_16x16x32_bf16 v[208:211], v[92:95], v[76:79], v[208:211]\n\t" \
  "v_mfma_f32_16x16x32_bf16 v[188:191], v[96:99], v[64:67], v[188:191]\n\t" \
  "v_mfma_f32_16x16x32_bf16 v[196:199], v[96:99], v[68:71], v[196:199]\n\t" \
  "v_mfma_f32_16x16x32_bf16 v[204:207], v[96:99], v[72:75], v[204:207]\n\t" \
  "v_mfma_f32_16x16x32_bf16 v[212:215], v[96:99], v[76:79], v[212:215]\n\t" \
  "v_mfma_f32_16x16x32_bf16 v[248:251], v[80:83], v[100:103], v[248:251]\n\t" \
  "v_mfma_f32_16x16x32_bf16 v[216:219], v[64:67], v[100:103], v[216:219]\n\t" \
  "v_mfma_f32_16x16x32_bf16 v[224:227], v[68:71], v[100:103], v[224:227]\n\t" \
  "v_mfma_f32_16x16x32_bf16 v[232:235], v[72:75], v[100:103], v[232:235]\n\t" \
  "v_mfma_f32_16x16x32_bf16 v[240:243], v[76:79], v[100:103], v[240:243]\n\t" \
  "s_waitcnt lgkmcnt(0)\n\t" \
  "s_barrier\n\t" \
  "v_mfma_f32_16x16x32_bf16 v[252:255], v[80:83], v[104:107], v[252:255]\n\t" \
  "ds_read_b64_tr_b16 v[92:93], v54 offset:26624\n\t" \
  "v_mfma_f32_16x16x32_bf16 v[220:223], v[64:67], v[104:107], v[220:223]\n\t" \
  "ds_read_b64_tr_b16 v[94:95], v54 offset:27136\n\t" \
  "v_mfma_f32_16x16x32_bf16 v[228:231], v[68:71], v[104:107], v[228:231]\n\t" \
  "ds_read_b64_tr_b16 v[96:97], v55 offset:26624\n\t" \
  "v_mfma_f32_16x16x32_bf16 v[236:239], v[72:75], v[104:107], v[236:239]\n\t" \
  "ds_read_b64_tr_b16 v[98:99], v55 offset:27136\n\t" \
  "v_mfma_f32_16x16x32_bf16 v[244:247], v[76:79], v[104:107], v[244:247]\n\t" \
  "ds_read_b64_tr_b16 v[100:101], v54 offset:34816\n\t" \
  "v_mfma_f32_16x16x32_bf16 v[152:155], v[84:87], v[64:67], v[152:155]\n\t" \
  "ds_read_b64_tr_b16 v[102:103], v54 offset:35328\n\t" \
  "v_mfma_f32_16x16x32_bf16 v[160:163], v[84:87], v[68:71], v[160:163]\n\t" \
  "ds_read_b128 v[80:83], v52 offset:0\n\t" \
  "v_mfma_f32_16x16x32_bf16 v[168:171], v[84:87], v[72:75], v[168:171]\n\t" \
  "ds_read_b64_tr_b16 v[104:105], v55 offset:34816\n\t" \
  "v_mfma_f32_16x16x32_bf16 v[176:179], v[84:87], v[76:79], v[176:179]\n\t" \
  "501:\n\t" \
  "s_cbranch_execz 505f\n\t" \
  "ds_read_b64_tr_b16 v[106:107], v55 offset:35328\n\t" \
  "v_mfma_f32_16x16x32_bf16 v[156:159], v[88:91], v[64:67], v[156:159]\n\t" \
  "ds_read_b128 v[64:67], v52 offset:10240\n\t" \
  "v_mfma_f32_16x16x32_bf16 v[164:167], v[88:91], v[68:71], v[164:167]\n\t" \
  "ds_read_b128 v[68:71], v52 offset:12288\n\t" \
  "v_mfma_f32_16x16x32_bf16 v[172:175], v[88:91], v[72:75], v[172:175]\n\t" \
  "ds_read_b128 v[72:75], v52 offset:14336\n\t" \
  "v_mfma_f32_16x16x32_bf16 v[180:183], v[88:91], v[76:79], v[180:183]\n\t" \
  "ds_read_b128 v[76:79], v52 offset:16384\n\t" \
  "v_mfma_f32_16x16x32_bf16 v[184:187], v[136:139], v[108:111], v[184:187]\n\t" \
  "ds_read_b64_tr_b16 v[84:85], v54 offset:18432\n\t" \
  "v_mfma_f32_16x16x32_bf16 v[192:195], v[136:139], v[112:115], v[192:195]\n\t" \
  "ds_read_b64_tr_b16 v[86:87], v54 offset:18944\n\t" \
  "v_mfma_f32_16x16x32_bf16 v[200:203], v[136:139], v[116:119], v[200:203]\n\t" \
  "ds_read_b64_tr_b16 v[88:89], v55 offset:18432\n\t" \
  "v_mfma_f32_16x16x32_bf16 v[208:211], v[136:139], v[120:123], v[208:211]\n\t" \
  "ds_read_b64_tr_b16 v[90:91], v55 offset:18944\n\t" \
  "v_mfma_f32_16x16x32_bf16 v[188:191], v[140:143], v[108:111], v[188:191]\n\t" \
  "ds_read_b64_tr_b16 v[136:137], v54 offset:30720\n\t" \
  "v_mfma_f32_16x16x32_bf16 v[196:199], v[140:143], v[112:115], v[196:199]\n\t" \
  "ds_read_b64_tr_b16 v[138:139], v54 offset:31232\n\t" \
  "v_mfma_f32_16x16x32_bf16 v[204:207], v[140:143], v[116:119], v[204:207]\n\t" \
  "v_mfma_f32_16x16x32_bf16 v[212:215], v[140:143], v[120:123], v[212:215]\n\t" \
  "ds_read_b64_tr_b16 v[140:141], v55 offset:30720\n\t" \
  "v_mfma_f32_16x16x32_bf16 v[248:251], v[124:127], v[144:147], v[248:251]\n\t" \
  "ds_read_b64_tr_b16 v[142:143], v55 offset:31232\n\t" \
  "v_mfma_f32_16x16x32_bf16 v[216:219], v[108:111], v[144:147], v[216:219]\n\t" \
  "v_mfma_f32_16x16x32_bf16 v[224:227], v[112:115], v[144:147], v[224:227]\n\t" \
  "v_mfma_f32_16x16x32_bf16 v[232:235], v[116:119], v[144:147], v[232:235]\n\t" \
  "v_mfma_f32_16x16x32_bf16 v[240:243], v[120:123], v[144:147], v[240:243]\n\t" \
  "ds_read_b64_tr_b16 v[144:145], v54 offset:38912\n\t" \
  "v_mfma_f32_16x16x32_bf16 v[252:255], v[124:127], v[148:151], v[252:255]\n\t" \
  "ds_read_b64_tr_b16 v[146:147], v54 offset:39424\n\t" \
  "v_mfma_f32_16x16x32_bf16 v[220:223], v[108:111], v[148:151], v[220:223]\n\t" \
  "ds_read_b128 v[124:127], v53 offset:0\n\t" \
  "v_mfma_f32_16x16x32_bf16 v[228:231], v[112:115], v[148:151], v[228:231]\n\t" \
  "v_mfma_f32_16x16x32_bf16 v[236:239], v[116:119], v[148:151], v[236:239]\n\t" \
  "v_mfma_f32_16x16x32_bf16 v[244:247], v[120:123], v[148:151], v[244:247]\n\t" \
  "ds_read_b64_tr_b16 v[148:149], v55 offset:38912\n\t" \
  "v_mfma_f32_16x16x32_bf16 v[152:155], v[128:131], v[108:111], v[152:155]\n\t" \
  "ds_read_b64_tr_b16 v[150:151], v55 offset:39424\n\t" \
  "v_mfma_f32_16x16x32_bf16 v[160:163], v[128:131], v[112:115], v[160:163]\n\t" \
  "v_mfma_f32_16x16x32_bf16 v[168:171], v[128:131], v[116:119], v[168:171]\n\t" \
  "v_mfma_f32_16x16x32_bf16 v[176:179], v[128:131], v[120:123], v[176:179]\n\t" \
  "ds_read_b64_tr_b16 v[128:129], v54 offset:22528\n\t" \
  "v_mfma_f32_16x16x32_bf16 v[156:159], v[132:135], v[108:111], v[156:159]\n\t" \
  "ds_read_b128 v[108:111], v53 offset:10240\n\t" \
  "v_mfma_f32_16x16x32_bf16 v[164:167], v[132:135], v[112:115], v[164:167]\n\t" \
  "ds_read_b128 v[112:115], v53 offset:12288\n\t" \
  "v_mfma_f32_16x16x32_bf16 v[172:175], v[132:135], v[116:119], v[172:175]\n\t" \
  "ds_read_b128 v[116:119], v53 offset:14336\n\t" \
  "v_mfma_f32_16x16x32_bf16 v[180:183], v[132:135], v[120:123], v[180:183]\n\t" \
  "ds_read_b128 v[120:123], v53 offset:16384\n\t" \
  "s_waitcnt lgkmcnt(5)\n\t" \
  "v_mfma_f32_16x16x32_bf16 v[184:187], v[92:95], v[64:67], v[184:187]\n\t" \
  "ds_read_b64_tr_b16 v[130:131], v54 offset:23040\n\t" \
  "v_mfma_f32_16x16x32_bf16 v[192:195], v[92:95], v[68:71], v[192:195]\n\t" \
  "ds_read_b64_tr_b16 v[132:133], v55 offset:22528\n\t" \
  "v_mfma_f32_16x16x32_bf16 v[200:203], v[92:95], v[72:75], v[200:203]\n\t" \
  "ds_read_b64_tr_b16 v[134:135], v55 offset:23040\n\t" \
  "v_mfma_f32_16x16x32_bf16 v[208:211], v[92:95], v[76:79], v[208:211]\n\t" \
  "v_mfma_f32_16x16x32_bf16 v[188:191], v[96:99], v[64:67], v[188:191]\n\t" \
  "v_mfma_f32_16x16x32_bf16 v[196:199], v[96:99], v[68:71], v[196:199]\n\t" \
  "v_mfma_f32_16x16x32_bf16 v[204:207], v[96:99], v[72:75], v[204:207]\n\t" \
  "v_mfma_f32_16x16x32_bf16 v[212:215], v[96:99], v[76:79], v[212:215]\n\t" \
  "v_mfma_f32_16x16x32_bf16 v[248:251], v[80:83], v[100:103], v[248:251]\n\t" \
  "v_mfma_f32_16x16x32_bf16 v[216:219], v[64:67], v[100:103], v[216:219]\n\t" \
  "v_mfma_f32_16x16x32_bf16 v[224:227], v[68:71], v[100:103], v[224:227]\n\t" \
  "v_mfma_f32_16x16x32_bf16 v[232:235], v[72:75], v[100:103], v[232:235]\n\t" \
  "v_mfma_f32_16x16x32_bf16 v[240:243], v[76:79], v[100:103], v[240:243]\n\t" \
  "s_waitcnt lgkmcnt(0)\n\t" \
  "s_barrier\n\t" \
  "v_mfma_f32_16x16x32_bf16 v[252:255], v[80:83], v[104:107], v[252:255]\n\t" \
  "ds_read_b64_tr_b16 v[92:93], v58 offset:26624\n\t" \
  "v_mfma_f32_16x16x32_bf16 v[220:223], v[64:67], v[104:107], v[220:223]\n\t" \
  "ds_read_b64_tr_b16 v[94:95], v58 offset:27136\n\t" \
  "v_mfma_f32_16x16x32_bf16 v[228:231], v[68:71], v[104:107], v[228:231]\n\t" \
  "ds_read_b64_tr_b16 v[96:97], v59 offset:26624\n\t" \
  "v_mfma_f32_16x16x32_bf16 v[236:239], v[72:75], v[104:107], v[236:239]\n\t" \
  "ds_read_b64_tr_b16 v[98:99], v59 offset:27136\n\t" \
  "v_mfma_f32_16x16x32_bf16 v[244:247], v[76:79], v[104:107], v[244:247]\n\t" \
  "ds_read_b64_tr_b16 v[100:101], v58 offset:34816\n\t" \
  "v_mfma_f32_16x16x32_bf16 v[152:155], v[84:87], v[64:67], v[152:155]\n\t" \
  "ds_read_b64_tr_b16 v[102:103], v58 offset:35328\n\t" \
  "v_mfma_f32_16x16x32_bf16 v[160:163], v[84:87], v[68:71], v[160:163]\n\t" \
  "ds_read_b128 v[80:83], v56 offset:0\n\t" \
  "v_mfma_f32_16x16x32_bf16 v[168:171], v[84:87], v[72:75], v[168:171]\n\t" \
  "ds_read_b64_tr_b16 v[104:105], v59 offset:34816\n\t" \
  "v_mfma_f32_16x16x32_bf16 v[176:179], v[84:87], v[76:79], v[176:179]\n\t" \
  "ds_read_b64_tr_b16 v[106:107], v59 offset:35328\n\t" \
  "v_mfma_f32_16x16x32_bf16 v[156:159], v[88:91], v[64:67], v[156:159]\n\t" \
  "ds_read_b128 v[64:67], v56 offset:10240\n\t" \
  "v_mfma_f32_16x16x32_bf16 v[164:167], v[88:91], v[68:71], v[164:167]\n\t" \
  "ds_read_b128 v[68:71], v56 offset:12288\n\t" \
  "v_mfma_f32_16x16x32_bf16 v[172:175], v[88:91], v[72:75], v[172:175]\n\t" \
  "ds_read_b128 v[72:75], v56 offset:14336\n\t" \
  "v_mfma_f32_16x16x32_bf16 v[180:183], v[88:91], v[76:79], v[180:183]\n\t" \
  "ds_read_b128 v[76:79], v56 offset:16384\n\t" \
  "v_mfma_f32_16x16x32_bf16 v[184:187], v[136:139], v[108:111], v[184:187]\n\t" \
  "ds_read_b64_tr_b16 v[84:85], v58 offset:18432\n\t" \
  "v_mfma_f32_16x16x32_bf16 v[192:195], v[136:139], v[112:115], v[192:195]\n\t" \
  "ds_read_b64_tr_b16 v[86:87], v58 offset:18944\n\t" \
  "v_mfma_f32_16x16x32_bf16 v[200:203], v[136:139], v[116:119], v[200:203]\n\t" \
  "ds_read_b64_tr_b16 v[88:89], v59 offset:18432\n\t" \
  "v_mfma_f32_16x16x32_bf16 v[208:211], v[136:139], v[120:123], v[208:211]\n\t" \
  "ds_read_b64_tr_b16 v[90:91], v59 offset:18944\n\t" \
  "v_mfma_f32_16x16x32_bf16 v[188:191], v[140:143], v[108:111], v[188:191]\n\t" \
  "ds_read_b64_tr_b16 v[136:137], v58 offset:30720\n\t" \
  "v_mfma_f32_16x16x32_bf16 v[196:199], v[140:143], v[112:115], v[196:199]\n\t" \
  "ds_read_b64_tr_b16 v[138:139], v58 offset:31232\n\t" \
  "v_mfma_f32_16x16x32_bf16 v[204:207], v[140:143], v[116:119], v[204:207]\n\t" \
  "v_mfma_f32_16x16x32_bf16 v[212:215], v[140:143], v[120:123], v[212:215]\n\t" \
  "ds_read_b64_tr_b16 v[140:141], v59 offset:30720\n\t" \
  "v_mfma_f32_16x16x32_bf16 v[248:251], v[124:127], v[144:147], v[248:251]\n\t" \
  "ds_read_b64_tr_b16 v[142:143], v59 offset:31232\n\t" \
  "v_mfma_f32_16x16x32_bf16 v[216:219], v[108:111], v[144:147], v[216:219]\n\t" \
  "v_mfma_f32_16x16x32_bf16 v[224:227], v[112:115], v[144:147], v[224:227]\n\t" \
  "v_mfma_f32_16x16x32_bf16 v[232:235], v[116:119], v[144:147], v[232:235]\n\t" \
  "v_mfma_f32_16x16x32_bf16 v[240:243], v[120:123], v[144:147], v[240:243]\n\t" \
  "ds_read_b64_tr_b16 v[144:145], v58 offset:38912\n\t" \
  "v_mfma_f32_16x16x32_bf16 v[252:255], v[124:127], v[148:151], v[252:255]\n\t" \
  "ds_read_b64_tr_b16 v[146:147], v58 offset:39424\n\t" \
  "v_mfma_f32_16x16x32_bf16 v[220:223], v[108:111], v[148:151], v[220:223]\n\t" \
  "ds_read_b128 v[124:127], v57 offset:0\n\t" \
  "v_mfma_f32_16x16x32_bf16 v[228:231], v[112:115], v[148:151], v[228:231]\n\t" \
  "v_mfma_f32_16x16x32_bf16 v[236:239], v[116:119], v[148:151], v[236:239]\n\t" \
  "v_mfma_f32_16x16x32_bf16 v[244:247], v[120:123], v[148:151], v[244:247]\n\t" \
  "ds_read_b64_tr_b16 v[148:149], v59 offset:38912\n\t" \
  "v_mfma_f32_16x16x32_bf16 v[152:155], v[128:131], v[108:111], v[152:155]\n\t" \
  "ds_read_b64_tr_b16 v[150:151], v59 offset:39424\n\t" \
  "v_mfma_f32_16x16x32_bf16 v[160:163], v[128:131], v[112:115], v[160:163]\n\t" \
  "v_mfma_f32_16x16x32_bf16 v[168:171], v[128:131], v[116:119], v[168:171]\n\t" \
  "v_mfma_f32_16x16x32_bf16 v[176:179], v[128:131], v[120:123], v[176:179]\n\t" \
  "ds_read_b64_tr_b16 v[128:129], v58 offset:22528\n\t" \
  "v_mfma_f32_16x16x32_bf16 v[156:159], v[132:135], v[108:111], v[156:159]\n\t" \
  "ds_read_b128 v[108:111], v57 offset:10240\n\t" \
  "v_mfma_f32_16x16x32_bf16 v[164:167], v[132:135], v[112:115], v[164:167]\n\t" \
  "ds_read_b128 v[112:115], v57 offset:12288\n\t" \
  "v_mfma_f32_16x16x32_bf16 v[172:175], v[132:135], v[116:119], v[172:175]\n\t" \
  "ds_read_b128 v[116:119], v57 offset:14336\n\t" \
  "v_mfma_f32_16x16x32_bf16 v[180:183], v[132:135], v[120:123], v[180:183]\n\t" \
  "ds_read_b128 v[120:123], v57 offset:16384\n\t" \
  "s_waitcnt lgkmcnt(5)\n\t" \
  "v_mfma_f32_16x16x32_bf16 v[184:187], v[92:95], v[64:67], v[184:187]\n\t" \
  "ds_read_b64_tr_b16 v[130:131], v58 offset:23040\n\t" \
  "v_mfma_f32_16x16x32_bf16 v[192:195], v[92:95], v[68:71], v[192:195]\n\t" \
  "ds_read_b64_tr_b16 v[132:133], v59 offset:22528\n\t" \
  "v_mfma_f32_16x16x32_bf16 v[200:203], v[92:95], v[72:75], v[200:203]\n\t" \
  "ds_read_b64_tr_b16 v[134:135], v59 offset:23040\n\t" \
  "v_mfma_f32_16x16x32_bf16 v[208:211], v[92:95], v[76:79], v[208:211]\n\t" \
  "v_mfma_f32_16x16x32_bf16 v[188:191], v[96:99], v[64:67], v[188:191]\n\t" \
  "v_mfma_f32_16x16x32_bf16 v[196:199], v[96:99], v[68:71], v[196:199]\n\t" \
  "v_mfma_f32_16x16x32_bf16 v[204:207], v[96:99], v[72:75], v[204:207]\n\t" \
  "v_mfma_f32_16x16x32_bf16 v[212:215], v[96:99], v[76:79], v[212:215]\n\t" \
  "v_mfma_f32_16x16x32_bf16 v[248:251], v[80:83], v[100:103], v[248:251]\n\t" \
  "v_mfma_f32_16x16x32_bf16 v[216:219], v[64:67], v[100:103], v[216:219]\n\t" \
  "v_mfma_f32_16x16x32_bf16 v[224:227], v[68:71], v[100:103], v[224:227]\n\t" \
  "v_mfma_f32_16x16x32_bf16 v[232:235], v[72:75], v[100:103], v[232:235]\n\t" \
  "v_mfma_f32_16x16x32_bf16 v[240:243], v[76:79], v[100:103], v[240:243]\n\t" \
  "s_waitcnt lgkmcnt(0)\n\t" \
  "s_barrier\n\t" \
  "v_mfma_f32_16x16x32_bf16 v[252:255], v[80:83], v[104:107], v[252:255]\n\t" \
  "ds_read_b64_tr_b16 v[92:93], v62 offset:26624\n\t" \
  "v_mfma_f32_16x16x32_bf16 v[220:223], v[64:67], v[104:107], v[220:223]\n\t" \
  "ds_read_b64_tr_b16 v[94:95], v62 offset:27136\n\t" \
  "v_mfma_f32_16x16x32_bf16 v[228:231], v[68:71], v[104:107], v[228:231]\n\t" \
  "ds_read_b64_tr_b16 v[96:97], v63 offset:26624\n\t" \
  "v_mfma_f32_16x16x32_bf16 v[236:239], v[72:75], v[104:107], v[236:239]\n\t" \
  "ds_read_b64_tr_b16 v[98:99], v63 offset:27136\n\t" \
  "v_mfma_f32_16x16x32_bf16 v[244:247], v[76:79], v[104:107], v[244:247]\n\t" \
  "ds_read_b64_tr_b16 v[100:101], v62 offset:34816\n\t" \
  "v_mfma_f32_16x16x32_bf16 v[152:155], v[84:87], v[64:67], v[152:155]\n\t" \
  "ds_read_b64_tr_b16 v[102:103], v62 offset:35328\n\t" \
  "v_mfma_f32_16x16x32_bf16 v[160:163], v[84:87], v[68:71], v[160:163]\n\t" \
  "ds_read_b128 v[80:83], v60 offset:0\n\t" \
  "v_mfma_f32_16x16x32_bf16 v[168:171], v[84:87], v[72:75], v[168:171]\n\t" \
  "ds_read_b64_tr_b16 v[104:105], v63 offset:34816\n\t" \
  "v_mfma_f32_16x16x32_bf16 v[176:179], v[84:87], v[76:79], v[176:179]\n\t" \
  "ds_read_b64_tr_b16 v[106:107], v63 offset:35328\n\t" \
  "v_mfma_f32_16x16x32_bf16 v[156:159], v[88:91], v[64:67], v[156:159]\n\t" \
  "ds_read_b128 v[64:67], v60 offset:10240\n\t" \
  "v_mfma_f32_16x16x32_bf16 v[164:167], v[88:91], v[68:71], v[164:167]\n\t" \
  "ds_read_b128 v[68:71], v60 offset:12288\n\t" \
  "v_mfma_f32_16x16x32_bf16 v[172:175], v[88:91], v[72:75], v[172:175]\n\t" \
  "ds_read_b128 v[72:75], v60 offset:14336\n\t" \
  "v_mfma_f32_16x16x32_bf16 v[180:183], v[88:91], v[76:79], v[180:183]\n\t" \
  "ds_read_b128 v[76:79], v60 offset:16384\n\t" \
  "v_mfma_f32_16x16x32_bf16 v[184:187], v[136:139], v[108:111], v[184:187]\n\t" \
  "ds_read_b64_tr_b16 v[84:85], v62 offset:18432\n\t" \
  "v_mfma_f32_16x16x32_bf16 v[192:195], v[136:139], v[112:115], v[192:195]\n\t" \
  "ds_read_b64_tr_b16 v[86:87], v62 offset:18944\n\t" \
  "v_mfma_f32_16x16x32_bf16 v[200:203], v[136:139], v[116:119], v[200:203]\n\t" \
  "ds_read_b64_tr_b16 v[88:89], v63 offset:18432\n\t" \
  "v_mfma_f32_16x16x32_bf16 v[208:211], v[136:139], v[120:123], v[208:211]\n\t" \
  "ds_read_b64_tr_b16 v[90:91], v63 offset:18944\n\t" \
  "v_mfma_f32_16x16x32_bf16 v[188:191], v[140:143], v[108:111], v[188:191]\n\t" \
  "ds_read_b64_tr_b16 v[136:137], v62 offset:30720\n\t" \
  "v_mfma_f32_16x16x32_bf16 v[196:199], v[140:143], v[112:115], v[196:199]\n\t" \
  "ds_read_b64_tr_b16 v[138:139], v62 offset:31232\n\t" \
  "v_mfma_f32_16x16x32_bf16 v[204:207], v[140:143], v[116:119], v[204:207]\n\t" \
  "v_mfma_f32_16x16x32_bf16 v[212:215], v[140:143], v[120:123], v[212:215]\n\t" \
  "ds_read_b64_tr_b16 v[140:141], v63 offset:30720\n\t" \
  "v_mfma_f32_16x16x32_bf16 v[248:251], v[124:127], v[144:147], v[248:251]\n\t" \
  "ds_read_b64_tr_b16 v[142:143], v63 offset:31232\n\t" \
  "v_mfma_f32_16x16x32_bf16 v[216:219], v[108:111], v[144:147], v[216:219]\n\t" \
  "v_mfma_f32_16x16x32_bf16 v[224:227], v[112:115], v[144:147], v[224:227]\n\t" \
  "v_mfma_f32_16x16x32_bf16 v[232:235], v[116:119], v[144:147], v[232:235]\n\t" \
  "v_mfma_f32_16x16x32_bf16 v[240:243], v[120:123], v[144:147], v[240:243]\n\t" \
  "ds_read_b64_tr_b16 v[144:145], v62 offset:38912\n\t" \
  "v_mfma_f32_16x16x32_bf16 v[252:255], v[124:127], v[148:151], v[252:255]\n\t" \
  "ds_read_b64_tr_b16 v[146:147], v62 offset:39424\n\t" \
  "v_mfma_f32_16x16x32_bf16 v[220:223], v[108:111], v[148:151], v[220:223]\n\t" \
  "ds_read_b128 v[124:127], v61 offset:0\n\t" \
  "v_mfma_f32_16x16x32_bf16 v[228:231], v[112:115], v[148:151], v[228:231]\n\t" \
  "v_mfma_f32_16x16x32_bf16 v[236:239], v[116:119], v[148:151], v[236:239]\n\t" \
  "v_mfma_f32_16x16x32_bf16 v[244:247], v[120:123], v[148:151], v[244:247]\n\t" \
  "ds_read_b64_tr_b16 v[148:149], v63 offset:38912\n\t" \
  "v_mfma_f32_16x16x32_bf16 v[152:155], v[128:131], v[108:111], v[152:155]\n\t" \
  "ds_read_b64_tr_b16 v[150:151], v63 offset:39424\n\t" \
  "v_mfma_f32_16x16x32_bf16 v[160:163], v[128:131], v[112:115], v[160:163]\n\t" \
  "v_mfma_f32_16x16x32_bf16 v[168:171], v[128:131], v[116:119], v[168:171]\n\t" \
  "v_mfma_f32_16x16x32_bf16 v[176:179], v[128:131], v[120:123], v[176:179]\n\t" \
  "ds_read_b64_tr_b16 v[128:129], v62 offset:22528\n\t" \
  "v_mfma_f32_16x16x32_bf16 v[156:159], v[132:135], v[108:111], v[156:159]\n\t" \
  "ds_read_b128 v[108:111], v61 offset:10240\n\t" \
  "v_mfma_f32_16x16x32_bf16 v[164:167], v[132:135], v[112:115], v[164:167]\n\t" \
  "ds_read_b128 v[112:115], v61 offset:12288\n\t" \
  "v_mfma_f32_16x16x32_bf16 v[172:175], v[132:135], v[116:119], v[172:175]\n\t" \
  "ds_read_b128 v[116:119], v61 offset:14336\n\t" \
  "v_mfma_f32_16x16x32_bf16 v[180:183], v[132:135], v[120:123], v[180:183]\n\t" \
  "ds_read_b128 v[120:123], v61 offset:16384\n\t" \
  "s_waitcnt lgkmcnt(5)\n\t" \
  "v_mfma_f32_16x16x32_bf16 v[184:187], v[92:95], v[64:67], v[184:187]\n\t" \
  "ds_read_b64_tr_b16 v[130:131], v62 offset:23040\n\t" \
  "v_mfma_f32_16x16x32_bf16 v[192:195], v[92:95], v[68:71], v[192:195]\n\t" \
  "ds_read_b64_tr_b16 v[132:133], v63 offset:22528\n\t" \
  "v_mfma_f32_16x16x32_bf16 v[200:203], v[92:95], v[72:75], v[200:203]\n\t" \
  "ds_read_b64_tr_b16 v[134:135], v63 offset:23040\n\t" \
  "v_mfma_f32_16x16x32_bf16 v[208:211], v[92:95], v[76:79], v[208:211]\n\t" \
  "v_mfma_f32_16x16x32_bf16 v[188:191], v[96:99], v[64:67], v[188:191]\n\t" \
  "v_mfma_f32_16x16x32_bf16 v[196:199], v[96:99], v[68:71], v[196:199]\n\t" \
  "v_mfma_f32_16x16x32_bf16 v[204:207], v[96:99], v[72:75], v[204:207]\n\t" \
  "v_mfma_f32_16x16x32_bf16 v[212:215], v[96:99], v[76:79], v[212:215]\n\t" \
  "v_mfma_f32_16x16x32_bf16 v[248:251], v[80:83], v[100:103], v[248:251]\n\t" \
  "v_mfma_f32_16x16x32_bf16 v[216:219], v[64:67], v[100:103], v[216:219]\n\t" \
  "v_mfma_f32_16x16x32_bf16 v[224:227], v[68:71], v[100:103], v[224:227]\n\t" \
  "v_mfma_f32_16x16x32_bf16 v[232:235], v[72:75], v[100:103], v[232:235]\n\t" \
  "v_mfma_f32_16x16x32_bf16 v[240:243], v[76:79], v[100:103], v[240:243]\n\t" \
  "s_waitcnt lgkmcnt(0)\n\t" \
  "s_barrier\n\t" \
  "v_mfma_f32_16x16x32_bf16 v[252:255], v[80:83], v[104:107], v[252:255]\n\t" \
  "ds_read_b64_tr_b16 v[92:93], v54 offset:26624\n\t" \
  "v_mfma_f32_16x16x32_bf16 v[220:223], v[64:67], v[104:107], v[220:223]\n\t" \
  "ds_read_b64_tr_b16 v[94:95], v54 offset:27136\n\t" \
  "v_mfma_f32_16x16x32_bf16 v[228:231], v[68:71], v[104:107], v[228:231]\n\t" \
  "ds_read_b64_tr_b16 v[96:97], v55 offset:26624\n\t" \
  "v_mfma_f32_16x16x32_bf16 v[236:239], v[72:75], v[104:107], v[236:239]\n\t" \
  "ds_read_b64_tr_b16 v[98:99], v55 offset:27136\n\t" \
  "v_mfma_f32_16x16x32_bf16 v[244:247], v[76:79], v[104:107], v[244:247]\n\t" \
  "ds_read_b64_tr_b16 v[100:101], v54 offset:34816\n\t" \
  "v_mfma_f32_16x16x32_bf16 v[152:155], v[84:87], v[64:67], v[152:155]\n\t" \
  "ds_read_b64_tr_b16 v[102:103], v54 offset:35328\n\t" \
  "v_mfma_f32_16x16x32_bf16 v[160:163], v[84:87], v[68:71], v[160:163]\n\t" \
  "ds_read_b128 v[80:83], v52 offset:0\n\t" \
  "v_mfma_f32_16x16x32_bf16 v[168:171], v[84:87], v[72:75], v[168:171]\n\t" \
  "ds_read_b64_tr_b16 v[104:105], v55 offset:34816\n\t" \
  "v_mfma_f32_16x16x32_bf16 v[176:179], v[84:87], v[76:79], v[176:179]\n\t" \
  "ds_read_b64_tr_b16 v[106:107], v55 offset:35328\n\t" \
  "v_mfma_f32_16x16x32_bf16 v[156:159], v[88:91], v[64:67], v[156:159]\n\t" \
  "ds_read_b128 v[64:67], v52 offset:10240\n\t" \
  "v_mfma_f32_16x16x32_bf16 v[164:167], v[88:91], v[68:71], v[164:167]\n\t" \
  "ds_read_b128 v[68:71], v52 offset:12288\n\t" \
  "v_mfma_f32_16x16x32_bf16 v[172:175], v[88:91], v[72:75], v[172:175]\n\t" \
  "ds_read_b128 v[72:75], v52 offset:14336\n\t" \
  "v_mfma_f32_16x16x32_bf16 v[180:183], v[88:91], v[76:79], v[180:183]\n\t" \
  "ds_read_b128 v[76:79], v52 offset:16384\n\t" \
  "v_mfma_f32_16x16x32_bf16 v[184:187], v[136:139], v[108:111], v[184:187]\n\t" \
  "ds_read_b64_tr_b16 v[84:85], v54 offset:18432\n\t" \
  "v_mfma_f32_16x16x32_bf16 v[192:195], v[136:139], v[112:115], v[192:195]\n\t" \
  "ds_read_b64_tr_b16 v[86:87], v54 offset:18944\n\t" \
  "v_mfma_f32_16x16x32_bf16 v[200:203], v[136:139], v[116:119], v[200:203]\n\t" \
  "ds_read_b64_tr_b16 v[88:89], v55 offset:18432\n\t" \
  "v_mfma_f32_16x16x32_bf16 v[208:211], v[136:139], v[120:123], v[208:211]\n\t" \
  "ds_read_b64_tr_b16 v[90:91], v55 offset:18944\n\t" \
  "v_mfma_f32_16x16x32_bf16 v[188:191], v[140:143], v[108:111], v[188:191]\n\t" \
  "ds_read_b64_tr_b16 v[136:137], v54 offset:30720\n\t" \
  "v_mfma_f32_16x16x32_bf16 v[196:199], v[140:143], v[112:115], v[196:199]\n\t" \
  "ds_read_b64_tr_b16 v[138:139], v54 offset:31232\n\t" \
  "v_mfma_f32_16x16x32_bf16 v[204:207], v[140:143], v[116:119], v[204:207]\n\t" \
  "v_mfma_f32_16x16x32_bf16 v[212:215], v[140:143], v[120:123], v[212:215]\n\t" \
  "ds_read_b64_tr_b16 v[140:141], v55 offset:30720\n\t" \
  "v_mfma_f32_16x16x32_bf16 v[248:251], v[124:127], v[144:147], v[248:251]\n\t" \
  "ds_read_b64_tr_b16 v[142:143], v55 offset:31232\n\t" \
  "v_mfma_f32_16x16x32_bf16 v[216:219], v[108:111], v[144:147], v[216:219]\n\t" \
  "v_mfma_f32_16x16x32_bf16 v[224:227], v[112:115], v[144:147], v[224:227]\n\t" \
  "v_mfma_f32_16x16x32_bf16 v[232:235], v[116:119], v[144:147], v[232:235]\n\t" \
  "v_mfma_f32_16x16x32_bf16 v[240:243], v[120:123], v[144:147], v[240:243]\n\t" \
  "ds_read_b64_tr_b16 v[144:145], v54 offset:38912\n\t" \
  "v_mfma_f32_16x16x32_bf16 v[252:255], v[124:127], v[148:151], v[252:255]\n\t" \
  "ds_read_b64_tr_b16 v[146:147], v54 offset:39424\n\t" \
  "v_mfma_f32_16x16x32_bf16 v[220:223], v[108:111], v[148:151], v[220:223]\n\t" \
  "ds_read_b128 v[124:127], v53 offset:0\n\t" \
  "v_mfma_f32_16x16x32_bf16 v[228:231], v[112:115], v[148:151], v[228:231]\n\t" \
  "v_mfma_f32_16x16x32_bf16 v[236:239], v[116:119], v[148:151], v[236:239]\n\t" \
  "v_mfma_f32_16x16x32_bf16 v[244:247], v[120:123], v[148:151], v[244:247]\n\t" \
  "ds_read_b64_tr_b16 v[148:149], v55 offset:38912\n\t" \
  "v_mfma_f32_16x16x32_bf16 v[152:155], v[128:131], v[108:111], v[152:155]\n\t" \
  "ds_read_b64_tr_b16 v[150:151], v55 offset:39424\n\t" \
  "v_mfma_f32_16x16x32_bf16 v[160:163], v[128:131], v[112:115], v[160:163]\n\t" \
  "v_mfma_f32_16x16x32_bf16 v[168:171], v[128:131], v[116:119], v[168:171]\n\t" \
  "v_mfma_f32_16x16x32_bf16 v[176:179], v[128:131], v[120:123], v[176:179]\n\t" \
  "ds_read_b64_tr_b16 v[128:129], v54 offset:22528\n\t" \
  "v_mfma_f32_16x16x32_bf16 v[156:159], v[132:135], v[108:111], v[156:159]\n\t" \
  "ds_read_b128 v[108:111], v53 offset:10240\n\t" \
  "v_mfma_f32_16x16x32_bf16 v[164:167], v[132:135], v[112:115], v[164:167]\n\t" \
  "ds_read_b128 v[112:115], v53 offset:12288\n\t" \
  "v_mfma_f32_16x16x32_bf16 v[172:175], v[132:135], v[116:119], v[172:175]\n\t" \
  "ds_read_b128 v[116:119], v53 offset:14336\n\t" \
  "v_mfma_f32_16x16x32_bf16 v[180:183], v[132:135], v[120:123], v[180:183]\n\t" \
  "ds_read_b128 v[120:123], v53 offset:16384\n\t" \
  "s_waitcnt lgkmcnt(5)\n\t" \
  "v_mfma_f32_16x16x32_bf16 v[184:187], v[92:95], v[64:67], v[184:187]\n\t" \
  "ds_read_b64_tr_b16 v[130:131], v54 offset:23040\n\t" \
  "v_mfma_f32_16x16x32_bf16 v[192:195], v[92:95], v[68:71], v[192:195]\n\t" \
  "ds_read_b64_tr_b16 v[132:133], v55 offset:22528\n\t" \
  "v_mfma_f32_16x16x32_bf16 v[200:203], v[92:95], v[72:75], v[200:203]\n\t" \
  "ds_read_b64_tr_b16 v[134:135], v55 offset:23040\n\t" \
  "v_mfma_f32_16x16x32_bf16 v[208:211], v[92:95], v[76:79], v[208:211]\n\t" \
  "v_mfma_f32_16x16x32_bf16 v[188:191], v[96:99], v[64:67], v[188:191]\n\t" \
  "v_mfma_f32_16x16x32_bf16 v[196:199], v[96:99], v[68:71], v[196:199]\n\t" \
  "v_mfma_f32_16x16x32_bf16 v[204:207], v[96:99], v[72:75], v[204:207]\n\t" \
  "v_mfma_f32_16x16x32_bf16 v[212:215], v[96:99], v[76:79], v[212:215]\n\t" \
  "v_mfma_f32_16x16x32_bf16 v[248:251], v[80:83], v[100:103], v[248:251]\n\t" \
  "v_mfma_f32_16x16x32_bf16 v[216:219], v[64:67], v[100:103], v[216:219]\n\t" \
  "v_mfma_f32_16x16x32_bf16 v[224:227], v[68:71], v[100:103], v[224:227]\n\t" \
  "v_mfma_f32_16x16x32_bf16 v[232:235], v[72:75], v[100:103], v[232:235]\n\t" \
  "v_mfma_f32_16x16x32_bf16 v[240:243], v[76:79], v[100:103], v[240:243]\n\t" \
  "s_waitcnt lgkmcnt(0)\n\t" \
  "s_barrier\n\t" \
  "v_mfma_f32_16x16x32_bf16 v[252:255], v[80:83], v[104:107], v[252:255]\n\t" \
  "ds_read_b64_tr_b16 v[92:93], v58 offset:26624\n\t" \
  "v_mfma_f32_16x16x32_bf16 v[220:223], v[64:67], v[104:107], v[220:223]\n\t" \
  "ds_read_b64_tr_b16 v[94:95], v58 offset:27136\n\t" \
  "v_mfma_f32_16x16x32_bf16 v[228:231], v[68:71], v[104:107], v[228:231]\n\t" \
  "ds_read_b64_tr_b16 v[96:97], v59 offset:26624\n\t" \
  "v_mfma_f32_16x16x32_bf16 v[236:239], v[72:75], v[104:107], v[236:239]\n\t" \
  "ds_read_b64_tr_b16 v[98:99], v59 offset:27136\n\t" \
  "v_mfma_f32_16x16x32_bf16 v[244:247], v[76:79], v[104:107], v[244:247]\n\t" \
  "ds_read_b64_tr_b16 v[100:101], v58 offset:34816\n\t" \
  "v_mfma_f32_16x16x32_bf16 v[152:155], v[84:87], v[64:67], v[152:155]\n\t" \
  "ds_read_b64_tr_b16 v[102:103], v58 offset:35328\n\t" \
  "v_mfma_f32_16x16x32_bf16 v[160:163], v[84:87], v[68:71], v[160:163]\n\t" \
  "ds_read_b128 v[80:83], v56 offset:0\n\t" \
  "v_mfma_f32_16x16x32_bf16 v[168:171], v[84:87], v[72:75], v[168:171]\n\t" \
  "ds_read_b64_tr_b16 v[104:105], v59 offset:34816\n\t" \
  "v_mfma_f32_16x16x32_bf16 v[176:179], v[84:87], v[76:79], v[176:179]\n\t" \
  "ds_read_b64_tr_b16 v[106:107], v59 offset:35328\n\t" \
  "v_mfma_f32_16x16x32_bf16 v[156:159], v[88:91], v[64:67], v[156:159]\n\t" \
  "ds_read_b128 v[64:67], v56 offset:10240\n\t" \
  "v_mfma_f32_16x16x32_bf16 v[164:167], v[88:91], v[68:71], v[164:167]\n\t" \
  "ds_read_b128 v[68:71], v56 offset:12288\n\t" \
  "v_mfma_f32_16x16x32_bf16 v[172:175], v[88:91], v[72:75], v[172:175]\n\t" \
  "ds_read_b128 v[72:75], v56 offset:14336\n\t" \
  "v_mfma_f32_16x16x32_bf16 v[180:183], v[88:91], v[76:79], v[180:183]\n\t" \
  "ds_read_b128 v[76:79], v56 offset:16384\n\t" \
  "v_mfma_f32_16x16x32_bf16 v[184:187], v[136:139], v[108:111], v[184:187]\n\t" \
  "ds_read_b64_tr_b16 v[84:85], v58 offset:18432\n\t" \
  "v_mfma_f32_16x16x32_bf16 v[192:195], v[136:139], v[112:115], v[192:195]\n\t" \
  "ds_read_b64_tr_b16 v[86:87], v58 offset:18944\n\t" \
  "v_mfma_f32_16x16x32_bf16 v[200:203], v[136:139], v[116:119], v[200:203]\n\t" \
  "ds_read_b64_tr_b16 v[88:89], v59 offset:18432\n\t" \
  "v_mfma_f32_16x16x32_bf16 v[208:211], v[136:139], v[120:123], v[208:211]\n\t" \
  "ds_read_b64_tr_b16 v[90:91], v59 offset:18944\n\t" \
  "v_mfma_f32_16x16x32_bf16 v[188:191], v[140:143], v[108:111], v[188:191]\n\t" \
  "ds_read_b64_tr_b16 v[136:137], v58 offset:30720\n\t" \
  "v_mfma_f32_16x16x32_bf16 v[196:199], v[140:143], v[112:115], v[196:199]\n\t" \
  "ds_read_b64_tr_b16 v[138:139], v58 offset:31232\n\t" \
  "v_mfma_f32_16x16x32_bf16 v[204:207], v[140:143], v[116:119], v[204:207]\n\t" \
  "v_mfma_f32_16x16x32_bf16 v[212:215], v[140:143], v[120:123], v[212:215]\n\t" \
  "ds_read_b64_tr_b16 v[140:141], v59 offset:30720\n\t" \
  "v_mfma_f32_16x16x32_bf16 v[248:251], v[124:127], v[144:147], v[248:251]\n\t" \
  "ds_read_b64_tr_b16 v[142:143], v59 offset:31232\n\t" \
  "v_mfma_f32_16x16x32_bf16 v[216:219], v[108:111], v[144:147], v[216:219]\n\t" \
  "v_mfma_f32_16x16x32_bf16 v[224:227], v[112:115], v[144:147], v[224:227]\n\t" \
  "v_mfma_f32_16x16x32_bf16 v[232:235], v[116:119], v[144:147], v[232:235]\n\t" \
  "v_mfma_f32_16x16x32_bf16 v[240:243], v[120:123], v[144:147], v[240:243]\n\t" \
  "ds_read_b64_tr_b16 v[144:145], v58 offset:38912\n\t" \
  "v_mfma_f32_16x16x32_bf16 v[252:255], v[124:127], v[148:151], v[252:255]\n\t" \
  "ds_read_b64_tr_b16 v[146:147], v58 offset:39424\n\t" \
  "v_mfma_f32_16x16x32_bf16 v[220:223], v[108:111], v[148:151], v[220:223]\n\t" \
  "ds_read_b128 v[124:127], v57 offset:0\n\t" \
  "v_mfma_f32_16x16x32_bf16 v[228:231], v[112:115], v[148:151], v[228:231]\n\t" \
  "v_mfma_f32_16x16x32_bf16 v[236:239], v[116:119], v[148:151], v[236:239]\n\t" \
  "v_mfma_f32_16x16x32_bf16 v[244:247], v[120:123], v[148:151], v[244:247]\n\t" \
  "ds_read_b64_tr_b16 v[148:149], v59 offset:38912\n\t" \
  "v_mfma_f32_16x16x32_bf16 v[152:155], v[128:131], v[108:111], v[152:155]\n\t" \
  "ds_read_b64_tr_b16 v[150:151], v59 offset:39424\n\t" \
  "v_mfma_f32_16x16x32_bf16 v[160:163], v[128:131], v[112:115], v[160:163]\n\t" \
  "v_mfma_f32_16x16x32_bf16 v[168:171], v[128:131], v[116:119], v[168:171]\n\t" \
  "v_mfma_f32_16x16x32_bf16 v[176:179], v[128:131], v[120:123], v[176:179]\n\t" \
  "ds_read_b64_tr_b16 v[128:129], v58 offset:22528\n\t" \
  "v_mfma_f32_16x16x32_bf16 v[156:159], v[132:135], v[108:111], v[156:159]\n\t" \
  "ds_read_b128 v[108:111], v57 offset:10240\n\t" \
  "v_mfma_f32_16x16x32_bf16 v[164:167], v[132:135], v[112:115], v[164:167]\n\t" \
  "ds_read_b128 v[112:115], v57 offset:12288\n\t" \
  "v_mfma_f32_16x16x32_bf16 v[172:175], v[132:135], v[116:119], v[172:175]\n\t" \
  "ds_read_b128 v[116:119], v57 offset:14336\n\t" \
  "v_mfma_f32_16x16x32_bf16 v[180:183], v[132:135], v[120:123], v[180:183]\n\t" \
  "ds_read_b128 v[120:123], v57 offset:16384\n\t" \
  "s_waitcnt lgkmcnt(5)\n\t" \
  "v_mfma_f32_16x16x32_bf16 v[184:187], v[92:95], v[64:67], v[184:187]\n\t" \
  "ds_read_b64_tr_b16 v[130:131], v58 offset:23040\n\t" \
  "v_mfma_f32_16x16x32_bf16 v[192:195], v[92:95], v[68:71], v[192:195]\n\t" \
  "v_and_b32 v14, -16, v32\n\t" \
  "ds_read_b64_tr_b16 v[132:133], v59 offset:22528\n\t" \
  "v_mfma_f32_16x16x32_bf16 v[200:203], v[92:95], v[72:75], v[200:203]\n\t" \
  "ds_read_b64_tr_b16 v[134:135], v59 offset:23040\n\t" \
  "v_mfma_f32_16x16x32_bf16 v[208:211], v[92:95], v[76:79], v[208:211]\n\t" \
  "ds_read_b128 v[10:13], v14 offset:128\n\t" \
  "v_mfma_f32_16x16x32_bf16 v[188:191], v[96:99], v[64:67], v[188:191]\n\t" \
  "v_mfma_f32_16x16x32_bf16 v[196:199], v[96:99], v[68:71], v[196:199]\n\t" \
  "v_mfma_f32_16x16x32_bf16 v[204:207], v[96:99], v[72:75], v[204:207]\n\t" \
  "v_mfma_f32_16x16x32_bf16 v[212:215], v[96:99], v[76:79], v[212:215]\n\t" \
  "v_mfma_f32_16x16x32_bf16 v[248:251], v[80:83], v[100:103], v[248:251]\n\t" \
  "v_mfma_f32_16x16x32_bf16 v[216:219], v[64:67], v[100:103], v[216:219]\n\t" \
  "s_waitcnt lgkmcnt(0)\n\t" \
  "v_and_b32 v10, v10, v11\n\t" \
  "v_and_b32 v12, v12, v13\n\t" \
  "v_mfma_f32_16x16x32_bf16 v[224:227], v[68:71], v[100:103], v[224:227]\n\t" \
  "v_and_b32 v10, v10, v12\n\t" \
  "v_mfma_f32_16x16x32_bf16 v[232:235], v[72:75], v[100:103], v[232:235]\n\t" \
  "v_readfirstlane_b32 vcc_lo, v10\n\t" \
  "v_mfma_f32_16x16x32_bf16 v[240:243], v[76:79], v[100:103], v[240:243]\n\t" \
  "ds_write_b32 v32, v0 offset:144\n\t" \
  "s_cmp_eq_u32 vcc_lo, 0\n\t" \
  "s_cbranch_scc1 15f\n\t" \
  "14:\n\t" \
  "v_mfma_f32_16x16x32_bf16 v[252:255], v[80:83], v[104:107], v[252:255]\n\t" \
  "ds_read_b64_tr_b16 v[92:93], v62 offset:26624\n\t" \
  "v_mfma_f32_16x16x32_bf16 v[220:223], v[64:67], v[104:107], v[220:223]\n\t" \
  "ds_read_b64_tr_b16 v[94:95], v62 offset:27136\n\t" \
  "v_mfma_f32_16x16x32_bf16 v[228:231], v[68:71], v[104:107], v[228:231]\n\t" \
  "ds_read_b64_tr_b16 v[96:97], v63 offset:26624\n\t" \
  "v_mfma_f32_16x16x32_bf16 v[236:239], v[72:75], v[104:107], v[236:239]\n\t" \
  "ds_read_b64_tr_b16 v[98:99], v63 offset:27136\n\t" \
  "v_mfma_f32_16x16x32_bf16 v[244:247], v[76:79], v[104:107], v[244:247]\n\t" \
  "ds_read_b64_tr_b16 v[100:101], v62 offset:34816\n\t" \
  "v_mfma_f32_16x16x32_bf16 v[152:155], v[84:87], v[64:67], v[152:155]\n\t" \
  "ds_read_b64_tr_b16 v[102:103], v62 offset:35328\n\t" \
  "v_mfma_f32_16x16x32_bf16 v[160:163], v[84:87], v[68:71], v[160:163]\n\t" \
  "ds_read_b128 v[80:83], v60 offset:0\n\t" \
  "v_mfma_f32_16x16x32_bf16 v[168:171], v[84:87], v[72:75], v[168:171]\n\t" \
  "ds_read_b64_tr_b16 v[104:105], v63 offset:34816\n\t" \
  "v_mfma_f32_16x16x32_bf16 v[176:179], v[84:87], v[76:79], v[176:179]\n\t" \
  "ds_read_b64_tr_b16 v[106:107], v63 offset:35328\n\t" \
  "v_mfma_f32_16x16x32_bf16 v[156:159], v[88:91], v[64:67], v[156:159]\n\t" \
  "ds_read_b128 v[64:67], v60 offset:10240\n\t" \
  "v_mfma_f32_16x16x32_bf16 v[164:167], v[88:91], v[68:71], v[164:167]\n\t" \
  "ds_read_b128 v[68:71], v60 offset:12288\n\t" \
  "v_mfma_f32_16x16x32_bf16 v[172:175], v[88:91], v[72:75], v[172:175]\n\t" \
  "ds_read_b128 v[72:75], v60 offset:14336\n\t" \
  "v_mfma_f32_16x16x32_bf16 v[180:183], v[88:91], v[76:79], v[180:183]\n\t" \
  "ds_read_b128 v[76:79], v60 offset:16384\n\t" \
  "v_mfma_f32_16x16x32_bf16 v[184:187], v[136:139], v[108:111], v[184:187]\n\t" \
  "ds_read_b64_tr_b16 v[84:85], v62 offset:18432\n\t" \
  "v_mfma_f32_16x16x32_bf16 v[192:195], v[136:139], v[112:115], v[192:195]\n\t" \
  "ds_read_b64_tr_b16 v[86:87], v62 offset:18944\n\t" \
  "v_mfma_f32_16x16x32_bf16 v[200:203], v[136:139], v[116:119], v[200:203]\n\t" \
  "ds_read_b64_tr_b16 v[88:89], v63 offset:18432\n\t" \
  "v_mfma_f32_16x16x32_bf16 v[208:211], v[136:139], v[120:123], v[208:211]\n\t" \
  "ds_read_b64_tr_b16 v[90:91], v63 offset:18944\n\t" \
  "v_mfma_f32_16x16x32_bf16 v[188:191], v[140:143], v[108:111], v[188:191]\n\t" \
  "ds_read_b64_tr_b16 v[136:137], v62 offset:30720\n\t" \
  "v_mfma_f32_16x16x32_bf16 v[196:199], v[140:143], v[112:115], v[196:199]\n\t" \
  "ds_read_b64_tr_b16 v[138:139], v62 offset:31232\n\t" \
  "v_mfma_f32_16x16x32_bf16 v[204:207], v[140:143], v[116:119], v[204:207]\n\t" \
  "v_mfma_f32_16x16x32_bf16 v[212:215], v[140:143], v[120:123], v[212:215]\n\t" \
  "ds_read_b64_tr_b16 v[140:141], v63 offset:30720\n\t" \
  "v_mfma_f32_16x16x32_bf16 v[248:251], v[124:127], v[144:147], v[248:251]\n\t" \
  "ds_read_b64_tr_b16 v[142:143], v63 offset:31232\n\t" \
  "v_mfma_f32_16x16x32_bf16 v[216:219], v[108:111], v[144:147], v[216:219]\n\t" \
  "v_mfma_f32_16x16x32_bf16 v[224:227], v[112:115], v[144:147], v[224:227]\n\t" \
  "v_mfma_f32_16x16x32_bf16 v[232:235], v[116:119], v[144:147], v[232:235]\n\t" \
  "v_mfma_f32_16x16x32_bf16 v[240:243], v[120:123], v[144:147], v[240:243]\n\t" \
  "ds_read_b64_tr_b16 v[144:145], v62 offset:38912\n\t" \
  "v_mfma_f32_16x16x32_bf16 v[252:255], v[124:127], v[148:151], v[252:255]\n\t" \
  "ds_read_b64_tr_b16 v[146:147], v62 offset:39424\n\t" \
  "v_mfma_f32_16x16x32_bf16 v[220:223], v[108:111], v[148:151], v[220:223]\n\t" \
  "ds_read_b128 v[124:127], v61 offset:0\n\t" \
  "v_mfma_f32_16x16x32_bf16 v[228:231], v[112:115], v[148:151], v[228:231]\n\t" \
  "v_mfma_f32_16x16x32_bf16 v[236:239], v[116:119], v[148:151], v[236:239]\n\t" \
  "v_mfma_f32_16x16x32_bf16 v[244:247], v[120:123], v[148:151], v[244:247]\n\t" \
  "ds_read_b64_tr_b16 v[148:149], v63 offset:38912\n\t" \
  "v_mfma_f32_16x16x32_bf16 v[152:155], v[128:131], v[108:111], v[152:155]\n\t" \
  "ds_read_b64_tr_b16 v[150:151], v63 offset:39424\n\t" \
  "v_mfma_f32_16x16x32_bf16 v[160:163], v[128:131], v[112:115], v[160:163]\n\t" \
  "v_mfma_f32_16x16x32_bf16 v[168:171], v[128:131], v[116:119], v[168:171]\n\t" \
  "v_mfma_f32_16x16x32_bf16 v[176:179], v[128:131], v[120:123], v[176:179]\n\t" \
  "ds_read_b64_tr_b16 v[128:129], v62 offset:22528\n\t" \
  "v_mfma_f32_16x16x32_bf16 v[156:159], v[132:135], v[108:111], v[156:159]\n\t" \
  "ds_read_b128 v[108:111], v61 offset:10240\n\t" \
  "v_mfma_f32_16x16x32_bf16 v[164:167], v[132:135], v[112:115], v[164:167]\n\t" \
  "ds_read_b128 v[112:115], v61 offset:12288\n\t" \
  "v_mfma_f32_16x16x32_bf16 v[172:175], v[132:135], v[116:119], v[172:175]\n\t" \
  "ds_read_b128 v[116:119], v61 offset:14336\n\t" \
  "v_mfma_f32_16x16x32_bf16 v[180:183], v[132:135], v[120:123], v[180:183]\n\t" \
  "ds_read_b128 v[120:123], v61 offset:16384\n\t" \
  "s_waitcnt lgkmcnt(5)\n\t" \
  "v_mfma_f32_16x16x32_bf16 v[184:187], v[92:95], v[64:67], v[184:187]\n\t" \
  "ds_read_b64_tr_b16 v[130:131], v62 offset:23040\n\t" \
  "v_mfma_f32_16x16x32_bf16 v[192:195], v[92:95], v[68:71], v[192:195]\n\t" \
  "ds_read_b64_tr_b16 v[132:133], v63 offset:22528\n\t" \
  "v_mfma_f32_16x16x32_bf16 v[200:203], v[92:95], v[72:75], v[200:203]\n\t" \
  "ds_read_b64_tr_b16 v[134:135], v63 offset:23040\n\t" \
  "v_mfma_f32_16x16x32_bf16 v[208:211], v[92:95], v[76:79], v[208:211]\n\t" \
  "v_mfma_f32_16x16x32_bf16 v[188:191], v[96:99], v[64:67], v[188:191]\n\t" \
  "v_mfma_f32_16x16x32_bf16 v[196:199], v[96:99], v[68:71], v[196:199]\n\t" \
  "v_mfma_f32_16x16x32_bf16 v[204:207], v[96:99], v[72:75], v[204:207]\n\t" \
  "v_mfma_f32_16x16x32_bf16 v[212:215], v[96:99], v[76:79], v[212:215]\n\t" \
  "v_mfma_f32_16x16x32_bf16 v[248:251], v[80:83], v[100:103], v[248:251]\n\t" \
  "v_mfma_f32_16x16x32_bf16 v[216:219], v[64:67], v[100:103], v[216:219]\n\t" \
  "v_mfma_f32_16x16x32_bf16 v[224:227], v[68:71], v[100:103], v[224:227]\n\t" \
  "v_mfma_f32_16x16x32_bf16 v[232:235], v[72:75], v[100:103], v[232:235]\n\t" \
  "502:\n\t" \
  "s_cbranch_execz 506f\n\t" \
  "v_mfma_f32_16x16x32_bf16 v[240:243], v[76:79], v[100:103], v[240:243]\n\t" \
  "s_waitcnt lgkmcnt(0)\n\t" \
  "ds_write_b32 v32, v0\n\t" \
  "v_mfma_f32_16x16x32_bf16 v[252:255], v[80:83], v[104:107], v[252:255]\n\t" \
  "v_mfma_f32_16x16x32_bf16 v[220:223], v[64:67], v[104:107], v[220:223]\n\t" \
  "v_mfma_f32_16x16x32_bf16 v[228:231], v[68:71], v[104:107], v[228:231]\n\t" \
  "v_mfma_f32_16x16x32_bf16 v[236:239], v[72:75], v[104:107], v[236:239]\n\t" \
  "v_mfma_f32_16x16x32_bf16 v[244:247], v[76:79], v[104:107], v[244:247]\n\t" \
  "v_mfma_f32_16x16x32_bf16 v[152:155], v[84:87], v[64:67], v[152:155]\n\t" \
  "v_mfma_f32_16x16x32_bf16 v[160:163], v[84:87], v[68:71], v[160:163]\n\t" \
  "v_mfma_f32_16x16x32_bf16 v[168:171], v[84:87], v[72:75], v[168:171]\n\t" \
  "v_mfma_f32_16x16x32_bf16 v[176:179], v[84:87], v[76:79], v[176:179]\n\t" \
  "v_mfma_f32_16x16x32_bf16 v[156:159], v[88:91], v[64:67], v[156:159]\n\t" \
  "v_mfma_f32_16x16x32_bf16 v[164:167], v[88:91], v[68:71], v[164:167]\n\t" \
  "v_mfma_f32_16x16x32_bf16 v[172:175], v[88:91], v[72:75], v[172:175]\n\t" \
  "v_mfma_f32_16x16x32_bf16 v[180:183], v[88:91], v[76:79], v[180:183]\n\t" \
  "v_mfma_f32_16x16x32_bf16 v[184:187], v[136:139], v[108:111], v[184:187]\n\t" \
  "v_and_b32 v56, -16, v32\n\t" \
  "v_mfma_f32_16x16x32_bf16 v[192:195], v[136:139], v[112:115], v[192:195]\n\t" \
  "v_mfma_f32_16x16x32_bf16 v[200:203], v[136:139], v[116:119], v[200:203]\n\t" \
  "v_mfma_f32_16x16x32_bf16 v[208:211], v[136:139], v[120:123], v[208:211]\n\t" \
  "v_mfma_f32_16x16x32_bf16 v[188:191], v[140:143], v[108:111], v[188:191]\n\t" \
  "v_mfma_f32_16x16x32_bf16 v[196:199], v[140:143], v[112:115], v[196:199]\n\t" \
  "v_mfma_f32_16x16x32_bf16 v[204:207], v[140:143], v[116:119], v[204:207]\n\t" \
  "v_mfma_f32_16x16x32_bf16 v[212:215], v[140:143], v[120:123], v[212:215]\n\t" \
  "v_cvt_pk_bf16_f32 v14, v184, v185\n\t" \
  "v_cvt_pk_bf16_f32 v15, v186, v187\n\t" \
  "v_mfma_f32_16x16x32_bf16 v[152:155], v[128:131], v[108:111], v[152:155]\n\t" \
  "v_cvt_pk_bf16_f32 v18, v192, v193\n\t" \
  "v_cvt_pk_bf16_f32 v19, v194, v195\n\t" \
  "v_mfma_f32_16x16x32_bf16 v[160:163], v[128:131], v[112:115], v[160:163]\n\t" \
  "v_cvt_pk_bf16_f32 v16, v188, v189\n\t" \
  "v_cvt_pk_bf16_f32 v17, v190, v191\n\t" \
  "v_mfma_f32_16x16x32_bf16 v[168:171], v[128:131], v[116:119], v[168:171]\n\t" \
  "v_cvt_pk_bf16_f32 v20, v196, v197\n\t" \
  "v_cvt_pk_bf16_f32 v21, v198, v199\n\t" \
  "v_mfma_f32_16x16x32_bf16 v[176:179], v[128:131], v[120:123], v[176:179]\n\t" \
  "v_cvt_pk_bf16_f32 v22, v200, v201\n\t" \
  "v_cvt_pk_bf16_f32 v23, v202, v203\n\t" \
  "v_mfma_f32_16x16x32_bf16 v[156:159], v[132:135], v[108:111], v[156:159]\n\t" \
  "v_cvt_pk_bf16_f32 v24, v204, v205\n\t" \
  "v_cvt_pk_bf16_f32 v25, v206, v207\n\t" \
  "v_mfma_f32_16x16x32_bf16 v[164:167], v[132:135], v[112:115], v[164:167]\n\t" \
  "v_cvt_pk_bf16_f32 v26, v208, v209\n\t" \
  "v_cvt_pk_bf16_f32 v27, v210, v211\n\t" \
  "v_mfma_f32_16x16x32_bf16 v[172:175], v[132:135], v[116:119], v[172:175]\n\t" \
  "v_cvt_pk_bf16_f32 v28, v212, v213\n\t" \
  "v_cvt_pk_bf16_f32 v29, v214, v215\n\t" \
  "v_mfma_f32_16x16x32_bf16 v[180:183], v[132:135], v[120:123], v[180:183]\n\t" \
  "v_cvt_pk_bf16_f32 v84, v152, v153\n\t" \
  "v_cvt_pk_bf16_f32 v85, v154, v155\n\t" \
  "v_mfma_f32_16x16x32_bf16 v[248:251], v[124:127], v[144:147], v[248:251]\n\t" \
  "v_cvt_pk_bf16_f32 v86, v156, v157\n\t" \
  "v_cvt_pk_bf16_f32 v87, v158, v159\n\t" \
  "v_mfma_f32_16x16x32_bf16 v[216:219], v[108:111], v[144:147], v[216:219]\n\t" \
  "v_cvt_pk_bf16_f32 v88, v160, v161\n\t" \
  "v_cvt_pk_bf16_f32 v89, v162, v163\n\t" \
  "v_mfma_f32_16x16x32_bf16 v[224:227], v[112:115], v[144:147], v[224:227]\n\t" \
  "v_cvt_pk_bf16_f32 v90, v164, v165\n\t" \
  "v_cvt_pk_bf16_f32 v91, v166, v167\n\t" \
  "v_mfma_f32_16x16x32_bf16 v[232:235], v[116:119], v[144:147], v[232:235]\n\t" \
  "v_cvt_pk_bf16_f32 v72, v248, v249\n\t" \
  "v_cvt_pk_bf16_f32 v73, v250, v251\n\t" \
  "v_mfma_f32_16x16x32_bf16 v[240:243], v[120:123], v[144:147], v[240:243]\n\t" \
  "ds_read_b32 v52, v33 offset:48\n\t" \
  "ds_read_b128 v[10:13], v31\n\t" \
  "v_cvt_pk_bf16_f32 v92, v168, v169\n\t" \
  "v_mfma_f32_16x16x32_bf16 v[252:255], v[124:127], v[148:151], v[252:255]\n\t" \
  "v_cvt_pk_bf16_f32 v93, v170, v171\n\t" \
  "v_cvt_pk_bf16_f32 v94, v172, v173\n\t" \
  "v_mfma_f32_16x16x32_bf16 v[220:223], v[108:111], v[148:151], v[220:223]\n\t" \
  "v_cvt_pk_bf16_f32 v95, v174, v175\n\t" \
  "v_cvt_pk_bf16_f32 v96, v176, v177\n\t" \
  "v_mfma_f32_16x16x32_bf16 v[228:231], v[112:115], v[148:151], v[228:231]\n\t" \
  "v_cvt_pk_bf16_f32 v97, v178, v179\n\t" \
  "v_cvt_pk_bf16_f32 v98, v180, v181\n\t" \
  "v_mfma_f32_16x16x32_bf16 v[236:239], v[116:119], v[148:151], v[236:239]\n\t" \
  "v_cvt_pk_bf16_f32 v82, v252, v253\n\t" \
  "v_cvt_pk_bf16_f32 v83, v254, v255\n\t" \
  "v_mfma_f32_16x16x32_bf16 v[244:247], v[120:123], v[148:151], v[244:247]\n\t" \
  "ds_write_b64 v30, v[72:73]\n\t" \
  "ds_write_b64 v30, v[82:83] offset:8\n\t" \
  "ds_write_b32 v32, v0 offset:48\n\t" \
  "v_mfma_f32_16x16x32_bf16 v[200:203], v[22:25], v[92:95], v[34:37]\n\t" \
  "v_cvt_pk_bf16_f32 v99, v182, v183\n\t" \
  "s_waitcnt lgkmcnt(0)\n\t" \
  "v_readfirstlane_b32 vcc_lo, v52\n\t" \
  "s_nop 1\n\t" \
  "s_cmp_eq_u32 vcc_lo, 0\n\t" \
  "s_cbranch_scc1 5f\n\t" \
  "4:\n\t" \
  "v_mfma_f32_16x16x32_bf16 v[204:207], v[18:21], v[92:95], v[38:41]\n\t" \
  "v_cvt_pk_bf16_f32 v66, v232, v233\n\t" \
  "v_cvt_pk_bf16_f32 v67, v234, v235\n\t" \
  "v_mfma_f32_16x16x32_bf16 v[208:211], v[26:29], v[96:99], v[34:37]\n\t" \
  "v_cvt_pk_bf16_f32 v76, v236, v237\n\t" \
  "v_cvt_pk_bf16_f32 v77, v238, v239\n\t" \
  "v_mfma_f32_16x16x32_bf16 v[212:215], v[22:25], v[96:99], v[38:41]\n\t" \
  "v_cvt_pk_bf16_f32 v64, v240, v241\n\t" \
  "v_cvt_pk_bf16_f32 v65, v242, v243\n\t" \
  "v_mfma_f32_16x16x32_bf16 v[192:195], v[18:21], v[88:91], v[34:37]\n\t" \
  "v_max_f32 v200, v200, v204\n\t" \
  "v_max_f32 v201, v201, v205\n\t" \
  "v_mfma_f32_16x16x32_bf16 v[196:199], v[14:17], v[88:91], v[38:41]\n\t" \
  "v_max_f32 v202, v202, v206\n\t" \
  "v_max_f32 v203, v203, v207\n\t" \
  "v_mfma_f32_16x16x32_bf16 v[184:187], v[14:17], v[84:87], v[34:37]\n\t" \
  "v_max_f32 v208, v208, v212\n\t" \
  "v_max_f32 v209, v209, v213\n\t" \
  "v_mfma_f32_16x16x32_bf16 v[188:191], v[10:13], v[84:87], v[42:45]\n\t" \
  "v_max_f32 v210, v210, v214\n\t" \
  "v_max_f32 v211, v211, v215\n\t" \
  "v_max_f32 v192, v192, v196\n\t" \
  "v_max_f32 v193, v193, v197\n\t" \
  "v_max_f32 v194, v194, v198\n\t" \
  "v_max3_f32 v106, v208, v209, v210\n\t" \
  "v_max3_f32 v104, v200, v201, v202\n\t" \
  "v_max_f32 v195, v195, v199\n\t" \
  "v_max_f32 v184, v184, v188\n\t" \
  "v_max_f32 v185, v185, v189\n\t" \
  "v_max_f32 v186, v186, v190\n\t" \
  "v_max3_f32 v102, v192, v193, v194\n\t" \
  "v_max_f32 v187, v187, v191\n\t" \
  "v_max3_f32 v100, v184, v185, v186\n\t" \
  "v_max_f32 v106, v106, v211\n\t" \
  "v_max_f32 v104, v104, v203\n\t" \
  "v_max_f32 v102, v102, v195\n\t" \
  "v_max_f32 v100, v100, v187\n\t" \
  "v_permlane16_swap_b32 v104, v106\n\t" \
  "v_cvt_pk_bf16_f32 v74, v244, v245\n\t" \
  "v_permlane16_swap_b32 v100, v102\n\t" \
  "v_max_f32 v104, v104, v106\n\t" \
  "v_max_f32 v100, v100, v102\n\t" \
  "v_cvt_pk_bf16_f32 v75, v246, v247\n\t" \
  "v_cvt_pk_bf16_f32 v68, v224, v225\n\t" \
  "v_permlane32_swap_b32 v100, v104\n\t" \
  "v_cvt_pk_bf16_f32 v69, v226, v227\n\t" \
  "v_max_f32 v100, v100, v104\n\t" \
  "v_mul_f32 v100, 0xbe8293ee, v100\n\t" \
  "v_mov_b32 v104, v100\n\t" \
  "v_cvt_pk_bf16_f32 v78, v228, v229\n\t" \
  "v_cvt_pk_bf16_f32 v79, v230, v231\n\t" \
  "v_permlane32_swap_b32 v100, v104\n\t" \
  "v_cvt_pk_bf16_f32 v70, v216, v217\n\t" \
  "v_mov_b32 v106, v104\n\t" \
  "v_mov_b32 v102, v100\n\t" \
  "v_cvt_pk_bf16_f32 v71, v218, v219\n\t" \
  "v_permlane16_swap_b32 v104, v106\n\t" \
  "v_permlane16_swap_b32 v100, v102\n\t" \
  "v_fma_f32 v208, v208, %16, v106\n\t" \
  "v_fma_f32 v209, v209, %16, v106\n\t" \
  "v_fma_f32 v210, v210, %16, v106\n\t" \
  "v_fma_f32 v211, v211, %16, v106\n\t" \
  "v_exp_f32 v208, v208\n\t" \
  "v_exp_f32 v209, v209\n\t" \
  "v_exp_f32 v210, v210\n\t" \
  "v_exp_f32 v211, v211\n\t" \
  "v_cvt_pk_bf16_f32 v212, v208, v209\n\t" \
  "v_cvt_pk_bf16_f32 v213, v210, v211\n\t" \
  "v_bfi_b32 v214, v46, 0, v212\n\t" \
  "v_bfi_b32 v215, v47, 0, v213\n\t" \
  "v_and_b32 v212, v46, v212\n\t" \
  "v_and_b32 v213, v47, v213\n\t" \
  "v_fma_f32 v200, v200, %16, v104\n\t" \
  "v_fma_f32 v201, v201, %16, v104\n\t" \
  "v_mfma_f32_16x16x32_bf16 v[208:211], v[48:51], v[212:215], 0\n\t" \
  "v_fma_f32 v202, v202, %16, v104\n\t" \
  "v_fma_f32 v203, v203, %16, v104\n\t" \
  "v_mfma_f32_16x16x32_bf16 v[176:179], v[64:67], v[212:215], 0\n\t" \
  "v_exp_f32 v200, v200\n\t" \
  "v_mfma_f32_16x16x32_bf16 v[180:183], v[74:77], v[212:215], 0\n\t" \
  "v_exp_f32 v201, v201\n\t" \
  "v_exp_f32 v202, v202\n\t" \
  "v_exp_f32 v203, v203\n\t" \
  "v_cvt_pk_bf16_f32 v204, v200, v201\n\t" \
  "v_cvt_pk_bf16_f32 v205, v202, v203\n\t" \
  "v_bfi_b32 v206, v46, 0, v204\n\t" \
  "v_bfi_b32 v207, v47, 0, v205\n\t" \
  "v_rcp_f32 v106, v208\n\t" \
  "v_and_b32 v204, v46, v204\n\t" \
  "v_and_b32 v205, v47, v205\n\t" \
  "v_pk_mul_f32 v[176:177], v[176:177], v[106:107] op_sel_hi:[1,0]\n\t" \
  "v_pk_mul_f32 v[178:179], v[178:179], v[106:107] op_sel_hi:[1,0]\n\t" \
  "v_mfma_f32_16x16x32_bf16 v[200:203], v[48:51], v[204:207], 0\n\t" \
  "v_pk_mul_f32 v[180:181], v[180:181], v[106:107] op_sel_hi:[1,0]\n\t" \
  "v_pk_mul_f32 v[182:183], v[182:183], v[106:107] op_sel_hi:[1,0]\n\t" \
  "v_mfma_f32_16x16x32_bf16 v[168:171], v[66:69], v[204:207], 0\n\t" \
  "v_cvt_pk_bf16_f32 v240, v176, v177\n\t" \
  "v_cvt_pk_bf16_f32 v241, v178, v179\n\t" \
  "v_mfma_f32_16x16x32_bf16 v[172:175], v[76:79], v[204:207], 0\n\t" \
  "v_cvt_pk_bf16_f32 v242, v180, v181\n\t" \
  "v_cvt_pk_bf16_f32 v243, v182, v183\n\t" \
  "global_store_dwordx4 v1, v[240:243], %14 offset:0 sc1\n\t" \
  "v_fma_f32 v192, v192, %16, v102\n\t" \
  "v_fma_f32 v193, v193, %16, v102\n\t" \
  "v_fma_f32 v194, v194, %16, v102\n\t" \
  "v_fma_f32 v195, v195, %16, v102\n\t" \
  "v_exp_f32 v192, v192\n\t" \
  "v_exp_f32 v193, v193\n\t" \
  "v_exp_f32 v194, v194\n\t" \
  "v_exp_f32 v195, v195\n\t" \
  "v_cvt_pk_bf16_f32 v196, v192, v193\n\t" \
  "v_cvt_pk_bf16_f32 v197, v194, v195\n\t" \
  "v_bfi_b32 v198, v46, 0, v196\n\t" \
  "v_bfi_b32 v199, v47, 0, v197\n\t" \
  "v_rcp_f32 v104, v200\n\t" \
  "v_and_b32 v196, v46, v196\n\t" \
  "v_and_b32 v197, v47, v197\n\t" \
  "v_cvt_pk_bf16_f32 v80, v220, v221\n\t" \
  "v_cvt_pk_bf16_f32 v81, v222, v223\n\t" \
  "v_mfma_f32_16x16x32_bf16 v[192:195], v[48:51], v[196:199], 0\n\t" \
  "v_pk_mul_f32 v[168:169], v[168:169], v[104:105] op_sel_hi:[1,0]\n\t" \
  "v_pk_mul_f32 v[170:171], v[170:171], v[104:105] op_sel_hi:[1,0]\n\t" \
  "v_mfma_f32_16x16x32_bf16 v[160:163], v[68:71], v[196:199], 0\n\t" \
  "v_pk_mul_f32 v[172:173], v[172:173], v[104:105] op_sel_hi:[1,0]\n\t" \
  "v_pk_mul_f32 v[174:175], v[174:175], v[104:105] op_sel_hi:[1,0]\n\t" \
  "v_mfma_f32_16x16x32_bf16 v[164:167], v[78:81], v[196:199], 0\n\t" \
  "v_cvt_pk_bf16_f32 v232, v168, v169\n\t" \
  "v_cvt_pk_bf16_f32 v233, v170, v171\n\t" \
  "v_cvt_pk_bf16_f32 v234, v172, v173\n\t" \
  "v_cvt_pk_bf16_f32 v235, v174, v175\n\t" \
  "global_store_dwordx4 v1, v[232:235], %14 offset:-4096 sc1\n\t" \
  "v_fma_f32 v184, v184, %16, v100\n\t" \
  "v_fma_f32 v185, v185, %16, v100\n\t" \
  "v_fma_f32 v186, v186, %16, v100\n\t" \
  "v_fma_f32 v187, v187, %16, v100\n\t" \
  "v_exp_f32 v184, v184\n\t" \
  "v_exp_f32 v185, v185\n\t" \
  "v_exp_f32 v186, v186\n\t" \
  "v_exp_f32 v187, v187\n\t" \
  "v_cvt_pk_bf16_f32 v188, v184, v185\n\t" \
  "v_cvt_pk_bf16_f32 v189, v186, v187\n\t" \
  "v_bfi_b32 v190, v46, 0, v188\n\t" \
  "v_bfi_b32 v191, v47, 0, v189\n\t" \
  "v_rcp_f32 v102, v192\n\t" \
  "v_and_b32 v188, v46, v188\n\t" \
  "v_and_b32 v189, v47, v189\n\t" \
  "v_pk_mul_f32 v[160:161], v[160:161], v[102:103] op_sel_hi:[1,0]\n\t" \
  "v_pk_mul_f32 v[162:163], v[162:163], v[102:103] op_sel_hi:[1,0]\n\t" \
  "v_mfma_f32_16x16x32_bf16 v[184:187], v[48:51], v[188:191], 0\n\t" \
  "v_pk_mul_f32 v[164:165], v[164:165], v[102:103] op_sel_hi:[1,0]\n\t" \
  "v_pk_mul_f32 v[166:167], v[166:167], v[102:103] op_sel_hi:[1,0]\n\t" \
  "v_mfma_f32_16x16x32_bf16 v[152:155], v[70:73], v[188:191], 0\n\t" \
  "v_cvt_pk_bf16_f32 v224, v160, v161\n\t" \
  "v_cvt_pk_bf16_f32 v225, v162, v163\n\t" \
  "v_mfma_f32_16x16x32_bf16 v[156:159], v[80:83], v[188:191], 0\n\t" \
  "v_cvt_pk_bf16_f32 v226, v164, v165\n\t" \
  "v_cvt_pk_bf16_f32 v227, v166, v167\n\t" \
  "global_store_dwordx4 v4, v[224:227], %14 offset:0 sc1\n\t" \
  "v_rcp_f32 v100, v184\n\t" \
  "ds_read_b128 v[60:63], v56 offset:64\n\t" \
  "ds_read_b64_tr_b16 v[108:109], v5 offset:0\n\t" \
  "ds_read_b64_tr_b16 v[110:111], v5 offset:16\n\t" \
  "ds_read_b128 v[112:115], v6 offset:16384\n\t" \
  "ds_read_b128 v[116:119], v6 offset:15360\n\t" \
  "ds_read_b64_tr_b16 v[120:121], v5 offset:512\n\t" \
  "ds_read_b64_tr_b16 v[122:123], v5 offset:528\n\t" \
  "ds_read_b128 v[124:127], v6 offset:14336\n\t" \
  "ds_read_b64_tr_b16 v[128:129], v5 offset:1024\n\t" \
  "ds_read_b64_tr_b16 v[130:131], v5 offset:1040\n\t" \
  "ds_read_b128 v[132:135], v6 offset:13312\n\t" \
  "ds_read_b64_tr_b16 v[136:137], v5 offset:1536\n\t" \
  "ds_read_b64_tr_b16 v[138:139], v5 offset:1552\n\t" \
  "ds_read_b128 v[140:143], v6 offset:12288\n\t" \
  "ds_read_b64_tr_b16 v[144:145], v5 offset:2048\n\t" \
  "v_pk_mul_f32 v[152:153], v[152:153], v[100:101] op_sel_hi:[1,0]\n\t" \
  "v_pk_mul_f32 v[154:155], v[154:155], v[100:101] op_sel_hi:[1,0]\n\t" \
  "v_cvt_pk_bf16_f32 v216, v152, v153\n\t" \
  "v_pk_mul_f32 v[156:157], v[156:157], v[100:101] op_sel_hi:[1,0]\n\t" \
  "v_pk_mul_f32 v[158:159], v[158:159], v[100:101] op_sel_hi:[1,0]\n\t" \
  "v_cvt_pk_bf16_f32 v217, v154, v155\n\t" \
  "v_cvt_pk_bf16_f32 v218, v156, v157\n\t" \
  "v_cvt_pk_bf16_f32 v219, v158, v159\n\t" \
  "global_store_dwordx4 v4, v[216:219], %14 offset:-4096 sc1\n\t" \
  "ds_write_b32 v32, v0 offset:112\n\t" \
  "s_branch 9f\n\t" \
  "5:\n\t" \
  "ds_read_b32 v52, v33 offset:48\n\t" \
  "ds_read_b128 v[10:13], v31\n\t" \
  "s_waitcnt lgkmcnt(0)\n\t" \
  "v_readfirstlane_b32 vcc_lo, v52\n\t" \
  "s_nop 1\n\t" \
  "s_cmp_eq_u32 vcc_lo, 0\n\t" \
  "s_cbranch_scc1 5b\n\t" \
  "s_branch 4b\n\t" \
  "15:\n\t" \
  "ds_read_b128 v[10:13], v14 offset:128\n\t" \
  "s_waitcnt lgkmcnt(0)\n\t" \
  "v_and_b32 v10, v10, v11\n\t" \
  "v_and_b32 v12, v12, v13\n\t" \
  "v_and_b32 v10, v10, v12\n\t" \
  "s_nop 1\n\t" \
  "v_readfirstlane_b32 vcc_lo, v10\n\t" \
  "s_nop 1\n\t" \
  "s_cmp_eq_u32 vcc_lo, 0\n\t" \
  "s_cbranch_scc1 15b\n\t" \
  "s_branch 14b\n\t" \
  "8:\n\t" \
  "global_load_dwordx2 v[10:11], v51, %11\n\t" \
  "global_load_dwordx2 v[12:13], v51, %11 offset:128\n\t" \
  "v_xor_b32 v53, 64, v52\n\t" \
  "v_xor_b32 v55, 32, v54\n\t" \
  "v_add_u32 v56, 43008, v52\n\t" \
  "v_add_u32 v58, 43008, v54\n\t" \
  "v_xor_b32 v57, 64, v56\n\t" \
  "v_xor_b32 v59, 32, v58\n\t" \
  "v_add_u32 v60, 86016, v52\n\t" \
  "v_add_u32 v62, 86016, v54\n\t" \
  "v_xor_b32 v61, 64, v60\n\t" \
  "v_xor_b32 v63, 32, v62\n\t" \
  "s_mov_b32 vcc_hi, 0x05040100\n\t" \
  "s_waitcnt vmcnt(2)\n\t" \
  "s_barrier\n\t" \
  "ds_read_b64_tr_b16 v[92:93], v62 offset:26624\n\t" \
  "ds_read_b64_tr_b16 v[94:95], v62 offset:27136\n\t" \
  "ds_read_b128 v[80:83], v60 offset:0\n\t" \
  "ds_read_b128 v[64:67], v60 offset:2048\n\t" \
  "ds_read_b128 v[68:71], v60 offset:4096\n\t" \
  "ds_read_b128 v[72:75], v60 offset:6144\n\t" \
  "ds_read_b128 v[76:79], v60 offset:8192\n\t" \
  "ds_read_b64_tr_b16 v[96:97], v63 offset:26624\n\t" \
  "ds_read_b64_tr_b16 v[98:99], v63 offset:27136\n\t" \
  "ds_read_b64_tr_b16 v[100:101], v62 offset:34816\n\t" \
  "ds_read_b64_tr_b16 v[102:103], v62 offset:35328\n\t" \
  "ds_read_b64_tr_b16 v[104:105], v63 offset:34816\n\t" \
  "ds_read_b64_tr_b16 v[106:107], v63 offset:35328\n\t" \
  "ds_read_b64_tr_b16 v[84:85], v62 offset:18432\n\t" \
  "ds_read_b64_tr_b16 v[86:87], v62 offset:18944\n\t" \
  "ds_read_b64_tr_b16 v[88:89], v63 offset:18432\n\t" \
  "s_waitcnt lgkmcnt(13)\n\t" \
  "v_mfma_f32_16x16x32_bf16 v[248:251], v[92:95], v[80:83], 0\n\t" \
  "ds_read_b64_tr_b16 v[90:91], v63 offset:18944\n\t" \
  "ds_read_b64_tr_b16 v[136:137], v62 offset:30720\n\t" \
  "s_waitcnt lgkmcnt(14)\n\t" \
  "v_mfma_f32_16x16x32_bf16 v[184:187], v[92:95], v[64:67], 0\n\t" \
  "ds_read_b64_tr_b16 v[138:139], v62 offset:31232\n\t" \
  "ds_read_b128 v[124:127], v61 offset:0\n\t" \
  "s_waitcnt lgkmcnt(15)\n\t" \
  "v_mfma_f32_16x16x32_bf16 v[192:195], v[92:95], v[68:71], 0\n\t" \
  "ds_read_b128 v[108:111], v61 offset:2048\n\t" \
  "ds_read_b128 v[112:115], v61 offset:4096\n\t" \
  "s_waitcnt lgkmcnt(15)\n\t" \
  "v_mfma_f32_16x16x32_bf16 v[200:203], v[92:95], v[72:75], 0\n\t" \
  "ds_read_b128 v[116:119], v61 offset:6144\n\t" \
  "ds_read_b128 v[120:123], v61 offset:8192\n\t" \
  "v_mfma_f32_16x16x32_bf16 v[208:211], v[92:95], v[76:79], 0\n\t" \
  "ds_read_b64_tr_b16 v[140:141], v63 offset:30720\n\t" \
  "ds_read_b64_tr_b16 v[142:143], v63 offset:31232\n\t" \
  "s_waitcnt lgkmcnt(15)\n\t" \
  "v_mfma_f32_16x16x32_bf16 v[252:255], v[96:99], v[80:83], 0\n\t" \
  "ds_read_b64_tr_b16 v[144:145], v62 offset:38912\n\t" \
  "ds_read_b64_tr_b16 v[146:147], v62 offset:39424\n\t" \
  "v_mfma_f32_16x16x32_bf16 v[188:191], v[96:99], v[64:67], 0\n\t" \
  "ds_read_b64_tr_b16 v[148:149], v63 offset:38912\n\t" \
  "ds_read_b64_tr_b16 v[150:151], v63 offset:39424\n\t" \
  "v_mfma_f32_16x16x32_bf16 v[196:199], v[96:99], v[68:71], 0\n\t" \
  "ds_read_b64_tr_b16 v[128:129], v62 offset:22528\n\t" \
  "ds_read_b64_tr_b16 v[130:131], v62 offset:23040\n\t" \
  "v_mfma_f32_16x16x32_bf16 v[204:207], v[96:99], v[72:75], 0\n\t" \
  "ds_read_b64_tr_b16 v[132:133], v63 offset:22528\n\t" \
  "ds_read_b64_tr_b16 v[134:135], v63 offset:23040\n\t" \
  "v_mfma_f32_16x16x32_bf16 v[212:215], v[96:99], v[76:79], 0\n\t" \
  "v_mfma_f32_16x16x32_bf16 v[216:219], v[64:67], v[100:103], 0\n\t" \
  "v_mfma_f32_16x16x32_bf16 v[224:227], v[68:71], v[100:103], 0\n\t" \
  "v_mfma_f32_16x16x32_bf16 v[232:235], v[72:75], v[100:103], 0\n\t" \
  "s_waitcnt lgkmcnt(0)\n\t" \
  "s_getpc_b64 s[88:89]\n\t" \
  "s_branch 28b\n\t" \
  "s_barrier\n\t" \
  "v_mfma_f32_16x16x32_bf16 v[240:243], v[76:79], v[100:103], 0\n\t" \
  "ds_read_b64_tr_b16 v[92:93], v54 offset:26624\n\t" \
  "v_mfma_f32_16x16x32_bf16 v[220:223], v[64:67], v[104:107], 0\n\t" \
  "ds_read_b64_tr_b16 v[94:95], v54 offset:27136\n\t" \
  "v_mfma_f32_16x16x32_bf16 v[228:231], v[68:71], v[104:107], 0\n\t" \
  "ds_read_b128 v[80:83], v52 offset:0\n\t" \
  "v_mfma_f32_16x16x32_bf16 v[236:239], v[72:75], v[104:107], 0\n\t" \
  "ds_read_b64_tr_b16 v[96:97], v55 offset:26624\n\t" \
  "v_mfma_f32_16x16x32_bf16 v[244:247], v[76:79], v[104:107], 0\n\t" \
  "ds_read_b64_tr_b16 v[98:99], v55 offset:27136\n\t" \
  "v_mfma_f32_16x16x32_bf16 v[152:155], v[84:87], v[64:67], 0\n\t" \
  "ds_read_b64_tr_b16 v[100:101], v54 offset:34816\n\t" \
  "v_mfma_f32_16x16x32_bf16 v[160:163], v[84:87], v[68:71], 0\n\t" \
  "ds_read_b64_tr_b16 v[102:103], v54 offset:35328\n\t" \
  "v_mfma_f32_16x16x32_bf16 v[168:171], v[84:87], v[72:75], 0\n\t" \
  "ds_read_b64_tr_b16 v[104:105], v55 offset:34816\n\t" \
  "v_mfma_f32_16x16x32_bf16 v[176:179], v[84:87], v[76:79], 0\n\t" \
  "ds_read_b64_tr_b16 v[106:107], v55 offset:35328\n\t" \
  "v_mfma_f32_16x16x32_bf16 v[156:159], v[88:91], v[64:67], 0\n\t" \
  "ds_read_b128 v[64:67], v52 offset:2048\n\t" \
  "v_mfma_f32_16x16x32_bf16 v[164:167], v[88:91], v[68:71], 0\n\t" \
  "ds_read_b128 v[68:71], v52 offset:4096\n\t" \
  "v_mfma_f32_16x16x32_bf16 v[172:175], v[88:91], v[72:75], 0\n\t" \
  "ds_read_b128 v[72:75], v52 offset:6144\n\t" \
  "v_mfma_f32_16x16x32_bf16 v[180:183], v[88:91], v[76:79], 0\n\t" \
  "ds_read_b128 v[76:79], v52 offset:8192\n\t" \
  "v_mfma_f32_16x16x32_bf16 v[248:251], v[136:139], v[124:127], v[248:251]\n\t" \
  "ds_read_b64_tr_b16 v[84:85], v54 offset:18432\n\t" \
  "v_mfma_f32_16x16x32_bf16 v[184:187], v[136:139], v[108:111], v[184:187]\n\t" \
  "ds_read_b64_tr_b16 v[86:87], v54 offset:18944\n\t" \
  "v_mfma_f32_16x16x32_bf16 v[192:195], v[136:139], v[112:115], v[192:195]\n\t" \
  "ds_read_b64_tr_b16 v[88:89], v55 offset:18432\n\t" \
  "v_mfma_f32_16x16x32_bf16 v[200:203], v[136:139], v[116:119], v[200:203]\n\t" \
  "ds_read_b64_tr_b16 v[90:91], v55 offset:18944\n\t" \
  "v_mfma_f32_16x16x32_bf16 v[208:211], v[136:139], v[120:123], v[208:211]\n\t" \
  "ds_read_b64_tr_b16 v[136:137], v54 offset:30720\n\t" \
  "v_mfma_f32_16x16x32_bf16 v[252:255], v[140:143], v[124:127], v[252:255]\n\t" \
  "ds_read_b64_tr_b16 v[138:139], v54 offset:31232\n\t" \
  "v_mfma_f32_16x16x32_bf16 v[188:191], v[140:143], v[108:111], v[188:191]\n\t" \
  "ds_read_b128 v[124:127], v53 offset:0\n\t" \
  "v_mfma_f32_16x16x32_bf16 v[196:199], v[140:143], v[112:115], v[196:199]\n\t" \
  "v_mfma_f32_16x16x32_bf16 v[204:207], v[140:143], v[116:119], v[204:207]\n\t" \
  "v_mfma_f32_16x16x32_bf16 v[212:215], v[140:143], v[120:123], v[212:215]\n\t" \
  "ds_read_b64_tr_b16 v[140:141], v55 offset:30720\n\t" \
  "v_mfma_f32_16x16x32_bf16 v[216:219], v[108:111], v[144:147], v[216:219]\n\t" \
  "ds_read_b64_tr_b16 v[142:143], v55 offset:31232\n\t" \
  "v_mfma_f32_16x16x32_bf16 v[224:227], v[112:115], v[144:147], v[224:227]\n\t" \
  "v_mfma_f32_16x16x32_bf16 v[232:235], v[116:119], v[144:147], v[232:235]\n\t" \
  "v_mfma_f32_16x16x32_bf16 v[240:243], v[120:123], v[144:147], v[240:243]\n\t" \
  "ds_read_b64_tr_b16 v[144:145], v54 offset:38912\n\t" \
  "v_mfma_f32_16x16x32_bf16 v[220:223], v[108:111], v[148:151], v[220:223]\n\t" \
  "ds_read_b64_tr_b16 v[146:147], v54 offset:39424\n\t" \
  "v_mfma_f32_16x16x32_bf16 v[228:231], v[112:115], v[148:151], v[228:231]\n\t" \
  "v_mfma_f32_16x16x32_bf16 v[236:239], v[116:119], v[148:151], v[236:239]\n\t" \
  "v_mfma_f32_16x16x32_bf16 v[244:247], v[120:123], v[148:151], v[244:247]\n\t" \
  "ds_read_b64_tr_b16 v[148:149], v55 offset:38912\n\t" \
  "v_mfma_f32_16x16x32_bf16 v[152:155], v[128:131], v[108:111], v[152:155]\n\t" \
  "ds_read_b64_tr_b16 v[150:151], v55 offset:39424\n\t" \
  "v_mfma_f32_16x16x32_bf16 v[160:163], v[128:131], v[112:115], v[160:163]\n\t" \
  "v_mfma_f32_16x16x32_bf16 v[168:171], v[128:131], v[116:119], v[168:171]\n\t" \
  "v_mfma_f32_16x16x32_bf16 v[176:179], v[128:131], v[120:123], v[176:179]\n\t" \
  "ds_read_b64_tr_b16 v[128:129], v54 offset:22528\n\t" \
  "v_mfma_f32_16x16x32_bf16 v[156:159], v[132:135], v[108:111], v[156:159]\n\t" \
  "ds_read_b128 v[108:111], v53 offset:2048\n\t" \
  "v_mfma_f32_16x16x32_bf16 v[164:167], v[132:135], v[112:115], v[164:167]\n\t" \
  "ds_read_b128 v[112:115], v53 offset:4096\n\t" \
  "v_mfma_f32_16x16x32_bf16 v[172:175], v[132:135], v[116:119], v[172:175]\n\t" \
  "ds_read_b128 v[116:119], v53 offset:6144\n\t" \
  "v_mfma_f32_16x16x32_bf16 v[180:183], v[132:135], v[120:123], v[180:183]\n\t" \
  "ds_read_b128 v[120:123], v53 offset:8192\n\t" \
  "s_waitcnt lgkmcnt(5)\n\t" \
  "v_mfma_f32_16x16x32_bf16 v[248:251], v[92:95], v[80:83], v[248:251]\n\t" \
  "ds_read_b64_tr_b16 v[130:131], v54 offset:23040\n\t" \
  "v_mfma_f32_16x16x32_bf16 v[184:187], v[92:95], v[64:67], v[184:187]\n\t" \
  "ds_read_b64_tr_b16 v[132:133], v55 offset:22528\n\t" \
  "v_mfma_f32_16x16x32_bf16 v[192:195], v[92:95], v[68:71], v[192:195]\n\t" \
  "ds_read_b64_tr_b16 v[134:135], v55 offset:23040\n\t" \
  "v_mfma_f32_16x16x32_bf16 v[200:203], v[92:95], v[72:75], v[200:203]\n\t" \
  "v_mfma_f32_16x16x32_bf16 v[208:211], v[92:95], v[76:79], v[208:211]\n\t" \
  "v_mfma_f32_16x16x32_bf16 v[252:255], v[96:99], v[80:83], v[252:255]\n\t" \
  "v_mfma_f32_16x16x32_bf16 v[188:191], v[96:99], v[64:67], v[188:191]\n\t" \
  "v_mfma_f32_16x16x32_bf16 v[196:199], v[96:99], v[68:71], v[196:199]\n\t" \
  "v_mfma_f32_16x16x32_bf16 v[204:207], v[96:99], v[72:75], v[204:207]\n\t" \
  "v_mfma_f32_16x16x32_bf16 v[212:215], v[96:99], v[76:79], v[212:215]\n\t" \
  "v_mfma_f32_16x16x32_bf16 v[216:219], v[64:67], v[100:103], v[216:219]\n\t" \
  "v_mfma_f32_16x16x32_bf16 v[224:227], v[68:71], v[100:103], v[224:227]\n\t" \
  "v_mfma_f32_16x16x32_bf16 v[232:235], v[72:75], v[100:103], v[232:235]\n\t" \
  "s_waitcnt lgkmcnt(0)\n\t" \
  "s_barrier\n\t" \
  "v_mfma_f32_16x16x32_bf16 v[240:243], v[76:79], v[100:103], v[240:243]\n\t" \
  "ds_read_b64_tr_b16 v[92:93], v58 offset:26624\n\t" \
  "v_mfma_f32_16x16x32_bf16 v[220:223], v[64:67], v[104:107], v[220:223]\n\t" \
  "ds_read_b64_tr_b16 v[94:95], v58 offset:27136\n\t" \
  "v_mfma_f32_16x16x32_bf16 v[228:231], v[68:71], v[104:107], v[228:231]\n\t" \
  "ds_read_b128 v[80:83], v56 offset:0\n\t" \
  "v_mfma_f32_16x16x32_bf16 v[236:239], v[72:75], v[104:107], v[236:239]\n\t" \
  "ds_read_b64_tr_b16 v[96:97], v59 offset:26624\n\t" \
  "v_mfma_f32_16x16x32_bf16 v[244:247], v[76:79], v[104:107], v[244:247]\n\t" \
  "ds_read_b64_tr_b16 v[98:99], v59 offset:27136\n\t" \
  "v_mfma_f32_16x16x32_bf16 v[152:155], v[84:87], v[64:67], v[152:155]\n\t" \
  "ds_read_b64_tr_b16 v[100:101], v58 offset:34816\n\t" \
  "v_mfma_f32_16x16x32_bf16 v[160:163], v[84:87], v[68:71], v[160:163]\n\t" \
  "ds_read_b64_tr_b16 v[102:103], v58 offset:35328\n\t" \
  "v_mfma_f32_16x16x32_bf16 v[168:171], v[84:87], v[72:75], v[168:171]\n\t" \
  "ds_read_b64_tr_b16 v[104:105], v59 offset:34816\n\t" \
  "v_mfma_f32_16x16x32_bf16 v[176:179], v[84:87], v[76:79], v[176:179]\n\t" \
  "ds_read_b64_tr_b16 v[106:107], v59 offset:35328\n\t" \
  "v_mfma_f32_16x16x32_bf16 v[156:159], v[88:91], v[64:67], v[156:159]\n\t" \
  "ds_read_b128 v[64:67], v56 offset:2048\n\t" \
  "v_mfma_f32_16x16x32_bf16 v[164:167], v[88:91], v[68:71], v[164:167]\n\t" \
  "ds_read_b128 v[68:71], v56 offset:4096\n\t" \
  "v_mfma_f32_16x16x32_bf16 v[172:175], v[88:91], v[72:75], v[172:175]\n\t" \
  "ds_read_b128 v[72:75], v56 offset:6144\n\t" \
  "v_mfma_f32_16x16x32_bf16 v[180:183], v[88:91], v[76:79], v[180:183]\n\t" \
  "ds_read_b128 v[76:79], v56 offset:8192\n\t" \
  "v_mfma_f32_16x16x32_bf16 v[248:251], v[136:139], v[124:127], v[248:251]\n\t" \
  "ds_read_b64_tr_b16 v[84:85], v58 offset:18432\n\t" \
  "v_mfma_f32_16x16x32_bf16 v[184:187], v[136:139], v[108:111], v[184:187]\n\t" \
  "ds_read_b64_tr_b16 v[86:87], v58 offset:18944\n\t" \
  "v_mfma_f32_16x16x32_bf16 v[192:195], v[136:139], v[112:115], v[192:195]\n\t" \
  "ds_read_b64_tr_b16 v[88:89], v59 offset:18432\n\t" \
  "v_mfma_f32_16x16x32_bf16 v[200:203], v[136:139], v[116:119], v[200:203]\n\t" \
  "ds_read_b64_tr_b16 v[90:91], v59 offset:18944\n\t" \
  "v_mfma_f32_16x16x32_bf16 v[208:211], v[136:139], v[120:123], v[208:211]\n\t" \
  "ds_read_b64_tr_b16 v[136:137], v58 offset:30720\n\t" \
  "v_mfma_f32_16x16x32_bf16 v[252:255], v[140:143], v[124:127], v[252:255]\n\t" \
  "ds_read_b64_tr_b16 v[138:139], v58 offset:31232\n\t" \
  "v_mfma_f32_16x16x32_bf16 v[188:191], v[140:143], v[108:111], v[188:191]\n\t" \
  "ds_read_b128 v[124:127], v57 offset:0\n\t" \
  "v_mfma_f32_16x16x32_bf16 v[196:199], v[140:143], v[112:115], v[196:199]\n\t" \
  "v_mfma_f32_16x16x32_bf16 v[204:207], v[140:143], v[116:119], v[204:207]\n\t" \
  "v_mfma_f32_16x16x32_bf16 v[212:215], v[140:143], v[120:123], v[212:215]\n\t" \
  "ds_read_b64_tr_b16 v[140:141], v59 offset:30720\n\t" \
  "v_mfma_f32_16x16x32_bf16 v[216:219], v[108:111], v[144:147], v[216:219]\n\t" \
  "ds_read_b64_tr_b16 v[142:143], v59 offset:31232\n\t" \
  "v_mfma_f32_16x16x32_bf16 v[224:227], v[112:115], v[144:147], v[224:227]\n\t" \
  "v_mfma_f32_16x16x32_bf16 v[232:235], v[116:119], v[144:147], v[232:235]\n\t" \
  "v_mfma_f32_16x16x32_bf16 v[240:243], v[120:123], v[144:147], v[240:243]\n\t" \
  "ds_read_b64_tr_b16 v[144:145], v58 offset:38912\n\t" \
  "v_mfma_f32_16x16x32_bf16 v[220:223], v[108:111], v[148:151], v[220:223]\n\t" \
  "ds_read_b64_tr_b16 v[146:147], v58 offset:39424\n\t" \
  "v_mfma_f32_16x16x32_bf16 v[228:231], v[112:115], v[148:151], v[228:231]\n\t" \
  "v_mfma_f32_16x16x32_bf16 v[236:239], v[116:119], v[148:151], v[236:239]\n\t" \
  "v_mfma_f32_16x16x32_bf16 v[244:247], v[120:123], v[148:151], v[244:247]\n\t" \
  "ds_read_b64_tr_b16 v[148:149], v59 offset:38912\n\t" \
  "v_mfma_f32_16x16x32_bf16 v[152:155], v[128:131], v[108:111], v[152:155]\n\t" \
  "ds_read_b64_tr_b16 v[150:151], v59 offset:39424\n\t" \
  "v_mfma_f32_16x16x32_bf16 v[160:163], v[128:131], v[112:115], v[160:163]\n\t" \
  "v_mfma_f32_16x16x32_bf16 v[168:171], v[128:131], v[116:119], v[168:171]\n\t" \
  "v_mfma_f32_16x16x32_bf16 v[176:179], v[128:131], v[120:123], v[176:179]\n\t" \
  "ds_read_b64_tr_b16 v[128:129], v58 offset:22528\n\t" \
  "v_mfma_f32_16x16x32_bf16 v[156:159], v[132:135], v[108:111], v[156:159]\n\t" \
  "ds_read_b128 v[108:111], v57 offset:2048\n\t" \
  "v_mfma_f32_16x16x32_bf16 v[164:167], v[132:135], v[112:115], v[164:167]\n\t" \
  "ds_read_b128 v[112:115], v57 offset:4096\n\t" \
  "v_mfma_f32_16x16x32_bf16 v[172:175], v[132:135], v[116:119], v[172:175]\n\t" \
  "ds_read_b128 v[116:119], v57 offset:6144\n\t" \
  "v_mfma_f32_16x16x32_bf16 v[180:183], v[132:135], v[120:123], v[180:183]\n\t" \
  "ds_read_b128 v[120:123], v57 offset:8192\n\t" \
  "s_waitcnt lgkmcnt(5)\n\t" \
  "v_mfma_f32_16x16x32_bf16 v[248:251], v[92:95], v[80:83], v[248:251]\n\t" \
  "ds_read_b64_tr_b16 v[130:131], v58 offset:23040\n\t" \
  "v_mfma_f32_16x16x32_bf16 v[184:187], v[92:95], v[64:67], v[184:187]\n\t" \
  "ds_read_b64_tr_b16 v[132:133], v59 offset:22528\n\t" \
  "v_mfma_f32_16x16x32_bf16 v[192:195], v[92:95], v[68:71], v[192:195]\n\t" \
  "ds_read_b64_tr_b16 v[134:135], v59 offset:23040\n\t" \
  "v_mfma_f32_16x16x32_bf16 v[200:203], v[92:95], v[72:75], v[200:203]\n\t" \
  "v_mfma_f32_16x16x32_bf16 v[208:211], v[92:95], v[76:79], v[208:211]\n\t" \
  "v_mfma_f32_16x16x32_bf16 v[252:255], v[96:99], v[80:83], v[252:255]\n\t" \
  "v_mfma_f32_16x16x32_bf16 v[188:191], v[96:99], v[64:67], v[188:191]\n\t" \
  "v_mfma_f32_16x16x32_bf16 v[196:199], v[96:99], v[68:71], v[196:199]\n\t" \
  "v_mfma_f32_16x16x32_bf16 v[204:207], v[96:99], v[72:75], v[204:207]\n\t" \
  "v_mfma_f32_16x16x32_bf16 v[212:215], v[96:99], v[76:79], v[212:215]\n\t" \
  "v_mfma_f32_16x16x32_bf16 v[216:219], v[64:67], v[100:103], v[216:219]\n\t" \
  "v_mfma_f32_16x16x32_bf16 v[224:227], v[68:71], v[100:103], v[224:227]\n\t" \
  "v_mfma_f32_16x16x32_bf16 v[232:235], v[72:75], v[100:103], v[232:235]\n\t" \
  "s_waitcnt lgkmcnt(0)\n\t" \
  "s_barrier\n\t" \
  "v_mfma_f32_16x16x32_bf16 v[240:243], v[76:79], v[100:103], v[240:243]\n\t" \
  "ds_read_b64_tr_b16 v[92:93], v62 offset:26624\n\t" \
  "v_bfe_u32 v30, v4, 9, 1\n\t" \
  "v_mfma_f32_16x16x32_bf16 v[220:223], v[64:67], v[104:107], v[220:223]\n\t" \
  "ds_read_b64_tr_b16 v[94:95], v62 offset:27136\n\t" \
  "v_mad_i32_i24 v30, v30, -16, v0\n\t" \
  "v_mfma_f32_16x16x32_bf16 v[228:231], v[68:71], v[104:107], v[228:231]\n\t" \
  "ds_read_b128 v[80:83], v60 offset:0\n\t" \
  "ds_read_b128 v[26:29], v0 offset:1552\n\t" \
  "v_mfma_f32_16x16x32_bf16 v[236:239], v[72:75], v[104:107], v[236:239]\n\t" \
  "ds_read_b64_tr_b16 v[96:97], v63 offset:26624\n\t" \
  "ds_read_b128 v[30:33], v30 offset:1040\n\t" \
  "v_mfma_f32_16x16x32_bf16 v[244:247], v[76:79], v[104:107], v[244:247]\n\t" \
  "ds_read_b64_tr_b16 v[98:99], v63 offset:27136\n\t" \
  "ds_read_b128 v[34:37], v0 offset:512\n\t" \
  "v_mfma_f32_16x16x32_bf16 v[152:155], v[84:87], v[64:67], v[152:155]\n\t" \
  "ds_read_b64_tr_b16 v[100:101], v62 offset:34816\n\t" \
  "ds_read_b64_tr_b16 v[38:39], v1 offset:512\n\t" \
  "v_mfma_f32_16x16x32_bf16 v[160:163], v[84:87], v[68:71], v[160:163]\n\t" \
  "ds_read_b64_tr_b16 v[102:103], v62 offset:35328\n\t" \
  "ds_read_b64_tr_b16 v[40:41], v1 offset:0\n\t" \
  "v_mfma_f32_16x16x32_bf16 v[168:171], v[84:87], v[72:75], v[168:171]\n\t" \
  "503:\n\t" \
  "s_cbranch_execz 507f\n\t" \
  "ds_read_b64_tr_b16 v[104:105], v63 offset:34816\n\t" \
  "s_waitcnt vmcnt(0)\n\t" \
  "v_mfma_f32_16x16x32_bf16 v[176:179], v[84:87], v[76:79], v[176:179]\n\t" \
  "ds_read_b64_tr_b16 v[106:107], v63 offset:35328\n\t" \
  "v_perm_b32 v18, v10, v10, v14\n\t" \
  "v_mfma_f32_16x16x32_bf16 v[156:159], v[88:91], v[64:67], v[156:159]\n\t" \
  "ds_read_b128 v[64:67], v60 offset:2048\n\t" \
  "v_perm_b32 v19, v10, v10, v15\n\t" \
  "v_mfma_f32_16x16x32_bf16 v[164:167], v[88:91], v[68:71], v[164:167]\n\t" \
  "ds_read_b128 v[68:71], v60 offset:4096\n\t" \
  "v_perm_b32 v20, v10, v10, v16\n\t" \
  "v_mfma_f32_16x16x32_bf16 v[172:175], v[88:91], v[72:75], v[172:175]\n\t" \
  "ds_read_b128 v[72:75], v60 offset:6144\n\t" \
  "v_perm_b32 v21, v10, v10, v17\n\t" \
  "v_mfma_f32_16x16x32_bf16 v[180:183], v[88:91], v[76:79], v[180:183]\n\t" \
  "ds_read_b128 v[76:79], v60 offset:8192\n\t" \
  "v_perm_b32 v22, v11, v11, v14\n\t" \
  "v_mfma_f32_16x16x32_bf16 v[248:251], v[136:139], v[124:127], v[248:251]\n\t" \
  "ds_read_b64_tr_b16 v[84:85], v62 offset:18432\n\t" \
  "v_perm_b32 v23, v11, v11, v15\n\t" \
  "v_mfma_f32_16x16x32_bf16 v[184:187], v[136:139], v[108:111], v[184:187]\n\t" \
  "ds_read_b64_tr_b16 v[86:87], v62 offset:18944\n\t" \
  "v_perm_b32 v24, v11, v11, v16\n\t" \
  "v_mfma_f32_16x16x32_bf16 v[192:195], v[136:139], v[112:115], v[192:195]\n\t" \
  "ds_read_b64_tr_b16 v[88:89], v63 offset:18432\n\t" \
  "v_perm_b32 v25, v11, v11, v17\n\t" \
  "v_mfma_f32_16x16x32_bf16 v[200:203], v[136:139], v[116:119], v[200:203]\n\t" \
  "ds_read_b64_tr_b16 v[90:91], v63 offset:18944\n\t" \
  "v_mfma_f32_16x16x32_bf16 v[208:211], v[136:139], v[120:123], v[208:211]\n\t" \
  "ds_read_b64_tr_b16 v[136:137], v62 offset:30720\n\t" \
  "v_mfma_f32_16x16x32_bf16 v[252:255], v[140:143], v[124:127], v[252:255]\n\t" \
  "ds_read_b64_tr_b16 v[138:139], v62 offset:31232\n\t" \
  "s_waitcnt lgkmcnt(6)\n\t" \
  "v_mfma_f32_16x16x32_bf16 v[42:45], v[26:29], v[18:21], 0\n\t" \
  "v_mfma_f32_16x16x32_bf16 v[42:45], v[30:33], v[22:25], v[42:45]\n\t" \
  "v_mfma_f32_16x16x32_bf16 v[46:49], v[30:33], v[18:21], 0\n\t" \
  "v_mfma_f32_16x16x32_bf16 v[46:49], v[34:37], v[22:25], v[46:49]\n\t" \
  "ds_read_b128 v[26:29], v0 offset:1808\n\t" \
  "ds_read_b128 v[30:33], v0 offset:1296\n\t" \
  "v_mfma_f32_16x16x32_bf16 v[188:191], v[140:143], v[108:111], v[188:191]\n\t" \
  "ds_read_b128 v[124:127], v61 offset:0\n\t" \
  "ds_read_b128 v[34:37], v0 offset:768\n\t" \
  "v_mfma_f32_16x16x32_bf16 v[196:199], v[140:143], v[112:115], v[196:199]\n\t" \
  "v_mfma_f32_16x16x32_bf16 v[204:207], v[140:143], v[116:119], v[204:207]\n\t" \
  "v_mfma_f32_16x16x32_bf16 v[212:215], v[140:143], v[120:123], v[212:215]\n\t" \
  "ds_read_b64_tr_b16 v[140:141], v63 offset:30720\n\t" \
  "v_lshlrev_b32 v50, 16, v38\n\t" \
  "v_mfma_f32_16x16x32_bf16 v[216:219], v[108:111], v[144:147], v[216:219]\n\t" \
  "ds_read_b64_tr_b16 v[142:143], v63 offset:31232\n\t" \
  "v_lshlrev_b32 v51, 16, v40\n\t" \
  "v_mfma_f32_16x16x32_bf16 v[224:227], v[112:115], v[144:147], v[224:227]\n\t" \
  "v_mul_f32 v42, v42, v50\n\t" \
  "v_mul_f32 v46, v46, v51\n\t" \
  "v_mfma_f32_16x16x32_bf16 v[232:235], v[116:119], v[144:147], v[232:235]\n\t" \
  "v_cvt_pk_bf16_f32 v42, v42, v46\n\t" \
  "v_and_b32 v50, 0xffff0000, v38\n\t" \
  "v_mfma_f32_16x16x32_bf16 v[240:243], v[120:123], v[144:147], v[240:243]\n\t" \
  "ds_read_b64_tr_b16 v[144:145], v62 offset:38912\n\t" \
  "v_and_b32 v51, 0xffff0000, v40\n\t" \
  "v_mfma_f32_16x16x32_bf16 v[220:223], v[108:111], v[148:151], v[220:223]\n\t" \
  "ds_read_b64_tr_b16 v[146:147], v62 offset:39424\n\t" \
  "v_mul_f32 v43, v43, v50\n\t" \
  "v_mfma_f32_16x16x32_bf16 v[228:231], v[112:115], v[148:151], v[228:231]\n\t" \
  "v_mul_f32 v47, v47, v51\n\t" \
  "v_cvt_pk_bf16_f32 v43, v43, v47\n\t" \
  "v_mfma_f32_16x16x32_bf16 v[236:239], v[116:119], v[148:151], v[236:239]\n\t" \
  "v_lshlrev_b32 v50, 16, v39\n\t" \
  "v_lshlrev_b32 v51, 16, v41\n\t" \
  "v_mfma_f32_16x16x32_bf16 v[244:247], v[120:123], v[148:151], v[244:247]\n\t" \
  "ds_read_b64_tr_b16 v[148:149], v63 offset:38912\n\t" \
  "v_mul_f32 v44, v44, v50\n\t" \
  "v_mfma_f32_16x16x32_bf16 v[152:155], v[128:131], v[108:111], v[152:155]\n\t" \
  "ds_read_b64_tr_b16 v[150:151], v63 offset:39424\n\t" \
  "v_mul_f32 v48, v48, v51\n\t" \
  "v_mfma_f32_16x16x32_bf16 v[160:163], v[128:131], v[112:115], v[160:163]\n\t" \
  "v_cvt_pk_bf16_f32 v44, v44, v48\n\t" \
  "v_and_b32 v50, 0xffff0000, v39\n\t" \
  "v_mfma_f32_16x16x32_bf16 v[168:171], v[128:131], v[116:119], v[168:171]\n\t" \
  "v_and_b32 v51, 0xffff0000, v41\n\t" \
  "v_mul_f32 v45, v45, v50\n\t" \
  "v_mfma_f32_16x16x32_bf16 v[176:179], v[128:131], v[120:123], v[176:179]\n\t" \
  "ds_read_b64_tr_b16 v[128:129], v62 offset:22528\n\t" \
  "v_mul_f32 v49, v49, v51\n\t" \
  "v_mfma_f32_16x16x32_bf16 v[156:159], v[132:135], v[108:111], v[156:159]\n\t" \
  "ds_read_b128 v[108:111], v61 offset:2048\n\t" \
  "ds_read_b64_tr_b16 v[38:39], v1 offset:768\n\t" \
  "v_mfma_f32_16x16x32_bf16 v[164:167], v[132:135], v[112:115], v[164:167]\n\t" \
  "ds_read_b128 v[112:115], v61 offset:4096\n\t" \
  "ds_read_b64_tr_b16 v[40:41], v1 offset:256\n\t" \
  "v_mfma_f32_16x16x32_bf16 v[172:175], v[132:135], v[116:119], v[172:175]\n\t" \
  "ds_read_b128 v[116:119], v61 offset:6144\n\t" \
  "v_cvt_pk_bf16_f32 v45, v45, v49\n\t" \
  "v_mfma_f32_16x16x32_bf16 v[180:183], v[132:135], v[120:123], v[180:183]\n\t" \
  "ds_read_b128 v[120:123], v61 offset:8192\n\t" \
  "v_mfma_f32_16x16x32_bf16 v[248:251], v[92:95], v[80:83], v[248:251]\n\t" \
  "ds_read_b64_tr_b16 v[130:131], v62 offset:23040\n\t" \
  "v_mfma_f32_16x16x32_bf16 v[184:187], v[92:95], v[64:67], v[184:187]\n\t" \
  "ds_read_b64_tr_b16 v[132:133], v63 offset:22528\n\t" \
  "v_mfma_f32_16x16x32_bf16 v[192:195], v[92:95], v[68:71], v[192:195]\n\t" \
  "ds_read_b64_tr_b16 v[134:135], v63 offset:23040\n\t" \
  "v_mfma_f32_16x16x32_bf16 v[200:203], v[92:95], v[72:75], v[200:203]\n\t" \
  "v_mfma_f32_16x16x32_bf16 v[208:211], v[92:95], v[76:79], v[208:211]\n\t" \
  "v_mfma_f32_16x16x32_bf16 v[252:255], v[96:99], v[80:83], v[252:255]\n\t" \
  "v_mfma_f32_16x16x32_bf16 v[188:191], v[96:99], v[64:67], v[188:191]\n\t" \
  "v_mfma_f32_16x16x32_bf16 v[196:199], v[96:99], v[68:71], v[196:199]\n\t" \
  "v_mfma_f32_16x16x32_bf16 v[204:207], v[96:99], v[72:75], v[204:207]\n\t" \
  "v_mfma_f32_16x16x32_bf16 v[212:215], v[96:99], v[76:79], v[212:215]\n\t" \
  "global_store_dwordx4 v2, v[42:45], %24 offset:0 sc1\n\t" \
  "v_mfma_f32_16x16x32_bf16 v[216:219], v[64:67], v[100:103], v[216:219]\n\t" \
  "s_waitcnt lgkmcnt(0)\n\t" \
  "v_mfma_f32_16x16x32_bf16 v[42:45], v[26:29], v[18:21], 0\n\t" \
  "v_mfma_f32_16x16x32_bf16 v[42:45], v[30:33], v[22:25], v[42:45]\n\t" \
  "v_mfma_f32_16x16x32_bf16 v[46:49], v[30:33], v[18:21], 0\n\t" \
  "v_mfma_f32_16x16x32_bf16 v[46:49], v[34:37], v[22:25], v[46:49]\n\t" \
  "v_bfe_u32 v30, v4, 9, 1\n\t" \
  "v_mad_i32_i24 v30, v30, -16, v0\n\t" \
  "v_mfma_f32_16x16x32_bf16 v[224:227], v[68:71], v[100:103], v[224:227]\n\t" \
  "ds_read_b128 v[26:29], v0 offset:1584\n\t" \
  "ds_read_b128 v[30:33], v30 offset:1072\n\t" \
  "v_mfma_f32_16x16x32_bf16 v[232:235], v[72:75], v[100:103], v[232:235]\n\t" \
  "ds_read_b128 v[34:37], v0 offset:544\n\t" \
  "v_perm_b32 v18, v12, v12, v14\n\t" \
  "s_waitcnt lgkmcnt(0)\n\t" \
  "s_barrier\n\t" \
  "v_mfma_f32_16x16x32_bf16 v[240:243], v[76:79], v[100:103], v[240:243]\n\t" \
  "ds_read_b64_tr_b16 v[92:93], v54 offset:26624\n\t" \
  "v_perm_b32 v19, v12, v12, v15\n\t" \
  "v_mfma_f32_16x16x32_bf16 v[220:223], v[64:67], v[104:107], v[220:223]\n\t" \
  "ds_read_b64_tr_b16 v[94:95], v54 offset:27136\n\t" \
  "v_perm_b32 v20, v12, v12, v16\n\t" \
  "v_mfma_f32_16x16x32_bf16 v[228:231], v[68:71], v[104:107], v[228:231]\n\t" \
  "ds_read_b128 v[80:83], v52 offset:0\n\t" \
  "v_perm_b32 v21, v12, v12, v17\n\t" \
  "v_mfma_f32_16x16x32_bf16 v[236:239], v[72:75], v[104:107], v[236:239]\n\t" \
  "ds_read_b64_tr_b16 v[96:97], v55 offset:26624\n\t" \
  "v_perm_b32 v22, v13, v13, v14\n\t" \
  "v_mfma_f32_16x16x32_bf16 v[244:247], v[76:79], v[104:107], v[244:247]\n\t" \
  "ds_read_b64_tr_b16 v[98:99], v55 offset:27136\n\t" \
  "v_perm_b32 v23, v13, v13, v15\n\t" \
  "v_mfma_f32_16x16x32_bf16 v[152:155], v[84:87], v[64:67], v[152:155]\n\t" \
  "ds_read_b64_tr_b16 v[100:101], v54 offset:34816\n\t" \
  "v_perm_b32 v24, v13, v13, v16\n\t" \
  "v_mfma_f32_16x16x32_bf16 v[160:163], v[84:87], v[68:71], v[160:163]\n\t" \
  "ds_read_b64_tr_b16 v[102:103], v54 offset:35328\n\t" \
  "v_perm_b32 v25, v13, v13, v17\n\t" \
  "v_mfma_f32_16x16x32_bf16 v[168:171], v[84:87], v[72:75], v[168:171]\n\t" \
  "ds_read_b64_tr_b16 v[104:105], v55 offset:34816\n\t" \
  "v_lshlrev_b32 v50, 16, v38\n\t" \
  "v_mfma_f32_16x16x32_bf16 v[176:179], v[84:87], v[76:79], v[176:179]\n\t" \
  "ds_read_b64_tr_b16 v[106:107], v55 offset:35328\n\t" \
  "v_lshlrev_b32 v51, 16, v40\n\t" \
  "v_mfma_f32_16x16x32_bf16 v[156:159], v[88:91], v[64:67], v[156:159]\n\t" \
  "ds_read_b128 v[64:67], v52 offset:2048\n\t" \
  "v_mul_f32 v42, v42, v50\n\t" \
  "v_mfma_f32_16x16x32_bf16 v[164:167], v[88:91], v[68:71], v[164:167]\n\t" \
  "ds_read_b128 v[68:71], v52 offset:4096\n\t" \
  "v_mul_f32 v46, v46, v51\n\t" \
  "v_mfma_f32_16x16x32_bf16 v[172:175], v[88:91], v[72:75], v[172:175]\n\t" \
  "ds_read_b128 v[72:75], v52 offset:6144\n\t" \
  "v_cvt_pk_bf16_f32 v42, v42, v46\n\t" \
  "v_mfma_f32_16x16x32_bf16 v[180:183], v[88:91], v[76:79], v[180:183]\n\t" \
  "ds_read_b128 v[76:79], v52 offset:8192\n\t" \
  "v_and_b32 v50, 0xffff0000, v38\n\t" \
  "v_mfma_f32_16x16x32_bf16 v[248:251], v[136:139], v[124:127], v[248:251]\n\t" \
  "ds_read_b64_tr_b16 v[84:85], v54 offset:18432\n\t" \
  "v_and_b32 v51, 0xffff0000, v40\n\t" \
  "v_mfma_f32_16x16x32_bf16 v[184:187], v[136:139], v[108:111], v[184:187]\n\t" \
  "ds_read_b64_tr_b16 v[86:87], v54 offset:18944\n\t" \
  "v_mul_f32 v43, v43, v50\n\t" \
  "v_mfma_f32_16x16x32_bf16 v[192:195], v[136:139], v[112:115], v[192:195]\n\t" \
  "ds_read_b64_tr_b16 v[88:89], v55 offset:18432\n\t" \
  "v_mul_f32 v47, v47, v51\n\t" \
  "v_mfma_f32_16x16x32_bf16 v[200:203], v[136:139], v[116:119], v[200:203]\n\t" \
  "ds_read_b64_tr_b16 v[90:91], v55 offset:18944\n\t" \
  "v_cvt_pk_bf16_f32 v43, v43, v47\n\t" \
  "v_mfma_f32_16x16x32_bf16 v[208:211], v[136:139], v[120:123], v[208:211]\n\t" \
  "ds_read_b64_tr_b16 v[136:137], v54 offset:30720\n\t" \
  "v_lshlrev_b32 v50, 16, v39\n\t" \
  "v_mfma_f32_16x16x32_bf16 v[252:255], v[140:143], v[124:127], v[252:255]\n\t" \
  "ds_read_b64_tr_b16 v[138:139], v54 offset:31232\n\t" \
  "v_lshlrev_b32 v51, 16, v41\n\t" \
  "v_mfma_f32_16x16x32_bf16 v[188:191], v[140:143], v[108:111], v[188:191]\n\t" \
  "ds_read_b128 v[124:127], v53 offset:0\n\t" \
  "v_mul_f32 v44, v44, v50\n\t" \
  "v_mfma_f32_16x16x32_bf16 v[196:199], v[140:143], v[112:115], v[196:199]\n\t" \
  "v_mul_f32 v48, v48, v51\n\t" \
  "v_cvt_pk_bf16_f32 v44, v44, v48\n\t" \
  "v_mfma_f32_16x16x32_bf16 v[204:207], v[140:143], v[116:119], v[204:207]\n\t" \
  "v_and_b32 v50, 0xffff0000, v39\n\t" \
  "v_and_b32 v51, 0xffff0000, v41\n\t" \
  "v_mfma_f32_16x16x32_bf16 v[212:215], v[140:143], v[120:123], v[212:215]\n\t" \
  "ds_read_b64_tr_b16 v[140:141], v55 offset:30720\n\t" \
  "v_mul_f32 v45, v45, v50\n\t" \
  "v_mfma_f32_16x16x32_bf16 v[216:219], v[108:111], v[144:147], v[216:219]\n\t" \
  "ds_read_b64_tr_b16 v[142:143], v55 offset:31232\n\t" \
  "v_mul_f32 v49, v49, v51\n\t" \
  "v_mfma_f32_16x16x32_bf16 v[224:227], v[112:115], v[144:147], v[224:227]\n\t" \
  "ds_read_b64_tr_b16 v[38:39], v1 offset:544\n\t" \
  "ds_read_b64_tr_b16 v[40:41], v1 offset:32\n\t" \
  "v_mfma_f32_16x16x32_bf16 v[232:235], v[116:119], v[144:147], v[232:235]\n\t" \
  "v_cvt_pk_bf16_f32 v45, v45, v49\n\t" \
  "v_mfma_f32_16x16x32_bf16 v[240:243], v[120:123], v[144:147], v[240:243]\n\t" \
  "ds_read_b64_tr_b16 v[144:145], v54 offset:38912\n\t" \
  "v_mfma_f32_16x16x32_bf16 v[220:223], v[108:111], v[148:151], v[220:223]\n\t" \
  "ds_read_b64_tr_b16 v[146:147], v54 offset:39424\n\t" \
  "v_mfma_f32_16x16x32_bf16 v[228:231], v[112:115], v[148:151], v[228:231]\n\t" \
  "v_mfma_f32_16x16x32_bf16 v[236:239], v[116:119], v[148:151], v[236:239]\n\t" \
  "v_mfma_f32_16x16x32_bf16 v[244:247], v[120:123], v[148:151], v[244:247]\n\t" \
  "ds_read_b64_tr_b16 v[148:149], v55 offset:38912\n\t" \
  "v_mfma_f32_16x16x32_bf16 v[152:155], v[128:131], v[108:111], v[152:155]\n\t" \
  "ds_read_b64_tr_b16 v[150:151], v55 offset:39424\n\t" \
  "v_mfma_f32_16x16x32_bf16 v[160:163], v[128:131], v[112:115], v[160:163]\n\t" \
  "v_mfma_f32_16x16x32_bf16 v[168:171], v[128:131], v[116:119], v[168:171]\n\t" \
  "v_mfma_f32_16x16x32_bf16 v[176:179], v[128:131], v[120:123], v[176:179]\n\t" \
  "ds_read_b64_tr_b16 v[128:129], v54 offset:22528\n\t" \
  "v_mfma_f32_16x16x32_bf16 v[156:159], v[132:135], v[108:111], v[156:159]\n\t" \
  "ds_read_b128 v[108:111], v53 offset:2048\n\t" \
  "v_mfma_f32_16x16x32_bf16 v[164:167], v[132:135], v[112:115], v[164:167]\n\t" \
  "ds_read_b128 v[112:115], v53 offset:4096\n\t" \
  "v_mfma_f32_16x16x32_bf16 v[172:175], v[132:135], v[116:119], v[172:175]\n\t" \
  "ds_read_b128 v[116:119], v53 offset:6144\n\t" \
  "v_mfma_f32_16x16x32_bf16 v[180:183], v[132:135], v[120:123], v[180:183]\n\t" \
  "ds_read_b128 v[120:123], v53 offset:8192\n\t" \
  "s_waitcnt lgkmcnt(5)\n\t" \
  "v_mfma_f32_16x16x32_bf16 v[248:251], v[92:95], v[80:83], v[248:251]\n\t" \
  "ds_read_b64_tr_b16 v[130:131], v54 offset:23040\n\t" \
  "v_mfma_f32_16x16x32_bf16 v[184:187], v[92:95], v[64:67], v[184:187]\n\t" \
  "ds_read_b64_tr_b16 v[132:133], v55 offset:22528\n\t" \
  "v_mfma_f32_16x16x32_bf16 v[192:195], v[92:95], v[68:71], v[192:195]\n\t" \
  "ds_read_b64_tr_b16 v[134:135], v55 offset:23040\n\t" \
  "v_mfma_f32_16x16x32_bf16 v[200:203], v[92:95], v[72:75], v[200:203]\n\t" \
  "v_mfma_f32_16x16x32_bf16 v[208:211], v[92:95], v[76:79], v[208:211]\n\t" \
  "v_mfma_f32_16x16x32_bf16 v[252:255], v[96:99], v[80:83], v[252:255]\n\t" \
  "v_mfma_f32_16x16x32_bf16 v[188:191], v[96:99], v[64:67], v[188:191]\n\t" \
  "v_mfma_f32_16x16x32_bf16 v[196:199], v[96:99], v[68:71], v[196:199]\n\t" \
  "v_mfma_f32_16x16x32_bf16 v[204:207], v[96:99], v[72:75], v[204:207]\n\t" \
  "v_mfma_f32_16x16x32_bf16 v[212:215], v[96:99], v[76:79], v[212:215]\n\t" \
  "global_store_dwordx4 v2, v[42:45], %24 offset:1024 sc1\n\t" \
  "v_add_u32 v2, 0x10000, v2\n\t" \
  "v_mfma_f32_16x16x32_bf16 v[216:219], v[64:67], v[100:103], v[216:219]\n\t" \
  "v_mfma_f32_16x16x32_bf16 v[224:227], v[68:71], v[100:103], v[224:227]\n\t" \
  "v_mfma_f32_16x16x32_bf16 v[42:45], v[26:29], v[18:21], 0\n\t" \
  "v_mfma_f32_16x16x32_bf16 v[42:45], v[30:33], v[22:25], v[42:45]\n\t" \
  "v_mfma_f32_16x16x32_bf16 v[46:49], v[30:33], v[18:21], 0\n\t" \
  "v_mfma_f32_16x16x32_bf16 v[46:49], v[34:37], v[22:25], v[46:49]\n\t" \
  "ds_read_b128 v[26:29], v0 offset:1840\n\t" \
  "ds_read_b128 v[30:33], v0 offset:1328\n\t" \
  "v_mfma_f32_16x16x32_bf16 v[232:235], v[72:75], v[100:103], v[232:235]\n\t" \
  "ds_read_b128 v[34:37], v0 offset:800\n\t" \
  "s_waitcnt lgkmcnt(0)\n\t" \
  "s_barrier\n\t" \
  "v_mfma_f32_16x16x32_bf16 v[240:243], v[76:79], v[100:103], v[240:243]\n\t" \
  "ds_read_b64_tr_b16 v[92:93], v58 offset:26624\n\t" \
  "v_mfma_f32_16x16x32_bf16 v[220:223], v[64:67], v[104:107], v[220:223]\n\t" \
  "ds_read_b64_tr_b16 v[94:95], v58 offset:27136\n\t" \
  "v_lshlrev_b32 v50, 16, v38\n\t" \
  "v_mfma_f32_16x16x32_bf16 v[228:231], v[68:71], v[104:107], v[228:231]\n\t" \
  "ds_read_b128 v[80:83], v56 offset:0\n\t" \
  "v_lshlrev_b32 v51, 16, v40\n\t" \
  "v_mfma_f32_16x16x32_bf16 v[236:239], v[72:75], v[104:107], v[236:239]\n\t" \
  "ds_read_b64_tr_b16 v[96:97], v59 offset:26624\n\t" \
  "v_mul_f32 v42, v42, v50\n\t" \
  "v_mfma_f32_16x16x32_bf16 v[244:247], v[76:79], v[104:107], v[244:247]\n\t" \
  "ds_read_b64_tr_b16 v[98:99], v59 offset:27136\n\t" \
  "v_mul_f32 v46, v46, v51\n\t" \
  "v_mfma_f32_16x16x32_bf16 v[152:155], v[84:87], v[64:67], v[152:155]\n\t" \
  "ds_read_b64_tr_b16 v[100:101], v58 offset:34816\n\t" \
  "v_cvt_pk_bf16_f32 v42, v42, v46\n\t" \
  "v_mfma_f32_16x16x32_bf16 v[160:163], v[84:87], v[68:71], v[160:163]\n\t" \
  "ds_read_b64_tr_b16 v[102:103], v58 offset:35328\n\t" \
  "v_and_b32 v50, 0xffff0000, v38\n\t" \
  "v_mfma_f32_16x16x32_bf16 v[168:171], v[84:87], v[72:75], v[168:171]\n\t" \
  "ds_read_b64_tr_b16 v[104:105], v59 offset:34816\n\t" \
  "v_and_b32 v51, 0xffff0000, v40\n\t" \
  "v_mfma_f32_16x16x32_bf16 v[176:179], v[84:87], v[76:79], v[176:179]\n\t" \
  "ds_read_b64_tr_b16 v[106:107], v59 offset:35328\n\t" \
  "v_mul_f32 v43, v43, v50\n\t" \
  "v_mfma_f32_16x16x32_bf16 v[156:159], v[88:91], v[64:67], v[156:159]\n\t" \
  "ds_read_b128 v[64:67], v56 offset:2048\n\t" \
  "v_mul_f32 v47, v47, v51\n\t" \
  "v_mfma_f32_16x16x32_bf16 v[164:167], v[88:91], v[68:71], v[164:167]\n\t" \
  "ds_read_b128 v[68:71], v56 offset:4096\n\t" \
  "v_cvt_pk_bf16_f32 v43, v43, v47\n\t" \
  "v_mfma_f32_16x16x32_bf16 v[172:175], v[88:91], v[72:75], v[172:175]\n\t" \
  "ds_read_b128 v[72:75], v56 offset:6144\n\t" \
  "v_lshlrev_b32 v50, 16, v39\n\t" \
  "v_mfma_f32_16x16x32_bf16 v[180:183], v[88:91], v[76:79], v[180:183]\n\t" \
  "ds_read_b128 v[76:79], v56 offset:8192\n\t" \
  "v_lshlrev_b32 v51, 16, v41\n\t" \
  "v_mfma_f32_16x16x32_bf16 v[248:251], v[136:139], v[124:127], v[248:251]\n\t" \
  "ds_read_b64_tr_b16 v[84:85], v58 offset:18432\n\t" \
  "v_mul_f32 v44, v44, v50\n\t" \
  "v_mfma_f32_16x16x32_bf16 v[184:187], v[136:139], v[108:111], v[184:187]\n\t" \
  "ds_read_b64_tr_b16 v[86:87], v58 offset:18944\n\t" \
  "v_mul_f32 v48, v48, v51\n\t" \
  "v_mfma_f32_16x16x32_bf16 v[192:195], v[136:139], v[112:115], v[192:195]\n\t" \
  "ds_read_b64_tr_b16 v[88:89], v59 offset:18432\n\t" \
  "v_cvt_pk_bf16_f32 v44, v44, v48\n\t" \
  "v_mfma_f32_16x16x32_bf16 v[200:203], v[136:139], v[116:119], v[200:203]\n\t" \
  "ds_read_b64_tr_b16 v[90:91], v59 offset:18944\n\t" \
  "v_and_b32 v50, 0xffff0000, v39\n\t" \
  "v_mfma_f32_16x16x32_bf16 v[208:211], v[136:139], v[120:123], v[208:211]\n\t" \
  "ds_read_b64_tr_b16 v[136:137], v58 offset:30720\n\t" \
  "v_and_b32 v51, 0xffff0000, v41\n\t" \
  "v_mfma_f32_16x16x32_bf16 v[252:255], v[140:143], v[124:127], v[252:255]\n\t" \
  "ds_read_b64_tr_b16 v[138:139], v58 offset:31232\n\t" \
  "v_mul_f32 v45, v45, v50\n\t" \
  "v_mfma_f32_16x16x32_bf16 v[188:191], v[140:143], v[108:111], v[188:191]\n\t" \
  "ds_read_b128 v[124:127], v57 offset:0\n\t" \
  "v_mul_f32 v49, v49, v51\n\t" \
  "v_mfma_f32_16x16x32_bf16 v[196:199], v[140:143], v[112:115], v[196:199]\n\t" \
  "ds_read_b64_tr_b16 v[38:39], v1 offset:800\n\t" \
  "ds_read_b64_tr_b16 v[40:41], v1 offset:288\n\t" \
  "v_mfma_f32_16x16x32_bf16 v[204:207], v[140:143], v[116:119], v[204:207]\n\t" \
  "v_cvt_pk_bf16_f32 v45, v45, v49\n\t" \
  "v_mfma_f32_16x16x32_bf16 v[212:215], v[140:143], v[120:123], v[212:215]\n\t" \
  "ds_read_b64_tr_b16 v[140:141], v59 offset:30720\n\t" \
  "v_mfma_f32_16x16x32_bf16 v[216:219], v[108:111], v[144:147], v[216:219]\n\t" \
  "ds_read_b64_tr_b16 v[142:143], v59 offset:31232\n\t" \
  "v_mfma_f32_16x16x32_bf16 v[224:227], v[112:115], v[144:147], v[224:227]\n\t" \
  "v_mfma_f32_16x16x32_bf16 v[232:235], v[116:119], v[144:147], v[232:235]\n\t" \
  "v_mfma_f32_16x16x32_bf16 v[240:243], v[120:123], v[144:147], v[240:243]\n\t" \
  "ds_read_b64_tr_b16 v[144:145], v58 offset:38912\n\t" \
  "v_mfma_f32_16x16x32_bf16 v[220:223], v[108:111], v[148:151], v[220:223]\n\t" \
  "ds_read_b64_tr_b16 v[146:147], v58 offset:39424\n\t" \
  "v_mfma_f32_16x16x32_bf16 v[228:231], v[112:115], v[148:151], v[228:231]\n\t" \
  "v_mfma_f32_16x16x32_bf16 v[236:239], v[116:119], v[148:151], v[236:239]\n\t" \
  "v_mfma_f32_16x16x32_bf16 v[244:247], v[120:123], v[148:151], v[244:247]\n\t" \
  "ds_read_b64_tr_b16 v[148:149], v59 offset:38912\n\t" \
  "v_mfma_f32_16x16x32_bf16 v[152:155], v[128:131], v[108:111], v[152:155]\n\t" \
  "ds_read_b64_tr_b16 v[150:151], v59 offset:39424\n\t" \
  "v_mfma_f32_16x16x32_bf16 v[160:163], v[128:131], v[112:115], v[160:163]\n\t" \
  "v_mfma_f32_16x16x32_bf16 v[168:171], v[128:131], v[116:119], v[168:171]\n\t" \
  "v_mfma_f32_16x16x32_bf16 v[176:179], v[128:131], v[120:123], v[176:179]\n\t" \
  "ds_read_b64_tr_b16 v[128:129], v58 offset:22528\n\t" \
  "v_mfma_f32_16x16x32_bf16 v[156:159], v[132:135], v[108:111], v[156:159]\n\t" \
  "ds_read_b128 v[108:111], v57 offset:2048\n\t" \
  "v_mfma_f32_16x16x32_bf16 v[164:167], v[132:135], v[112:115], v[164:167]\n\t" \
  "ds_read_b128 v[112:115], v57 offset:4096\n\t" \
  "v_mfma_f32_16x16x32_bf16 v[172:175], v[132:135], v[116:119], v[172:175]\n\t" \
  "ds_read_b128 v[116:119], v57 offset:6144\n\t" \
  "v_mfma_f32_16x16x32_bf16 v[180:183], v[132:135], v[120:123], v[180:183]\n\t" \
  "ds_read_b128 v[120:123], v57 offset:8192\n\t" \
  "s_waitcnt lgkmcnt(5)\n\t" \
  "v_mfma_f32_16x16x32_bf16 v[248:251], v[92:95], v[80:83], v[248:251]\n\t" \
  "ds_read_b64_tr_b16 v[130:131], v58 offset:23040\n\t" \
  "v_mfma_f32_16x16x32_bf16 v[184:187], v[92:95], v[64:67], v[184:187]\n\t" \
  "ds_read_b64_tr_b16 v[132:133], v59 offset:22528\n\t" \
  "v_mfma_f32_16x16x32_bf16 v[192:195], v[92:95], v[68:71], v[192:195]\n\t" \
  "ds_read_b64_tr_b16 v[134:135], v59 offset:23040\n\t" \
  "v_mfma_f32_16x16x32_bf16 v[200:203], v[92:95], v[72:75], v[200:203]\n\t" \
  "v_mfma_f32_16x16x32_bf16 v[208:211], v[92:95], v[76:79], v[208:211]\n\t" \
  "v_mfma_f32_16x16x32_bf16 v[252:255], v[96:99], v[80:83], v[252:255]\n\t" \
  "v_mfma_f32_16x16x32_bf16 v[188:191], v[96:99], v[64:67], v[188:191]\n\t" \
  "v_mfma_f32_16x16x32_bf16 v[196:199], v[96:99], v[68:71], v[196:199]\n\t" \
  "v_mfma_f32_16x16x32_bf16 v[204:207], v[96:99], v[72:75], v[204:207]\n\t" \
  "v_mfma_f32_16x16x32_bf16 v[212:215], v[96:99], v[76:79], v[212:215]\n\t" \
  "global_store_dwordx4 v2, v[42:45], %24 offset:0 sc1\n\t" \
  "v_mfma_f32_16x16x32_bf16 v[216:219], v[64:67], v[100:103], v[216:219]\n\t" \
  "s_nop 0\n\t" \
  "v_mfma_f32_16x16x32_bf16 v[42:45], v[26:29], v[18:21], 0\n\t" \
  "v_mfma_f32_16x16x32_bf16 v[42:45], v[30:33], v[22:25], v[42:45]\n\t" \
  "v_mfma_f32_16x16x32_bf16 v[46:49], v[30:33], v[18:21], 0\n\t" \
  "v_mfma_f32_16x16x32_bf16 v[46:49], v[34:37], v[22:25], v[46:49]\n\t" \
  "v_mfma_f32_16x16x32_bf16 v[224:227], v[68:71], v[100:103], v[224:227]\n\t" \
  "v_mfma_f32_16x16x32_bf16 v[232:235], v[72:75], v[100:103], v[232:235]\n\t" \
  "v_lshlrev_b32 v50, 16, v38\n\t" \
  "v_lshlrev_b32 v51, 16, v40\n\t" \
  "s_waitcnt lgkmcnt(0)\n\t" \
  "s_barrier\n\t" \
  "v_mfma_f32_16x16x32_bf16 v[240:243], v[76:79], v[100:103], v[240:243]\n\t" \
  "ds_read_b64_tr_b16 v[92:93], v62 offset:26624\n\t" \
  "v_mul_f32 v42, v42, v50\n\t" \
  "v_mfma_f32_16x16x32_bf16 v[220:223], v[64:67], v[104:107], v[220:223]\n\t" \
  "ds_read_b64_tr_b16 v[94:95], v62 offset:27136\n\t" \
  "v_mul_f32 v46, v46, v51\n\t" \
  "v_mfma_f32_16x16x32_bf16 v[228:231], v[68:71], v[104:107], v[228:231]\n\t" \
  "ds_read_b128 v[80:83], v60 offset:0\n\t" \
  "v_cvt_pk_bf16_f32 v42, v42, v46\n\t" \
  "v_mfma_f32_16x16x32_bf16 v[236:239], v[72:75], v[104:107], v[236:239]\n\t" \
  "ds_read_b64_tr_b16 v[96:97], v63 offset:26624\n\t" \
  "v_and_b32 v50, 0xffff0000, v38\n\t" \
  "v_mfma_f32_16x16x32_bf16 v[244:247], v[76:79], v[104:107], v[244:247]\n\t" \
  "ds_read_b64_tr_b16 v[98:99], v63 offset:27136\n\t" \
  "v_and_b32 v51, 0xffff0000, v40\n\t" \
  "v_mfma_f32_16x16x32_bf16 v[152:155], v[84:87], v[64:67], v[152:155]\n\t" \
  "ds_read_b64_tr_b16 v[100:101], v62 offset:34816\n\t" \
  "v_mul_f32 v43, v43, v50\n\t" \
  "v_mfma_f32_16x16x32_bf16 v[160:163], v[84:87], v[68:71], v[160:163]\n\t" \
  "ds_read_b64_tr_b16 v[102:103], v62 offset:35328\n\t" \
  "v_mul_f32 v47, v47, v51\n\t" \
  "v_mfma_f32_16x16x32_bf16 v[168:171], v[84:87], v[72:75], v[168:171]\n\t" \
  "ds_read_b64_tr_b16 v[104:105], v63 offset:34816\n\t" \
  "v_cvt_pk_bf16_f32 v43, v43, v47\n\t" \
  "v_mfma_f32_16x16x32_bf16 v[176:179], v[84:87], v[76:79], v[176:179]\n\t" \
  "ds_read_b64_tr_b16 v[106:107], v63 offset:35328\n\t" \
  "v_lshlrev_b32 v50, 16, v39\n\t" \
  "v_mfma_f32_16x16x32_bf16 v[156:159], v[88:91], v[64:67], v[156:159]\n\t" \
  "ds_read_b128 v[64:67], v60 offset:2048\n\t" \
  "v_lshlrev_b32 v51, 16, v41\n\t" \
  "v_mfma_f32_16x16x32_bf16 v[164:167], v[88:91], v[68:71], v[164:167]\n\t" \
  "ds_read_b128 v[68:71], v60 offset:4096\n\t" \
  "v_mul_f32 v44, v44, v50\n\t" \
  "v_mfma_f32_16x16x32_bf16 v[172:175], v[88:91], v[72:75], v[172:175]\n\t" \
  "ds_read_b128 v[72:75], v60 offset:6144\n\t" \
  "v_mul_f32 v48, v48, v51\n\t" \
  "v_mfma_f32_16x16x32_bf16 v[180:183], v[88:91], v[76:79], v[180:183]\n\t" \
  "ds_read_b128 v[76:79], v60 offset:8192\n\t" \
  "v_cvt_pk_bf16_f32 v44, v44, v48\n\t" \
  "v_mfma_f32_16x16x32_bf16 v[248:251], v[136:139], v[124:127], v[248:251]\n\t" \
  "ds_read_b64_tr_b16 v[84:85], v62 offset:18432\n\t" \
  "v_and_b32 v50, 0xffff0000, v39\n\t" \
  "v_mfma_f32_16x16x32_bf16 v[184:187], v[136:139], v[108:111], v[184:187]\n\t" \
  "ds_read_b64_tr_b16 v[86:87], v62 offset:18944\n\t" \
  "v_and_b32 v51, 0xffff0000, v41\n\t" \
  "v_mfma_f32_16x16x32_bf16 v[192:195], v[136:139], v[112:115], v[192:195]\n\t" \
  "ds_read_b64_tr_b16 v[88:89], v63 offset:18432\n\t" \
  "v_mul_f32 v45, v45, v50\n\t" \
  "v_mfma_f32_16x16x32_bf16 v[200:203], v[136:139], v[116:119], v[200:203]\n\t" \
  "ds_read_b64_tr_b16 v[90:91], v63 offset:18944\n\t" \
  "v_mul_f32 v49, v49, v51\n\t" \
  "v_mfma_f32_16x16x32_bf16 v[208:211], v[136:139], v[120:123], v[208:211]\n\t" \
  "ds_read_b64_tr_b16 v[136:137], v62 offset:30720\n\t" \
  "v_cvt_pk_bf16_f32 v45, v45, v49\n\t" \
  "v_mfma_f32_16x16x32_bf16 v[252:255], v[140:143], v[124:127], v[252:255]\n\t" \
  "ds_read_b64_tr_b16 v[138:139], v62 offset:31232\n\t" \
  "v_mfma_f32_16x16x32_bf16 v[188:191], v[140:143], v[108:111], v[188:191]\n\t" \
  "ds_read_b128 v[124:127], v61 offset:0\n\t" \
  "v_mfma_f32_16x16x32_bf16 v[196:199], v[140:143], v[112:115], v[196:199]\n\t" \
  "v_mfma_f32_16x16x32_bf16 v[204:207], v[140:143], v[116:119], v[204:207]\n\t" \
  "v_mfma_f32_16x16x32_bf16 v[212:215], v[140:143], v[120:123], v[212:215]\n\t" \
  "ds_read_b64_tr_b16 v[140:141], v63 offset:30720\n\t" \
  "v_mfma_f32_16x16x32_bf16 v[216:219], v[108:111], v[144:147], v[216:219]\n\t" \
  "ds_read_b64_tr_b16 v[142:143], v63 offset:31232\n\t" \
  "v_mfma_f32_16x16x32_bf16 v[224:227], v[112:115], v[144:147], v[224:227]\n\t" \
  "v_mfma_f32_16x16x32_bf16 v[232:235], v[116:119], v[144:147], v[232:235]\n\t" \
  "v_mfma_f32_16x16x32_bf16 v[240:243], v[120:123], v[144:147], v[240:243]\n\t" \
  "ds_read_b64_tr_b16 v[144:145], v62 offset:38912\n\t" \
  "v_mfma_f32_16x16x32_bf16 v[220:223], v[108:111], v[148:151], v[220:223]\n\t" \
  "ds_read_b64_tr_b16 v[146:147], v62 offset:39424\n\t" \
  "v_mfma_f32_16x16x32_bf16 v[228:231], v[112:115], v[148:151], v[228:231]\n\t" \
  "v_mfma_f32_16x16x32_bf16 v[236:239], v[116:119], v[148:151], v[236:239]\n\t" \
  "v_mfma_f32_16x16x32_bf16 v[244:247], v[120:123], v[148:151], v[244:247]\n\t" \
  "ds_read_b64_tr_b16 v[148:149], v63 offset:38912\n\t" \
  "v_mfma_f32_16x16x32_bf16 v[152:155], v[128:131], v[108:111], v[152:155]\n\t" \
  "ds_read_b64_tr_b16 v[150:151], v63 offset:39424\n\t" \
  "v_mfma_f32_16x16x32_bf16 v[160:163], v[128:131], v[112:115], v[160:163]\n\t" \
  "v_mfma_f32_16x16x32_bf16 v[168:171], v[128:131], v[116:119], v[168:171]\n\t" \
  "v_mfma_f32_16x16x32_bf16 v[176:179], v[128:131], v[120:123], v[176:179]\n\t" \
  "ds_read_b64_tr_b16 v[128:129], v62 offset:22528\n\t" \
  "v_mfma_f32_16x16x32_bf16 v[156:159], v[132:135], v[108:111], v[156:159]\n\t" \
  "ds_read_b128 v[108:111], v61 offset:2048\n\t" \
  "v_mfma_f32_16x16x32_bf16 v[164:167], v[132:135], v[112:115], v[164:167]\n\t" \
  "ds_read_b128 v[112:115], v61 offset:4096\n\t" \
  "v_mfma_f32_16x16x32_bf16 v[172:175], v[132:135], v[116:119], v[172:175]\n\t" \
  "ds_read_b128 v[116:119], v61 offset:6144\n\t" \
  "v_mfma_f32_16x16x32_bf16 v[180:183], v[132:135], v[120:123], v[180:183]\n\t" \
  "ds_read_b128 v[120:123], v61 offset:8192\n\t" \
  "s_waitcnt lgkmcnt(5)\n\t" \
  "v_mfma_f32_16x16x32_bf16 v[248:251], v[92:95], v[80:83], v[248:251]\n\t" \
  "ds_read_b64_tr_b16 v[130:131], v62 offset:23040\n\t" \
  "v_mfma_f32_16x16x32_bf16 v[184:187], v[92:95], v[64:67], v[184:187]\n\t" \
  "ds_read_b64_tr_b16 v[132:133], v63 offset:22528\n\t" \
  "v_mfma_f32_16x16x32_bf16 v[192:195], v[92:95], v[68:71], v[192:195]\n\t" \
  "ds_read_b64_tr_b16 v[134:135], v63 offset:23040\n\t" \
  "v_mfma_f32_16x16x32_bf16 v[200:203], v[92:95], v[72:75], v[200:203]\n\t" \
  "v_mfma_f32_16x16x32_bf16 v[208:211], v[92:95], v[76:79], v[208:211]\n\t" \
  "v_mfma_f32_16x16x32_bf16 v[252:255], v[96:99], v[80:83], v[252:255]\n\t" \
  "v_mfma_f32_16x16x32_bf16 v[188:191], v[96:99], v[64:67], v[188:191]\n\t" \
  "v_mfma_f32_16x16x32_bf16 v[196:199], v[96:99], v[68:71], v[196:199]\n\t" \
  "v_mfma_f32_16x16x32_bf16 v[204:207], v[96:99], v[72:75], v[204:207]\n\t" \
  "v_mfma_f32_16x16x32_bf16 v[212:215], v[96:99], v[76:79], v[212:215]\n\t" \
  "global_store_dwordx4 v2, v[42:45], %24 offset:1024 sc1\n\t" \
  "v_mfma_f32_16x16x32_bf16 v[216:219], v[64:67], v[100:103], v[216:219]\n\t" \
  "v_mfma_f32_16x16x32_bf16 v[224:227], v[68:71], v[100:103], v[224:227]\n\t" \
  "v_mfma_f32_16x16x32_bf16 v[232:235], v[72:75], v[100:103], v[232:235]\n\t" \
  "v_mov_b32 v30, 0xff61b1e6\n\t" \
  "v_bfe_i32 v34, v3, 0, 1\n\t" \
  "s_waitcnt lgkmcnt(0)\n\t" \
  "s_barrier\n\t" \
  "v_mfma_f32_16x16x32_bf16 v[240:243], v[76:79], v[100:103], v[240:243]\n\t" \
  "ds_read_b64_tr_b16 v[92:93], v54 offset:26624\n\t" \
  "v_and_b32 v34, v30, v34\n\t" \
  "v_mfma_f32_16x16x32_bf16 v[220:223], v[64:67], v[104:107], v[220:223]\n\t" \
  "ds_read_b64_tr_b16 v[94:95], v54 offset:27136\n\t" \
  "v_bfe_i32 v38, v3, 4, 1\n\t" \
  "v_mfma_f32_16x16x32_bf16 v[228:231], v[68:71], v[104:107], v[228:231]\n\t" \
  "ds_read_b128 v[80:83], v52 offset:0\n\t" \
  "v_and_b32 v38, v30, v38\n\t" \
  "v_mfma_f32_16x16x32_bf16 v[236:239], v[72:75], v[104:107], v[236:239]\n\t" \
  "ds_read_b64_tr_b16 v[96:97], v55 offset:26624\n\t" \
  "v_bfe_i32 v42, v3, 8, 1\n\t" \
  "v_mfma_f32_16x16x32_bf16 v[244:247], v[76:79], v[104:107], v[244:247]\n\t" \
  "ds_read_b64_tr_b16 v[98:99], v55 offset:27136\n\t" \
  "v_bfi_b32 v42, v42, v30, %15\n\t" \
  "v_mfma_f32_16x16x32_bf16 v[152:155], v[84:87], v[64:67], v[152:155]\n\t" \
  "ds_read_b64_tr_b16 v[100:101], v54 offset:34816\n\t" \
  "v_mov_b32 v48, 0x3f803f80\n\t" \
  "v_mfma_f32_16x16x32_bf16 v[160:163], v[84:87], v[68:71], v[160:163]\n\t" \
  "ds_read_b64_tr_b16 v[102:103], v54 offset:35328\n\t" \
  "v_bfe_i32 v35, v3, 1, 1\n\t" \
  "v_mfma_f32_16x16x32_bf16 v[168:171], v[84:87], v[72:75], v[168:171]\n\t" \
  "ds_read_b64_tr_b16 v[104:105], v55 offset:34816\n\t" \
  "v_and_b32 v35, v30, v35\n\t" \
  "v_mfma_f32_16x16x32_bf16 v[176:179], v[84:87], v[76:79], v[176:179]\n\t" \
  "ds_read_b64_tr_b16 v[106:107], v55 offset:35328\n\t" \
  "v_bfe_i32 v39, v3, 5, 1\n\t" \
  "v_mfma_f32_16x16x32_bf16 v[156:159], v[88:91], v[64:67], v[156:159]\n\t" \
  "ds_read_b128 v[64:67], v52 offset:2048\n\t" \
  "v_and_b32 v39, v30, v39\n\t" \
  "v_mfma_f32_16x16x32_bf16 v[164:167], v[88:91], v[68:71], v[164:167]\n\t" \
  "ds_read_b128 v[68:71], v52 offset:4096\n\t" \
  "v_bfe_i32 v43, v3, 9, 1\n\t" \
  "v_mfma_f32_16x16x32_bf16 v[172:175], v[88:91], v[72:75], v[172:175]\n\t" \
  "ds_read_b128 v[72:75], v52 offset:6144\n\t" \
  "v_bfi_b32 v43, v43, v30, %15\n\t" \
  "v_mfma_f32_16x16x32_bf16 v[180:183], v[88:91], v[76:79], v[180:183]\n\t" \
  "ds_read_b128 v[76:79], v52 offset:8192\n\t" \
  "v_mov_b32 v49, 0x3f803f80\n\t" \
  "v_mfma_f32_16x16x32_bf16 v[248:251], v[136:139], v[124:127], v[248:251]\n\t" \
  "ds_read_b64_tr_b16 v[84:85], v54 offset:18432\n\t" \
  "v_bfe_i32 v36, v3, 2, 1\n\t" \
  "v_mfma_f32_16x16x32_bf16 v[184:187], v[136:139], v[108:111], v[184:187]\n\t" \
  "ds_read_b64_tr_b16 v[86:87], v54 offset:18944\n\t" \
  "v_and_b32 v36, v30, v36\n\t" \
  "v_mfma_f32_16x16x32_bf16 v[192:195], v[136:139], v[112:115], v[192:195]\n\t" \
  "ds_read_b64_tr_b16 v[88:89], v55 offset:18432\n\t" \
  "v_bfe_i32 v40, v3, 6, 1\n\t" \
  "v_mfma_f32_16x16x32_bf16 v[200:203], v[136:139], v[116:119], v[200:203]\n\t" \
  "ds_read_b64_tr_b16 v[90:91], v55 offset:18944\n\t" \
  "v_and_b32 v40, v30, v40\n\t" \
  "v_mfma_f32_16x16x32_bf16 v[208:211], v[136:139], v[120:123], v[208:211]\n\t" \
  "ds_read_b64_tr_b16 v[136:137], v54 offset:30720\n\t" \
  "v_bfe_i32 v44, v3, 10, 1\n\t" \
  "v_mfma_f32_16x16x32_bf16 v[252:255], v[140:143], v[124:127], v[252:255]\n\t" \
  "ds_read_b64_tr_b16 v[138:139], v54 offset:31232\n\t" \
  "v_bfi_b32 v44, v44, v30, %15\n\t" \
  "v_mfma_f32_16x16x32_bf16 v[188:191], v[140:143], v[108:111], v[188:191]\n\t" \
  "ds_read_b128 v[124:127], v53 offset:0\n\t" \
  "v_mov_b32 v50, 0x3f803f80\n\t" \
  "v_mfma_f32_16x16x32_bf16 v[196:199], v[140:143], v[112:115], v[196:199]\n\t" \
  "v_bfe_i32 v37, v3, 3, 1\n\t" \
  "v_and_b32 v37, v30, v37\n\t" \
  "v_mfma_f32_16x16x32_bf16 v[204:207], v[140:143], v[116:119], v[204:207]\n\t" \
  "v_bfe_i32 v41, v3, 7, 1\n\t" \
  "v_and_b32 v41, v30, v41\n\t" \
  "v_mfma_f32_16x16x32_bf16 v[212:215], v[140:143], v[120:123], v[212:215]\n\t" \
  "504:\n\t" \
  "s_cbranch_execz 508f\n\t" \
  "ds_read_b64_tr_b16 v[140:141], v55 offset:30720\n\t" \
  "v_bfe_i32 v45, v3, 11, 1\n\t" \
  "v_mfma_f32_16x16x32_bf16 v[216:219], v[108:111], v[144:147], v[216:219]\n\t" \
  "ds_read_b64_tr_b16 v[142:143], v55 offset:31232\n\t" \
  "v_bfi_b32 v45, v45, v30, %15\n\t" \
  "v_mfma_f32_16x16x32_bf16 v[224:227], v[112:115], v[144:147], v[224:227]\n\t" \
  "v_mov_b32 v51, 0x3f803f80\n\t" \
  "v_bfe_i32 v46, v3, 12, 1\n\t" \
  "v_mfma_f32_16x16x32_bf16 v[232:235], v[116:119], v[144:147], v[232:235]\n\t" \
  "v_bfe_i32 v31, v3, 13, 1\n\t" \
  "v_perm_b32 v46, v31, v46, vcc_hi\n\t" \
  "v_mfma_f32_16x16x32_bf16 v[240:243], v[120:123], v[144:147], v[240:243]\n\t" \
  "ds_read_b64_tr_b16 v[144:145], v54 offset:38912\n\t" \
  "v_bfe_i32 v47, v3, 14, 1\n\t" \
  "v_mfma_f32_16x16x32_bf16 v[220:223], v[108:111], v[148:151], v[220:223]\n\t" \
  "ds_read_b64_tr_b16 v[146:147], v54 offset:39424\n\t" \
  "v_bfe_i32 v31, v3, 15, 1\n\t" \
  "v_mfma_f32_16x16x32_bf16 v[228:231], v[112:115], v[148:151], v[228:231]\n\t" \
  "v_perm_b32 v47, v31, v47, vcc_hi\n\t" \
  "v_add_u32 v30, 18432, v4\n\t" \
  "v_mfma_f32_16x16x32_bf16 v[236:239], v[116:119], v[148:151], v[236:239]\n\t" \
  "v_xor_b32 v31, 1024, v30\n\t" \
  "v_mov_b32 v32, %12\n\t" \
  "v_mfma_f32_16x16x32_bf16 v[244:247], v[120:123], v[148:151], v[244:247]\n\t" \
  "ds_read_b64_tr_b16 v[148:149], v55 offset:38912\n\t" \
  "v_xor_b32 v33, 4, v32\n\t" \
  "v_mfma_f32_16x16x32_bf16 v[152:155], v[128:131], v[108:111], v[152:155]\n\t" \
  "ds_read_b64_tr_b16 v[150:151], v55 offset:39424\n\t" \
  "v_mov_b32 v0, 1\n\t" \
  "v_mfma_f32_16x16x32_bf16 v[160:163], v[128:131], v[112:115], v[160:163]\n\t" \
  "v_and_b32 v4, 1023, v4\n\t" \
  "v_add_u32 v1, 8192, v4\n\t" \
  "v_mfma_f32_16x16x32_bf16 v[168:171], v[128:131], v[116:119], v[168:171]\n\t" \
  "v_mfma_f32_16x16x32_bf16 v[176:179], v[128:131], v[120:123], v[176:179]\n\t" \
  "ds_read_b64_tr_b16 v[128:129], v54 offset:22528\n\t" \
  "v_mfma_f32_16x16x32_bf16 v[156:159], v[132:135], v[108:111], v[156:159]\n\t" \
  "ds_read_b128 v[108:111], v53 offset:2048\n\t" \
  "v_mfma_f32_16x16x32_bf16 v[164:167], v[132:135], v[112:115], v[164:167]\n\t" \
  "ds_read_b128 v[112:115], v53 offset:4096\n\t" \
  "v_mfma_f32_16x16x32_bf16 v[172:175], v[132:135], v[116:119], v[172:175]\n\t" \
  "ds_read_b128 v[116:119], v53 offset:6144\n\t" \
  "v_mfma_f32_16x16x32_bf16 v[180:183], v[132:135], v[120:123], v[180:183]\n\t" \
  "ds_read_b128 v[120:123], v53 offset:8192\n\t" \
  "s_waitcnt lgkmcnt(5)\n\t" \
  "v_mfma_f32_16x16x32_bf16 v[248:251], v[92:95], v[80:83], v[248:251]\n\t" \
  "ds_read_b64_tr_b16 v[130:131], v54 offset:23040\n\t" \
  "v_mfma_f32_16x16x32_bf16 v[184:187], v[92:95], v[64:67], v[184:187]\n\t" \
  "ds_read_b64_tr_b16 v[132:133], v55 offset:22528\n\t" \
  "v_mfma_f32_16x16x32_bf16 v[192:195], v[92:95], v[68:71], v[192:195]\n\t" \
  "ds_read_b64_tr_b16 v[134:135], v55 offset:23040\n\t" \
  "v_mfma_f32_16x16x32_bf16 v[200:203], v[92:95], v[72:75], v[200:203]\n\t" \
  "v_mfma_f32_16x16x32_bf16 v[208:211], v[92:95], v[76:79], v[208:211]\n\t" \
  "v_mfma_f32_16x16x32_bf16 v[252:255], v[96:99], v[80:83], v[252:255]\n\t" \
  "v_mfma_f32_16x16x32_bf16 v[188:191], v[96:99], v[64:67], v[188:191]\n\t" \
  "v_mfma_f32_16x16x32_bf16 v[196:199], v[96:99], v[68:71], v[196:199]\n\t" \
  "v_mfma_f32_16x16x32_bf16 v[204:207], v[96:99], v[72:75], v[204:207]\n\t" \
  "v_mfma_f32_16x16x32_bf16 v[212:215], v[96:99], v[76:79], v[212:215]\n\t" \
  "v_mfma_f32_16x16x32_bf16 v[216:219], v[64:67], v[100:103], v[216:219]\n\t" \
  "v_mfma_f32_16x16x32_bf16 v[224:227], v[68:71], v[100:103], v[224:227]\n\t" \
  "v_mfma_f32_16x16x32_bf16 v[232:235], v[72:75], v[100:103], v[232:235]\n\t" \
  "s_waitcnt lgkmcnt(0)\n\t" \
  "s_barrier\n\t" \
  "v_mfma_f32_16x16x32_bf16 v[240:243], v[76:79], v[100:103], v[240:243]\n\t" \
  "ds_read_b64_tr_b16 v[92:93], v58 offset:26624\n\t" \
  "v_mfma_f32_16x16x32_bf16 v[220:223], v[64:67], v[104:107], v[220:223]\n\t" \
  "ds_read_b64_tr_b16 v[94:95], v58 offset:27136\n\t" \
  "v_mfma_f32_16x16x32_bf16 v[228:231], v[68:71], v[104:107], v[228:231]\n\t" \
  "ds_read_b128 v[80:83], v56 offset:0\n\t" \
  "v_mfma_f32_16x16x32_bf16 v[236:239], v[72:75], v[104:107], v[236:239]\n\t" \
  "ds_read_b64_tr_b16 v[96:97], v59 offset:26624\n\t" \
  "v_mfma_f32_16x16x32_bf16 v[244:247], v[76:79], v[104:107], v[244:247]\n\t" \
  "ds_read_b64_tr_b16 v[98:99], v59 offset:27136\n\t" \
  "v_mfma_f32_16x16x32_bf16 v[152:155], v[84:87], v[64:67], v[152:155]\n\t" \
  "ds_read_b64_tr_b16 v[100:101], v58 offset:34816\n\t" \
  "v_mfma_f32_16x16x32_bf16 v[160:163], v[84:87], v[68:71], v[160:163]\n\t" \
  "ds_read_b64_tr_b16 v[102:103], v58 offset:35328\n\t" \
  "v_mfma_f32_16x16x32_bf16 v[168:171], v[84:87], v[72:75], v[168:171]\n\t" \
  "ds_read_b64_tr_b16 v[104:105], v59 offset:34816\n\t" \
  "v_mfma_f32_16x16x32_bf16 v[176:179], v[84:87], v[76:79], v[176:179]\n\t" \
  "ds_read_b64_tr_b16 v[106:107], v59 offset:35328\n\t" \
  "v_mfma_f32_16x16x32_bf16 v[156:159], v[88:91], v[64:67], v[156:159]\n\t" \
  "ds_read_b128 v[64:67], v56 offset:2048\n\t" \
  "v_mfma_f32_16x16x32_bf16 v[164:167], v[88:91], v[68:71], v[164:167]\n\t" \
  "ds_read_b128 v[68:71], v56 offset:4096\n\t" \
  "v_mfma_f32_16x16x32_bf16 v[172:175], v[88:91], v[72:75], v[172:175]\n\t" \
  "ds_read_b128 v[72:75], v56 offset:6144\n\t" \
  "v_mfma_f32_16x16x32_bf16 v[180:183], v[88:91], v[76:79], v[180:183]\n\t" \
  "ds_read_b128 v[76:79], v56 offset:8192\n\t" \
  "v_mfma_f32_16x16x32_bf16 v[248:251], v[136:139], v[124:127], v[248:251]\n\t" \
  "ds_read_b64_tr_b16 v[84:85], v58 offset:18432\n\t" \
  "v_mfma_f32_16x16x32_bf16 v[184:187], v[136:139], v[108:111], v[184:187]\n\t" \
  "ds_read_b64_tr_b16 v[86:87], v58 offset:18944\n\t" \
  "v_mfma_f32_16x16x32_bf16 v[192:195], v[136:139], v[112:115], v[192:195]\n\t" \
  "ds_read_b64_tr_b16 v[88:89], v59 offset:18432\n\t" \
  "v_mfma_f32_16x16x32_bf16 v[200:203], v[136:139], v[116:119], v[200:203]\n\t" \
  "ds_read_b64_tr_b16 v[90:91], v59 offset:18944\n\t" \
  "v_mfma_f32_16x16x32_bf16 v[208:211], v[136:139], v[120:123], v[208:211]\n\t" \
  "ds_read_b64_tr_b16 v[136:137], v58 offset:30720\n\t" \
  "v_mfma_f32_16x16x32_bf16 v[252:255], v[140:143], v[124:127], v[252:255]\n\t" \
  "ds_read_b64_tr_b16 v[138:139], v58 offset:31232\n\t" \
  "v_mfma_f32_16x16x32_bf16 v[188:191], v[140:143], v[108:111], v[188:191]\n\t" \
  "ds_read_b128 v[124:127], v57 offset:0\n\t" \
  "v_mfma_f32_16x16x32_bf16 v[196:199], v[140:143], v[112:115], v[196:199]\n\t" \
  "v_mfma_f32_16x16x32_bf16 v[204:207], v[140:143], v[116:119], v[204:207]\n\t" \
  "v_mfma_f32_16x16x32_bf16 v[212:215], v[140:143], v[120:123], v[212:215]\n\t" \
  "ds_read_b64_tr_b16 v[140:141], v59 offset:30720\n\t" \
  "v_mfma_f32_16x16x32_bf16 v[216:219], v[108:111], v[144:147], v[216:219]\n\t" \
  "ds_read_b64_tr_b16 v[142:143], v59 offset:31232\n\t" \
  "v_mfma_f32_16x16x32_bf16 v[224:227], v[112:115], v[144:147], v[224:227]\n\t" \
  "v_mfma_f32_16x16x32_bf16 v[232:235], v[116:119], v[144:147], v[232:235]\n\t" \
  "v_mfma_f32_16x16x32_bf16 v[240:243], v[120:123], v[144:147], v[240:243]\n\t" \
  "ds_read_b64_tr_b16 v[144:145], v58 offset:38912\n\t" \
  "v_mfma_f32_16x16x32_bf16 v[220:223], v[108:111], v[148:151], v[220:223]\n\t" \
  "ds_read_b64_tr_b16 v[146:147], v58 offset:39424\n\t" \
  "v_mfma_f32_16x16x32_bf16 v[228:231], v[112:115], v[148:151], v[228:231]\n\t" \
  "v_mfma_f32_16x16x32_bf16 v[236:239], v[116:119], v[148:151], v[236:239]\n\t" \
  "v_mfma_f32_16x16x32_bf16 v[244:247], v[120:123], v[148:151], v[244:247]\n\t" \
  "ds_read_b64_tr_b16 v[148:149], v59 offset:38912\n\t" \
  "v_mfma_f32_16x16x32_bf16 v[152:155], v[128:131], v[108:111], v[152:155]\n\t" \
  "ds_read_b64_tr_b16 v[150:151], v59 offset:39424\n\t" \
  "v_mfma_f32_16x16x32_bf16 v[160:163], v[128:131], v[112:115], v[160:163]\n\t" \
  "v_mfma_f32_16x16x32_bf16 v[168:171], v[128:131], v[116:119], v[168:171]\n\t" \
  "v_mfma_f32_16x16x32_bf16 v[176:179], v[128:131], v[120:123], v[176:179]\n\t" \
  "ds_read_b64_tr_b16 v[128:129], v58 offset:22528\n\t" \
  "v_mfma_f32_16x16x32_bf16 v[156:159], v[132:135], v[108:111], v[156:159]\n\t" \
  "ds_read_b128 v[108:111], v57 offset:2048\n\t" \
  "v_mfma_f32_16x16x32_bf16 v[164:167], v[132:135], v[112:115], v[164:167]\n\t" \
  "ds_read_b128 v[112:115], v57 offset:4096\n\t" \
  "v_mfma_f32_16x16x32_bf16 v[172:175], v[132:135], v[116:119], v[172:175]\n\t" \
  "ds_read_b128 v[116:119], v57 offset:6144\n\t" \
  "v_mfma_f32_16x16x32_bf16 v[180:183], v[132:135], v[120:123], v[180:183]\n\t" \
  "ds_read_b128 v[120:123], v57 offset:8192\n\t" \
  "s_waitcnt lgkmcnt(5)\n\t" \
  "v_mfma_f32_16x16x32_bf16 v[248:251], v[92:95], v[80:83], v[248:251]\n\t" \
  "ds_read_b64_tr_b16 v[130:131], v58 offset:23040\n\t" \
  "v_mfma_f32_16x16x32_bf16 v[184:187], v[92:95], v[64:67], v[184:187]\n\t" \
  "ds_read_b64_tr_b16 v[132:133], v59 offset:22528\n\t" \
  "v_mfma_f32_16x16x32_bf16 v[192:195], v[92:95], v[68:71], v[192:195]\n\t" \
  "ds_read_b64_tr_b16 v[134:135], v59 offset:23040\n\t" \
  "v_mfma_f32_16x16x32_bf16 v[200:203], v[92:95], v[72:75], v[200:203]\n\t" \
  "v_mfma_f32_16x16x32_bf16 v[208:211], v[92:95], v[76:79], v[208:211]\n\t" \
  "v_mfma_f32_16x16x32_bf16 v[252:255], v[96:99], v[80:83], v[252:255]\n\t" \
  "v_mfma_f32_16x16x32_bf16 v[188:191], v[96:99], v[64:67], v[188:191]\n\t" \
  "v_mfma_f32_16x16x32_bf16 v[196:199], v[96:99], v[68:71], v[196:199]\n\t" \
  "v_mfma_f32_16x16x32_bf16 v[204:207], v[96:99], v[72:75], v[204:207]\n\t" \
  "v_mfma_f32_16x16x32_bf16 v[212:215], v[96:99], v[76:79], v[212:215]\n\t" \
  "v_mfma_f32_16x16x32_bf16 v[216:219], v[64:67], v[100:103], v[216:219]\n\t" \
  "v_mfma_f32_16x16x32_bf16 v[224:227], v[68:71], v[100:103], v[224:227]\n\t" \
  "v_mfma_f32_16x16x32_bf16 v[232:235], v[72:75], v[100:103], v[232:235]\n\t" \
  "s_waitcnt lgkmcnt(0)\n\t" \
  "s_waitcnt vmcnt(0)\n\t" \
  "s_barrier\n\t" \
  "v_mfma_f32_16x16x32_bf16 v[240:243], v[76:79], v[100:103], v[240:243]\n\t" \
  "ds_read_b64_tr_b16 v[92:93], v62 offset:26624\n\t" \
  "v_mfma_f32_16x16x32_bf16 v[220:223], v[64:67], v[104:107], v[220:223]\n\t" \
  "ds_read_b64_tr_b16 v[94:95], v62 offset:27136\n\t" \
  "v_mfma_f32_16x16x32_bf16 v[228:231], v[68:71], v[104:107], v[228:231]\n\t" \
  "ds_read_b128 v[80:83], v60 offset:0\n\t" \
  "v_mfma_f32_16x16x32_bf16 v[236:239], v[72:75], v[104:107], v[236:239]\n\t" \
  "ds_read_b64_tr_b16 v[96:97], v63 offset:26624\n\t" \
  "v_mfma_f32_16x16x32_bf16 v[244:247], v[76:79], v[104:107], v[244:247]\n\t" \
  "ds_read_b64_tr_b16 v[98:99], v63 offset:27136\n\t" \
  "v_mfma_f32_16x16x32_bf16 v[152:155], v[84:87], v[64:67], v[152:155]\n\t" \
  "ds_read_b64_tr_b16 v[100:101], v62 offset:34816\n\t" \
  "v_mfma_f32_16x16x32_bf16 v[160:163], v[84:87], v[68:71], v[160:163]\n\t" \
  "ds_read_b64_tr_b16 v[102:103], v62 offset:35328\n\t" \
  "v_mfma_f32_16x16x32_bf16 v[168:171], v[84:87], v[72:75], v[168:171]\n\t" \
  "ds_read_b64_tr_b16 v[104:105], v63 offset:34816\n\t" \
  "v_mfma_f32_16x16x32_bf16 v[176:179], v[84:87], v[76:79], v[176:179]\n\t" \
  "ds_read_b64_tr_b16 v[106:107], v63 offset:35328\n\t" \
  "v_mfma_f32_16x16x32_bf16 v[156:159], v[88:91], v[64:67], v[156:159]\n\t" \
  "ds_read_b128 v[64:67], v60 offset:2048\n\t" \
  "v_mfma_f32_16x16x32_bf16 v[164:167], v[88:91], v[68:71], v[164:167]\n\t" \
  "ds_read_b128 v[68:71], v60 offset:4096\n\t" \
  "v_mfma_f32_16x16x32_bf16 v[172:175], v[88:91], v[72:75], v[172:175]\n\t" \
  "ds_read_b128 v[72:75], v60 offset:6144\n\t" \
  "v_mfma_f32_16x16x32_bf16 v[180:183], v[88:91], v[76:79], v[180:183]\n\t" \
  "ds_read_b128 v[76:79], v60 offset:8192\n\t" \
  "v_mfma_f32_16x16x32_bf16 v[248:251], v[136:139], v[124:127], v[248:251]\n\t" \
  "ds_read_b64_tr_b16 v[84:85], v62 offset:18432\n\t" \
  "v_mfma_f32_16x16x32_bf16 v[184:187], v[136:139], v[108:111], v[184:187]\n\t" \
  "ds_read_b64_tr_b16 v[86:87], v62 offset:18944\n\t" \
  "v_mfma_f32_16x16x32_bf16 v[192:195], v[136:139], v[112:115], v[192:195]\n\t" \
  "ds_read_b64_tr_b16 v[88:89], v63 offset:18432\n\t" \
  "v_mfma_f32_16x16x32_bf16 v[200:203], v[136:139], v[116:119], v[200:203]\n\t" \
  "ds_read_b64_tr_b16 v[90:91], v63 offset:18944\n\t" \
  "v_mfma_f32_16x16x32_bf16 v[208:211], v[136:139], v[120:123], v[208:211]\n\t" \
  "ds_read_b64_tr_b16 v[136:137], v62 offset:30720\n\t" \
  "v_mfma_f32_16x16x32_bf16 v[252:255], v[140:143], v[124:127], v[252:255]\n\t" \
  "ds_read_b64_tr_b16 v[138:139], v62 offset:31232\n\t" \
  "v_mfma_f32_16x16x32_bf16 v[188:191], v[140:143], v[108:111], v[188:191]\n\t" \
  "ds_read_b128 v[124:127], v61 offset:0\n\t" \
  "v_mfma_f32_16x16x32_bf16 v[196:199], v[140:143], v[112:115], v[196:199]\n\t" \
  "v_mfma_f32_16x16x32_bf16 v[204:207], v[140:143], v[116:119], v[204:207]\n\t" \
  "v_mfma_f32_16x16x32_bf16 v[212:215], v[140:143], v[120:123], v[212:215]\n\t" \
  "ds_read_b64_tr_b16 v[140:141], v63 offset:30720\n\t" \
  "v_mfma_f32_16x16x32_bf16 v[216:219], v[108:111], v[144:147], v[216:219]\n\t" \
  "ds_read_b64_tr_b16 v[142:143], v63 offset:31232\n\t" \
  "v_mfma_f32_16x16x32_bf16 v[224:227], v[112:115], v[144:147], v[224:227]\n\t" \
  "v_mfma_f32_16x16x32_bf16 v[232:235], v[116:119], v[144:147], v[232:235]\n\t" \
  "v_mfma_f32_16x16x32_bf16 v[240:243], v[120:123], v[144:147], v[240:243]\n\t" \
  "ds_read_b64_tr_b16 v[144:145], v62 offset:38912\n\t" \
  "v_mfma_f32_16x16x32_bf16 v[220:223], v[108:111], v[148:151], v[220:223]\n\t" \
  "ds_read_b64_tr_b16 v[146:147], v62 offset:39424\n\t" \
  "v_mfma_f32_16x16x32_bf16 v[228:231], v[112:115], v[148:151], v[228:231]\n\t" \
  "v_mfma_f32_16x16x32_bf16 v[236:239], v[116:119], v[148:151], v[236:239]\n\t" \
  "v_mfma_f32_16x16x32_bf16 v[244:247], v[120:123], v[148:151], v[244:247]\n\t" \
  "ds_read_b64_tr_b16 v[148:149], v63 offset:38912\n\t" \
  "v_mfma_f32_16x16x32_bf16 v[152:155], v[128:131], v[108:111], v[152:155]\n\t" \
  "ds_read_b64_tr_b16 v[150:151], v63 offset:39424\n\t" \
  "v_mfma_f32_16x16x32_bf16 v[160:163], v[128:131], v[112:115], v[160:163]\n\t" \
  "v_mfma_f32_16x16x32_bf16 v[168:171], v[128:131], v[116:119], v[168:171]\n\t" \
  "v_mfma_f32_16x16x32_bf16 v[176:179], v[128:131], v[120:123], v[176:179]\n\t" \
  "ds_read_b64_tr_b16 v[128:129], v62 offset:22528\n\t" \
  "v_mfma_f32_16x16x32_bf16 v[156:159], v[132:135], v[108:111], v[156:159]\n\t" \
  "ds_read_b128 v[108:111], v61 offset:2048\n\t" \
  "v_mfma_f32_16x16x32_bf16 v[164:167], v[132:135], v[112:115], v[164:167]\n\t" \
  "ds_read_b128 v[112:115], v61 offset:4096\n\t" \
  "v_mfma_f32_16x16x32_bf16 v[172:175], v[132:135], v[116:119], v[172:175]\n\t" \
  "ds_read_b128 v[116:119], v61 offset:6144\n\t" \
  "v_mfma_f32_16x16x32_bf16 v[180:183], v[132:135], v[120:123], v[180:183]\n\t" \
  "ds_read_b32 v10, v32 offset:104\n\t" \
  "ds_read_b128 v[120:123], v61 offset:8192\n\t" \
  "s_waitcnt lgkmcnt(6)\n\t" \
  "v_mfma_f32_16x16x32_bf16 v[248:251], v[92:95], v[80:83], v[248:251]\n\t" \
  "v_lshlrev_b32 v11, 3, v4\n\t" \
  "ds_read_b64_tr_b16 v[130:131], v62 offset:23040\n\t" \
  "v_mfma_f32_16x16x32_bf16 v[184:187], v[92:95], v[64:67], v[184:187]\n\t" \
  "ds_read_b64_tr_b16 v[132:133], v63 offset:22528\n\t" \
  "v_mfma_f32_16x16x32_bf16 v[192:195], v[92:95], v[68:71], v[192:195]\n\t" \
  "ds_read_b64_tr_b16 v[134:135], v63 offset:23040\n\t" \
  "v_mfma_f32_16x16x32_bf16 v[200:203], v[92:95], v[72:75], v[200:203]\n\t" \
  "v_mfma_f32_16x16x32_bf16 v[208:211], v[92:95], v[76:79], v[208:211]\n\t" \
  "v_mfma_f32_16x16x32_bf16 v[252:255], v[96:99], v[80:83], v[252:255]\n\t" \
  "v_mfma_f32_16x16x32_bf16 v[188:191], v[96:99], v[64:67], v[188:191]\n\t" \
  "v_mfma_f32_16x16x32_bf16 v[196:199], v[96:99], v[68:71], v[196:199]\n\t" \
  "v_mfma_f32_16x16x32_bf16 v[204:207], v[96:99], v[72:75], v[204:207]\n\t" \
  "v_mfma_f32_16x16x32_bf16 v[212:215], v[96:99], v[76:79], v[212:215]\n\t" \
  "s_waitcnt lgkmcnt(4)\n\t" \
  "s_cmp_lg_u32 %13, 0\n\t" \
  "s_cbranch_scc1 27f\n\t" \
  "s_mov_b32 exec_hi, 0\n\t" \
  "global_store_dword v11, v10, %25 sc1\n\t" \
  "s_mov_b32 exec_hi, -1\n\t" \
  "27:\n\t" \
  "v_mfma_f32_16x16x32_bf16 v[216:219], v[64:67], v[100:103], v[216:219]\n\t" \
  "v_mfma_f32_16x16x32_bf16 v[224:227], v[68:71], v[100:103], v[224:227]\n\t" \
  "v_mfma_f32_16x16x32_bf16 v[232:235], v[72:75], v[100:103], v[232:235]\n\t" \
  "s_waitcnt lgkmcnt(0)\n\t" \
  "s_barrier\n\t" \
  "v_mfma_f32_16x16x32_bf16 v[240:243], v[76:79], v[100:103], v[240:243]\n\t" \
  "ds_read_b64_tr_b16 v[92:93], v54 offset:26624\n\t" \
  "v_mfma_f32_16x16x32_bf16 v[220:223], v[64:67], v[104:107], v[220:223]\n\t" \
  "ds_read_b64_tr_b16 v[94:95], v54 offset:27136\n\t" \
  "v_mfma_f32_16x16x32_bf16 v[228:231], v[68:71], v[104:107], v[228:231]\n\t" \
  "ds_read_b128 v[80:83], v52 offset:0\n\t" \
  "v_mfma_f32_16x16x32_bf16 v[236:239], v[72:75], v[104:107], v[236:239]\n\t" \
  "ds_read_b64_tr_b16 v[96:97], v55 offset:26624\n\t" \
  "v_mfma_f32_16x16x32_bf16 v[244:247], v[76:79], v[104:107], v[244:247]\n\t" \
  "ds_read_b64_tr_b16 v[98:99], v55 offset:27136\n\t" \
  "v_mfma_f32_16x16x32_bf16 v[152:155], v[84:87], v[64:67], v[152:155]\n\t" \
  "ds_read_b64_tr_b16 v[100:101], v54 offset:34816\n\t" \
  "v_mfma_f32_16x16x32_bf16 v[160:163], v[84:87], v[68:71], v[160:163]\n\t" \
  "ds_read_b64_tr_b16 v[102:103], v54 offset:35328\n\t" \
  "v_mfma_f32_16x16x32_bf16 v[168:171], v[84:87], v[72:75], v[168:171]\n\t" \
  "ds_read_b64_tr_b16 v[104:105], v55 offset:34816\n\t" \
  "v_mfma_f32_16x16x32_bf16 v[176:179], v[84:87], v[76:79], v[176:179]\n\t" \
  "ds_read_b64_tr_b16 v[106:107], v55 offset:35328\n\t" \
  "v_mfma_f32_16x16x32_bf16 v[156:159], v[88:91], v[64:67], v[156:159]\n\t" \
  "ds_read_b128 v[64:67], v52 offset:2048\n\t" \
  "v_mfma_f32_16x16x32_bf16 v[164:167], v[88:91], v[68:71], v[164:167]\n\t" \
  "ds_read_b128 v[68:71], v52 offset:4096\n\t" \
  "v_mfma_f32_16x16x32_bf16 v[172:175], v[88:91], v[72:75], v[172:175]\n\t" \
  "ds_read_b128 v[72:75], v52 offset:6144\n\t" \
  "v_mfma_f32_16x16x32_bf16 v[180:183], v[88:91], v[76:79], v[180:183]\n\t" \
  "ds_read_b128 v[76:79], v52 offset:8192\n\t" \
  "v_mfma_f32_16x16x32_bf16 v[248:251], v[136:139], v[124:127], v[248:251]\n\t" \
  "ds_read_b64_tr_b16 v[84:85], v54 offset:18432\n\t" \
  "v_mfma_f32_16x16x32_bf16 v[184:187], v[136:139], v[108:111], v[184:187]\n\t" \
  "ds_read_b64_tr_b16 v[86:87], v54 offset:18944\n\t" \
  "v_mfma_f32_16x16x32_bf16 v[192:195], v[136:139], v[112:115], v[192:195]\n\t" \
  "ds_read_b64_tr_b16 v[88:89], v55 offset:18432\n\t" \
  "v_mfma_f32_16x16x32_bf16 v[200:203], v[136:139], v[116:119], v[200:203]\n\t" \
  "ds_read_b64_tr_b16 v[90:91], v55 offset:18944\n\t" \
  "v_mfma_f32_16x16x32_bf16 v[208:211], v[136:139], v[120:123], v[208:211]\n\t" \
  "ds_read_b64_tr_b16 v[136:137], v54 offset:30720\n\t" \
  "v_mfma_f32_16x16x32_bf16 v[252:255], v[140:143], v[124:127], v[252:255]\n\t" \
  "ds_read_b64_tr_b16 v[138:139], v54 offset:31232\n\t" \
  "v_mfma_f32_16x16x32_bf16 v[188:191], v[140:143], v[108:111], v[188:191]\n\t" \
  "ds_read_b128 v[124:127], v53 offset:0\n\t" \
  "v_mfma_f32_16x16x32_bf16 v[196:199], v[140:143], v[112:115], v[196:199]\n\t" \
  "v_mfma_f32_16x16x32_bf16 v[204:207], v[140:143], v[116:119], v[204:207]\n\t" \
  "v_mfma_f32_16x16x32_bf16 v[212:215], v[140:143], v[120:123], v[212:215]\n\t" \
  "ds_read_b64_tr_b16 v[140:141], v55 offset:30720\n\t" \
  "v_mfma_f32_16x16x32_bf16 v[216:219], v[108:111], v[144:147], v[216:219]\n\t" \
  "ds_read_b64_tr_b16 v[142:143], v55 offset:31232\n\t" \
  "v_mfma_f32_16x16x32_bf16 v[224:227], v[112:115], v[144:147], v[224:227]\n\t" \
  "v_mfma_f32_16x16x32_bf16 v[232:235], v[116:119], v[144:147], v[232:235]\n\t" \
  "v_mfma_f32_16x16x32_bf16 v[240:243], v[120:123], v[144:147], v[240:243]\n\t" \
  "ds_read_b64_tr_b16 v[144:145], v54 offset:38912\n\t" \
  "v_mfma_f32_16x16x32_bf16 v[220:223], v[108:111], v[148:151], v[220:223]\n\t" \
  "ds_read_b64_tr_b16 v[146:147], v54 offset:39424\n\t" \
  "v_mfma_f32_16x16x32_bf16 v[228:231], v[112:115], v[148:151], v[228:231]\n\t" \
  "v_mfma_f32_16x16x32_bf16 v[236:239], v[116:119], v[148:151], v[236:239]\n\t" \
  "v_mfma_f32_16x16x32_bf16 v[244:247], v[120:123], v[148:151], v[244:247]\n\t" \
  "ds_read_b64_tr_b16 v[148:149], v55 offset:38912\n\t" \
  "v_mfma_f32_16x16x32_bf16 v[152:155], v[128:131], v[108:111], v[152:155]\n\t" \
  "ds_read_b64_tr_b16 v[150:151], v55 offset:39424\n\t" \
  "v_mfma_f32_16x16x32_bf16 v[160:163], v[128:131], v[112:115], v[160:163]\n\t" \
  "v_mfma_f32_16x16x32_bf16 v[168:171], v[128:131], v[116:119], v[168:171]\n\t" \
  "v_mfma_f32_16x16x32_bf16 v[176:179], v[128:131], v[120:123], v[176:179]\n\t" \
  "ds_read_b64_tr_b16 v[128:129], v54 offset:22528\n\t" \
  "v_mfma_f32_16x16x32_bf16 v[156:159], v[132:135], v[108:111], v[156:159]\n\t" \
  "ds_read_b128 v[108:111], v53 offset:2048\n\t" \
  "v_mfma_f32_16x16x32_bf16 v[164:167], v[132:135], v[112:115], v[164:167]\n\t" \
  "ds_read_b128 v[112:115], v53 offset:4096\n\t" \
  "v_mfma_f32_16x16x32_bf16 v[172:175], v[132:135], v[116:119], v[172:175]\n\t" \
  "ds_read_b128 v[116:119], v53 offset:6144\n\t" \
  "v_mfma_f32_16x16x32_bf16 v[180:183], v[132:135], v[120:123], v[180:183]\n\t" \
  "ds_read_b128 v[120:123], v53 offset:8192\n\t" \
  "s_waitcnt lgkmcnt(5)\n\t" \
  "v_mfma_f32_16x16x32_bf16 v[248:251], v[92:95], v[80:83], v[248:251]\n\t" \
  "ds_read_b64_tr_b16 v[130:131], v54 offset:23040\n\t" \
  "v_mfma_f32_16x16x32_bf16 v[184:187], v[92:95], v[64:67], v[184:187]\n\t" \
  "ds_read_b64_tr_b16 v[132:133], v55 offset:22528\n\t" \
  "v_mfma_f32_16x16x32_bf16 v[192:195], v[92:95], v[68:71], v[192:195]\n\t" \
  "ds_read_b64_tr_b16 v[134:135], v55 offset:23040\n\t" \
  "v_mfma_f32_16x16x32_bf16 v[200:203], v[92:95], v[72:75], v[200:203]\n\t" \
  "v_mfma_f32_16x16x32_bf16 v[208:211], v[92:95], v[76:79], v[208:211]\n\t" \
  "v_mfma_f32_16x16x32_bf16 v[252:255], v[96:99], v[80:83], v[252:255]\n\t" \
  "v_mfma_f32_16x16x32_bf16 v[188:191], v[96:99], v[64:67], v[188:191]\n\t" \
  "v_mfma_f32_16x16x32_bf16 v[196:199], v[96:99], v[68:71], v[196:199]\n\t" \
  "v_mfma_f32_16x16x32_bf16 v[204:207], v[96:99], v[72:75], v[204:207]\n\t" \
  "v_mfma_f32_16x16x32_bf16 v[212:215], v[96:99], v[76:79], v[212:215]\n\t" \
  "v_mfma_f32_16x16x32_bf16 v[216:219], v[64:67], v[100:103], v[216:219]\n\t" \
  "v_mfma_f32_16x16x32_bf16 v[224:227], v[68:71], v[100:103], v[224:227]\n\t" \
  "v_mfma_f32_16x16x32_bf16 v[232:235], v[72:75], v[100:103], v[232:235]\n\t" \
  "s_waitcnt lgkmcnt(0)\n\t" \
  "s_barrier\n\t" \
  "v_mfma_f32_16x16x32_bf16 v[240:243], v[76:79], v[100:103], v[240:243]\n\t" \
  "ds_read_b64_tr_b16 v[92:93], v58 offset:26624\n\t" \
  "v_mfma_f32_16x16x32_bf16 v[220:223], v[64:67], v[104:107], v[220:223]\n\t" \
  "ds_read_b64_tr_b16 v[94:95], v58 offset:27136\n\t" \
  "v_mfma_f32_16x16x32_bf16 v[228:231], v[68:71], v[104:107], v[228:231]\n\t" \
  "ds_read_b128 v[80:83], v56 offset:0\n\t" \
  "v_mfma_f32_16x16x32_bf16 v[236:239], v[72:75], v[104:107], v[236:239]\n\t" \
  "ds_read_b64_tr_b16 v[96:97], v59 offset:26624\n\t" \
  "v_mfma_f32_16x16x32_bf16 v[244:247], v[76:79], v[104:107], v[244:247]\n\t" \
  "ds_read_b64_tr_b16 v[98:99], v59 offset:27136\n\t" \
  "v_mfma_f32_16x16x32_bf16 v[152:155], v[84:87], v[64:67], v[152:155]\n\t" \
  "ds_read_b64_tr_b16 v[100:101], v58 offset:34816\n\t" \
  "v_mfma_f32_16x16x32_bf16 v[160:163], v[84:87], v[68:71], v[160:163]\n\t" \
  "ds_read_b64_tr_b16 v[102:103], v58 offset:35328\n\t" \
  "v_mfma_f32_16x16x32_bf16 v[168:171], v[84:87], v[72:75], v[168:171]\n\t" \
  "ds_read_b64_tr_b16 v[104:105], v59 offset:34816\n\t" \
  "v_mfma_f32_16x16x32_bf16 v[176:179], v[84:87], v[76:79], v[176:179]\n\t" \
  "ds_read_b64_tr_b16 v[106:107], v59 offset:35328\n\t" \
  "v_mfma_f32_16x16x32_bf16 v[156:159], v[88:91], v[64:67], v[156:159]\n\t" \
  "ds_read_b128 v[64:67], v56 offset:2048\n\t" \
  "v_mfma_f32_16x16x32_bf16 v[164:167], v[88:91], v[68:71], v[164:167]\n\t" \
  "ds_read_b128 v[68:71], v56 offset:4096\n\t" \
  "v_mfma_f32_16x16x32_bf16 v[172:175], v[88:91], v[72:75], v[172:175]\n\t" \
  "ds_read_b128 v[72:75], v56 offset:6144\n\t" \
  "v_mfma_f32_16x16x32_bf16 v[180:183], v[88:91], v[76:79], v[180:183]\n\t" \
  "ds_read_b128 v[76:79], v56 offset:8192\n\t" \
  "v_mfma_f32_16x16x32_bf16 v[248:251], v[136:139], v[124:127], v[248:251]\n\t" \
  "ds_read_b64_tr_b16 v[84:85], v58 offset:18432\n\t" \
  "v_mfma_f32_16x16x32_bf16 v[184:187], v[136:139], v[108:111], v[184:187]\n\t" \
  "ds_read_b64_tr_b16 v[86:87], v58 offset:18944\n\t" \
  "v_mfma_f32_16x16x32_bf16 v[192:195], v[136:139], v[112:115], v[192:195]\n\t" \
  "ds_read_b64_tr_b16 v[88:89], v59 offset:18432\n\t" \
  "v_mfma_f32_16x16x32_bf16 v[200:203], v[136:139], v[116:119], v[200:203]\n\t" \
  "ds_read_b64_tr_b16 v[90:91], v59 offset:18944\n\t" \
  "v_mfma_f32_16x16x32_bf16 v[208:211], v[136:139], v[120:123], v[208:211]\n\t" \
  "ds_read_b64_tr_b16 v[136:137], v58 offset:30720\n\t" \
  "v_mfma_f32_16x16x32_bf16 v[252:255], v[140:143], v[124:127], v[252:255]\n\t" \
  "ds_read_b64_tr_b16 v[138:139], v58 offset:31232\n\t" \
  "v_mfma_f32_16x16x32_bf16 v[188:191], v[140:143], v[108:111], v[188:191]\n\t" \
  "ds_read_b128 v[124:127], v57 offset:0\n\t" \
  "v_mfma_f32_16x16x32_bf16 v[196:199], v[140:143], v[112:115], v[196:199]\n\t" \
  "v_mfma_f32_16x16x32_bf16 v[204:207], v[140:143], v[116:119], v[204:207]\n\t" \
  "v_mfma_f32_16x16x32_bf16 v[212:215], v[140:143], v[120:123], v[212:215]\n\t" \
  "ds_read_b64_tr_b16 v[140:141], v59 offset:30720\n\t" \
  "v_mfma_f32_16x16x32_bf16 v[216:219], v[108:111], v[144:147], v[216:219]\n\t" \
  "ds_read_b64_tr_b16 v[142:143], v59 offset:31232\n\t" \
  "v_mfma_f32_16x16x32_bf16 v[224:227], v[112:115], v[144:147], v[224:227]\n\t" \
  "v_mfma_f32_16x16x32_bf16 v[232:235], v[116:119], v[144:147], v[232:235]\n\t" \
  "v_mfma_f32_16x16x32_bf16 v[240:243], v[120:123], v[144:147], v[240:243]\n\t" \
  "ds_read_b64_tr_b16 v[144:145], v58 offset:38912\n\t" \
  "v_mfma_f32_16x16x32_bf16 v[220:223], v[108:111], v[148:151], v[220:223]\n\t" \
  "ds_read_b64_tr_b16 v[146:147], v58 offset:39424\n\t" \
  "v_mfma_f32_16x16x32_bf16 v[228:231], v[112:115], v[148:151], v[228:231]\n\t" \
  "v_mfma_f32_16x16x32_bf16 v[236:239], v[116:119], v[148:151], v[236:239]\n\t" \
  "v_mfma_f32_16x16x32_bf16 v[244:247], v[120:123], v[148:151], v[244:247]\n\t" \
  "ds_read_b64_tr_b16 v[148:149], v59 offset:38912\n\t" \
  "v_mfma_f32_16x16x32_bf16 v[152:155], v[128:131], v[108:111], v[152:155]\n\t" \
  "ds_read_b64_tr_b16 v[150:151], v59 offset:39424\n\t" \
  "v_mfma_f32_16x16x32_bf16 v[160:163], v[128:131], v[112:115], v[160:163]\n\t" \
  "v_mfma_f32_16x16x32_bf16 v[168:171], v[128:131], v[116:119], v[168:171]\n\t" \
  "v_mfma_f32_16x16x32_bf16 v[176:179], v[128:131], v[120:123], v[176:179]\n\t" \
  "ds_read_b64_tr_b16 v[128:129], v58 offset:22528\n\t" \
  "v_mfma_f32_16x16x32_bf16 v[156:159], v[132:135], v[108:111], v[156:159]\n\t" \
  "ds_read_b128 v[108:111], v57 offset:2048\n\t" \
  "v_mfma_f32_16x16x32_bf16 v[164:167], v[132:135], v[112:115], v[164:167]\n\t" \
  "ds_read_b128 v[112:115], v57 offset:4096\n\t" \
  "v_mfma_f32_16x16x32_bf16 v[172:175], v[132:135], v[116:119], v[172:175]\n\t" \
  "ds_read_b128 v[116:119], v57 offset:6144\n\t" \
  "v_mfma_f32_16x16x32_bf16 v[180:183], v[132:135], v[120:123], v[180:183]\n\t" \
  "ds_read_b128 v[120:123], v57 offset:8192\n\t" \
  "s_waitcnt lgkmcnt(5)\n\t" \
  "v_mfma_f32_16x16x32_bf16 v[248:251], v[92:95], v[80:83], v[248:251]\n\t" \
  "ds_read_b64_tr_b16 v[130:131], v58 offset:23040\n\t" \
  "v_mfma_f32_16x16x32_bf16 v[184:187], v[92:95], v[64:67], v[184:187]\n\t" \
  "ds_read_b64_tr_b16 v[132:133], v59 offset:22528\n\t" \
  "v_mfma_f32_16x16x32_bf16 v[192:195], v[92:95], v[68:71], v[192:195]\n\t" \
  "ds_read_b64_tr_b16 v[134:135], v59 offset:23040\n\t" \
  "v_mfma_f32_16x16x32_bf16 v[200:203], v[92:95], v[72:75], v[200:203]\n\t" \
  "v_mfma_f32_16x16x32_bf16 v[208:211], v[92:95], v[76:79], v[208:211]\n\t" \
  "v_mfma_f32_16x16x32_bf16 v[252:255], v[96:99], v[80:83], v[252:255]\n\t" \
  "v_mfma_f32_16x16x32_bf16 v[188:191], v[96:99], v[64:67], v[188:191]\n\t" \
  "v_mfma_f32_16x16x32_bf16 v[196:199], v[96:99], v[68:71], v[196:199]\n\t" \
  "v_mfma_f32_16x16x32_bf16 v[204:207], v[96:99], v[72:75], v[204:207]\n\t" \
  "v_mfma_f32_16x16x32_bf16 v[212:215], v[96:99], v[76:79], v[212:215]\n\t" \
  "v_mfma_f32_16x16x32_bf16 v[216:219], v[64:67], v[100:103], v[216:219]\n\t" \
  "v_mfma_f32_16x16x32_bf16 v[224:227], v[68:71], v[100:103], v[224:227]\n\t" \
  "v_mfma_f32_16x16x32_bf16 v[232:235], v[72:75], v[100:103], v[232:235]\n\t" \
  "s_waitcnt lgkmcnt(0)\n\t" \
  "s_barrier\n\t" \
  "v_mfma_f32_16x16x32_bf16 v[240:243], v[76:79], v[100:103], v[240:243]\n\t" \
  "ds_read_b64_tr_b16 v[92:93], v62 offset:26624\n\t" \
  "v_mfma_f32_16x16x32_bf16 v[220:223], v[64:67], v[104:107], v[220:223]\n\t" \
  "ds_read_b64_tr_b16 v[94:95], v62 offset:27136\n\t" \
  "v_mfma_f32_16x16x32_bf16 v[228:231], v[68:71], v[104:107], v[228:231]\n\t" \
  "ds_read_b128 v[80:83], v60 offset:0\n\t" \
  "v_mfma_f32_16x16x32_bf16 v[236:239], v[72:75], v[104:107], v[236:239]\n\t" \
  "ds_read_b64_tr_b16 v[96:97], v63 offset:26624\n\t" \
  "v_mfma_f32_16x16x32_bf16 v[244:247], v[76:79], v[104:107], v[244:247]\n\t" \
  "ds_read_b64_tr_b16 v[98:99], v63 offset:27136\n\t" \
  "v_mfma_f32_16x16x32_bf16 v[152:155], v[84:87], v[64:67], v[152:155]\n\t" \
  "ds_read_b64_tr_b16 v[100:101], v62 offset:34816\n\t" \
  "v_mfma_f32_16x16x32_bf16 v[160:163], v[84:87], v[68:71], v[160:163]\n\t" \
  "ds_read_b64_tr_b16 v[102:103], v62 offset:35328\n\t" \
  "v_mfma_f32_16x16x32_bf16 v[168:171], v[84:87], v[72:75], v[168:171]\n\t" \
  "ds_read_b64_tr_b16 v[104:105], v63 offset:34816\n\t" \
  "v_mfma_f32_16x16x32_bf16 v[176:179], v[84:87], v[76:79], v[176:179]\n\t" \
  "ds_read_b64_tr_b16 v[106:107], v63 offset:35328\n\t" \
  "v_mfma_f32_16x16x32_bf16 v[156:159], v[88:91], v[64:67], v[156:159]\n\t" \
  "ds_read_b128 v[64:67], v60 offset:2048\n\t" \
  "v_mfma_f32_16x16x32_bf16 v[164:167], v[88:91], v[68:71], v[164:167]\n\t" \
  "ds_read_b128 v[68:71], v60 offset:4096\n\t" \
  "v_mfma_f32_16x16x32_bf16 v[172:175], v[88:91], v[72:75], v[172:175]\n\t" \
  "ds_read_b128 v[72:75], v60 offset:6144\n\t" \
  "v_mfma_f32_16x16x32_bf16 v[180:183], v[88:91], v[76:79], v[180:183]\n\t" \
  "ds_read_b128 v[76:79], v60 offset:8192\n\t" \
  "v_mfma_f32_16x16x32_bf16 v[248:251], v[136:139], v[124:127], v[248:251]\n\t" \
  "ds_read_b64_tr_b16 v[84:85], v62 offset:18432\n\t" \
  "v_mfma_f32_16x16x32_bf16 v[184:187], v[136:139], v[108:111], v[184:187]\n\t" \
  "ds_read_b64_tr_b16 v[86:87], v62 offset:18944\n\t" \
  "v_mfma_f32_16x16x32_bf16 v[192:195], v[136:139], v[112:115], v[192:195]\n\t" \
  "ds_read_b64_tr_b16 v[88:89], v63 offset:18432\n\t" \
  "v_mfma_f32_16x16x32_bf16 v[200:203], v[136:139], v[116:119], v[200:203]\n\t" \
  "ds_read_b64_tr_b16 v[90:91], v63 offset:18944\n\t" \
  "v_mfma_f32_16x16x32_bf16 v[208:211], v[136:139], v[120:123], v[208:211]\n\t" \
  "ds_read_b64_tr_b16 v[136:137], v62 offset:30720\n\t" \
  "v_mfma_f32_16x16x32_bf16 v[252:255], v[140:143], v[124:127], v[252:255]\n\t" \
  "ds_read_b64_tr_b16 v[138:139], v62 offset:31232\n\t" \
  "v_mfma_f32_16x16x32_bf16 v[188:191], v[140:143], v[108:111], v[188:191]\n\t" \
  "ds_read_b128 v[124:127], v61 offset:0\n\t" \
  "v_mfma_f32_16x16x32_bf16 v[196:199], v[140:143], v[112:115], v[196:199]\n\t" \
  "v_mfma_f32_16x16x32_bf16 v[204:207], v[140:143], v[116:119], v[204:207]\n\t" \
  "v_mfma_f32_16x16x32_bf16 v[212:215], v[140:143], v[120:123], v[212:215]\n\t" \
  "ds_read_b64_tr_b16 v[140:141], v63 offset:30720\n\t" \
  "v_mfma_f32_16x16x32_bf16 v[216:219], v[108:111], v[144:147], v[216:219]\n\t" \
  "ds_read_b64_tr_b16 v[142:143], v63 offset:31232\n\t" \
  "v_mfma_f32_16x16x32_bf16 v[224:227], v[112:115], v[144:147], v[224:227]\n\t" \
  "v_mfma_f32_16x16x32_bf16 v[232:235], v[116:119], v[144:147], v[232:235]\n\t" \
  "v_mfma_f32_16x16x32_bf16 v[240:243], v[120:123], v[144:147], v[240:243]\n\t" \
  "ds_read_b64_tr_b16 v[144:145], v62 offset:38912\n\t" \
  "v_mfma_f32_16x16x32_bf16 v[220:223], v[108:111], v[148:151], v[220:223]\n\t" \
  "ds_read_b64_tr_b16 v[146:147], v62 offset:39424\n\t" \
  "v_mfma_f32_16x16x32_bf16 v[228:231], v[112:115], v[148:151], v[228:231]\n\t" \
  "v_mfma_f32_16x16x32_bf16 v[236:239], v[116:119], v[148:151], v[236:239]\n\t" \
  "v_mfma_f32_16x16x32_bf16 v[244:247], v[120:123], v[148:151], v[244:247]\n\t" \
  "ds_read_b64_tr_b16 v[148:149], v63 offset:38912\n\t" \
  "v_mfma_f32_16x16x32_bf16 v[152:155], v[128:131], v[108:111], v[152:155]\n\t" \
  "ds_read_b64_tr_b16 v[150:151], v63 offset:39424\n\t" \
  "v_mfma_f32_16x16x32_bf16 v[160:163], v[128:131], v[112:115], v[160:163]\n\t" \
  "v_mfma_f32_16x16x32_bf16 v[168:171], v[128:131], v[116:119], v[168:171]\n\t" \
  "v_mfma_f32_16x16x32_bf16 v[176:179], v[128:131], v[120:123], v[176:179]\n\t" \
  "ds_read_b64_tr_b16 v[128:129], v62 offset:22528\n\t" \
  "v_mfma_f32_16x16x32_bf16 v[156:159], v[132:135], v[108:111], v[156:159]\n\t" \
  "ds_read_b128 v[108:111], v61 offset:2048\n\t" \
  "v_mfma_f32_16x16x32_bf16 v[164:167], v[132:135], v[112:115], v[164:167]\n\t" \
  "ds_read_b128 v[112:115], v61 offset:4096\n\t" \
  "v_mfma_f32_16x16x32_bf16 v[172:175], v[132:135], v[116:119], v[172:175]\n\t" \
  "ds_read_b128 v[116:119], v61 offset:6144\n\t" \
  "v_mfma_f32_16x16x32_bf16 v[180:183], v[132:135], v[120:123], v[180:183]\n\t" \
  "ds_read_b128 v[120:123], v61 offset:8192\n\t" \
  "s_waitcnt lgkmcnt(5)\n\t" \
  "v_mfma_f32_16x16x32_bf16 v[248:251], v[92:95], v[80:83], v[248:251]\n\t" \
  "ds_read_b64_tr_b16 v[130:131], v62 offset:23040\n\t" \
  "v_mfma_f32_16x16x32_bf16 v[184:187], v[92:95], v[64:67], v[184:187]\n\t" \
  "ds_read_b64_tr_b16 v[132:133], v63 offset:22528\n\t" \
  "v_mfma_f32_16x16x32_bf16 v[192:195], v[92:95], v[68:71], v[192:195]\n\t" \
  "ds_read_b64_tr_b16 v[134:135], v63 offset:23040\n\t" \
  "v_mfma_f32_16x16x32_bf16 v[200:203], v[92:95], v[72:75], v[200:203]\n\t" \
  "v_mfma_f32_16x16x32_bf16 v[208:211], v[92:95], v[76:79], v[208:211]\n\t" \
  "v_mfma_f32_16x16x32_bf16 v[252:255], v[96:99], v[80:83], v[252:255]\n\t" \
  "v_mfma_f32_16x16x32_bf16 v[188:191], v[96:99], v[64:67], v[188:191]\n\t" \
  "v_mfma_f32_16x16x32_bf16 v[196:199], v[96:99], v[68:71], v[196:199]\n\t" \
  "v_mfma_f32_16x16x32_bf16 v[204:207], v[96:99], v[72:75], v[204:207]\n\t" \
  "v_mfma_f32_16x16x32_bf16 v[212:215], v[96:99], v[76:79], v[212:215]\n\t" \
  "v_mfma_f32_16x16x32_bf16 v[216:219], v[64:67], v[100:103], v[216:219]\n\t" \
  "v_mfma_f32_16x16x32_bf16 v[224:227], v[68:71], v[100:103], v[224:227]\n\t" \
  "v_mfma_f32_16x16x32_bf16 v[232:235], v[72:75], v[100:103], v[232:235]\n\t" \
  "s_waitcnt lgkmcnt(0)\n\t" \
  "s_barrier\n\t" \
  "v_mfma_f32_16x16x32_bf16 v[240:243], v[76:79], v[100:103], v[240:243]\n\t" \
  "ds_read_b64_tr_b16 v[92:93], v54 offset:26624\n\t" \
  "v_mfma_f32_16x16x32_bf16 v[220:223], v[64:67], v[104:107], v[220:223]\n\t" \
  "ds_read_b64_tr_b16 v[94:95], v54 offset:27136\n\t" \
  "v_mfma_f32_16x16x32_bf16 v[228:231], v[68:71], v[104:107], v[228:231]\n\t" \
  "ds_read_b128 v[80:83], v52 offset:0\n\t" \
  "v_mfma_f32_16x16x32_bf16 v[236:239], v[72:75], v[104:107], v[236:239]\n\t" \
  "ds_read_b64_tr_b16 v[96:97], v55 offset:26624\n\t" \
  "v_mfma_f32_16x16x32_bf16 v[244:247], v[76:79], v[104:107], v[244:247]\n\t" \
  "ds_read_b64_tr_b16 v[98:99], v55 offset:27136\n\t" \
  "505:\n\t" \
  "s_cbranch_execz 509f\n\t" \
  "v_mfma_f32_16x16x32_bf16 v[152:155], v[84:87], v[64:67], v[152:155]\n\t" \
  "ds_read_b64_tr_b16 v[100:101], v54 offset:34816\n\t" \
  "v_mfma_f32_16x16x32_bf16 v[160:163], v[84:87], v[68:71], v[160:163]\n\t" \
  "ds_read_b64_tr_b16 v[102:103], v54 offset:35328\n\t" \
  "v_mfma_f32_16x16x32_bf16 v[168:171], v[84:87], v[72:75], v[168:171]\n\t" \
  "ds_read_b64_tr_b16 v[104:105], v55 offset:34816\n\t" \
  "v_mfma_f32_16x16x32_bf16 v[176:179], v[84:87], v[76:79], v[176:179]\n\t" \
  "ds_read_b64_tr_b16 v[106:107], v55 offset:35328\n\t" \
  "v_mfma_f32_16x16x32_bf16 v[156:159], v[88:91], v[64:67], v[156:159]\n\t" \
  "ds_read_b128 v[64:67], v52 offset:2048\n\t" \
  "v_mfma_f32_16x16x32_bf16 v[164:167], v[88:91], v[68:71], v[164:167]\n\t" \
  "ds_read_b128 v[68:71], v52 offset:4096\n\t" \
  "v_mfma_f32_16x16x32_bf16 v[172:175], v[88:91], v[72:75], v[172:175]\n\t" \
  "ds_read_b128 v[72:75], v52 offset:6144\n\t" \
  "v_mfma_f32_16x16x32_bf16 v[180:183], v[88:91], v[76:79], v[180:183]\n\t" \
  "ds_read_b128 v[76:79], v52 offset:8192\n\t" \
  "v_mfma_f32_16x16x32_bf16 v[248:251], v[136:139], v[124:127], v[248:251]\n\t" \
  "ds_read_b64_tr_b16 v[84:85], v54 offset:18432\n\t" \
  "v_mfma_f32_16x16x32_bf16 v[184:187], v[136:139], v[108:111], v[184:187]\n\t" \
  "ds_read_b64_tr_b16 v[86:87], v54 offset:18944\n\t" \
  "v_mfma_f32_16x16x32_bf16 v[192:195], v[136:139], v[112:115], v[192:195]\n\t" \
  "ds_read_b64_tr_b16 v[88:89], v55 offset:18432\n\t" \
  "v_mfma_f32_16x16x32_bf16 v[200:203], v[136:139], v[116:119], v[200:203]\n\t" \
  "ds_read_b64_tr_b16 v[90:91], v55 offset:18944\n\t" \
  "v_mfma_f32_16x16x32_bf16 v[208:211], v[136:139], v[120:123], v[208:211]\n\t" \
  "ds_read_b64_tr_b16 v[136:137], v54 offset:30720\n\t" \
  "v_mfma_f32_16x16x32_bf16 v[252:255], v[140:143], v[124:127], v[252:255]\n\t" \
  "ds_read_b64_tr_b16 v[138:139], v54 offset:31232\n\t" \
  "v_mfma_f32_16x16x32_bf16 v[188:191], v[140:143], v[108:111], v[188:191]\n\t" \
  "ds_read_b128 v[124:127], v53 offset:0\n\t" \
  "v_mfma_f32_16x16x32_bf16 v[196:199], v[140:143], v[112:115], v[196:199]\n\t" \
  "v_mfma_f32_16x16x32_bf16 v[204:207], v[140:143], v[116:119], v[204:207]\n\t" \
  "v_mfma_f32_16x16x32_bf16 v[212:215], v[140:143], v[120:123], v[212:215]\n\t" \
  "ds_read_b64_tr_b16 v[140:141], v55 offset:30720\n\t" \
  "v_mfma_f32_16x16x32_bf16 v[216:219], v[108:111], v[144:147], v[216:219]\n\t" \
  "ds_read_b64_tr_b16 v[142:143], v55 offset:31232\n\t" \
  "v_mfma_f32_16x16x32_bf16 v[224:227], v[112:115], v[144:147], v[224:227]\n\t" \
  "v_mfma_f32_16x16x32_bf16 v[232:235], v[116:119], v[144:147], v[232:235]\n\t" \
  "v_mfma_f32_16x16x32_bf16 v[240:243], v[120:123], v[144:147], v[240:243]\n\t" \
  "ds_read_b64_tr_b16 v[144:145], v54 offset:38912\n\t" \
  "v_mfma_f32_16x16x32_bf16 v[220:223], v[108:111], v[148:151], v[220:223]\n\t" \
  "ds_read_b64_tr_b16 v[146:147], v54 offset:39424\n\t" \
  "v_mfma_f32_16x16x32_bf16 v[228:231], v[112:115], v[148:151], v[228:231]\n\t" \
  "v_mfma_f32_16x16x32_bf16 v[236:239], v[116:119], v[148:151], v[236:239]\n\t" \
  "v_mfma_f32_16x16x32_bf16 v[244:247], v[120:123], v[148:151], v[244:247]\n\t" \
  "ds_read_b64_tr_b16 v[148:149], v55 offset:38912\n\t" \
  "v_mfma_f32_16x16x32_bf16 v[152:155], v[128:131], v[108:111], v[152:155]\n\t" \
  "ds_read_b64_tr_b16 v[150:151], v55 offset:39424\n\t" \
  "v_mfma_f32_16x16x32_bf16 v[160:163], v[128:131], v[112:115], v[160:163]\n\t" \
  "v_mfma_f32_16x16x32_bf16 v[168:171], v[128:131], v[116:119], v[168:171]\n\t" \
  "v_mfma_f32_16x16x32_bf16 v[176:179], v[128:131], v[120:123], v[176:179]\n\t" \
  "ds_read_b64_tr_b16 v[128:129], v54 offset:22528\n\t" \
  "v_mfma_f32_16x16x32_bf16 v[156:159], v[132:135], v[108:111], v[156:159]\n\t" \
  "ds_read_b128 v[108:111], v53 offset:2048\n\t" \
  "v_mfma_f32_16x16x32_bf16 v[164:167], v[132:135], v[112:115], v[164:167]\n\t" \
  "ds_read_b128 v[112:115], v53 offset:4096\n\t" \
  "v_mfma_f32_16x16x32_bf16 v[172:175], v[132:135], v[116:119], v[172:175]\n\t" \
  "ds_read_b128 v[116:119], v53 offset:6144\n\t" \
  "v_mfma_f32_16x16x32_bf16 v[180:183], v[132:135], v[120:123], v[180:183]\n\t" \
  "ds_read_b128 v[120:123], v53 offset:8192\n\t" \
  "s_waitcnt lgkmcnt(5)\n\t" \
  "v_mfma_f32_16x16x32_bf16 v[248:251], v[92:95], v[80:83], v[248:251]\n\t" \
  "ds_read_b64_tr_b16 v[130:131], v54 offset:23040\n\t" \
  "v_mfma_f32_16x16x32_bf16 v[184:187], v[92:95], v[64:67], v[184:187]\n\t" \
  "ds_read_b64_tr_b16 v[132:133], v55 offset:22528\n\t" \
  "v_mfma_f32_16x16x32_bf16 v[192:195], v[92:95], v[68:71], v[192:195]\n\t" \
  "ds_read_b64_tr_b16 v[134:135], v55 offset:23040\n\t" \
  "v_mfma_f32_16x16x32_bf16 v[200:203], v[92:95], v[72:75], v[200:203]\n\t" \
  "v_mfma_f32_16x16x32_bf16 v[208:211], v[92:95], v[76:79], v[208:211]\n\t" \
  "v_mfma_f32_16x16x32_bf16 v[252:255], v[96:99], v[80:83], v[252:255]\n\t" \
  "v_mfma_f32_16x16x32_bf16 v[188:191], v[96:99], v[64:67], v[188:191]\n\t" \
  "v_mfma_f32_16x16x32_bf16 v[196:199], v[96:99], v[68:71], v[196:199]\n\t" \
  "v_mfma_f32_16x16x32_bf16 v[204:207], v[96:99], v[72:75], v[204:207]\n\t" \
  "v_mfma_f32_16x16x32_bf16 v[212:215], v[96:99], v[76:79], v[212:215]\n\t" \
  "v_mfma_f32_16x16x32_bf16 v[216:219], v[64:67], v[100:103], v[216:219]\n\t" \
  "v_mfma_f32_16x16x32_bf16 v[224:227], v[68:71], v[100:103], v[224:227]\n\t" \
  "v_mfma_f32_16x16x32_bf16 v[232:235], v[72:75], v[100:103], v[232:235]\n\t" \
  "s_waitcnt lgkmcnt(0)\n\t" \
  "s_barrier\n\t" \
  "v_mfma_f32_16x16x32_bf16 v[240:243], v[76:79], v[100:103], v[240:243]\n\t" \
  "ds_read_b64_tr_b16 v[92:93], v58 offset:26624\n\t" \
  "v_mfma_f32_16x16x32_bf16 v[220:223], v[64:67], v[104:107], v[220:223]\n\t" \
  "ds_read_b64_tr_b16 v[94:95], v58 offset:27136\n\t" \
  "v_mfma_f32_16x16x32_bf16 v[228:231], v[68:71], v[104:107], v[228:231]\n\t" \
  "ds_read_b128 v[80:83], v56 offset:0\n\t" \
  "v_mfma_f32_16x16x32_bf16 v[236:239], v[72:75], v[104:107], v[236:239]\n\t" \
  "ds_read_b64_tr_b16 v[96:97], v59 offset:26624\n\t" \
  "v_mfma_f32_16x16x32_bf16 v[244:247], v[76:79], v[104:107], v[244:247]\n\t" \
  "ds_read_b64_tr_b16 v[98:99], v59 offset:27136\n\t" \
  "v_mfma_f32_16x16x32_bf16 v[152:155], v[84:87], v[64:67], v[152:155]\n\t" \
  "ds_read_b64_tr_b16 v[100:101], v58 offset:34816\n\t" \
  "v_mfma_f32_16x16x32_bf16 v[160:163], v[84:87], v[68:71], v[160:163]\n\t" \
  "ds_read_b64_tr_b16 v[102:103], v58 offset:35328\n\t" \
  "v_mfma_f32_16x16x32_bf16 v[168:171], v[84:87], v[72:75], v[168:171]\n\t" \
  "ds_read_b64_tr_b16 v[104:105], v59 offset:34816\n\t" \
  "v_mfma_f32_16x16x32_bf16 v[176:179], v[84:87], v[76:79], v[176:179]\n\t" \
  "ds_read_b64_tr_b16 v[106:107], v59 offset:35328\n\t" \
  "v_mfma_f32_16x16x32_bf16 v[156:159], v[88:91], v[64:67], v[156:159]\n\t" \
  "ds_read_b128 v[64:67], v56 offset:2048\n\t" \
  "v_mfma_f32_16x16x32_bf16 v[164:167], v[88:91], v[68:71], v[164:167]\n\t" \
  "ds_read_b128 v[68:71], v56 offset:4096\n\t" \
  "v_mfma_f32_16x16x32_bf16 v[172:175], v[88:91], v[72:75], v[172:175]\n\t" \
  "ds_read_b128 v[72:75], v56 offset:6144\n\t" \
  "v_mfma_f32_16x16x32_bf16 v[180:183], v[88:91], v[76:79], v[180:183]\n\t" \
  "ds_read_b128 v[76:79], v56 offset:8192\n\t" \
  "v_mfma_f32_16x16x32_bf16 v[248:251], v[136:139], v[124:127], v[248:251]\n\t" \
  "ds_read_b64_tr_b16 v[84:85], v58 offset:18432\n\t" \
  "v_mfma_f32_16x16x32_bf16 v[184:187], v[136:139], v[108:111], v[184:187]\n\t" \
  "ds_read_b64_tr_b16 v[86:87], v58 offset:18944\n\t" \
  "v_mfma_f32_16x16x32_bf16 v[192:195], v[136:139], v[112:115], v[192:195]\n\t" \
  "ds_read_b64_tr_b16 v[88:89], v59 offset:18432\n\t" \
  "v_mfma_f32_16x16x32_bf16 v[200:203], v[136:139], v[116:119], v[200:203]\n\t" \
  "ds_read_b64_tr_b16 v[90:91], v59 offset:18944\n\t" \
  "v_mfma_f32_16x16x32_bf16 v[208:211], v[136:139], v[120:123], v[208:211]\n\t" \
  "ds_read_b64_tr_b16 v[136:137], v58 offset:30720\n\t" \
  "v_mfma_f32_16x16x32_bf16 v[252:255], v[140:143], v[124:127], v[252:255]\n\t" \
  "ds_read_b64_tr_b16 v[138:139], v58 offset:31232\n\t" \
  "v_mfma_f32_16x16x32_bf16 v[188:191], v[140:143], v[108:111], v[188:191]\n\t" \
  "ds_read_b128 v[124:127], v57 offset:0\n\t" \
  "v_mfma_f32_16x16x32_bf16 v[196:199], v[140:143], v[112:115], v[196:199]\n\t" \
  "v_mfma_f32_16x16x32_bf16 v[204:207], v[140:143], v[116:119], v[204:207]\n\t" \
  "v_mfma_f32_16x16x32_bf16 v[212:215], v[140:143], v[120:123], v[212:215]\n\t" \
  "ds_read_b64_tr_b16 v[140:141], v59 offset:30720\n\t" \
  "v_mfma_f32_16x16x32_bf16 v[216:219], v[108:111], v[144:147], v[216:219]\n\t" \
  "ds_read_b64_tr_b16 v[142:143], v59 offset:31232\n\t" \
  "v_mfma_f32_16x16x32_bf16 v[224:227], v[112:115], v[144:147], v[224:227]\n\t" \
  "v_mfma_f32_16x16x32_bf16 v[232:235], v[116:119], v[144:147], v[232:235]\n\t" \
  "v_mfma_f32_16x16x32_bf16 v[240:243], v[120:123], v[144:147], v[240:243]\n\t" \
  "ds_read_b64_tr_b16 v[144:145], v58 offset:38912\n\t" \
  "v_mfma_f32_16x16x32_bf16 v[220:223], v[108:111], v[148:151], v[220:223]\n\t" \
  "ds_read_b64_tr_b16 v[146:147], v58 offset:39424\n\t" \
  "v_mfma_f32_16x16x32_bf16 v[228:231], v[112:115], v[148:151], v[228:231]\n\t" \
  "v_mfma_f32_16x16x32_bf16 v[236:239], v[116:119], v[148:151], v[236:239]\n\t" \
  "v_mfma_f32_16x16x32_bf16 v[244:247], v[120:123], v[148:151], v[244:247]\n\t" \
  "ds_read_b64_tr_b16 v[148:149], v59 offset:38912\n\t" \
  "v_mfma_f32_16x16x32_bf16 v[152:155], v[128:131], v[108:111], v[152:155]\n\t" \
  "ds_read_b64_tr_b16 v[150:151], v59 offset:39424\n\t" \
  "v_mfma_f32_16x16x32_bf16 v[160:163], v[128:131], v[112:115], v[160:163]\n\t" \
  "v_mfma_f32_16x16x32_bf16 v[168:171], v[128:131], v[116:119], v[168:171]\n\t" \
  "v_mfma_f32_16x16x32_bf16 v[176:179], v[128:131], v[120:123], v[176:179]\n\t" \
  "ds_read_b64_tr_b16 v[128:129], v58 offset:22528\n\t" \
  "v_mfma_f32_16x16x32_bf16 v[156:159], v[132:135], v[108:111], v[156:159]\n\t" \
  "ds_read_b128 v[108:111], v57 offset:2048\n\t" \
  "v_mfma_f32_16x16x32_bf16 v[164:167], v[132:135], v[112:115], v[164:167]\n\t" \
  "ds_read_b128 v[112:115], v57 offset:4096\n\t" \
  "v_mfma_f32_16x16x32_bf16 v[172:175], v[132:135], v[116:119], v[172:175]\n\t" \
  "ds_read_b128 v[116:119], v57 offset:6144\n\t" \
  "v_mfma_f32_16x16x32_bf16 v[180:183], v[132:135], v[120:123], v[180:183]\n\t" \
  "ds_read_b128 v[120:123], v57 offset:8192\n\t" \
  "s_waitcnt lgkmcnt(5)\n\t" \
  "v_mfma_f32_16x16x32_bf16 v[248:251], v[92:95], v[80:83], v[248:251]\n\t" \
  "ds_read_b64_tr_b16 v[130:131], v58 offset:23040\n\t" \
  "v_mfma_f32_16x16x32_bf16 v[184:187], v[92:95], v[64:67], v[184:187]\n\t" \
  "v_and_b32 v14, -16, v32\n\t" \
  "ds_read_b64_tr_b16 v[132:133], v59 offset:22528\n\t" \
  "v_mfma_f32_16x16x32_bf16 v[192:195], v[92:95], v[68:71], v[192:195]\n\t" \
  "ds_read_b64_tr_b16 v[134:135], v59 offset:23040\n\t" \
  "v_mfma_f32_16x16x32_bf16 v[200:203], v[92:95], v[72:75], v[200:203]\n\t" \
  "ds_read_b128 v[10:13], v14 offset:128\n\t" \
  "v_mfma_f32_16x16x32_bf16 v[208:211], v[92:95], v[76:79], v[208:211]\n\t" \
  "v_mfma_f32_16x16x32_bf16 v[252:255], v[96:99], v[80:83], v[252:255]\n\t" \
  "v_mfma_f32_16x16x32_bf16 v[188:191], v[96:99], v[64:67], v[188:191]\n\t" \
  "v_mfma_f32_16x16x32_bf16 v[196:199], v[96:99], v[68:71], v[196:199]\n\t" \
  "v_mfma_f32_16x16x32_bf16 v[204:207], v[96:99], v[72:75], v[204:207]\n\t" \
  "v_mfma_f32_16x16x32_bf16 v[212:215], v[96:99], v[76:79], v[212:215]\n\t" \
  "s_waitcnt lgkmcnt(0)\n\t" \
  "v_and_b32 v10, v10, v11\n\t" \
  "v_and_b32 v12, v12, v13\n\t" \
  "v_mfma_f32_16x16x32_bf16 v[216:219], v[64:67], v[100:103], v[216:219]\n\t" \
  "v_and_b32 v10, v10, v12\n\t" \
  "v_mfma_f32_16x16x32_bf16 v[224:227], v[68:71], v[100:103], v[224:227]\n\t" \
  "v_readfirstlane_b32 vcc_lo, v10\n\t" \
  "v_mfma_f32_16x16x32_bf16 v[232:235], v[72:75], v[100:103], v[232:235]\n\t" \
  "ds_write_b32 v32, v0 offset:144\n\t" \
  "s_cmp_eq_u32 vcc_lo, 0\n\t" \
  "s_cbranch_scc1 15f\n\t" \
  "14:\n\t" \
  "v_mfma_f32_16x16x32_bf16 v[240:243], v[76:79], v[100:103], v[240:243]\n\t" \
  "ds_read_b64_tr_b16 v[92:93], v62 offset:26624\n\t" \
  "v_mfma_f32_16x16x32_bf16 v[220:223], v[64:67], v[104:107], v[220:223]\n\t" \
  "ds_read_b64_tr_b16 v[94:95], v62 offset:27136\n\t" \
  "v_mfma_f32_16x16x32_bf16 v[228:231], v[68:71], v[104:107], v[228:231]\n\t" \
  "ds_read_b128 v[80:83], v60 offset:0\n\t" \
  "v_mfma_f32_16x16x32_bf16 v[236:239], v[72:75], v[104:107], v[236:239]\n\t" \
  "ds_read_b64_tr_b16 v[96:97], v63 offset:26624\n\t" \
  "v_mfma_f32_16x16x32_bf16 v[244:247], v[76:79], v[104:107], v[244:247]\n\t" \
  "ds_read_b64_tr_b16 v[98:99], v63 offset:27136\n\t" \
  "v_mfma_f32_16x16x32_bf16 v[152:155], v[84:87], v[64:67], v[152:155]\n\t" \
  "ds_read_b64_tr_b16 v[100:101], v62 offset:34816\n\t" \
  "v_mfma_f32_16x16x32_bf16 v[160:163], v[84:87], v[68:71], v[160:163]\n\t" \
  "ds_read_b64_tr_b16 v[102:103], v62 offset:35328\n\t" \
  "v_mfma_f32_16x16x32_bf16 v[168:171], v[84:87], v[72:75], v[168:171]\n\t" \
  "ds_read_b64_tr_b16 v[104:105], v63 offset:34816\n\t" \
  "v_mfma_f32_16x16x32_bf16 v[176:179], v[84:87], v[76:79], v[176:179]\n\t" \
  "ds_read_b64_tr_b16 v[106:107], v63 offset:35328\n\t" \
  "v_mfma_f32_16x16x32_bf16 v[156:159], v[88:91], v[64:67], v[156:159]\n\t" \
  "ds_read_b128 v[64:67], v60 offset:2048\n\t" \
  "v_mfma_f32_16x16x32_bf16 v[164:167], v[88:91], v[68:71], v[164:167]\n\t" \
  "ds_read_b128 v[68:71], v60 offset:4096\n\t" \
  "v_mfma_f32_16x16x32_bf16 v[172:175], v[88:91], v[72:75], v[172:175]\n\t" \
  "ds_read_b128 v[72:75], v60 offset:6144\n\t" \
  "v_mfma_f32_16x16x32_bf16 v[180:183], v[88:91], v[76:79], v[180:183]\n\t" \
  "ds_read_b128 v[76:79], v60 offset:8192\n\t" \
  "v_mfma_f32_16x16x32_bf16 v[248:251], v[136:139], v[124:127], v[248:251]\n\t" \
  "ds_read_b64_tr_b16 v[84:85], v62 offset:18432\n\t" \
  "v_mfma_f32_16x16x32_bf16 v[184:187], v[136:139], v[108:111], v[184:187]\n\t" \
  "ds_read_b64_tr_b16 v[86:87], v62 offset:18944\n\t" \
  "v_mfma_f32_16x16x32_bf16 v[192:195], v[136:139], v[112:115], v[192:195]\n\t" \
  "ds_read_b64_tr_b16 v[88:89], v63 offset:18432\n\t" \
  "v_mfma_f32_16x16x32_bf16 v[200:203], v[136:139], v[116:119], v[200:203]\n\t" \
  "ds_read_b64_tr_b16 v[90:91], v63 offset:18944\n\t" \
  "v_mfma_f32_16x16x32_bf16 v[208:211], v[136:139], v[120:123], v[208:211]\n\t" \
  "ds_read_b64_tr_b16 v[136:137], v62 offset:30720\n\t" \
  "v_mfma_f32_16x16x32_bf16 v[252:255], v[140:143], v[124:127], v[252:255]\n\t" \
  "ds_read_b64_tr_b16 v[138:139], v62 offset:31232\n\t" \
  "v_mfma_f32_16x16x32_bf16 v[188:191], v[140:143], v[108:111], v[188:191]\n\t" \
  "ds_read_b128 v[124:127], v61 offset:0\n\t" \
  "v_mfma_f32_16x16x32_bf16 v[196:199], v[140:143], v[112:115], v[196:199]\n\t" \
  "v_mfma_f32_16x16x32_bf16 v[204:207], v[140:143], v[116:119], v[204:207]\n\t" \
  "v_mfma_f32_16x16x32_bf16 v[212:215], v[140:143], v[120:123], v[212:215]\n\t" \
  "ds_read_b64_tr_b16 v[140:141], v63 offset:30720\n\t" \
  "v_mfma_f32_16x16x32_bf16 v[216:219], v[108:111], v[144:147], v[216:219]\n\t" \
  "ds_read_b64_tr_b16 v[142:143], v63 offset:31232\n\t" \
  "v_mfma_f32_16x16x32_bf16 v[224:227], v[112:115], v[144:147], v[224:227]\n\t" \
  "v_mfma_f32_16x16x32_bf16 v[232:235], v[116:119], v[144:147], v[232:235]\n\t" \
  "v_mfma_f32_16x16x32_bf16 v[240:243], v[120:123], v[144:147], v[240:243]\n\t" \
  "ds_read_b64_tr_b16 v[144:145], v62 offset:38912\n\t" \
  "v_mfma_f32_16x16x32_bf16 v[220:223], v[108:111], v[148:151], v[220:223]\n\t" \
  "ds_read_b64_tr_b16 v[146:147], v62 offset:39424\n\t" \
  "v_mfma_f32_16x16x32_bf16 v[228:231], v[112:115], v[148:151], v[228:231]\n\t" \
  "v_mfma_f32_16x16x32_bf16 v[236:239], v[116:119], v[148:151], v[236:239]\n\t" \
  "v_mfma_f32_16x16x32_bf16 v[244:247], v[120:123], v[148:151], v[244:247]\n\t" \
  "ds_read_b64_tr_b16 v[148:149], v63 offset:38912\n\t" \
  "v_mfma_f32_16x16x32_bf16 v[152:155], v[128:131], v[108:111], v[152:155]\n\t" \
  "ds_read_b64_tr_b16 v[150:151], v63 offset:39424\n\t" \
  "v_mfma_f32_16x16x32_bf16 v[160:163], v[128:131], v[112:115], v[160:163]\n\t" \
  "v_mfma_f32_16x16x32_bf16 v[168:171], v[128:131], v[116:119], v[168:171]\n\t" \
  "v_mfma_f32_16x16x32_bf16 v[176:179], v[128:131], v[120:123], v[176:179]\n\t" \
  "ds_read_b64_tr_b16 v[128:129], v62 offset:22528\n\t" \
  "v_mfma_f32_16x16x32_bf16 v[156:159], v[132:135], v[108:111], v[156:159]\n\t" \
  "ds_read_b128 v[108:111], v61 offset:2048\n\t" \
  "v_mfma_f32_16x16x32_bf16 v[164:167], v[132:135], v[112:115], v[164:167]\n\t" \
  "ds_read_b128 v[112:115], v61 offset:4096\n\t" \
  "v_mfma_f32_16x16x32_bf16 v[172:175], v[132:135], v[116:119], v[172:175]\n\t" \
  "ds_read_b128 v[116:119], v61 offset:6144\n\t" \
  "v_mfma_f32_16x16x32_bf16 v[180:183], v[132:135], v[120:123], v[180:183]\n\t" \
  "ds_read_b128 v[120:123], v61 offset:8192\n\t" \
  "s_waitcnt lgkmcnt(5)\n\t" \
  "v_mfma_f32_16x16x32_bf16 v[248:251], v[92:95], v[80:83], v[248:251]\n\t" \
  "ds_read_b64_tr_b16 v[130:131], v62 offset:23040\n\t" \
  "v_mfma_f32_16x16x32_bf16 v[184:187], v[92:95], v[64:67], v[184:187]\n\t" \
  "ds_read_b64_tr_b16 v[132:133], v63 offset:22528\n\t" \
  "v_mfma_f32_16x16x32_bf16 v[192:195], v[92:95], v[68:71], v[192:195]\n\t" \
  "ds_read_b64_tr_b16 v[134:135], v63 offset:23040\n\t" \
  "v_mfma_f32_16x16x32_bf16 v[200:203], v[92:95], v[72:75], v[200:203]\n\t" \
  "v_mfma_f32_16x16x32_bf16 v[208:211], v[92:95], v[76:79], v[208:211]\n\t" \
  "v_mfma_f32_16x16x32_bf16 v[252:255], v[96:99], v[80:83], v[252:255]\n\t" \
  "v_mfma_f32_16x16x32_bf16 v[188:191], v[96:99], v[64:67], v[188:191]\n\t" \
  "v_mfma_f32_16x16x32_bf16 v[196:199], v[96:99], v[68:71], v[196:199]\n\t" \
  "v_mfma_f32_16x16x32_bf16 v[204:207], v[96:99], v[72:75], v[204:207]\n\t" \
  "v_mfma_f32_16x16x32_bf16 v[212:215], v[96:99], v[76:79], v[212:215]\n\t" \
  "v_mfma_f32_16x16x32_bf16 v[216:219], v[64:67], v[100:103], v[216:219]\n\t" \
  "v_mfma_f32_16x16x32_bf16 v[224:227], v[68:71], v[100:103], v[224:227]\n\t" \
  "v_mfma_f32_16x16x32_bf16 v[232:235], v[72:75], v[100:103], v[232:235]\n\t" \
  "s_waitcnt lgkmcnt(0)\n\t" \
  "ds_write_b32 v32, v0\n\t" \
  "v_mfma_f32_16x16x32_bf16 v[240:243], v[76:79], v[100:103], v[240:243]\n\t" \
  "v_mfma_f32_16x16x32_bf16 v[220:223], v[64:67], v[104:107], v[220:223]\n\t" \
  "v_mfma_f32_16x16x32_bf16 v[228:231], v[68:71], v[104:107], v[228:231]\n\t" \
  "v_mfma_f32_16x16x32_bf16 v[236:239], v[72:75], v[104:107], v[236:239]\n\t" \
  "v_mfma_f32_16x16x32_bf16 v[244:247], v[76:79], v[104:107], v[244:247]\n\t" \
  "v_mfma_f32_16x16x32_bf16 v[152:155], v[84:87], v[64:67], v[152:155]\n\t" \
  "v_mfma_f32_16x16x32_bf16 v[160:163], v[84:87], v[68:71], v[160:163]\n\t" \
  "v_mfma_f32_16x16x32_bf16 v[168:171], v[84:87], v[72:75], v[168:171]\n\t" \
  "v_mfma_f32_16x16x32_bf16 v[176:179], v[84:87], v[76:79], v[176:179]\n\t" \
  "v_mfma_f32_16x16x32_bf16 v[156:159], v[88:91], v[64:67], v[156:159]\n\t" \
  "v_mfma_f32_16x16x32_bf16 v[164:167], v[88:91], v[68:71], v[164:167]\n\t" \
  "v_mfma_f32_16x16x32_bf16 v[172:175], v[88:91], v[72:75], v[172:175]\n\t" \
  "v_mfma_f32_16x16x32_bf16 v[180:183], v[88:91], v[76:79], v[180:183]\n\t" \
  "v_mfma_f32_16x16x32_bf16 v[248:251], v[136:139], v[124:127], v[248:251]\n\t" \
  "v_and_b32 v56, -16, v32\n\t" \
  "v_mfma_f32_16x16x32_bf16 v[184:187], v[136:139], v[108:111], v[184:187]\n\t" \
  "v_mfma_f32_16x16x32_bf16 v[192:195], v[136:139], v[112:115], v[192:195]\n\t" \
  "v_mfma_f32_16x16x32_bf16 v[200:203], v[136:139], v[116:119], v[200:203]\n\t" \
  "v_mfma_f32_16x16x32_bf16 v[208:211], v[136:139], v[120:123], v[208:211]\n\t" \
  "v_mfma_f32_16x16x32_bf16 v[252:255], v[140:143], v[124:127], v[252:255]\n\t" \
  "v_mfma_f32_16x16x32_bf16 v[188:191], v[140:143], v[108:111], v[188:191]\n\t" \
  "v_mfma_f32_16x16x32_bf16 v[196:199], v[140:143], v[112:115], v[196:199]\n\t" \
  "v_cvt_pk_bf16_f32 v10, v248, v249\n\t" \
  "v_cvt_pk_bf16_f32 v11, v250, v251\n\t" \
  "v_mfma_f32_16x16x32_bf16 v[204:207], v[140:143], v[116:119], v[204:207]\n\t" \
  "v_cvt_pk_bf16_f32 v14, v184, v185\n\t" \
  "v_cvt_pk_bf16_f32 v15, v186, v187\n\t" \
  "v_mfma_f32_16x16x32_bf16 v[212:215], v[140:143], v[120:123], v[212:215]\n\t" \
  "v_cvt_pk_bf16_f32 v12, v252, v253\n\t" \
  "v_cvt_pk_bf16_f32 v13, v254, v255\n\t" \
  "v_mfma_f32_16x16x32_bf16 v[152:155], v[128:131], v[108:111], v[152:155]\n\t" \
  "ds_write_b128 v30, v[10:13]\n\t" \
  "ds_write_b32 v32, v0 offset:48\n\t" \
  "v_mfma_f32_16x16x32_bf16 v[160:163], v[128:131], v[112:115], v[160:163]\n\t" \
  "v_cvt_pk_bf16_f32 v16, v188, v189\n\t" \
  "v_cvt_pk_bf16_f32 v17, v190, v191\n\t" \
  "v_mfma_f32_16x16x32_bf16 v[168:171], v[128:131], v[116:119], v[168:171]\n\t" \
  "v_cvt_pk_bf16_f32 v18, v192, v193\n\t" \
  "v_cvt_pk_bf16_f32 v19, v194, v195\n\t" \
  "v_mfma_f32_16x16x32_bf16 v[176:179], v[128:131], v[120:123], v[176:179]\n\t" \
  "v_cvt_pk_bf16_f32 v20, v196, v197\n\t" \
  "v_cvt_pk_bf16_f32 v21, v198, v199\n\t" \
  "v_mfma_f32_16x16x32_bf16 v[156:159], v[132:135], v[108:111], v[156:159]\n\t" \
  "v_cvt_pk_bf16_f32 v22, v200, v201\n\t" \
  "v_cvt_pk_bf16_f32 v23, v202, v203\n\t" \
  "v_mfma_f32_16x16x32_bf16 v[164:167], v[132:135], v[112:115], v[164:167]\n\t" \
  "v_cvt_pk_bf16_f32 v24, v204, v205\n\t" \
  "v_cvt_pk_bf16_f32 v25, v206, v207\n\t" \
  "v_mfma_f32_16x16x32_bf16 v[172:175], v[132:135], v[116:119], v[172:175]\n\t" \
  "v_cvt_pk_bf16_f32 v26, v208, v209\n\t" \
  "v_cvt_pk_bf16_f32 v27, v210, v211\n\t" \
  "v_mfma_f32_16x16x32_bf16 v[180:183], v[132:135], v[120:123], v[180:183]\n\t" \
  "v_cvt_pk_bf16_f32 v28, v212, v213\n\t" \
  "v_cvt_pk_bf16_f32 v29, v214, v215\n\t" \
  "v_mfma_f32_16x16x32_bf16 v[216:219], v[108:111], v[144:147], v[216:219]\n\t" \
  "v_cvt_pk_bf16_f32 v84, v152, v153\n\t" \
  "v_cvt_pk_bf16_f32 v85, v154, v155\n\t" \
  "v_mfma_f32_16x16x32_bf16 v[224:227], v[112:115], v[144:147], v[224:227]\n\t" \
  "v_cvt_pk_bf16_f32 v86, v156, v157\n\t" \
  "v_cvt_pk_bf16_f32 v87, v158, v159\n\t" \
  "v_mfma_f32_16x16x32_bf16 v[232:235], v[116:119], v[144:147], v[232:235]\n\t" \
  "v_cvt_pk_bf16_f32 v88, v160, v161\n\t" \
  "v_cvt_pk_bf16_f32 v89, v162, v163\n\t" \
  "v_mfma_f32_16x16x32_bf16 v[240:243], v[120:123], v[144:147], v[240:243]\n\t" \
  "v_cvt_pk_bf16_f32 v90, v164, v165\n\t" \
  "v_cvt_pk_bf16_f32 v91, v166, v167\n\t" \
  "v_mfma_f32_16x16x32_bf16 v[220:223], v[108:111], v[148:151], v[220:223]\n\t" \
  "v_cvt_pk_bf16_f32 v92, v168, v169\n\t" \
  "v_cvt_pk_bf16_f32 v93, v170, v171\n\t" \
  "v_mfma_f32_16x16x32_bf16 v[228:231], v[112:115], v[148:151], v[228:231]\n\t" \
  "v_cvt_pk_bf16_f32 v94, v172, v173\n\t" \
  "v_cvt_pk_bf16_f32 v95, v174, v175\n\t" \
  "v_mfma_f32_16x16x32_bf16 v[236:239], v[116:119], v[148:151], v[236:239]\n\t" \
  "v_cvt_pk_bf16_f32 v96, v176, v177\n\t" \
  "v_cvt_pk_bf16_f32 v97, v178, v179\n\t" \
  "v_mfma_f32_16x16x32_bf16 v[244:247], v[120:123], v[148:151], v[244:247]\n\t" \
  "v_cvt_pk_bf16_f32 v98, v180, v181\n\t" \
  "v_cvt_pk_bf16_f32 v99, v182, v183\n\t" \
  "v_mfma_f32_16x16x32_bf16 v[200:203], v[22:25], v[92:95], v[34:37]\n\t" \
  "v_cvt_pk_bf16_f32 v66, v232, v233\n\t" \
  "v_cvt_pk_bf16_f32 v67, v234, v235\n\t" \
  "v_mfma_f32_16x16x32_bf16 v[208:211], v[26:29], v[96:99], v[34:37]\n\t" \
  "v_cvt_pk_bf16_f32 v76, v236, v237\n\t" \
  "v_cvt_pk_bf16_f32 v77, v238, v239\n\t" \
  "v_mfma_f32_16x16x32_bf16 v[212:215], v[22:25], v[96:99], v[38:41]\n\t" \
  "v_cvt_pk_bf16_f32 v64, v240, v241\n\t" \
  "v_cvt_pk_bf16_f32 v65, v242, v243\n\t" \
  "v_mfma_f32_16x16x32_bf16 v[204:207], v[18:21], v[92:95], v[38:41]\n\t" \
  "v_cvt_pk_bf16_f32 v74, v244, v245\n\t" \
  "v_cvt_pk_bf16_f32 v75, v246, v247\n\t" \
  "v_mfma_f32_16x16x32_bf16 v[192:195], v[18:21], v[88:91], v[34:37]\n\t" \
  "v_cvt_pk_bf16_f32 v68, v224, v225\n\t" \
  "v_cvt_pk_bf16_f32 v69, v226, v227\n\t" \
  "v_mfma_f32_16x16x32_bf16 v[196:199], v[14:17], v[88:91], v[38:41]\n\t" \
  "v_max_f32 v208, v208, v212\n\t" \
  "v_max_f32 v209, v209, v213\n\t" \
  "v_mfma_f32_16x16x32_bf16 v[184:187], v[14:17], v[84:87], v[34:37]\n\t" \
  "v_max_f32 v210, v210, v214\n\t" \
  "v_max_f32 v200, v200, v204\n\t" \
  "v_mfma_f32_16x16x32_bf16 v[188:191], v[10:13], v[84:87], v[42:45]\n\t" \
  "v_max_f32 v201, v201, v205\n\t" \
  "v_max_f32 v202, v202, v206\n\t" \
  "v_max_f32 v192, v192, v196\n\t" \
  "v_max_f32 v193, v193, v197\n\t" \
  "v_max_f32 v194, v194, v198\n\t" \
  "v_max_f32 v211, v211, v215\n\t" \
  "ds_read_b32 v52, v33 offset:48\n\t" \
  "ds_read_b64 v[72:73], v31\n\t" \
  "ds_read_b64 v[82:83], v31 offset:8\n\t" \
  "v_max3_f32 v106, v208, v209, v210\n\t" \
  "v_max_f32 v184, v184, v188\n\t" \
  "v_max_f32 v185, v185, v189\n\t" \
  "v_max_f32 v186, v186, v190\n\t" \
  "v_max_f32 v203, v203, v207\n\t" \
  "v_max3_f32 v104, v200, v201, v202\n\t" \
  "v_max_f32 v195, v195, v199\n\t" \
  "v_max3_f32 v102, v192, v193, v194\n\t" \
  "v_max_f32 v187, v187, v191\n\t" \
  "v_max3_f32 v100, v184, v185, v186\n\t" \
  "v_max_f32 v106, v106, v211\n\t" \
  "v_max_f32 v104, v104, v203\n\t" \
  "v_max_f32 v102, v102, v195\n\t" \
  "v_max_f32 v100, v100, v187\n\t" \
  "v_permlane16_swap_b32 v104, v106\n\t" \
  "v_cvt_pk_bf16_f32 v78, v228, v229\n\t" \
  "v_permlane16_swap_b32 v100, v102\n\t" \
  "v_max_f32 v104, v104, v106\n\t" \
  "v_max_f32 v100, v100, v102\n\t" \
  "v_cvt_pk_bf16_f32 v79, v230, v231\n\t" \
  "v_cvt_pk_bf16_f32 v70, v216, v217\n\t" \
  "v_permlane32_swap_b32 v100, v104\n\t" \
  "v_cvt_pk_bf16_f32 v71, v218, v219\n\t" \
  "v_max_f32 v100, v100, v104\n\t" \
  "v_mul_f32 v100, 0xbe8293ee, v100\n\t" \
  "v_mov_b32 v104, v100\n\t" \
  "v_cvt_pk_bf16_f32 v80, v220, v221\n\t" \
  "v_cvt_pk_bf16_f32 v81, v222, v223\n\t" \
  "v_permlane32_swap_b32 v100, v104\n\t" \
  "s_waitcnt lgkmcnt(0)\n\t" \
  "v_readfirstlane_b32 vcc_lo, v52\n\t" \
  "s_nop 1\n\t" \
  "s_cmp_eq_u32 vcc_lo, 0\n\t" \
  "s_cbranch_scc1 5f\n\t" \
  "4:\n\t" \
  "v_mov_b32 v106, v104\n\t" \
  "v_mov_b32 v102, v100\n\t" \
  "s_nop 0\n\t" \
  "v_permlane16_swap_b32 v104, v106\n\t" \
  "v_permlane16_swap_b32 v100, v102\n\t" \
  "v_fma_f32 v208, v208, %16, v106\n\t" \
  "v_fma_f32 v209, v209, %16, v106\n\t" \
  "v_fma_f32 v210, v210, %16, v106\n\t" \
  "v_fma_f32 v211, v211, %16, v106\n\t" \
  "v_exp_f32 v208, v208\n\t" \
  "v_exp_f32 v209, v209\n\t" \
  "v_exp_f32 v210, v210\n\t" \
  "v_exp_f32 v211, v211\n\t" \
  "v_cvt_pk_bf16_f32 v212, v208, v209\n\t" \
  "v_cvt_pk_bf16_f32 v213, v210, v211\n\t" \
  "v_bfi_b32 v214, v46, 0, v212\n\t" \
  "v_bfi_b32 v215, v47, 0, v213\n\t" \
  "v_and_b32 v212, v46, v212\n\t" \
  "v_and_b32 v213, v47, v213\n\t" \
  "v_fma_f32 v200, v200, %16, v104\n\t" \
  "v_fma_f32 v201, v201, %16, v104\n\t" \
  "v_mfma_f32_16x16x32_bf16 v[208:211], v[48:51], v[212:215], 0\n\t" \
  "v_fma_f32 v202, v202, %16, v104\n\t" \
  "v_fma_f32 v203, v203, %16, v104\n\t" \
  "v_mfma_f32_16x16x32_bf16 v[176:179], v[64:67], v[212:215], 0\n\t" \
  "v_exp_f32 v200, v200\n\t" \
  "v_mfma_f32_16x16x32_bf16 v[180:183], v[74:77], v[212:215], 0\n\t" \
  "v_exp_f32 v201, v201\n\t" \
  "v_exp_f32 v202, v202\n\t" \
  "v_exp_f32 v203, v203\n\t" \
  "v_cvt_pk_bf16_f32 v204, v200, v201\n\t" \
  "v_cvt_pk_bf16_f32 v205, v202, v203\n\t" \
  "v_bfi_b32 v206, v46, 0, v204\n\t" \
  "v_bfi_b32 v207, v47, 0, v205\n\t" \
  "v_rcp_f32 v106, v208\n\t" \
  "v_and_b32 v204, v46, v204\n\t" \
  "v_and_b32 v205, v47, v205\n\t" \
  "v_pk_mul_f32 v[176:177], v[176:177], v[106:107] op_sel_hi:[1,0]\n\t" \
  "v_pk_mul_f32 v[178:179], v[178:179], v[106:107] op_sel_hi:[1,0]\n\t" \
  "v_mfma_f32_16x16x32_bf16 v[200:203], v[48:51], v[204:207], 0\n\t" \
  "v_pk_mul_f32 v[180:181], v[180:181], v[106:107] op_sel_hi:[1,0]\n\t" \
  "v_pk_mul_f32 v[182:183], v[182:183], v[106:107] op_sel_hi:[1,0]\n\t" \
  "v_mfma_f32_16x16x32_bf16 v[168:171], v[66:69], v[204:207], 0\n\t" \
  "v_cvt_pk_bf16_f32 v240, v176, v177\n\t" \
  "v_cvt_pk_bf16_f32 v241, v178, v179\n\t" \
  "v_mfma_f32_16x16x32_bf16 v[172:175], v[76:79], v[204:207], 0\n\t" \
  "v_cvt_pk_bf16_f32 v242, v180, v181\n\t" \
  "v_cvt_pk_bf16_f32 v243, v182, v183\n\t" \
  "global_store_dwordx4 v1, v[240:243], %14 offset:0 sc1\n\t" \
  "v_fma_f32 v192, v192, %16, v102\n\t" \
  "v_fma_f32 v193, v193, %16, v102\n\t" \
  "v_fma_f32 v194, v194, %16, v102\n\t" \
  "v_fma_f32 v195, v195, %16, v102\n\t" \
  "v_exp_f32 v192, v192\n\t" \
  "v_exp_f32 v193, v193\n\t" \
  "v_exp_f32 v194, v194\n\t" \
  "v_exp_f32 v195, v195\n\t" \
  "v_cvt_pk_bf16_f32 v196, v192, v193\n\t" \
  "v_cvt_pk_bf16_f32 v197, v194, v195\n\t" \
  "v_bfi_b32 v198, v46, 0, v196\n\t" \
  "v_bfi_b32 v199, v47, 0, v197\n\t" \
  "v_rcp_f32 v104, v200\n\t" \
  "v_and_b32 v196, v46, v196\n\t" \
  "v_and_b32 v197, v47, v197\n\t" \
  "v_pk_mul_f32 v[168:169], v[168:169], v[104:105] op_sel_hi:[1,0]\n\t" \
  "v_pk_mul_f32 v[170:171], v[170:171], v[104:105] op_sel_hi:[1,0]\n\t" \
  "v_mfma_f32_16x16x32_bf16 v[192:195], v[48:51], v[196:199], 0\n\t" \
  "v_pk_mul_f32 v[172:173], v[172:173], v[104:105] op_sel_hi:[1,0]\n\t" \
  "v_pk_mul_f32 v[174:175], v[174:175], v[104:105] op_sel_hi:[1,0]\n\t" \
  "v_mfma_f32_16x16x32_bf16 v[160:163], v[68:71], v[196:199], 0\n\t" \
  "v_cvt_pk_bf16_f32 v232, v168, v169\n\t" \
  "v_cvt_pk_bf16_f32 v233, v170, v171\n\t" \
  "v_mfma_f32_16x16x32_bf16 v[164:167], v[78:81], v[196:199], 0\n\t" \
  "v_cvt_pk_bf16_f32 v234, v172, v173\n\t" \
  "v_cvt_pk_bf16_f32 v235, v174, v175\n\t" \
  "global_store_dwordx4 v1, v[232:235], %14 offset:-4096 sc1\n\t" \
  "v_fma_f32 v184, v184, %16, v100\n\t" \
  "v_fma_f32 v185, v185, %16, v100\n\t" \
  "v_fma_f32 v186, v186, %16, v100\n\t" \
  "v_fma_f32 v187, v187, %16, v100\n\t" \
  "v_exp_f32 v184, v184\n\t" \
  "v_exp_f32 v185, v185\n\t" \
  "v_exp_f32 v186, v186\n\t" \
  "v_exp_f32 v187, v187\n\t" \
  "v_cvt_pk_bf16_f32 v188, v184, v185\n\t" \
  "v_cvt_pk_bf16_f32 v189, v186, v187\n\t" \
  "v_bfi_b32 v190, v46, 0, v188\n\t" \
  "v_bfi_b32 v191, v47, 0, v189\n\t" \
  "v_rcp_f32 v102, v192\n\t" \
  "v_and_b32 v188, v46, v188\n\t" \
  "v_and_b32 v189, v47, v189\n\t" \
  "v_pk_mul_f32 v[160:161], v[160:161], v[102:103] op_sel_hi:[1,0]\n\t" \
  "v_pk_mul_f32 v[162:163], v[162:163], v[102:103] op_sel_hi:[1,0]\n\t" \
  "v_mfma_f32_16x16x32_bf16 v[184:187], v[48:51], v[188:191], 0\n\t" \
  "v_pk_mul_f32 v[164:165], v[164:165], v[102:103] op_sel_hi:[1,0]\n\t" \
  "v_pk_mul_f32 v[166:167], v[166:167], v[102:103] op_sel_hi:[1,0]\n\t" \
  "v_mfma_f32_16x16x32_bf16 v[152:155], v[70:73], v[188:191], 0\n\t" \
  "v_cvt_pk_bf16_f32 v224, v160, v161\n\t" \
  "v_cvt_pk_bf16_f32 v225, v162, v163\n\t" \
  "v_mfma_f32_16x16x32_bf16 v[156:159], v[80:83], v[188:191], 0\n\t" \
  "v_cvt_pk_bf16_f32 v226, v164, v165\n\t" \
  "v_cvt_pk_bf16_f32 v227, v166, v167\n\t" \
  "global_store_dwordx4 v4, v[224:227], %14 offset:0 sc1\n\t" \
  "v_rcp_f32 v100, v184\n\t" \
  "ds_read_b128 v[60:63], v56 offset:64\n\t" \
  "ds_read_b64_tr_b16 v[108:109], v5 offset:0\n\t" \
  "ds_read_b64_tr_b16 v[110:111], v5 offset:16\n\t" \
  "ds_read_b128 v[112:115], v6 offset:16384\n\t" \
  "ds_read_b128 v[116:119], v6 offset:15360\n\t" \
  "ds_read_b64_tr_b16 v[120:121], v5 offset:512\n\t" \
  "ds_read_b64_tr_b16 v[122:123], v5 offset:528\n\t" \
  "ds_read_b128 v[124:127], v6 offset:14336\n\t" \
  "ds_read_b64_tr_b16 v[128:129], v5 offset:1024\n\t" \
  "ds_read_b64_tr_b16 v[130:131], v5 offset:1040\n\t" \
  "ds_read_b128 v[132:135], v6 offset:13312\n\t" \
  "ds_read_b64_tr_b16 v[136:137], v5 offset:1536\n\t" \
  "ds_read_b64_tr_b16 v[138:139], v5 offset:1552\n\t" \
  "ds_read_b128 v[140:143], v6 offset:12288\n\t" \
  "ds_read_b64_tr_b16 v[144:145], v5 offset:2048\n\t" \
  "v_pk_mul_f32 v[152:153], v[152:153], v[100:101] op_sel_hi:[1,0]\n\t" \
  "v_pk_mul_f32 v[154:155], v[154:155], v[100:101] op_sel_hi:[1,0]\n\t" \
  "v_cvt_pk_bf16_f32 v216, v152, v153\n\t" \
  "v_pk_mul_f32 v[156:157], v[156:157], v[100:101] op_sel_hi:[1,0]\n\t" \
  "v_pk_mul_f32 v[158:159], v[158:159], v[100:101] op_sel_hi:[1,0]\n\t" \
  "v_cvt_pk_bf16_f32 v217, v154, v155\n\t" \
  "v_cvt_pk_bf16_f32 v218, v156, v157\n\t" \
  "v_cvt_pk_bf16_f32 v219, v158, v159\n\t" \
  "global_store_dwordx4 v4, v[216:219], %14 offset:-4096 sc1\n\t" \
  "ds_write_b32 v32, v0 offset:112\n\t" \
  "s_branch 9f\n\t" \
  "5:\n\t" \
  "ds_read_b32 v52, v33 offset:48\n\t" \
  "ds_read_b64 v[72:73], v31\n\t" \
  "ds_read_b64 v[82:83], v31 offset:8\n\t" \
  "s_waitcnt lgkmcnt(0)\n\t" \
  "v_readfirstlane_b32 vcc_lo, v52\n\t" \
  "s_nop 1\n\t" \
  "s_cmp_eq_u32 vcc_lo, 0\n\t" \
  "s_cbranch_scc1 5b\n\t" \
  "s_branch 4b\n\t" \
  "15:\n\t" \
  "ds_read_b128 v[10:13], v14 offset:128\n\t" \
  "s_waitcnt lgkmcnt(0)\n\t" \
  "v_and_b32 v10, v10, v11\n\t" \
  "v_and_b32 v12, v12, v13\n\t" \
  "v_and_b32 v10, v10, v12\n\t" \
  "s_nop 1\n\t" \
  "v_readfirstlane_b32 vcc_lo, v10\n\t" \
  "s_nop 1\n\t" \
  "s_cmp_eq_u32 vcc_lo, 0\n\t" \
  "s_cbranch_scc1 15b\n\t" \
  "s_branch 14b\n\t" \
  "9:\n\t" \
  "506:\n\t" \
  "s_cbranch_execz 510f\n\t" \
  "s_waitcnt lgkmcnt(14)\n\t" \
  "v_and_b32 v60, v60, v61\n\t" \
  "v_and_b32 v62, v62, v63\n\t" \
  "507:\n\t" \
  "s_cbranch_execz 26b\n\t" \
  "v_mov_b32 v12, %13\n\t" \
  "v_and_b32 v60, v60, v62\n\t" \
  "s_nop 0\n\t" \
  "v_readfirstlane_b32 vcc_lo, v60\n\t" \
  "s_nop 1\n\t" \
  "s_cmp_eq_u32 vcc_lo, 0\n\t" \
  "s_cbranch_scc1 7f\n\t" \
  "6:\n\t" \
  "s_memrealtime s[98:99]\n\t" \
  "s_waitcnt lgkmcnt(0)\n\t" \
  "s_waitcnt lgkmcnt(11)\n\t" \
  "v_mfma_f32_16x16x32_bf16 v[192:195], v[108:111], v[112:115], 0\n\t" \
  "ds_read_b64_tr_b16 v[146:147], v5 offset:2064\n\t" \
  "ds_read_b128 v[148:151], v6 offset:11264\n\t" \
  "s_waitcnt lgkmcnt(12)\n\t" \
  "v_mfma_f32_16x16x32_bf16 v[196:199], v[108:111], v[116:119], 0\n\t" \
  "ds_read_b64_tr_b16 v[64:65], v5 offset:2560\n\t" \
  "ds_read_b64_tr_b16 v[66:67], v5 offset:2576\n\t" \
  "s_waitcnt lgkmcnt(12)\n\t" \
  "v_mfma_f32_16x16x32_bf16 v[196:199], v[120:123], v[112:115], v[196:199]\n\t" \
  "ds_read_b128 v[68:71], v6 offset:10240\n\t" \
  "ds_read_b64_tr_b16 v[72:73], v5 offset:3072\n\t" \
  "s_waitcnt lgkmcnt(13)\n\t" \
  "v_mfma_f32_16x16x32_bf16 v[200:203], v[108:111], v[124:127], 0\n\t" \
  "ds_read_b64_tr_b16 v[74:75], v5 offset:3088\n\t" \
  "ds_read_b128 v[76:79], v6 offset:9216\n\t" \
  "v_mfma_f32_16x16x32_bf16 v[200:203], v[120:123], v[116:119], v[200:203]\n\t" \
  "ds_read_b64_tr_b16 v[80:81], v5 offset:3584\n\t" \
  "ds_read_b64_tr_b16 v[82:83], v5 offset:3600\n\t" \
  "s_waitcnt lgkmcnt(15)\n\t" \
  "v_mfma_f32_16x16x32_bf16 v[200:203], v[128:131], v[112:115], v[200:203]\n\t" \
  "ds_read_b128 v[84:87], v6 offset:0\n\t" \
  "ds_read_b128 v[88:91], v6 offset:1024\n\t" \
  "s_waitcnt lgkmcnt(15)\n\t" \
  "v_mfma_f32_16x16x32_bf16 v[204:207], v[108:111], v[132:135], 0\n\t" \
  "ds_read_b128 v[92:95], v6 offset:2048\n\t" \
  "ds_read_b128 v[96:99], v6 offset:3072\n\t" \
  "v_mfma_f32_16x16x32_bf16 v[204:207], v[120:123], v[124:127], v[204:207]\n\t" \
  "ds_read_b128 v[100:103], v6 offset:4096\n\t" \
  "ds_read_b128 v[104:107], v6 offset:5120\n\t" \
  "v_mfma_f32_16x16x32_bf16 v[204:207], v[128:131], v[116:119], v[204:207]\n\t" \
  "ds_read_b128 v[152:155], v6 offset:6144\n\t" \
  "ds_read_b128 v[156:159], v6 offset:7168\n\t" \
  "s_waitcnt lgkmcnt(14)\n\t" \
  "v_mfma_f32_16x16x32_bf16 v[204:207], v[136:139], v[112:115], v[204:207]\n\t" \
  "v_lshl_add_u32 v12, v12, 12, v4\n\t" \
  "s_lshl_b32 s88, %13, 5\n\t" \
  "v_mfma_f32_16x16x32_bf16 v[208:211], v[108:111], v[140:143], 0\n\t" \
  "v_mfma_f32_16x16x32_bf16 v[208:211], v[120:123], v[132:135], v[208:211]\n\t" \
  "v_mfma_f32_16x16x32_bf16 v[208:211], v[128:131], v[124:127], v[208:211]\n\t" \
  "s_and_b32 s88, s88, 64\n\t" \
  "v_xor_b32 v13, s88, v54\n\t" \
  "v_mfma_f32_16x16x32_bf16 v[208:211], v[136:139], v[116:119], v[208:211]\n\t" \
  "v_mfma_f32_16x16x32_bf16 v[208:211], v[144:147], v[112:115], v[208:211]\n\t" \
  "v_mfma_f32_16x16x32_bf16 v[212:215], v[108:111], v[148:151], 0\n\t" \
  "ds_read_b32 v55, v56 offset:104\n\t" \
  "v_mfma_f32_16x16x32_bf16 v[212:215], v[120:123], v[140:143], v[212:215]\n\t" \
  "v_mfma_f32_16x16x32_bf16 v[212:215], v[128:131], v[132:135], v[212:215]\n\t" \
  "v_mfma_f32_16x16x32_bf16 v[212:215], v[136:139], v[124:127], v[212:215]\n\t" \
  "v_mfma_f32_16x16x32_bf16 v[212:215], v[144:147], v[116:119], v[212:215]\n\t" \
  "v_mfma_f32_16x16x32_bf16 v[212:215], v[64:67], v[112:115], v[212:215]\n\t" \
  "s_waitcnt lgkmcnt(1)\n\t" \
  "v_mfma_f32_16x16x32_bf16 v[216:219], v[108:111], v[68:71], 0\n\t" \
  "v_mfma_f32_16x16x32_bf16 v[216:219], v[120:123], v[148:151], v[216:219]\n\t" \
  "v_mfma_f32_16x16x32_bf16 v[216:219], v[128:131], v[140:143], v[216:219]\n\t" \
  "v_mfma_f32_16x16x32_bf16 v[216:219], v[136:139], v[132:135], v[216:219]\n\t" \
  "v_mfma_f32_16x16x32_bf16 v[216:219], v[144:147], v[124:127], v[216:219]\n\t" \
  "v_mfma_f32_16x16x32_bf16 v[216:219], v[64:67], v[116:119], v[216:219]\n\t" \
  "v_mfma_f32_16x16x32_bf16 v[216:219], v[72:75], v[112:115], v[216:219]\n\t" \
  "v_mfma_f32_16x16x32_bf16 v[220:223], v[108:111], v[76:79], 0\n\t" \
  "v_mfma_f32_16x16x32_bf16 v[220:223], v[120:123], v[68:71], v[220:223]\n\t" \
  "v_mfma_f32_16x16x32_bf16 v[220:223], v[128:131], v[148:151], v[220:223]\n\t" \
  "v_mfma_f32_16x16x32_bf16 v[220:223], v[136:139], v[140:143], v[220:223]\n\t" \
  "v_mfma_f32_16x16x32_bf16 v[220:223], v[144:147], v[132:135], v[220:223]\n\t" \
  "ds_read_b64_tr_b16 v[160:161], v5 offset:128\n\t" \
  "v_mfma_f32_16x16x32_bf16 v[220:223], v[64:67], v[124:127], v[220:223]\n\t" \
  "ds_read_b64_tr_b16 v[162:163], v5 offset:144\n\t" \
  "v_mfma_f32_16x16x32_bf16 v[220:223], v[72:75], v[116:119], v[220:223]\n\t" \
  "ds_read_b128 v[164:167], v7 offset:16384\n\t" \
  "v_mfma_f32_16x16x32_bf16 v[220:223], v[80:83], v[112:115], v[220:223]\n\t" \
  "ds_read_b128 v[168:171], v7 offset:15360\n\t" \
  "v_mfma_f32_16x16x32_bf16 v[252:255], v[108:111], v[84:87], 0\n\t" \
  "ds_read_b64_tr_b16 v[172:173], v5 offset:640\n\t" \
  "v_mfma_f32_16x16x32_bf16 v[252:255], v[120:123], v[88:91], v[252:255]\n\t" \
  "ds_read_b64_tr_b16 v[174:175], v5 offset:656\n\t" \
  "v_mfma_f32_16x16x32_bf16 v[252:255], v[128:131], v[92:95], v[252:255]\n\t" \
  "ds_read_b128 v[176:179], v7 offset:14336\n\t" \
  "v_mfma_f32_16x16x32_bf16 v[252:255], v[136:139], v[96:99], v[252:255]\n\t" \
  "ds_read_b64_tr_b16 v[180:181], v5 offset:1152\n\t" \
  "v_mfma_f32_16x16x32_bf16 v[252:255], v[144:147], v[100:103], v[252:255]\n\t" \
  "ds_read_b64_tr_b16 v[182:183], v5 offset:1168\n\t" \
  "v_mfma_f32_16x16x32_bf16 v[252:255], v[64:67], v[104:107], v[252:255]\n\t" \
  "ds_read_b128 v[184:187], v7 offset:13312\n\t" \
  "v_mfma_f32_16x16x32_bf16 v[252:255], v[72:75], v[152:155], v[252:255]\n\t" \
  "ds_read_b64_tr_b16 v[188:189], v5 offset:1664\n\t" \
  "v_mfma_f32_16x16x32_bf16 v[252:255], v[80:83], v[156:159], v[252:255]\n\t" \
  "ds_read_b64_tr_b16 v[190:191], v5 offset:1680\n\t" \
  "v_mfma_f32_16x16x32_bf16 v[248:251], v[108:111], v[88:91], 0\n\t" \
  "ds_read_b128 v[76:79], v7 offset:12288\n\t" \
  "v_mfma_f32_16x16x32_bf16 v[248:251], v[120:123], v[92:95], v[248:251]\n\t" \
  "v_mfma_f32_16x16x32_bf16 v[248:251], v[128:131], v[96:99], v[248:251]\n\t" \
  "ds_read_b64_tr_b16 v[68:69], v5 offset:2176\n\t" \
  "v_mfma_f32_16x16x32_bf16 v[248:251], v[136:139], v[100:103], v[248:251]\n\t" \
  "ds_read_b64_tr_b16 v[70:71], v5 offset:2192\n\t" \
  "v_mfma_f32_16x16x32_bf16 v[248:251], v[144:147], v[104:107], v[248:251]\n\t" \
  "ds_read_b128 v[148:151], v7 offset:11264\n\t" \
  "v_mfma_f32_16x16x32_bf16 v[248:251], v[64:67], v[152:155], v[248:251]\n\t" \
  "v_mfma_f32_16x16x32_bf16 v[248:251], v[72:75], v[156:159], v[248:251]\n\t" \
  "v_mfma_f32_16x16x32_bf16 v[244:247], v[108:111], v[92:95], 0\n\t" \
  "v_mfma_f32_16x16x32_bf16 v[244:247], v[120:123], v[96:99], v[244:247]\n\t" \
  "ds_read_b64_tr_b16 v[140:141], v5 offset:2688\n\t" \
  "v_mfma_f32_16x16x32_bf16 v[244:247], v[128:131], v[100:103], v[244:247]\n\t" \
  "ds_read_b64_tr_b16 v[142:143], v5 offset:2704\n\t" \
  "v_mfma_f32_16x16x32_bf16 v[244:247], v[136:139], v[104:107], v[244:247]\n\t" \
  "ds_read_b128 v[132:135], v7 offset:10240\n\t" \
  "v_mfma_f32_16x16x32_bf16 v[244:247], v[144:147], v[152:155], v[244:247]\n\t" \
  "v_mfma_f32_16x16x32_bf16 v[244:247], v[64:67], v[156:159], v[244:247]\n\t" \
  "v_mfma_f32_16x16x32_bf16 v[240:243], v[108:111], v[96:99], 0\n\t" \
  "v_mfma_f32_16x16x32_bf16 v[240:243], v[120:123], v[100:103], v[240:243]\n\t" \
  "v_mov_b32 v11, 1\n\t" \
  "v_mfma_f32_16x16x32_bf16 v[240:243], v[128:131], v[104:107], v[240:243]\n\t" \
  "ds_read_b64_tr_b16 v[124:125], v5 offset:3200\n\t" \
  "v_mfma_f32_16x16x32_bf16 v[240:243], v[136:139], v[152:155], v[240:243]\n\t" \
  "s_waitcnt vmcnt(0)\n\t" \
  "s_mov_b64 exec, 1\n\t" \
  "ds_add_rtn_u32 v10, v56, v11 offset:16\n\t" \
  "s_mov_b64 exec, -1\n\t" \
  "v_mfma_f32_16x16x32_bf16 v[240:243], v[144:147], v[156:159], v[240:243]\n\t" \
  "ds_read_b64_tr_b16 v[126:127], v5 offset:3216\n\t" \
  "v_mfma_f32_16x16x32_bf16 v[236:239], v[108:111], v[100:103], 0\n\t" \
  "ds_read_b128 v[116:119], v7 offset:9216\n\t" \
  "v_mfma_f32_16x16x32_bf16 v[236:239], v[120:123], v[104:107], v[236:239]\n\t" \
  "v_bfe_u32 v11, v12, 4, 4\n\t" \
  "v_mfma_f32_16x16x32_bf16 v[236:239], v[128:131], v[152:155], v[236:239]\n\t" \
  "v_lshlrev_b32 v11, 7, v11\n\t" \
  "v_mfma_f32_16x16x32_bf16 v[236:239], v[136:139], v[156:159], v[236:239]\n\t" \
  "v_mfma_f32_16x16x32_bf16 v[232:235], v[108:111], v[104:107], 0\n\t" \
  "v_mfma_f32_16x16x32_bf16 v[232:235], v[120:123], v[152:155], v[232:235]\n\t" \
  "s_waitcnt lgkmcnt(2)\n\t" \
  "v_readfirstlane_b32 vcc_lo, v10\n\t" \
  "v_mfma_f32_16x16x32_bf16 v[232:235], v[128:131], v[156:159], v[232:235]\n\t" \
  "s_cmp_eq_u32 vcc_lo, 3\n\t" \
  "s_cbranch_scc0 23f\n\t" \
  "s_mov_b64 exec, 0xffff\n\t" \
  "global_store_dword v11, v55, %19 sc1\n\t" \
  "s_mov_b64 exec, -1\n\t" \
  "23:\n\t" \
  "v_mfma_f32_16x16x32_bf16 v[228:231], v[108:111], v[152:155], 0\n\t" \
  "ds_read_b64_tr_b16 v[64:65], v5 offset:3712\n\t" \
  "v_mfma_f32_16x16x32_bf16 v[228:231], v[120:123], v[156:159], v[228:231]\n\t" \
  "ds_read_b64_tr_b16 v[66:67], v5 offset:3728\n\t" \
  "v_mfma_f32_16x16x32_bf16 v[224:227], v[108:111], v[156:159], 0\n\t" \
  "ds_read_b128 v[72:75], v7 offset:0\n\t" \
  "v_mfma_f32_16x16x32_bf16 v[192:195], v[160:163], v[164:167], v[192:195]\n\t" \
  "ds_read_b128 v[80:83], v7 offset:1024\n\t" \
  "v_mfma_f32_16x16x32_bf16 v[196:199], v[160:163], v[168:171], v[196:199]\n\t" \
  "ds_read_b128 v[84:87], v7 offset:2048\n\t" \
  "v_mfma_f32_16x16x32_bf16 v[196:199], v[172:175], v[164:167], v[196:199]\n\t" \
  "ds_read_b128 v[88:91], v7 offset:3072\n\t" \
  "v_mfma_f32_16x16x32_bf16 v[200:203], v[160:163], v[176:179], v[200:203]\n\t" \
  "ds_read_b128 v[92:95], v7 offset:4096\n\t" \
  "v_mfma_f32_16x16x32_bf16 v[200:203], v[172:175], v[168:171], v[200:203]\n\t" \
  "ds_read_b128 v[108:111], v7 offset:5120\n\t" \
  "v_mfma_f32_16x16x32_bf16 v[200:203], v[180:183], v[164:167], v[200:203]\n\t" \
  "ds_read_b128 v[96:99], v7 offset:6144\n\t" \
  "v_mfma_f32_16x16x32_bf16 v[204:207], v[160:163], v[184:187], v[204:207]\n\t" \
  "ds_read_b128 v[100:103], v7 offset:7168\n\t" \
  "v_mfma_f32_16x16x32_bf16 v[204:207], v[172:175], v[176:179], v[204:207]\n\t" \
  "v_mfma_f32_16x16x32_bf16 v[204:207], v[180:183], v[168:171], v[204:207]\n\t" \
  "v_mfma_f32_16x16x32_bf16 v[204:207], v[188:191], v[164:167], v[204:207]\n\t" \
  "v_mfma_f32_16x16x32_bf16 v[208:211], v[160:163], v[76:79], v[208:211]\n\t" \
  "v_mfma_f32_16x16x32_bf16 v[208:211], v[172:175], v[184:187], v[208:211]\n\t" \
  "v_mfma_f32_16x16x32_bf16 v[208:211], v[180:183], v[176:179], v[208:211]\n\t" \
  "v_mfma_f32_16x16x32_bf16 v[208:211], v[188:191], v[168:171], v[208:211]\n\t" \
  "v_mfma_f32_16x16x32_bf16 v[208:211], v[68:71], v[164:167], v[208:211]\n\t" \
  "s_getpc_b64 s[70:71]\n\t" \
  "24:\n\t" \
  "s_add_u32 s70, s70, 25f-24b\n\t" \
  "s_addc_u32 s71, s71, 0\n\t" \
  "v_mfma_f32_16x16x32_bf16 v[212:215], v[160:163], v[148:151], v[212:215]\n\t" \
  "v_mfma_f32_16x16x32_bf16 v[212:215], v[172:175], v[76:79], v[212:215]\n\t" \
  "v_mfma_f32_16x16x32_bf16 v[212:215], v[180:183], v[184:187], v[212:215]\n\t" \
  "v_mfma_f32_16x16x32_bf16 v[212:215], v[188:191], v[176:179], v[212:215]\n\t" \
  "v_lshrrev_b32 v14, 1, v4\n\t" \
  "v_mfma_f32_16x16x32_bf16 v[212:215], v[68:71], v[168:171], v[212:215]\n\t" \
  "v_mfma_f32_16x16x32_bf16 v[212:215], v[140:143], v[164:167], v[212:215]\n\t" \
  "v_lshrrev_b32 v15, 24, v3\n\t" \
  "v_mfma_f32_16x16x32_bf16 v[216:219], v[160:163], v[132:135], v[216:219]\n\t" \
  "v_mfma_f32_16x16x32_bf16 v[216:219], v[172:175], v[148:151], v[216:219]\n\t" \
  "v_add_u32 v14, %20, v14\n\t" \
  "v_mfma_f32_16x16x32_bf16 v[216:219], v[180:183], v[76:79], v[216:219]\n\t" \
  "v_mfma_f32_16x16x32_bf16 v[216:219], v[188:191], v[184:187], v[216:219]\n\t" \
  "v_lshrrev_b32 v18, 10, v12\n\t" \
  "v_mfma_f32_16x16x32_bf16 v[216:219], v[68:71], v[176:179], v[216:219]\n\t" \
  "v_mfma_f32_16x16x32_bf16 v[216:219], v[140:143], v[168:171], v[216:219]\n\t" \
  "v_and_b32 v18, 12, v18\n\t" \
  "s_waitcnt lgkmcnt(0)\n\t" \
  "v_mfma_f32_16x16x32_bf16 v[216:219], v[124:127], v[164:167], v[216:219]\n\t" \
  "v_mfma_f32_16x16x32_bf16 v[220:223], v[160:163], v[116:119], v[220:223]\n\t" \
  "v_mov_b32 v17, 16\n\t" \
  "v_mfma_f32_16x16x32_bf16 v[220:223], v[172:175], v[132:135], v[220:223]\n\t" \
  "v_mfma_f32_16x16x32_bf16 v[220:223], v[180:183], v[148:151], v[220:223]\n\t" \
  "v_add_u32 v18, v56, v18\n\t" \
  "v_mfma_f32_16x16x32_bf16 v[220:223], v[188:191], v[76:79], v[220:223]\n\t" \
  "v_mfma_f32_16x16x32_bf16 v[220:223], v[68:71], v[184:187], v[220:223]\n\t" \
  "s_mov_b32 s76, 0\n\t" \
  "ds_read_b64_tr_b16 v[76:77], v5 offset:256\n\t" \
  "v_mfma_f32_16x16x32_bf16 v[220:223], v[140:143], v[176:179], v[220:223]\n\t" \
  "ds_read_b64_tr_b16 v[78:79], v5 offset:272\n\t" \
  "v_mfma_f32_16x16x32_bf16 v[220:223], v[124:127], v[168:171], v[220:223]\n\t" \
  "s_brev_b32 s77, 1\n\t" \
  "ds_read_b128 v[104:107], v8 offset:16384\n\t" \
  "v_mfma_f32_16x16x32_bf16 v[220:223], v[64:67], v[164:167], v[220:223]\n\t" \
  "ds_read_b128 v[112:115], v8 offset:15360\n\t" \
  "v_mfma_f32_16x16x32_bf16 v[252:255], v[160:163], v[72:75], v[252:255]\n\t" \
  "s_mov_b32 s75, 0\n\t" \
  "ds_read_b64_tr_b16 v[120:121], v5 offset:768\n\t" \
  "v_mfma_f32_16x16x32_bf16 v[252:255], v[172:175], v[80:83], v[252:255]\n\t" \
  "ds_read_b64_tr_b16 v[122:123], v5 offset:784\n\t" \
  "v_mfma_f32_16x16x32_bf16 v[252:255], v[180:183], v[84:87], v[252:255]\n\t" \
  "v_add_u32 v28, 22528, v13\n\t" \
  "ds_read_b128 v[116:119], v8 offset:14336\n\t" \
  "v_mfma_f32_16x16x32_bf16 v[252:255], v[188:191], v[88:91], v[252:255]\n\t" \
  "ds_read_b64_tr_b16 v[72:73], v5 offset:1280\n\t" \
  "v_mfma_f32_16x16x32_bf16 v[252:255], v[68:71], v[92:95], v[252:255]\n\t" \
  "v_xor_b32 v29, 32, v28\n\t" \
  "ds_read_b64_tr_b16 v[74:75], v5 offset:1296\n\t" \
  "v_mfma_f32_16x16x32_bf16 v[252:255], v[140:143], v[108:111], v[252:255]\n\t" \
  "ds_read_b128 v[128:131], v8 offset:13312\n\t" \
  "v_mfma_f32_16x16x32_bf16 v[252:255], v[124:127], v[96:99], v[252:255]\n\t" \
  "v_xor_b32 v30, 64, v28\n\t" \
  "ds_read_b64_tr_b16 v[136:137], v5 offset:1792\n\t" \
  "v_mfma_f32_16x16x32_bf16 v[252:255], v[64:67], v[100:103], v[252:255]\n\t" \
  "ds_read_b64_tr_b16 v[138:139], v5 offset:1808\n\t" \
  "v_mfma_f32_16x16x32_bf16 v[248:251], v[160:163], v[80:83], v[248:251]\n\t" \
  "v_xor_b32 v31, 96, v28\n\t" \
  "ds_read_b128 v[144:147], v8 offset:12288\n\t" \
  "v_mfma_f32_16x16x32_bf16 v[248:251], v[172:175], v[84:87], v[248:251]\n\t" \
  "v_mfma_f32_16x16x32_bf16 v[248:251], v[180:183], v[88:91], v[248:251]\n\t" \
  "v_add_u32 v32, 88064, v13\n\t" \
  "ds_read_b64_tr_b16 v[64:65], v5 offset:2304\n\t" \
  "v_mfma_f32_16x16x32_bf16 v[248:251], v[188:191], v[92:95], v[248:251]\n\t" \
  "ds_read_b64_tr_b16 v[66:67], v5 offset:2320\n\t" \
  "v_mfma_f32_16x16x32_bf16 v[248:251], v[68:71], v[108:111], v[248:251]\n\t" \
  "v_xor_b32 v33, 32, v32\n\t" \
  "ds_read_b128 v[80:83], v8 offset:11264\n\t" \
  "v_mfma_f32_16x16x32_bf16 v[248:251], v[140:143], v[96:99], v[248:251]\n\t" \
  "v_mfma_f32_16x16x32_bf16 v[248:251], v[124:127], v[100:103], v[248:251]\n\t" \
  "v_xor_b32 v34, 64, v32\n\t" \
  "v_mfma_f32_16x16x32_bf16 v[244:247], v[160:163], v[84:87], v[244:247]\n\t" \
  "v_mfma_f32_16x16x32_bf16 v[244:247], v[172:175], v[88:91], v[244:247]\n\t" \
  "v_xor_b32 v35, 96, v32\n\t" \
  "ds_read_b64_tr_b16 v[84:85], v5 offset:2816\n\t" \
  "v_mfma_f32_16x16x32_bf16 v[244:247], v[180:183], v[92:95], v[244:247]\n\t" \
  "ds_read_b64_tr_b16 v[86:87], v5 offset:2832\n\t" \
  "v_mfma_f32_16x16x32_bf16 v[244:247], v[188:191], v[108:111], v[244:247]\n\t" \
  "v_add_u32 v36, 0, v13\n\t" \
  "ds_read_b128 v[124:127], v8 offset:10240\n\t" \
  "v_mfma_f32_16x16x32_bf16 v[244:247], v[68:71], v[96:99], v[244:247]\n\t" \
  "v_mfma_f32_16x16x32_bf16 v[244:247], v[140:143], v[100:103], v[244:247]\n\t" \
  "v_xor_b32 v37, 32, v36\n\t" \
  "v_mfma_f32_16x16x32_bf16 v[240:243], v[160:163], v[88:91], v[240:243]\n\t" \
  "v_mfma_f32_16x16x32_bf16 v[240:243], v[172:175], v[92:95], v[240:243]\n\t" \
  "v_xor_b32 v38, 64, v36\n\t" \
  "v_mfma_f32_16x16x32_bf16 v[240:243], v[180:183], v[108:111], v[240:243]\n\t" \
  "ds_read_b64_tr_b16 v[88:89], v5 offset:3328\n\t" \
  "v_mfma_f32_16x16x32_bf16 v[240:243], v[188:191], v[96:99], v[240:243]\n\t" \
  "v_xor_b32 v39, 96, v36\n\t" \
  "ds_read_b64_tr_b16 v[90:91], v5 offset:3344\n\t" \
  "v_mfma_f32_16x16x32_bf16 v[240:243], v[68:71], v[100:103], v[240:243]\n\t" \
  "ds_read_b128 v[152:155], v8 offset:9216\n\t" \
  "v_mfma_f32_16x16x32_bf16 v[236:239], v[160:163], v[92:95], v[236:239]\n\t" \
  "v_lshrrev_b32 v57, 5, v4\n\t" \
  "v_mfma_f32_16x16x32_bf16 v[236:239], v[172:175], v[108:111], v[236:239]\n\t" \
  "v_mfma_f32_16x16x32_bf16 v[236:239], v[180:183], v[96:99], v[236:239]\n\t" \
  "v_and_b32 v57, 0x18, v57\n\t" \
  "v_mfma_f32_16x16x32_bf16 v[236:239], v[188:191], v[100:103], v[236:239]\n\t" \
  "v_mfma_f32_16x16x32_bf16 v[232:235], v[160:163], v[108:111], v[232:235]\n\t" \
  "v_and_b32 v62, 0x70, v4\n\t" \
  "v_mfma_f32_16x16x32_bf16 v[232:235], v[172:175], v[96:99], v[232:235]\n\t" \
  "ds_read_b64_tr_b16 v[68:69], v5 offset:3840\n\t" \
  "v_mfma_f32_16x16x32_bf16 v[232:235], v[180:183], v[100:103], v[232:235]\n\t" \
  "v_xor_b32 v57, v57, v62\n\t" \
  "ds_read_b64_tr_b16 v[70:71], v5 offset:3856\n\t" \
  "v_mfma_f32_16x16x32_bf16 v[228:231], v[160:163], v[96:99], v[228:231]\n\t" \
  "ds_read_b128 v[92:95], v8 offset:0\n\t" \
  "v_mfma_f32_16x16x32_bf16 v[228:231], v[172:175], v[100:103], v[228:231]\n\t" \
  "v_and_b32 v58, 0xf0, v4\n\t" \
  "ds_read_b128 v[108:111], v8 offset:1024\n\t" \
  "v_mfma_f32_16x16x32_bf16 v[224:227], v[160:163], v[100:103], v[224:227]\n\t" \
  "ds_read_b128 v[96:99], v8 offset:2048\n\t" \
  "s_waitcnt lgkmcnt(5)\n\t" \
  "v_mfma_f32_16x16x32_bf16 v[192:195], v[76:79], v[104:107], v[192:195]\n\t" \
  "v_lshl_add_u32 v57, v58, 3, v57\n\t" \
  "ds_read_b128 v[100:103], v8 offset:3072\n\t" \
  "v_mfma_f32_16x16x32_bf16 v[196:199], v[76:79], v[112:115], v[196:199]\n\t" \
  "ds_read_b128 v[156:159], v8 offset:4096\n\t" \
  "v_mfma_f32_16x16x32_bf16 v[196:199], v[120:123], v[104:107], v[196:199]\n\t" \
  "v_add_u32 v57, %23, v57\n\t" \
  "ds_read_b128 v[160:163], v8 offset:5120\n\t" \
  "v_mfma_f32_16x16x32_bf16 v[200:203], v[76:79], v[116:119], v[200:203]\n\t" \
  "ds_read_b128 v[164:167], v8 offset:6144\n\t" \
  "v_mfma_f32_16x16x32_bf16 v[200:203], v[120:123], v[112:115], v[200:203]\n\t" \
  "v_xor_b32 v58, 32, v57\n\t" \
  "ds_read_b128 v[168:171], v8 offset:7168\n\t" \
  "v_mfma_f32_16x16x32_bf16 v[200:203], v[72:75], v[104:107], v[200:203]\n\t" \
  "v_mfma_f32_16x16x32_bf16 v[204:207], v[76:79], v[128:131], v[204:207]\n\t" \
  "v_xor_b32 v59, 64, v57\n\t" \
  "v_mfma_f32_16x16x32_bf16 v[204:207], v[120:123], v[116:119], v[204:207]\n\t" \
  "v_mfma_f32_16x16x32_bf16 v[204:207], v[72:75], v[112:115], v[204:207]\n\t" \
  "v_xor_b32 v60, 96, v57\n\t" \
  "v_mfma_f32_16x16x32_bf16 v[204:207], v[136:139], v[104:107], v[204:207]\n\t" \
  "v_mfma_f32_16x16x32_bf16 v[208:211], v[76:79], v[144:147], v[208:211]\n\t" \
  "v_lshrrev_b32 v61, 3, v4\n\t" \
  "v_mfma_f32_16x16x32_bf16 v[208:211], v[120:123], v[128:131], v[208:211]\n\t" \
  "v_mfma_f32_16x16x32_bf16 v[208:211], v[72:75], v[116:119], v[208:211]\n\t" \
  "v_and_b32 v61, 0x70, v61\n\t" \
  "v_mfma_f32_16x16x32_bf16 v[208:211], v[136:139], v[112:115], v[208:211]\n\t" \
  "v_mfma_f32_16x16x32_bf16 v[208:211], v[64:67], v[104:107], v[208:211]\n\t" \
  "v_xor_b32 v61, v61, v4\n\t" \
  "v_mfma_f32_16x16x32_bf16 v[212:215], v[76:79], v[80:83], v[212:215]\n\t" \
  "v_mfma_f32_16x16x32_bf16 v[212:215], v[120:123], v[144:147], v[212:215]\n\t" \
  "v_add_u32 v61, %23, v61\n\t" \
  "v_mfma_f32_16x16x32_bf16 v[212:215], v[72:75], v[128:131], v[212:215]\n\t" \
  "v_mfma_f32_16x16x32_bf16 v[212:215], v[136:139], v[116:119], v[212:215]\n\t" \
  "v_and_b32 v63, 0x380, v4\n\t" \
  "v_mfma_f32_16x16x32_bf16 v[212:215], v[64:67], v[112:115], v[212:215]\n\t" \
  "v_mfma_f32_16x16x32_bf16 v[212:215], v[84:87], v[104:107], v[212:215]\n\t" \
  "v_lshl_add_u32 v62, v63, 6, v62\n\t" \
  "v_mfma_f32_16x16x32_bf16 v[216:219], v[76:79], v[124:127], v[216:219]\n\t" \
  "v_mfma_f32_16x16x32_bf16 v[216:219], v[120:123], v[80:83], v[216:219]\n\t" \
  "v_add_u32 v63, 0x10000, v62\n\t" \
  "v_mfma_f32_16x16x32_bf16 v[216:219], v[72:75], v[144:147], v[216:219]\n\t" \
  "v_mfma_f32_16x16x32_bf16 v[216:219], v[136:139], v[128:131], v[216:219]\n\t" \
  "v_mfma_f32_16x16x32_bf16 v[216:219], v[64:67], v[116:119], v[216:219]\n\t" \
  "v_mfma_f32_16x16x32_bf16 v[216:219], v[84:87], v[112:115], v[216:219]\n\t" \
  "v_mfma_f32_16x16x32_bf16 v[216:219], v[88:91], v[104:107], v[216:219]\n\t" \
  "v_mfma_f32_16x16x32_bf16 v[220:223], v[76:79], v[152:155], v[220:223]\n\t" \
  "v_mfma_f32_16x16x32_bf16 v[220:223], v[120:123], v[124:127], v[220:223]\n\t" \
  "v_mfma_f32_16x16x32_bf16 v[220:223], v[72:75], v[80:83], v[220:223]\n\t" \
  "v_mfma_f32_16x16x32_bf16 v[220:223], v[136:139], v[144:147], v[220:223]\n\t" \
  "v_mfma_f32_16x16x32_bf16 v[220:223], v[64:67], v[128:131], v[220:223]\n\t" \
  "ds_read_b64_tr_b16 v[172:173], v5 offset:384\n\t" \
  "v_mfma_f32_16x16x32_bf16 v[220:223], v[84:87], v[116:119], v[220:223]\n\t" \
  "ds_read_b64_tr_b16 v[174:175], v5 offset:400\n\t" \
  "v_mfma_f32_16x16x32_bf16 v[220:223], v[88:91], v[112:115], v[220:223]\n\t" \
  "ds_read_b128 v[80:83], v9 offset:9216\n\t" \
  "s_waitcnt lgkmcnt(3)\n\t" \
  "v_mfma_f32_16x16x32_bf16 v[220:223], v[68:71], v[104:107], v[220:223]\n\t" \
  "ds_read_b64_tr_b16 v[176:177], v5 offset:896\n\t" \
  "v_mfma_f32_16x16x32_bf16 v[252:255], v[76:79], v[92:95], v[252:255]\n\t" \
  "ds_read_b64_tr_b16 v[178:179], v5 offset:912\n\t" \
  "v_mfma_f32_16x16x32_bf16 v[252:255], v[120:123], v[108:111], v[252:255]\n\t" \
  "ds_read_b128 v[116:119], v9 offset:10240\n\t" \
  "v_mfma_f32_16x16x32_bf16 v[252:255], v[72:75], v[96:99], v[252:255]\n\t" \
  "ds_read_b64_tr_b16 v[180:181], v5 offset:1408\n\t" \
  "v_mfma_f32_16x16x32_bf16 v[252:255], v[136:139], v[100:103], v[252:255]\n\t" \
  "ds_read_b64_tr_b16 v[182:183], v5 offset:1424\n\t" \
  "v_mfma_f32_16x16x32_bf16 v[252:255], v[64:67], v[156:159], v[252:255]\n\t" \
  "ds_read_b128 v[112:115], v9 offset:11264\n\t" \
  "v_mfma_f32_16x16x32_bf16 v[252:255], v[84:87], v[160:163], v[252:255]\n\t" \
  "ds_read_b64_tr_b16 v[184:185], v5 offset:1920\n\t" \
  "v_mfma_f32_16x16x32_bf16 v[252:255], v[88:91], v[164:167], v[252:255]\n\t" \
  "ds_read_b64_tr_b16 v[186:187], v5 offset:1936\n\t" \
  "v_mfma_f32_16x16x32_bf16 v[252:255], v[68:71], v[168:171], v[252:255]\n\t" \
  "ds_read_b128 v[104:107], v9 offset:12288\n\t" \
  "v_mfma_f32_16x16x32_bf16 v[248:251], v[76:79], v[108:111], v[248:251]\n\t" \
  "ds_read_b64_tr_b16 v[188:189], v5 offset:2432\n\t" \
  "v_mfma_f32_16x16x32_bf16 v[248:251], v[120:123], v[96:99], v[248:251]\n\t" \
  "ds_read_b64_tr_b16 v[190:191], v5 offset:2448\n\t" \
  "v_mfma_f32_16x16x32_bf16 v[248:251], v[72:75], v[100:103], v[248:251]\n\t" \
  "ds_read_b128 v[124:127], v9 offset:13312\n\t" \
  "v_mfma_f32_16x16x32_bf16 v[248:251], v[136:139], v[156:159], v[248:251]\n\t" \
  "ds_read_b64_tr_b16 v[148:149], v5 offset:2944\n\t" \
  "v_mfma_f32_16x16x32_bf16 v[248:251], v[64:67], v[160:163], v[248:251]\n\t" \
  "ds_read_b64_tr_b16 v[150:151], v5 offset:2960\n\t" \
  "v_mfma_f32_16x16x32_bf16 v[248:251], v[84:87], v[164:167], v[248:251]\n\t" \
  "ds_read_b128 v[140:143], v9 offset:14336\n\t" \
  "v_mfma_f32_16x16x32_bf16 v[248:251], v[88:91], v[168:171], v[248:251]\n\t" \
  "ds_read_b64_tr_b16 v[132:133], v5 offset:3456\n\t" \
  "v_mfma_f32_16x16x32_bf16 v[244:247], v[76:79], v[96:99], v[244:247]\n\t" \
  "ds_read_b64_tr_b16 v[134:135], v5 offset:3472\n\t" \
  "v_mfma_f32_16x16x32_bf16 v[244:247], v[120:123], v[100:103], v[244:247]\n\t" \
  "ds_read_b128 v[128:131], v9 offset:15360\n\t" \
  "v_mfma_f32_16x16x32_bf16 v[244:247], v[72:75], v[156:159], v[244:247]\n\t" \
  "ds_read_b64_tr_b16 v[144:145], v5 offset:3968\n\t" \
  "v_mfma_f32_16x16x32_bf16 v[244:247], v[136:139], v[160:163], v[244:247]\n\t" \
  "ds_read_b64_tr_b16 v[146:147], v5 offset:3984\n\t" \
  "v_mfma_f32_16x16x32_bf16 v[244:247], v[64:67], v[164:167], v[244:247]\n\t" \
  "ds_read_b128 v[152:155], v9 offset:16384\n\t" \
  "v_mfma_f32_16x16x32_bf16 v[244:247], v[84:87], v[168:171], v[244:247]\n\t" \
  "v_mfma_f32_16x16x32_bf16 v[240:243], v[76:79], v[100:103], v[240:243]\n\t" \
  "v_mfma_f32_16x16x32_bf16 v[240:243], v[120:123], v[156:159], v[240:243]\n\t" \
  "v_mfma_f32_16x16x32_bf16 v[240:243], v[72:75], v[160:163], v[240:243]\n\t" \
  "v_mfma_f32_16x16x32_bf16 v[240:243], v[136:139], v[164:167], v[240:243]\n\t" \
  "v_mfma_f32_16x16x32_bf16 v[240:243], v[64:67], v[168:171], v[240:243]\n\t" \
  "v_mfma_f32_16x16x32_bf16 v[236:239], v[76:79], v[156:159], v[236:239]\n\t" \
  "v_mfma_f32_16x16x32_bf16 v[236:239], v[120:123], v[160:163], v[236:239]\n\t" \
  "v_mfma_f32_16x16x32_bf16 v[236:239], v[72:75], v[164:167], v[236:239]\n\t" \
  "v_mfma_f32_16x16x32_bf16 v[236:239], v[136:139], v[168:171], v[236:239]\n\t" \
  "v_mfma_f32_16x16x32_bf16 v[232:235], v[76:79], v[160:163], v[232:235]\n\t" \
  "v_mfma_f32_16x16x32_bf16 v[232:235], v[120:123], v[164:167], v[232:235]\n\t" \
  "v_mfma_f32_16x16x32_bf16 v[232:235], v[72:75], v[168:171], v[232:235]\n\t" \
  "ds_read_b32 v16, v56 offset:96\n\t" \
  "ds_read_b128 v[136:139], v9 offset:7168\n\t" \
  "v_mfma_f32_16x16x32_bf16 v[228:231], v[76:79], v[164:167], v[228:231]\n\t" \
  "ds_read_b128 v[156:159], v9 offset:6144\n\t" \
  "v_mfma_f32_16x16x32_bf16 v[228:231], v[120:123], v[168:171], v[228:231]\n\t" \
  "v_mfma_f32_16x16x32_bf16 v[224:227], v[76:79], v[168:171], v[224:227]\n\t" \
  "ds_read_b128 v[160:163], v9 offset:5120\n\t" \
  "s_waitcnt lgkmcnt(4)\n\t" \
  "v_mfma_f32_16x16x32_bf16 v[220:223], v[172:175], v[80:83], v[220:223]\n\t" \
  "v_mfma_f32_16x16x32_bf16 v[220:223], v[176:179], v[116:119], v[220:223]\n\t" \
  "v_mfma_f32_16x16x32_bf16 v[220:223], v[180:183], v[112:115], v[220:223]\n\t" \
  "ds_read_b128 v[164:167], v9 offset:4096\n\t" \
  "v_mfma_f32_16x16x32_bf16 v[220:223], v[184:187], v[104:107], v[220:223]\n\t" \
  "s_waitcnt lgkmcnt(4)\n\t" \
  "v_readfirstlane_b32 vcc_lo, v16\n\t" \
  "v_mfma_f32_16x16x32_bf16 v[220:223], v[188:191], v[124:127], v[220:223]\n\t" \
  "s_cmp_eq_u32 vcc_lo, 0\n\t" \
  "s_cbranch_scc1 30f\n\t" \
  "s_lshr_b64 s[90:91], %18, s75\n\t" \
  "s_and_b32 s90, s90, 15\n\t" \
  "s_lshl_b32 s90, s90, 14\n\t" \
  "s_add_u32 s90, s90, s92\n\t" \
  "s_addc_u32 s91, s93, 0\n\t" \
  "s_add_u32 s75, s75, 4\n\t" \
  "s_swappc_b64 s[68:69], s[70:71]\n\t" \
  "30:\n\t" \
  "v_mfma_f32_16x16x32_bf16 v[220:223], v[148:151], v[140:143], v[220:223]\n\t" \
  "v_mfma_f32_16x16x32_bf16 v[220:223], v[132:135], v[128:131], v[220:223]\n\t" \
  "ds_read_b128 v[168:171], v9 offset:3072\n\t" \
  "v_mfma_f32_16x16x32_bf16 v[220:223], v[144:147], v[152:155], v[220:223]\n\t" \
  "v_mfma_f32_16x16x32_bf16 v[216:219], v[172:175], v[116:119], v[216:219]\n\t" \
  "v_mfma_f32_16x16x32_bf16 v[216:219], v[176:179], v[112:115], v[216:219]\n\t" \
  "v_mfma_f32_16x16x32_bf16 v[216:219], v[180:183], v[104:107], v[216:219]\n\t" \
  "ds_read_b32 v16, v56 offset:96\n\t" \
  "v_mfma_f32_16x16x32_bf16 v[216:219], v[184:187], v[124:127], v[216:219]\n\t" \
  "s_nop 2\n\t" \
  "v_add_f32 v253, v253, v220\n\t" \
  "v_add_f32 v254, v254, v221\n\t" \
  "v_mfma_f32_16x16x32_bf16 v[216:219], v[188:191], v[140:143], v[216:219]\n\t" \
  "v_add_f32 v255, v255, v222\n\t" \
  "v_mfma_f32_16x16x32_bf16 v[216:219], v[148:151], v[128:131], v[216:219]\n\t" \
  "v_mfma_f32_16x16x32_bf16 v[216:219], v[132:135], v[152:155], v[216:219]\n\t" \
  "v_mfma_f32_16x16x32_bf16 v[212:215], v[172:175], v[112:115], v[212:215]\n\t" \
  "v_mfma_f32_16x16x32_bf16 v[212:215], v[176:179], v[104:107], v[212:215]\n\t" \
  "v_mfma_f32_16x16x32_bf16 v[212:215], v[180:183], v[124:127], v[212:215]\n\t" \
  "s_waitcnt lgkmcnt(0)\n\t" \
  "v_readfirstlane_b32 vcc_lo, v16\n\t" \
  "v_mfma_f32_16x16x32_bf16 v[212:215], v[184:187], v[140:143], v[212:215]\n\t" \
  "s_cmp_eq_u32 vcc_lo, 0\n\t" \
  "s_cbranch_scc1 31f\n\t" \
  "s_lshr_b64 s[90:91], %18, s75\n\t" \
  "s_and_b32 s90, s90, 15\n\t" \
  "s_lshl_b32 s90, s90, 14\n\t" \
  "s_add_u32 s90, s90, s92\n\t" \
  "s_addc_u32 s91, s93, 0\n\t" \
  "s_add_u32 s75, s75, 4\n\t" \
  "s_swappc_b64 s[68:69], s[70:71]\n\t" \
  "31:\n\t" \
  "v_mfma_f32_16x16x32_bf16 v[212:215], v[188:191], v[128:131], v[212:215]\n\t" \
  "v_add_f32 v249, v249, v216\n\t" \
  "v_add_f32 v250, v250, v217\n\t" \
  "v_mfma_f32_16x16x32_bf16 v[212:215], v[148:151], v[152:155], v[212:215]\n\t" \
  "v_add_f32 v251, v251, v218\n\t" \
  "v_cndmask_b32 v27, v223, v219, s[76:77]\n\t" \
  "v_mfma_f32_16x16x32_bf16 v[208:211], v[172:175], v[104:107], v[208:211]\n\t" \
  "ds_bpermute_b32 v27, v15, v27\n\t" \
  "v_mfma_f32_16x16x32_bf16 v[208:211], v[176:179], v[124:127], v[208:211]\n\t" \
  "v_mfma_f32_16x16x32_bf16 v[208:211], v[180:183], v[140:143], v[208:211]\n\t" \
  "v_mfma_f32_16x16x32_bf16 v[208:211], v[184:187], v[128:131], v[208:211]\n\t" \
  "s_nop 0\n\t" \
  "v_add_f32 v245, v245, v212\n\t" \
  "v_add_f32 v246, v246, v213\n\t" \
  "v_mfma_f32_16x16x32_bf16 v[208:211], v[188:191], v[152:155], v[208:211]\n\t" \
  "v_add_f32 v247, v247, v214\n\t" \
  "v_cndmask_b32 v26, v219, v215, s[76:77]\n\t" \
  "v_mfma_f32_16x16x32_bf16 v[204:207], v[172:175], v[124:127], v[204:207]\n\t" \
  "ds_read_b32 v16, v56 offset:96\n\t" \
  "ds_bpermute_b32 v26, v15, v26\n\t" \
  "s_waitcnt lgkmcnt(2)\n\t" \
  "v_add_f32 v252, v252, v27\n\t" \
  "v_mfma_f32_16x16x32_bf16 v[204:207], v[176:179], v[140:143], v[204:207]\n\t" \
  "v_mfma_f32_16x16x32_bf16 v[204:207], v[180:183], v[128:131], v[204:207]\n\t" \
  "v_mfma_f32_16x16x32_bf16 v[204:207], v[184:187], v[152:155], v[204:207]\n\t" \
  "v_cndmask_b32 v25, v215, v211, s[76:77]\n\t" \
  "ds_bpermute_b32 v25, v15, v25\n\t" \
  "v_mfma_f32_16x16x32_bf16 v[200:203], v[172:175], v[140:143], v[200:203]\n\t" \
  "ds_read_b128 v[140:143], v9 offset:2048\n\t" \
  "s_waitcnt lgkmcnt(2)\n\t" \
  "v_add_f32 v248, v248, v26\n\t" \
  "v_mfma_f32_16x16x32_bf16 v[200:203], v[176:179], v[128:131], v[200:203]\n\t" \
  "v_mfma_f32_16x16x32_bf16 v[200:203], v[180:183], v[152:155], v[200:203]\n\t" \
  "v_mfma_f32_16x16x32_bf16 v[196:199], v[172:175], v[128:131], v[196:199]\n\t" \
  "v_readfirstlane_b32 vcc_lo, v16\n\t" \
  "ds_read_b128 v[128:131], v9 offset:1024\n\t" \
  "v_cndmask_b32 v24, v211, v207, s[76:77]\n\t" \
  "v_mfma_f32_16x16x32_bf16 v[196:199], v[176:179], v[152:155], v[196:199]\n\t" \
  "s_cmp_eq_u32 vcc_lo, 0\n\t" \
  "s_cbranch_scc1 32f\n\t" \
  "s_lshr_b64 s[90:91], %18, s75\n\t" \
  "s_and_b32 s90, s90, 15\n\t" \
  "s_lshl_b32 s90, s90, 14\n\t" \
  "s_add_u32 s90, s90, s92\n\t" \
  "s_addc_u32 s91, s93, 0\n\t" \
  "s_add_u32 s75, s75, 4\n\t" \
  "s_swappc_b64 s[68:69], s[70:71]\n\t" \
  "32:\n\t" \
  "v_mfma_f32_16x16x32_bf16 v[192:195], v[172:175], v[152:155], v[192:195]\n\t" \
  "ds_read_b128 v[152:155], v9 offset:0\n\t" \
  "ds_bpermute_b32 v24, v15, v24\n\t" \
  "s_waitcnt lgkmcnt(4)\n\t" \
  "v_add_f32 v244, v244, v25\n\t" \
  "v_mfma_f32_16x16x32_bf16 v[224:227], v[172:175], v[136:139], v[224:227]\n\t" \
  "v_cndmask_b32 v23, v207, v203, s[76:77]\n\t" \
  "ds_bpermute_b32 v23, v15, v23\n\t" \
  "v_mfma_f32_16x16x32_bf16 v[228:231], v[172:175], v[156:159], v[228:231]\n\t" \
  "v_mfma_f32_16x16x32_bf16 v[228:231], v[176:179], v[136:139], v[228:231]\n\t" \
  "v_cndmask_b32 v22, v203, v199, s[76:77]\n\t" \
  "ds_bpermute_b32 v22, v15, v22\n\t" \
  "v_mfma_f32_16x16x32_bf16 v[232:235], v[172:175], v[160:163], v[232:235]\n\t" \
  "v_cndmask_b32 v20, v195, 0, s[76:77]\n\t" \
  "ds_bpermute_b32 v20, v15, v20\n\t" \
  "v_mfma_f32_16x16x32_bf16 v[232:235], v[176:179], v[156:159], v[232:235]\n\t" \
  "v_cndmask_b32 v21, v199, v195, s[76:77]\n\t" \
  "ds_bpermute_b32 v21, v15, v21\n\t" \
  "v_mfma_f32_16x16x32_bf16 v[232:235], v[180:183], v[136:139], v[232:235]\n\t" \
  "v_add_f32 v225, v225, v192\n\t" \
  "v_add_f32 v226, v226, v193\n\t" \
  "v_mfma_f32_16x16x32_bf16 v[236:239], v[172:175], v[164:167], v[236:239]\n\t" \
  "ds_read_b32 v16, v56 offset:96\n\t" \
  "v_add_f32 v227, v227, v194\n\t" \
  "v_add_f32 v229, v229, v196\n\t" \
  "v_mfma_f32_16x16x32_bf16 v[236:239], v[176:179], v[160:163], v[236:239]\n\t" \
  "v_add_f32 v230, v230, v197\n\t" \
  "v_add_f32 v231, v231, v198\n\t" \
  "v_mfma_f32_16x16x32_bf16 v[236:239], v[180:183], v[156:159], v[236:239]\n\t" \
  "v_mfma_f32_16x16x32_bf16 v[236:239], v[184:187], v[136:139], v[236:239]\n\t" \
  "s_waitcnt lgkmcnt(2)\n\t" \
  "v_add_f32 v224, v224, v20\n\t" \
  "v_cvt_pk_bf16_f32 v45, v226, v227\n\t" \
  "v_mfma_f32_16x16x32_bf16 v[240:243], v[172:175], v[168:171], v[240:243]\n\t" \
  "v_cvt_pk_bf16_f32 v44, v224, v225\n\t" \
  "ds_write_b64 v14, v[44:45] offset:0\n\t" \
  "v_mfma_f32_16x16x32_bf16 v[240:243], v[176:179], v[164:167], v[240:243]\n\t" \
  "s_waitcnt lgkmcnt(2)\n\t" \
  "v_add_f32 v228, v228, v21\n\t" \
  "v_cvt_pk_bf16_f32 v47, v230, v231\n\t" \
  "v_mfma_f32_16x16x32_bf16 v[240:243], v[180:183], v[160:163], v[240:243]\n\t" \
  "v_cvt_pk_bf16_f32 v46, v228, v229\n\t" \
  "ds_write_b64 v14, v[46:47] offset:512\n\t" \
  "v_mfma_f32_16x16x32_bf16 v[240:243], v[184:187], v[156:159], v[240:243]\n\t" \
  "s_waitcnt lgkmcnt(2)\n\t" \
  "v_readfirstlane_b32 vcc_lo, v16\n\t" \
  "v_add_f32 v233, v233, v200\n\t" \
  "v_mfma_f32_16x16x32_bf16 v[240:243], v[188:191], v[136:139], v[240:243]\n\t" \
  "s_cmp_eq_u32 vcc_lo, 0\n\t" \
  "s_cbranch_scc1 33f\n\t" \
  "s_lshr_b64 s[90:91], %18, s75\n\t" \
  "s_and_b32 s90, s90, 15\n\t" \
  "s_lshl_b32 s90, s90, 14\n\t" \
  "s_add_u32 s90, s90, s92\n\t" \
  "s_addc_u32 s91, s93, 0\n\t" \
  "s_add_u32 s75, s75, 4\n\t" \
  "s_swappc_b64 s[68:69], s[70:71]\n\t" \
  "33:\n\t" \
  "v_mfma_f32_16x16x32_bf16 v[244:247], v[172:175], v[140:143], v[244:247]\n\t" \
  "ds_read_b128 v[40:43], v56 offset:80\n\t" \
  "v_add_f32 v234, v234, v201\n\t" \
  "v_add_f32 v235, v235, v202\n\t" \
  "v_mfma_f32_16x16x32_bf16 v[244:247], v[176:179], v[168:171], v[244:247]\n\t" \
  "v_add_f32 v232, v232, v22\n\t" \
  "v_cvt_pk_bf16_f32 v49, v234, v235\n\t" \
  "v_mfma_f32_16x16x32_bf16 v[244:247], v[180:183], v[164:167], v[244:247]\n\t" \
  "v_cvt_pk_bf16_f32 v48, v232, v233\n\t" \
  "ds_write_b64 v14, v[48:49] offset:1024\n\t" \
  "v_mfma_f32_16x16x32_bf16 v[244:247], v[184:187], v[160:163], v[244:247]\n\t" \
  "v_add_f32 v237, v237, v204\n\t" \
  "v_add_f32 v238, v238, v205\n\t" \
  "v_mfma_f32_16x16x32_bf16 v[244:247], v[188:191], v[156:159], v[244:247]\n\t" \
  "v_add_f32 v239, v239, v206\n\t" \
  "v_add_f32 v236, v236, v23\n\t" \
  "v_mfma_f32_16x16x32_bf16 v[244:247], v[148:151], v[136:139], v[244:247]\n\t" \
  "ds_read_b32 v16, v56 offset:96\n\t" \
  "v_cvt_pk_bf16_f32 v51, v238, v239\n\t" \
  "v_cvt_pk_bf16_f32 v50, v236, v237\n\t" \
  "v_mfma_f32_16x16x32_bf16 v[248:251], v[172:175], v[128:131], v[248:251]\n\t" \
  "508:\n\t" \
  "s_cbranch_execz 26b\n\t" \
  "ds_write_b64 v14, v[50:51] offset:1536\n\t" \
  "v_add_f32 v241, v241, v208\n\t" \
  "v_mfma_f32_16x16x32_bf16 v[248:251], v[176:179], v[140:143], v[248:251]\n\t" \
  "s_waitcnt lgkmcnt(3)\n\t" \
  "v_min_u32 v40, v40, v41\n\t" \
  "v_min3_u32 v40, v40, v42, v43\n\t" \
  "v_mfma_f32_16x16x32_bf16 v[248:251], v[180:183], v[168:171], v[248:251]\n\t" \
  "v_readfirstlane_b32 vcc_lo, v40\n\t" \
  "v_add_f32 v242, v242, v209\n\t" \
  "v_add_f32 v243, v243, v210\n\t" \
  "v_mfma_f32_16x16x32_bf16 v[248:251], v[184:187], v[164:167], v[248:251]\n\t" \
  "s_cmp_ge_u32 vcc_lo, 13\n\t" \
  "s_cbranch_scc0 35f\n\t" \
  "36:\n\t" \
  "v_mfma_f32_16x16x32_bf16 v[248:251], v[188:191], v[160:163], v[248:251]\n\t" \
  "v_add_f32 v240, v240, v24\n\t" \
  "v_cvt_pk_bf16_f32 v45, v242, v243\n\t" \
  "ds_read_b64_tr_b16 v[192:193], v28 offset:0\n\t" \
  "v_mfma_f32_16x16x32_bf16 v[248:251], v[148:151], v[156:159], v[248:251]\n\t" \
  "v_cvt_pk_bf16_f32 v44, v240, v241\n\t" \
  "ds_write_b64 v14, v[44:45] offset:2048\n\t" \
  "ds_read_b64_tr_b16 v[194:195], v28 offset:512\n\t" \
  "v_mfma_f32_16x16x32_bf16 v[248:251], v[132:135], v[136:139], v[248:251]\n\t" \
  "s_waitcnt lgkmcnt(4)\n\t" \
  "v_readfirstlane_b32 vcc_lo, v16\n\t" \
  "v_cvt_pk_bf16_f32 v47, v246, v247\n\t" \
  "v_mfma_f32_16x16x32_bf16 v[252:255], v[172:175], v[152:155], v[252:255]\n\t" \
  "s_cmp_eq_u32 vcc_lo, 0\n\t" \
  "s_cbranch_scc1 34f\n\t" \
  "s_cmp_ge_u32 s75, 16\n\t" \
  "s_cbranch_scc1 34f\n\t" \
  "s_lshr_b64 s[90:91], %18, s75\n\t" \
  "s_and_b32 s90, s90, 15\n\t" \
  "s_lshl_b32 s90, s90, 14\n\t" \
  "s_add_u32 s90, s90, s92\n\t" \
  "s_addc_u32 s91, s93, 0\n\t" \
  "s_add_u32 s75, s75, 4\n\t" \
  "s_swappc_b64 s[68:69], s[70:71]\n\t" \
  "34:\n\t" \
  "v_mfma_f32_16x16x32_bf16 v[252:255], v[176:179], v[128:131], v[252:255]\n\t" \
  "v_cvt_pk_bf16_f32 v46, v244, v245\n\t" \
  "ds_write_b64 v14, v[46:47] offset:2560\n\t" \
  "ds_read_b64_tr_b16 v[196:197], v28 offset:4096\n\t" \
  "v_mfma_f32_16x16x32_bf16 v[252:255], v[180:183], v[140:143], v[252:255]\n\t" \
  "ds_read_b64_tr_b16 v[198:199], v28 offset:4608\n\t" \
  "v_mfma_f32_16x16x32_bf16 v[252:255], v[184:187], v[168:171], v[252:255]\n\t" \
  "v_cvt_pk_bf16_f32 v49, v250, v251\n\t" \
  "v_cvt_pk_bf16_f32 v48, v248, v249\n\t" \
  "ds_read_b64_tr_b16 v[200:201], v29 offset:0\n\t" \
  "v_mfma_f32_16x16x32_bf16 v[252:255], v[188:191], v[164:167], v[252:255]\n\t" \
  "ds_write_b64 v14, v[48:49] offset:3072\n\t" \
  "ds_read_b64_tr_b16 v[202:203], v29 offset:512\n\t" \
  "v_mfma_f32_16x16x32_bf16 v[252:255], v[148:151], v[160:163], v[252:255]\n\t" \
  "ds_read_b64_tr_b16 v[204:205], v29 offset:4096\n\t" \
  "v_mfma_f32_16x16x32_bf16 v[252:255], v[132:135], v[156:159], v[252:255]\n\t" \
  "ds_read_b64_tr_b16 v[206:207], v29 offset:4608\n\t" \
  "v_mfma_f32_16x16x32_bf16 v[252:255], v[144:147], v[136:139], v[252:255]\n\t" \
  "ds_read_b64_tr_b16 v[208:209], v30 offset:0\n\t" \
  "ds_read_b32 v16, v56 offset:96\n\t" \
  "ds_read_b64_tr_b16 v[210:211], v30 offset:512\n\t" \
  "ds_read_b64_tr_b16 v[212:213], v30 offset:4096\n\t" \
  "ds_read_b64_tr_b16 v[214:215], v30 offset:4608\n\t" \
  "ds_read_b64_tr_b16 v[216:217], v31 offset:0\n\t" \
  "ds_read_b64_tr_b16 v[218:219], v31 offset:512\n\t" \
  "ds_read_b64_tr_b16 v[220:221], v31 offset:4096\n\t" \
  "ds_read_b64_tr_b16 v[222:223], v31 offset:4608\n\t" \
  "v_cvt_pk_bf16_f32 v51, v254, v255\n\t" \
  "v_cvt_pk_bf16_f32 v50, v252, v253\n\t" \
  "ds_write_b64 v14, v[50:51] offset:3584\n\t" \
  "ds_write_b32 v18, v17 offset:32\n\t" \
  "s_memrealtime s[94:95]\n\t" \
  "s_waitcnt lgkmcnt(0)\n\t" \
  "s_cmp_lg_u32 s75, 0\n\t" \
  "s_cbranch_scc1 41f\n\t" \
  "s_waitcnt lgkmcnt(9)\n\t" \
  "v_readfirstlane_b32 vcc_lo, v16\n\t" \
  "s_nop 1\n\t" \
  "s_cmp_ge_u32 vcc_lo, 1\n\t" \
  "s_cbranch_scc0 43f\n\t" \
  "44:\n\t" \
  "41:\n\t" \
  "s_cmp_ge_u32 s75, 16\n\t" \
  "s_cbranch_scc1 42f\n\t" \
  "s_lshr_b64 s[90:91], %18, s75\n\t" \
  "s_and_b32 s90, s90, 15\n\t" \
  "s_lshl_b32 s90, s90, 14\n\t" \
  "s_add_u32 s90, s90, s92\n\t" \
  "s_addc_u32 s91, s93, 0\n\t" \
  "s_add_u32 s75, s75, 4\n\t" \
  "s_swappc_b64 s[68:69], s[70:71]\n\t" \
  "s_branch 41b\n\t" \
  "42:\n\t" \
  "s_memrealtime s[96:97]\n\t" \
  "s_waitcnt lgkmcnt(0)\n\t" \
  "s_waitcnt lgkmcnt(15)\n\t" \
  "s_waitcnt vmcnt(14)\n\t" \
  "v_mfma_f32_16x16x32_bf16 v[224:227], v[192:195], v[64:67], 0\n\t" \
  "s_memrealtime s[78:79]\n\t" \
  "s_waitcnt lgkmcnt(0)\n\t" \
  "v_mfma_f32_16x16x32_bf16 v[224:227], v[196:199], v[68:71], v[224:227]\n\t" \
  "s_waitcnt vmcnt(12)\n\t" \
  "v_mfma_f32_16x16x32_bf16 v[228:231], v[196:199], v[76:79], 0\n\t" \
  "s_lshr_b64 s[90:91], %18, 16\n\t" \
  "s_and_b32 s90, s90, 15\n\t" \
  "s_lshl_b32 s90, s90, 14\n\t" \
  "s_add_u32 s90, s90, s92\n\t" \
  "s_addc_u32 s91, s93, 0\n\t" \
  "global_load_dwordx4 v[128:131], v12, s[90:91] offset:0 sc1\n\t" \
  "v_mfma_f32_16x16x32_bf16 v[228:231], v[192:195], v[72:75], v[228:231]\n\t" \
  "v_mfma_f32_16x16x32_bf16 v[232:235], v[200:203], v[64:67], 0\n\t" \
  "ds_read_b64_tr_b16 v[192:193], v28 offset:8192\n\t" \
  "v_mfma_f32_16x16x32_bf16 v[232:235], v[204:207], v[68:71], v[232:235]\n\t" \
  "ds_read_b64_tr_b16 v[194:195], v28 offset:8704\n\t" \
  "v_mfma_f32_16x16x32_bf16 v[236:239], v[204:207], v[76:79], 0\n\t" \
  "ds_read_b64_tr_b16 v[196:197], v28 offset:12288\n\t" \
  "global_load_dwordx4 v[132:135], v12, s[90:91] offset:1024 sc1\n\t" \
  "v_mfma_f32_16x16x32_bf16 v[236:239], v[200:203], v[72:75], v[236:239]\n\t" \
  "ds_read_b64_tr_b16 v[198:199], v28 offset:12800\n\t" \
  "v_mfma_f32_16x16x32_bf16 v[240:243], v[208:211], v[64:67], 0\n\t" \
  "ds_read_b64_tr_b16 v[200:201], v29 offset:8192\n\t" \
  "v_mfma_f32_16x16x32_bf16 v[240:243], v[212:215], v[68:71], v[240:243]\n\t" \
  "ds_read_b64_tr_b16 v[202:203], v29 offset:8704\n\t" \
  "v_mfma_f32_16x16x32_bf16 v[244:247], v[212:215], v[76:79], 0\n\t" \
  "ds_read_b64_tr_b16 v[204:205], v29 offset:12288\n\t" \
  "global_load_dwordx4 v[136:139], v12, s[90:91] offset:2048 sc1\n\t" \
  "v_mfma_f32_16x16x32_bf16 v[244:247], v[208:211], v[72:75], v[244:247]\n\t" \
  "ds_read_b64_tr_b16 v[206:207], v29 offset:12800\n\t" \
  "v_mfma_f32_16x16x32_bf16 v[248:251], v[216:219], v[64:67], 0\n\t" \
  "ds_read_b64_tr_b16 v[208:209], v30 offset:8192\n\t" \
  "v_mfma_f32_16x16x32_bf16 v[248:251], v[220:223], v[68:71], v[248:251]\n\t" \
  "ds_read_b64_tr_b16 v[210:211], v30 offset:8704\n\t" \
  "v_mfma_f32_16x16x32_bf16 v[252:255], v[220:223], v[76:79], 0\n\t" \
  "ds_read_b64_tr_b16 v[212:213], v30 offset:12288\n\t" \
  "global_load_dwordx4 v[140:143], v12, s[90:91] offset:3072 sc1\n\t" \
  "v_mfma_f32_16x16x32_bf16 v[252:255], v[216:219], v[72:75], v[252:255]\n\t" \
  "ds_read_b64_tr_b16 v[214:215], v30 offset:12800\n\t" \
  "s_waitcnt lgkmcnt(6)\n\t" \
  "s_waitcnt vmcnt(14)\n\t" \
  "v_mfma_f32_16x16x32_bf16 v[224:227], v[192:195], v[80:83], v[224:227]\n\t" \
  "ds_read_b64_tr_b16 v[216:217], v31 offset:8192\n\t" \
  "v_mfma_f32_16x16x32_bf16 v[224:227], v[196:199], v[84:87], v[224:227]\n\t" \
  "ds_read_b64_tr_b16 v[218:219], v31 offset:8704\n\t" \
  "s_waitcnt vmcnt(12)\n\t" \
  "v_mfma_f32_16x16x32_bf16 v[228:231], v[196:199], v[92:95], v[228:231]\n\t" \
  "ds_read_b64_tr_b16 v[220:221], v31 offset:12288\n\t" \
  "s_lshr_b64 s[90:91], %18, 20\n\t" \
  "s_and_b32 s90, s90, 15\n\t" \
  "s_lshl_b32 s90, s90, 14\n\t" \
  "s_add_u32 s90, s90, s92\n\t" \
  "s_addc_u32 s91, s93, 0\n\t" \
  "global_load_dwordx4 v[144:147], v12, s[90:91] offset:0 sc1\n\t" \
  "v_mfma_f32_16x16x32_bf16 v[228:231], v[192:195], v[88:91], v[228:231]\n\t" \
  "ds_read_b64_tr_b16 v[222:223], v31 offset:12800\n\t" \
  "v_mfma_f32_16x16x32_bf16 v[232:235], v[200:203], v[80:83], v[232:235]\n\t" \
  "ds_read_b64_tr_b16 v[192:193], v28 offset:16384\n\t" \
  "s_waitcnt lgkmcnt(6)\n\t" \
  "v_mfma_f32_16x16x32_bf16 v[232:235], v[204:207], v[84:87], v[232:235]\n\t" \
  "ds_read_b64_tr_b16 v[194:195], v28 offset:16896\n\t" \
  "ds_write_b32 v18, v17 offset:160\n\t" \
  "v_mfma_f32_16x16x32_bf16 v[236:239], v[204:207], v[92:95], v[236:239]\n\t" \
  "ds_read_b64_tr_b16 v[196:197], v28 offset:20480\n\t" \
  "global_load_dwordx4 v[148:151], v12, s[90:91] offset:1024 sc1\n\t" \
  "v_mfma_f32_16x16x32_bf16 v[236:239], v[200:203], v[88:91], v[236:239]\n\t" \
  "ds_read_b64_tr_b16 v[198:199], v28 offset:20992\n\t" \
  "v_mfma_f32_16x16x32_bf16 v[240:243], v[208:211], v[80:83], v[240:243]\n\t" \
  "ds_read_b64_tr_b16 v[200:201], v29 offset:16384\n\t" \
  "s_waitcnt lgkmcnt(7)\n\t" \
  "v_mfma_f32_16x16x32_bf16 v[240:243], v[212:215], v[84:87], v[240:243]\n\t" \
  "ds_read_b64_tr_b16 v[202:203], v29 offset:16896\n\t" \
  "v_mfma_f32_16x16x32_bf16 v[244:247], v[212:215], v[92:95], v[244:247]\n\t" \
  "ds_read_b64_tr_b16 v[204:205], v29 offset:20480\n\t" \
  "global_load_dwordx4 v[152:155], v12, s[90:91] offset:2048 sc1\n\t" \
  "v_mfma_f32_16x16x32_bf16 v[244:247], v[208:211], v[88:91], v[244:247]\n\t" \
  "ds_read_b64_tr_b16 v[206:207], v29 offset:20992\n\t" \
  "v_mfma_f32_16x16x32_bf16 v[248:251], v[216:219], v[80:83], v[248:251]\n\t" \
  "ds_read_b64_tr_b16 v[208:209], v30 offset:16384\n\t" \
  "s_waitcnt lgkmcnt(6)\n\t" \
  "v_mfma_f32_16x16x32_bf16 v[248:251], v[220:223], v[84:87], v[248:251]\n\t" \
  "ds_read_b64_tr_b16 v[210:211], v30 offset:16896\n\t" \
  "v_mfma_f32_16x16x32_bf16 v[252:255], v[220:223], v[92:95], v[252:255]\n\t" \
  "ds_read_b64_tr_b16 v[212:213], v30 offset:20480\n\t" \
  "global_load_dwordx4 v[156:159], v12, s[90:91] offset:3072 sc1\n\t" \
  "v_mfma_f32_16x16x32_bf16 v[252:255], v[216:219], v[88:91], v[252:255]\n\t" \
  "ds_read_b64_tr_b16 v[214:215], v30 offset:20992\n\t" \
  "s_waitcnt vmcnt(14)\n\t" \
  "v_mfma_f32_16x16x32_bf16 v[224:227], v[192:195], v[96:99], v[224:227]\n\t" \
  "s_memrealtime s[80:81]\n\t" \
  "s_waitcnt lgkmcnt(0)\n\t" \
  "ds_read_b64_tr_b16 v[216:217], v31 offset:16384\n\t" \
  "v_mfma_f32_16x16x32_bf16 v[224:227], v[196:199], v[100:103], v[224:227]\n\t" \
  "ds_read_b64_tr_b16 v[218:219], v31 offset:16896\n\t" \
  "s_lshr_b64 s[90:91], %18, 24\n\t" \
  "s_and_b32 s90, s90, 15\n\t" \
  "s_lshl_b32 s90, s90, 14\n\t" \
  "s_add_u32 s90, s90, s92\n\t" \
  "s_addc_u32 s91, s93, 0\n\t" \
  "global_load_dwordx4 v[160:163], v12, s[90:91] offset:0 sc1\n\t" \
  "s_waitcnt vmcnt(13)\n\t" \
  "v_mfma_f32_16x16x32_bf16 v[228:231], v[196:199], v[108:111], v[228:231]\n\t" \
  "ds_read_b64_tr_b16 v[220:221], v31 offset:20480\n\t" \
  "v_mfma_f32_16x16x32_bf16 v[228:231], v[192:195], v[104:107], v[228:231]\n\t" \
  "ds_read_b64_tr_b16 v[222:223], v31 offset:20992\n\t" \
  "global_load_dwordx4 v[164:167], v12, s[90:91] offset:1024 sc1\n\t" \
  "v_mfma_f32_16x16x32_bf16 v[232:235], v[200:203], v[96:99], v[232:235]\n\t" \
  "ds_read_b64_tr_b16 v[192:193], v28 offset:24576\n\t" \
  "v_mfma_f32_16x16x32_bf16 v[232:235], v[204:207], v[100:103], v[232:235]\n\t" \
  "ds_read_b64_tr_b16 v[194:195], v28 offset:25088\n\t" \
  "global_load_dwordx4 v[168:171], v12, s[90:91] offset:2048 sc1\n\t" \
  "v_mfma_f32_16x16x32_bf16 v[236:239], v[204:207], v[108:111], v[236:239]\n\t" \
  "ds_read_b64_tr_b16 v[196:197], v28 offset:28672\n\t" \
  "v_mfma_f32_16x16x32_bf16 v[236:239], v[200:203], v[104:107], v[236:239]\n\t" \
  "ds_read_b64_tr_b16 v[198:199], v28 offset:29184\n\t" \
  "global_load_dwordx4 v[172:175], v12, s[90:91] offset:3072 sc1\n\t" \
  "v_mfma_f32_16x16x32_bf16 v[240:243], v[208:211], v[96:99], v[240:243]\n\t" \
  "ds_read_b64_tr_b16 v[200:201], v29 offset:24576\n\t" \
  "v_mfma_f32_16x16x32_bf16 v[240:243], v[212:215], v[100:103], v[240:243]\n\t" \
  "ds_read_b64_tr_b16 v[202:203], v29 offset:25088\n\t" \
  "s_lshr_b64 s[90:91], %18, 28\n\t" \
  "s_and_b32 s90, s90, 15\n\t" \
  "s_lshl_b32 s90, s90, 14\n\t" \
  "s_add_u32 s90, s90, s92\n\t" \
  "s_addc_u32 s91, s93, 0\n\t" \
  "global_load_dwordx4 v[176:179], v12, s[90:91] offset:0 sc1\n\t" \
  "v_mfma_f32_16x16x32_bf16 v[244:247], v[212:215], v[108:111], v[244:247]\n\t" \
  "ds_read_b64_tr_b16 v[204:205], v29 offset:28672\n\t" \
  "v_mfma_f32_16x16x32_bf16 v[244:247], v[208:211], v[104:107], v[244:247]\n\t" \
  "ds_read_b64_tr_b16 v[206:207], v29 offset:29184\n\t" \
  "global_load_dwordx4 v[180:183], v12, s[90:91] offset:1024 sc1\n\t" \
  "s_waitcnt lgkmcnt(6)\n\t" \
  "v_mfma_f32_16x16x32_bf16 v[248:251], v[216:219], v[96:99], v[248:251]\n\t" \
  "ds_read_b64_tr_b16 v[208:209], v30 offset:24576\n\t" \
  "v_mfma_f32_16x16x32_bf16 v[248:251], v[220:223], v[100:103], v[248:251]\n\t" \
  "ds_read_b64_tr_b16 v[210:211], v30 offset:25088\n\t" \
  "global_load_dwordx4 v[184:187], v12, s[90:91] offset:2048 sc1\n\t" \
  "v_mfma_f32_16x16x32_bf16 v[252:255], v[220:223], v[108:111], v[252:255]\n\t" \
  "ds_read_b64_tr_b16 v[212:213], v30 offset:28672\n\t" \
  "v_mfma_f32_16x16x32_bf16 v[252:255], v[216:219], v[104:107], v[252:255]\n\t" \
  "ds_read_b64_tr_b16 v[214:215], v30 offset:29184\n\t" \
  "global_load_dwordx4 v[188:191], v12, s[90:91] offset:3072 sc1\n\t" \
  "s_waitcnt vmcnt(18)\n\t" \
  "v_mfma_f32_16x16x32_bf16 v[224:227], v[192:195], v[112:115], v[224:227]\n\t" \
  "ds_read_b64_tr_b16 v[216:217], v31 offset:24576\n\t" \
  "s_waitcnt lgkmcnt(6)\n\t" \
  "v_mfma_f32_16x16x32_bf16 v[224:227], v[196:199], v[116:119], v[224:227]\n\t" \
  "ds_read_b64_tr_b16 v[218:219], v31 offset:25088\n\t" \
  "s_waitcnt vmcnt(16)\n\t" \
  "v_mfma_f32_16x16x32_bf16 v[228:231], v[196:199], v[124:127], v[228:231]\n\t" \
  "ds_read_b64_tr_b16 v[220:221], v31 offset:28672\n\t" \
  "s_lshr_b64 s[90:91], %18, 32\n\t" \
  "s_and_b32 s90, s90, 15\n\t" \
  "s_lshl_b32 s90, s90, 14\n\t" \
  "s_add_u32 s90, s90, s92\n\t" \
  "s_addc_u32 s91, s93, 0\n\t" \
  "global_load_dwordx4 v[64:67], v12, s[90:91] offset:0 sc1\n\t" \
  "v_mfma_f32_16x16x32_bf16 v[228:231], v[192:195], v[120:123], v[228:231]\n\t" \
  "ds_read_b64_tr_b16 v[222:223], v31 offset:29184\n\t" \
  "v_mfma_f32_16x16x32_bf16 v[232:235], v[200:203], v[112:115], v[232:235]\n\t" \
  "ds_read_b64_tr_b16 v[192:193], v28 offset:32768\n\t" \
  "s_waitcnt lgkmcnt(6)\n\t" \
  "v_mfma_f32_16x16x32_bf16 v[232:235], v[204:207], v[116:119], v[232:235]\n\t" \
  "ds_read_b64_tr_b16 v[194:195], v28 offset:33280\n\t" \
  "v_mfma_f32_16x16x32_bf16 v[236:239], v[204:207], v[124:127], v[236:239]\n\t" \
  "ds_read_b64_tr_b16 v[196:197], v28 offset:36864\n\t" \
  "global_load_dwordx4 v[68:71], v12, s[90:91] offset:1024 sc1\n\t" \
  "v_mfma_f32_16x16x32_bf16 v[236:239], v[200:203], v[120:123], v[236:239]\n\t" \
  "ds_read_b64_tr_b16 v[198:199], v28 offset:37376\n\t" \
  "v_mfma_f32_16x16x32_bf16 v[240:243], v[208:211], v[112:115], v[240:243]\n\t" \
  "ds_read_b64_tr_b16 v[200:201], v29 offset:32768\n\t" \
  "s_waitcnt lgkmcnt(6)\n\t" \
  "v_mfma_f32_16x16x32_bf16 v[240:243], v[212:215], v[116:119], v[240:243]\n\t" \
  "ds_read_b64_tr_b16 v[202:203], v29 offset:33280\n\t" \
  "v_mfma_f32_16x16x32_bf16 v[244:247], v[212:215], v[124:127], v[244:247]\n\t" \
  "ds_read_b64_tr_b16 v[204:205], v29 offset:36864\n\t" \
  "global_load_dwordx4 v[72:75], v12, s[90:91] offset:2048 sc1\n\t" \
  "v_mfma_f32_16x16x32_bf16 v[244:247], v[208:211], v[120:123], v[244:247]\n\t" \
  "ds_read_b64_tr_b16 v[206:207], v29 offset:37376\n\t" \
  "v_mfma_f32_16x16x32_bf16 v[248:251], v[216:219], v[112:115], v[248:251]\n\t" \
  "ds_read_b64_tr_b16 v[208:209], v30 offset:32768\n\t" \
  "s_waitcnt lgkmcnt(6)\n\t" \
  "v_mfma_f32_16x16x32_bf16 v[248:251], v[220:223], v[116:119], v[248:251]\n\t" \
  "ds_read_b64_tr_b16 v[210:211], v30 offset:33280\n\t" \
  "v_mfma_f32_16x16x32_bf16 v[252:255], v[220:223], v[124:127], v[252:255]\n\t" \
  "ds_read_b64_tr_b16 v[212:213], v30 offset:36864\n\t" \
  "global_load_dwordx4 v[76:79], v12, s[90:91] offset:3072 sc1\n\t" \
  "v_mfma_f32_16x16x32_bf16 v[252:255], v[216:219], v[120:123], v[252:255]\n\t" \
  "ds_read_b64_tr_b16 v[214:215], v30 offset:37376\n\t" \
  "s_waitcnt vmcnt(18)\n\t" \
  "v_mfma_f32_16x16x32_bf16 v[224:227], v[192:195], v[128:131], v[224:227]\n\t" \
  "s_memrealtime s[82:83]\n\t" \
  "s_waitcnt lgkmcnt(0)\n\t" \
  "ds_read_b64_tr_b16 v[216:217], v31 offset:32768\n\t" \
  "v_mfma_f32_16x16x32_bf16 v[224:227], v[196:199], v[132:135], v[224:227]\n\t" \
  "ds_read_b64_tr_b16 v[218:219], v31 offset:33280\n\t" \
  "s_waitcnt vmcnt(16)\n\t" \
  "v_mfma_f32_16x16x32_bf16 v[228:231], v[196:199], v[140:143], v[228:231]\n\t" \
  "ds_read_b64_tr_b16 v[220:221], v31 offset:36864\n\t" \
  "s_lshr_b64 s[90:91], %18, 36\n\t" \
  "s_and_b32 s90, s90, 15\n\t" \
  "s_lshl_b32 s90, s90, 14\n\t" \
  "s_add_u32 s90, s90, s92\n\t" \
  "s_addc_u32 s91, s93, 0\n\t" \
  "global_load_dwordx4 v[80:83], v12, s[90:91] offset:0 sc1\n\t" \
  "v_mfma_f32_16x16x32_bf16 v[228:231], v[192:195], v[136:139], v[228:231]\n\t" \
  "ds_read_b64_tr_b16 v[222:223], v31 offset:37376\n\t" \
  "v_mfma_f32_16x16x32_bf16 v[232:235], v[200:203], v[128:131], v[232:235]\n\t" \
  "ds_read_b64_tr_b16 v[192:193], v28 offset:40960\n\t" \
  "v_mfma_f32_16x16x32_bf16 v[232:235], v[204:207], v[132:135], v[232:235]\n\t" \
  "ds_read_b64_tr_b16 v[194:195], v28 offset:41472\n\t" \
  "v_mfma_f32_16x16x32_bf16 v[236:239], v[204:207], v[140:143], v[236:239]\n\t" \
  "ds_read_b64_tr_b16 v[196:197], v28 offset:45056\n\t" \
  "global_load_dwordx4 v[84:87], v12, s[90:91] offset:1024 sc1\n\t" \
  "v_mfma_f32_16x16x32_bf16 v[236:239], v[200:203], v[136:139], v[236:239]\n\t" \
  "ds_read_b64_tr_b16 v[198:199], v28 offset:45568\n\t" \
  "v_mfma_f32_16x16x32_bf16 v[240:243], v[208:211], v[128:131], v[240:243]\n\t" \
  "ds_read_b64_tr_b16 v[200:201], v29 offset:40960\n\t" \
  "v_mfma_f32_16x16x32_bf16 v[240:243], v[212:215], v[132:135], v[240:243]\n\t" \
  "ds_read_b64_tr_b16 v[202:203], v29 offset:41472\n\t" \
  "v_mfma_f32_16x16x32_bf16 v[244:247], v[212:215], v[140:143], v[244:247]\n\t" \
  "ds_read_b64_tr_b16 v[204:205], v29 offset:45056\n\t" \
  "global_load_dwordx4 v[88:91], v12, s[90:91] offset:2048 sc1\n\t" \
  "v_mfma_f32_16x16x32_bf16 v[244:247], v[208:211], v[136:139], v[244:247]\n\t" \
  "ds_read_b64_tr_b16 v[206:207], v29 offset:45568\n\t" \
  "s_waitcnt lgkmcnt(6)\n\t" \
  "v_mfma_f32_16x16x32_bf16 v[248:251], v[216:219], v[128:131], v[248:251]\n\t" \
  "ds_read_b64_tr_b16 v[208:209], v30 offset:40960\n\t" \
  "v_mfma_f32_16x16x32_bf16 v[248:251], v[220:223], v[132:135], v[248:251]\n\t" \
  "ds_read_b64_tr_b16 v[210:211], v30 offset:41472\n\t" \
  "v_mfma_f32_16x16x32_bf16 v[252:255], v[220:223], v[140:143], v[252:255]\n\t" \
  "ds_read_b64_tr_b16 v[212:213], v30 offset:45056\n\t" \
  "global_load_dwordx4 v[92:95], v12, s[90:91] offset:3072 sc1\n\t" \
  "v_mfma_f32_16x16x32_bf16 v[252:255], v[216:219], v[136:139], v[252:255]\n\t" \
  "ds_read_b64_tr_b16 v[214:215], v30 offset:45568\n\t" \
  "s_waitcnt vmcnt(18)\n\t" \
  "v_mfma_f32_16x16x32_bf16 v[224:227], v[192:195], v[144:147], v[224:227]\n\t" \
  "ds_read_b64_tr_b16 v[216:217], v31 offset:40960\n\t" \
  "s_waitcnt lgkmcnt(6)\n\t" \
  "v_mfma_f32_16x16x32_bf16 v[224:227], v[196:199], v[148:151], v[224:227]\n\t" \
  "ds_read_b64_tr_b16 v[218:219], v31 offset:41472\n\t" \
  "s_waitcnt vmcnt(16)\n\t" \
  "v_mfma_f32_16x16x32_bf16 v[228:231], v[196:199], v[156:159], v[228:231]\n\t" \
  "ds_read_b64_tr_b16 v[220:221], v31 offset:45056\n\t" \
  "s_lshr_b64 s[90:91], %18, 40\n\t" \
  "s_and_b32 s90, s90, 15\n\t" \
  "s_lshl_b32 s90, s90, 14\n\t" \
  "s_add_u32 s90, s90, s92\n\t" \
  "s_addc_u32 s91, s93, 0\n\t" \
  "global_load_dwordx4 v[96:99], v12, s[90:91] offset:0 sc1\n\t" \
  "v_mfma_f32_16x16x32_bf16 v[228:231], v[192:195], v[152:155], v[228:231]\n\t" \
  "ds_read_b64_tr_b16 v[222:223], v31 offset:45568\n\t" \
  "v_mfma_f32_16x16x32_bf16 v[232:235], v[200:203], v[144:147], v[232:235]\n\t" \
  "ds_read_b64_tr_b16 v[192:193], v28 offset:49152\n\t" \
  "s_waitcnt lgkmcnt(6)\n\t" \
  "v_mfma_f32_16x16x32_bf16 v[232:235], v[204:207], v[148:151], v[232:235]\n\t" \
  "ds_read_b64_tr_b16 v[194:195], v28 offset:49664\n\t" \
  "v_mfma_f32_16x16x32_bf16 v[236:239], v[204:207], v[156:159], v[236:239]\n\t" \
  "ds_read_b64_tr_b16 v[196:197], v28 offset:53248\n\t" \
  "global_load_dwordx4 v[100:103], v12, s[90:91] offset:1024 sc1\n\t" \
  "v_mfma_f32_16x16x32_bf16 v[236:239], v[200:203], v[152:155], v[236:239]\n\t" \
  "ds_read_b64_tr_b16 v[198:199], v28 offset:53760\n\t" \
  "v_mfma_f32_16x16x32_bf16 v[240:243], v[208:211], v[144:147], v[240:243]\n\t" \
  "ds_read_b64_tr_b16 v[200:201], v29 offset:49152\n\t" \
  "s_waitcnt lgkmcnt(6)\n\t" \
  "v_mfma_f32_16x16x32_bf16 v[240:243], v[212:215], v[148:151], v[240:243]\n\t" \
  "ds_read_b64_tr_b16 v[202:203], v29 offset:49664\n\t" \
  "v_mfma_f32_16x16x32_bf16 v[244:247], v[212:215], v[156:159], v[244:247]\n\t" \
  "ds_read_b64_tr_b16 v[204:205], v29 offset:53248\n\t" \
  "global_load_dwordx4 v[104:107], v12, s[90:91] offset:2048 sc1\n\t" \
  "v_mfma_f32_16x16x32_bf16 v[244:247], v[208:211], v[152:155], v[244:247]\n\t" \
  "ds_read_b64_tr_b16 v[206:207], v29 offset:53760\n\t" \
  "v_mfma_f32_16x16x32_bf16 v[248:251], v[216:219], v[144:147], v[248:251]\n\t" \
  "ds_read_b64_tr_b16 v[208:209], v30 offset:49152\n\t" \
  "s_waitcnt lgkmcnt(6)\n\t" \
  "v_mfma_f32_16x16x32_bf16 v[248:251], v[220:223], v[148:151], v[248:251]\n\t" \
  "ds_read_b64_tr_b16 v[210:211], v30 offset:49664\n\t" \
  "v_mfma_f32_16x16x32_bf16 v[252:255], v[220:223], v[156:159], v[252:255]\n\t" \
  "ds_read_b64_tr_b16 v[212:213], v30 offset:53248\n\t" \
  "global_load_dwordx4 v[108:111], v12, s[90:91] offset:3072 sc1\n\t" \
  "ds_write_b32 v56, v56 offset:108\n\t" \
  "v_mfma_f32_16x16x32_bf16 v[252:255], v[216:219], v[152:155], v[252:255]\n\t" \
  "ds_read_b64_tr_b16 v[214:215], v30 offset:53760\n\t" \
  "s_waitcnt vmcnt(18)\n\t" \
  "v_mfma_f32_16x16x32_bf16 v[224:227], v[192:195], v[160:163], v[224:227]\n\t" \
  "ds_read_b64_tr_b16 v[216:217], v31 offset:49152\n\t" \
  "s_waitcnt lgkmcnt(7)\n\t" \
  "v_mfma_f32_16x16x32_bf16 v[224:227], v[196:199], v[164:167], v[224:227]\n\t" \
  "ds_read_b64_tr_b16 v[218:219], v31 offset:49664\n\t" \
  "s_waitcnt vmcnt(16)\n\t" \
  "v_mfma_f32_16x16x32_bf16 v[228:231], v[196:199], v[172:175], v[228:231]\n\t" \
  "ds_read_b64_tr_b16 v[220:221], v31 offset:53248\n\t" \
  "s_lshr_b64 s[90:91], %18, 44\n\t" \
  "s_and_b32 s90, s90, 15\n\t" \
  "s_lshl_b32 s90, s90, 14\n\t" \
  "s_add_u32 s90, s90, s92\n\t" \
  "s_addc_u32 s91, s93, 0\n\t" \
  "global_load_dwordx4 v[112:115], v12, s[90:91] offset:0 sc1\n\t" \
  "v_mfma_f32_16x16x32_bf16 v[228:231], v[192:195], v[168:171], v[228:231]\n\t" \
  "ds_read_b64_tr_b16 v[222:223], v31 offset:53760\n\t" \
  "v_mfma_f32_16x16x32_bf16 v[232:235], v[200:203], v[160:163], v[232:235]\n\t" \
  "ds_read_b64_tr_b16 v[192:193], v28 offset:57344\n\t" \
  "s_waitcnt lgkmcnt(6)\n\t" \
  "v_mfma_f32_16x16x32_bf16 v[232:235], v[204:207], v[164:167], v[232:235]\n\t" \
  "ds_read_b64_tr_b16 v[194:195], v28 offset:57856\n\t" \
  "v_mfma_f32_16x16x32_bf16 v[236:239], v[204:207], v[172:175], v[236:239]\n\t" \
  "ds_read_b64_tr_b16 v[196:197], v28 offset:61440\n\t" \
  "global_load_dwordx4 v[116:119], v12, s[90:91] offset:1024 sc1\n\t" \
  "v_mfma_f32_16x16x32_bf16 v[236:239], v[200:203], v[168:171], v[236:239]\n\t" \
  "ds_read_b64_tr_b16 v[198:199], v28 offset:61952\n\t" \
  "v_mfma_f32_16x16x32_bf16 v[240:243], v[208:211], v[160:163], v[240:243]\n\t" \
  "ds_read_b64_tr_b16 v[200:201], v29 offset:57344\n\t" \
  "s_waitcnt lgkmcnt(6)\n\t" \
  "v_mfma_f32_16x16x32_bf16 v[240:243], v[212:215], v[164:167], v[240:243]\n\t" \
  "ds_read_b64_tr_b16 v[202:203], v29 offset:57856\n\t" \
  "v_mfma_f32_16x16x32_bf16 v[244:247], v[212:215], v[172:175], v[244:247]\n\t" \
  "ds_read_b64_tr_b16 v[204:205], v29 offset:61440\n\t" \
  "global_load_dwordx4 v[120:123], v12, s[90:91] offset:2048 sc1\n\t" \
  "v_mfma_f32_16x16x32_bf16 v[244:247], v[208:211], v[168:171], v[244:247]\n\t" \
  "ds_read_b64_tr_b16 v[206:207], v29 offset:61952\n\t" \
  "v_mfma_f32_16x16x32_bf16 v[248:251], v[216:219], v[160:163], v[248:251]\n\t" \
  "ds_read_b64_tr_b16 v[208:209], v30 offset:57344\n\t" \
  "s_waitcnt lgkmcnt(6)\n\t" \
  "v_mfma_f32_16x16x32_bf16 v[248:251], v[220:223], v[164:167], v[248:251]\n\t" \
  "ds_read_b64_tr_b16 v[210:211], v30 offset:57856\n\t" \
  "v_mfma_f32_16x16x32_bf16 v[252:255], v[220:223], v[172:175], v[252:255]\n\t" \
  "ds_read_b64_tr_b16 v[212:213], v30 offset:61440\n\t" \
  "global_load_dwordx4 v[124:127], v12, s[90:91] offset:3072 sc1\n\t" \
  "v_mfma_f32_16x16x32_bf16 v[252:255], v[216:219], v[168:171], v[252:255]\n\t" \
  "ds_read_b64_tr_b16 v[214:215], v30 offset:61952\n\t" \
  "s_waitcnt vmcnt(18)\n\t" \
  "v_mfma_f32_16x16x32_bf16 v[224:227], v[192:195], v[176:179], v[224:227]\n\t" \
  "ds_read_b64_tr_b16 v[216:217], v31 offset:57344\n\t" \
  "s_waitcnt lgkmcnt(6)\n\t" \
  "v_mfma_f32_16x16x32_bf16 v[224:227], v[196:199], v[180:183], v[224:227]\n\t" \
  "ds_read_b64_tr_b16 v[218:219], v31 offset:57856\n\t" \
  "s_waitcnt vmcnt(16)\n\t" \
  "v_mfma_f32_16x16x32_bf16 v[228:231], v[196:199], v[188:191], v[228:231]\n\t" \
  "ds_read_b64_tr_b16 v[220:221], v31 offset:61440\n\t" \
  "s_lshr_b64 s[90:91], %18, 48\n\t" \
  "s_and_b32 s90, s90, 15\n\t" \
  "s_lshl_b32 s90, s90, 14\n\t" \
  "s_add_u32 s90, s90, s92\n\t" \
  "s_addc_u32 s91, s93, 0\n\t" \
  "global_load_dwordx4 v[128:131], v12, s[90:91] offset:0 sc1\n\t" \
  "v_mfma_f32_16x16x32_bf16 v[228:231], v[192:195], v[184:187], v[228:231]\n\t" \
  "ds_read_b64_tr_b16 v[222:223], v31 offset:61952\n\t" \
  "v_mfma_f32_16x16x32_bf16 v[232:235], v[200:203], v[176:179], v[232:235]\n\t" \
  "ds_read_b64_tr_b16 v[192:193], v32 offset:0\n\t" \
  "s_waitcnt lgkmcnt(6)\n\t" \
  "v_mfma_f32_16x16x32_bf16 v[232:235], v[204:207], v[180:183], v[232:235]\n\t" \
  "ds_read_b64_tr_b16 v[194:195], v32 offset:512\n\t" \
  "v_mfma_f32_16x16x32_bf16 v[236:239], v[204:207], v[188:191], v[236:239]\n\t" \
  "ds_read_b64_tr_b16 v[196:197], v32 offset:4096\n\t" \
  "global_load_dwordx4 v[132:135], v12, s[90:91] offset:1024 sc1\n\t" \
  "v_mfma_f32_16x16x32_bf16 v[236:239], v[200:203], v[184:187], v[236:239]\n\t" \
  "ds_read_b64_tr_b16 v[198:199], v32 offset:4608\n\t" \
  "v_mfma_f32_16x16x32_bf16 v[240:243], v[208:211], v[176:179], v[240:243]\n\t" \
  "ds_read_b64_tr_b16 v[200:201], v33 offset:0\n\t" \
  "s_waitcnt lgkmcnt(6)\n\t" \
  "v_mfma_f32_16x16x32_bf16 v[240:243], v[212:215], v[180:183], v[240:243]\n\t" \
  "ds_read_b64_tr_b16 v[202:203], v33 offset:512\n\t" \
  "v_mfma_f32_16x16x32_bf16 v[244:247], v[212:215], v[188:191], v[244:247]\n\t" \
  "ds_read_b64_tr_b16 v[204:205], v33 offset:4096\n\t" \
  "global_load_dwordx4 v[136:139], v12, s[90:91] offset:2048 sc1\n\t" \
  "v_mfma_f32_16x16x32_bf16 v[244:247], v[208:211], v[184:187], v[244:247]\n\t" \
  "ds_read_b64_tr_b16 v[206:207], v33 offset:4608\n\t" \
  "v_mfma_f32_16x16x32_bf16 v[248:251], v[216:219], v[176:179], v[248:251]\n\t" \
  "ds_read_b64_tr_b16 v[208:209], v34 offset:0\n\t" \
  "s_waitcnt lgkmcnt(6)\n\t" \
  "v_mfma_f32_16x16x32_bf16 v[248:251], v[220:223], v[180:183], v[248:251]\n\t" \
  "ds_read_b64_tr_b16 v[210:211], v34 offset:512\n\t" \
  "v_mfma_f32_16x16x32_bf16 v[252:255], v[220:223], v[188:191], v[252:255]\n\t" \
  "ds_read_b64_tr_b16 v[212:213], v34 offset:4096\n\t" \
  "global_load_dwordx4 v[140:143], v12, s[90:91] offset:3072 sc1\n\t" \
  "v_mfma_f32_16x16x32_bf16 v[252:255], v[216:219], v[184:187], v[252:255]\n\t" \
  "ds_read_b64_tr_b16 v[214:215], v34 offset:4608\n\t" \
  "s_waitcnt vmcnt(18)\n\t" \
  "v_mfma_f32_16x16x32_bf16 v[224:227], v[192:195], v[64:67], v[224:227]\n\t" \
  "s_memrealtime s[84:85]\n\t" \
  "s_waitcnt lgkmcnt(0)\n\t" \
  "ds_read_b64_tr_b16 v[216:217], v35 offset:0\n\t" \
  "v_mfma_f32_16x16x32_bf16 v[224:227], v[196:199], v[68:71], v[224:227]\n\t" \
  "ds_read_b64_tr_b16 v[218:219], v35 offset:512\n\t" \
  "s_waitcnt vmcnt(16)\n\t" \
  "v_mfma_f32_16x16x32_bf16 v[228:231], v[196:199], v[76:79], v[228:231]\n\t" \
  "ds_read_b64_tr_b16 v[220:221], v35 offset:4096\n\t" \
  "s_lshr_b64 s[90:91], %18, 52\n\t" \
  "s_and_b32 s90, s90, 15\n\t" \
  "s_lshl_b32 s90, s90, 14\n\t" \
  "s_add_u32 s90, s90, s92\n\t" \
  "s_addc_u32 s91, s93, 0\n\t" \
  "global_load_dwordx4 v[144:147], v12, s[90:91] offset:0 sc1\n\t" \
  "v_mfma_f32_16x16x32_bf16 v[228:231], v[192:195], v[72:75], v[228:231]\n\t" \
  "ds_read_b64_tr_b16 v[222:223], v35 offset:4608\n\t" \
  "v_mfma_f32_16x16x32_bf16 v[232:235], v[200:203], v[64:67], v[232:235]\n\t" \
  "ds_read_b64_tr_b16 v[192:193], v32 offset:8192\n\t" \
  "v_mfma_f32_16x16x32_bf16 v[232:235], v[204:207], v[68:71], v[232:235]\n\t" \
  "ds_read_b64_tr_b16 v[194:195], v32 offset:8704\n\t" \
  "v_mfma_f32_16x16x32_bf16 v[236:239], v[204:207], v[76:79], v[236:239]\n\t" \
  "ds_read_b64_tr_b16 v[196:197], v32 offset:12288\n\t" \
  "global_load_dwordx4 v[148:151], v12, s[90:91] offset:1024 sc1\n\t" \
  "ds_read_b32 v16, v56 offset:96\n\t" \
  "v_mfma_f32_16x16x32_bf16 v[236:239], v[200:203], v[72:75], v[236:239]\n\t" \
  "ds_read_b64_tr_b16 v[198:199], v32 offset:12800\n\t" \
  "v_mfma_f32_16x16x32_bf16 v[240:243], v[208:211], v[64:67], v[240:243]\n\t" \
  "ds_read_b64_tr_b16 v[200:201], v33 offset:8192\n\t" \
  "v_mfma_f32_16x16x32_bf16 v[240:243], v[212:215], v[68:71], v[240:243]\n\t" \
  "ds_read_b64_tr_b16 v[202:203], v33 offset:8704\n\t" \
  "v_mfma_f32_16x16x32_bf16 v[244:247], v[212:215], v[76:79], v[244:247]\n\t" \
  "ds_read_b64_tr_b16 v[204:205], v33 offset:12288\n\t" \
  "global_load_dwordx4 v[152:155], v12, s[90:91] offset:2048 sc1\n\t" \
  "v_mfma_f32_16x16x32_bf16 v[244:247], v[208:211], v[72:75], v[244:247]\n\t" \
  "ds_read_b64_tr_b16 v[206:207], v33 offset:12800\n\t" \
  "s_waitcnt lgkmcnt(7)\n\t" \
  "v_mfma_f32_16x16x32_bf16 v[248:251], v[216:219], v[64:67], v[248:251]\n\t" \
  "ds_read_b64_tr_b16 v[208:209], v34 offset:8192\n\t" \
  "v_mfma_f32_16x16x32_bf16 v[248:251], v[220:223], v[68:71], v[248:251]\n\t" \
  "ds_read_b64_tr_b16 v[210:211], v34 offset:8704\n\t" \
  "v_mfma_f32_16x16x32_bf16 v[252:255], v[220:223], v[76:79], v[252:255]\n\t" \
  "ds_read_b64_tr_b16 v[212:213], v34 offset:12288\n\t" \
  "global_load_dwordx4 v[156:159], v12, s[90:91] offset:3072 sc1\n\t" \
  "s_waitcnt lgkmcnt(8)\n\t" \
  "v_readfirstlane_b32 vcc_lo, v16\n\t" \
  "v_mfma_f32_16x16x32_bf16 v[252:255], v[216:219], v[72:75], v[252:255]\n\t" \
  "ds_read_b64_tr_b16 v[214:215], v34 offset:12800\n\t" \
  "s_cmp_ge_u32 vcc_lo, 2\n\t" \
  "s_cbranch_scc0 46f\n\t" \
  "47:\n\t" \
  "s_waitcnt vmcnt(18)\n\t" \
  "v_mfma_f32_16x16x32_bf16 v[224:227], v[192:195], v[80:83], v[224:227]\n\t" \
  "ds_read_b64_tr_b16 v[216:217], v35 offset:8192\n\t" \
  "s_waitcnt lgkmcnt(6)\n\t" \
  "v_mfma_f32_16x16x32_bf16 v[224:227], v[196:199], v[84:87], v[224:227]\n\t" \
  "ds_read_b64_tr_b16 v[218:219], v35 offset:8704\n\t" \
  "s_waitcnt vmcnt(16)\n\t" \
  "v_mfma_f32_16x16x32_bf16 v[228:231], v[196:199], v[92:95], v[228:231]\n\t" \
  "ds_read_b64_tr_b16 v[220:221], v35 offset:12288\n\t" \
  "s_lshr_b64 s[90:91], %18, 56\n\t" \
  "s_and_b32 s90, s90, 15\n\t" \
  "s_lshl_b32 s90, s90, 14\n\t" \
  "s_add_u32 s90, s90, s92\n\t" \
  "s_addc_u32 s91, s93, 0\n\t" \
  "global_load_dwordx4 v[160:163], v12, s[90:91] offset:0 sc1\n\t" \
  "v_mfma_f32_16x16x32_bf16 v[228:231], v[192:195], v[88:91], v[228:231]\n\t" \
  "ds_read_b64_tr_b16 v[222:223], v35 offset:12800\n\t" \
  "v_mfma_f32_16x16x32_bf16 v[232:235], v[200:203], v[80:83], v[232:235]\n\t" \
  "ds_read_b64_tr_b16 v[192:193], v32 offset:16384\n\t" \
  "s_waitcnt lgkmcnt(6)\n\t" \
  "v_mfma_f32_16x16x32_bf16 v[232:235], v[204:207], v[84:87], v[232:235]\n\t" \
  "ds_read_b64_tr_b16 v[194:195], v32 offset:16896\n\t" \
  "v_mfma_f32_16x16x32_bf16 v[236:239], v[204:207], v[92:95], v[236:239]\n\t" \
  "ds_read_b64_tr_b16 v[196:197], v32 offset:20480\n\t" \
  "global_load_dwordx4 v[164:167], v12, s[90:91] offset:1024 sc1\n\t" \
  "v_mfma_f32_16x16x32_bf16 v[236:239], v[200:203], v[88:91], v[236:239]\n\t" \
  "ds_read_b64_tr_b16 v[198:199], v32 offset:20992\n\t" \
  "v_mfma_f32_16x16x32_bf16 v[240:243], v[208:211], v[80:83], v[240:243]\n\t" \
  "ds_read_b64_tr_b16 v[200:201], v33 offset:16384\n\t" \
  "s_waitcnt lgkmcnt(6)\n\t" \
  "v_mfma_f32_16x16x32_bf16 v[240:243], v[212:215], v[84:87], v[240:243]\n\t" \
  "ds_read_b64_tr_b16 v[202:203], v33 offset:16896\n\t" \
  "v_mfma_f32_16x16x32_bf16 v[244:247], v[212:215], v[92:95], v[244:247]\n\t" \
  "ds_read_b64_tr_b16 v[204:205], v33 offset:20480\n\t" \
  "global_load_dwordx4 v[168:171], v12, s[90:91] offset:2048 sc1\n\t" \
  "v_mfma_f32_16x16x32_bf16 v[244:247], v[208:211], v[88:91], v[244:247]\n\t" \
  "ds_read_b64_tr_b16 v[206:207], v33 offset:20992\n\t" \
  "v_mfma_f32_16x16x32_bf16 v[248:251], v[216:219], v[80:83], v[248:251]\n\t" \
  "ds_read_b64_tr_b16 v[208:209], v34 offset:16384\n\t" \
  "s_waitcnt lgkmcnt(6)\n\t" \
  "v_mfma_f32_16x16x32_bf16 v[248:251], v[220:223], v[84:87], v[248:251]\n\t" \
  "ds_read_b64_tr_b16 v[210:211], v34 offset:16896\n\t" \
  "v_mfma_f32_16x16x32_bf16 v[252:255], v[220:223], v[92:95], v[252:255]\n\t" \
  "ds_read_b64_tr_b16 v[212:213], v34 offset:20480\n\t" \
  "global_load_dwordx4 v[172:175], v12, s[90:91] offset:3072 sc1\n\t" \
  "v_mfma_f32_16x16x32_bf16 v[252:255], v[216:219], v[88:91], v[252:255]\n\t" \
  "ds_read_b64_tr_b16 v[214:215], v34 offset:20992\n\t" \
  "s_waitcnt vmcnt(18)\n\t" \
  "v_mfma_f32_16x16x32_bf16 v[224:227], v[192:195], v[96:99], v[224:227]\n\t" \
  "ds_read_b64_tr_b16 v[216:217], v35 offset:16384\n\t" \
  "s_waitcnt lgkmcnt(6)\n\t" \
  "v_mfma_f32_16x16x32_bf16 v[224:227], v[196:199], v[100:103], v[224:227]\n\t" \
  "ds_read_b64_tr_b16 v[218:219], v35 offset:16896\n\t" \
  "s_waitcnt vmcnt(16)\n\t" \
  "v_mfma_f32_16x16x32_bf16 v[228:231], v[196:199], v[108:111], v[228:231]\n\t" \
  "ds_read_b64_tr_b16 v[220:221], v35 offset:20480\n\t" \
  "s_lshr_b64 s[90:91], %18, 60\n\t" \
  "s_and_b32 s90, s90, 15\n\t" \
  "s_lshl_b32 s90, s90, 14\n\t" \
  "s_add_u32 s90, s90, s92\n\t" \
  "s_addc_u32 s91, s93, 0\n\t" \
  "global_load_dwordx4 v[176:179], v12, s[90:91] offset:0 sc1\n\t" \
  "v_mfma_f32_16x16x32_bf16 v[228:231], v[192:195], v[104:107], v[228:231]\n\t" \
  "ds_read_b64_tr_b16 v[222:223], v35 offset:20992\n\t" \
  "v_mfma_f32_16x16x32_bf16 v[232:235], v[200:203], v[96:99], v[232:235]\n\t" \
  "ds_read_b64_tr_b16 v[192:193], v32 offset:24576\n\t" \
  "s_waitcnt lgkmcnt(6)\n\t" \
  "v_mfma_f32_16x16x32_bf16 v[232:235], v[204:207], v[100:103], v[232:235]\n\t" \
  "ds_read_b64_tr_b16 v[194:195], v32 offset:25088\n\t" \
  "v_mfma_f32_16x16x32_bf16 v[236:239], v[204:207], v[108:111], v[236:239]\n\t" \
  "ds_read_b64_tr_b16 v[196:197], v32 offset:28672\n\t" \
  "global_load_dwordx4 v[180:183], v12, s[90:91] offset:1024 sc1\n\t" \
  "509:\n\t" \
  "s_cbranch_execz 26b\n\t" \
  "v_mfma_f32_16x16x32_bf16 v[236:239], v[200:203], v[104:107], v[236:239]\n\t" \
  "ds_read_b64_tr_b16 v[198:199], v32 offset:29184\n\t" \
  "v_mfma_f32_16x16x32_bf16 v[240:243], v[208:211], v[96:99], v[240:243]\n\t" \
  "ds_read_b64_tr_b16 v[200:201], v33 offset:24576\n\t" \
  "s_waitcnt lgkmcnt(6)\n\t" \
  "v_mfma_f32_16x16x32_bf16 v[240:243], v[212:215], v[100:103], v[240:243]\n\t" \
  "ds_read_b64_tr_b16 v[202:203], v33 offset:25088\n\t" \
  "v_mfma_f32_16x16x32_bf16 v[244:247], v[212:215], v[108:111], v[244:247]\n\t" \
  "ds_read_b64_tr_b16 v[204:205], v33 offset:28672\n\t" \
  "global_load_dwordx4 v[184:187], v12, s[90:91] offset:2048 sc1\n\t" \
  "v_mfma_f32_16x16x32_bf16 v[244:247], v[208:211], v[104:107], v[244:247]\n\t" \
  "ds_read_b64_tr_b16 v[206:207], v33 offset:29184\n\t" \
  "v_mfma_f32_16x16x32_bf16 v[248:251], v[216:219], v[96:99], v[248:251]\n\t" \
  "ds_read_b64_tr_b16 v[208:209], v34 offset:24576\n\t" \
  "s_waitcnt lgkmcnt(6)\n\t" \
  "v_mfma_f32_16x16x32_bf16 v[248:251], v[220:223], v[100:103], v[248:251]\n\t" \
  "ds_read_b64_tr_b16 v[210:211], v34 offset:25088\n\t" \
  "v_mfma_f32_16x16x32_bf16 v[252:255], v[220:223], v[108:111], v[252:255]\n\t" \
  "ds_read_b64_tr_b16 v[212:213], v34 offset:28672\n\t" \
  "global_load_dwordx4 v[188:191], v12, s[90:91] offset:3072 sc1\n\t" \
  "v_mfma_f32_16x16x32_bf16 v[252:255], v[216:219], v[104:107], v[252:255]\n\t" \
  "ds_read_b64_tr_b16 v[214:215], v34 offset:29184\n\t" \
  "s_waitcnt vmcnt(18)\n\t" \
  "v_mfma_f32_16x16x32_bf16 v[224:227], v[192:195], v[112:115], v[224:227]\n\t" \
  "ds_read_b64_tr_b16 v[216:217], v35 offset:24576\n\t" \
  "s_waitcnt lgkmcnt(6)\n\t" \
  "v_mfma_f32_16x16x32_bf16 v[224:227], v[196:199], v[116:119], v[224:227]\n\t" \
  "ds_read_b64_tr_b16 v[218:219], v35 offset:25088\n\t" \
  "s_waitcnt vmcnt(16)\n\t" \
  "v_mfma_f32_16x16x32_bf16 v[228:231], v[196:199], v[124:127], v[228:231]\n\t" \
  "ds_read_b64_tr_b16 v[220:221], v35 offset:28672\n\t" \
  "v_mfma_f32_16x16x32_bf16 v[228:231], v[192:195], v[120:123], v[228:231]\n\t" \
  "ds_read_b64_tr_b16 v[222:223], v35 offset:29184\n\t" \
  "v_mfma_f32_16x16x32_bf16 v[232:235], v[200:203], v[112:115], v[232:235]\n\t" \
  "ds_read_b64_tr_b16 v[192:193], v32 offset:32768\n\t" \
  "s_waitcnt lgkmcnt(6)\n\t" \
  "v_mfma_f32_16x16x32_bf16 v[232:235], v[204:207], v[116:119], v[232:235]\n\t" \
  "ds_read_b64_tr_b16 v[194:195], v32 offset:33280\n\t" \
  "v_mfma_f32_16x16x32_bf16 v[236:239], v[204:207], v[124:127], v[236:239]\n\t" \
  "ds_read_b64_tr_b16 v[196:197], v32 offset:36864\n\t" \
  "v_mfma_f32_16x16x32_bf16 v[236:239], v[200:203], v[120:123], v[236:239]\n\t" \
  "ds_read_b64_tr_b16 v[198:199], v32 offset:37376\n\t" \
  "v_mfma_f32_16x16x32_bf16 v[240:243], v[208:211], v[112:115], v[240:243]\n\t" \
  "ds_read_b64_tr_b16 v[200:201], v33 offset:32768\n\t" \
  "s_waitcnt lgkmcnt(6)\n\t" \
  "v_mfma_f32_16x16x32_bf16 v[240:243], v[212:215], v[116:119], v[240:243]\n\t" \
  "ds_read_b128 v[40:43], v56 offset:80\n\t" \
  "ds_read_b64_tr_b16 v[202:203], v33 offset:33280\n\t" \
  "v_mfma_f32_16x16x32_bf16 v[244:247], v[212:215], v[124:127], v[244:247]\n\t" \
  "ds_read_b64_tr_b16 v[204:205], v33 offset:36864\n\t" \
  "v_mfma_f32_16x16x32_bf16 v[244:247], v[208:211], v[120:123], v[244:247]\n\t" \
  "ds_read_b64_tr_b16 v[206:207], v33 offset:37376\n\t" \
  "v_mfma_f32_16x16x32_bf16 v[248:251], v[216:219], v[112:115], v[248:251]\n\t" \
  "ds_read_b64_tr_b16 v[208:209], v34 offset:32768\n\t" \
  "s_waitcnt lgkmcnt(7)\n\t" \
  "v_mfma_f32_16x16x32_bf16 v[248:251], v[220:223], v[116:119], v[248:251]\n\t" \
  "ds_read_b64_tr_b16 v[210:211], v34 offset:33280\n\t" \
  "v_mfma_f32_16x16x32_bf16 v[252:255], v[220:223], v[124:127], v[252:255]\n\t" \
  "ds_read_b64_tr_b16 v[212:213], v34 offset:36864\n\t" \
  "v_mfma_f32_16x16x32_bf16 v[252:255], v[216:219], v[120:123], v[252:255]\n\t" \
  "ds_read_b64_tr_b16 v[214:215], v34 offset:37376\n\t" \
  "s_waitcnt vmcnt(14)\n\t" \
  "v_mfma_f32_16x16x32_bf16 v[224:227], v[192:195], v[128:131], v[224:227]\n\t" \
  "s_memrealtime s[86:87]\n\t" \
  "s_waitcnt lgkmcnt(0)\n\t" \
  "v_min_u32 v40, v40, v41\n\t" \
  "v_min3_u32 v40, v40, v42, v43\n\t" \
  "ds_read_b64_tr_b16 v[216:217], v35 offset:32768\n\t" \
  "v_mfma_f32_16x16x32_bf16 v[224:227], v[196:199], v[132:135], v[224:227]\n\t" \
  "v_readfirstlane_b32 vcc_lo, v40\n\t" \
  "ds_read_b64_tr_b16 v[218:219], v35 offset:33280\n\t" \
  "s_waitcnt vmcnt(12)\n\t" \
  "v_mfma_f32_16x16x32_bf16 v[228:231], v[196:199], v[140:143], v[228:231]\n\t" \
  "s_cmp_ge_u32 vcc_lo, 16\n\t" \
  "s_cbranch_scc0 37f\n\t" \
  "38:\n\t" \
  "ds_read_b64_tr_b16 v[220:221], v35 offset:36864\n\t" \
  "v_mfma_f32_16x16x32_bf16 v[228:231], v[192:195], v[136:139], v[228:231]\n\t" \
  "ds_read_b64_tr_b16 v[222:223], v35 offset:37376\n\t" \
  "v_mfma_f32_16x16x32_bf16 v[232:235], v[200:203], v[128:131], v[232:235]\n\t" \
  "ds_read_b64_tr_b16 v[192:193], v36 offset:0\n\t" \
  "v_mfma_f32_16x16x32_bf16 v[232:235], v[204:207], v[132:135], v[232:235]\n\t" \
  "ds_read_b64_tr_b16 v[194:195], v36 offset:512\n\t" \
  "v_mfma_f32_16x16x32_bf16 v[236:239], v[204:207], v[140:143], v[236:239]\n\t" \
  "ds_read_b64_tr_b16 v[196:197], v36 offset:4096\n\t" \
  "v_mfma_f32_16x16x32_bf16 v[236:239], v[200:203], v[136:139], v[236:239]\n\t" \
  "ds_read_b64_tr_b16 v[198:199], v36 offset:4608\n\t" \
  "v_mfma_f32_16x16x32_bf16 v[240:243], v[208:211], v[128:131], v[240:243]\n\t" \
  "ds_read_b64_tr_b16 v[200:201], v37 offset:0\n\t" \
  "v_mfma_f32_16x16x32_bf16 v[240:243], v[212:215], v[132:135], v[240:243]\n\t" \
  "ds_read_b64_tr_b16 v[202:203], v37 offset:512\n\t" \
  "v_mfma_f32_16x16x32_bf16 v[244:247], v[212:215], v[140:143], v[244:247]\n\t" \
  "ds_read_b64_tr_b16 v[204:205], v37 offset:4096\n\t" \
  "v_mfma_f32_16x16x32_bf16 v[244:247], v[208:211], v[136:139], v[244:247]\n\t" \
  "ds_read_b64_tr_b16 v[206:207], v37 offset:4608\n\t" \
  "s_waitcnt lgkmcnt(6)\n\t" \
  "v_mfma_f32_16x16x32_bf16 v[248:251], v[216:219], v[128:131], v[248:251]\n\t" \
  "ds_read_b64_tr_b16 v[208:209], v38 offset:0\n\t" \
  "v_mfma_f32_16x16x32_bf16 v[248:251], v[220:223], v[132:135], v[248:251]\n\t" \
  "ds_read_b64_tr_b16 v[210:211], v38 offset:512\n\t" \
  "v_mfma_f32_16x16x32_bf16 v[252:255], v[220:223], v[140:143], v[252:255]\n\t" \
  "ds_read_b64_tr_b16 v[212:213], v38 offset:4096\n\t" \
  "v_mfma_f32_16x16x32_bf16 v[252:255], v[216:219], v[136:139], v[252:255]\n\t" \
  "ds_read_b64_tr_b16 v[214:215], v38 offset:4608\n\t" \
  "s_waitcnt vmcnt(10)\n\t" \
  "v_mfma_f32_16x16x32_bf16 v[224:227], v[192:195], v[144:147], v[224:227]\n\t" \
  "ds_read_b64_tr_b16 v[216:217], v39 offset:0\n\t" \
  "s_waitcnt lgkmcnt(6)\n\t" \
  "v_mfma_f32_16x16x32_bf16 v[224:227], v[196:199], v[148:151], v[224:227]\n\t" \
  "ds_read_b64_tr_b16 v[218:219], v39 offset:512\n\t" \
  "s_waitcnt vmcnt(8)\n\t" \
  "v_mfma_f32_16x16x32_bf16 v[228:231], v[196:199], v[156:159], v[228:231]\n\t" \
  "ds_read_b64_tr_b16 v[220:221], v39 offset:4096\n\t" \
  "v_mfma_f32_16x16x32_bf16 v[228:231], v[192:195], v[152:155], v[228:231]\n\t" \
  "ds_read_b64_tr_b16 v[222:223], v39 offset:4608\n\t" \
  "v_mfma_f32_16x16x32_bf16 v[232:235], v[200:203], v[144:147], v[232:235]\n\t" \
  "ds_read_b64_tr_b16 v[192:193], v36 offset:8192\n\t" \
  "s_waitcnt lgkmcnt(6)\n\t" \
  "v_mfma_f32_16x16x32_bf16 v[232:235], v[204:207], v[148:151], v[232:235]\n\t" \
  "ds_read_b64_tr_b16 v[194:195], v36 offset:8704\n\t" \
  "v_mfma_f32_16x16x32_bf16 v[236:239], v[204:207], v[156:159], v[236:239]\n\t" \
  "ds_read_b64_tr_b16 v[196:197], v36 offset:12288\n\t" \
  "v_mfma_f32_16x16x32_bf16 v[236:239], v[200:203], v[152:155], v[236:239]\n\t" \
  "ds_read_b64_tr_b16 v[198:199], v36 offset:12800\n\t" \
  "v_mfma_f32_16x16x32_bf16 v[240:243], v[208:211], v[144:147], v[240:243]\n\t" \
  "ds_read_b64_tr_b16 v[200:201], v37 offset:8192\n\t" \
  "s_waitcnt lgkmcnt(6)\n\t" \
  "v_mfma_f32_16x16x32_bf16 v[240:243], v[212:215], v[148:151], v[240:243]\n\t" \
  "ds_read_b128 v[40:43], v56 offset:160\n\t" \
  "ds_read_b64_tr_b16 v[202:203], v37 offset:8704\n\t" \
  "v_mfma_f32_16x16x32_bf16 v[244:247], v[212:215], v[156:159], v[244:247]\n\t" \
  "ds_read_b64_tr_b16 v[204:205], v37 offset:12288\n\t" \
  "v_mfma_f32_16x16x32_bf16 v[244:247], v[208:211], v[152:155], v[244:247]\n\t" \
  "ds_read_b64_tr_b16 v[206:207], v37 offset:12800\n\t" \
  "v_mfma_f32_16x16x32_bf16 v[248:251], v[216:219], v[144:147], v[248:251]\n\t" \
  "ds_read_b64_tr_b16 v[208:209], v38 offset:8192\n\t" \
  "s_waitcnt lgkmcnt(7)\n\t" \
  "v_mfma_f32_16x16x32_bf16 v[248:251], v[220:223], v[148:151], v[248:251]\n\t" \
  "ds_read_b64_tr_b16 v[210:211], v38 offset:8704\n\t" \
  "v_mfma_f32_16x16x32_bf16 v[252:255], v[220:223], v[156:159], v[252:255]\n\t" \
  "ds_read_b64_tr_b16 v[212:213], v38 offset:12288\n\t" \
  "v_mfma_f32_16x16x32_bf16 v[252:255], v[216:219], v[152:155], v[252:255]\n\t" \
  "ds_read_b64_tr_b16 v[214:215], v38 offset:12800\n\t" \
  "s_waitcnt vmcnt(6)\n\t" \
  "v_mfma_f32_16x16x32_bf16 v[224:227], v[192:195], v[160:163], v[224:227]\n\t" \
  "s_waitcnt lgkmcnt(7)\n\t" \
  "v_min_u32 v40, v40, v41\n\t" \
  "v_min3_u32 v40, v40, v42, v43\n\t" \
  "ds_read_b64_tr_b16 v[216:217], v39 offset:8192\n\t" \
  "v_mfma_f32_16x16x32_bf16 v[224:227], v[196:199], v[164:167], v[224:227]\n\t" \
  "v_readfirstlane_b32 vcc_lo, v40\n\t" \
  "ds_read_b64_tr_b16 v[218:219], v39 offset:8704\n\t" \
  "s_waitcnt vmcnt(4)\n\t" \
  "v_mfma_f32_16x16x32_bf16 v[228:231], v[196:199], v[172:175], v[228:231]\n\t" \
  "s_cmp_ge_u32 vcc_lo, 16\n\t" \
  "s_cbranch_scc0 39f\n\t" \
  "40:\n\t" \
  "ds_read_b64_tr_b16 v[220:221], v39 offset:12288\n\t" \
  "v_mfma_f32_16x16x32_bf16 v[228:231], v[192:195], v[168:171], v[228:231]\n\t" \
  "ds_read_b64_tr_b16 v[222:223], v39 offset:12800\n\t" \
  "s_waitcnt lgkmcnt(6)\n\t" \
  "v_mfma_f32_16x16x32_bf16 v[232:235], v[200:203], v[160:163], v[232:235]\n\t" \
  "ds_read_b64_tr_b16 v[192:193], v32 offset:45312\n\t" \
  "v_mfma_f32_16x16x32_bf16 v[232:235], v[204:207], v[164:167], v[232:235]\n\t" \
  "ds_read_b64_tr_b16 v[194:195], v32 offset:45824\n\t" \
  "v_mfma_f32_16x16x32_bf16 v[236:239], v[204:207], v[172:175], v[236:239]\n\t" \
  "ds_read_b64_tr_b16 v[196:197], v32 offset:53760\n\t" \
  "v_mfma_f32_16x16x32_bf16 v[236:239], v[200:203], v[168:171], v[236:239]\n\t" \
  "ds_read_b64_tr_b16 v[198:199], v32 offset:54272\n\t" \
  "v_mfma_f32_16x16x32_bf16 v[240:243], v[208:211], v[160:163], v[240:243]\n\t" \
  "ds_read_b64_tr_b16 v[200:201], v33 offset:45312\n\t" \
  "s_waitcnt lgkmcnt(6)\n\t" \
  "v_mfma_f32_16x16x32_bf16 v[240:243], v[212:215], v[164:167], v[240:243]\n\t" \
  "ds_read_b64_tr_b16 v[202:203], v33 offset:45824\n\t" \
  "v_mfma_f32_16x16x32_bf16 v[244:247], v[212:215], v[172:175], v[244:247]\n\t" \
  "ds_read_b64_tr_b16 v[204:205], v33 offset:53760\n\t" \
  "v_mfma_f32_16x16x32_bf16 v[244:247], v[208:211], v[168:171], v[244:247]\n\t" \
  "ds_read_b64_tr_b16 v[206:207], v33 offset:54272\n\t" \
  "v_mfma_f32_16x16x32_bf16 v[248:251], v[216:219], v[160:163], v[248:251]\n\t" \
  "ds_read_b64_tr_b16 v[208:209], v34 offset:45312\n\t" \
  "s_waitcnt lgkmcnt(6)\n\t" \
  "v_mfma_f32_16x16x32_bf16 v[248:251], v[220:223], v[164:167], v[248:251]\n\t" \
  "ds_read_b64_tr_b16 v[210:211], v34 offset:45824\n\t" \
  "v_mfma_f32_16x16x32_bf16 v[252:255], v[220:223], v[172:175], v[252:255]\n\t" \
  "ds_read_b64_tr_b16 v[212:213], v34 offset:53760\n\t" \
  "v_mfma_f32_16x16x32_bf16 v[252:255], v[216:219], v[168:171], v[252:255]\n\t" \
  "ds_read_b64_tr_b16 v[214:215], v34 offset:54272\n\t" \
  "s_waitcnt vmcnt(2)\n\t" \
  "v_mfma_f32_16x16x32_bf16 v[224:227], v[192:195], v[176:179], v[224:227]\n\t" \
  "ds_read_b64_tr_b16 v[216:217], v35 offset:45312\n\t" \
  "s_waitcnt lgkmcnt(6)\n\t" \
  "v_mfma_f32_16x16x32_bf16 v[224:227], v[196:199], v[180:183], v[224:227]\n\t" \
  "ds_read_b64_tr_b16 v[218:219], v35 offset:45824\n\t" \
  "s_waitcnt vmcnt(0)\n\t" \
  "v_mfma_f32_16x16x32_bf16 v[228:231], v[196:199], v[188:191], v[228:231]\n\t" \
  "ds_read_b64_tr_b16 v[220:221], v35 offset:53760\n\t" \
  "v_mfma_f32_16x16x32_bf16 v[228:231], v[192:195], v[184:187], v[228:231]\n\t" \
  "ds_read_b64_tr_b16 v[222:223], v35 offset:54272\n\t" \
  "v_mfma_f32_16x16x32_bf16 v[232:235], v[200:203], v[176:179], v[232:235]\n\t" \
  "s_waitcnt lgkmcnt(5)\n\t" \
  "v_mfma_f32_16x16x32_bf16 v[232:235], v[204:207], v[180:183], v[232:235]\n\t" \
  "v_mfma_f32_16x16x32_bf16 v[236:239], v[204:207], v[188:191], v[236:239]\n\t" \
  "v_cvt_pk_bf16_f32 v224, v224, v225\n\t" \
  "v_cvt_pk_bf16_f32 v225, v226, v227\n\t" \
  "v_mfma_f32_16x16x32_bf16 v[236:239], v[200:203], v[184:187], v[236:239]\n\t" \
  "ds_write_b64 v57, v[224:225] offset:0\n\t" \
  "v_mfma_f32_16x16x32_bf16 v[240:243], v[208:211], v[176:179], v[240:243]\n\t" \
  "v_cvt_pk_bf16_f32 v228, v228, v229\n\t" \
  "v_cvt_pk_bf16_f32 v229, v230, v231\n\t" \
  "s_waitcnt lgkmcnt(2)\n\t" \
  "v_mfma_f32_16x16x32_bf16 v[240:243], v[212:215], v[180:183], v[240:243]\n\t" \
  "ds_write_b64 v57, v[228:229] offset:2048\n\t" \
  "v_mfma_f32_16x16x32_bf16 v[244:247], v[212:215], v[188:191], v[244:247]\n\t" \
  "v_cvt_pk_bf16_f32 v232, v232, v233\n\t" \
  "v_cvt_pk_bf16_f32 v233, v234, v235\n\t" \
  "v_mfma_f32_16x16x32_bf16 v[244:247], v[208:211], v[184:187], v[244:247]\n\t" \
  "ds_write_b64 v58, v[232:233] offset:0\n\t" \
  "v_mfma_f32_16x16x32_bf16 v[248:251], v[216:219], v[176:179], v[248:251]\n\t" \
  "v_cvt_pk_bf16_f32 v236, v236, v237\n\t" \
  "v_cvt_pk_bf16_f32 v237, v238, v239\n\t" \
  "s_waitcnt lgkmcnt(3)\n\t" \
  "v_mfma_f32_16x16x32_bf16 v[248:251], v[220:223], v[180:183], v[248:251]\n\t" \
  "ds_write_b64 v58, v[236:237] offset:2048\n\t" \
  "v_mfma_f32_16x16x32_bf16 v[252:255], v[220:223], v[188:191], v[252:255]\n\t" \
  "v_cvt_pk_bf16_f32 v240, v240, v241\n\t" \
  "v_cvt_pk_bf16_f32 v241, v242, v243\n\t" \
  "v_mfma_f32_16x16x32_bf16 v[252:255], v[216:219], v[184:187], v[252:255]\n\t" \
  "ds_write_b64 v59, v[240:241] offset:0\n\t" \
  "v_cvt_pk_bf16_f32 v244, v244, v245\n\t" \
  "v_cvt_pk_bf16_f32 v245, v246, v247\n\t" \
  "ds_write_b64 v59, v[244:245] offset:2048\n\t" \
  "v_cvt_pk_bf16_f32 v248, v248, v249\n\t" \
  "v_cvt_pk_bf16_f32 v249, v250, v251\n\t" \
  "ds_write_b64 v60, v[248:249] offset:0\n\t" \
  "s_nop 0\n\t" \
  "v_cvt_pk_bf16_f32 v252, v252, v253\n\t" \
  "v_cvt_pk_bf16_f32 v253, v254, v255\n\t" \
  "ds_write_b64 v60, v[252:253] offset:2048\n\t" \
  "s_memrealtime s[66:67]\n\t" \
  "s_waitcnt lgkmcnt(0)\n\t" \
  "ds_read_b128 v[64:67], v61 offset:0\n\t" \
  "ds_read_b128 v[68:71], v61 offset:1024\n\t" \
  "ds_read_b128 v[72:75], v61 offset:2048\n\t" \
  "ds_read_b128 v[76:79], v61 offset:3072\n\t" \
  "s_waitcnt lgkmcnt(3)\n\t" \
  "global_store_dwordx4 v62, v[64:67], %22 offset:-4096" OUTPOL "\n\t" \
  "s_waitcnt lgkmcnt(2)\n\t" \
  "global_store_dwordx4 v63, v[68:71], %22 offset:-4096" OUTPOL "\n\t" \
  "s_waitcnt lgkmcnt(1)\n\t" \
  "global_store_dwordx4 v62, v[72:75], %22 offset:0" OUTPOL "\n\t" \
  "s_waitcnt lgkmcnt(0)\n\t" \
  "global_store_dwordx4 v63, v[76:79], %22 offset:0" OUTPOL "\n\t" \
  "s_branch 19f\n\t" \
  "7:\n\t" \
  "s_sleep 1\n\t" \
  "ds_read_b128 v[60:63], v56 offset:64\n\t" \
  "ds_read_b64_tr_b16 v[108:109], v5 offset:0\n\t" \
  "ds_read_b64_tr_b16 v[110:111], v5 offset:16\n\t" \
  "ds_read_b128 v[112:115], v6 offset:16384\n\t" \
  "ds_read_b128 v[116:119], v6 offset:15360\n\t" \
  "ds_read_b64_tr_b16 v[120:121], v5 offset:512\n\t" \
  "ds_read_b64_tr_b16 v[122:123], v5 offset:528\n\t" \
  "ds_read_b128 v[124:127], v6 offset:14336\n\t" \
  "ds_read_b64_tr_b16 v[128:129], v5 offset:1024\n\t" \
  "ds_read_b64_tr_b16 v[130:131], v5 offset:1040\n\t" \
  "ds_read_b128 v[132:135], v6 offset:13312\n\t" \
  "ds_read_b64_tr_b16 v[136:137], v5 offset:1536\n\t" \
  "ds_read_b64_tr_b16 v[138:139], v5 offset:1552\n\t" \
  "ds_read_b128 v[140:143], v6 offset:12288\n\t" \
  "ds_read_b64_tr_b16 v[144:145], v5 offset:2048\n\t" \
  "s_waitcnt lgkmcnt(0)\n\t" \
  "v_and_b32 v60, v60, v61\n\t" \
  "v_and_b32 v62, v62, v63\n\t" \
  "v_mov_b32 v12, %13\n\t" \
  "v_and_b32 v60, v60, v62\n\t" \
  "s_nop 0\n\t" \
  "v_readfirstlane_b32 vcc_lo, v60\n\t" \
  "s_nop 1\n\t" \
  "s_cmp_eq_u32 vcc_lo, 0\n\t" \
  "s_cbranch_scc1 7b\n\t" \
  "s_branch 6b\n\t" \
  "25:\n\t" \
  "510:\n\t" \
  "s_cbranch_execz 26b\n\t" \
  "global_load_dwordx4 v[64:67], v12, s[90:91] offset:0 sc1\n\t" \
  "global_load_dwordx4 v[68:71], v12, s[90:91] offset:1024 sc1\n\t" \
  "global_load_dwordx4 v[72:75], v12, s[90:91] offset:2048 sc1\n\t" \
  "global_load_dwordx4 v[76:79], v12, s[90:91] offset:3072 sc1\n\t" \
  "s_swappc_b64 s[70:71], s[68:69]\n\t" \
  "global_load_dwordx4 v[80:83], v12, s[90:91] offset:0 sc1\n\t" \
  "global_load_dwordx4 v[84:87], v12, s[90:91] offset:1024 sc1\n\t" \
  "global_load_dwordx4 v[88:91], v12, s[90:91] offset:2048 sc1\n\t" \
  "global_load_dwordx4 v[92:95], v12, s[90:91] offset:3072 sc1\n\t" \
  "s_swappc_b64 s[70:71], s[68:69]\n\t" \
  "global_load_dwordx4 v[96:99], v12, s[90:91] offset:0 sc1\n\t" \
  "global_load_dwordx4 v[100:103], v12, s[90:91] offset:1024 sc1\n\t" \
  "global_load_dwordx4 v[104:107], v12, s[90:91] offset:2048 sc1\n\t" \
  "global_load_dwordx4 v[108:111], v12, s[90:91] offset:3072 sc1\n\t" \
  "s_swappc_b64 s[70:71], s[68:69]\n\t" \
  "global_load_dwordx4 v[112:115], v12, s[90:91] offset:0 sc1\n\t" \
  "global_load_dwordx4 v[116:119], v12, s[90:91] offset:1024 sc1\n\t" \
  "global_load_dwordx4 v[120:123], v12, s[90:91] offset:2048 sc1\n\t" \
  "global_load_dwordx4 v[124:127], v12, s[90:91] offset:3072 sc1\n\t" \
  "s_swappc_b64 s[70:71], s[68:69]\n\t" \
  "35:\n\t" \
  "ds_read_b128 v[40:43], v56 offset:80\n\t" \
  "s_waitcnt lgkmcnt(0)\n\t" \
  "v_min_u32 v40, v40, v41\n\t" \
  "v_min3_u32 v40, v40, v42, v43\n\t" \
  "s_nop 1\n\t" \
  "v_readfirstlane_b32 vcc_lo, v40\n\t" \
  "s_nop 1\n\t" \
  "s_cmp_ge_u32 vcc_lo, 13\n\t" \
  "s_cbranch_scc0 35b\n\t" \
  "s_branch 36b\n\t" \
  "37:\n\t" \
  "ds_read_b128 v[40:43], v56 offset:80\n\t" \
  "s_waitcnt lgkmcnt(0)\n\t" \
  "v_min_u32 v40, v40, v41\n\t" \
  "v_min3_u32 v40, v40, v42, v43\n\t" \
  "s_nop 1\n\t" \
  "v_readfirstlane_b32 vcc_lo, v40\n\t" \
  "s_nop 1\n\t" \
  "s_cmp_ge_u32 vcc_lo, 16\n\t" \
  "s_cbranch_scc0 37b\n\t" \
  "s_branch 38b\n\t" \
  "39:\n\t" \
  "ds_read_b128 v[40:43], v56 offset:160\n\t" \
  "s_waitcnt lgkmcnt(0)\n\t" \
  "v_min_u32 v40, v40, v41\n\t" \
  "v_min3_u32 v40, v40, v42, v43\n\t" \
  "s_nop 1\n\t" \
  "v_readfirstlane_b32 vcc_lo, v40\n\t" \
  "s_nop 1\n\t" \
  "s_cmp_ge_u32 vcc_lo, 16\n\t" \
  "s_cbranch_scc0 39b\n\t" \
  "s_branch 40b\n\t" \
  "43:\n\t" \
  "s_cmp_eq_u32 %13, 0\n\t" \
  "s_cbranch_scc1 45f\n\t" \
  "s_sleep 1\n\t" \
  "ds_read_b32 v16, v56 offset:96\n\t" \
  "s_waitcnt lgkmcnt(0)\n\t" \
  "v_readfirstlane_b32 vcc_lo, v16\n\t" \
  "s_nop 1\n\t" \
  "s_cmp_ge_u32 vcc_lo, 1\n\t" \
  "s_cbranch_scc0 43b\n\t" \
  "s_branch 44b\n\t" \
  "45:\n\t" \
  "v_bfe_u32 v19, v12, 4, 4\n\t" \
  "v_lshlrev_b32 v19, 2, v19\n\t" \
  "s_mov_b32 s90, 0\n\t" \
  "145:\n\t" \
  "global_load_dword v16, v19, %21 sc1\n\t" \
  "s_add_u32 s90, s90, 1\n\t" \
  "s_waitcnt vmcnt(0)\n\t" \
  "v_cmp_ne_u32 vcc, v55, v16\n\t" \
  "s_mov_b32 s88, 0xf7f7f7f7\n\t" \
  "s_mov_b32 s89, s88\n\t" \
  "s_and_b64 vcc, vcc, s[88:89]\n\t" \
  "s_cbranch_scc0 245f\n\t" \
  "s_sleep 2\n\t" \
  "s_cmp_lt_u32 s90, 0x400000\n\t" \
  "s_cbranch_scc1 145b\n\t" \
  "245:\n\t" \
  "v_mov_b32 v16, 1\n\t" \
  "ds_max_u32 v56, v16 offset:96\n\t" \
  "s_branch 44b\n\t" \
  "46:\n\t" \
  "s_cmp_eq_u32 %13, 0\n\t" \
  "s_cbranch_scc1 48f\n\t" \
  "s_sleep 1\n\t" \
  "ds_read_b32 v16, v56 offset:96\n\t" \
  "s_waitcnt lgkmcnt(0)\n\t" \
  "v_readfirstlane_b32 vcc_lo, v16\n\t" \
  "s_nop 1\n\t" \
  "s_cmp_ge_u32 vcc_lo, 2\n\t" \
  "s_cbranch_scc0 46b\n\t" \
  "s_branch 47b\n\t" \
  "48:\n\t" \
  "v_bfe_u32 v19, v12, 4, 4\n\t" \
  "v_lshlrev_b32 v19, 2, v19\n\t" \
  "s_mov_b32 s90, 0\n\t" \
  "148:\n\t" \
  "global_load_dword v16, v19, %21 sc1\n\t" \
  "s_add_u32 s90, s90, 1\n\t" \
  "s_waitcnt vmcnt(0)\n\t" \
  "v_cmp_ne_u32 vcc, v55, v16\n\t" \
  "s_mov_b32 s88, 0xf7f7f7f7\n\t" \
  "s_mov_b32 s89, s88\n\t" \
  "s_and_b64 vcc, vcc, -1\n\t" \
  "s_cbranch_scc0 248f\n\t" \
  "s_sleep 2\n\t" \
  "s_cmp_lt_u32 s90, 0x400000\n\t" \
  "s_cbranch_scc1 148b\n\t" \
  "248:\n\t" \
  "v_mov_b32 v16, 2\n\t" \
  "ds_max_u32 v56, v16 offset:96\n\t" \
  "s_branch 47b\n\t" \
  "19:\n\t"
#define C_CLOB "v10","v11","v12","v13","v18","v19","v20","v21","v22","v23","v24","v25","v26","v27","v28","v29","v30","v31","v32","v33","v34","v35","v36","v37","v38","v39","v40","v41","v42","v43","v44","v45","v46","v47","v48","v49","v50","v53","v55","v56","v57","v58","v59","v60","v61","v62","v63","v64","v65","v66","v67","v68","v69","v70","v71","v72","v73","v74","v75","v76","v77","v78","v79","v80","v81","v82","v83","v84","v85","v86","v87","v88","v89","v90","v91","v92","v93","v94","v95","v96","v97","v98","v99","v100","v101","v102","v103","v104","v105","v106","v107","v108","v109","v110","v111","v112","v113","v114","v115","v116","v117","v118","v119","v120","v121","v122","v123","v124","v125","v126","v127","v128","v129","v130","v131","v132","v133","v134","v135","v136","v137","v138","v139","v140","v141","v142","v143","v144","v145","v146","v147","v148","v149","v150","v151","v152","v153","v154","v155","v156","v157","v158","v159","v160","v161","v162","v163","v164","v165","v166","v167","v168","v169","v170","v171","v172","v173","v174","v175","v176","v177","v178","v179","v180","v181","v182","v183","v184","v185","v186","v187","v188","v189","v190","v191","v192","v193","v194","v195","v196","v197","v198","v199","v200","v201","v202","v203","v204","v205","v206","v207","v208","v209","v210","v211","v212","v213","v214","v215","v216","v217","v218","v219","v220","v221","v222","v223","v224","v225","v226","v227","v228","v229","v230","v231","v232","v233","v234","v235","v236","v237","v238","v239","v240","v241","v242","v243","v244","v245","v246","v247","v248","v249","v250","v251","v252","v253","v254","v255"

static_assert(KVP<13 && KVP>TGS+1, "");

#define NST 24
#define TS(i) do{ unsigned long long t_; asm volatile("s_memrealtime %0\n\ts_waitcnt lgkmcnt(0)" : "=s"(t_) :: "memory"); STW(i,(unsigned)(t_-T0)); }while(0)
#define STW(i,v) asm volatile("s_nop 0\n\tv_writelane_b32 %0, %1, %2" : "+v"(stv) : "s"(v), "n"(i))
#define TSDUMP() do{ unsigned pv_=0, r0_; asm volatile("s_nop 4\n\tv_readlane_b32 %0, %2, %3\n\tv_readlane_b32 %1, %2, 62\n\ts_nop 4" : "=&s"(pv_), "=&s"(r0_) : "v"(stv), "n"(PIDX)); if(WB+w!=PWAVE || !(PSEL)) pv_=0; \
  asm volatile("s_waitcnt vmcnt(0)":::"memory"); while((unsigned)__builtin_amdgcn_s_memrealtime()-r0_<4000u+2u*pv_) __builtin_amdgcn_s_sleep(1); }while(0)
#define TSB(i) do{ if(w==3) TS(i); }while(0)
#define TSR(i) do{ if(w==3){ unsigned long long t_; asm volatile("s_memrealtime %0\n\ts_waitcnt lgkmcnt(0)" : "=s"(t_) :: "memory"); asm volatile("s_mov_b32 m0, %2\n\ts_nop 0\n\tv_writelane_b32 %0, %1, m0" : "+v"(stv) : "s"((unsigned)(t_-T0)), "s"(i)); } }while(0)
#define TSH(i) do{ if(w!=3) TS(i); }while(0)
#define TSDECL() unsigned stv=0

typedef unsigned long long u64;
typedef u64 u64x8 __attribute__((ext_vector_type(8)));
typedef u64 u64x4 __attribute__((ext_vector_type(4)));
typedef unsigned u32x8 __attribute__((ext_vector_type(8)));
typedef unsigned u32x16 __attribute__((ext_vector_type(16)));
template<class T> __device__ __forceinline__ T sg(T v){
  if constexpr(sizeof(T)==4) return (T)__builtin_amdgcn_readfirstlane((unsigned)v); else return v; }
#define GP(T,v) ((T*)(__attribute__((address_space(1))) T*)(v))
__global__ __launch_bounds__(512,2) __attribute__((aligned(4096))) void fused_kernel(const u16* x_, const u16* wqkv_, const u16* kin_, const u16* vin_, const u16* shk_, const u16* hk_,
    const u16* wo_, const u16* qin_, const u16* sD_, u16* out_, u16* ws_, u16* unused_)
{
#define ST0() unsigned long long T0, R0; asm volatile("s_memrealtime %0\n\ts_memrealtime %1" : "=&s"(T0), "=&s"(R0) :: "memory");
#define ST1() asm volatile("" : "+s"(T0), "+s"(R0));
#define ARGS0() u64x8 ka; u64x4 kb; asm volatile("s_load_dwordx16 %0, %2, 0\n\ts_load_dwordx8 %1, %2, 64" : "=&s"(ka), "=&s"(kb) : "s"(__builtin_amdgcn_kernarg_segment_ptr()) : "memory")
#define ARGS() asm volatile("s_waitcnt lgkmcnt(0)" : "+s"(ka), "+s"(kb) :: "memory"); ST1() \
  const u16 *x=GP(const u16,ka[0]), *wqkv=GP(const u16,ka[1]), *kin=GP(const u16,ka[2]), *vin=GP(const u16,ka[3]), *shk=GP(const u16,ka[4]), *hk=GP(const u16,ka[5]), *wo=GP(const u16,ka[6]), *qin=GP(const u16,ka[7]), *sD=GP(const u16,kb[0]); \
  u16 *out=GP(u16,kb[1]), *o=GP(u16,kb[2]), *xrT=o+(size_t)S*DM, *yT=o+(size_t)2*S*DM; unsigned* flags=(unsigned*)(o+(size_t)6*S*DM); \
  (void)x; (void)wqkv; (void)kin; (void)vin; (void)shk; (void)hk; (void)wo; (void)qin; (void)sD; (void)out; (void)xrT; (void)yT; (void)flags
  ST0()
  __shared__ __attribute__((aligned(1024))) char smem[ALL_LDS];
  const int tid=threadIdx.x;
  const int w8=__builtin_amdgcn_readfirstlane(tid>>6);
  asm volatile("s_cmp_lg_u32 %0, 7\n\ts_cbranch_scc1 1f\n\ts_lshr_b32 vcc_lo, %2, 3\n\ts_mul_i32 vcc_lo, vcc_lo, %3\n\tv_and_b32 v254, 15, %1\n\tv_lshl_add_u32 v254, v254, 7, vcc_lo\n\ts_getpc_b64 vcc\n\tglobal_load_dword v255, v254, vcc\n1:"
      :: "s"(w8), "v"(tid), "s"(blockIdx.x), "n"(CODE_ST) : "memory", "scc", "vcc", "v254", "v255");
  LDS_AS unsigned* const ct=(LDS_AS unsigned*)(LDS_AS char*)(smem+CT_OFF);
  volatile LDS_AS unsigned* const vct=ct;
  auto all4=[&](int i)->bool{ u32x4 v=*(volatile LDS_AS u32x4*)(ct+i); return __builtin_amdgcn_readfirstlane(v[0]&v[1]&v[2]&v[3])!=0; };
#define ROTF(b) ((4+4*xr+4*xc+8*(b))&15)
#define BLOCK() int bid=blockIdx.x, w=w8&3; asm volatile("" : "+s"(bid), "+s"(w)); __builtin_assume(bid>=0 && bid<256 && w>=0 && w<4); \
  const int xcd=bid&7, idx=bid>>3, ra=idx&3, rbb=idx>>2, xr=xcd>>1, xc=xcd&1, rb=xr*4+ra, hp=xc*8+rbb, s0=rb*128, tm=rb, tn=hp^TNX; (void)s0; \
  auto korder=[&]()->unsigned long long{ const int r=KR; const unsigned long long N=0xfedca987654210ull, m=(1ull<<56)-1; const int sh=4*(r>=14? r-14 : r); \
    return (((N>>sh)|(N<<(56-sh)))&m) | ((idx&4)? 0xb3ull<<56 : 0x3bull<<56); }

  if(__builtin_expect(w8>=4,0)){
  BLOCK(); constexpr int WB=4; (void)WB;
  const int hd=w>>1, par=w&1;
  __builtin_amdgcn_s_setprio(CPRIO);
  {
    ARGS0(); LANES();
    if(w==0){ if(l<44) ct[l]=0u; if(idx==0) *(u32x4*)(smem+AUX+l*16)=u32x4{0,0,0,0}; }
    unsigned a0b=fr*128+((g^(fr&7))*16);
    const int r0=8*g+tq;
    unsigned bbb=r0*128+(((2*hd)^wswz(r0))*32)+8*tp;
    unsigned kA=AUX+(fr^3)*KVP_+w*64+(g&1)*16-(g>>1)*256, kB=AUX+(20+4*g-tq)*KVP_+w*64+8*tp;
    const int cch=127-4*idx-g;
    unsigned kvo=(xcd*32+8*w+tq)*16384+(cch>>4)*2048+(cch&15)*64+((tp^((cch>>2)&3))*16);
    unsigned sho=(xcd*128+32*w+fr)*8; const unsigned ctl=CT_OFF+4*w;
    u32x4 sel;
    { const unsigned b2=(g>>1)? 0x0302u : 0x0100u, sv=(fr&1)? (b2<<16|0x0c0cu) : (0x0c0c0000u|b2);
      #pragma unroll
      for(int i=0;i<4;i++) sel[i]= (fr>>1)==(g&1)*4+i ? sv : 0x0c0c0c0cu; }
    unsigned msk=0, lx=w*1024+l*16;
    #pragma unroll
    for(int e=0;e<4;e++){ const int d=4*g+e-fr; const unsigned pv=d<9, hv=par? pv : (unsigned)(d<1 || 4*g+e>7);
      msk|=((unsigned)(d+7)>7u?1u:0u)<<e | pv<<(4+e) | hv<<(8+e) | (d<=0?1u:0u)<<(12+e); }
    msk|=(unsigned)(g? l-16 : 48+((l-1)&15))<<26;
    const unsigned hb=__builtin_amdgcn_readfirstlane(rb==0 ? 0xcfa8965fu : 0u);
    unsigned ap=AUX+w*CV_W+(tq&1)*(CV_CP-4)+8+32*g+4*tq+8*tp;
    u32x4 bp;
    #pragma unroll
    for(int b=0;b<4;b++){ const int c=15+4*b-fr+g; bp[b]=c*64+((w^((c>>2)&3))*16); }
    asm volatile("" : "+v"(a0b), "+v"(bbb), "+v"(kA), "+v"(kB), "+v"(kvo), "+v"(sho), "+v"(msk), "+v"(lx), "+v"(ap), "+v"(bp), "+v"(sel));
    ARGS();
    const u16* ow=o+((size_t)(rb*16+hp)*16+par*2+hd)*512+2048;
    asm volatile("" : "+s"(yT));
    asm volatile("" : "+s"(shk));
    asm volatile(C_ASM : "+{v52}"(a0b), "+{v54}"(bbb), "+{v0}"(kA), "+{v1}"(kB), "+{v2}"(kvo), "+{v51}"(sho), "+{v3}"(msk), "+{v4}"(lx), "+{v5}"(ap), "+{v[6:9]}"(bp), "+{v[14:17]}"(sel)
       : "s"(shk), "s"(ctl), "s"(w), "s"(ow), "s"(hb), "s"(0.17677669529663687f*1.44269504088896341f), "{s[92:93]}"(o+(size_t)tm*16*8192), "s"(korder()),
         "s"(flags+FL_O+rb*512+hp), "s"(AUX+w*(CV_W+64)), "s"(flags+FL_O+(tm*16+tn)*32),
         "s"(out+(size_t)(tm*128+w*32+1)*2048+1024+tn*64), "s"(w<2? 16384+w*4096 : AUX+4352+w*CV_W), "s"(xrT), "s"(flags+FL_KV+xcd*1024+idx) : "memory", "scc", "vcc", "s68", "s69", "s70", "s71", "s75", "s76", "s77", "s88", "s89", "s90", "s91",
       "s66","s67","s78","s79","s80","s81","s82","s83","s84","s85","s86","s87","s94","s95","s96","s97","s98","s99",
       C_CLOB);
    TSDECL();
    STW(62,(unsigned)R0);
    { unsigned d[8]; asm volatile("s_mov_b32 %0, s78\n\ts_mov_b32 %1, s80\n\ts_mov_b32 %2, s82\n\ts_mov_b32 %3, s84\n\ts_mov_b32 %4, s86\n\ts_mov_b32 %5, s94\n\ts_mov_b32 %6, s96\n\ts_mov_b32 %7, s98" : "=s"(d[0]), "=s"(d[1]), "=s"(d[2]), "=s"(d[3]), "=s"(d[4]), "=s"(d[5]), "=s"(d[6]), "=s"(d[7]));
      STW(10,d[0]-(unsigned)T0); STW(11,d[1]-(unsigned)T0); STW(12,d[2]-(unsigned)T0); STW(13,d[3]-(unsigned)T0); STW(14,d[4]-(unsigned)T0); STW(7,d[5]-(unsigned)T0); STW(8,d[6]-(unsigned)T0); STW(5,d[7]-(unsigned)T0); asm volatile("s_mov_b32 %0, s66" : "=s"(d[0])); STW(9,d[0]-(unsigned)T0); }
    TS(20); TSDUMP();
  }
  return;
  }
  {
    unsigned long long T1; asm volatile("s_memrealtime %0" : "=&s"(T1) :: "memory");
    ARGS0(); BLOCK(); constexpr int WB=0; (void)WB; LANES(); TSDECL();
    const unsigned vx=(l>>3)*4096+(((l&7)^(l>>3))*16), vxs=vx+(s0+32*w)*2048, vw=(l>>3)*6144+((((l&7)>>1)^((l>>4)&1))*32)+(l&1)*16+hp*128+w*98304, vw1=(vw^64)+49152-1024;
    u32x8 va; u32x2 vb; unsigned vsp;
    #pragma unroll
    for(int i=0;i<6;i++) va[i]=((i&1)? vw1 : vw)+(i>>1)*2048;
    va[6]=vxs; va[7]=vxs+32768-1024; vb[0]=vxs+2048; vb[1]=vxs+2048+32768-1024;
    vsp=vx+(rb? s0-16+w : 0)*2048;
    const unsigned sw1=w*1024, sw2=w*2048;
    unsigned rot=ROTF(rbb);
    asm volatile("" : "+v"(va), "+v"(vb), "+v"(vsp));
    asm volatile("" : "+s"(rot));
    unsigned vt=(w&1)? (xr*512+idx*16+(l>>1))*2048+(l&1)*1024 : (2*idx+(l&1))*6144+((l>>1)&3)*2048+xc*1024;
    const int kt0=idx*64; int kvs=KS0+KVSEL;
    unsigned kvl=(kt0+16*w+g)*2048+xcd*256+fr*16, l16=l*16;
    const bool hl= w==0 && kt0;
    unsigned c46=sg(w<2? 128u : w==2? 393216u : 0u), c49=sg(w<2? sw1 : (unsigned)ALL_LDS-96), c64=sg(hl? 8192u : 0u), c68=sg(hl? AUX : ALL_LDS-96);
    unsigned long long c50= w<2? ~0ull : w==2? 63ull : 1ull, c66= hl? ~0ull : 1ull;
    asm volatile("" : "+v"(vt), "+v"(kvl), "+v"(l16));
    asm volatile("" : "+s"(c46), "+s"(c49), "+s"(c64), "+s"(c68), "+s"(c50), "+s"(c66), "+s"(kvs));
    unsigned long long T2; asm volatile("s_memrealtime %0" : "=&s"(T2) :: "memory");
    ARGS();
    asm volatile("" : "+s"(T1), "+s"(T2)); STW(18,(unsigned)(T1-T0)); STW(19,(unsigned)(T2-T0));
    STW(62,(unsigned)R0);
    TSB(16);
    asm volatile(H0_ASM : "+{s[40:41]}"(x), "+{s[42:43]}"(wqkv) : "{v[0:7]}"(va), "{v[8:9]}"(vb), "{v10}"(vsp), "{s52}"(rot), "{s53}"(sw2), "{s59}"(w)
      : "memory", "scc", "s76", "s77", "s78", "s79", "s82", "s83");
    const u16* spb= w==2? wqkv : x;
    const int hl3=(3-(int)((size_t)spb>>7))&7, roth=ROTF(hl3), xs=((hl3-ROTF(0))&7)-LEAD;
    unsigned i11=sg((w==2? roth+LEAD+1 : rot+1)&15), sbo, sst;
    if(w>=2){
      vsp=vt+hl3*128;
      touch(spb, vsp+((roth+(l>>3))&15)*c46, w==2? 0x3f3f3f3f3f3f3f3full>>(56-8*LEAD) : xs<=1? 0xffffffffull : 1ull);
    }
    TSB(17);
    unsigned gof=(((bid>>4)*128+64*(w&1)+4*(w>>1)+16*((l>>5)&1)+((l>>3)&3))*DM+(bid&15)*64)*2+(l16&127);
    unsigned wv[2];
    wv[0]=(((l>>5)*16+w*4+((l>>3)&3))*DM+tn*64)*2+((l16&127)^((l&16)*2)^((w&1)*64)); wv[1]=wv[0]+32*DM*2;
    unsigned zpa=(w>>1)*9216+(w&1)*512+l16, hda=AUX+w*CV_W+(l&1)*CV_CP+(l>>1)*16, yra=AUX+tq*(CV_W+64)+(g>>1)*128+(g&1)*32+tp*8+w*1024;
    constexpr unsigned FLB=12*S*DM;
    unsigned vc=(w&1)? FLB+(g? (1024+(g-1)*8192+(g==2? tm*16+tn : bid)*32)*4 : 0) : idx*CODE_ST+fr*128u;
    unsigned kfo=FLB+(FL_KV+bid*32+(l&31))*4, ofo= w==0? FLB+(FL_O+(tm*16+tn)*32+fr)*4 : FLB;
    asm volatile("" : "+v"(kfo), "+v"(ofo), "+v"(gof), "+v"(wv[0]), "+v"(wv[1]), "+v"(hda), "+v"(yra), "+v"(zpa), "+v"(vc));
    unsigned long long kt=korder(); asm volatile("" : "+s"(kt));
    unsigned nbar=0;
#define HDBG_O , "+{v40}"(stv), "+{s86}"(nbar)
#define HDBG_I , "{s87}"((unsigned)T0)
#define H_IN "{v[0:7]}"(va), "{v[8:9]}"(vb), "{v10}"(vsp), "{v11}"(kvl), "{s[40:41]}"(x), "{s[42:43]}"(wqkv), "{s[44:45]}"(spb), "{s46}"(c46), "{s49}"(c49), "{s[50:51]}"(c50), "{s53}"(sw2), \
      "{s58}"(kvs), "{s59}"(w), "{s[60:61]}"(kin), "{s[62:63]}"(vin), "{s64}"(c64), "{s[66:67]}"(c66), "{s68}"(c68) HDBG_I
#define H_CLOB "memory", "scc", "vcc", "s76", "s77", "s78", "s79", "s80", "s81", "s82", "s83", "s84", "s85", "v20", "v21", "v22", "v253", "v254", "v255"
    asm volatile(H1_ASM : "+{s47}"(i11), "+{s52}"(rot), "={s54}"(sbo), "={s55}"(sst) HDBG_O : H_IN : H_CLOB);
    const u16* hrow=hk+(size_t)(bid*4+w)*S;
    u32x4 h0; unsigned dvu;
    asm volatile("s_load_dwordx4 %0, %2, 0\n\ts_load_dword %1, %3, %4" : "=&s"(h0), "=&s"(dvu) : "s"(hrow), "s"(sD), "s"(bid*8+(w&2)*2) : "memory");
    unsigned long long pc; asm volatile("s_getpc_b64 %0" : "=s"(pc)); pc&=~4095ull;
    __builtin_amdgcn_s_setprio(HP1);
    asm volatile(H_ASM : "+{s47}"(i11), "+{s52}"(rot), "+{s54}"(sbo), "+{s55}"(sst) HDBG_O
      : H_IN, "{v14}"(vc), "{v16}"(kfo), "{s56}"(sg(3u<<(kvs-1) | 1u<<15)),
        "{s57}"(sg(((w&1)? (1u<<TGS | 1u<<KVP | 1u<<14 | ((1u<<(xs+8)>>8)&~3u)) : 1u<<CPS)&~(unsigned)(c50>>32))), "{s[70:71]}"(o), "{s[72:73]}"(pc) : H_CLOB);
    asm volatile("s_waitcnt lgkmcnt(0)" : "+s"(h0), "+s"(dvu) :: "memory");
    u32x4 qv[4], wr[6];
    BAR(); TSB(14);
    __builtin_amdgcn_s_setprio(HP2);
    { const unsigned fa=AUX+w*CV_W+32;
      asm volatile("s_mov_b64 exec, -2\n\ts_add_u32 m0, %3, %4\n\ts_nop 0\n\tglobal_load_lds_dwordx4 %0, %1" LDPOL "\n\ts_mov_b32 m0, %3\n\ts_nop 0\n\tglobal_load_lds_dwordx4 %0, %2" LDPOL "\n\ts_mov_b64 exec, -1"
        :: "v"(l16), "s"(hrow), "s"(hrow-2), "s"(fa), "n"(CV_CP) : "memory", "scc");
      #pragma unroll
      for(int i=1;i<4;i++){ gldsn(hrow+i*512, l16, fa, CV_CP+i*1024); gldsn(hrow-2+i*512, l16, fa, i*1024); } }
    const unsigned tag=__builtin_amdgcn_readfirstlane(vct[26]); const bool kvok=__builtin_amdgcn_readfirstlane(vct[5])!=0;
    u32x4 hv;
    { const unsigned d0=(h0[0]&0xffff0000u)|(pack2(bflo(h0[0])+((w&1)? bfhi(dvu) : bflo(dvu)),0.f)&0xffffu);
      hv= l==4? u32x4{0,d0,h0[1],h0[2]} : l==5? u32x4{d0,h0[1],h0[2],h0[3]} : u32x4{0,0,0,0}; }
    asm volatile("s_mov_b64 exec, 63\n\tds_write_b128 %0, %1\n\ts_mov_b64 exec, %3\n\tds_write_b128 %2, %1 offset:7680\n\ts_mov_b64 exec, -1"
      :: "v"(hda), "v"(hv), "v"(zpa), "s"(0xffffffff00000000ull) : "memory");
#define QLD() asm volatile("global_load_dwordx4 %0, %4, %5" LDPOL "\n\tglobal_load_dwordx4 %1, %4, %6" LDPOL "\n\tglobal_load_dwordx4 %2, %4, %7" LDPOL "\n\tglobal_load_dwordx4 %3, %4, %8" LDPOL \
        : "=&v"(qv[0]), "=&v"(qv[1]), "=&v"(qv[2]), "=&v"(qv[3]) : "v"(gof), "s"(qin), "s"(qin+8*DM), "s"(qin+32*DM), "s"(qin+40*DM) : "memory")
    auto kvt=[&](){
      #pragma unroll
      for(int i=0;i<4;i++) gldsc(xrT+(size_t)bid*8192+w*2048+i*512, l16, sw2, (i&1)*9216+(i>>1)*1024); };
    WAITV(8);
    vct[32+w]=1u;
    TSH(6);
    if(!kvok) wait_flag((const unsigned*)((const char*)o+kfo), tag);
    kvt();
    TSH(7);
    auto wos=[&](int st){ const u16* b=wo+(size_t)((kt>>(4*st))&15)*64*DM; glds(b,wv[0],sw1,WOB+st*8192); glds(b,wv[1],sw1,WOB+st*8192+4096); };
    wos(0); wos(1);
    if(QP==0) QLD();
    WAITV(QP==0? 8:4); WAITL();
    vct[16+w]=1u;
    TSH(8);
    __builtin_amdgcn_s_setprio(HP4);
    if(QP==1) QLD();
    while(!all4(36)) __builtin_amdgcn_s_sleep(1);
    #pragma unroll
    for(int st=2;st<7;st++){ wos(st); WAITV(W1); }
    while(!all4(0)) __builtin_amdgcn_s_sleep(1);
    #pragma unroll
    for(int st=7;st<13;st++){ wos(st); WAITV(W2); }
    #pragma unroll
    for(int st=0;st<3;st++){
      asm volatile("global_load_dwordx4 %0, %2, %4\n\tglobal_load_dwordx4 %1, %3, %4" : "=&v"(wr[2*st]), "=&v"(wr[2*st+1]) : "v"(wv[0]), "v"(wv[1]), "s"(wo+(size_t)((kt>>(52+4*st))&15)*64*DM) : "memory");
      WAITV(W3); }
    unsigned op;
    asm volatile("global_load_dword %0, %1, %2 sc1" : "=v"(op) : "v"(ofo), "s"(o) : "memory");
    if(QP==2) QLD();
    WAITV(QP==2? 11:7); vct[20+w]=13u;
    TSH(9);
    unsigned oseen=0;
    auto olevel=[&](unsigned v){ const unsigned long long ne=__ballot(v!=tag); const unsigned lv= ne==0 ? 2u : (ne&0xf7f7f7f7f7f7f7f7ull)==0 ? 1u : 0u;
      if(lv>oseen){ if(oseen==0){ TSH(10); } oseen=lv; if(l==0) __hip_atomic_fetch_max(ct+24, lv, __ATOMIC_RELAXED, __HIP_MEMORY_SCOPE_WORKGROUP); } };
    asm volatile("s_waitcnt vmcnt(0)" : "+v"(wr[0]), "+v"(wr[1]), "+v"(wr[2]), "+v"(wr[3]), "+v"(wr[4]), "+v"(wr[5]),
                                        "+v"(qv[0]), "+v"(qv[1]), "+v"(qv[2]), "+v"(qv[3]), "+v"(op) :: "memory");
    if(w==0) olevel(op);
    __builtin_amdgcn_s_setprio(HP3);
    bool gen= !(bid==0 && w==1);
    auto trygen=[&](){
      unsigned v[4];
      #pragma unroll
      for(int i=0;i<4;i++) v[i]=__hip_atomic_load(flags+FL_KV+(i*2+(l>>5))*1024+(l&31), __ATOMIC_RELAXED, __HIP_MEMORY_SCOPE_AGENT);
      if(__all(v[0]==tag && v[1]==tag && v[2]==tag && v[3]==tag)){ if(l==0) st4c(flags, tag); gen=true; } };
    if(!gen) trygen();
    float qf[32];
    #pragma unroll
    for(int i=0;i<16;i++){ qf[2*i]=bflo(qv[i>>2][i&3]); qf[2*i+1]=bfhi(qv[i>>2][i&3]); }
    #pragma unroll
    for(int i=0;i<32;i+=8) asm volatile("" : "+v"(qf[i]), "+v"(qf[i+1]), "+v"(qf[i+2]), "+v"(qf[i+3]), "+v"(qf[i+4]), "+v"(qf[i+5]), "+v"(qf[i+6]), "+v"(qf[i+7]));
    while(!all4(8)){
      if(w==0 && oseen<2) olevel(__hip_atomic_load((const unsigned*)((const char*)o+ofo), __ATOMIC_RELAXED, __HIP_MEMORY_SCOPE_AGENT));
      __builtin_amdgcn_s_sleep(OPOLLS);
    }
    TSH(11);
    __builtin_amdgcn_s_setprio(3);
    {
      const char* yb=smem+yra;
      s16x4 ya[4], yc[4];
      #pragma unroll
      for(int i=0;i<4;i++){ ya[i]=trread(yb+(i>>1)*512+(i&1)*64); yc[i]=trread(yb+(i>>1)*512+(i&1)*64+256); }
      #pragma unroll
      for(int i=0;i<4;i++) if(NOY!=1) st16c(yT+((size_t)(w*4+i)*256+bid)*512, l16, __builtin_bit_cast(u32x4,__builtin_shufflevector(ya[i],yc[i],0,1,2,3,4,5,6,7)));
    }
    #pragma unroll
    for(int i=0;i<6;i++) *(u32x4*)(smem+(i<4? i*4096 : AUX+4352+(i&1)*CV_W)+w*1024+l16)=wr[i];
    WAITL();
    vct[20+w]=16u;
    TSH(12);
    if(NOY){ for(int sp_=0; !gen && sp_<(1<<22); sp_++) trygen(); TS(20); TSDUMP(); return; }
    unsigned po=gof+(gof&~2047u);
    const u16* p0=yT+((size_t)(bid>>4)*256+(bid&15)*16)*512;
    unsigned y0=g*1024+w*256+((fr^(g&2))*16);
    asm volatile("" : "+v"(y0), "+v"(po));
    WAITV(0);
    if(l<4) st4c(flags+FL_Y+((w*4+l)*16+(bid>>4))*32+(bid&15), tag);
    TSH(13);
    if(w==0){ int sp_=0; while(oseen<2 && vct[24]<2u && ++sp_<(1<<22)){ olevel(__hip_atomic_load((const unsigned*)((const char*)o+ofo), __ATOMIC_RELAXED, __HIP_MEMORY_SCOPE_AGENT)); __builtin_amdgcn_s_sleep(8); } }
    if(w==0){
      const unsigned* fp=flags+FL_Y+bid*32+(l&15); int spins=0;
      while(vct[27]==0u) __builtin_amdgcn_s_sleep(2);
      while(true){
        unsigned v=__hip_atomic_load(fp, __ATOMIC_RELAXED, __HIP_MEMORY_SCOPE_AGENT);
        if(__all(v==tag) || ++spins>(1<<20)) break;
        __builtin_amdgcn_s_sleep(POLLS);
      }
      { u32x4 v; do{ v=*(volatile LDS_AS u32x4*)(ct+20); }while(__builtin_amdgcn_readfirstlane(v[0]&v[1]&v[2]&v[3]&16u)==0); }
      vct[25]=1u;
    } else { while(vct[25]==0u) __builtin_amdgcn_s_sleep(2); }
    TSH(14);
    {
      u32x4 yv[4];
      #pragma unroll
      for(int i=0;i<4;i++) gldsc(p0+i*2048, y0^(i*64), w*CV_W, AUX+i*1024);
      const char* ys=smem+AUX+w*CV_W+(l&7)*512;
      const int yi=((l>>3)^((l&7)*2))*16;
      WAITV(0);
      yv[0]=*(const u32x4*)(ys+yi); yv[1]=*(const u32x4*)(ys+yi+256); yv[2]=*(const u32x4*)(ys+(yi^128)); yv[3]=*(const u32x4*)(ys+(yi^128)+256);
      TSH(15);
      #pragma unroll
      for(int r=0;r<4;r++){
        u32x4 ov;
        #pragma unroll
        for(int e=0;e<4;e++){ const unsigned y2=yv[(r>>1)*2+(e>>1)][(r&1)*2+(e&1)]; ov[e]=pack2(qf[r*8+2*e]*bflo(y2), qf[r*8+2*e+1]*bfhi(y2)); }
        stos(out+((r&1)*8+(r>>1)*32)*2048, po, ov);
      }
    }
    for(int sp_=0; !gen && sp_<(1<<22); sp_++) trygen();
    TS(20); TSDUMP();
  }

}

extern "C" void kernel_launch(void* const* d_in, const int* in_sizes, int n_in, void* d_out, int out_size, void* d_ws, size_t ws_size, hipStream_t stream){
  const u16* q=(const u16*)d_in[0]; const u16* k=(const u16*)d_in[1]; const u16* v=(const u16*)d_in[2];
  const u16* shk=(const u16*)d_in[3]; const u16* hk=(const u16*)d_in[4]; const u16* sD=(const u16*)d_in[5];
  const u16* x=(const u16*)d_in[6]; const u16* wqkv=(const u16*)d_in[7]; const u16* wo=(const u16*)d_in[8];
  fused_kernel<<<256,512,0,stream>>>(x,wqkv,k,v,shk,hk,wo,q,sD,(u16*)d_out,(u16*)d_ws,(u16*)d_ws);
}
```

```cpp
#include <hip/hip_runtime.h>
#include <hip/hip_bf16.h>
#include <stdint.h>

typedef unsigned short u16;
typedef short s16x4 __attribute__((ext_vector_type(4)));
typedef float f32x32 __attribute__((ext_vector_type(32)));
typedef unsigned u32x4 __attribute__((ext_vector_type(4)));
typedef unsigned u32x2 __attribute__((ext_vector_type(2)));
#define LDS_AS __attribute__((address_space(3)))

__device__ __forceinline__ float bflo(unsigned x){ return __uint_as_float(x<<16); }
__device__ __forceinline__ float bfhi(unsigned x){ return __uint_as_float(x&0xffff0000u); }
typedef float f32x2 __attribute__((ext_vector_type(2)));
typedef __bf16 bf16x2_t __attribute__((ext_vector_type(2)));
__device__ __forceinline__ unsigned pack2(float lo, float hi){ return __builtin_bit_cast(unsigned, __builtin_convertvector(f32x2{lo,hi}, bf16x2_t)); }

#define GLDS(name,pol) __device__ __forceinline__ void name(const void* sb, unsigned voff, unsigned ldsb, int imm){ \
  asm volatile("s_add_u32 m0, %2, %3\n\ts_nop 0\n\tglobal_load_lds_dwordx4 %0, %1" pol :: "v"(voff), "s"(sb), "s"(ldsb), "n"(imm) : "memory", "scc"); }
#define LDPOL " nt"
GLDS(glds,"") GLDS(gldsn,LDPOL) GLDS(gldsc," sc1")
#define PACE 63
__device__ __forceinline__ void gldsp(const void* sb, unsigned voff, unsigned ldsb, int imm){
  asm volatile("s_add_u32 m0, %2, %3\n\ts_waitcnt vmcnt(%4)\n\tglobal_load_lds_dwordx4 %0, %1" :: "v"(voff), "s"(sb), "s"(ldsb), "n"(imm), "n"(PACE) : "memory", "scc"); }
__device__ __forceinline__ void touch(const void* sb, unsigned voff, unsigned long long mask){
  asm volatile("s_mov_b64 exec, %2\n\tglobal_load_dword v255, %0, %1\n\ts_mov_b64 exec, -1" :: "v"(voff), "s"(sb), "s"(mask) : "memory", "v255"); }
__device__ __forceinline__ void st16c(void* sb, unsigned vo, u32x4 v){ asm volatile("global_store_dwordx4 %0, %1, %2 sc1\n\ts_nop 1" :: "v"(vo), "v"(v), "s"(sb) : "memory"); }
#define OUTPOL " sc1 nt"
__device__ __forceinline__ void stos(void* sb, unsigned vo, u32x4 v){ asm volatile("global_store_dwordx4 %0, %1, %2" OUTPOL "\n\ts_nop 1" :: "v"(vo), "v"(v), "s"(sb) : "memory"); }
__device__ __forceinline__ void stoa(void* p, u32x4 v){ asm volatile("global_store_dwordx4 %0, %1, off" OUTPOL "\n\ts_nop 1" :: "v"(p), "v"(v) : "memory"); }
__device__ __forceinline__ void st4c(void* p, unsigned v){ asm volatile("global_store_dword %0, %1, off sc1" :: "v"(p), "v"(v) : "memory"); }
__device__ __forceinline__ void wait_flag(const unsigned* p, unsigned tag){
  int spins=0;
  while(true){
    unsigned v=__hip_atomic_load(p, __ATOMIC_RELAXED, __HIP_MEMORY_SCOPE_AGENT);
    if(__all(v==tag)) break;
    __builtin_amdgcn_s_sleep(2);
    if(++spins>(1<<22)) break;
  }
}
#define WAITV(n) asm volatile("s_waitcnt vmcnt(%0)"::"n"(n):"memory")
#define WAITL() asm volatile("s_waitcnt lgkmcnt(0)":::"memory")
#define BAR() do{ asm volatile("s_waitcnt lgkmcnt(0)":::"memory"); __builtin_amdgcn_s_barrier(); asm volatile("":::"memory"); }while(0)
#define LANES() int l; asm volatile("v_mbcnt_lo_u32_b32 %0, -1, 0\n\tv_mbcnt_hi_u32_b32 %0, -1, %0" : "=v"(l)); __builtin_assume(l>=0 && l<64); const int g=l>>4, fr=l&15, tq=(l&15)>>2, tp=l&3; (void)g; (void)fr; (void)tq; (void)tp;

__device__ __forceinline__ s16x4 trread(const char* p){ return __builtin_amdgcn_ds_read_tr16_b64_v4i16((LDS_AS s16x4*)p); }
__device__ __forceinline__ int wswz(int row){ return ((row>>1)&1) | (((row>>3)&1)<<1); }
__device__ __forceinline__ u32x4 widen(u32x2 a, u32x2 b){
  auto r0=__builtin_amdgcn_permlane16_swap(a[0],b[0],false,false);
  auto r1=__builtin_amdgcn_permlane16_swap(a[1],b[1],false,false);
  return u32x4{r0[0],r1[0],r0[1],r1[1]};
}

constexpr int S=2048, DM=1024;
constexpr int K1_XB=144*128, K1_WB=3*64*128, K1_ST=K1_XB+K1_WB;
constexpr int AUX=3*K1_ST, KVP_=1040;
constexpr int WOB=22528, CV_W=8448, CV_CP=4224;
constexpr int CT_OFF=AUX+33*KVP_;
constexpr int ALL_LDS=CT_OFF+176+96;
constexpr int FL_KV=1024, FL_O=FL_KV+8192, FL_Y=FL_O+8192;

#define CPRIO 2
#define CODE_ST 1590
#define LEAD 3
#define HP3 3
#define HP2 3
#define HP4 0
#define QP 1
#define TNX 1
#define HWS 99
#define HP1 0
#define HUNR 15
#define W0 6
#define OPD 8
#define WT0 99
#define W1 4
#define W2 4
#define W3 4
#define OPOLLS 2
#define POLLS 3
#define HPRE 0
#define CPS2 99
#define KR ((((idx>>2)*7)>>2)+(((idx&3)*7)>>1))
#define NOY 0
#define YLDS 1
#define OLDS 0
#define KVSEL (rbb==3? 0 : idx%KSN)
#define KS0 1
#define KSN 3
#define KST 9
#define KVP 5
#define TGS 2
#define CPS 13
#define H0_ASM \
  "s_mul_i32 s76, s52, 0x60000\n\t" \
  "s_add_u32 s76, s42, s76\n\t" \
  "s_addc_u32 s77, s43, 0\n\t" \
  "s_add_u32 s82, s53, 86016\n\t" \
  "s_add_u32 m0, s82, 18432\n\t" \
  "s_lshl_b32 s78, s52, 7\n\t" \
  "global_load_lds_dwordx4 v0, s[76:77]\n\t" \
  "global_load_lds_dwordx4 v1, s[76:77] offset:1024\n\t" \
  "s_add_u32 s78, s40, s78\n\t" \
  "s_addc_u32 s79, s41, 0\n\t" \
  "s_add_u32 m0, s82, 2048\n\t" \
  "s_nop 0\n\t" \
  "global_load_lds_dwordx4 v6, s[78:79]\n\t" \
  "global_load_lds_dwordx4 v7, s[78:79] offset:1024\n\t" \
  "s_add_u32 m0, s82, 26624\n\t" \
  "s_nop 0\n\t" \
  "global_load_lds_dwordx4 v2, s[76:77]\n\t" \
  "global_load_lds_dwordx4 v3, s[76:77] offset:1024\n\t" \
  "s_add_u32 m0, s82, 10240\n\t" \
  "s_nop 0\n\t" \
  "global_load_lds_dwordx4 v8, s[78:79]\n\t" \
  "global_load_lds_dwordx4 v9, s[78:79] offset:1024\n\t" \
  "s_add_u32 m0, s82, 34816\n\t" \
  "s_nop 0\n\t" \
  "global_load_lds_dwordx4 v4, s[76:77]\n\t" \
  "global_load_lds_dwordx4 v5, s[76:77] offset:1024\n\t" \
  "s_cmp_gt_u32 s59, 1\n\t" \
  "s_cbranch_scc1 1f\n\t" \
  "s_lshr_b32 s83, s53, 1\n\t" \
  "s_add_u32 m0, s83, 86016\n\t" \
  "s_nop 0\n\t" \
  "global_load_lds_dwordx4 v10, s[78:79]\n\t" \
  "1:\n\t"
#define H1_ASM \
  "s_mov_b32 s55, 0\n\t" \
  "s_mov_b32 s54, 0\n\t" \
  "s_add_u32 s52, s52, 1\n\t" \
  "s_and_b32 s52, s52, 15\n\t" \
  "s_cmp_eq_u32 s58, 1\n\t" \
  "s_cbranch_scc1 1f\n\t" \
  "s_mul_i32 s76, s52, 0x60000\n\t" \
  "s_add_u32 s76, s42, s76\n\t" \
  "s_addc_u32 s77, s43, 0\n\t" \
  "s_lshl_b32 s78, s52, 7\n\t" \
  "s_add_u32 s78, s40, s78\n\t" \
  "s_addc_u32 s79, s41, 0\n\t" \
  "s_mul_i32 s80, s47, s46\n\t" \
  "s_add_u32 s80, s44, s80\n\t" \
  "s_addc_u32 s81, s45, 0\n\t" \
  "s_mov_b32 s82, s53\n\t" \
  "s_add_u32 m0, s82, 18432\n\t" \
  "s_add_u32 s52, s52, 1\n\t" \
  "global_load_lds_dwordx4 v0, s[76:77]\n\t" \
  "global_load_lds_dwordx4 v1, s[76:77] offset:1024\n\t" \
  "s_add_u32 m0, s82, 2048\n\t" \
  "s_and_b32 s52, s52, 15\n\t" \
  "global_load_lds_dwordx4 v6, s[78:79]\n\t" \
  "global_load_lds_dwordx4 v7, s[78:79] offset:1024\n\t" \
  "s_add_u32 m0, s82, 26624\n\t" \
  "s_add_u32 s47, s47, 1\n\t" \
  "global_load_lds_dwordx4 v2, s[76:77]\n\t" \
  "global_load_lds_dwordx4 v3, s[76:77] offset:1024\n\t" \
  "s_add_u32 m0, s82, 10240\n\t" \
  "s_and_b32 s47, s47, 15\n\t" \
  "global_load_lds_dwordx4 v8, s[78:79]\n\t" \
  "global_load_lds_dwordx4 v9, s[78:79] offset:1024\n\t" \
  "s_add_u32 m0, s82, 34816\n\t" \
  "s_and_b32 s83, s54, s51\n\t" \
  "global_load_lds_dwordx4 v4, s[76:77]\n\t" \
  "global_load_lds_dwordx4 v5, s[76:77] offset:1024\n\t" \
  "s_add_u32 m0, s83, s49\n\t" \
  "s_mov_b64 exec, s[50:51]\n\t" \
  "global_load_lds_dwordx4 v10, s[80:81]\n\t" \
  "s_mov_b64 exec, -1\n\t" \
  "s_mov_b32 s55, 1\n\t" \
  "s_mov_b32 s54, 43008\n\t" \
  "s_waitcnt vmcnt(11)\n\t" \
  "s_branch 2f\n\t" \
  "1:\n\t" \
  "s_mul_i32 s83, s59, 4160\n\t" \
  "v_add_u32 v20, 7152, v11\n\t" \
  "v_add_u32 v21, 14304, v11\n\t" \
  "v_add_u32 v22, 21456, v11\n\t" \
  "s_add_u32 m0, s83, 130064\n\t" \
  "s_nop 0\n\t" \
  "global_load_lds_dwordx4 v11, s[60:61]" LDPOL "\n\t" \
  "global_load_lds_dwordx4 v20, s[60:61] offset:1040" LDPOL "\n\t" \
  "global_load_lds_dwordx4 v21, s[60:61] offset:2080" LDPOL "\n\t" \
  "global_load_lds_dwordx4 v22, s[60:61] offset:3120" LDPOL "\n\t" \
  "s_add_u32 m0, s83, 146704\n\t" \
  "s_nop 0\n\t" \
  "global_load_lds_dwordx4 v11, s[62:63]" LDPOL "\n\t" \
  "global_load_lds_dwordx4 v20, s[62:63] offset:1040" LDPOL "\n\t" \
  "global_load_lds_dwordx4 v21, s[62:63] offset:2080" LDPOL "\n\t" \
  "global_load_lds_dwordx4 v22, s[62:63] offset:3120" LDPOL "\n\t" \
  "v_subrev_u32 v20, s64, v11\n\t" \
  "s_mov_b32 m0, s68\n\t" \
  "s_mov_b64 exec, s[66:67]\n\t" \
  "global_load_lds_dwordx4 v20, s[60:61]" LDPOL "\n\t" \
  "s_mov_b64 exec, -1\n\t" \
  "s_waitcnt vmcnt(9)\n\t" \
  "2:\n\t" \
  "s_waitcnt lgkmcnt(0)\n\t" \
  "s_barrier\n\t"
#define H_ASM \
  "s_mul_i32 s76, s52, 0x60000\n\t" \
  "s_add_u32 s76, s42, s76\n\t" \
  "s_addc_u32 s77, s43, 0\n\t" \
  "s_lshl_b32 s78, s52, 7\n\t" \
  "s_add_u32 s78, s40, s78\n\t" \
  "s_addc_u32 s79, s41, 0\n\t" \
  "s_mul_i32 s80, s47, s46\n\t" \
  "s_add_u32 s80, s44, s80\n\t" \
  "s_addc_u32 s81, s45, 0\n\t" \
  "s_add_u32 s82, s54, s53\n\t" \
  "s_cmp_eq_u32 s58, 2\n\t" \
  "s_cbranch_scc1 5f\n\t" \
  "s_branch 3f\n\t" \
  "1:\n\t" \
  "s_waitcnt vmcnt(11)\n\t" \
  "2:\n\t" \
  "s_barrier\n\t" \
  "3:\n\t" \
  "s_add_u32 s55, s55, 1\n\t" \
  "s_add_u32 m0, s82, 18432\n\t" \
  "s_add_u32 s52, s52, 1\n\t" \
  "global_load_lds_dwordx4 v0, s[76:77]\n\t" \
  "global_load_lds_dwordx4 v1, s[76:77] offset:1024\n\t" \
  "s_add_u32 m0, s82, 2048\n\t" \
  "s_and_b32 s52, s52, 15\n\t" \
  "global_load_lds_dwordx4 v6, s[78:79]\n\t" \
  "global_load_lds_dwordx4 v7, s[78:79] offset:1024\n\t" \
  "s_add_u32 m0, s82, 26624\n\t" \
  "s_add_u32 s47, s47, 1\n\t" \
  "global_load_lds_dwordx4 v2, s[76:77]\n\t" \
  "global_load_lds_dwordx4 v3, s[76:77] offset:1024\n\t" \
  "s_add_u32 m0, s82, 10240\n\t" \
  "s_and_b32 s47, s47, 15\n\t" \
  "global_load_lds_dwordx4 v8, s[78:79]\n\t" \
  "global_load_lds_dwordx4 v9, s[78:79] offset:1024\n\t" \
  "s_add_u32 m0, s82, 34816\n\t" \
  "s_and_b32 s83, s54, s51\n\t" \
  "global_load_lds_dwordx4 v4, s[76:77]\n\t" \
  "global_load_lds_dwordx4 v5, s[76:77] offset:1024\n\t" \
  "s_bitcmp1_b32 s57, s55\n\t" \
  "s_cbranch_scc1 10f\n\t" \
  "8:\n\t" \
  "s_add_u32 m0, s83, s49\n\t" \
  "s_mov_b64 exec, s[50:51]\n\t" \
  "global_load_lds_dwordx4 v10, s[80:81]\n\t" \
  "s_mov_b64 exec, -1\n\t" \
  "4:\n\t" \
  "s_mul_i32 s76, s52, 0x60000\n\t" \
  "s_add_u32 s76, s42, s76\n\t" \
  "s_addc_u32 s77, s43, 0\n\t" \
  "s_lshl_b32 s78, s52, 7\n\t" \
  "s_add_u32 s78, s40, s78\n\t" \
  "s_addc_u32 s79, s41, 0\n\t" \
  "s_mul_i32 s80, s47, s46\n\t" \
  "s_add_u32 s80, s44, s80\n\t" \
  "s_addc_u32 s81, s45, 0\n\t" \
  "s_add_u32 s54, s54, 43008\n\t" \
  "s_cmp_eq_u32 s54, 129024\n\t" \
  "s_cselect_b32 s54, 0, s54\n\t" \
  "s_add_u32 s82, s54, s53\n\t" \
  "s_bitcmp0_b32 s56, s55\n\t" \
  "s_cbranch_scc1 1b\n\t" \
  "s_cmp_eq_u32 s55, 15\n\t" \
  "s_cbranch_scc1 19f\n\t" \
  "s_cmp_eq_u32 s55, s58\n\t" \
  "s_cbranch_scc1 3b\n\t" \
  "s_waitcnt vmcnt(11)\n\t" \
  "s_barrier\n\t" \
  "5:\n\t" \
  "s_mul_i32 s83, s59, 4160\n\t" \
  "v_add_u32 v20, 7152, v11\n\t" \
  "v_add_u32 v21, 14304, v11\n\t" \
  "v_add_u32 v22, 21456, v11\n\t" \
  "s_add_u32 m0, s83, 130064\n\t" \
  "s_nop 0\n\t" \
  "global_load_lds_dwordx4 v11, s[60:61]" LDPOL "\n\t" \
  "global_load_lds_dwordx4 v20, s[60:61] offset:1040" LDPOL "\n\t" \
  "global_load_lds_dwordx4 v21, s[60:61] offset:2080" LDPOL "\n\t" \
  "global_load_lds_dwordx4 v22, s[60:61] offset:3120" LDPOL "\n\t" \
  "s_add_u32 m0, s83, 146704\n\t" \
  "s_nop 0\n\t" \
  "global_load_lds_dwordx4 v11, s[62:63]" LDPOL "\n\t" \
  "global_load_lds_dwordx4 v20, s[62:63] offset:1040" LDPOL "\n\t" \
  "global_load_lds_dwordx4 v21, s[62:63] offset:2080" LDPOL "\n\t" \
  "global_load_lds_dwordx4 v22, s[62:63] offset:3120" LDPOL "\n\t" \
  "v_subrev_u32 v20, s64, v11\n\t" \
  "s_mov_b32 m0, s68\n\t" \
  "s_mov_b64 exec, s[66:67]\n\t" \
  "global_load_lds_dwordx4 v20, s[60:61]" LDPOL "\n\t" \
  "s_mov_b64 exec, -1\n\t" \
  "s_waitcnt vmcnt(9)\n\t" \
  "s_branch 2b\n\t" \
  "10:\n\t" \
  "s_cmp_eq_u32 s59, 2\n\t" \
  "s_cbranch_scc0 11f\n\t" \
  "s_mov_b64 exec, 0xffff\n\t" \
  "global_load_dword v255, v14, s[72:73]\n\t" \
  "s_mov_b64 exec, -1\n\t" \
  "s_branch 4b\n\t" \
  "11:\n\t" \
  "s_cmp_eq_u32 s55, 2\n\t" \
  "s_cbranch_scc0 12f\n\t" \
  "global_load_dword v254, v14, s[70:71] sc1\n\t" \
  "s_branch 4b\n\t" \
  "12:\n\t" \
  "s_cmp_eq_u32 s55, 5\n\t" \
  "s_cbranch_scc0 13f\n\t" \
  "v_readfirstlane_b32 s84, v254\n\t" \
  "v_mov_b32 v253, 163448\n\t" \
  "s_nop 1\n\t" \
  "s_add_u32 s84, s84, 1\n\t" \
  "v_mov_b32 v254, s84\n\t" \
  "ds_write_b32 v253, v254\n\t" \
  "s_waitcnt lgkmcnt(0)\n\t" \
  "s_branch 8b\n\t" \
  "13:\n\t" \
  "s_cmp_eq_u32 s55, 14\n\t" \
  "s_cbranch_scc0 14f\n\t" \
  "global_load_dword v253, v16, s[70:71] sc1\n\t" \
  "s_branch 4b\n\t" \
  "14:\n\t" \
  "s_mov_b32 exec_hi, 0\n\t" \
  "global_load_dword v255, v10, s[44:45]\n\t" \
  "s_mov_b64 exec, -1\n\t" \
  "s_branch 4b\n\t" \
  "19:\n\t" \
  "s_waitcnt vmcnt(11)\n\t" \
  "s_cmp_lg_u32 s59, 3\n\t" \
  "s_cbranch_scc1 18f\n\t" \
  "v_cmp_ne_u32 vcc, v253, v254\n\t" \
  "v_mov_b32 v253, 163364\n\t" \
  "s_nop 1\n\t" \
  "s_cmp_eq_u64 vcc, 0\n\t" \
  "s_cselect_b32 s83, 1, 0\n\t" \
  "v_mov_b32 v254, s83\n\t" \
  "ds_write_b32 v253, v254\n\t" \
  "18:\n\t"
#define C_ASM \
  "s_branch 29f\n\t" \
  "28:\n\t" \
  "s_mov_b64 exec, 0\n\t" \
  "s_cmp_eq_u32 %13, 0\n\t" \
  "s_cbranch_scc1 500f\n\t" \
  "s_cmp_eq_u32 %13, 1\n\t" \
  "s_cbranch_scc1 501f\n\t" \
  "s_cmp_eq_u32 %13, 2\n\t" \
  "s_cbranch_scc1 502f\n\t" \
  "s_branch 503f\n\t" \
  "26:\n\t" \
  "s_mov_b64 exec, -1\n\t" \
  "s_add_u32 s88, s88, 4\n\t" \
  "s_addc_u32 s89, s89, 0\n\t" \
  "s_setpc_b64 s[88:89]\n\t" \
  ".p2align 12\n\t" \
  "29:\n\t" \
  "s_bitcmp0_b32 %13, 0\n\t" \
  "s_cbranch_scc1 8f\n\t" \
  "global_load_dwordx2 v[10:11], v51, %11\n\t" \
  "global_load_dwordx2 v[12:13], v51, %11 offset:128\n\t" \
  "v_xor_b32 v53, 64, v52\n\t" \
  "v_xor_b32 v55, 32, v54\n\t" \
  "v_add_u32 v56, 43008, v52\n\t" \
  "v_add_u32 v58, 43008, v54\n\t" \
  "v_xor_b32 v57, 64, v56\n\t" \
  "v_xor_b32 v59, 32, v58\n\t" \
  "v_add_u32 v60, 86016, v52\n\t" \
  "v_add_u32 v62, 86016, v54\n\t" \
  "v_xor_b32 v61, 64, v60\n\t" \
  "v_xor_b32 v63, 32, v62\n\t" \
  "s_mov_b32 vcc_hi, 0x05040100\n\t" \
  "s_waitcnt vmcnt(2)\n\t" \
  "s_barrier\n\t" \
  "ds_read_b64_tr_b16 v[64:65], v62 offset:26624\n\t" \
  "ds_read_b64_tr_b16 v[66:67], v62 offset:27136\n\t" \
  "ds_read_b128 v[68:71], v60 offset:10240\n\t" \
  "ds_read_b64_tr_b16 v[72:73], v62 offset:30720\n\t" \
  "ds_read_b64_tr_b16 v[74:75], v62 offset:31232\n\t" \
  "ds_read_b128 v[76:79], v61 offset:10240\n\t" \
  "ds_read_b128 v[80:83], v61 offset:12288\n\t" \
  "ds_read_b128 v[84:87], v60 offset:12288\n\t" \
  "ds_read_b128 v[88:91], v60 offset:14336\n\t" \
  "ds_read_b128 v[92:95], v61 offset:14336\n\t" \
  "ds_read_b128 v[96:99], v61 offset:16384\n\t" \
  "ds_read_b128 v[100:103], v60 offset:16384\n\t" \
  "ds_read_b64_tr_b16 v[104:105], v63 offset:26624\n\t" \
  "ds_read_b64_tr_b16 v[106:107], v63 offset:27136\n\t" \
  "ds_read_b64_tr_b16 v[108:109], v63 offset:30720\n\t" \
  "ds_read_b64_tr_b16 v[110:111], v63 offset:31232\n\t" \
  "s_waitcnt lgkmcnt(13)\n\t" \
  "v_mfma_f32_16x16x32_bf16 v[184:187], v[64:67], v[68:71], 0\n\t" \
  "ds_read_b64_tr_b16 v[112:113], v62 offset:34816\n\t" \
  "ds_read_b64_tr_b16 v[114:115], v62 offset:35328\n\t" \
  "s_waitcnt lgkmcnt(12)\n\t" \
  "v_mfma_f32_16x16x32_bf16 v[184:187], v[72:75], v[76:79], v[184:187]\n\t" \
  "ds_read_b64_tr_b16 v[116:117], v62 offset:38912\n\t" \
  "ds_read_b64_tr_b16 v[118:119], v62 offset:39424\n\t" \
  "s_waitcnt lgkmcnt(13)\n\t" \
  "v_mfma_f32_16x16x32_bf16 v[192:195], v[72:75], v[80:83], 0\n\t" \
  "ds_read_b128 v[120:123], v60 offset:0\n\t" \
  "s_waitcnt lgkmcnt(13)\n\t" \
  "v_mfma_f32_16x16x32_bf16 v[192:195], v[64:67], v[84:87], v[192:195]\n\t" \
  "ds_read_b128 v[124:127], v61 offset:0\n\t" \
  "s_waitcnt lgkmcnt(13)\n\t" \
  "v_mfma_f32_16x16x32_bf16 v[200:203], v[64:67], v[88:91], 0\n\t" \
  "ds_read_b64_tr_b16 v[128:129], v63 offset:34816\n\t" \
  "s_waitcnt lgkmcnt(13)\n\t" \
  "v_mfma_f32_16x16x32_bf16 v[200:203], v[72:75], v[92:95], v[200:203]\n\t" \
  "ds_read_b64_tr_b16 v[130:131], v63 offset:35328\n\t" \
  "s_waitcnt lgkmcnt(13)\n\t" \
  "v_mfma_f32_16x16x32_bf16 v[208:211], v[72:75], v[96:99], 0\n\t" \
  "ds_read_b64_tr_b16 v[132:133], v63 offset:38912\n\t" \
  "s_waitcnt lgkmcnt(9)\n\t" \
  "v_mfma_f32_16x16x32_bf16 v[208:211], v[64:67], v[100:103], v[208:211]\n\t" \
  "ds_read_b64_tr_b16 v[134:135], v63 offset:39424\n\t" \
  "v_mfma_f32_16x16x32_bf16 v[188:191], v[104:107], v[68:71], 0\n\t" \
  "ds_read_b64_tr_b16 v[136:137], v62 offset:18432\n\t" \
  "v_mfma_f32_16x16x32_bf16 v[188:191], v[108:111], v[76:79], v[188:191]\n\t" \
  "ds_read_b64_tr_b16 v[138:139], v62 offset:18944\n\t" \
  "v_mfma_f32_16x16x32_bf16 v[196:199], v[108:111], v[80:83], 0\n\t" \
  "ds_read_b64_tr_b16 v[140:141], v62 offset:22528\n\t" \
  "v_mfma_f32_16x16x32_bf16 v[196:199], v[104:107], v[84:87], v[196:199]\n\t" \
  "ds_read_b64_tr_b16 v[142:143], v62 offset:23040\n\t" \
  "v_mfma_f32_16x16x32_bf16 v[204:207], v[104:107], v[88:91], 0\n\t" \
  "ds_read_b64_tr_b16 v[144:145], v63 offset:18432\n\t" \
  "v_mfma_f32_16x16x32_bf16 v[204:207], v[108:111], v[92:95], v[204:207]\n\t" \
  "ds_read_b64_tr_b16 v[146:147], v63 offset:18944\n\t" \
  "v_mfma_f32_16x16x32_bf16 v[212:215], v[108:111], v[96:99], 0\n\t" \
  "ds_read_b64_tr_b16 v[148:149], v63 offset:22528\n\t" \
  "v_mfma_f32_16x16x32_bf16 v[212:215], v[104:107], v[100:103], v[212:215]\n\t" \
  "ds_read_b64_tr_b16 v[150:151], v63 offset:23040\n\t" \
  "s_waitcnt lgkmcnt(7)\n\t" \
  "v_mfma_f32_16x16x32_bf16 v[216:219], v[68:71], v[112:115], 0\n\t" \
  "v_mfma_f32_16x16x32_bf16 v[216:219], v[76:79], v[116:119], v[216:219]\n\t" \
  "v_mfma_f32_16x16x32_bf16 v[224:227], v[80:83], v[116:119], 0\n\t" \
  "v_mfma_f32_16x16x32_bf16 v[224:227], v[84:87], v[112:115], v[224:227]\n\t" \
  "v_mfma_f32_16x16x32_bf16 v[232:235], v[88:91], v[112:115], 0\n\t" \
  "v_mfma_f32_16x16x32_bf16 v[232:235], v[92:95], v[116:119], v[232:235]\n\t" \
  "v_mfma_f32_16x16x32_bf16 v[240:243], v[96:99], v[116:119], 0\n\t" \
  "v_mfma_f32_16x16x32_bf16 v[240:243], v[100:103], v[112:115], v[240:243]\n\t" \
  "v_mfma_f32_16x16x32_bf16 v[248:251], v[120:123], v[112:115], 0\n\t" \
  "v_mfma_f32_16x16x32_bf16 v[248:251], v[124:127], v[116:119], v[248:251]\n\t" \
  "v_mfma_f32_16x16x32_bf16 v[252:255], v[120:123], v[128:131], 0\n\t" \
  "v_mfma_f32_16x16x32_bf16 v[252:255], v[124:127], v[132:135], v[252:255]\n\t" \
  "v_mfma_f32_16x16x32_bf16 v[220:223], v[76:79], v[132:135], 0\n\t" \
  "v_mfma_f32_16x16x32_bf16 v[220:223], v[68:71], v[128:131], v[220:223]\n\t" \
  "s_waitcnt lgkmcnt(0)\n\t" \
  "s_getpc_b64 s[88:89]\n\t" \
  "s_branch 28b\n\t" \
  "s_barrier\n\t" \
  "v_mfma_f32_16x16x32_bf16 v[228:231], v[84:87], v[128:131], 0\n\t" \
  "ds_read_b64_tr_b16 v[72:73], v54 offset:26624\n\t" \
  "v_mfma_f32_16x16x32_bf16 v[228:231], v[80:83], v[132:135], v[228:231]\n\t" \
  "ds_read_b64_tr_b16 v[74:75], v54 offset:27136\n\t" \
  "v_mfma_f32_16x16x32_bf16 v[236:239], v[92:95], v[132:135], 0\n\t" \
  "ds_read_b128 v[64:67], v52 offset:10240\n\t" \
  "v_mfma_f32_16x16x32_bf16 v[236:239], v[88:91], v[128:131], v[236:239]\n\t" \
  "ds_read_b64_tr_b16 v[108:109], v54 offset:30720\n\t" \
  "v_mfma_f32_16x16x32_bf16 v[244:247], v[100:103], v[128:131], 0\n\t" \
  "ds_read_b64_tr_b16 v[110:111], v54 offset:31232\n\t" \
  "v_mfma_f32_16x16x32_bf16 v[244:247], v[96:99], v[132:135], v[244:247]\n\t" \
  "ds_read_b128 v[104:107], v53 offset:10240\n\t" \
  "v_mfma_f32_16x16x32_bf16 v[152:155], v[136:139], v[68:71], 0\n\t" \
  "ds_read_b128 v[112:115], v53 offset:12288\n\t" \
  "v_mfma_f32_16x16x32_bf16 v[152:155], v[140:143], v[76:79], v[152:155]\n\t" \
  "ds_read_b128 v[116:119], v52 offset:12288\n\t" \
  "v_mfma_f32_16x16x32_bf16 v[160:163], v[140:143], v[80:83], 0\n\t" \
  "ds_read_b128 v[120:123], v52 offset:14336\n\t" \
  "v_mfma_f32_16x16x32_bf16 v[160:163], v[136:139], v[84:87], v[160:163]\n\t" \
  "ds_read_b128 v[124:127], v53 offset:14336\n\t" \
  "v_mfma_f32_16x16x32_bf16 v[168:171], v[136:139], v[88:91], 0\n\t" \
  "ds_read_b128 v[128:131], v53 offset:16384\n\t" \
  "v_mfma_f32_16x16x32_bf16 v[168:171], v[140:143], v[92:95], v[168:171]\n\t" \
  "ds_read_b128 v[132:135], v52 offset:16384\n\t" \
  "v_mfma_f32_16x16x32_bf16 v[176:179], v[140:143], v[96:99], 0\n\t" \
  "ds_read_b64_tr_b16 v[140:141], v55 offset:26624\n\t" \
  "v_mfma_f32_16x16x32_bf16 v[176:179], v[136:139], v[100:103], v[176:179]\n\t" \
  "ds_read_b64_tr_b16 v[142:143], v55 offset:27136\n\t" \
  "v_mfma_f32_16x16x32_bf16 v[156:159], v[144:147], v[68:71], 0\n\t" \
  "ds_read_b64_tr_b16 v[136:137], v55 offset:30720\n\t" \
  "v_mfma_f32_16x16x32_bf16 v[156:159], v[148:151], v[76:79], v[156:159]\n\t" \
  "ds_read_b64_tr_b16 v[138:139], v55 offset:31232\n\t" \
  "v_mfma_f32_16x16x32_bf16 v[164:167], v[148:151], v[80:83], 0\n\t" \
  "ds_read_b64_tr_b16 v[68:69], v54 offset:34816\n\t" \
  "v_mfma_f32_16x16x32_bf16 v[164:167], v[144:147], v[84:87], v[164:167]\n\t" \
  "ds_read_b64_tr_b16 v[70:71], v54 offset:35328\n\t" \
  "v_mfma_f32_16x16x32_bf16 v[172:175], v[144:147], v[88:91], 0\n\t" \
  "ds_read_b64_tr_b16 v[76:77], v54 offset:38912\n\t" \
  "v_mfma_f32_16x16x32_bf16 v[172:175], v[148:151], v[92:95], v[172:175]\n\t" \
  "ds_read_b64_tr_b16 v[78:79], v54 offset:39424\n\t" \
  "v_mfma_f32_16x16x32_bf16 v[180:183], v[148:151], v[96:99], 0\n\t" \
  "ds_read_b128 v[80:83], v52 offset:0\n\t" \
  "v_mfma_f32_16x16x32_bf16 v[180:183], v[144:147], v[100:103], v[180:183]\n\t" \
  "ds_read_b128 v[84:87], v53 offset:0\n\t" \
  "s_waitcnt lgkmcnt(7)\n\t" \
  "v_mfma_f32_16x16x32_bf16 v[184:187], v[72:75], v[64:67], v[184:187]\n\t" \
  "ds_read_b64_tr_b16 v[88:89], v55 offset:34816\n\t" \
  "v_mfma_f32_16x16x32_bf16 v[184:187], v[108:111], v[104:107], v[184:187]\n\t" \
  "ds_read_b64_tr_b16 v[90:91], v55 offset:35328\n\t" \
  "v_mfma_f32_16x16x32_bf16 v[192:195], v[108:111], v[112:115], v[192:195]\n\t" \
  "ds_read_b64_tr_b16 v[92:93], v55 offset:38912\n\t" \
  "v_mfma_f32_16x16x32_bf16 v[192:195], v[72:75], v[116:119], v[192:195]\n\t" \
  "ds_read_b64_tr_b16 v[94:95], v55 offset:39424\n\t" \
  "v_mfma_f32_16x16x32_bf16 v[200:203], v[72:75], v[120:123], v[200:203]\n\t" \
  "ds_read_b64_tr_b16 v[96:97], v54 offset:18432\n\t" \
  "v_mfma_f32_16x16x32_bf16 v[200:203], v[108:111], v[124:127], v[200:203]\n\t" \
  "ds_read_b64_tr_b16 v[98:99], v54 offset:18944\n\t" \
  "v_mfma_f32_16x16x32_bf16 v[208:211], v[108:111], v[128:131], v[208:211]\n\t" \
  "ds_read_b64_tr_b16 v[148:149], v54 offset:22528\n\t" \
  "v_mfma_f32_16x16x32_bf16 v[208:211], v[72:75], v[132:135], v[208:211]\n\t" \
  "ds_read_b64_tr_b16 v[150:151], v54 offset:23040\n\t" \
  "v_mfma_f32_16x16x32_bf16 v[188:191], v[140:143], v[64:67], v[188:191]\n\t" \
  "ds_read_b64_tr_b16 v[100:101], v55 offset:18432\n\t" \
  "s_waitcnt lgkmcnt(7)\n\t" \
  "v_mfma_f32_16x16x32_bf16 v[188:191], v[136:139], v[104:107], v[188:191]\n\t" \
  "ds_read_b64_tr_b16 v[102:103], v55 offset:18944\n\t" \
  "v_mfma_f32_16x16x32_bf16 v[196:199], v[136:139], v[112:115], v[196:199]\n\t" \
  "ds_read_b64_tr_b16 v[144:145], v55 offset:22528\n\t" \
  "v_mfma_f32_16x16x32_bf16 v[196:199], v[140:143], v[116:119], v[196:199]\n\t" \
  "ds_read_b64_tr_b16 v[146:147], v55 offset:23040\n\t" \
  "v_mfma_f32_16x16x32_bf16 v[204:207], v[140:143], v[120:123], v[204:207]\n\t" \
  "v_mfma_f32_16x16x32_bf16 v[204:207], v[136:139], v[124:127], v[204:207]\n\t" \
  "v_mfma_f32_16x16x32_bf16 v[212:215], v[136:139], v[128:131], v[212:215]\n\t" \
  "v_mfma_f32_16x16x32_bf16 v[212:215], v[140:143], v[132:135], v[212:215]\n\t" \
  "v_mfma_f32_16x16x32_bf16 v[216:219], v[64:67], v[68:71], v[216:219]\n\t" \
  "v_mfma_f32_16x16x32_bf16 v[216:219], v[104:107], v[76:79], v[216:219]\n\t" \
  "v_mfma_f32_16x16x32_bf16 v[224:227], v[112:115], v[76:79], v[224:227]\n\t" \
  "v_mfma_f32_16x16x32_bf16 v[224:227], v[116:119], v[68:71], v[224:227]\n\t" \
  "v_mfma_f32_16x16x32_bf16 v[232:235], v[120:123], v[68:71], v[232:235]\n\t" \
  "v_mfma_f32_16x16x32_bf16 v[232:235], v[124:127], v[76:79], v[232:235]\n\t" \
  "v_mfma_f32_16x16x32_bf16 v[240:243], v[128:131], v[76:79], v[240:243]\n\t" \
  "v_mfma_f32_16x16x32_bf16 v[240:243], v[132:135], v[68:71], v[240:243]\n\t" \
  "v_mfma_f32_16x16x32_bf16 v[248:251], v[80:83], v[68:71], v[248:251]\n\t" \
  "v_mfma_f32_16x16x32_bf16 v[248:251], v[84:87], v[76:79], v[248:251]\n\t" \
  "v_mfma_f32_16x16x32_bf16 v[252:255], v[80:83], v[88:91], v[252:255]\n\t" \
  "s_waitcnt lgkmcnt(0)\n\t" \
  "v_mfma_f32_16x16x32_bf16 v[252:255], v[84:87], v[92:95], v[252:255]\n\t" \
  "v_mfma_f32_16x16x32_bf16 v[220:223], v[104:107], v[92:95], v[220:223]\n\t" \
  "v_mfma_f32_16x16x32_bf16 v[220:223], v[64:67], v[88:91], v[220:223]\n\t" \
  "s_barrier\n\t" \
  "v_mfma_f32_16x16x32_bf16 v[228:231], v[116:119], v[88:91], v[228:231]\n\t" \
  "ds_read_b64_tr_b16 v[108:109], v58 offset:26624\n\t" \
  "v_mfma_f32_16x16x32_bf16 v[228:231], v[112:115], v[92:95], v[228:231]\n\t" \
  "ds_read_b64_tr_b16 v[110:111], v58 offset:27136\n\t" \
  "v_mfma_f32_16x16x32_bf16 v[236:239], v[124:127], v[92:95], v[236:239]\n\t" \
  "ds_read_b128 v[72:75], v56 offset:10240\n\t" \
  "v_mfma_f32_16x16x32_bf16 v[236:239], v[120:123], v[88:91], v[236:239]\n\t" \
  "ds_read_b64_tr_b16 v[136:137], v58 offset:30720\n\t" \
  "v_mfma_f32_16x16x32_bf16 v[244:247], v[132:135], v[88:91], v[244:247]\n\t" \
  "ds_read_b64_tr_b16 v[138:139], v58 offset:31232\n\t" \
  "v_mfma_f32_16x16x32_bf16 v[244:247], v[128:131], v[92:95], v[244:247]\n\t" \
  "ds_read_b128 v[140:143], v57 offset:10240\n\t" \
  "v_mfma_f32_16x16x32_bf16 v[152:155], v[96:99], v[64:67], v[152:155]\n\t" \
  "ds_read_b128 v[68:71], v57 offset:12288\n\t" \
  "v_mfma_f32_16x16x32_bf16 v[152:155], v[148:151], v[104:107], v[152:155]\n\t" \
  "ds_read_b128 v[76:79], v56 offset:12288\n\t" \
  "v_mfma_f32_16x16x32_bf16 v[160:163], v[148:151], v[112:115], v[160:163]\n\t" \
  "ds_read_b128 v[80:83], v56 offset:14336\n\t" \
  "v_mfma_f32_16x16x32_bf16 v[160:163], v[96:99], v[116:119], v[160:163]\n\t" \
  "ds_read_b128 v[84:87], v57 offset:14336\n\t" \
  "v_mfma_f32_16x16x32_bf16 v[168:171], v[96:99], v[120:123], v[168:171]\n\t" \
  "ds_read_b128 v[88:91], v57 offset:16384\n\t" \
  "v_mfma_f32_16x16x32_bf16 v[168:171], v[148:151], v[124:127], v[168:171]\n\t" \
  "ds_read_b128 v[92:95], v56 offset:16384\n\t" \
  "v_mfma_f32_16x16x32_bf16 v[176:179], v[148:151], v[128:131], v[176:179]\n\t" \
  "ds_read_b64_tr_b16 v[148:149], v59 offset:26624\n\t" \
  "v_mfma_f32_16x16x32_bf16 v[176:179], v[96:99], v[132:135], v[176:179]\n\t" \
  "ds_read_b64_tr_b16 v[150:151], v59 offset:27136\n\t" \
  "v_mfma_f32_16x16x32_bf16 v[156:159], v[100:103], v[64:67], v[156:159]\n\t" \
  "ds_read_b64_tr_b16 v[96:97], v59 offset:30720\n\t" \
  "v_mfma_f32_16x16x32_bf16 v[156:159], v[144:147], v[104:107], v[156:159]\n\t" \
  "ds_read_b64_tr_b16 v[98:99], v59 offset:31232\n\t" \
  "v_mfma_f32_16x16x32_bf16 v[164:167], v[144:147], v[112:115], v[164:167]\n\t" \
  "ds_read_b64_tr_b16 v[64:65], v58 offset:34816\n\t" \
  "v_mfma_f32_16x16x32_bf16 v[164:167], v[100:103], v[116:119], v[164:167]\n\t" \
  "ds_read_b64_tr_b16 v[66:67], v58 offset:35328\n\t" \
  "v_mfma_f32_16x16x32_bf16 v[172:175], v[100:103], v[120:123], v[172:175]\n\t" \
  "ds_read_b64_tr_b16 v[104:105], v58 offset:38912\n\t" \
  "v_mfma_f32_16x16x32_bf16 v[172:175], v[144:147], v[124:127], v[172:175]\n\t" \
  "ds_read_b64_tr_b16 v[106:107], v58 offset:39424\n\t" \
  "v_mfma_f32_16x16x32_bf16 v[180:183], v[144:147], v[128:131], v[180:183]\n\t" \
  "ds_read_b128 v[112:115], v56 offset:0\n\t" \
  "v_mfma_f32_16x16x32_bf16 v[180:183], v[100:103], v[132:135], v[180:183]\n\t" \
  "ds_read_b128 v[116:119], v57 offset:0\n\t" \
  "s_waitcnt lgkmcnt(7)\n\t" \
  "v_mfma_f32_16x16x32_bf16 v[184:187], v[108:111], v[72:75], v[184:187]\n\t" \
  "ds_read_b64_tr_b16 v[120:121], v59 offset:34816\n\t" \
  "v_mfma_f32_16x16x32_bf16 v[184:187], v[136:139], v[140:143], v[184:187]\n\t" \
  "ds_read_b64_tr_b16 v[122:123], v59 offset:35328\n\t" \
  "v_mfma_f32_16x16x32_bf16 v[192:195], v[136:139], v[68:71], v[192:195]\n\t" \
  "ds_read_b64_tr_b16 v[124:125], v59 offset:38912\n\t" \
  "v_mfma_f32_16x16x32_bf16 v[192:195], v[108:111], v[76:79], v[192:195]\n\t" \
  "ds_read_b64_tr_b16 v[126:127], v59 offset:39424\n\t" \
  "v_mfma_f32_16x16x32_bf16 v[200:203], v[108:111], v[80:83], v[200:203]\n\t" \
  "ds_read_b64_tr_b16 v[128:129], v58 offset:18432\n\t" \
  "v_mfma_f32_16x16x32_bf16 v[200:203], v[136:139], v[84:87], v[200:203]\n\t" \
  "ds_read_b64_tr_b16 v[130:131], v58 offset:18944\n\t" \
  "v_mfma_f32_16x16x32_bf16 v[208:211], v[136:139], v[88:91], v[208:211]\n\t" \
  "ds_read_b64_tr_b16 v[144:145], v58 offset:22528\n\t" \
  "v_mfma_f32_16x16x32_bf16 v[208:211], v[108:111], v[92:95], v[208:211]\n\t" \
  "ds_read_b64_tr_b16 v[146:147], v58 offset:23040\n\t" \
  "v_mfma_f32_16x16x32_bf16 v[188:191], v[148:151], v[72:75], v[188:191]\n\t" \
  "ds_read_b64_tr_b16 v[100:101], v59 offset:18432\n\t" \
  "s_waitcnt lgkmcnt(7)\n\t" \
  "v_mfma_f32_16x16x32_bf16 v[188:191], v[96:99], v[140:143], v[188:191]\n\t" \
  "ds_read_b64_tr_b16 v[102:103], v59 offset:18944\n\t" \
  "v_mfma_f32_16x16x32_bf16 v[196:199], v[96:99], v[68:71], v[196:199]\n\t" \
  "ds_read_b64_tr_b16 v[132:133], v59 offset:22528\n\t" \
  "v_mfma_f32_16x16x32_bf16 v[196:199], v[148:151], v[76:79], v[196:199]\n\t" \
  "ds_read_b64_tr_b16 v[134:135], v59 offset:23040\n\t" \
  "v_mfma_f32_16x16x32_bf16 v[204:207], v[148:151], v[80:83], v[204:207]\n\t" \
  "v_mfma_f32_16x16x32_bf16 v[204:207], v[96:99], v[84:87], v[204:207]\n\t" \
  "v_mfma_f32_16x16x32_bf16 v[212:215], v[96:99], v[88:91], v[212:215]\n\t" \
  "v_mfma_f32_16x16x32_bf16 v[212:215], v[148:151], v[92:95], v[212:215]\n\t" \
  "v_mfma_f32_16x16x32_bf16 v[216:219], v[72:75], v[64:67], v[216:219]\n\t" \
  "v_mfma_f32_16x16x32_bf16 v[216:219], v[140:143], v[104:107], v[216:219]\n\t" \
  "v_mfma_f32_16x16x32_bf16 v[224:227], v[68:71], v[104:107], v[224:227]\n\t" \
  "v_mfma_f32_16x16x32_bf16 v[224:227], v[76:79], v[64:67], v[224:227]\n\t" \
  "v_mfma_f32_16x16x32_bf16 v[232:235], v[80:83], v[64:67], v[232:235]\n\t" \
  "v_mfma_f32_16x16x32_bf16 v[232:235], v[84:87], v[104:107], v[232:235]\n\t" \
  "v_mfma_f32_16x16x32_bf16 v[240:243], v[88:91], v[104:107], v[240:243]\n\t" \
  "v_mfma_f32_16x16x32_bf16 v[240:243], v[92:95], v[64:67], v[240:243]\n\t" \
  "v_mfma_f32_16x16x32_bf16 v[248:251], v[112:115], v[64:67], v[248:251]\n\t" \
  "v_mfma_f32_16x16x32_bf16 v[248:251], v[116:119], v[104:107], v[248:251]\n\t" \
  "v_mfma_f32_16x16x32_bf16 v[252:255], v[112:115], v[120:123], v[252:255]\n\t" \
  "s_waitcnt lgkmcnt(0)\n\t" \
  "v_mfma_f32_16x16x32_bf16 v[252:255], v[116:119], v[124:127], v[252:255]\n\t" \
  "v_mfma_f32_16x16x32_bf16 v[220:223], v[140:143], v[124:127], v[220:223]\n\t" \
  "v_mfma_f32_16x16x32_bf16 v[220:223], v[72:75], v[120:123], v[220:223]\n\t" \
  "s_barrier\n\t" \
  "v_mfma_f32_16x16x32_bf16 v[228:231], v[76:79], v[120:123], v[228:231]\n\t" \
  "ds_read_b64_tr_b16 v[136:137], v62 offset:26624\n\t" \
  "v_bfe_u32 v30, v4, 9, 1\n\t" \
  "v_mfma_f32_16x16x32_bf16 v[228:231], v[68:71], v[124:127], v[228:231]\n\t" \
  "ds_read_b64_tr_b16 v[138:139], v62 offset:27136\n\t" \
  "v_mad_i32_i24 v30, v30, -16, v0\n\t" \
  "v_mfma_f32_16x16x32_bf16 v[236:239], v[84:87], v[124:127], v[236:239]\n\t" \
  "ds_read_b128 v[108:111], v60 offset:10240\n\t" \
  "ds_read_b128 v[26:29], v0 offset:1552\n\t" \
  "v_mfma_f32_16x16x32_bf16 v[236:239], v[80:83], v[120:123], v[236:239]\n\t" \
  "ds_read_b64_tr_b16 v[96:97], v62 offset:30720\n\t" \
  "ds_read_b128 v[30:33], v30 offset:1040\n\t" \
  "v_mfma_f32_16x16x32_bf16 v[244:247], v[92:95], v[120:123], v[244:247]\n\t" \
  "ds_read_b64_tr_b16 v[98:99], v62 offset:31232\n\t" \
  "ds_read_b128 v[34:37], v0 offset:512\n\t" \
  "v_mfma_f32_16x16x32_bf16 v[244:247], v[88:91], v[124:127], v[244:247]\n\t" \
  "ds_read_b128 v[148:151], v61 offset:10240\n\t" \
  "ds_read_b64_tr_b16 v[38:39], v1 offset:512\n\t" \
  "v_mfma_f32_16x16x32_bf16 v[152:155], v[128:131], v[72:75], v[152:155]\n\t" \
  "ds_read_b128 v[64:67], v61 offset:12288\n\t" \
  "ds_read_b64_tr_b16 v[40:41], v1 offset:0\n\t" \
  "v_mfma_f32_16x16x32_bf16 v[152:155], v[144:147], v[140:143], v[152:155]\n\t" \
  "ds_read_b128 v[104:107], v60 offset:12288\n\t" \
  "s_waitcnt vmcnt(0)\n\t" \
  "v_mfma_f32_16x16x32_bf16 v[160:163], v[144:147], v[68:71], v[160:163]\n\t" \
  "ds_read_b128 v[112:115], v60 offset:14336\n\t" \
  "v_perm_b32 v18, v10, v10, v14\n\t" \
  "v_mfma_f32_16x16x32_bf16 v[160:163], v[128:131], v[76:79], v[160:163]\n\t" \
  "ds_read_b128 v[116:119], v61 offset:14336\n\t" \
  "v_perm_b32 v19, v10, v10, v15\n\t" \
  "v_mfma_f32_16x16x32_bf16 v[168:171], v[128:131], v[80:83], v[168:171]\n\t" \
  "ds_read_b128 v[120:123], v61 offset:16384\n\t" \
  "v_perm_b32 v20, v10, v10, v16\n\t" \
  "v_mfma_f32_16x16x32_bf16 v[168:171], v[144:147], v[84:87], v[168:171]\n\t" \
  "ds_read_b128 v[124:127], v60 offset:16384\n\t" \
  "v_perm_b32 v21, v10, v10, v17\n\t" \
  "v_mfma_f32_16x16x32_bf16 v[176:179], v[144:147], v[88:91], v[176:179]\n\t" \
  "ds_read_b64_tr_b16 v[144:145], v63 offset:26624\n\t" \
  "v_perm_b32 v22, v11, v11, v14\n\t" \
  "v_mfma_f32_16x16x32_bf16 v[176:179], v[128:131], v[92:95], v[176:179]\n\t" \
  "ds_read_b64_tr_b16 v[146:147], v63 offset:27136\n\t" \
  "v_perm_b32 v23, v11, v11, v15\n\t" \
  "v_mfma_f32_16x16x32_bf16 v[156:159], v[100:103], v[72:75], v[156:159]\n\t" \
  "ds_read_b64_tr_b16 v[128:129], v63 offset:30720\n\t" \
  "v_perm_b32 v24, v11, v11, v16\n\t" \
  "v_mfma_f32_16x16x32_bf16 v[156:159], v[132:135], v[140:143], v[156:159]\n\t" \
  "ds_read_b64_tr_b16 v[130:131], v63 offset:31232\n\t" \
  "v_perm_b32 v25, v11, v11, v17\n\t" \
  "v_mfma_f32_16x16x32_bf16 v[164:167], v[132:135], v[68:71], v[164:167]\n\t" \
  "ds_read_b64_tr_b16 v[72:73], v62 offset:34816\n\t" \
  "v_mfma_f32_16x16x32_bf16 v[164:167], v[100:103], v[76:79], v[164:167]\n\t" \
  "ds_read_b64_tr_b16 v[74:75], v62 offset:35328\n\t" \
  "v_mfma_f32_16x16x32_bf16 v[172:175], v[100:103], v[80:83], v[172:175]\n\t" \
  "ds_read_b64_tr_b16 v[140:141], v62 offset:38912\n\t" \
  "s_waitcnt lgkmcnt(6)\n\t" \
  "v_mfma_f32_16x16x32_bf16 v[42:45], v[26:29], v[18:21], 0\n\t" \
  "v_mfma_f32_16x16x32_bf16 v[42:45], v[30:33], v[22:25], v[42:45]\n\t" \
  "v_mfma_f32_16x16x32_bf16 v[46:49], v[30:33], v[18:21], 0\n\t" \
  "v_mfma_f32_16x16x32_bf16 v[46:49], v[34:37], v[22:25], v[46:49]\n\t" \
  "ds_read_b128 v[26:29], v0 offset:1808\n\t" \
  "ds_read_b128 v[30:33], v0 offset:1296\n\t" \
  "v_mfma_f32_16x16x32_bf16 v[172:175], v[132:135], v[84:87], v[172:175]\n\t" \
  "ds_read_b64_tr_b16 v[142:143], v62 offset:39424\n\t" \
  "ds_read_b128 v[34:37], v0 offset:768\n\t" \
  "v_mfma_f32_16x16x32_bf16 v[180:183], v[132:135], v[88:91], v[180:183]\n\t" \
  "ds_read_b128 v[68:71], v60 offset:0\n\t" \
  "v_mfma_f32_16x16x32_bf16 v[180:183], v[100:103], v[92:95], v[180:183]\n\t" \
  "ds_read_b128 v[76:79], v61 offset:0\n\t" \
  "v_mfma_f32_16x16x32_bf16 v[184:187], v[136:139], v[108:111], v[184:187]\n\t" \
  "ds_read_b64_tr_b16 v[80:81], v63 offset:34816\n\t" \
  "v_lshlrev_b32 v50, 16, v38\n\t" \
  "v_mfma_f32_16x16x32_bf16 v[184:187], v[96:99], v[148:151], v[184:187]\n\t" \
  "ds_read_b64_tr_b16 v[82:83], v63 offset:35328\n\t" \
  "v_lshlrev_b32 v51, 16, v40\n\t" \
  "v_mfma_f32_16x16x32_bf16 v[192:195], v[96:99], v[64:67], v[192:195]\n\t" \
  "ds_read_b64_tr_b16 v[84:85], v63 offset:38912\n\t" \
  "v_mul_f32 v42, v42, v50\n\t" \
  "v_mfma_f32_16x16x32_bf16 v[192:195], v[136:139], v[104:107], v[192:195]\n\t" \
  "ds_read_b64_tr_b16 v[86:87], v63 offset:39424\n\t" \
  "v_mul_f32 v46, v46, v51\n\t" \
  "v_mfma_f32_16x16x32_bf16 v[200:203], v[136:139], v[112:115], v[200:203]\n\t" \
  "ds_read_b64_tr_b16 v[88:89], v62 offset:18432\n\t" \
  "v_cvt_pk_bf16_f32 v42, v42, v46\n\t" \
  "v_mfma_f32_16x16x32_bf16 v[200:203], v[96:99], v[116:119], v[200:203]\n\t" \
  "ds_read_b64_tr_b16 v[90:91], v62 offset:18944\n\t" \
  "v_and_b32 v50, 0xffff0000, v38\n\t" \
  "v_mfma_f32_16x16x32_bf16 v[208:211], v[96:99], v[120:123], v[208:211]\n\t" \
  "ds_read_b64_tr_b16 v[132:133], v62 offset:22528\n\t" \
  "v_and_b32 v51, 0xffff0000, v40\n\t" \
  "v_mfma_f32_16x16x32_bf16 v[208:211], v[136:139], v[124:127], v[208:211]\n\t" \
  "ds_read_b64_tr_b16 v[134:135], v62 offset:23040\n\t" \
  "v_mul_f32 v43, v43, v50\n\t" \
  "s_waitcnt lgkmcnt(7)\n\t" \
  "v_mfma_f32_16x16x32_bf16 v[188:191], v[144:147], v[108:111], v[188:191]\n\t" \
  "ds_read_b64_tr_b16 v[92:93], v63 offset:18432\n\t" \
  "v_mul_f32 v47, v47, v51\n\t" \
  "v_mfma_f32_16x16x32_bf16 v[188:191], v[128:131], v[148:151], v[188:191]\n\t" \
  "ds_read_b64_tr_b16 v[94:95], v63 offset:18944\n\t" \
  "v_cvt_pk_bf16_f32 v43, v43, v47\n\t" \
  "v_mfma_f32_16x16x32_bf16 v[196:199], v[128:131], v[64:67], v[196:199]\n\t" \
  "ds_read_b64_tr_b16 v[100:101], v63 offset:22528\n\t" \
  "v_lshlrev_b32 v50, 16, v39\n\t" \
  "v_mfma_f32_16x16x32_bf16 v[196:199], v[144:147], v[104:107], v[196:199]\n\t" \
  "ds_read_b64_tr_b16 v[102:103], v63 offset:23040\n\t" \
  "v_lshlrev_b32 v51, 16, v41\n\t" \
  "v_mfma_f32_16x16x32_bf16 v[204:207], v[144:147], v[112:115], v[204:207]\n\t" \
  "v_mul_f32 v44, v44, v50\n\t" \
  "v_mul_f32 v48, v48, v51\n\t" \
  "v_mfma_f32_16x16x32_bf16 v[204:207], v[128:131], v[116:119], v[204:207]\n\t" \
  "v_cvt_pk_bf16_f32 v44, v44, v48\n\t" \
  "v_and_b32 v50, 0xffff0000, v39\n\t" \
  "v_mfma_f32_16x16x32_bf16 v[212:215], v[128:131], v[120:123], v[212:215]\n\t" \
  "v_and_b32 v51, 0xffff0000, v41\n\t" \
  "v_mul_f32 v45, v45, v50\n\t" \
  "v_mfma_f32_16x16x32_bf16 v[212:215], v[144:147], v[124:127], v[212:215]\n\t" \
  "v_mul_f32 v49, v49, v51\n\t" \
  "ds_read_b64_tr_b16 v[38:39], v1 offset:768\n\t" \
  "v_mfma_f32_16x16x32_bf16 v[216:219], v[108:111], v[72:75], v[216:219]\n\t" \
  "ds_read_b64_tr_b16 v[40:41], v1 offset:256\n\t" \
  "v_cvt_pk_bf16_f32 v45, v45, v49\n\t" \
  "v_mfma_f32_16x16x32_bf16 v[216:219], v[148:151], v[140:143], v[216:219]\n\t" \
  "v_mfma_f32_16x16x32_bf16 v[224:227], v[64:67], v[140:143], v[224:227]\n\t" \
  "v_mfma_f32_16x16x32_bf16 v[224:227], v[104:107], v[72:75], v[224:227]\n\t" \
  "v_mfma_f32_16x16x32_bf16 v[232:235], v[112:115], v[72:75], v[232:235]\n\t" \
  "v_mfma_f32_16x16x32_bf16 v[232:235], v[116:119], v[140:143], v[232:235]\n\t" \
  "v_mfma_f32_16x16x32_bf16 v[240:243], v[120:123], v[140:143], v[240:243]\n\t" \
  "v_mfma_f32_16x16x32_bf16 v[240:243], v[124:127], v[72:75], v[240:243]\n\t" \
  "v_mfma_f32_16x16x32_bf16 v[248:251], v[68:71], v[72:75], v[248:251]\n\t" \
  "v_mfma_f32_16x16x32_bf16 v[248:251], v[76:79], v[140:143], v[248:251]\n\t" \
  "s_waitcnt lgkmcnt(0)\n\t" \
  "v_mfma_f32_16x16x32_bf16 v[252:255], v[68:71], v[80:83], v[252:255]\n\t" \
  "global_store_dwordx4 v2, v[42:45], %24 offset:0 sc1\n\t" \
  "v_mfma_f32_16x16x32_bf16 v[252:255], v[76:79], v[84:87], v[252:255]\n\t" \
  "s_nop 0\n\t" \
  "v_mfma_f32_16x16x32_bf16 v[42:45], v[26:29], v[18:21], 0\n\t" \
  "v_mfma_f32_16x16x32_bf16 v[42:45], v[30:33], v[22:25], v[42:45]\n\t" \
  "v_mfma_f32_16x16x32_bf16 v[46:49], v[30:33], v[18:21], 0\n\t" \
  "v_mfma_f32_16x16x32_bf16 v[46:49], v[34:37], v[22:25], v[46:49]\n\t" \
  "v_bfe_u32 v30, v4, 9, 1\n\t" \
  "v_mad_i32_i24 v30, v30, -16, v0\n\t" \
  "v_mfma_f32_16x16x32_bf16 v[220:223], v[148:151], v[84:87], v[220:223]\n\t" \
  "ds_read_b128 v[26:29], v0 offset:1584\n\t" \
  "ds_read_b128 v[30:33], v30 offset:1072\n\t" \
  "v_mfma_f32_16x16x32_bf16 v[220:223], v[108:111], v[80:83], v[220:223]\n\t" \
  "ds_read_b128 v[34:37], v0 offset:544\n\t" \
  "v_perm_b32 v18, v12, v12, v14\n\t" \
  "s_waitcnt lgkmcnt(0)\n\t" \
  "s_barrier\n\t" \
  "v_mfma_f32_16x16x32_bf16 v[228:231], v[104:107], v[80:83], v[228:231]\n\t" \
  "ds_read_b64_tr_b16 v[96:97], v54 offset:26624\n\t" \
  "v_perm_b32 v19, v12, v12, v15\n\t" \
  "v_mfma_f32_16x16x32_bf16 v[228:231], v[64:67], v[84:87], v[228:231]\n\t" \
  "ds_read_b64_tr_b16 v[98:99], v54 offset:27136\n\t" \
  "v_perm_b32 v20, v12, v12, v16\n\t" \
  "v_mfma_f32_16x16x32_bf16 v[236:239], v[116:119], v[84:87], v[236:239]\n\t" \
  "ds_read_b128 v[136:139], v52 offset:10240\n\t" \
  "v_perm_b32 v21, v12, v12, v17\n\t" \
  "v_mfma_f32_16x16x32_bf16 v[236:239], v[112:115], v[80:83], v[236:239]\n\t" \
  "ds_read_b64_tr_b16 v[128:129], v54 offset:30720\n\t" \
  "v_perm_b32 v22, v13, v13, v14\n\t" \
  "v_mfma_f32_16x16x32_bf16 v[244:247], v[124:127], v[80:83], v[244:247]\n\t" \
  "ds_read_b64_tr_b16 v[130:131], v54 offset:31232\n\t" \
  "v_perm_b32 v23, v13, v13, v15\n\t" \
  "v_mfma_f32_16x16x32_bf16 v[244:247], v[120:123], v[84:87], v[244:247]\n\t" \
  "ds_read_b128 v[144:147], v53 offset:10240\n\t" \
  "v_perm_b32 v24, v13, v13, v16\n\t" \
  "v_mfma_f32_16x16x32_bf16 v[152:155], v[88:91], v[108:111], v[152:155]\n\t" \
  "ds_read_b128 v[72:75], v53 offset:12288\n\t" \
  "v_perm_b32 v25, v13, v13, v17\n\t" \
  "v_mfma_f32_16x16x32_bf16 v[152:155], v[132:135], v[148:151], v[152:155]\n\t" \
  "ds_read_b128 v[140:143], v52 offset:12288\n\t" \
  "v_lshlrev_b32 v50, 16, v38\n\t" \
  "v_mfma_f32_16x16x32_bf16 v[160:163], v[132:135], v[64:67], v[160:163]\n\t" \
  "ds_read_b128 v[68:71], v52 offset:14336\n\t" \
  "v_lshlrev_b32 v51, 16, v40\n\t" \
  "v_mfma_f32_16x16x32_bf16 v[160:163], v[88:91], v[104:107], v[160:163]\n\t" \
  "ds_read_b128 v[76:79], v53 offset:14336\n\t" \
  "v_mul_f32 v42, v42, v50\n\t" \
  "v_mfma_f32_16x16x32_bf16 v[168:171], v[88:91], v[112:115], v[168:171]\n\t" \
  "ds_read_b128 v[80:83], v53 offset:16384\n\t" \
  "v_mul_f32 v46, v46, v51\n\t" \
  "v_mfma_f32_16x16x32_bf16 v[168:171], v[132:135], v[116:119], v[168:171]\n\t" \
  "ds_read_b128 v[84:87], v52 offset:16384\n\t" \
  "v_cvt_pk_bf16_f32 v42, v42, v46\n\t" \
  "v_mfma_f32_16x16x32_bf16 v[176:179], v[132:135], v[120:123], v[176:179]\n\t" \
  "ds_read_b64_tr_b16 v[132:133], v55 offset:26624\n\t" \
  "v_and_b32 v50, 0xffff0000, v38\n\t" \
  "v_mfma_f32_16x16x32_bf16 v[176:179], v[88:91], v[124:127], v[176:179]\n\t" \
  "ds_read_b64_tr_b16 v[134:135], v55 offset:27136\n\t" \
  "v_and_b32 v51, 0xffff0000, v40\n\t" \
  "v_mfma_f32_16x16x32_bf16 v[156:159], v[92:95], v[108:111], v[156:159]\n\t" \
  "ds_read_b64_tr_b16 v[88:89], v55 offset:30720\n\t" \
  "v_mul_f32 v43, v43, v50\n\t" \
  "v_mfma_f32_16x16x32_bf16 v[156:159], v[100:103], v[148:151], v[156:159]\n\t" \
  "ds_read_b64_tr_b16 v[90:91], v55 offset:31232\n\t" \
  "v_mul_f32 v47, v47, v51\n\t" \
  "v_mfma_f32_16x16x32_bf16 v[164:167], v[100:103], v[64:67], v[164:167]\n\t" \
  "ds_read_b64_tr_b16 v[108:109], v54 offset:34816\n\t" \
  "v_cvt_pk_bf16_f32 v43, v43, v47\n\t" \
  "v_mfma_f32_16x16x32_bf16 v[164:167], v[92:95], v[104:107], v[164:167]\n\t" \
  "ds_read_b64_tr_b16 v[110:111], v54 offset:35328\n\t" \
  "v_lshlrev_b32 v50, 16, v39\n\t" \
  "v_mfma_f32_16x16x32_bf16 v[172:175], v[92:95], v[112:115], v[172:175]\n\t" \
  "ds_read_b64_tr_b16 v[148:149], v54 offset:38912\n\t" \
  "v_lshlrev_b32 v51, 16, v41\n\t" \
  "v_mfma_f32_16x16x32_bf16 v[172:175], v[100:103], v[116:119], v[172:175]\n\t" \
  "ds_read_b64_tr_b16 v[150:151], v54 offset:39424\n\t" \
  "v_mul_f32 v44, v44, v50\n\t" \
  "v_mfma_f32_16x16x32_bf16 v[180:183], v[100:103], v[120:123], v[180:183]\n\t" \
  "ds_read_b128 v[64:67], v52 offset:0\n\t" \
  "v_mul_f32 v48, v48, v51\n\t" \
  "v_mfma_f32_16x16x32_bf16 v[180:183], v[92:95], v[124:127], v[180:183]\n\t" \
  "ds_read_b128 v[104:107], v53 offset:0\n\t" \
  "v_cvt_pk_bf16_f32 v44, v44, v48\n\t" \
  "s_waitcnt lgkmcnt(7)\n\t" \
  "v_mfma_f32_16x16x32_bf16 v[184:187], v[96:99], v[136:139], v[184:187]\n\t" \
  "ds_read_b64_tr_b16 v[112:113], v55 offset:34816\n\t" \
  "v_and_b32 v50, 0xffff0000, v39\n\t" \
  "v_mfma_f32_16x16x32_bf16 v[184:187], v[128:131], v[144:147], v[184:187]\n\t" \
  "ds_read_b64_tr_b16 v[114:115], v55 offset:35328\n\t" \
  "v_and_b32 v51, 0xffff0000, v41\n\t" \
  "v_mfma_f32_16x16x32_bf16 v[192:195], v[128:131], v[72:75], v[192:195]\n\t" \
  "ds_read_b64_tr_b16 v[116:117], v55 offset:38912\n\t" \
  "v_mul_f32 v45, v45, v50\n\t" \
  "v_mfma_f32_16x16x32_bf16 v[192:195], v[96:99], v[140:143], v[192:195]\n\t" \
  "ds_read_b64_tr_b16 v[118:119], v55 offset:39424\n\t" \
  "v_mul_f32 v49, v49, v51\n\t" \
  "v_mfma_f32_16x16x32_bf16 v[200:203], v[96:99], v[68:71], v[200:203]\n\t" \
  "ds_read_b64_tr_b16 v[100:101], v54 offset:18432\n\t" \
  "ds_read_b64_tr_b16 v[38:39], v1 offset:544\n\t" \
  "v_mfma_f32_16x16x32_bf16 v[200:203], v[128:131], v[76:79], v[200:203]\n\t" \
  "ds_read_b64_tr_b16 v[102:103], v54 offset:18944\n\t" \
  "ds_read_b64_tr_b16 v[40:41], v1 offset:32\n\t" \
  "v_mfma_f32_16x16x32_bf16 v[208:211], v[128:131], v[80:83], v[208:211]\n\t" \
  "ds_read_b64_tr_b16 v[120:121], v54 offset:22528\n\t" \
  "v_cvt_pk_bf16_f32 v45, v45, v49\n\t" \
  "v_mfma_f32_16x16x32_bf16 v[208:211], v[96:99], v[84:87], v[208:211]\n\t" \
  "ds_read_b64_tr_b16 v[122:123], v54 offset:23040\n\t" \
  "v_mfma_f32_16x16x32_bf16 v[188:191], v[132:135], v[136:139], v[188:191]\n\t" \
  "ds_read_b64_tr_b16 v[92:93], v55 offset:18432\n\t" \
  "s_waitcnt lgkmcnt(9)\n\t" \
  "v_mfma_f32_16x16x32_bf16 v[188:191], v[88:91], v[144:147], v[188:191]\n\t" \
  "ds_read_b64_tr_b16 v[94:95], v55 offset:18944\n\t" \
  "v_mfma_f32_16x16x32_bf16 v[196:199], v[88:91], v[72:75], v[196:199]\n\t" \
  "ds_read_b64_tr_b16 v[124:125], v55 offset:22528\n\t" \
  "v_mfma_f32_16x16x32_bf16 v[196:199], v[132:135], v[140:143], v[196:199]\n\t" \
  "ds_read_b64_tr_b16 v[126:127], v55 offset:23040\n\t" \
  "v_mfma_f32_16x16x32_bf16 v[204:207], v[132:135], v[68:71], v[204:207]\n\t" \
  "v_mfma_f32_16x16x32_bf16 v[204:207], v[88:91], v[76:79], v[204:207]\n\t" \
  "v_mfma_f32_16x16x32_bf16 v[212:215], v[88:91], v[80:83], v[212:215]\n\t" \
  "v_mfma_f32_16x16x32_bf16 v[212:215], v[132:135], v[84:87], v[212:215]\n\t" \
  "v_mfma_f32_16x16x32_bf16 v[216:219], v[136:139], v[108:111], v[216:219]\n\t" \
  "v_mfma_f32_16x16x32_bf16 v[216:219], v[144:147], v[148:151], v[216:219]\n\t" \
  "v_mfma_f32_16x16x32_bf16 v[224:227], v[72:75], v[148:151], v[224:227]\n\t" \
  "v_mfma_f32_16x16x32_bf16 v[224:227], v[140:143], v[108:111], v[224:227]\n\t" \
  "v_mfma_f32_16x16x32_bf16 v[232:235], v[68:71], v[108:111], v[232:235]\n\t" \
  "v_mfma_f32_16x16x32_bf16 v[232:235], v[76:79], v[148:151], v[232:235]\n\t" \
  "v_mfma_f32_16x16x32_bf16 v[240:243], v[80:83], v[148:151], v[240:243]\n\t" \
  "v_mfma_f32_16x16x32_bf16 v[240:243], v[84:87], v[108:111], v[240:243]\n\t" \
  "v_mfma_f32_16x16x32_bf16 v[248:251], v[64:67], v[108:111], v[248:251]\n\t" \
  "v_mfma_f32_16x16x32_bf16 v[248:251], v[104:107], v[148:151], v[248:251]\n\t" \
  "v_mfma_f32_16x16x32_bf16 v[252:255], v[64:67], v[112:115], v[252:255]\n\t" \
  "global_store_dwordx4 v2, v[42:45], %24 offset:1024 sc1\n\t" \
  "v_add_u32 v2, 0x10000, v2\n\t" \
  "s_waitcnt lgkmcnt(0)\n\t" \
  "v_mfma_f32_16x16x32_bf16 v[252:255], v[104:107], v[116:119], v[252:255]\n\t" \
  "v_mfma_f32_16x16x32_bf16 v[220:223], v[144:147], v[116:119], v[220:223]\n\t" \
  "v_mfma_f32_16x16x32_bf16 v[42:45], v[26:29], v[18:21], 0\n\t" \
  "v_mfma_f32_16x16x32_bf16 v[42:45], v[30:33], v[22:25], v[42:45]\n\t" \
  "v_mfma_f32_16x16x32_bf16 v[46:49], v[30:33], v[18:21], 0\n\t" \
  "v_mfma_f32_16x16x32_bf16 v[46:49], v[34:37], v[22:25], v[46:49]\n\t" \
  "500:\n\t" \
  "s_cbranch_execz 504f\n\t" \
  "ds_read_b128 v[26:29], v0 offset:1840\n\t" \
  "ds_read_b128 v[30:33], v0 offset:1328\n\t" \
  "v_mfma_f32_16x16x32_bf16 v[220:223], v[136:139], v[112:115], v[220:223]\n\t" \
  "ds_read_b128 v[34:37], v0 offset:800\n\t" \
  "s_waitcnt lgkmcnt(0)\n\t" \
  "s_barrier\n\t" \
  "v_mfma_f32_16x16x32_bf16 v[228:231], v[140:143], v[112:115], v[228:231]\n\t" \
  "ds_read_b64_tr_b16 v[128:129], v58 offset:26624\n\t" \
  "v_mfma_f32_16x16x32_bf16 v[228:231], v[72:75], v[116:119], v[228:231]\n\t" \
  "ds_read_b64_tr_b16 v[130:131], v58 offset:27136\n\t" \
  "v_lshlrev_b32 v50, 16, v38\n\t" \
  "v_mfma_f32_16x16x32_bf16 v[236:239], v[76:79], v[116:119], v[236:239]\n\t" \
  "ds_read_b128 v[96:99], v56 offset:10240\n\t" \
  "v_lshlrev_b32 v51, 16, v40\n\t" \
  "v_mfma_f32_16x16x32_bf16 v[236:239], v[68:71], v[112:115], v[236:239]\n\t" \
  "ds_read_b64_tr_b16 v[88:89], v58 offset:30720\n\t" \
  "v_mul_f32 v42, v42, v50\n\t" \
  "v_mfma_f32_16x16x32_bf16 v[244:247], v[84:87], v[112:115], v[244:247]\n\t" \
  "ds_read_b64_tr_b16 v[90:91], v58 offset:31232\n\t" \
  "v_mul_f32 v46, v46, v51\n\t" \
  "v_mfma_f32_16x16x32_bf16 v[244:247], v[80:83], v[116:119], v[244:247]\n\t" \
  "ds_read_b128 v[132:135], v57 offset:10240\n\t" \
  "v_cvt_pk_bf16_f32 v42, v42, v46\n\t" \
  "v_mfma_f32_16x16x32_bf16 v[152:155], v[100:103], v[136:139], v[152:155]\n\t" \
  "ds_read_b128 v[108:111], v57 offset:12288\n\t" \
  "v_and_b32 v50, 0xffff0000, v38\n\t" \
  "v_mfma_f32_16x16x32_bf16 v[152:155], v[120:123], v[144:147], v[152:155]\n\t" \
  "ds_read_b128 v[148:151], v56 offset:12288\n\t" \
  "v_and_b32 v51, 0xffff0000, v40\n\t" \
  "v_mfma_f32_16x16x32_bf16 v[160:163], v[120:123], v[72:75], v[160:163]\n\t" \
  "ds_read_b128 v[64:67], v56 offset:14336\n\t" \
  "v_mul_f32 v43, v43, v50\n\t" \
  "v_mfma_f32_16x16x32_bf16 v[160:163], v[100:103], v[140:143], v[160:163]\n\t" \
  "ds_read_b128 v[104:107], v57 offset:14336\n\t" \
  "v_mul_f32 v47, v47, v51\n\t" \
  "v_mfma_f32_16x16x32_bf16 v[168:171], v[100:103], v[68:71], v[168:171]\n\t" \
  "ds_read_b128 v[112:115], v57 offset:16384\n\t" \
  "v_cvt_pk_bf16_f32 v43, v43, v47\n\t" \
  "v_mfma_f32_16x16x32_bf16 v[168:171], v[120:123], v[76:79], v[168:171]\n\t" \
  "ds_read_b128 v[116:119], v56 offset:16384\n\t" \
  "v_lshlrev_b32 v50, 16, v39\n\t" \
  "v_mfma_f32_16x16x32_bf16 v[176:179], v[120:123], v[80:83], v[176:179]\n\t" \
  "ds_read_b64_tr_b16 v[120:121], v59 offset:26624\n\t" \
  "v_lshlrev_b32 v51, 16, v41\n\t" \
  "v_mfma_f32_16x16x32_bf16 v[176:179], v[100:103], v[84:87], v[176:179]\n\t" \
  "ds_read_b64_tr_b16 v[122:123], v59 offset:27136\n\t" \
  "v_mul_f32 v44, v44, v50\n\t" \
  "v_mfma_f32_16x16x32_bf16 v[156:159], v[92:95], v[136:139], v[156:159]\n\t" \
  "ds_read_b64_tr_b16 v[100:101], v59 offset:30720\n\t" \
  "v_mul_f32 v48, v48, v51\n\t" \
  "v_mfma_f32_16x16x32_bf16 v[156:159], v[124:127], v[144:147], v[156:159]\n\t" \
  "ds_read_b64_tr_b16 v[102:103], v59 offset:31232\n\t" \
  "v_cvt_pk_bf16_f32 v44, v44, v48\n\t" \
  "v_mfma_f32_16x16x32_bf16 v[164:167], v[124:127], v[72:75], v[164:167]\n\t" \
  "ds_read_b64_tr_b16 v[136:137], v58 offset:34816\n\t" \
  "v_and_b32 v50, 0xffff0000, v39\n\t" \
  "v_mfma_f32_16x16x32_bf16 v[164:167], v[92:95], v[140:143], v[164:167]\n\t" \
  "ds_read_b64_tr_b16 v[138:139], v58 offset:35328\n\t" \
  "v_and_b32 v51, 0xffff0000, v41\n\t" \
  "v_mfma_f32_16x16x32_bf16 v[172:175], v[92:95], v[68:71], v[172:175]\n\t" \
  "ds_read_b64_tr_b16 v[144:145], v58 offset:38912\n\t" \
  "v_mul_f32 v45, v45, v50\n\t" \
  "v_mfma_f32_16x16x32_bf16 v[172:175], v[124:127], v[76:79], v[172:175]\n\t" \
  "ds_read_b64_tr_b16 v[146:147], v58 offset:39424\n\t" \
  "v_mul_f32 v49, v49, v51\n\t" \
  "v_mfma_f32_16x16x32_bf16 v[180:183], v[124:127], v[80:83], v[180:183]\n\t" \
  "ds_read_b128 v[72:75], v56 offset:0\n\t" \
  "ds_read_b64_tr_b16 v[38:39], v1 offset:800\n\t" \
  "v_mfma_f32_16x16x32_bf16 v[180:183], v[92:95], v[84:87], v[180:183]\n\t" \
  "ds_read_b128 v[140:143], v57 offset:0\n\t" \
  "ds_read_b64_tr_b16 v[40:41], v1 offset:288\n\t" \
  "s_waitcnt lgkmcnt(9)\n\t" \
  "v_mfma_f32_16x16x32_bf16 v[184:187], v[128:131], v[96:99], v[184:187]\n\t" \
  "ds_read_b64_tr_b16 v[68:69], v59 offset:34816\n\t" \
  "v_cvt_pk_bf16_f32 v45, v45, v49\n\t" \
  "v_mfma_f32_16x16x32_bf16 v[184:187], v[88:91], v[132:135], v[184:187]\n\t" \
  "ds_read_b64_tr_b16 v[70:71], v59 offset:35328\n\t" \
  "v_mfma_f32_16x16x32_bf16 v[192:195], v[88:91], v[108:111], v[192:195]\n\t" \
  "ds_read_b64_tr_b16 v[76:77], v59 offset:38912\n\t" \
  "v_mfma_f32_16x16x32_bf16 v[192:195], v[128:131], v[148:151], v[192:195]\n\t" \
  "ds_read_b64_tr_b16 v[78:79], v59 offset:39424\n\t" \
  "v_mfma_f32_16x16x32_bf16 v[200:203], v[128:131], v[64:67], v[200:203]\n\t" \
  "ds_read_b64_tr_b16 v[80:81], v58 offset:18432\n\t" \
  "v_mfma_f32_16x16x32_bf16 v[200:203], v[88:91], v[104:107], v[200:203]\n\t" \
  "ds_read_b64_tr_b16 v[82:83], v58 offset:18944\n\t" \
  "v_mfma_f32_16x16x32_bf16 v[208:211], v[88:91], v[112:115], v[208:211]\n\t" \
  "ds_read_b64_tr_b16 v[124:125], v58 offset:22528\n\t" \
  "v_mfma_f32_16x16x32_bf16 v[208:211], v[128:131], v[116:119], v[208:211]\n\t" \
  "ds_read_b64_tr_b16 v[126:127], v58 offset:23040\n\t" \
  "v_mfma_f32_16x16x32_bf16 v[188:191], v[120:123], v[96:99], v[188:191]\n\t" \
  "ds_read_b64_tr_b16 v[84:85], v59 offset:18432\n\t" \
  "s_waitcnt lgkmcnt(7)\n\t" \
  "v_mfma_f32_16x16x32_bf16 v[188:191], v[100:103], v[132:135], v[188:191]\n\t" \
  "ds_read_b64_tr_b16 v[86:87], v59 offset:18944\n\t" \
  "v_mfma_f32_16x16x32_bf16 v[196:199], v[100:103], v[108:111], v[196:199]\n\t" \
  "ds_read_b64_tr_b16 v[92:93], v59 offset:22528\n\t" \
  "v_mfma_f32_16x16x32_bf16 v[196:199], v[120:123], v[148:151], v[196:199]\n\t" \
  "ds_read_b64_tr_b16 v[94:95], v59 offset:23040\n\t" \
  "v_mfma_f32_16x16x32_bf16 v[204:207], v[120:123], v[64:67], v[204:207]\n\t" \
  "v_mfma_f32_16x16x32_bf16 v[204:207], v[100:103], v[104:107], v[204:207]\n\t" \
  "v_mfma_f32_16x16x32_bf16 v[212:215], v[100:103], v[112:115], v[212:215]\n\t" \
  "v_mfma_f32_16x16x32_bf16 v[212:215], v[120:123], v[116:119], v[212:215]\n\t" \
  "v_mfma_f32_16x16x32_bf16 v[216:219], v[96:99], v[136:139], v[216:219]\n\t" \
  "v_mfma_f32_16x16x32_bf16 v[216:219], v[132:135], v[144:147], v[216:219]\n\t" \
  "v_mfma_f32_16x16x32_bf16 v[224:227], v[108:111], v[144:147], v[224:227]\n\t" \
  "v_mfma_f32_16x16x32_bf16 v[224:227], v[148:151], v[136:139], v[224:227]\n\t" \
  "v_mfma_f32_16x16x32_bf16 v[232:235], v[64:67], v[136:139], v[232:235]\n\t" \
  "v_mfma_f32_16x16x32_bf16 v[232:235], v[104:107], v[144:147], v[232:235]\n\t" \
  "v_mfma_f32_16x16x32_bf16 v[240:243], v[112:115], v[144:147], v[240:243]\n\t" \
  "v_mfma_f32_16x16x32_bf16 v[240:243], v[116:119], v[136:139], v[240:243]\n\t" \
  "v_mfma_f32_16x16x32_bf16 v[248:251], v[72:75], v[136:139], v[248:251]\n\t" \
  "v_mfma_f32_16x16x32_bf16 v[248:251], v[140:143], v[144:147], v[248:251]\n\t" \
  "v_mfma_f32_16x16x32_bf16 v[252:255], v[72:75], v[68:71], v[252:255]\n\t" \
  "global_store_dwordx4 v2, v[42:45], %24 offset:0 sc1\n\t" \
  "s_waitcnt lgkmcnt(0)\n\t" \
  "v_mfma_f32_16x16x32_bf16 v[252:255], v[140:143], v[76:79], v[252:255]\n\t" \
  "v_mfma_f32_16x16x32_bf16 v[42:45], v[26:29], v[18:21], 0\n\t" \
  "v_mfma_f32_16x16x32_bf16 v[42:45], v[30:33], v[22:25], v[42:45]\n\t" \
  "v_mfma_f32_16x16x32_bf16 v[46:49], v[30:33], v[18:21], 0\n\t" \
  "v_mfma_f32_16x16x32_bf16 v[46:49], v[34:37], v[22:25], v[46:49]\n\t" \
  "v_mfma_f32_16x16x32_bf16 v[220:223], v[132:135], v[76:79], v[220:223]\n\t" \
  "v_mfma_f32_16x16x32_bf16 v[220:223], v[96:99], v[68:71], v[220:223]\n\t" \
  "v_lshlrev_b32 v50, 16, v38\n\t" \
  "v_lshlrev_b32 v51, 16, v40\n\t" \
  "s_barrier\n\t" \
  "v_mfma_f32_16x16x32_bf16 v[228:231], v[148:151], v[68:71], v[228:231]\n\t" \
  "ds_read_b64_tr_b16 v[88:89], v62 offset:26624\n\t" \
  "v_mul_f32 v42, v42, v50\n\t" \
  "v_mfma_f32_16x16x32_bf16 v[228:231], v[108:111], v[76:79], v[228:231]\n\t" \
  "ds_read_b64_tr_b16 v[90:91], v62 offset:27136\n\t" \
  "v_mul_f32 v46, v46, v51\n\t" \
  "v_mfma_f32_16x16x32_bf16 v[236:239], v[104:107], v[76:79], v[236:239]\n\t" \
  "ds_read_b128 v[128:131], v60 offset:10240\n\t" \
  "v_cvt_pk_bf16_f32 v42, v42, v46\n\t" \
  "v_mfma_f32_16x16x32_bf16 v[236:239], v[64:67], v[68:71], v[236:239]\n\t" \
  "ds_read_b64_tr_b16 v[100:101], v62 offset:30720\n\t" \
  "v_and_b32 v50, 0xffff0000, v38\n\t" \
  "v_mfma_f32_16x16x32_bf16 v[244:247], v[116:119], v[68:71], v[244:247]\n\t" \
  "ds_read_b64_tr_b16 v[102:103], v62 offset:31232\n\t" \
  "v_and_b32 v51, 0xffff0000, v40\n\t" \
  "v_mfma_f32_16x16x32_bf16 v[244:247], v[112:115], v[76:79], v[244:247]\n\t" \
  "ds_read_b128 v[120:123], v61 offset:10240\n\t" \
  "v_mul_f32 v43, v43, v50\n\t" \
  "v_mfma_f32_16x16x32_bf16 v[152:155], v[80:83], v[96:99], v[152:155]\n\t" \
  "ds_read_b128 v[136:139], v61 offset:12288\n\t" \
  "v_mul_f32 v47, v47, v51\n\t" \
  "v_mfma_f32_16x16x32_bf16 v[152:155], v[124:127], v[132:135], v[152:155]\n\t" \
  "ds_read_b128 v[144:147], v60 offset:12288\n\t" \
  "v_cvt_pk_bf16_f32 v43, v43, v47\n\t" \
  "v_mfma_f32_16x16x32_bf16 v[160:163], v[124:127], v[108:111], v[160:163]\n\t" \
  "ds_read_b128 v[72:75], v60 offset:14336\n\t" \
  "v_lshlrev_b32 v50, 16, v39\n\t" \
  "v_mfma_f32_16x16x32_bf16 v[160:163], v[80:83], v[148:151], v[160:163]\n\t" \
  "ds_read_b128 v[140:143], v61 offset:14336\n\t" \
  "v_lshlrev_b32 v51, 16, v41\n\t" \
  "v_mfma_f32_16x16x32_bf16 v[168:171], v[80:83], v[64:67], v[168:171]\n\t" \
  "ds_read_b128 v[68:71], v61 offset:16384\n\t" \
  "v_mul_f32 v44, v44, v50\n\t" \
  "v_mfma_f32_16x16x32_bf16 v[168:171], v[124:127], v[104:107], v[168:171]\n\t" \
  "ds_read_b128 v[76:79], v60 offset:16384\n\t" \
  "v_mul_f32 v48, v48, v51\n\t" \
  "v_mfma_f32_16x16x32_bf16 v[176:179], v[124:127], v[112:115], v[176:179]\n\t" \
  "ds_read_b64_tr_b16 v[124:125], v63 offset:26624\n\t" \
  "v_cvt_pk_bf16_f32 v44, v44, v48\n\t" \
  "v_mfma_f32_16x16x32_bf16 v[176:179], v[80:83], v[116:119], v[176:179]\n\t" \
  "ds_read_b64_tr_b16 v[126:127], v63 offset:27136\n\t" \
  "v_and_b32 v50, 0xffff0000, v39\n\t" \
  "v_mfma_f32_16x16x32_bf16 v[156:159], v[84:87], v[96:99], v[156:159]\n\t" \
  "ds_read_b64_tr_b16 v[80:81], v63 offset:30720\n\t" \
  "v_and_b32 v51, 0xffff0000, v41\n\t" \
  "v_mfma_f32_16x16x32_bf16 v[156:159], v[92:95], v[132:135], v[156:159]\n\t" \
  "ds_read_b64_tr_b16 v[82:83], v63 offset:31232\n\t" \
  "v_mul_f32 v45, v45, v50\n\t" \
  "v_mfma_f32_16x16x32_bf16 v[164:167], v[92:95], v[108:111], v[164:167]\n\t" \
  "ds_read_b64_tr_b16 v[96:97], v62 offset:34816\n\t" \
  "v_mul_f32 v49, v49, v51\n\t" \
  "v_mfma_f32_16x16x32_bf16 v[164:167], v[84:87], v[148:151], v[164:167]\n\t" \
  "ds_read_b64_tr_b16 v[98:99], v62 offset:35328\n\t" \
  "v_cvt_pk_bf16_f32 v45, v45, v49\n\t" \
  "v_mfma_f32_16x16x32_bf16 v[172:175], v[84:87], v[64:67], v[172:175]\n\t" \
  "ds_read_b64_tr_b16 v[132:133], v62 offset:38912\n\t" \
  "v_mfma_f32_16x16x32_bf16 v[172:175], v[92:95], v[104:107], v[172:175]\n\t" \
  "ds_read_b64_tr_b16 v[134:135], v62 offset:39424\n\t" \
  "v_mfma_f32_16x16x32_bf16 v[180:183], v[92:95], v[112:115], v[180:183]\n\t" \
  "ds_read_b128 v[108:111], v60 offset:0\n\t" \
  "v_mfma_f32_16x16x32_bf16 v[180:183], v[84:87], v[116:119], v[180:183]\n\t" \
  "ds_read_b128 v[148:151], v61 offset:0\n\t" \
  "s_waitcnt lgkmcnt(7)\n\t" \
  "v_mfma_f32_16x16x32_bf16 v[184:187], v[88:91], v[128:131], v[184:187]\n\t" \
  "ds_read_b64_tr_b16 v[64:65], v63 offset:34816\n\t" \
  "v_mfma_f32_16x16x32_bf16 v[184:187], v[100:103], v[120:123], v[184:187]\n\t" \
  "ds_read_b64_tr_b16 v[66:67], v63 offset:35328\n\t" \
  "v_mfma_f32_16x16x32_bf16 v[192:195], v[100:103], v[136:139], v[192:195]\n\t" \
  "ds_read_b64_tr_b16 v[104:105], v63 offset:38912\n\t" \
  "v_mfma_f32_16x16x32_bf16 v[192:195], v[88:91], v[144:147], v[192:195]\n\t" \
  "ds_read_b64_tr_b16 v[106:107], v63 offset:39424\n\t" \
  "v_mfma_f32_16x16x32_bf16 v[200:203], v[88:91], v[72:75], v[200:203]\n\t" \
  "ds_read_b64_tr_b16 v[92:93], v62 offset:18432\n\t" \
  "v_mfma_f32_16x16x32_bf16 v[200:203], v[100:103], v[140:143], v[200:203]\n\t" \
  "ds_read_b64_tr_b16 v[94:95], v62 offset:18944\n\t" \
  "v_mfma_f32_16x16x32_bf16 v[208:211], v[100:103], v[68:71], v[208:211]\n\t" \
  "ds_read_b64_tr_b16 v[112:113], v62 offset:22528\n\t" \
  "v_mfma_f32_16x16x32_bf16 v[208:211], v[88:91], v[76:79], v[208:211]\n\t" \
  "ds_read_b64_tr_b16 v[114:115], v62 offset:23040\n\t" \
  "v_mfma_f32_16x16x32_bf16 v[188:191], v[124:127], v[128:131], v[188:191]\n\t" \
  "ds_read_b64_tr_b16 v[84:85], v63 offset:18432\n\t" \
  "s_waitcnt lgkmcnt(7)\n\t" \
  "v_mfma_f32_16x16x32_bf16 v[188:191], v[80:83], v[120:123], v[188:191]\n\t" \
  "ds_read_b64_tr_b16 v[86:87], v63 offset:18944\n\t" \
  "v_mfma_f32_16x16x32_bf16 v[196:199], v[80:83], v[136:139], v[196:199]\n\t" \
  "ds_read_b64_tr_b16 v[116:117], v63 offset:22528\n\t" \
  "v_mfma_f32_16x16x32_bf16 v[196:199], v[124:127], v[144:147], v[196:199]\n\t" \
  "ds_read_b64_tr_b16 v[118:119], v63 offset:23040\n\t" \
  "v_mfma_f32_16x16x32_bf16 v[204:207], v[124:127], v[72:75], v[204:207]\n\t" \
  "v_mfma_f32_16x16x32_bf16 v[204:207], v[80:83], v[140:143], v[204:207]\n\t" \
  "v_mfma_f32_16x16x32_bf16 v[212:215], v[80:83], v[68:71], v[212:215]\n\t" \
  "v_mfma_f32_16x16x32_bf16 v[212:215], v[124:127], v[76:79], v[212:215]\n\t" \
  "v_mfma_f32_16x16x32_bf16 v[216:219], v[128:131], v[96:99], v[216:219]\n\t" \
  "v_mfma_f32_16x16x32_bf16 v[216:219], v[120:123], v[132:135], v[216:219]\n\t" \
  "v_mfma_f32_16x16x32_bf16 v[224:227], v[136:139], v[132:135], v[224:227]\n\t" \
  "v_mfma_f32_16x16x32_bf16 v[224:227], v[144:147], v[96:99], v[224:227]\n\t" \
  "v_mfma_f32_16x16x32_bf16 v[232:235], v[72:75], v[96:99], v[232:235]\n\t" \
  "v_mfma_f32_16x16x32_bf16 v[232:235], v[140:143], v[132:135], v[232:235]\n\t" \
  "v_mfma_f32_16x16x32_bf16 v[240:243], v[68:71], v[132:135], v[240:243]\n\t" \
  "v_mfma_f32_16x16x32_bf16 v[240:243], v[76:79], v[96:99], v[240:243]\n\t" \
  "v_mfma_f32_16x16x32_bf16 v[248:251], v[108:111], v[96:99], v[248:251]\n\t" \
  "v_mfma_f32_16x16x32_bf16 v[248:251], v[148:151], v[132:135], v[248:251]\n\t" \
  "v_mfma_f32_16x16x32_bf16 v[252:255], v[108:111], v[64:67], v[252:255]\n\t" \
  "global_store_dwordx4 v2, v[42:45], %24 offset:1024 sc1\n\t" \
  "s_waitcnt lgkmcnt(0)\n\t" \
  "v_mfma_f32_16x16x32_bf16 v[252:255], v[148:151], v[104:107], v[252:255]\n\t" \
  "v_mfma_f32_16x16x32_bf16 v[220:223], v[120:123], v[104:107], v[220:223]\n\t" \
  "v_mfma_f32_16x16x32_bf16 v[220:223], v[128:131], v[64:67], v[220:223]\n\t" \
  "v_mov_b32 v30, 0xff61b1e6\n\t" \
  "v_bfe_i32 v34, v3, 0, 1\n\t" \
  "s_barrier\n\t" \
  "v_mfma_f32_16x16x32_bf16 v[228:231], v[144:147], v[64:67], v[228:231]\n\t" \
  "ds_read_b64_tr_b16 v[100:101], v54 offset:26624\n\t" \
  "v_and_b32 v34, v30, v34\n\t" \
  "v_mfma_f32_16x16x32_bf16 v[228:231], v[136:139], v[104:107], v[228:231]\n\t" \
  "ds_read_b64_tr_b16 v[102:103], v54 offset:27136\n\t" \
  "v_bfe_i32 v38, v3, 4, 1\n\t" \
  "v_mfma_f32_16x16x32_bf16 v[236:239], v[140:143], v[104:107], v[236:239]\n\t" \
  "ds_read_b128 v[88:91], v52 offset:10240\n\t" \
  "v_and_b32 v38, v30, v38\n\t" \
  "v_mfma_f32_16x16x32_bf16 v[236:239], v[72:75], v[64:67], v[236:239]\n\t" \
  "ds_read_b64_tr_b16 v[80:81], v54 offset:30720\n\t" \
  "v_bfe_i32 v42, v3, 8, 1\n\t" \
  "v_mfma_f32_16x16x32_bf16 v[244:247], v[76:79], v[64:67], v[244:247]\n\t" \
  "ds_read_b64_tr_b16 v[82:83], v54 offset:31232\n\t" \
  "v_bfi_b32 v42, v42, v30, %15\n\t" \
  "v_mfma_f32_16x16x32_bf16 v[244:247], v[68:71], v[104:107], v[244:247]\n\t" \
  "ds_read_b128 v[124:127], v53 offset:10240\n\t" \
  "v_mov_b32 v48, 0x3f803f80\n\t" \
  "v_mfma_f32_16x16x32_bf16 v[152:155], v[92:95], v[128:131], v[152:155]\n\t" \
  "ds_read_b128 v[96:99], v53 offset:12288\n\t" \
  "v_bfe_i32 v35, v3, 1, 1\n\t" \
  "v_mfma_f32_16x16x32_bf16 v[152:155], v[112:115], v[120:123], v[152:155]\n\t" \
  "ds_read_b128 v[132:135], v52 offset:12288\n\t" \
  "v_and_b32 v35, v30, v35\n\t" \
  "v_mfma_f32_16x16x32_bf16 v[160:163], v[112:115], v[136:139], v[160:163]\n\t" \
  "ds_read_b128 v[108:111], v52 offset:14336\n\t" \
  "v_bfe_i32 v39, v3, 5, 1\n\t" \
  "v_mfma_f32_16x16x32_bf16 v[160:163], v[92:95], v[144:147], v[160:163]\n\t" \
  "ds_read_b128 v[148:151], v53 offset:14336\n\t" \
  "v_and_b32 v39, v30, v39\n\t" \
  "v_mfma_f32_16x16x32_bf16 v[168:171], v[92:95], v[72:75], v[168:171]\n\t" \
  "ds_read_b128 v[64:67], v53 offset:16384\n\t" \
  "v_bfe_i32 v43, v3, 9, 1\n\t" \
  "v_mfma_f32_16x16x32_bf16 v[168:171], v[112:115], v[140:143], v[168:171]\n\t" \
  "ds_read_b128 v[104:107], v52 offset:16384\n\t" \
  "v_bfi_b32 v43, v43, v30, %15\n\t" \
  "v_mfma_f32_16x16x32_bf16 v[176:179], v[112:115], v[68:71], v[176:179]\n\t" \
  "ds_read_b64_tr_b16 v[112:113], v55 offset:26624\n\t" \
  "v_mov_b32 v49, 0x3f803f80\n\t" \
  "v_mfma_f32_16x16x32_bf16 v[176:179], v[92:95], v[76:79], v[176:179]\n\t" \
  "ds_read_b64_tr_b16 v[114:115], v55 offset:27136\n\t" \
  "v_bfe_i32 v36, v3, 2, 1\n\t" \
  "v_mfma_f32_16x16x32_bf16 v[156:159], v[84:87], v[128:131], v[156:159]\n\t" \
  "ds_read_b64_tr_b16 v[92:93], v55 offset:30720\n\t" \
  "v_and_b32 v36, v30, v36\n\t" \
  "v_mfma_f32_16x16x32_bf16 v[156:159], v[116:119], v[120:123], v[156:159]\n\t" \
  "ds_read_b64_tr_b16 v[94:95], v55 offset:31232\n\t" \
  "v_bfe_i32 v40, v3, 6, 1\n\t" \
  "v_mfma_f32_16x16x32_bf16 v[164:167], v[116:119], v[136:139], v[164:167]\n\t" \
  "ds_read_b64_tr_b16 v[128:129], v54 offset:34816\n\t" \
  "v_and_b32 v40, v30, v40\n\t" \
  "v_mfma_f32_16x16x32_bf16 v[164:167], v[84:87], v[144:147], v[164:167]\n\t" \
  "ds_read_b64_tr_b16 v[130:131], v54 offset:35328\n\t" \
  "v_bfe_i32 v44, v3, 10, 1\n\t" \
  "v_mfma_f32_16x16x32_bf16 v[172:175], v[84:87], v[72:75], v[172:175]\n\t" \
  "ds_read_b64_tr_b16 v[120:121], v54 offset:38912\n\t" \
  "v_bfi_b32 v44, v44, v30, %15\n\t" \
  "v_mfma_f32_16x16x32_bf16 v[172:175], v[116:119], v[140:143], v[172:175]\n\t" \
  "ds_read_b64_tr_b16 v[122:123], v54 offset:39424\n\t" \
  "v_mov_b32 v50, 0x3f803f80\n\t" \
  "v_mfma_f32_16x16x32_bf16 v[180:183], v[116:119], v[68:71], v[180:183]\n\t" \
  "ds_read_b128 v[136:139], v52 offset:0\n\t" \
  "v_bfe_i32 v37, v3, 3, 1\n\t" \
  "v_mfma_f32_16x16x32_bf16 v[180:183], v[84:87], v[76:79], v[180:183]\n\t" \
  "ds_read_b128 v[144:147], v53 offset:0\n\t" \
  "v_and_b32 v37, v30, v37\n\t" \
  "s_waitcnt lgkmcnt(7)\n\t" \
  "v_mfma_f32_16x16x32_bf16 v[184:187], v[100:103], v[88:91], v[184:187]\n\t" \
  "ds_read_b64_tr_b16 v[72:73], v55 offset:34816\n\t" \
  "v_bfe_i32 v41, v3, 7, 1\n\t" \
  "v_mfma_f32_16x16x32_bf16 v[184:187], v[80:83], v[124:127], v[184:187]\n\t" \
  "ds_read_b64_tr_b16 v[74:75], v55 offset:35328\n\t" \
  "v_and_b32 v41, v30, v41\n\t" \
  "v_mfma_f32_16x16x32_bf16 v[192:195], v[80:83], v[96:99], v[192:195]\n\t" \
  "ds_read_b64_tr_b16 v[140:141], v55 offset:38912\n\t" \
  "v_bfe_i32 v45, v3, 11, 1\n\t" \
  "v_mfma_f32_16x16x32_bf16 v[192:195], v[100:103], v[132:135], v[192:195]\n\t" \
  "ds_read_b64_tr_b16 v[142:143], v55 offset:39424\n\t" \
  "v_bfi_b32 v45, v45, v30, %15\n\t" \
  "v_mfma_f32_16x16x32_bf16 v[200:203], v[100:103], v[108:111], v[200:203]\n\t" \
  "ds_read_b64_tr_b16 v[68:69], v54 offset:18432\n\t" \
  "v_mov_b32 v51, 0x3f803f80\n\t" \
  "v_mfma_f32_16x16x32_bf16 v[200:203], v[80:83], v[148:151], v[200:203]\n\t" \
  "ds_read_b64_tr_b16 v[70:71], v54 offset:18944\n\t" \
  "v_bfe_i32 v46, v3, 12, 1\n\t" \
  "v_mfma_f32_16x16x32_bf16 v[208:211], v[80:83], v[64:67], v[208:211]\n\t" \
  "ds_read_b64_tr_b16 v[116:117], v54 offset:22528\n\t" \
  "v_bfe_i32 v31, v3, 13, 1\n\t" \
  "v_mfma_f32_16x16x32_bf16 v[208:211], v[100:103], v[104:107], v[208:211]\n\t" \
  "ds_read_b64_tr_b16 v[118:119], v54 offset:23040\n\t" \
  "v_perm_b32 v46, v31, v46, vcc_hi\n\t" \
  "v_mfma_f32_16x16x32_bf16 v[188:191], v[112:115], v[88:91], v[188:191]\n\t" \
  "ds_read_b64_tr_b16 v[76:77], v55 offset:18432\n\t" \
  "v_bfe_i32 v47, v3, 14, 1\n\t" \
  "s_waitcnt lgkmcnt(7)\n\t" \
  "v_mfma_f32_16x16x32_bf16 v[188:191], v[92:95], v[124:127], v[188:191]\n\t" \
  "ds_read_b64_tr_b16 v[78:79], v55 offset:18944\n\t" \
  "v_bfe_i32 v31, v3, 15, 1\n\t" \
  "v_mfma_f32_16x16x32_bf16 v[196:199], v[92:95], v[96:99], v[196:199]\n\t" \
  "ds_read_b64_tr_b16 v[84:85], v55 offset:22528\n\t" \
  "v_perm_b32 v47, v31, v47, vcc_hi\n\t" \
  "v_mfma_f32_16x16x32_bf16 v[196:199], v[112:115], v[132:135], v[196:199]\n\t" \
  "ds_read_b64_tr_b16 v[86:87], v55 offset:23040\n\t" \
  "v_add_u32 v30, 18432, v4\n\t" \
  "v_mfma_f32_16x16x32_bf16 v[204:207], v[112:115], v[108:111], v[204:207]\n\t" \
  "v_xor_b32 v31, 1024, v30\n\t" \
  "v_mov_b32 v32, %12\n\t" \
  "v_mfma_f32_16x16x32_bf16 v[204:207], v[92:95], v[148:151], v[204:207]\n\t" \
  "v_xor_b32 v33, 4, v32\n\t" \
  "v_mov_b32 v0, 1\n\t" \
  "v_mfma_f32_16x16x32_bf16 v[212:215], v[92:95], v[64:67], v[212:215]\n\t" \
  "v_and_b32 v4, 1023, v4\n\t" \
  "v_add_u32 v1, 8192, v4\n\t" \
  "v_mfma_f32_16x16x32_bf16 v[212:215], v[112:115], v[104:107], v[212:215]\n\t" \
  "v_mfma_f32_16x16x32_bf16 v[216:219], v[88:91], v[128:131], v[216:219]\n\t" \
  "v_mfma_f32_16x16x32_bf16 v[216:219], v[124:127], v[120:123], v[216:219]\n\t" \
  "v_mfma_f32_16x16x32_bf16 v[224:227], v[96:99], v[120:123], v[224:227]\n\t" \
  "v_mfma_f32_16x16x32_bf16 v[224:227], v[132:135], v[128:131], v[224:227]\n\t" \
  "v_mfma_f32_16x16x32_bf16 v[232:235], v[108:111], v[128:131], v[232:235]\n\t" \
  "v_mfma_f32_16x16x32_bf16 v[232:235], v[148:151], v[120:123], v[232:235]\n\t" \
  "v_mfma_f32_16x16x32_bf16 v[240:243], v[64:67], v[120:123], v[240:243]\n\t" \
  "v_mfma_f32_16x16x32_bf16 v[240:243], v[104:107], v[128:131], v[240:243]\n\t" \
  "v_mfma_f32_16x16x32_bf16 v[248:251], v[136:139], v[128:131], v[248:251]\n\t" \
  "v_mfma_f32_16x16x32_bf16 v[248:251], v[144:147], v[120:123], v[248:251]\n\t" \
  "v_mfma_f32_16x16x32_bf16 v[252:255], v[136:139], v[72:75], v[252:255]\n\t" \
  "s_waitcnt lgkmcnt(0)\n\t" \
  "v_mfma_f32_16x16x32_bf16 v[252:255], v[144:147], v[140:143], v[252:255]\n\t" \
  "v_mfma_f32_16x16x32_bf16 v[220:223], v[124:127], v[140:143], v[220:223]\n\t" \
  "v_mfma_f32_16x16x32_bf16 v[220:223], v[88:91], v[72:75], v[220:223]\n\t" \
  "s_barrier\n\t" \
  "v_mfma_f32_16x16x32_bf16 v[228:231], v[132:135], v[72:75], v[228:231]\n\t" \
  "ds_read_b64_tr_b16 v[80:81], v58 offset:26624\n\t" \
  "v_mfma_f32_16x16x32_bf16 v[228:231], v[96:99], v[140:143], v[228:231]\n\t" \
  "ds_read_b64_tr_b16 v[82:83], v58 offset:27136\n\t" \
  "v_mfma_f32_16x16x32_bf16 v[236:239], v[148:151], v[140:143], v[236:239]\n\t" \
  "ds_read_b128 v[100:103], v56 offset:10240\n\t" \
  "v_mfma_f32_16x16x32_bf16 v[236:239], v[108:111], v[72:75], v[236:239]\n\t" \
  "ds_read_b64_tr_b16 v[92:93], v58 offset:30720\n\t" \
  "v_mfma_f32_16x16x32_bf16 v[244:247], v[104:107], v[72:75], v[244:247]\n\t" \
  "ds_read_b64_tr_b16 v[94:95], v58 offset:31232\n\t" \
  "v_mfma_f32_16x16x32_bf16 v[244:247], v[64:67], v[140:143], v[244:247]\n\t" \
  "ds_read_b128 v[112:115], v57 offset:10240\n\t" \
  "v_mfma_f32_16x16x32_bf16 v[152:155], v[68:71], v[88:91], v[152:155]\n\t" \
  "ds_read_b128 v[128:131], v57 offset:12288\n\t" \
  "v_mfma_f32_16x16x32_bf16 v[152:155], v[116:119], v[124:127], v[152:155]\n\t" \
  "ds_read_b128 v[120:123], v56 offset:12288\n\t" \
  "v_mfma_f32_16x16x32_bf16 v[160:163], v[116:119], v[96:99], v[160:163]\n\t" \
  "ds_read_b128 v[136:139], v56 offset:14336\n\t" \
  "v_mfma_f32_16x16x32_bf16 v[160:163], v[68:71], v[132:135], v[160:163]\n\t" \
  "ds_read_b128 v[144:147], v57 offset:14336\n\t" \
  "v_mfma_f32_16x16x32_bf16 v[168:171], v[68:71], v[108:111], v[168:171]\n\t" \
  "ds_read_b128 v[72:75], v57 offset:16384\n\t" \
  "v_mfma_f32_16x16x32_bf16 v[168:171], v[116:119], v[148:151], v[168:171]\n\t" \
  "ds_read_b128 v[140:143], v56 offset:16384\n\t" \
  "v_mfma_f32_16x16x32_bf16 v[176:179], v[116:119], v[64:67], v[176:179]\n\t" \
  "ds_read_b64_tr_b16 v[116:117], v59 offset:26624\n\t" \
  "v_mfma_f32_16x16x32_bf16 v[176:179], v[68:71], v[104:107], v[176:179]\n\t" \
  "ds_read_b64_tr_b16 v[118:119], v59 offset:27136\n\t" \
  "v_mfma_f32_16x16x32_bf16 v[156:159], v[76:79], v[88:91], v[156:159]\n\t" \
  "ds_read_b64_tr_b16 v[68:69], v59 offset:30720\n\t" \
  "v_mfma_f32_16x16x32_bf16 v[156:159], v[84:87], v[124:127], v[156:159]\n\t" \
  "ds_read_b64_tr_b16 v[70:71], v59 offset:31232\n\t" \
  "v_mfma_f32_16x16x32_bf16 v[164:167], v[84:87], v[96:99], v[164:167]\n\t" \
  "ds_read_b64_tr_b16 v[88:89], v58 offset:34816\n\t" \
  "v_mfma_f32_16x16x32_bf16 v[164:167], v[76:79], v[132:135], v[164:167]\n\t" \
  "ds_read_b64_tr_b16 v[90:91], v58 offset:35328\n\t" \
  "v_mfma_f32_16x16x32_bf16 v[172:175], v[76:79], v[108:111], v[172:175]\n\t" \
  "ds_read_b64_tr_b16 v[124:125], v58 offset:38912\n\t" \
  "v_mfma_f32_16x16x32_bf16 v[172:175], v[84:87], v[148:151], v[172:175]\n\t" \
  "ds_read_b64_tr_b16 v[126:127], v58 offset:39424\n\t" \
  "v_mfma_f32_16x16x32_bf16 v[180:183], v[84:87], v[64:67], v[180:183]\n\t" \
  "ds_read_b128 v[96:99], v56 offset:0\n\t" \
  "v_mfma_f32_16x16x32_bf16 v[180:183], v[76:79], v[104:107], v[180:183]\n\t" \
  "ds_read_b128 v[132:135], v57 offset:0\n\t" \
  "s_waitcnt lgkmcnt(7)\n\t" \
  "v_mfma_f32_16x16x32_bf16 v[184:187], v[80:83], v[100:103], v[184:187]\n\t" \
  "ds_read_b64_tr_b16 v[108:109], v59 offset:34816\n\t" \
  "v_mfma_f32_16x16x32_bf16 v[184:187], v[92:95], v[112:115], v[184:187]\n\t" \
  "ds_read_b64_tr_b16 v[110:111], v59 offset:35328\n\t" \
  "v_mfma_f32_16x16x32_bf16 v[192:195], v[92:95], v[128:131], v[192:195]\n\t" \
  "ds_read_b64_tr_b16 v[148:149], v59 offset:38912\n\t" \
  "v_mfma_f32_16x16x32_bf16 v[192:195], v[80:83], v[120:123], v[192:195]\n\t" \
  "ds_read_b64_tr_b16 v[150:151], v59 offset:39424\n\t" \
  "v_mfma_f32_16x16x32_bf16 v[200:203], v[80:83], v[136:139], v[200:203]\n\t" \
  "ds_read_b64_tr_b16 v[64:65], v58 offset:18432\n\t" \
  "v_mfma_f32_16x16x32_bf16 v[200:203], v[92:95], v[144:147], v[200:203]\n\t" \
  "ds_read_b64_tr_b16 v[66:67], v58 offset:18944\n\t" \
  "v_mfma_f32_16x16x32_bf16 v[208:211], v[92:95], v[72:75], v[208:211]\n\t" \
  "ds_read_b64_tr_b16 v[84:85], v58 offset:22528\n\t" \
  "v_mfma_f32_16x16x32_bf16 v[208:211], v[80:83], v[140:143], v[208:211]\n\t" \
  "ds_read_b64_tr_b16 v[86:87], v58 offset:23040\n\t" \
  "v_mfma_f32_16x16x32_bf16 v[188:191], v[116:119], v[100:103], v[188:191]\n\t" \
  "ds_read_b64_tr_b16 v[76:77], v59 offset:18432\n\t" \
  "s_waitcnt lgkmcnt(7)\n\t" \
  "v_mfma_f32_16x16x32_bf16 v[188:191], v[68:71], v[112:115], v[188:191]\n\t" \
  "ds_read_b64_tr_b16 v[78:79], v59 offset:18944\n\t" \
  "v_mfma_f32_16x16x32_bf16 v[196:199], v[68:71], v[128:131], v[196:199]\n\t" \
  "ds_read_b64_tr_b16 v[104:105], v59 offset:22528\n\t" \
  "v_mfma_f32_16x16x32_bf16 v[196:199], v[116:119], v[120:123], v[196:199]\n\t" \
  "ds_read_b64_tr_b16 v[106:107], v59 offset:23040\n\t" \
  "v_mfma_f32_16x16x32_bf16 v[204:207], v[116:119], v[136:139], v[204:207]\n\t" \
  "v_mfma_f32_16x16x32_bf16 v[204:207], v[68:71], v[144:147], v[204:207]\n\t" \
  "v_mfma_f32_16x16x32_bf16 v[212:215], v[68:71], v[72:75], v[212:215]\n\t" \
  "v_mfma_f32_16x16x32_bf16 v[212:215], v[116:119], v[140:143], v[212:215]\n\t" \
  "v_mfma_f32_16x16x32_bf16 v[216:219], v[100:103], v[88:91], v[216:219]\n\t" \
  "v_mfma_f32_16x16x32_bf16 v[216:219], v[112:115], v[124:127], v[216:219]\n\t" \
  "v_mfma_f32_16x16x32_bf16 v[224:227], v[128:131], v[124:127], v[224:227]\n\t" \
  "v_mfma_f32_16x16x32_bf16 v[224:227], v[120:123], v[88:91], v[224:227]\n\t" \
  "v_mfma_f32_16x16x32_bf16 v[232:235], v[136:139], v[88:91], v[232:235]\n\t" \
  "v_mfma_f32_16x16x32_bf16 v[232:235], v[144:147], v[124:127], v[232:235]\n\t" \
  "v_mfma_f32_16x16x32_bf16 v[240:243], v[72:75], v[124:127], v[240:243]\n\t" \
  "v_mfma_f32_16x16x32_bf16 v[240:243], v[140:143], v[88:91], v[240:243]\n\t" \
  "v_mfma_f32_16x16x32_bf16 v[248:251], v[96:99], v[88:91], v[248:251]\n\t" \
  "v_mfma_f32_16x16x32_bf16 v[248:251], v[132:135], v[124:127], v[248:251]\n\t" \
  "v_mfma_f32_16x16x32_bf16 v[252:255], v[96:99], v[108:111], v[252:255]\n\t" \
  "s_waitcnt lgkmcnt(0)\n\t" \
  "v_mfma_f32_16x16x32_bf16 v[252:255], v[132:135], v[148:151], v[252:255]\n\t" \
  "v_mfma_f32_16x16x32_bf16 v[220:223], v[112:115], v[148:151], v[220:223]\n\t" \
  "v_mfma_f32_16x16x32_bf16 v[220:223], v[100:103], v[108:111], v[220:223]\n\t" \
  "s_waitcnt vmcnt(0)\n\t" \
  "s_barrier\n\t" \
  "v_mfma_f32_16x16x32_bf16 v[228:231], v[120:123], v[108:111], v[228:231]\n\t" \
  "ds_read_b64_tr_b16 v[92:93], v62 offset:26624\n\t" \
  "v_mfma_f32_16x16x32_bf16 v[228:231], v[128:131], v[148:151], v[228:231]\n\t" \
  "ds_read_b64_tr_b16 v[94:95], v62 offset:27136\n\t" \
  "v_mfma_f32_16x16x32_bf16 v[236:239], v[144:147], v[148:151], v[236:239]\n\t" \
  "ds_read_b128 v[80:83], v60 offset:10240\n\t" \
  "v_mfma_f32_16x16x32_bf16 v[236:239], v[136:139], v[108:111], v[236:239]\n\t" \
  "ds_read_b64_tr_b16 v[68:69], v62 offset:30720\n\t" \
  "v_mfma_f32_16x16x32_bf16 v[244:247], v[140:143], v[108:111], v[244:247]\n\t" \
  "ds_read_b64_tr_b16 v[70:71], v62 offset:31232\n\t" \
  "v_mfma_f32_16x16x32_bf16 v[244:247], v[72:75], v[148:151], v[244:247]\n\t" \
  "ds_read_b128 v[116:119], v61 offset:10240\n\t" \
  "v_mfma_f32_16x16x32_bf16 v[152:155], v[64:67], v[100:103], v[152:155]\n\t" \
  "ds_read_b128 v[88:91], v61 offset:12288\n\t" \
  "v_mfma_f32_16x16x32_bf16 v[152:155], v[84:87], v[112:115], v[152:155]\n\t" \
  "ds_read_b128 v[124:127], v60 offset:12288\n\t" \
  "v_mfma_f32_16x16x32_bf16 v[160:163], v[84:87], v[128:131], v[160:163]\n\t" \
  "ds_read_b128 v[96:99], v60 offset:14336\n\t" \
  "v_mfma_f32_16x16x32_bf16 v[160:163], v[64:67], v[120:123], v[160:163]\n\t" \
  "ds_read_b128 v[132:135], v61 offset:14336\n\t" \
  "v_mfma_f32_16x16x32_bf16 v[168:171], v[64:67], v[136:139], v[168:171]\n\t" \
  "ds_read_b128 v[108:111], v61 offset:16384\n\t" \
  "v_mfma_f32_16x16x32_bf16 v[168:171], v[84:87], v[144:147], v[168:171]\n\t" \
  "ds_read_b128 v[148:151], v60 offset:16384\n\t" \
  "v_mfma_f32_16x16x32_bf16 v[176:179], v[84:87], v[72:75], v[176:179]\n\t" \
  "ds_read_b64_tr_b16 v[84:85], v63 offset:26624\n\t" \
  "v_mfma_f32_16x16x32_bf16 v[176:179], v[64:67], v[140:143], v[176:179]\n\t" \
  "ds_read_b64_tr_b16 v[86:87], v63 offset:27136\n\t" \
  "v_mfma_f32_16x16x32_bf16 v[156:159], v[76:79], v[100:103], v[156:159]\n\t" \
  "ds_read_b64_tr_b16 v[64:65], v63 offset:30720\n\t" \
  "v_mfma_f32_16x16x32_bf16 v[156:159], v[104:107], v[112:115], v[156:159]\n\t" \
  "ds_read_b64_tr_b16 v[66:67], v63 offset:31232\n\t" \
  "v_mfma_f32_16x16x32_bf16 v[164:167], v[104:107], v[128:131], v[164:167]\n\t" \
  "ds_read_b64_tr_b16 v[100:101], v62 offset:34816\n\t" \
  "v_mfma_f32_16x16x32_bf16 v[164:167], v[76:79], v[120:123], v[164:167]\n\t" \
  "ds_read_b64_tr_b16 v[102:103], v62 offset:35328\n\t" \
  "v_mfma_f32_16x16x32_bf16 v[172:175], v[76:79], v[136:139], v[172:175]\n\t" \
  "ds_read_b64_tr_b16 v[112:113], v62 offset:38912\n\t" \
  "v_mfma_f32_16x16x32_bf16 v[172:175], v[104:107], v[144:147], v[172:175]\n\t" \
  "ds_read_b64_tr_b16 v[114:115], v62 offset:39424\n\t" \
  "v_mfma_f32_16x16x32_bf16 v[180:183], v[104:107], v[72:75], v[180:183]\n\t" \
  "ds_read_b128 v[128:131], v60 offset:0\n\t" \
  "v_mfma_f32_16x16x32_bf16 v[180:183], v[76:79], v[140:143], v[180:183]\n\t" \
  "ds_read_b128 v[120:123], v61 offset:0\n\t" \
  "s_waitcnt lgkmcnt(7)\n\t" \
  "v_mfma_f32_16x16x32_bf16 v[184:187], v[92:95], v[80:83], v[184:187]\n\t" \
  "ds_read_b64_tr_b16 v[136:137], v63 offset:34816\n\t" \
  "v_mfma_f32_16x16x32_bf16 v[184:187], v[68:71], v[116:119], v[184:187]\n\t" \
  "ds_read_b64_tr_b16 v[138:139], v63 offset:35328\n\t" \
  "v_mfma_f32_16x16x32_bf16 v[192:195], v[68:71], v[88:91], v[192:195]\n\t" \
  "ds_read_b64_tr_b16 v[144:145], v63 offset:38912\n\t" \
  "v_mfma_f32_16x16x32_bf16 v[192:195], v[92:95], v[124:127], v[192:195]\n\t" \
  "ds_read_b64_tr_b16 v[146:147], v63 offset:39424\n\t" \
  "v_mfma_f32_16x16x32_bf16 v[200:203], v[92:95], v[96:99], v[200:203]\n\t" \
  "ds_read_b64_tr_b16 v[72:73], v62 offset:18432\n\t" \
  "v_mfma_f32_16x16x32_bf16 v[200:203], v[68:71], v[132:135], v[200:203]\n\t" \
  "ds_read_b64_tr_b16 v[74:75], v62 offset:18944\n\t" \
  "v_mfma_f32_16x16x32_bf16 v[208:211], v[68:71], v[108:111], v[208:211]\n\t" \
  "ds_read_b64_tr_b16 v[104:105], v62 offset:22528\n\t" \
  "v_mfma_f32_16x16x32_bf16 v[208:211], v[92:95], v[148:151], v[208:211]\n\t" \
  "ds_read_b64_tr_b16 v[106:107], v62 offset:23040\n\t" \
  "v_mfma_f32_16x16x32_bf16 v[188:191], v[84:87], v[80:83], v[188:191]\n\t" \
  "ds_read_b64_tr_b16 v[76:77], v63 offset:18432\n\t" \
  "s_waitcnt lgkmcnt(7)\n\t" \
  "v_mfma_f32_16x16x32_bf16 v[188:191], v[64:67], v[116:119], v[188:191]\n\t" \
  "ds_read_b64_tr_b16 v[78:79], v63 offset:18944\n\t" \
  "v_mfma_f32_16x16x32_bf16 v[196:199], v[64:67], v[88:91], v[196:199]\n\t" \
  "ds_read_b64_tr_b16 v[140:141], v63 offset:22528\n\t" \
  "v_mfma_f32_16x16x32_bf16 v[196:199], v[84:87], v[124:127], v[196:199]\n\t" \
  "ds_read_b64_tr_b16 v[142:143], v63 offset:23040\n\t" \
  "v_mfma_f32_16x16x32_bf16 v[204:207], v[84:87], v[96:99], v[204:207]\n\t" \
  "v_mfma_f32_16x16x32_bf16 v[204:207], v[64:67], v[132:135], v[204:207]\n\t" \
  "v_mfma_f32_16x16x32_bf16 v[212:215], v[64:67], v[108:111], v[212:215]\n\t" \
  "v_mfma_f32_16x16x32_bf16 v[212:215], v[84:87], v[148:151], v[212:215]\n\t" \
  "v_mfma_f32_16x16x32_bf16 v[216:219], v[80:83], v[100:103], v[216:219]\n\t" \
  "v_mfma_f32_16x16x32_bf16 v[216:219], v[116:119], v[112:115], v[216:219]\n\t" \
  "v_mfma_f32_16x16x32_bf16 v[224:227], v[88:91], v[112:115], v[224:227]\n\t" \
  "v_mfma_f32_16x16x32_bf16 v[224:227], v[124:127], v[100:103], v[224:227]\n\t" \
  "v_mfma_f32_16x16x32_bf16 v[232:235], v[96:99], v[100:103], v[232:235]\n\t" \
  "v_mfma_f32_16x16x32_bf16 v[232:235], v[132:135], v[112:115], v[232:235]\n\t" \
  "v_mfma_f32_16x16x32_bf16 v[240:243], v[108:111], v[112:115], v[240:243]\n\t" \
  "v_mfma_f32_16x16x32_bf16 v[240:243], v[148:151], v[100:103], v[240:243]\n\t" \
  "501:\n\t" \
  "s_cbranch_execz 505f\n\t" \
  "v_mfma_f32_16x16x32_bf16 v[248:251], v[128:131], v[100:103], v[248:251]\n\t" \
  "v_mfma_f32_16x16x32_bf16 v[248:251], v[120:123], v[112:115], v[248:251]\n\t" \
  "v_mfma_f32_16x16x32_bf16 v[252:255], v[128:131], v[136:139], v[252:255]\n\t" \
  "s_waitcnt lgkmcnt(0)\n\t" \
  "v_mfma_f32_16x16x32_bf16 v[252:255], v[120:123], v[144:147], v[252:255]\n\t" \
  "v_mfma_f32_16x16x32_bf16 v[220:223], v[116:119], v[144:147], v[220:223]\n\t" \
  "v_mfma_f32_16x16x32_bf16 v[220:223], v[80:83], v[136:139], v[220:223]\n\t" \
  "s_barrier\n\t" \
  "v_mfma_f32_16x16x32_bf16 v[228:231], v[124:127], v[136:139], v[228:231]\n\t" \
  "ds_read_b64_tr_b16 v[68:69], v54 offset:26624\n\t" \
  "v_mfma_f32_16x16x32_bf16 v[228:231], v[88:91], v[144:147], v[228:231]\n\t" \
  "ds_read_b64_tr_b16 v[70:71], v54 offset:27136\n\t" \
  "v_mfma_f32_16x16x32_bf16 v[236:239], v[132:135], v[144:147], v[236:239]\n\t" \
  "ds_read_b128 v[92:95], v52 offset:10240\n\t" \
  "v_mfma_f32_16x16x32_bf16 v[236:239], v[96:99], v[136:139], v[236:239]\n\t" \
  "ds_read_b64_tr_b16 v[64:65], v54 offset:30720\n\t" \
  "v_mfma_f32_16x16x32_bf16 v[244:247], v[148:151], v[136:139], v[244:247]\n\t" \
  "ds_read_b64_tr_b16 v[66:67], v54 offset:31232\n\t" \
  "v_mfma_f32_16x16x32_bf16 v[244:247], v[108:111], v[144:147], v[244:247]\n\t" \
  "ds_read_b128 v[84:87], v53 offset:10240\n\t" \
  "v_mfma_f32_16x16x32_bf16 v[152:155], v[72:75], v[80:83], v[152:155]\n\t" \
  "ds_read_b128 v[100:103], v53 offset:12288\n\t" \
  "v_mfma_f32_16x16x32_bf16 v[152:155], v[104:107], v[116:119], v[152:155]\n\t" \
  "ds_read_b128 v[112:115], v52 offset:12288\n\t" \
  "v_mfma_f32_16x16x32_bf16 v[160:163], v[104:107], v[88:91], v[160:163]\n\t" \
  "ds_read_b128 v[128:131], v52 offset:14336\n\t" \
  "v_mfma_f32_16x16x32_bf16 v[160:163], v[72:75], v[124:127], v[160:163]\n\t" \
  "ds_read_b128 v[120:123], v53 offset:14336\n\t" \
  "v_mfma_f32_16x16x32_bf16 v[168:171], v[72:75], v[96:99], v[168:171]\n\t" \
  "ds_read_b128 v[136:139], v53 offset:16384\n\t" \
  "v_mfma_f32_16x16x32_bf16 v[168:171], v[104:107], v[132:135], v[168:171]\n\t" \
  "ds_read_b128 v[144:147], v52 offset:16384\n\t" \
  "v_mfma_f32_16x16x32_bf16 v[176:179], v[104:107], v[108:111], v[176:179]\n\t" \
  "ds_read_b64_tr_b16 v[104:105], v55 offset:26624\n\t" \
  "v_mfma_f32_16x16x32_bf16 v[176:179], v[72:75], v[148:151], v[176:179]\n\t" \
  "ds_read_b64_tr_b16 v[106:107], v55 offset:27136\n\t" \
  "v_mfma_f32_16x16x32_bf16 v[156:159], v[76:79], v[80:83], v[156:159]\n\t" \
  "ds_read_b64_tr_b16 v[72:73], v55 offset:30720\n\t" \
  "v_mfma_f32_16x16x32_bf16 v[156:159], v[140:143], v[116:119], v[156:159]\n\t" \
  "ds_read_b64_tr_b16 v[74:75], v55 offset:31232\n\t" \
  "v_mfma_f32_16x16x32_bf16 v[164:167], v[140:143], v[88:91], v[164:167]\n\t" \
  "ds_read_b64_tr_b16 v[80:81], v54 offset:34816\n\t" \
  "v_mfma_f32_16x16x32_bf16 v[164:167], v[76:79], v[124:127], v[164:167]\n\t" \
  "ds_read_b64_tr_b16 v[82:83], v54 offset:35328\n\t" \
  "v_mfma_f32_16x16x32_bf16 v[172:175], v[76:79], v[96:99], v[172:175]\n\t" \
  "ds_read_b64_tr_b16 v[116:117], v54 offset:38912\n\t" \
  "v_mfma_f32_16x16x32_bf16 v[172:175], v[140:143], v[132:135], v[172:175]\n\t" \
  "ds_read_b64_tr_b16 v[118:119], v54 offset:39424\n\t" \
  "v_mfma_f32_16x16x32_bf16 v[180:183], v[140:143], v[108:111], v[180:183]\n\t" \
  "ds_read_b128 v[88:91], v52 offset:0\n\t" \
  "v_mfma_f32_16x16x32_bf16 v[180:183], v[76:79], v[148:151], v[180:183]\n\t" \
  "ds_read_b128 v[124:127], v53 offset:0\n\t" \
  "s_waitcnt lgkmcnt(7)\n\t" \
  "v_mfma_f32_16x16x32_bf16 v[184:187], v[68:71], v[92:95], v[184:187]\n\t" \
  "ds_read_b64_tr_b16 v[96:97], v55 offset:34816\n\t" \
  "v_mfma_f32_16x16x32_bf16 v[184:187], v[64:67], v[84:87], v[184:187]\n\t" \
  "ds_read_b64_tr_b16 v[98:99], v55 offset:35328\n\t" \
  "v_mfma_f32_16x16x32_bf16 v[192:195], v[64:67], v[100:103], v[192:195]\n\t" \
  "ds_read_b64_tr_b16 v[132:133], v55 offset:38912\n\t" \
  "v_mfma_f32_16x16x32_bf16 v[192:195], v[68:71], v[112:115], v[192:195]\n\t" \
  "ds_read_b64_tr_b16 v[134:135], v55 offset:39424\n\t" \
  "v_mfma_f32_16x16x32_bf16 v[200:203], v[68:71], v[128:131], v[200:203]\n\t" \
  "ds_read_b64_tr_b16 v[108:109], v54 offset:18432\n\t" \
  "v_mfma_f32_16x16x32_bf16 v[200:203], v[64:67], v[120:123], v[200:203]\n\t" \
  "ds_read_b64_tr_b16 v[110:111], v54 offset:18944\n\t" \
  "v_mfma_f32_16x16x32_bf16 v[208:211], v[64:67], v[136:139], v[208:211]\n\t" \
  "ds_read_b64_tr_b16 v[140:141], v54 offset:22528\n\t" \
  "v_mfma_f32_16x16x32_bf16 v[208:211], v[68:71], v[144:147], v[208:211]\n\t" \
  "ds_read_b64_tr_b16 v[142:143], v54 offset:23040\n\t" \
  "v_mfma_f32_16x16x32_bf16 v[188:191], v[104:107], v[92:95], v[188:191]\n\t" \
  "ds_read_b64_tr_b16 v[76:77], v55 offset:18432\n\t" \
  "s_waitcnt lgkmcnt(7)\n\t" \
  "v_mfma_f32_16x16x32_bf16 v[188:191], v[72:75], v[84:87], v[188:191]\n\t" \
  "ds_read_b64_tr_b16 v[78:79], v55 offset:18944\n\t" \
  "v_mfma_f32_16x16x32_bf16 v[196:199], v[72:75], v[100:103], v[196:199]\n\t" \
  "ds_read_b64_tr_b16 v[148:149], v55 offset:22528\n\t" \
  "v_mfma_f32_16x16x32_bf16 v[196:199], v[104:107], v[112:115], v[196:199]\n\t" \
  "ds_read_b64_tr_b16 v[150:151], v55 offset:23040\n\t" \
  "v_mfma_f32_16x16x32_bf16 v[204:207], v[104:107], v[128:131], v[204:207]\n\t" \
  "v_mfma_f32_16x16x32_bf16 v[204:207], v[72:75], v[120:123], v[204:207]\n\t" \
  "v_mfma_f32_16x16x32_bf16 v[212:215], v[72:75], v[136:139], v[212:215]\n\t" \
  "v_mfma_f32_16x16x32_bf16 v[212:215], v[104:107], v[144:147], v[212:215]\n\t" \
  "v_mfma_f32_16x16x32_bf16 v[216:219], v[92:95], v[80:83], v[216:219]\n\t" \
  "v_mfma_f32_16x16x32_bf16 v[216:219], v[84:87], v[116:119], v[216:219]\n\t" \
  "v_mfma_f32_16x16x32_bf16 v[224:227], v[100:103], v[116:119], v[224:227]\n\t" \
  "v_mfma_f32_16x16x32_bf16 v[224:227], v[112:115], v[80:83], v[224:227]\n\t" \
  "v_mfma_f32_16x16x32_bf16 v[232:235], v[128:131], v[80:83], v[232:235]\n\t" \
  "v_mfma_f32_16x16x32_bf16 v[232:235], v[120:123], v[116:119], v[232:235]\n\t" \
  "v_mfma_f32_16x16x32_bf16 v[240:243], v[136:139], v[116:119], v[240:243]\n\t" \
  "v_mfma_f32_16x16x32_bf16 v[240:243], v[144:147], v[80:83], v[240:243]\n\t" \
  "v_mfma_f32_16x16x32_bf16 v[248:251], v[88:91], v[80:83], v[248:251]\n\t" \
  "v_mfma_f32_16x16x32_bf16 v[248:251], v[124:127], v[116:119], v[248:251]\n\t" \
  "v_mfma_f32_16x16x32_bf16 v[252:255], v[88:91], v[96:99], v[252:255]\n\t" \
  "s_waitcnt lgkmcnt(0)\n\t" \
  "v_mfma_f32_16x16x32_bf16 v[252:255], v[124:127], v[132:135], v[252:255]\n\t" \
  "v_mfma_f32_16x16x32_bf16 v[220:223], v[84:87], v[132:135], v[220:223]\n\t" \
  "v_mfma_f32_16x16x32_bf16 v[220:223], v[92:95], v[96:99], v[220:223]\n\t" \
  "s_barrier\n\t" \
  "v_mfma_f32_16x16x32_bf16 v[228:231], v[112:115], v[96:99], v[228:231]\n\t" \
  "ds_read_b64_tr_b16 v[64:65], v58 offset:26624\n\t" \
  "v_mfma_f32_16x16x32_bf16 v[228:231], v[100:103], v[132:135], v[228:231]\n\t" \
  "ds_read_b64_tr_b16 v[66:67], v58 offset:27136\n\t" \
  "v_mfma_f32_16x16x32_bf16 v[236:239], v[120:123], v[132:135], v[236:239]\n\t" \
  "ds_read_b128 v[68:71], v56 offset:10240\n\t" \
  "v_mfma_f32_16x16x32_bf16 v[236:239], v[128:131], v[96:99], v[236:239]\n\t" \
  "ds_read_b64_tr_b16 v[72:73], v58 offset:30720\n\t" \
  "v_mfma_f32_16x16x32_bf16 v[244:247], v[144:147], v[96:99], v[244:247]\n\t" \
  "ds_read_b64_tr_b16 v[74:75], v58 offset:31232\n\t" \
  "v_mfma_f32_16x16x32_bf16 v[244:247], v[136:139], v[132:135], v[244:247]\n\t" \
  "ds_read_b128 v[104:107], v57 offset:10240\n\t" \
  "v_mfma_f32_16x16x32_bf16 v[152:155], v[108:111], v[92:95], v[152:155]\n\t" \
  "ds_read_b128 v[80:83], v57 offset:12288\n\t" \
  "v_mfma_f32_16x16x32_bf16 v[152:155], v[140:143], v[84:87], v[152:155]\n\t" \
  "ds_read_b128 v[116:119], v56 offset:12288\n\t" \
  "v_mfma_f32_16x16x32_bf16 v[160:163], v[140:143], v[100:103], v[160:163]\n\t" \
  "ds_read_b128 v[88:91], v56 offset:14336\n\t" \
  "v_mfma_f32_16x16x32_bf16 v[160:163], v[108:111], v[112:115], v[160:163]\n\t" \
  "ds_read_b128 v[124:127], v57 offset:14336\n\t" \
  "v_mfma_f32_16x16x32_bf16 v[168:171], v[108:111], v[128:131], v[168:171]\n\t" \
  "ds_read_b128 v[96:99], v57 offset:16384\n\t" \
  "v_mfma_f32_16x16x32_bf16 v[168:171], v[140:143], v[120:123], v[168:171]\n\t" \
  "ds_read_b128 v[132:135], v56 offset:16384\n\t" \
  "v_mfma_f32_16x16x32_bf16 v[176:179], v[140:143], v[136:139], v[176:179]\n\t" \
  "ds_read_b64_tr_b16 v[140:141], v59 offset:26624\n\t" \
  "v_mfma_f32_16x16x32_bf16 v[176:179], v[108:111], v[144:147], v[176:179]\n\t" \
  "ds_read_b64_tr_b16 v[142:143], v59 offset:27136\n\t" \
  "v_mfma_f32_16x16x32_bf16 v[156:159], v[76:79], v[92:95], v[156:159]\n\t" \
  "ds_read_b64_tr_b16 v[108:109], v59 offset:30720\n\t" \
  "v_mfma_f32_16x16x32_bf16 v[156:159], v[148:151], v[84:87], v[156:159]\n\t" \
  "ds_read_b64_tr_b16 v[110:111], v59 offset:31232\n\t" \
  "v_mfma_f32_16x16x32_bf16 v[164:167], v[148:151], v[100:103], v[164:167]\n\t" \
  "ds_read_b64_tr_b16 v[92:93], v58 offset:34816\n\t" \
  "v_mfma_f32_16x16x32_bf16 v[164:167], v[76:79], v[112:115], v[164:167]\n\t" \
  "ds_read_b64_tr_b16 v[94:95], v58 offset:35328\n\t" \
  "v_mfma_f32_16x16x32_bf16 v[172:175], v[76:79], v[128:131], v[172:175]\n\t" \
  "ds_read_b64_tr_b16 v[84:85], v58 offset:38912\n\t" \
  "v_mfma_f32_16x16x32_bf16 v[172:175], v[148:151], v[120:123], v[172:175]\n\t" \
  "ds_read_b64_tr_b16 v[86:87], v58 offset:39424\n\t" \
  "v_mfma_f32_16x16x32_bf16 v[180:183], v[148:151], v[136:139], v[180:183]\n\t" \
  "ds_read_b128 v[100:103], v56 offset:0\n\t" \
  "v_mfma_f32_16x16x32_bf16 v[180:183], v[76:79], v[144:147], v[180:183]\n\t" \
  "ds_read_b128 v[112:115], v57 offset:0\n\t" \
  "s_waitcnt lgkmcnt(7)\n\t" \
  "v_mfma_f32_16x16x32_bf16 v[184:187], v[64:67], v[68:71], v[184:187]\n\t" \
  "ds_read_b64_tr_b16 v[128:129], v59 offset:34816\n\t" \
  "v_mfma_f32_16x16x32_bf16 v[184:187], v[72:75], v[104:107], v[184:187]\n\t" \
  "ds_read_b64_tr_b16 v[130:131], v59 offset:35328\n\t" \
  "v_mfma_f32_16x16x32_bf16 v[192:195], v[72:75], v[80:83], v[192:195]\n\t" \
  "ds_read_b64_tr_b16 v[120:121], v59 offset:38912\n\t" \
  "v_mfma_f32_16x16x32_bf16 v[192:195], v[64:67], v[116:119], v[192:195]\n\t" \
  "ds_read_b64_tr_b16 v[122:123], v59 offset:39424\n\t" \
  "v_mfma_f32_16x16x32_bf16 v[200:203], v[64:67], v[88:91], v[200:203]\n\t" \
  "ds_read_b64_tr_b16 v[136:137], v58 offset:18432\n\t" \
  "v_mfma_f32_16x16x32_bf16 v[200:203], v[72:75], v[124:127], v[200:203]\n\t" \
  "ds_read_b64_tr_b16 v[138:139], v58 offset:18944\n\t" \
  "v_mfma_f32_16x16x32_bf16 v[208:211], v[72:75], v[96:99], v[208:211]\n\t" \
  "ds_read_b64_tr_b16 v[148:149], v58 offset:22528\n\t" \
  "v_mfma_f32_16x16x32_bf16 v[208:211], v[64:67], v[132:135], v[208:211]\n\t" \
  "ds_read_b64_tr_b16 v[150:151], v58 offset:23040\n\t" \
  "v_mfma_f32_16x16x32_bf16 v[188:191], v[140:143], v[68:71], v[188:191]\n\t" \
  "ds_read_b64_tr_b16 v[76:77], v59 offset:18432\n\t" \
  "s_waitcnt lgkmcnt(7)\n\t" \
  "v_mfma_f32_16x16x32_bf16 v[188:191], v[108:111], v[104:107], v[188:191]\n\t" \
  "ds_read_b64_tr_b16 v[78:79], v59 offset:18944\n\t" \
  "v_mfma_f32_16x16x32_bf16 v[196:199], v[108:111], v[80:83], v[196:199]\n\t" \
  "ds_read_b64_tr_b16 v[144:145], v59 offset:22528\n\t" \
  "v_mfma_f32_16x16x32_bf16 v[196:199], v[140:143], v[116:119], v[196:199]\n\t" \
  "ds_read_b64_tr_b16 v[146:147], v59 offset:23040\n\t" \
  "v_mfma_f32_16x16x32_bf16 v[204:207], v[140:143], v[88:91], v[204:207]\n\t" \
  "v_mfma_f32_16x16x32_bf16 v[204:207], v[108:111], v[124:127], v[204:207]\n\t" \
  "v_mfma_f32_16x16x32_bf16 v[212:215], v[108:111], v[96:99], v[212:215]\n\t" \
  "v_mfma_f32_16x16x32_bf16 v[212:215], v[140:143], v[132:135], v[212:215]\n\t" \
  "v_mfma_f32_16x16x32_bf16 v[216:219], v[68:71], v[92:95], v[216:219]\n\t" \
  "v_mfma_f32_16x16x32_bf16 v[216:219], v[104:107], v[84:87], v[216:219]\n\t" \
  "v_mfma_f32_16x16x32_bf16 v[224:227], v[80:83], v[84:87], v[224:227]\n\t" \
  "v_mfma_f32_16x16x32_bf16 v[224:227], v[116:119], v[92:95], v[224:227]\n\t" \
  "v_mfma_f32_16x16x32_bf16 v[232:235], v[88:91], v[92:95], v[232:235]\n\t" \
  "v_mfma_f32_16x16x32_bf16 v[232:235], v[124:127], v[84:87], v[232:235]\n\t" \
  "v_mfma_f32_16x16x32_bf16 v[240:243], v[96:99], v[84:87], v[240:243]\n\t" \
  "v_mfma_f32_16x16x32_bf16 v[240:243], v[132:135], v[92:95], v[240:243]\n\t" \
  "v_mfma_f32_16x16x32_bf16 v[248:251], v[100:103], v[92:95], v[248:251]\n\t" \
  "v_mfma_f32_16x16x32_bf16 v[248:251], v[112:115], v[84:87], v[248:251]\n\t" \
  "v_mfma_f32_16x16x32_bf16 v[252:255], v[100:103], v[128:131], v[252:255]\n\t" \
  "s_waitcnt lgkmcnt(0)\n\t" \
  "v_mfma_f32_16x16x32_bf16 v[252:255], v[112:115], v[120:123], v[252:255]\n\t" \
  "v_mfma_f32_16x16x32_bf16 v[220:223], v[104:107], v[120:123], v[220:223]\n\t" \
  "v_mfma_f32_16x16x32_bf16 v[220:223], v[68:71], v[128:131], v[220:223]\n\t" \
  "s_barrier\n\t" \
  "v_mfma_f32_16x16x32_bf16 v[228:231], v[116:119], v[128:131], v[228:231]\n\t" \
  "ds_read_b64_tr_b16 v[72:73], v62 offset:26624\n\t" \
  "v_mfma_f32_16x16x32_bf16 v[228:231], v[80:83], v[120:123], v[228:231]\n\t" \
  "ds_read_b64_tr_b16 v[74:75], v62 offset:27136\n\t" \
  "v_mfma_f32_16x16x32_bf16 v[236:239], v[124:127], v[120:123], v[236:239]\n\t" \
  "ds_read_b128 v[64:67], v60 offset:10240\n\t" \
  "v_mfma_f32_16x16x32_bf16 v[236:239], v[88:91], v[128:131], v[236:239]\n\t" \
  "ds_read_b64_tr_b16 v[108:109], v62 offset:30720\n\t" \
  "v_mfma_f32_16x16x32_bf16 v[244:247], v[132:135], v[128:131], v[244:247]\n\t" \
  "ds_read_b64_tr_b16 v[110:111], v62 offset:31232\n\t" \
  "v_mfma_f32_16x16x32_bf16 v[244:247], v[96:99], v[120:123], v[244:247]\n\t" \
  "ds_read_b128 v[140:143], v61 offset:10240\n\t" \
  "v_mfma_f32_16x16x32_bf16 v[152:155], v[136:139], v[68:71], v[152:155]\n\t" \
  "ds_read_b128 v[92:95], v61 offset:12288\n\t" \
  "v_mfma_f32_16x16x32_bf16 v[152:155], v[148:151], v[104:107], v[152:155]\n\t" \
  "ds_read_b128 v[84:87], v60 offset:12288\n\t" \
  "v_mfma_f32_16x16x32_bf16 v[160:163], v[148:151], v[80:83], v[160:163]\n\t" \
  "ds_read_b128 v[100:103], v60 offset:14336\n\t" \
  "v_mfma_f32_16x16x32_bf16 v[160:163], v[136:139], v[116:119], v[160:163]\n\t" \
  "ds_read_b128 v[112:115], v61 offset:14336\n\t" \
  "v_mfma_f32_16x16x32_bf16 v[168:171], v[136:139], v[88:91], v[168:171]\n\t" \
  "ds_read_b128 v[128:131], v61 offset:16384\n\t" \
  "v_mfma_f32_16x16x32_bf16 v[168:171], v[148:151], v[124:127], v[168:171]\n\t" \
  "ds_read_b128 v[120:123], v60 offset:16384\n\t" \
  "v_mfma_f32_16x16x32_bf16 v[176:179], v[148:151], v[96:99], v[176:179]\n\t" \
  "ds_read_b64_tr_b16 v[148:149], v63 offset:26624\n\t" \
  "v_mfma_f32_16x16x32_bf16 v[176:179], v[136:139], v[132:135], v[176:179]\n\t" \
  "ds_read_b64_tr_b16 v[150:151], v63 offset:27136\n\t" \
  "v_mfma_f32_16x16x32_bf16 v[156:159], v[76:79], v[68:71], v[156:159]\n\t" \
  "ds_read_b64_tr_b16 v[136:137], v63 offset:30720\n\t" \
  "v_mfma_f32_16x16x32_bf16 v[156:159], v[144:147], v[104:107], v[156:159]\n\t" \
  "ds_read_b64_tr_b16 v[138:139], v63 offset:31232\n\t" \
  "v_mfma_f32_16x16x32_bf16 v[164:167], v[144:147], v[80:83], v[164:167]\n\t" \
  "ds_read_b64_tr_b16 v[68:69], v62 offset:34816\n\t" \
  "v_mfma_f32_16x16x32_bf16 v[164:167], v[76:79], v[116:119], v[164:167]\n\t" \
  "ds_read_b64_tr_b16 v[70:71], v62 offset:35328\n\t" \
  "v_mfma_f32_16x16x32_bf16 v[172:175], v[76:79], v[88:91], v[172:175]\n\t" \
  "ds_read_b64_tr_b16 v[104:105], v62 offset:38912\n\t" \
  "v_mfma_f32_16x16x32_bf16 v[172:175], v[144:147], v[124:127], v[172:175]\n\t" \
  "ds_read_b64_tr_b16 v[106:107], v62 offset:39424\n\t" \
  "v_mfma_f32_16x16x32_bf16 v[180:183], v[144:147], v[96:99], v[180:183]\n\t" \
  "ds_read_b128 v[80:83], v60 offset:0\n\t" \
  "v_mfma_f32_16x16x32_bf16 v[180:183], v[76:79], v[132:135], v[180:183]\n\t" \
  "ds_read_b128 v[116:119], v61 offset:0\n\t" \
  "s_waitcnt lgkmcnt(7)\n\t" \
  "v_mfma_f32_16x16x32_bf16 v[184:187], v[72:75], v[64:67], v[184:187]\n\t" \
  "ds_read_b64_tr_b16 v[88:89], v63 offset:34816\n\t" \
  "v_mfma_f32_16x16x32_bf16 v[184:187], v[108:111], v[140:143], v[184:187]\n\t" \
  "ds_read_b64_tr_b16 v[90:91], v63 offset:35328\n\t" \
  "v_mfma_f32_16x16x32_bf16 v[192:195], v[108:111], v[92:95], v[192:195]\n\t" \
  "ds_read_b64_tr_b16 v[124:125], v63 offset:38912\n\t" \
  "v_mfma_f32_16x16x32_bf16 v[192:195], v[72:75], v[84:87], v[192:195]\n\t" \
  "ds_read_b64_tr_b16 v[126:127], v63 offset:39424\n\t" \
  "v_mfma_f32_16x16x32_bf16 v[200:203], v[72:75], v[100:103], v[200:203]\n\t" \
  "ds_read_b64_tr_b16 v[96:97], v62 offset:18432\n\t" \
  "v_mfma_f32_16x16x32_bf16 v[200:203], v[108:111], v[112:115], v[200:203]\n\t" \
  "ds_read_b64_tr_b16 v[98:99], v62 offset:18944\n\t" \
  "v_mfma_f32_16x16x32_bf16 v[208:211], v[108:111], v[128:131], v[208:211]\n\t" \
  "ds_read_b64_tr_b16 v[144:145], v62 offset:22528\n\t" \
  "v_mfma_f32_16x16x32_bf16 v[208:211], v[72:75], v[120:123], v[208:211]\n\t" \
  "ds_read_b64_tr_b16 v[146:147], v62 offset:23040\n\t" \
  "v_mfma_f32_16x16x32_bf16 v[188:191], v[148:151], v[64:67], v[188:191]\n\t" \
  "ds_read_b64_tr_b16 v[76:77], v63 offset:18432\n\t" \
  "s_waitcnt lgkmcnt(7)\n\t" \
  "v_mfma_f32_16x16x32_bf16 v[188:191], v[136:139], v[140:143], v[188:191]\n\t" \
  "ds_read_b64_tr_b16 v[78:79], v63 offset:18944\n\t" \
  "v_mfma_f32_16x16x32_bf16 v[196:199], v[136:139], v[92:95], v[196:199]\n\t" \
  "ds_read_b64_tr_b16 v[132:133], v63 offset:22528\n\t" \
  "v_mfma_f32_16x16x32_bf16 v[196:199], v[148:151], v[84:87], v[196:199]\n\t" \
  "ds_read_b64_tr_b16 v[134:135], v63 offset:23040\n\t" \
  "v_mfma_f32_16x16x32_bf16 v[204:207], v[148:151], v[100:103], v[204:207]\n\t" \
  "v_mfma_f32_16x16x32_bf16 v[204:207], v[136:139], v[112:115], v[204:207]\n\t" \
  "v_mfma_f32_16x16x32_bf16 v[212:215], v[136:139], v[128:131], v[212:215]\n\t" \
  "v_mfma_f32_16x16x32_bf16 v[212:215], v[148:151], v[120:123], v[212:215]\n\t" \
  "v_mfma_f32_16x16x32_bf16 v[216:219], v[64:67], v[68:71], v[216:219]\n\t" \
  "v_mfma_f32_16x16x32_bf16 v[216:219], v[140:143], v[104:107], v[216:219]\n\t" \
  "v_mfma_f32_16x16x32_bf16 v[224:227], v[92:95], v[104:107], v[224:227]\n\t" \
  "v_mfma_f32_16x16x32_bf16 v[224:227], v[84:87], v[68:71], v[224:227]\n\t" \
  "v_mfma_f32_16x16x32_bf16 v[232:235], v[100:103], v[68:71], v[232:235]\n\t" \
  "v_mfma_f32_16x16x32_bf16 v[232:235], v[112:115], v[104:107], v[232:235]\n\t" \
  "v_mfma_f32_16x16x32_bf16 v[240:243], v[128:131], v[104:107], v[240:243]\n\t" \
  "v_mfma_f32_16x16x32_bf16 v[240:243], v[120:123], v[68:71], v[240:243]\n\t" \
  "v_mfma_f32_16x16x32_bf16 v[248:251], v[80:83], v[68:71], v[248:251]\n\t" \
  "v_mfma_f32_16x16x32_bf16 v[248:251], v[116:119], v[104:107], v[248:251]\n\t" \
  "v_mfma_f32_16x16x32_bf16 v[252:255], v[80:83], v[88:91], v[252:255]\n\t" \
  "s_waitcnt lgkmcnt(0)\n\t" \
  "v_mfma_f32_16x16x32_bf16 v[252:255], v[116:119], v[124:127], v[252:255]\n\t" \
  "v_mfma_f32_16x16x32_bf16 v[220:223], v[140:143], v[124:127], v[220:223]\n\t" \
  "v_mfma_f32_16x16x32_bf16 v[220:223], v[64:67], v[88:91], v[220:223]\n\t" \
  "s_barrier\n\t" \
  "v_mfma_f32_16x16x32_bf16 v[228:231], v[84:87], v[88:91], v[228:231]\n\t" \
  "ds_read_b64_tr_b16 v[108:109], v54 offset:26624\n\t" \
  "v_mfma_f32_16x16x32_bf16 v[228:231], v[92:95], v[124:127], v[228:231]\n\t" \
  "ds_read_b64_tr_b16 v[110:111], v54 offset:27136\n\t" \
  "v_mfma_f32_16x16x32_bf16 v[236:239], v[112:115], v[124:127], v[236:239]\n\t" \
  "ds_read_b128 v[72:75], v52 offset:10240\n\t" \
  "v_mfma_f32_16x16x32_bf16 v[236:239], v[100:103], v[88:91], v[236:239]\n\t" \
  "ds_read_b64_tr_b16 v[136:137], v54 offset:30720\n\t" \
  "v_mfma_f32_16x16x32_bf16 v[244:247], v[120:123], v[88:91], v[244:247]\n\t" \
  "ds_read_b64_tr_b16 v[138:139], v54 offset:31232\n\t" \
  "v_mfma_f32_16x16x32_bf16 v[244:247], v[128:131], v[124:127], v[244:247]\n\t" \
  "ds_read_b128 v[148:151], v53 offset:10240\n\t" \
  "v_mfma_f32_16x16x32_bf16 v[152:155], v[96:99], v[64:67], v[152:155]\n\t" \
  "ds_read_b128 v[68:71], v53 offset:12288\n\t" \
  "v_mfma_f32_16x16x32_bf16 v[152:155], v[144:147], v[140:143], v[152:155]\n\t" \
  "ds_read_b128 v[104:107], v52 offset:12288\n\t" \
  "v_mfma_f32_16x16x32_bf16 v[160:163], v[144:147], v[92:95], v[160:163]\n\t" \
  "ds_read_b128 v[80:83], v52 offset:14336\n\t" \
  "v_mfma_f32_16x16x32_bf16 v[160:163], v[96:99], v[84:87], v[160:163]\n\t" \
  "ds_read_b128 v[116:119], v53 offset:14336\n\t" \
  "v_mfma_f32_16x16x32_bf16 v[168:171], v[96:99], v[100:103], v[168:171]\n\t" \
  "ds_read_b128 v[88:91], v53 offset:16384\n\t" \
  "v_mfma_f32_16x16x32_bf16 v[168:171], v[144:147], v[112:115], v[168:171]\n\t" \
  "ds_read_b128 v[124:127], v52 offset:16384\n\t" \
  "v_mfma_f32_16x16x32_bf16 v[176:179], v[144:147], v[128:131], v[176:179]\n\t" \
  "ds_read_b64_tr_b16 v[144:145], v55 offset:26624\n\t" \
  "v_mfma_f32_16x16x32_bf16 v[176:179], v[96:99], v[120:123], v[176:179]\n\t" \
  "ds_read_b64_tr_b16 v[146:147], v55 offset:27136\n\t" \
  "v_mfma_f32_16x16x32_bf16 v[156:159], v[76:79], v[64:67], v[156:159]\n\t" \
  "ds_read_b64_tr_b16 v[96:97], v55 offset:30720\n\t" \
  "v_mfma_f32_16x16x32_bf16 v[156:159], v[132:135], v[140:143], v[156:159]\n\t" \
  "ds_read_b64_tr_b16 v[98:99], v55 offset:31232\n\t" \
  "v_mfma_f32_16x16x32_bf16 v[164:167], v[132:135], v[92:95], v[164:167]\n\t" \
  "ds_read_b64_tr_b16 v[64:65], v54 offset:34816\n\t" \
  "v_mfma_f32_16x16x32_bf16 v[164:167], v[76:79], v[84:87], v[164:167]\n\t" \
  "ds_read_b64_tr_b16 v[66:67], v54 offset:35328\n\t" \
  "v_mfma_f32_16x16x32_bf16 v[172:175], v[76:79], v[100:103], v[172:175]\n\t" \
  "ds_read_b64_tr_b16 v[140:141], v54 offset:38912\n\t" \
  "v_mfma_f32_16x16x32_bf16 v[172:175], v[132:135], v[112:115], v[172:175]\n\t" \
  "ds_read_b64_tr_b16 v[142:143], v54 offset:39424\n\t" \
  "v_mfma_f32_16x16x32_bf16 v[180:183], v[132:135], v[128:131], v[180:183]\n\t" \
  "ds_read_b128 v[92:95], v52 offset:0\n\t" \
  "v_mfma_f32_16x16x32_bf16 v[180:183], v[76:79], v[120:123], v[180:183]\n\t" \
  "ds_read_b128 v[84:87], v53 offset:0\n\t" \
  "s_waitcnt lgkmcnt(7)\n\t" \
  "v_mfma_f32_16x16x32_bf16 v[184:187], v[108:111], v[72:75], v[184:187]\n\t" \
  "ds_read_b64_tr_b16 v[100:101], v55 offset:34816\n\t" \
  "v_mfma_f32_16x16x32_bf16 v[184:187], v[136:139], v[148:151], v[184:187]\n\t" \
  "ds_read_b64_tr_b16 v[102:103], v55 offset:35328\n\t" \
  "v_mfma_f32_16x16x32_bf16 v[192:195], v[136:139], v[68:71], v[192:195]\n\t" \
  "ds_read_b64_tr_b16 v[112:113], v55 offset:38912\n\t" \
  "v_mfma_f32_16x16x32_bf16 v[192:195], v[108:111], v[104:107], v[192:195]\n\t" \
  "ds_read_b64_tr_b16 v[114:115], v55 offset:39424\n\t" \
  "v_mfma_f32_16x16x32_bf16 v[200:203], v[108:111], v[80:83], v[200:203]\n\t" \
  "ds_read_b64_tr_b16 v[128:129], v54 offset:18432\n\t" \
  "v_mfma_f32_16x16x32_bf16 v[200:203], v[136:139], v[116:119], v[200:203]\n\t" \
  "ds_read_b64_tr_b16 v[130:131], v54 offset:18944\n\t" \
  "v_mfma_f32_16x16x32_bf16 v[208:211], v[136:139], v[88:91], v[208:211]\n\t" \
  "ds_read_b64_tr_b16 v[132:133], v54 offset:22528\n\t" \
  "v_mfma_f32_16x16x32_bf16 v[208:211], v[108:111], v[124:127], v[208:211]\n\t" \
  "ds_read_b64_tr_b16 v[134:135], v54 offset:23040\n\t" \
  "v_mfma_f32_16x16x32_bf16 v[188:191], v[144:147], v[72:75], v[188:191]\n\t" \
  "ds_read_b64_tr_b16 v[76:77], v55 offset:18432\n\t" \
  "s_waitcnt lgkmcnt(7)\n\t" \
  "v_mfma_f32_16x16x32_bf16 v[188:191], v[96:99], v[148:151], v[188:191]\n\t" \
  "ds_read_b64_tr_b16 v[78:79], v55 offset:18944\n\t" \
  "v_mfma_f32_16x16x32_bf16 v[196:199], v[96:99], v[68:71], v[196:199]\n\t" \
  "ds_read_b64_tr_b16 v[120:121], v55 offset:22528\n\t" \
  "v_mfma_f32_16x16x32_bf16 v[196:199], v[144:147], v[104:107], v[196:199]\n\t" \
  "ds_read_b64_tr_b16 v[122:123], v55 offset:23040\n\t" \
  "v_mfma_f32_16x16x32_bf16 v[204:207], v[144:147], v[80:83], v[204:207]\n\t" \
  "v_mfma_f32_16x16x32_bf16 v[204:207], v[96:99], v[116:119], v[204:207]\n\t" \
  "v_mfma_f32_16x16x32_bf16 v[212:215], v[96:99], v[88:91], v[212:215]\n\t" \
  "v_mfma_f32_16x16x32_bf16 v[212:215], v[144:147], v[124:127], v[212:215]\n\t" \
  "v_mfma_f32_16x16x32_bf16 v[216:219], v[72:75], v[64:67], v[216:219]\n\t" \
  "v_mfma_f32_16x16x32_bf16 v[216:219], v[148:151], v[140:143], v[216:219]\n\t" \
  "v_mfma_f32_16x16x32_bf16 v[224:227], v[68:71], v[140:143], v[224:227]\n\t" \
  "v_mfma_f32_16x16x32_bf16 v[224:227], v[104:107], v[64:67], v[224:227]\n\t" \
  "v_mfma_f32_16x16x32_bf16 v[232:235], v[80:83], v[64:67], v[232:235]\n\t" \
  "v_mfma_f32_16x16x32_bf16 v[232:235], v[116:119], v[140:143], v[232:235]\n\t" \
  "v_mfma_f32_16x16x32_bf16 v[240:243], v[88:91], v[140:143], v[240:243]\n\t" \
  "v_mfma_f32_16x16x32_bf16 v[240:243], v[124:127], v[64:67], v[240:243]\n\t" \
  "v_mfma_f32_16x16x32_bf16 v[248:251], v[92:95], v[64:67], v[248:251]\n\t" \
  "v_mfma_f32_16x16x32_bf16 v[248:251], v[84:87], v[140:143], v[248:251]\n\t" \
  "v_mfma_f32_16x16x32_bf16 v[252:255], v[92:95], v[100:103], v[252:255]\n\t" \
  "s_waitcnt lgkmcnt(0)\n\t" \
  "v_mfma_f32_16x16x32_bf16 v[252:255], v[84:87], v[112:115], v[252:255]\n\t" \
  "v_mfma_f32_16x16x32_bf16 v[220:223], v[148:151], v[112:115], v[220:223]\n\t" \
  "v_mfma_f32_16x16x32_bf16 v[220:223], v[72:75], v[100:103], v[220:223]\n\t" \
  "s_barrier\n\t" \
  "v_mfma_f32_16x16x32_bf16 v[228:231], v[104:107], v[100:103], v[228:231]\n\t" \
  "ds_read_b64_tr_b16 v[96:97], v58 offset:26624\n\t" \
  "v_mfma_f32_16x16x32_bf16 v[228:231], v[68:71], v[112:115], v[228:231]\n\t" \
  "ds_read_b64_tr_b16 v[98:99], v58 offset:27136\n\t" \
  "v_mfma_f32_16x16x32_bf16 v[236:239], v[116:119], v[112:115], v[236:239]\n\t" \
  "ds_read_b128 v[136:139], v56 offset:10240\n\t" \
  "v_mfma_f32_16x16x32_bf16 v[236:239], v[80:83], v[100:103], v[236:239]\n\t" \
  "ds_read_b64_tr_b16 v[64:65], v58 offset:30720\n\t" \
  "v_mfma_f32_16x16x32_bf16 v[244:247], v[124:127], v[100:103], v[244:247]\n\t" \
  "ds_read_b64_tr_b16 v[66:67], v58 offset:31232\n\t" \
  "v_mfma_f32_16x16x32_bf16 v[244:247], v[88:91], v[112:115], v[244:247]\n\t" \
  "ds_read_b128 v[108:111], v57 offset:10240\n\t" \
  "v_mfma_f32_16x16x32_bf16 v[152:155], v[128:131], v[72:75], v[152:155]\n\t" \
  "ds_read_b128 v[144:147], v57 offset:12288\n\t" \
  "v_mfma_f32_16x16x32_bf16 v[152:155], v[132:135], v[148:151], v[152:155]\n\t" \
  "ds_read_b128 v[140:143], v56 offset:12288\n\t" \
  "v_mfma_f32_16x16x32_bf16 v[160:163], v[132:135], v[68:71], v[160:163]\n\t" \
  "ds_read_b128 v[112:115], v56 offset:14336\n\t" \
  "v_mfma_f32_16x16x32_bf16 v[160:163], v[128:131], v[104:107], v[160:163]\n\t" \
  "ds_read_b64_tr_b16 v[92:93], v59 offset:26624\n\t" \
  "v_mfma_f32_16x16x32_bf16 v[168:171], v[128:131], v[80:83], v[168:171]\n\t" \
  "ds_read_b64_tr_b16 v[94:95], v59 offset:27136\n\t" \
  "v_mfma_f32_16x16x32_bf16 v[168:171], v[132:135], v[116:119], v[168:171]\n\t" \
  "ds_read_b64_tr_b16 v[84:85], v59 offset:30720\n\t" \
  "v_mfma_f32_16x16x32_bf16 v[176:179], v[132:135], v[88:91], v[176:179]\n\t" \
  "ds_read_b128 v[132:135], v57 offset:14336\n\t" \
  "v_mfma_f32_16x16x32_bf16 v[176:179], v[128:131], v[124:127], v[176:179]\n\t" \
  "ds_read_b128 v[128:131], v57 offset:16384\n\t" \
  "v_mfma_f32_16x16x32_bf16 v[156:159], v[76:79], v[72:75], v[156:159]\n\t" \
  "ds_read_b64_tr_b16 v[86:87], v59 offset:31232\n\t" \
  "v_mfma_f32_16x16x32_bf16 v[156:159], v[120:123], v[148:151], v[156:159]\n\t" \
  "ds_read_b128 v[148:151], v56 offset:16384\n\t" \
  "v_mfma_f32_16x16x32_bf16 v[164:167], v[120:123], v[68:71], v[164:167]\n\t" \
  "ds_read_b64_tr_b16 v[100:101], v58 offset:34816\n\t" \
  "v_mfma_f32_16x16x32_bf16 v[164:167], v[76:79], v[104:107], v[164:167]\n\t" \
  "ds_read_b64_tr_b16 v[102:103], v58 offset:35328\n\t" \
  "v_mfma_f32_16x16x32_bf16 v[172:175], v[76:79], v[80:83], v[172:175]\n\t" \
  "ds_read_b64_tr_b16 v[72:73], v58 offset:38912\n\t" \
  "v_mfma_f32_16x16x32_bf16 v[172:175], v[120:123], v[116:119], v[172:175]\n\t" \
  "ds_read_b64_tr_b16 v[74:75], v58 offset:39424\n\t" \
  "v_mfma_f32_16x16x32_bf16 v[180:183], v[120:123], v[88:91], v[180:183]\n\t" \
  "ds_read_b128 v[68:71], v56 offset:0\n\t" \
  "v_mfma_f32_16x16x32_bf16 v[180:183], v[76:79], v[124:127], v[180:183]\n\t" \
  "ds_read_b128 v[104:107], v57 offset:0\n\t" \
  "s_waitcnt lgkmcnt(7)\n\t" \
  "v_mfma_f32_16x16x32_bf16 v[184:187], v[96:99], v[136:139], v[184:187]\n\t" \
  "ds_read_b64_tr_b16 v[80:81], v59 offset:34816\n\t" \
  "v_mfma_f32_16x16x32_bf16 v[184:187], v[64:67], v[108:111], v[184:187]\n\t" \
  "ds_read_b64_tr_b16 v[82:83], v59 offset:35328\n\t" \
  "v_mfma_f32_16x16x32_bf16 v[192:195], v[64:67], v[144:147], v[192:195]\n\t" \
  "ds_read_b64_tr_b16 v[88:89], v59 offset:38912\n\t" \
  "v_mfma_f32_16x16x32_bf16 v[192:195], v[96:99], v[140:143], v[192:195]\n\t" \
  "ds_read_b64_tr_b16 v[90:91], v59 offset:39424\n\t" \
  "v_mfma_f32_16x16x32_bf16 v[200:203], v[96:99], v[112:115], v[200:203]\n\t" \
  "ds_read_b64_tr_b16 v[116:117], v58 offset:18432\n\t" \
  "v_mfma_f32_16x16x32_bf16 v[200:203], v[64:67], v[132:135], v[200:203]\n\t" \
  "ds_read_b64_tr_b16 v[118:119], v58 offset:18944\n\t" \
  "v_mfma_f32_16x16x32_bf16 v[208:211], v[64:67], v[128:131], v[208:211]\n\t" \
  "ds_read_b64_tr_b16 v[120:121], v58 offset:22528\n\t" \
  "s_waitcnt lgkmcnt(7)\n\t" \
  "v_mfma_f32_16x16x32_bf16 v[208:211], v[96:99], v[148:151], v[208:211]\n\t" \
  "ds_read_b64_tr_b16 v[122:123], v58 offset:23040\n\t" \
  "v_mfma_f32_16x16x32_bf16 v[188:191], v[92:95], v[136:139], v[188:191]\n\t" \
  "ds_read_b64_tr_b16 v[124:125], v59 offset:18432\n\t" \
  "v_mfma_f32_16x16x32_bf16 v[188:191], v[84:87], v[108:111], v[188:191]\n\t" \
  "ds_read_b64_tr_b16 v[126:127], v59 offset:18944\n\t" \
  "v_mfma_f32_16x16x32_bf16 v[196:199], v[84:87], v[144:147], v[196:199]\n\t" \
  "ds_read_b64_tr_b16 v[76:77], v59 offset:22528\n\t" \
  "v_mfma_f32_16x16x32_bf16 v[196:199], v[92:95], v[140:143], v[196:199]\n\t" \
  "ds_read_b64_tr_b16 v[78:79], v59 offset:23040\n\t" \
  "v_mfma_f32_16x16x32_bf16 v[204:207], v[92:95], v[112:115], v[204:207]\n\t" \
  "v_mfma_f32_16x16x32_bf16 v[204:207], v[84:87], v[132:135], v[204:207]\n\t" \
  "v_mfma_f32_16x16x32_bf16 v[212:215], v[84:87], v[128:131], v[212:215]\n\t" \
  "v_mfma_f32_16x16x32_bf16 v[212:215], v[92:95], v[148:151], v[212:215]\n\t" \
  "v_mfma_f32_16x16x32_bf16 v[216:219], v[136:139], v[100:103], v[216:219]\n\t" \
  "v_mfma_f32_16x16x32_bf16 v[216:219], v[108:111], v[72:75], v[216:219]\n\t" \
  "v_mfma_f32_16x16x32_bf16 v[224:227], v[144:147], v[72:75], v[224:227]\n\t" \
  "v_and_b32 v14, -16, v32\n\t" \
  "v_mfma_f32_16x16x32_bf16 v[224:227], v[140:143], v[100:103], v[224:227]\n\t" \
  "v_mfma_f32_16x16x32_bf16 v[232:235], v[112:115], v[100:103], v[232:235]\n\t" \
  "ds_read_b128 v[10:13], v14 offset:128\n\t" \
  "v_mfma_f32_16x16x32_bf16 v[232:235], v[132:135], v[72:75], v[232:235]\n\t" \
  "v_mfma_f32_16x16x32_bf16 v[240:243], v[128:131], v[72:75], v[240:243]\n\t" \
  "v_mfma_f32_16x16x32_bf16 v[240:243], v[148:151], v[100:103], v[240:243]\n\t" \
  "v_mfma_f32_16x16x32_bf16 v[248:251], v[68:71], v[100:103], v[248:251]\n\t" \
  "v_mfma_f32_16x16x32_bf16 v[248:251], v[104:107], v[72:75], v[248:251]\n\t" \
  "s_waitcnt lgkmcnt(1)\n\t" \
  "v_mfma_f32_16x16x32_bf16 v[252:255], v[68:71], v[80:83], v[252:255]\n\t" \
  "s_waitcnt lgkmcnt(0)\n\t" \
  "v_and_b32 v10, v10, v11\n\t" \
  "v_and_b32 v12, v12, v13\n\t" \
  "v_mfma_f32_16x16x32_bf16 v[252:255], v[104:107], v[88:91], v[252:255]\n\t" \
  "v_and_b32 v10, v10, v12\n\t" \
  "v_mfma_f32_16x16x32_bf16 v[220:223], v[108:111], v[88:91], v[220:223]\n\t" \
  "v_readfirstlane_b32 vcc_lo, v10\n\t" \
  "v_mfma_f32_16x16x32_bf16 v[220:223], v[136:139], v[80:83], v[220:223]\n\t" \
  "ds_write_b32 v32, v0 offset:144\n\t" \
  "s_cmp_eq_u32 vcc_lo, 0\n\t" \
  "s_cbranch_scc1 15f\n\t" \
  "14:\n\t" \
  "v_mfma_f32_16x16x32_bf16 v[228:231], v[140:143], v[80:83], v[228:231]\n\t" \
  "ds_read_b64_tr_b16 v[64:65], v62 offset:26624\n\t" \
  "v_mfma_f32_16x16x32_bf16 v[228:231], v[144:147], v[88:91], v[228:231]\n\t" \
  "ds_read_b64_tr_b16 v[66:67], v62 offset:27136\n\t" \
  "v_mfma_f32_16x16x32_bf16 v[236:239], v[132:135], v[88:91], v[236:239]\n\t" \
  "ds_read_b128 v[96:99], v60 offset:10240\n\t" \
  "v_mfma_f32_16x16x32_bf16 v[236:239], v[112:115], v[80:83], v[236:239]\n\t" \
  "ds_read_b128 v[84:87], v60 offset:12288\n\t" \
  "v_mfma_f32_16x16x32_bf16 v[244:247], v[148:151], v[80:83], v[244:247]\n\t" \
  "ds_read_b128 v[92:95], v60 offset:14336\n\t" \
  "v_mfma_f32_16x16x32_bf16 v[244:247], v[128:131], v[88:91], v[244:247]\n\t" \
  "ds_read_b128 v[100:103], v60 offset:16384\n\t" \
  "v_mfma_f32_16x16x32_bf16 v[152:155], v[116:119], v[136:139], v[152:155]\n\t" \
  "ds_read_b64_tr_b16 v[72:73], v63 offset:26624\n\t" \
  "v_mfma_f32_16x16x32_bf16 v[152:155], v[120:123], v[108:111], v[152:155]\n\t" \
  "ds_read_b64_tr_b16 v[74:75], v63 offset:27136\n\t" \
  "v_mfma_f32_16x16x32_bf16 v[160:163], v[120:123], v[144:147], v[160:163]\n\t" \
  "ds_read_b64_tr_b16 v[68:69], v62 offset:34816\n\t" \
  "v_mfma_f32_16x16x32_bf16 v[160:163], v[116:119], v[140:143], v[160:163]\n\t" \
  "ds_read_b64_tr_b16 v[70:71], v62 offset:35328\n\t" \
  "v_mfma_f32_16x16x32_bf16 v[168:171], v[116:119], v[112:115], v[168:171]\n\t" \
  "ds_read_b128 v[104:107], v60 offset:0\n\t" \
  "v_mfma_f32_16x16x32_bf16 v[168:171], v[120:123], v[132:135], v[168:171]\n\t" \
  "ds_read_b64_tr_b16 v[80:81], v63 offset:34816\n\t" \
  "v_mfma_f32_16x16x32_bf16 v[176:179], v[120:123], v[128:131], v[176:179]\n\t" \
  "ds_read_b64_tr_b16 v[82:83], v63 offset:35328\n\t" \
  "v_mfma_f32_16x16x32_bf16 v[176:179], v[116:119], v[148:151], v[176:179]\n\t" \
  "ds_read_b64_tr_b16 v[88:89], v62 offset:18432\n\t" \
  "v_mfma_f32_16x16x32_bf16 v[156:159], v[124:127], v[136:139], v[156:159]\n\t" \
  "ds_read_b64_tr_b16 v[90:91], v62 offset:18944\n\t" \
  "v_mfma_f32_16x16x32_bf16 v[156:159], v[76:79], v[108:111], v[156:159]\n\t" \
  "ds_read_b64_tr_b16 v[120:121], v62 offset:30720\n\t" \
  "v_mfma_f32_16x16x32_bf16 v[164:167], v[76:79], v[144:147], v[164:167]\n\t" \
  "ds_read_b64_tr_b16 v[122:123], v62 offset:31232\n\t" \
  "v_mfma_f32_16x16x32_bf16 v[164:167], v[124:127], v[140:143], v[164:167]\n\t" \
  "ds_read_b128 v[116:119], v61 offset:10240\n\t" \
  "v_mfma_f32_16x16x32_bf16 v[172:175], v[124:127], v[112:115], v[172:175]\n\t" \
  "ds_read_b128 v[136:139], v61 offset:12288\n\t" \
  "v_mfma_f32_16x16x32_bf16 v[172:175], v[76:79], v[132:135], v[172:175]\n\t" \
  "ds_read_b128 v[108:111], v61 offset:14336\n\t" \
  "v_mfma_f32_16x16x32_bf16 v[180:183], v[76:79], v[128:131], v[180:183]\n\t" \
  "ds_read_b64_tr_b16 v[76:77], v63 offset:18432\n\t" \
  "v_mfma_f32_16x16x32_bf16 v[180:183], v[124:127], v[148:151], v[180:183]\n\t" \
  "ds_read_b64_tr_b16 v[78:79], v63 offset:18944\n\t" \
  "s_waitcnt lgkmcnt(7)\n\t" \
  "v_mfma_f32_16x16x32_bf16 v[184:187], v[64:67], v[96:99], v[184:187]\n\t" \
  "ds_read_b128 v[144:147], v61 offset:16384\n\t" \
  "v_mfma_f32_16x16x32_bf16 v[192:195], v[64:67], v[84:87], v[192:195]\n\t" \
  "ds_read_b64_tr_b16 v[140:141], v63 offset:30720\n\t" \
  "v_mfma_f32_16x16x32_bf16 v[200:203], v[64:67], v[92:95], v[200:203]\n\t" \
  "ds_read_b64_tr_b16 v[142:143], v63 offset:31232\n\t" \
  "v_mfma_f32_16x16x32_bf16 v[208:211], v[64:67], v[100:103], v[208:211]\n\t" \
  "ds_read_b64_tr_b16 v[112:113], v62 offset:22528\n\t" \
  "v_mfma_f32_16x16x32_bf16 v[188:191], v[72:75], v[96:99], v[188:191]\n\t" \
  "ds_read_b64_tr_b16 v[114:115], v62 offset:23040\n\t" \
  "v_mfma_f32_16x16x32_bf16 v[196:199], v[72:75], v[84:87], v[196:199]\n\t" \
  "ds_read_b64_tr_b16 v[132:133], v63 offset:22528\n\t" \
  "v_mfma_f32_16x16x32_bf16 v[204:207], v[72:75], v[92:95], v[204:207]\n\t" \
  "ds_read_b64_tr_b16 v[134:135], v63 offset:23040\n\t" \
  "v_mfma_f32_16x16x32_bf16 v[212:215], v[72:75], v[100:103], v[212:215]\n\t" \
  "502:\n\t" \
  "s_cbranch_execz 506f\n\t" \
  "ds_read_b64_tr_b16 v[128:129], v62 offset:38912\n\t" \
  "v_mfma_f32_16x16x32_bf16 v[248:251], v[104:107], v[68:71], v[248:251]\n\t" \
  "ds_read_b64_tr_b16 v[130:131], v62 offset:39424\n\t" \
  "v_mfma_f32_16x16x32_bf16 v[216:219], v[96:99], v[68:71], v[216:219]\n\t" \
  "ds_read_b128 v[124:127], v61 offset:0\n\t" \
  "v_mfma_f32_16x16x32_bf16 v[224:227], v[84:87], v[68:71], v[224:227]\n\t" \
  "ds_read_b64_tr_b16 v[148:149], v63 offset:38912\n\t" \
  "v_mfma_f32_16x16x32_bf16 v[232:235], v[92:95], v[68:71], v[232:235]\n\t" \
  "ds_read_b64_tr_b16 v[150:151], v63 offset:39424\n\t" \
  "v_mfma_f32_16x16x32_bf16 v[240:243], v[100:103], v[68:71], v[240:243]\n\t" \
  "v_mfma_f32_16x16x32_bf16 v[252:255], v[104:107], v[80:83], v[252:255]\n\t" \
  "v_mfma_f32_16x16x32_bf16 v[220:223], v[96:99], v[80:83], v[220:223]\n\t" \
  "v_mfma_f32_16x16x32_bf16 v[228:231], v[84:87], v[80:83], v[228:231]\n\t" \
  "v_mfma_f32_16x16x32_bf16 v[236:239], v[92:95], v[80:83], v[236:239]\n\t" \
  "v_mfma_f32_16x16x32_bf16 v[244:247], v[100:103], v[80:83], v[244:247]\n\t" \
  "v_mfma_f32_16x16x32_bf16 v[152:155], v[88:91], v[96:99], v[152:155]\n\t" \
  "v_mfma_f32_16x16x32_bf16 v[160:163], v[88:91], v[84:87], v[160:163]\n\t" \
  "v_mfma_f32_16x16x32_bf16 v[168:171], v[88:91], v[92:95], v[168:171]\n\t" \
  "v_mfma_f32_16x16x32_bf16 v[176:179], v[88:91], v[100:103], v[176:179]\n\t" \
  "s_waitcnt lgkmcnt(0)\n\t" \
  "v_mfma_f32_16x16x32_bf16 v[156:159], v[76:79], v[96:99], v[156:159]\n\t" \
  "v_mfma_f32_16x16x32_bf16 v[164:167], v[76:79], v[84:87], v[164:167]\n\t" \
  "ds_write_b32 v32, v0\n\t" \
  "v_mfma_f32_16x16x32_bf16 v[172:175], v[76:79], v[92:95], v[172:175]\n\t" \
  "v_mfma_f32_16x16x32_bf16 v[180:183], v[76:79], v[100:103], v[180:183]\n\t" \
  "v_mfma_f32_16x16x32_bf16 v[184:187], v[120:123], v[116:119], v[184:187]\n\t" \
  "v_and_b32 v56, -16, v32\n\t" \
  "v_mfma_f32_16x16x32_bf16 v[192:195], v[120:123], v[136:139], v[192:195]\n\t" \
  "v_mfma_f32_16x16x32_bf16 v[200:203], v[120:123], v[108:111], v[200:203]\n\t" \
  "v_mfma_f32_16x16x32_bf16 v[208:211], v[120:123], v[144:147], v[208:211]\n\t" \
  "v_mfma_f32_16x16x32_bf16 v[188:191], v[140:143], v[116:119], v[188:191]\n\t" \
  "v_mfma_f32_16x16x32_bf16 v[196:199], v[140:143], v[136:139], v[196:199]\n\t" \
  "v_mfma_f32_16x16x32_bf16 v[204:207], v[140:143], v[108:111], v[204:207]\n\t" \
  "v_mfma_f32_16x16x32_bf16 v[212:215], v[140:143], v[144:147], v[212:215]\n\t" \
  "v_cvt_pk_bf16_f32 v14, v184, v185\n\t" \
  "v_cvt_pk_bf16_f32 v15, v186, v187\n\t" \
  "v_mfma_f32_16x16x32_bf16 v[152:155], v[112:115], v[116:119], v[152:155]\n\t" \
  "v_cvt_pk_bf16_f32 v18, v192, v193\n\t" \
  "v_cvt_pk_bf16_f32 v19, v194, v195\n\t" \
  "v_mfma_f32_16x16x32_bf16 v[160:163], v[112:115], v[136:139], v[160:163]\n\t" \
  "v_cvt_pk_bf16_f32 v16, v188, v189\n\t" \
  "v_cvt_pk_bf16_f32 v17, v190, v191\n\t" \
  "v_mfma_f32_16x16x32_bf16 v[168:171], v[112:115], v[108:111], v[168:171]\n\t" \
  "v_cvt_pk_bf16_f32 v20, v196, v197\n\t" \
  "v_cvt_pk_bf16_f32 v21, v198, v199\n\t" \
  "v_mfma_f32_16x16x32_bf16 v[176:179], v[112:115], v[144:147], v[176:179]\n\t" \
  "v_cvt_pk_bf16_f32 v22, v200, v201\n\t" \
  "v_cvt_pk_bf16_f32 v23, v202, v203\n\t" \
  "v_mfma_f32_16x16x32_bf16 v[156:159], v[132:135], v[116:119], v[156:159]\n\t" \
  "v_cvt_pk_bf16_f32 v24, v204, v205\n\t" \
  "v_cvt_pk_bf16_f32 v25, v206, v207\n\t" \
  "v_mfma_f32_16x16x32_bf16 v[164:167], v[132:135], v[136:139], v[164:167]\n\t" \
  "v_cvt_pk_bf16_f32 v26, v208, v209\n\t" \
  "v_cvt_pk_bf16_f32 v27, v210, v211\n\t" \
  "v_mfma_f32_16x16x32_bf16 v[172:175], v[132:135], v[108:111], v[172:175]\n\t" \
  "v_cvt_pk_bf16_f32 v28, v212, v213\n\t" \
  "v_cvt_pk_bf16_f32 v29, v214, v215\n\t" \
  "v_mfma_f32_16x16x32_bf16 v[180:183], v[132:135], v[144:147], v[180:183]\n\t" \
  "v_cvt_pk_bf16_f32 v84, v152, v153\n\t" \
  "v_cvt_pk_bf16_f32 v85, v154, v155\n\t" \
  "v_mfma_f32_16x16x32_bf16 v[248:251], v[124:127], v[128:131], v[248:251]\n\t" \
  "v_cvt_pk_bf16_f32 v86, v156, v157\n\t" \
  "v_cvt_pk_bf16_f32 v87, v158, v159\n\t" \
  "v_mfma_f32_16x16x32_bf16 v[216:219], v[116:119], v[128:131], v[216:219]\n\t" \
  "v_cvt_pk_bf16_f32 v88, v160, v161\n\t" \
  "v_cvt_pk_bf16_f32 v89, v162, v163\n\t" \
  "v_mfma_f32_16x16x32_bf16 v[224:227], v[136:139], v[128:131], v[224:227]\n\t" \
  "v_cvt_pk_bf16_f32 v90, v164, v165\n\t" \
  "v_cvt_pk_bf16_f32 v91, v166, v167\n\t" \
  "v_mfma_f32_16x16x32_bf16 v[232:235], v[108:111], v[128:131], v[232:235]\n\t" \
  "v_cvt_pk_bf16_f32 v72, v248, v249\n\t" \
  "v_cvt_pk_bf16_f32 v73, v250, v251\n\t" \
  "v_mfma_f32_16x16x32_bf16 v[240:243], v[144:147], v[128:131], v[240:243]\n\t" \
  "ds_read_b32 v52, v33 offset:48\n\t" \
  "ds_read_b128 v[10:13], v31\n\t" \
  "v_cvt_pk_bf16_f32 v92, v168, v169\n\t" \
  "v_mfma_f32_16x16x32_bf16 v[252:255], v[124:127], v[148:151], v[252:255]\n\t" \
  "v_cvt_pk_bf16_f32 v93, v170, v171\n\t" \
  "v_cvt_pk_bf16_f32 v94, v172, v173\n\t" \
  "v_mfma_f32_16x16x32_bf16 v[220:223], v[116:119], v[148:151], v[220:223]\n\t" \
  "v_cvt_pk_bf16_f32 v95, v174, v175\n\t" \
  "v_cvt_pk_bf16_f32 v96, v176, v177\n\t" \
  "v_mfma_f32_16x16x32_bf16 v[228:231], v[136:139], v[148:151], v[228:231]\n\t" \
  "v_cvt_pk_bf16_f32 v97, v178, v179\n\t" \
  "v_cvt_pk_bf16_f32 v98, v180, v181\n\t" \
  "v_mfma_f32_16x16x32_bf16 v[236:239], v[108:111], v[148:151], v[236:239]\n\t" \
  "v_cvt_pk_bf16_f32 v82, v252, v253\n\t" \
  "v_cvt_pk_bf16_f32 v83, v254, v255\n\t" \
  "v_mfma_f32_16x16x32_bf16 v[244:247], v[144:147], v[148:151], v[244:247]\n\t" \
  "ds_write_b64 v30, v[72:73]\n\t" \
  "ds_write_b64 v30, v[82:83] offset:8\n\t" \
  "ds_write_b32 v32, v0 offset:48\n\t" \
  "v_mfma_f32_16x16x32_bf16 v[200:203], v[22:25], v[92:95], v[34:37]\n\t" \
  "v_cvt_pk_bf16_f32 v99, v182, v183\n\t" \
  "s_waitcnt lgkmcnt(0)\n\t" \
  "v_readfirstlane_b32 vcc_lo, v52\n\t" \
  "s_nop 1\n\t" \
  "s_cmp_eq_u32 vcc_lo, 0\n\t" \
  "s_cbranch_scc1 5f\n\t" \
  "4:\n\t" \
  "v_mfma_f32_16x16x32_bf16 v[204:207], v[18:21], v[92:95], v[38:41]\n\t" \
  "v_cvt_pk_bf16_f32 v66, v232, v233\n\t" \
  "v_cvt_pk_bf16_f32 v67, v234, v235\n\t" \
  "v_mfma_f32_16x16x32_bf16 v[208:211], v[26:29], v[96:99], v[34:37]\n\t" \
  "v_cvt_pk_bf16_f32 v76, v236, v237\n\t" \
  "v_cvt_pk_bf16_f32 v77, v238, v239\n\t" \
  "v_mfma_f32_16x16x32_bf16 v[212:215], v[22:25], v[96:99], v[38:41]\n\t" \
  "v_cvt_pk_bf16_f32 v64, v240, v241\n\t" \
  "v_cvt_pk_bf16_f32 v65, v242, v243\n\t" \
  "v_mfma_f32_16x16x32_bf16 v[192:195], v[18:21], v[88:91], v[34:37]\n\t" \
  "v_max_f32 v200, v200, v204\n\t" \
  "v_max_f32 v201, v201, v205\n\t" \
  "v_mfma_f32_16x16x32_bf16 v[196:199], v[14:17], v[88:91], v[38:41]\n\t" \
  "v_max_f32 v202, v202, v206\n\t" \
  "v_max_f32 v203, v203, v207\n\t" \
  "v_mfma_f32_16x16x32_bf16 v[184:187], v[14:17], v[84:87], v[34:37]\n\t" \
  "v_max_f32 v208, v208, v212\n\t" \
  "v_max_f32 v209, v209, v213\n\t" \
  "v_mfma_f32_16x16x32_bf16 v[188:191], v[10:13], v[84:87], v[42:45]\n\t" \
  "v_max_f32 v210, v210, v214\n\t" \
  "v_max_f32 v211, v211, v215\n\t" \
  "v_max_f32 v192, v192, v196\n\t" \
  "v_max_f32 v193, v193, v197\n\t" \
  "v_max_f32 v194, v194, v198\n\t" \
  "v_max3_f32 v106, v208, v209, v210\n\t" \
  "v_max3_f32 v104, v200, v201, v202\n\t" \
  "v_max_f32 v195, v195, v199\n\t" \
  "v_max_f32 v184, v184, v188\n\t" \
  "v_max_f32 v185, v185, v189\n\t" \
  "v_max_f32 v186, v186, v190\n\t" \
  "v_max3_f32 v102, v192, v193, v194\n\t" \
  "v_max_f32 v187, v187, v191\n\t" \
  "v_max3_f32 v100, v184, v185, v186\n\t" \
  "v_max_f32 v106, v106, v211\n\t" \
  "v_max_f32 v104, v104, v203\n\t" \
  "v_max_f32 v102, v102, v195\n\t" \
  "v_max_f32 v100, v100, v187\n\t" \
  "v_permlane16_swap_b32 v104, v106\n\t" \
  "v_cvt_pk_bf16_f32 v74, v244, v245\n\t" \
  "v_permlane16_swap_b32 v100, v102\n\t" \
  "v_max_f32 v104, v104, v106\n\t" \
  "v_max_f32 v100, v100, v102\n\t" \
  "v_cvt_pk_bf16_f32 v75, v246, v247\n\t" \
  "v_cvt_pk_bf16_f32 v68, v224, v225\n\t" \
  "v_permlane32_swap_b32 v100, v104\n\t" \
  "v_cvt_pk_bf16_f32 v69, v226, v227\n\t" \
  "v_max_f32 v100, v100, v104\n\t" \
  "v_mul_f32 v100, 0xbe8293ee, v100\n\t" \
  "v_mov_b32 v104, v100\n\t" \
  "v_cvt_pk_bf16_f32 v78, v228, v229\n\t" \
  "v_cvt_pk_bf16_f32 v79, v230, v231\n\t" \
  "v_permlane32_swap_b32 v100, v104\n\t" \
  "v_cvt_pk_bf16_f32 v70, v216, v217\n\t" \
  "v_mov_b32 v106, v104\n\t" \
  "v_mov_b32 v102, v100\n\t" \
  "v_cvt_pk_bf16_f32 v71, v218, v219\n\t" \
  "v_permlane16_swap_b32 v104, v106\n\t" \
  "v_permlane16_swap_b32 v100, v102\n\t" \
  "v_fma_f32 v208, v208, %16, v106\n\t" \
  "v_fma_f32 v209, v209, %16, v106\n\t" \
  "v_fma_f32 v210, v210, %16, v106\n\t" \
  "v_fma_f32 v211, v211, %16, v106\n\t" \
  "v_exp_f32 v208, v208\n\t" \
  "v_exp_f32 v209, v209\n\t" \
  "v_exp_f32 v210, v210\n\t" \
  "v_exp_f32 v211, v211\n\t" \
  "v_cvt_pk_bf16_f32 v212, v208, v209\n\t" \
  "v_cvt_pk_bf16_f32 v213, v210, v211\n\t" \
  "v_bfi_b32 v214, v46, 0, v212\n\t" \
  "v_bfi_b32 v215, v47, 0, v213\n\t" \
  "v_and_b32 v212, v46, v212\n\t" \
  "v_and_b32 v213, v47, v213\n\t" \
  "v_fma_f32 v200, v200, %16, v104\n\t" \
  "v_fma_f32 v201, v201, %16, v104\n\t" \
  "v_mfma_f32_16x16x32_bf16 v[208:211], v[48:51], v[212:215], 0\n\t" \
  "v_fma_f32 v202, v202, %16, v104\n\t" \
  "v_fma_f32 v203, v203, %16, v104\n\t" \
  "v_mfma_f32_16x16x32_bf16 v[176:179], v[64:67], v[212:215], 0\n\t" \
  "v_exp_f32 v200, v200\n\t" \
  "v_mfma_f32_16x16x32_bf16 v[180:183], v[74:77], v[212:215], 0\n\t" \
  "v_exp_f32 v201, v201\n\t" \
  "v_exp_f32 v202, v202\n\t" \
  "v_exp_f32 v203, v203\n\t" \
  "v_cvt_pk_bf16_f32 v204, v200, v201\n\t" \
  "v_cvt_pk_bf16_f32 v205, v202, v203\n\t" \
  "v_bfi_b32 v206, v46, 0, v204\n\t" \
  "v_bfi_b32 v207, v47, 0, v205\n\t" \
  "v_rcp_f32 v106, v208\n\t" \
  "v_and_b32 v204, v46, v204\n\t" \
  "v_and_b32 v205, v47, v205\n\t" \
  "v_pk_mul_f32 v[176:177], v[176:177], v[106:107] op_sel_hi:[1,0]\n\t" \
  "v_pk_mul_f32 v[178:179], v[178:179], v[106:107] op_sel_hi:[1,0]\n\t" \
  "v_mfma_f32_16x16x32_bf16 v[200:203], v[48:51], v[204:207], 0\n\t" \
  "v_pk_mul_f32 v[180:181], v[180:181], v[106:107] op_sel_hi:[1,0]\n\t" \
  "v_pk_mul_f32 v[182:183], v[182:183], v[106:107] op_sel_hi:[1,0]\n\t" \
  "v_mfma_f32_16x16x32_bf16 v[168:171], v[66:69], v[204:207], 0\n\t" \
  "v_cvt_pk_bf16_f32 v240, v176, v177\n\t" \
  "v_cvt_pk_bf16_f32 v241, v178, v179\n\t" \
  "v_mfma_f32_16x16x32_bf16 v[172:175], v[76:79], v[204:207], 0\n\t" \
  "v_cvt_pk_bf16_f32 v242, v180, v181\n\t" \
  "v_cvt_pk_bf16_f32 v243, v182, v183\n\t" \
  "global_store_dwordx4 v1, v[240:243], %14 offset:0 sc1\n\t" \
  "v_fma_f32 v192, v192, %16, v102\n\t" \
  "v_fma_f32 v193, v193, %16, v102\n\t" \
  "v_fma_f32 v194, v194, %16, v102\n\t" \
  "v_fma_f32 v195, v195, %16, v102\n\t" \
  "v_exp_f32 v192, v192\n\t" \
  "v_exp_f32 v193, v193\n\t" \
  "v_exp_f32 v194, v194\n\t" \
  "v_exp_f32 v195, v195\n\t" \
  "v_cvt_pk_bf16_f32 v196, v192, v193\n\t" \
  "v_cvt_pk_bf16_f32 v197, v194, v195\n\t" \
  "v_bfi_b32 v198, v46, 0, v196\n\t" \
  "v_bfi_b32 v199, v47, 0, v197\n\t" \
  "v_rcp_f32 v104, v200\n\t" \
  "v_and_b32 v196, v46, v196\n\t" \
  "v_and_b32 v197, v47, v197\n\t" \
  "v_cvt_pk_bf16_f32 v80, v220, v221\n\t" \
  "v_cvt_pk_bf16_f32 v81, v222, v223\n\t" \
  "v_mfma_f32_16x16x32_bf16 v[192:195], v[48:51], v[196:199], 0\n\t" \
  "v_pk_mul_f32 v[168:169], v[168:169], v[104:105] op_sel_hi:[1,0]\n\t" \
  "v_pk_mul_f32 v[170:171], v[170:171], v[104:105] op_sel_hi:[1,0]\n\t" \
  "v_mfma_f32_16x16x32_bf16 v[160:163], v[68:71], v[196:199], 0\n\t" \
  "v_pk_mul_f32 v[172:173], v[172:173], v[104:105] op_sel_hi:[1,0]\n\t" \
  "v_pk_mul_f32 v[174:175], v[174:175], v[104:105] op_sel_hi:[1,0]\n\t" \
  "v_mfma_f32_16x16x32_bf16 v[164:167], v[78:81], v[196:199], 0\n\t" \
  "v_cvt_pk_bf16_f32 v232, v168, v169\n\t" \
  "v_cvt_pk_bf16_f32 v233, v170, v171\n\t" \
  "v_cvt_pk_bf16_f32 v234, v172, v173\n\t" \
  "v_cvt_pk_bf16_f32 v235, v174, v175\n\t" \
  "global_store_dwordx4 v1, v[232:235], %14 offset:-4096 sc1\n\t" \
  "v_fma_f32 v184, v184, %16, v100\n\t" \
  "v_fma_f32 v185, v185, %16, v100\n\t" \
  "v_fma_f32 v186, v186, %16, v100\n\t" \
  "v_fma_f32 v187, v187, %16, v100\n\t" \
  "v_exp_f32 v184, v184\n\t" \
  "v_exp_f32 v185, v185\n\t" \
  "v_exp_f32 v186, v186\n\t" \
  "v_exp_f32 v187, v187\n\t" \
  "v_cvt_pk_bf16_f32 v188, v184, v185\n\t" \
  "v_cvt_pk_bf16_f32 v189, v186, v187\n\t" \
  "v_bfi_b32 v190, v46, 0, v188\n\t" \
  "v_bfi_b32 v191, v47, 0, v189\n\t" \
  "v_rcp_f32 v102, v192\n\t" \
  "v_and_b32 v188, v46, v188\n\t" \
  "v_and_b32 v189, v47, v189\n\t" \
  "v_pk_mul_f32 v[160:161], v[160:161], v[102:103] op_sel_hi:[1,0]\n\t" \
  "v_pk_mul_f32 v[162:163], v[162:163], v[102:103] op_sel_hi:[1,0]\n\t" \
  "v_mfma_f32_16x16x32_bf16 v[184:187], v[48:51], v[188:191], 0\n\t" \
  "v_pk_mul_f32 v[164:165], v[164:165], v[102:103] op_sel_hi:[1,0]\n\t" \
  "v_pk_mul_f32 v[166:167], v[166:167], v[102:103] op_sel_hi:[1,0]\n\t" \
  "v_mfma_f32_16x16x32_bf16 v[152:155], v[70:73], v[188:191], 0\n\t" \
  "v_cvt_pk_bf16_f32 v224, v160, v161\n\t" \
  "v_cvt_pk_bf16_f32 v225, v162, v163\n\t" \
  "v_mfma_f32_16x16x32_bf16 v[156:159], v[80:83], v[188:191], 0\n\t" \
  "v_cvt_pk_bf16_f32 v226, v164, v165\n\t" \
  "v_cvt_pk_bf16_f32 v227, v166, v167\n\t" \
  "global_store_dwordx4 v4, v[224:227], %14 offset:0 sc1\n\t" \
  "v_rcp_f32 v100, v184\n\t" \
  "ds_read_b128 v[60:63], v56 offset:64\n\t" \
  "ds_read_b64_tr_b16 v[108:109], v5 offset:0\n\t" \
  "ds_read_b64_tr_b16 v[110:111], v5 offset:16\n\t" \
  "ds_read_b128 v[112:115], v6 offset:16384\n\t" \
  "ds_read_b128 v[116:119], v6 offset:15360\n\t" \
  "ds_read_b64_tr_b16 v[120:121], v5 offset:512\n\t" \
  "ds_read_b64_tr_b16 v[122:123], v5 offset:528\n\t" \
  "ds_read_b128 v[124:127], v6 offset:14336\n\t" \
  "ds_read_b64_tr_b16 v[128:129], v5 offset:1024\n\t" \
  "ds_read_b64_tr_b16 v[130:131], v5 offset:1040\n\t" \
  "ds_read_b128 v[132:135], v6 offset:13312\n\t" \
  "ds_read_b64_tr_b16 v[136:137], v5 offset:1536\n\t" \
  "ds_read_b64_tr_b16 v[138:139], v5 offset:1552\n\t" \
  "ds_read_b128 v[140:143], v6 offset:12288\n\t" \
  "ds_read_b64_tr_b16 v[144:145], v5 offset:2048\n\t" \
  "v_pk_mul_f32 v[152:153], v[152:153], v[100:101] op_sel_hi:[1,0]\n\t" \
  "v_pk_mul_f32 v[154:155], v[154:155], v[100:101] op_sel_hi:[1,0]\n\t" \
  "v_cvt_pk_bf16_f32 v216, v152, v153\n\t" \
  "v_pk_mul_f32 v[156:157], v[156:157], v[100:101] op_sel_hi:[1,0]\n\t" \
  "v_pk_mul_f32 v[158:159], v[158:159], v[100:101] op_sel_hi:[1,0]\n\t" \
  "v_cvt_pk_bf16_f32 v217, v154, v155\n\t" \
  "v_cvt_pk_bf16_f32 v218, v156, v157\n\t" \
  "v_cvt_pk_bf16_f32 v219, v158, v159\n\t" \
  "global_store_dwordx4 v4, v[216:219], %14 offset:-4096 sc1\n\t" \
  "ds_write_b32 v32, v0 offset:112\n\t" \
  "s_branch 9f\n\t" \
  "5:\n\t" \
  "ds_read_b32 v52, v33 offset:48\n\t" \
  "ds_read_b128 v[10:13], v31\n\t" \
  "s_waitcnt lgkmcnt(0)\n\t" \
  "v_readfirstlane_b32 vcc_lo, v52\n\t" \
  "s_nop 1\n\t" \
  "s_cmp_eq_u32 vcc_lo, 0\n\t" \
  "s_cbranch_scc1 5b\n\t" \
  "s_branch 4b\n\t" \
  "15:\n\t" \
  "ds_read_b128 v[10:13], v14 offset:128\n\t" \
  "s_waitcnt lgkmcnt(0)\n\t" \
  "v_and_b32 v10, v10, v11\n\t" \
  "v_and_b32 v12, v12, v13\n\t" \
  "v_and_b32 v10, v10, v12\n\t" \
  "s_nop 1\n\t" \
  "v_readfirstlane_b32 vcc_lo, v10\n\t" \
  "s_nop 1\n\t" \
  "s_cmp_eq_u32 vcc_lo, 0\n\t" \
  "s_cbranch_scc1 15b\n\t" \
  "s_branch 14b\n\t" \
  "8:\n\t" \
  "global_load_dwordx2 v[10:11], v51, %11\n\t" \
  "global_load_dwordx2 v[12:13], v51, %11 offset:128\n\t" \
  "v_xor_b32 v53, 64, v52\n\t" \
  "v_xor_b32 v55, 32, v54\n\t" \
  "v_add_u32 v56, 43008, v52\n\t" \
  "v_add_u32 v58, 43008, v54\n\t" \
  "v_xor_b32 v57, 64, v56\n\t" \
  "v_xor_b32 v59, 32, v58\n\t" \
  "v_add_u32 v60, 86016, v52\n\t" \
  "v_add_u32 v62, 86016, v54\n\t" \
  "v_xor_b32 v61, 64, v60\n\t" \
  "v_xor_b32 v63, 32, v62\n\t" \
  "s_mov_b32 vcc_hi, 0x05040100\n\t" \
  "s_waitcnt vmcnt(2)\n\t" \
  "s_barrier\n\t" \
  "ds_read_b64_tr_b16 v[64:65], v62 offset:34816\n\t" \
  "ds_read_b64_tr_b16 v[66:67], v62 offset:35328\n\t" \
  "ds_read_b128 v[68:71], v60 offset:2048\n\t" \
  "ds_read_b64_tr_b16 v[72:73], v62 offset:38912\n\t" \
  "ds_read_b64_tr_b16 v[74:75], v62 offset:39424\n\t" \
  "ds_read_b128 v[76:79], v61 offset:2048\n\t" \
  "ds_read_b128 v[80:83], v61 offset:4096\n\t" \
  "ds_read_b128 v[84:87], v60 offset:4096\n\t" \
  "ds_read_b128 v[88:91], v60 offset:6144\n\t" \
  "ds_read_b128 v[92:95], v61 offset:6144\n\t" \
  "ds_read_b128 v[96:99], v61 offset:8192\n\t" \
  "ds_read_b128 v[100:103], v60 offset:8192\n\t" \
  "ds_read_b64_tr_b16 v[104:105], v63 offset:34816\n\t" \
  "ds_read_b64_tr_b16 v[106:107], v63 offset:35328\n\t" \
  "ds_read_b64_tr_b16 v[108:109], v63 offset:38912\n\t" \
  "ds_read_b64_tr_b16 v[110:111], v63 offset:39424\n\t" \
  "s_waitcnt lgkmcnt(13)\n\t" \
  "v_mfma_f32_16x16x32_bf16 v[216:219], v[68:71], v[64:67], 0\n\t" \
  "ds_read_b64_tr_b16 v[112:113], v62 offset:26624\n\t" \
  "ds_read_b64_tr_b16 v[114:115], v62 offset:27136\n\t" \
  "s_waitcnt lgkmcnt(12)\n\t" \
  "v_mfma_f32_16x16x32_bf16 v[216:219], v[76:79], v[72:75], v[216:219]\n\t" \
  "ds_read_b64_tr_b16 v[116:117], v62 offset:30720\n\t" \
  "ds_read_b64_tr_b16 v[118:119], v62 offset:31232\n\t" \
  "s_waitcnt lgkmcnt(13)\n\t" \
  "v_mfma_f32_16x16x32_bf16 v[224:227], v[80:83], v[72:75], 0\n\t" \
  "ds_read_b128 v[120:123], v60 offset:0\n\t" \
  "s_waitcnt lgkmcnt(13)\n\t" \
  "v_mfma_f32_16x16x32_bf16 v[224:227], v[84:87], v[64:67], v[224:227]\n\t" \
  "ds_read_b128 v[124:127], v61 offset:0\n\t" \
  "s_waitcnt lgkmcnt(13)\n\t" \
  "v_mfma_f32_16x16x32_bf16 v[232:235], v[88:91], v[64:67], 0\n\t" \
  "ds_read_b64_tr_b16 v[128:129], v63 offset:26624\n\t" \
  "s_waitcnt lgkmcnt(13)\n\t" \
  "v_mfma_f32_16x16x32_bf16 v[232:235], v[92:95], v[72:75], v[232:235]\n\t" \
  "ds_read_b64_tr_b16 v[130:131], v63 offset:27136\n\t" \
  "s_waitcnt lgkmcnt(13)\n\t" \
  "v_mfma_f32_16x16x32_bf16 v[240:243], v[96:99], v[72:75], 0\n\t" \
  "ds_read_b64_tr_b16 v[132:133], v63 offset:30720\n\t" \
  "s_waitcnt lgkmcnt(9)\n\t" \
  "v_mfma_f32_16x16x32_bf16 v[240:243], v[100:103], v[64:67], v[240:243]\n\t" \
  "ds_read_b64_tr_b16 v[134:135], v63 offset:31232\n\t" \
  "v_mfma_f32_16x16x32_bf16 v[220:223], v[68:71], v[104:107], 0\n\t" \
  "ds_read_b64_tr_b16 v[136:137], v62 offset:18432\n\t" \
  "v_mfma_f32_16x16x32_bf16 v[220:223], v[76:79], v[108:111], v[220:223]\n\t" \
  "ds_read_b64_tr_b16 v[138:139], v62 offset:18944\n\t" \
  "v_mfma_f32_16x16x32_bf16 v[228:231], v[80:83], v[108:111], 0\n\t" \
  "ds_read_b64_tr_b16 v[140:141], v62 offset:22528\n\t" \
  "v_mfma_f32_16x16x32_bf16 v[228:231], v[84:87], v[104:107], v[228:231]\n\t" \
  "ds_read_b64_tr_b16 v[142:143], v62 offset:23040\n\t" \
  "v_mfma_f32_16x16x32_bf16 v[236:239], v[88:91], v[104:107], 0\n\t" \
  "ds_read_b64_tr_b16 v[144:145], v63 offset:18432\n\t" \
  "v_mfma_f32_16x16x32_bf16 v[236:239], v[92:95], v[108:111], v[236:239]\n\t" \
  "ds_read_b64_tr_b16 v[146:147], v63 offset:18944\n\t" \
  "v_mfma_f32_16x16x32_bf16 v[244:247], v[96:99], v[108:111], 0\n\t" \
  "ds_read_b64_tr_b16 v[148:149], v63 offset:22528\n\t" \
  "v_mfma_f32_16x16x32_bf16 v[244:247], v[100:103], v[104:107], v[244:247]\n\t" \
  "ds_read_b64_tr_b16 v[150:151], v63 offset:23040\n\t" \
  "s_waitcnt lgkmcnt(7)\n\t" \
  "v_mfma_f32_16x16x32_bf16 v[184:187], v[112:115], v[68:71], 0\n\t" \
  "v_mfma_f32_16x16x32_bf16 v[184:187], v[116:119], v[76:79], v[184:187]\n\t" \
  "v_mfma_f32_16x16x32_bf16 v[192:195], v[116:119], v[80:83], 0\n\t" \
  "v_mfma_f32_16x16x32_bf16 v[192:195], v[112:115], v[84:87], v[192:195]\n\t" \
  "v_mfma_f32_16x16x32_bf16 v[200:203], v[112:115], v[88:91], 0\n\t" \
  "v_mfma_f32_16x16x32_bf16 v[200:203], v[116:119], v[92:95], v[200:203]\n\t" \
  "v_mfma_f32_16x16x32_bf16 v[208:211], v[116:119], v[96:99], 0\n\t" \
  "v_mfma_f32_16x16x32_bf16 v[208:211], v[112:115], v[100:103], v[208:211]\n\t" \
  "v_mfma_f32_16x16x32_bf16 v[248:251], v[112:115], v[120:123], 0\n\t" \
  "v_mfma_f32_16x16x32_bf16 v[248:251], v[116:119], v[124:127], v[248:251]\n\t" \
  "v_mfma_f32_16x16x32_bf16 v[252:255], v[128:131], v[120:123], 0\n\t" \
  "v_mfma_f32_16x16x32_bf16 v[252:255], v[132:135], v[124:127], v[252:255]\n\t" \
  "v_mfma_f32_16x16x32_bf16 v[188:191], v[132:135], v[76:79], 0\n\t" \
  "v_mfma_f32_16x16x32_bf16 v[188:191], v[128:131], v[68:71], v[188:191]\n\t" \
  "s_waitcnt lgkmcnt(0)\n\t" \
  "s_getpc_b64 s[88:89]\n\t" \
  "s_branch 28b\n\t" \
  "s_barrier\n\t" \
  "v_mfma_f32_16x16x32_bf16 v[196:199], v[128:131], v[84:87], 0\n\t" \
  "ds_read_b64_tr_b16 v[72:73], v54 offset:34816\n\t" \
  "v_mfma_f32_16x16x32_bf16 v[196:199], v[132:135], v[80:83], v[196:199]\n\t" \
  "ds_read_b64_tr_b16 v[74:75], v54 offset:35328\n\t" \
  "v_mfma_f32_16x16x32_bf16 v[204:207], v[132:135], v[92:95], 0\n\t" \
  "ds_read_b128 v[64:67], v52 offset:2048\n\t" \
  "v_mfma_f32_16x16x32_bf16 v[204:207], v[128:131], v[88:91], v[204:207]\n\t" \
  "ds_read_b64_tr_b16 v[108:109], v54 offset:38912\n\t" \
  "v_mfma_f32_16x16x32_bf16 v[212:215], v[128:131], v[100:103], 0\n\t" \
  "ds_read_b64_tr_b16 v[110:111], v54 offset:39424\n\t" \
  "v_mfma_f32_16x16x32_bf16 v[212:215], v[132:135], v[96:99], v[212:215]\n\t" \
  "ds_read_b128 v[104:107], v53 offset:2048\n\t" \
  "v_mfma_f32_16x16x32_bf16 v[152:155], v[136:139], v[68:71], 0\n\t" \
  "ds_read_b128 v[112:115], v53 offset:4096\n\t" \
  "v_mfma_f32_16x16x32_bf16 v[152:155], v[140:143], v[76:79], v[152:155]\n\t" \
  "ds_read_b128 v[116:119], v52 offset:4096\n\t" \
  "v_mfma_f32_16x16x32_bf16 v[160:163], v[140:143], v[80:83], 0\n\t" \
  "ds_read_b128 v[120:123], v52 offset:6144\n\t" \
  "v_mfma_f32_16x16x32_bf16 v[160:163], v[136:139], v[84:87], v[160:163]\n\t" \
  "ds_read_b128 v[124:127], v53 offset:6144\n\t" \
  "v_mfma_f32_16x16x32_bf16 v[168:171], v[136:139], v[88:91], 0\n\t" \
  "ds_read_b128 v[128:131], v53 offset:8192\n\t" \
  "v_mfma_f32_16x16x32_bf16 v[168:171], v[140:143], v[92:95], v[168:171]\n\t" \
  "ds_read_b128 v[132:135], v52 offset:8192\n\t" \
  "v_mfma_f32_16x16x32_bf16 v[176:179], v[140:143], v[96:99], 0\n\t" \
  "ds_read_b64_tr_b16 v[140:141], v55 offset:34816\n\t" \
  "v_mfma_f32_16x16x32_bf16 v[176:179], v[136:139], v[100:103], v[176:179]\n\t" \
  "ds_read_b64_tr_b16 v[142:143], v55 offset:35328\n\t" \
  "v_mfma_f32_16x16x32_bf16 v[156:159], v[144:147], v[68:71], 0\n\t" \
  "ds_read_b64_tr_b16 v[136:137], v55 offset:38912\n\t" \
  "v_mfma_f32_16x16x32_bf16 v[156:159], v[148:151], v[76:79], v[156:159]\n\t" \
  "ds_read_b64_tr_b16 v[138:139], v55 offset:39424\n\t" \
  "v_mfma_f32_16x16x32_bf16 v[164:167], v[148:151], v[80:83], 0\n\t" \
  "ds_read_b64_tr_b16 v[68:69], v54 offset:26624\n\t" \
  "v_mfma_f32_16x16x32_bf16 v[164:167], v[144:147], v[84:87], v[164:167]\n\t" \
  "ds_read_b64_tr_b16 v[70:71], v54 offset:27136\n\t" \
  "v_mfma_f32_16x16x32_bf16 v[172:175], v[144:147], v[88:91], 0\n\t" \
  "ds_read_b64_tr_b16 v[76:77], v54 offset:30720\n\t" \
  "v_mfma_f32_16x16x32_bf16 v[172:175], v[148:151], v[92:95], v[172:175]\n\t" \
  "ds_read_b64_tr_b16 v[78:79], v54 offset:31232\n\t" \
  "v_mfma_f32_16x16x32_bf16 v[180:183], v[148:151], v[96:99], 0\n\t" \
  "ds_read_b128 v[80:83], v52 offset:0\n\t" \
  "v_mfma_f32_16x16x32_bf16 v[180:183], v[144:147], v[100:103], v[180:183]\n\t" \
  "ds_read_b128 v[84:87], v53 offset:0\n\t" \
  "s_waitcnt lgkmcnt(7)\n\t" \
  "v_mfma_f32_16x16x32_bf16 v[216:219], v[64:67], v[72:75], v[216:219]\n\t" \
  "ds_read_b64_tr_b16 v[88:89], v55 offset:26624\n\t" \
  "v_mfma_f32_16x16x32_bf16 v[216:219], v[104:107], v[108:111], v[216:219]\n\t" \
  "ds_read_b64_tr_b16 v[90:91], v55 offset:27136\n\t" \
  "v_mfma_f32_16x16x32_bf16 v[224:227], v[112:115], v[108:111], v[224:227]\n\t" \
  "ds_read_b64_tr_b16 v[92:93], v55 offset:30720\n\t" \
  "v_mfma_f32_16x16x32_bf16 v[224:227], v[116:119], v[72:75], v[224:227]\n\t" \
  "ds_read_b64_tr_b16 v[94:95], v55 offset:31232\n\t" \
  "v_mfma_f32_16x16x32_bf16 v[232:235], v[120:123], v[72:75], v[232:235]\n\t" \
  "ds_read_b64_tr_b16 v[96:97], v54 offset:18432\n\t" \
  "v_mfma_f32_16x16x32_bf16 v[232:235], v[124:127], v[108:111], v[232:235]\n\t" \
  "ds_read_b64_tr_b16 v[98:99], v54 offset:18944\n\t" \
  "v_mfma_f32_16x16x32_bf16 v[240:243], v[128:131], v[108:111], v[240:243]\n\t" \
  "ds_read_b64_tr_b16 v[148:149], v54 offset:22528\n\t" \
  "v_mfma_f32_16x16x32_bf16 v[240:243], v[132:135], v[72:75], v[240:243]\n\t" \
  "ds_read_b64_tr_b16 v[150:151], v54 offset:23040\n\t" \
  "v_mfma_f32_16x16x32_bf16 v[220:223], v[64:67], v[140:143], v[220:223]\n\t" \
  "ds_read_b64_tr_b16 v[100:101], v55 offset:18432\n\t" \
  "s_waitcnt lgkmcnt(7)\n\t" \
  "v_mfma_f32_16x16x32_bf16 v[220:223], v[104:107], v[136:139], v[220:223]\n\t" \
  "ds_read_b64_tr_b16 v[102:103], v55 offset:18944\n\t" \
  "v_mfma_f32_16x16x32_bf16 v[228:231], v[112:115], v[136:139], v[228:231]\n\t" \
  "ds_read_b64_tr_b16 v[144:145], v55 offset:22528\n\t" \
  "v_mfma_f32_16x16x32_bf16 v[228:231], v[116:119], v[140:143], v[228:231]\n\t" \
  "ds_read_b64_tr_b16 v[146:147], v55 offset:23040\n\t" \
  "v_mfma_f32_16x16x32_bf16 v[236:239], v[120:123], v[140:143], v[236:239]\n\t" \
  "v_mfma_f32_16x16x32_bf16 v[236:239], v[124:127], v[136:139], v[236:239]\n\t" \
  "v_mfma_f32_16x16x32_bf16 v[244:247], v[128:131], v[136:139], v[244:247]\n\t" \
  "v_mfma_f32_16x16x32_bf16 v[244:247], v[132:135], v[140:143], v[244:247]\n\t" \
  "v_mfma_f32_16x16x32_bf16 v[184:187], v[68:71], v[64:67], v[184:187]\n\t" \
  "v_mfma_f32_16x16x32_bf16 v[184:187], v[76:79], v[104:107], v[184:187]\n\t" \
  "v_mfma_f32_16x16x32_bf16 v[192:195], v[76:79], v[112:115], v[192:195]\n\t" \
  "v_mfma_f32_16x16x32_bf16 v[192:195], v[68:71], v[116:119], v[192:195]\n\t" \
  "v_mfma_f32_16x16x32_bf16 v[200:203], v[68:71], v[120:123], v[200:203]\n\t" \
  "v_mfma_f32_16x16x32_bf16 v[200:203], v[76:79], v[124:127], v[200:203]\n\t" \
  "v_mfma_f32_16x16x32_bf16 v[208:211], v[76:79], v[128:131], v[208:211]\n\t" \
  "v_mfma_f32_16x16x32_bf16 v[208:211], v[68:71], v[132:135], v[208:211]\n\t" \
  "v_mfma_f32_16x16x32_bf16 v[248:251], v[68:71], v[80:83], v[248:251]\n\t" \
  "v_mfma_f32_16x16x32_bf16 v[248:251], v[76:79], v[84:87], v[248:251]\n\t" \
  "v_mfma_f32_16x16x32_bf16 v[252:255], v[88:91], v[80:83], v[252:255]\n\t" \
  "s_waitcnt lgkmcnt(0)\n\t" \
  "v_mfma_f32_16x16x32_bf16 v[252:255], v[92:95], v[84:87], v[252:255]\n\t" \
  "v_mfma_f32_16x16x32_bf16 v[188:191], v[92:95], v[104:107], v[188:191]\n\t" \
  "v_mfma_f32_16x16x32_bf16 v[188:191], v[88:91], v[64:67], v[188:191]\n\t" \
  "s_barrier\n\t" \
  "v_mfma_f32_16x16x32_bf16 v[196:199], v[88:91], v[116:119], v[196:199]\n\t" \
  "ds_read_b64_tr_b16 v[108:109], v58 offset:34816\n\t" \
  "v_mfma_f32_16x16x32_bf16 v[196:199], v[92:95], v[112:115], v[196:199]\n\t" \
  "ds_read_b64_tr_b16 v[110:111], v58 offset:35328\n\t" \
  "v_mfma_f32_16x16x32_bf16 v[204:207], v[92:95], v[124:127], v[204:207]\n\t" \
  "ds_read_b128 v[72:75], v56 offset:2048\n\t" \
  "v_mfma_f32_16x16x32_bf16 v[204:207], v[88:91], v[120:123], v[204:207]\n\t" \
  "ds_read_b64_tr_b16 v[136:137], v58 offset:38912\n\t" \
  "v_mfma_f32_16x16x32_bf16 v[212:215], v[88:91], v[132:135], v[212:215]\n\t" \
  "ds_read_b64_tr_b16 v[138:139], v58 offset:39424\n\t" \
  "v_mfma_f32_16x16x32_bf16 v[212:215], v[92:95], v[128:131], v[212:215]\n\t" \
  "ds_read_b128 v[140:143], v57 offset:2048\n\t" \
  "v_mfma_f32_16x16x32_bf16 v[152:155], v[96:99], v[64:67], v[152:155]\n\t" \
  "ds_read_b128 v[68:71], v57 offset:4096\n\t" \
  "v_mfma_f32_16x16x32_bf16 v[152:155], v[148:151], v[104:107], v[152:155]\n\t" \
  "ds_read_b128 v[76:79], v56 offset:4096\n\t" \
  "v_mfma_f32_16x16x32_bf16 v[160:163], v[148:151], v[112:115], v[160:163]\n\t" \
  "ds_read_b128 v[80:83], v56 offset:6144\n\t" \
  "v_mfma_f32_16x16x32_bf16 v[160:163], v[96:99], v[116:119], v[160:163]\n\t" \
  "ds_read_b128 v[84:87], v57 offset:6144\n\t" \
  "v_mfma_f32_16x16x32_bf16 v[168:171], v[96:99], v[120:123], v[168:171]\n\t" \
  "ds_read_b128 v[88:91], v57 offset:8192\n\t" \
  "v_mfma_f32_16x16x32_bf16 v[168:171], v[148:151], v[124:127], v[168:171]\n\t" \
  "ds_read_b128 v[92:95], v56 offset:8192\n\t" \
  "v_mfma_f32_16x16x32_bf16 v[176:179], v[148:151], v[128:131], v[176:179]\n\t" \
  "ds_read_b64_tr_b16 v[148:149], v59 offset:34816\n\t" \
  "v_mfma_f32_16x16x32_bf16 v[176:179], v[96:99], v[132:135], v[176:179]\n\t" \
  "ds_read_b64_tr_b16 v[150:151], v59 offset:35328\n\t" \
  "v_mfma_f32_16x16x32_bf16 v[156:159], v[100:103], v[64:67], v[156:159]\n\t" \
  "ds_read_b64_tr_b16 v[96:97], v59 offset:38912\n\t" \
  "v_mfma_f32_16x16x32_bf16 v[156:159], v[144:147], v[104:107], v[156:159]\n\t" \
  "ds_read_b64_tr_b16 v[98:99], v59 offset:39424\n\t" \
  "v_mfma_f32_16x16x32_bf16 v[164:167], v[144:147], v[112:115], v[164:167]\n\t" \
  "ds_read_b64_tr_b16 v[64:65], v58 offset:26624\n\t" \
  "v_mfma_f32_16x16x32_bf16 v[164:167], v[100:103], v[116:119], v[164:167]\n\t" \
  "ds_read_b64_tr_b16 v[66:67], v58 offset:27136\n\t" \
  "v_mfma_f32_16x16x32_bf16 v[172:175], v[100:103], v[120:123], v[172:175]\n\t" \
  "ds_read_b64_tr_b16 v[104:105], v58 offset:30720\n\t" \
  "v_mfma_f32_16x16x32_bf16 v[172:175], v[144:147], v[124:127], v[172:175]\n\t" \
  "ds_read_b64_tr_b16 v[106:107], v58 offset:31232\n\t" \
  "v_mfma_f32_16x16x32_bf16 v[180:183], v[144:147], v[128:131], v[180:183]\n\t" \
  "ds_read_b128 v[112:115], v56 offset:0\n\t" \
  "v_mfma_f32_16x16x32_bf16 v[180:183], v[100:103], v[132:135], v[180:183]\n\t" \
  "ds_read_b128 v[116:119], v57 offset:0\n\t" \
  "s_waitcnt lgkmcnt(7)\n\t" \
  "v_mfma_f32_16x16x32_bf16 v[216:219], v[72:75], v[108:111], v[216:219]\n\t" \
  "ds_read_b64_tr_b16 v[120:121], v59 offset:26624\n\t" \
  "v_mfma_f32_16x16x32_bf16 v[216:219], v[140:143], v[136:139], v[216:219]\n\t" \
  "ds_read_b64_tr_b16 v[122:123], v59 offset:27136\n\t" \
  "v_mfma_f32_16x16x32_bf16 v[224:227], v[68:71], v[136:139], v[224:227]\n\t" \
  "ds_read_b64_tr_b16 v[124:125], v59 offset:30720\n\t" \
  "v_mfma_f32_16x16x32_bf16 v[224:227], v[76:79], v[108:111], v[224:227]\n\t" \
  "ds_read_b64_tr_b16 v[126:127], v59 offset:31232\n\t" \
  "v_mfma_f32_16x16x32_bf16 v[232:235], v[80:83], v[108:111], v[232:235]\n\t" \
  "ds_read_b64_tr_b16 v[128:129], v58 offset:18432\n\t" \
  "v_mfma_f32_16x16x32_bf16 v[232:235], v[84:87], v[136:139], v[232:235]\n\t" \
  "ds_read_b64_tr_b16 v[130:131], v58 offset:18944\n\t" \
  "v_mfma_f32_16x16x32_bf16 v[240:243], v[88:91], v[136:139], v[240:243]\n\t" \
  "ds_read_b64_tr_b16 v[144:145], v58 offset:22528\n\t" \
  "v_mfma_f32_16x16x32_bf16 v[240:243], v[92:95], v[108:111], v[240:243]\n\t" \
  "ds_read_b64_tr_b16 v[146:147], v58 offset:23040\n\t" \
  "v_mfma_f32_16x16x32_bf16 v[220:223], v[72:75], v[148:151], v[220:223]\n\t" \
  "ds_read_b64_tr_b16 v[100:101], v59 offset:18432\n\t" \
  "s_waitcnt lgkmcnt(7)\n\t" \
  "v_mfma_f32_16x16x32_bf16 v[220:223], v[140:143], v[96:99], v[220:223]\n\t" \
  "ds_read_b64_tr_b16 v[102:103], v59 offset:18944\n\t" \
  "v_mfma_f32_16x16x32_bf16 v[228:231], v[68:71], v[96:99], v[228:231]\n\t" \
  "ds_read_b64_tr_b16 v[132:133], v59 offset:22528\n\t" \
  "v_mfma_f32_16x16x32_bf16 v[228:231], v[76:79], v[148:151], v[228:231]\n\t" \
  "ds_read_b64_tr_b16 v[134:135], v59 offset:23040\n\t" \
  "v_mfma_f32_16x16x32_bf16 v[236:239], v[80:83], v[148:151], v[236:239]\n\t" \
  "v_mfma_f32_16x16x32_bf16 v[236:239], v[84:87], v[96:99], v[236:239]\n\t" \
  "v_mfma_f32_16x16x32_bf16 v[244:247], v[88:91], v[96:99], v[244:247]\n\t" \
  "v_mfma_f32_16x16x32_bf16 v[244:247], v[92:95], v[148:151], v[244:247]\n\t" \
  "v_mfma_f32_16x16x32_bf16 v[184:187], v[64:67], v[72:75], v[184:187]\n\t" \
  "v_mfma_f32_16x16x32_bf16 v[184:187], v[104:107], v[140:143], v[184:187]\n\t" \
  "v_mfma_f32_16x16x32_bf16 v[192:195], v[104:107], v[68:71], v[192:195]\n\t" \
  "v_mfma_f32_16x16x32_bf16 v[192:195], v[64:67], v[76:79], v[192:195]\n\t" \
  "v_mfma_f32_16x16x32_bf16 v[200:203], v[64:67], v[80:83], v[200:203]\n\t" \
  "v_mfma_f32_16x16x32_bf16 v[200:203], v[104:107], v[84:87], v[200:203]\n\t" \
  "v_mfma_f32_16x16x32_bf16 v[208:211], v[104:107], v[88:91], v[208:211]\n\t" \
  "v_mfma_f32_16x16x32_bf16 v[208:211], v[64:67], v[92:95], v[208:211]\n\t" \
  "503:\n\t" \
  "s_cbranch_execz 507f\n\t" \
  "v_mfma_f32_16x16x32_bf16 v[248:251], v[64:67], v[112:115], v[248:251]\n\t" \
  "v_mfma_f32_16x16x32_bf16 v[248:251], v[104:107], v[116:119], v[248:251]\n\t" \
  "v_mfma_f32_16x16x32_bf16 v[252:255], v[120:123], v[112:115], v[252:255]\n\t" \
  "s_waitcnt lgkmcnt(0)\n\t" \
  "v_mfma_f32_16x16x32_bf16 v[252:255], v[124:127], v[116:119], v[252:255]\n\t" \
  "v_mfma_f32_16x16x32_bf16 v[188:191], v[124:127], v[140:143], v[188:191]\n\t" \
  "v_mfma_f32_16x16x32_bf16 v[188:191], v[120:123], v[72:75], v[188:191]\n\t" \
  "s_barrier\n\t" \
  "v_mfma_f32_16x16x32_bf16 v[196:199], v[120:123], v[76:79], v[196:199]\n\t" \
  "ds_read_b64_tr_b16 v[136:137], v62 offset:34816\n\t" \
  "v_bfe_u32 v30, v4, 9, 1\n\t" \
  "v_mfma_f32_16x16x32_bf16 v[196:199], v[124:127], v[68:71], v[196:199]\n\t" \
  "ds_read_b64_tr_b16 v[138:139], v62 offset:35328\n\t" \
  "v_mad_i32_i24 v30, v30, -16, v0\n\t" \
  "v_mfma_f32_16x16x32_bf16 v[204:207], v[124:127], v[84:87], v[204:207]\n\t" \
  "ds_read_b128 v[108:111], v60 offset:2048\n\t" \
  "ds_read_b128 v[26:29], v0 offset:1552\n\t" \
  "v_mfma_f32_16x16x32_bf16 v[204:207], v[120:123], v[80:83], v[204:207]\n\t" \
  "ds_read_b64_tr_b16 v[96:97], v62 offset:38912\n\t" \
  "ds_read_b128 v[30:33], v30 offset:1040\n\t" \
  "v_mfma_f32_16x16x32_bf16 v[212:215], v[120:123], v[92:95], v[212:215]\n\t" \
  "ds_read_b64_tr_b16 v[98:99], v62 offset:39424\n\t" \
  "ds_read_b128 v[34:37], v0 offset:512\n\t" \
  "v_mfma_f32_16x16x32_bf16 v[212:215], v[124:127], v[88:91], v[212:215]\n\t" \
  "ds_read_b128 v[148:151], v61 offset:2048\n\t" \
  "ds_read_b64_tr_b16 v[38:39], v1 offset:512\n\t" \
  "v_mfma_f32_16x16x32_bf16 v[152:155], v[128:131], v[72:75], v[152:155]\n\t" \
  "ds_read_b128 v[64:67], v61 offset:4096\n\t" \
  "ds_read_b64_tr_b16 v[40:41], v1 offset:0\n\t" \
  "v_mfma_f32_16x16x32_bf16 v[152:155], v[144:147], v[140:143], v[152:155]\n\t" \
  "ds_read_b128 v[104:107], v60 offset:4096\n\t" \
  "s_waitcnt vmcnt(0)\n\t" \
  "v_mfma_f32_16x16x32_bf16 v[160:163], v[144:147], v[68:71], v[160:163]\n\t" \
  "ds_read_b128 v[112:115], v60 offset:6144\n\t" \
  "v_perm_b32 v18, v10, v10, v14\n\t" \
  "v_mfma_f32_16x16x32_bf16 v[160:163], v[128:131], v[76:79], v[160:163]\n\t" \
  "ds_read_b128 v[116:119], v61 offset:6144\n\t" \
  "v_perm_b32 v19, v10, v10, v15\n\t" \
  "v_mfma_f32_16x16x32_bf16 v[168:171], v[128:131], v[80:83], v[168:171]\n\t" \
  "ds_read_b128 v[120:123], v61 offset:8192\n\t" \
  "v_perm_b32 v20, v10, v10, v16\n\t" \
  "v_mfma_f32_16x16x32_bf16 v[168:171], v[144:147], v[84:87], v[168:171]\n\t" \
  "ds_read_b128 v[124:127], v60 offset:8192\n\t" \
  "v_perm_b32 v21, v10, v10, v17\n\t" \
  "v_mfma_f32_16x16x32_bf16 v[176:179], v[144:147], v[88:91], v[176:179]\n\t" \
  "ds_read_b64_tr_b16 v[144:145], v63 offset:34816\n\t" \
  "v_perm_b32 v22, v11, v11, v14\n\t" \
  "v_mfma_f32_16x16x32_bf16 v[176:179], v[128:131], v[92:95], v[176:179]\n\t" \
  "ds_read_b64_tr_b16 v[146:147], v63 offset:35328\n\t" \
  "v_perm_b32 v23, v11, v11, v15\n\t" \
  "v_mfma_f32_16x16x32_bf16 v[156:159], v[100:103], v[72:75], v[156:159]\n\t" \
  "ds_read_b64_tr_b16 v[128:129], v63 offset:38912\n\t" \
  "v_perm_b32 v24, v11, v11, v16\n\t" \
  "v_mfma_f32_16x16x32_bf16 v[156:159], v[132:135], v[140:143], v[156:159]\n\t" \
  "ds_read_b64_tr_b16 v[130:131], v63 offset:39424\n\t" \
  "v_perm_b32 v25, v11, v11, v17\n\t" \
  "v_mfma_f32_16x16x32_bf16 v[164:167], v[132:135], v[68:71], v[164:167]\n\t" \
  "ds_read_b64_tr_b16 v[72:73], v62 offset:26624\n\t" \
  "v_mfma_f32_16x16x32_bf16 v[164:167], v[100:103], v[76:79], v[164:167]\n\t" \
  "ds_read_b64_tr_b16 v[74:75], v62 offset:27136\n\t" \
  "v_mfma_f32_16x16x32_bf16 v[172:175], v[100:103], v[80:83], v[172:175]\n\t" \
  "ds_read_b64_tr_b16 v[140:141], v62 offset:30720\n\t" \
  "s_waitcnt lgkmcnt(6)\n\t" \
  "v_mfma_f32_16x16x32_bf16 v[42:45], v[26:29], v[18:21], 0\n\t" \
  "v_mfma_f32_16x16x32_bf16 v[42:45], v[30:33], v[22:25], v[42:45]\n\t" \
  "v_mfma_f32_16x16x32_bf16 v[46:49], v[30:33], v[18:21], 0\n\t" \
  "v_mfma_f32_16x16x32_bf16 v[46:49], v[34:37], v[22:25], v[46:49]\n\t" \
  "ds_read_b128 v[26:29], v0 offset:1808\n\t" \
  "ds_read_b128 v[30:33], v0 offset:1296\n\t" \
  "v_mfma_f32_16x16x32_bf16 v[172:175], v[132:135], v[84:87], v[172:175]\n\t" \
  "ds_read_b64_tr_b16 v[142:143], v62 offset:31232\n\t" \
  "ds_read_b128 v[34:37], v0 offset:768\n\t" \
  "v_mfma_f32_16x16x32_bf16 v[180:183], v[132:135], v[88:91], v[180:183]\n\t" \
  "ds_read_b128 v[68:71], v60 offset:0\n\t" \
  "v_mfma_f32_16x16x32_bf16 v[180:183], v[100:103], v[92:95], v[180:183]\n\t" \
  "ds_read_b128 v[76:79], v61 offset:0\n\t" \
  "v_mfma_f32_16x16x32_bf16 v[216:219], v[108:111], v[136:139], v[216:219]\n\t" \
  "ds_read_b64_tr_b16 v[80:81], v63 offset:26624\n\t" \
  "v_lshlrev_b32 v50, 16, v38\n\t" \
  "v_mfma_f32_16x16x32_bf16 v[216:219], v[148:151], v[96:99], v[216:219]\n\t" \
  "ds_read_b64_tr_b16 v[82:83], v63 offset:27136\n\t" \
  "v_lshlrev_b32 v51, 16, v40\n\t" \
  "v_mfma_f32_16x16x32_bf16 v[224:227], v[64:67], v[96:99], v[224:227]\n\t" \
  "ds_read_b64_tr_b16 v[84:85], v63 offset:30720\n\t" \
  "v_mul_f32 v42, v42, v50\n\t" \
  "v_mfma_f32_16x16x32_bf16 v[224:227], v[104:107], v[136:139], v[224:227]\n\t" \
  "ds_read_b64_tr_b16 v[86:87], v63 offset:31232\n\t" \
  "v_mul_f32 v46, v46, v51\n\t" \
  "v_mfma_f32_16x16x32_bf16 v[232:235], v[112:115], v[136:139], v[232:235]\n\t" \
  "ds_read_b64_tr_b16 v[88:89], v62 offset:18432\n\t" \
  "v_cvt_pk_bf16_f32 v42, v42, v46\n\t" \
  "v_mfma_f32_16x16x32_bf16 v[232:235], v[116:119], v[96:99], v[232:235]\n\t" \
  "ds_read_b64_tr_b16 v[90:91], v62 offset:18944\n\t" \
  "v_and_b32 v50, 0xffff0000, v38\n\t" \
  "v_mfma_f32_16x16x32_bf16 v[240:243], v[120:123], v[96:99], v[240:243]\n\t" \
  "ds_read_b64_tr_b16 v[132:133], v62 offset:22528\n\t" \
  "v_and_b32 v51, 0xffff0000, v40\n\t" \
  "v_mfma_f32_16x16x32_bf16 v[240:243], v[124:127], v[136:139], v[240:243]\n\t" \
  "ds_read_b64_tr_b16 v[134:135], v62 offset:23040\n\t" \
  "v_mul_f32 v43, v43, v50\n\t" \
  "s_waitcnt lgkmcnt(7)\n\t" \
  "v_mfma_f32_16x16x32_bf16 v[220:223], v[108:111], v[144:147], v[220:223]\n\t" \
  "ds_read_b64_tr_b16 v[92:93], v63 offset:18432\n\t" \
  "v_mul_f32 v47, v47, v51\n\t" \
  "v_mfma_f32_16x16x32_bf16 v[220:223], v[148:151], v[128:131], v[220:223]\n\t" \
  "ds_read_b64_tr_b16 v[94:95], v63 offset:18944\n\t" \
  "v_cvt_pk_bf16_f32 v43, v43, v47\n\t" \
  "v_mfma_f32_16x16x32_bf16 v[228:231], v[64:67], v[128:131], v[228:231]\n\t" \
  "ds_read_b64_tr_b16 v[100:101], v63 offset:22528\n\t" \
  "v_lshlrev_b32 v50, 16, v39\n\t" \
  "v_mfma_f32_16x16x32_bf16 v[228:231], v[104:107], v[144:147], v[228:231]\n\t" \
  "ds_read_b64_tr_b16 v[102:103], v63 offset:23040\n\t" \
  "v_lshlrev_b32 v51, 16, v41\n\t" \
  "v_mfma_f32_16x16x32_bf16 v[236:239], v[112:115], v[144:147], v[236:239]\n\t" \
  "v_mul_f32 v44, v44, v50\n\t" \
  "v_mul_f32 v48, v48, v51\n\t" \
  "v_mfma_f32_16x16x32_bf16 v[236:239], v[116:119], v[128:131], v[236:239]\n\t" \
  "v_cvt_pk_bf16_f32 v44, v44, v48\n\t" \
  "v_and_b32 v50, 0xffff0000, v39\n\t" \
  "v_mfma_f32_16x16x32_bf16 v[244:247], v[120:123], v[128:131], v[244:247]\n\t" \
  "v_and_b32 v51, 0xffff0000, v41\n\t" \
  "v_mul_f32 v45, v45, v50\n\t" \
  "v_mfma_f32_16x16x32_bf16 v[244:247], v[124:127], v[144:147], v[244:247]\n\t" \
  "v_mul_f32 v49, v49, v51\n\t" \
  "ds_read_b64_tr_b16 v[38:39], v1 offset:768\n\t" \
  "v_mfma_f32_16x16x32_bf16 v[184:187], v[72:75], v[108:111], v[184:187]\n\t" \
  "ds_read_b64_tr_b16 v[40:41], v1 offset:256\n\t" \
  "v_cvt_pk_bf16_f32 v45, v45, v49\n\t" \
  "v_mfma_f32_16x16x32_bf16 v[184:187], v[140:143], v[148:151], v[184:187]\n\t" \
  "v_mfma_f32_16x16x32_bf16 v[192:195], v[140:143], v[64:67], v[192:195]\n\t" \
  "v_mfma_f32_16x16x32_bf16 v[192:195], v[72:75], v[104:107], v[192:195]\n\t" \
  "v_mfma_f32_16x16x32_bf16 v[200:203], v[72:75], v[112:115], v[200:203]\n\t" \
  "v_mfma_f32_16x16x32_bf16 v[200:203], v[140:143], v[116:119], v[200:203]\n\t" \
  "v_mfma_f32_16x16x32_bf16 v[208:211], v[140:143], v[120:123], v[208:211]\n\t" \
  "v_mfma_f32_16x16x32_bf16 v[208:211], v[72:75], v[124:127], v[208:211]\n\t" \
  "v_mfma_f32_16x16x32_bf16 v[248:251], v[72:75], v[68:71], v[248:251]\n\t" \
  "v_mfma_f32_16x16x32_bf16 v[248:251], v[140:143], v[76:79], v[248:251]\n\t" \
  "s_waitcnt lgkmcnt(0)\n\t" \
  "v_mfma_f32_16x16x32_bf16 v[252:255], v[80:83], v[68:71], v[252:255]\n\t" \
  "global_store_dwordx4 v2, v[42:45], %24 offset:0 sc1\n\t" \
  "v_mfma_f32_16x16x32_bf16 v[252:255], v[84:87], v[76:79], v[252:255]\n\t" \
  "s_nop 0\n\t" \
  "v_mfma_f32_16x16x32_bf16 v[42:45], v[26:29], v[18:21], 0\n\t" \
  "v_mfma_f32_16x16x32_bf16 v[42:45], v[30:33], v[22:25], v[42:45]\n\t" \
  "v_mfma_f32_16x16x32_bf16 v[46:49], v[30:33], v[18:21], 0\n\t" \
  "v_mfma_f32_16x16x32_bf16 v[46:49], v[34:37], v[22:25], v[46:49]\n\t" \
  "v_bfe_u32 v30, v4, 9, 1\n\t" \
  "v_mad_i32_i24 v30, v30, -16, v0\n\t" \
  "v_mfma_f32_16x16x32_bf16 v[188:191], v[84:87], v[148:151], v[188:191]\n\t" \
  "ds_read_b128 v[26:29], v0 offset:1584\n\t" \
  "ds_read_b128 v[30:33], v30 offset:1072\n\t" \
  "v_mfma_f32_16x16x32_bf16 v[188:191], v[80:83], v[108:111], v[188:191]\n\t" \
  "ds_read_b128 v[34:37], v0 offset:544\n\t" \
  "v_perm_b32 v18, v12, v12, v14\n\t" \
  "s_waitcnt lgkmcnt(0)\n\t" \
  "s_barrier\n\t" \
  "v_mfma_f32_16x16x32_bf16 v[196:199], v[80:83], v[104:107], v[196:199]\n\t" \
  "ds_read_b64_tr_b16 v[96:97], v54 offset:34816\n\t" \
  "v_perm_b32 v19, v12, v12, v15\n\t" \
  "v_mfma_f32_16x16x32_bf16 v[196:199], v[84:87], v[64:67], v[196:199]\n\t" \
  "ds_read_b64_tr_b16 v[98:99], v54 offset:35328\n\t" \
  "v_perm_b32 v20, v12, v12, v16\n\t" \
  "v_mfma_f32_16x16x32_bf16 v[204:207], v[84:87], v[116:119], v[204:207]\n\t" \
  "ds_read_b128 v[136:139], v52 offset:2048\n\t" \
  "v_perm_b32 v21, v12, v12, v17\n\t" \
  "v_mfma_f32_16x16x32_bf16 v[204:207], v[80:83], v[112:115], v[204:207]\n\t" \
  "ds_read_b64_tr_b16 v[128:129], v54 offset:38912\n\t" \
  "v_perm_b32 v22, v13, v13, v14\n\t" \
  "v_mfma_f32_16x16x32_bf16 v[212:215], v[80:83], v[124:127], v[212:215]\n\t" \
  "ds_read_b64_tr_b16 v[130:131], v54 offset:39424\n\t" \
  "v_perm_b32 v23, v13, v13, v15\n\t" \
  "v_mfma_f32_16x16x32_bf16 v[212:215], v[84:87], v[120:123], v[212:215]\n\t" \
  "ds_read_b128 v[144:147], v53 offset:2048\n\t" \
  "v_perm_b32 v24, v13, v13, v16\n\t" \
  "v_mfma_f32_16x16x32_bf16 v[152:155], v[88:91], v[108:111], v[152:155]\n\t" \
  "ds_read_b128 v[72:75], v53 offset:4096\n\t" \
  "v_perm_b32 v25, v13, v13, v17\n\t" \
  "v_mfma_f32_16x16x32_bf16 v[152:155], v[132:135], v[148:151], v[152:155]\n\t" \
  "ds_read_b128 v[140:143], v52 offset:4096\n\t" \
  "v_lshlrev_b32 v50, 16, v38\n\t" \
  "v_mfma_f32_16x16x32_bf16 v[160:163], v[132:135], v[64:67], v[160:163]\n\t" \
  "ds_read_b128 v[68:71], v52 offset:6144\n\t" \
  "v_lshlrev_b32 v51, 16, v40\n\t" \
  "v_mfma_f32_16x16x32_bf16 v[160:163], v[88:91], v[104:107], v[160:163]\n\t" \
  "ds_read_b128 v[76:79], v53 offset:6144\n\t" \
  "v_mul_f32 v42, v42, v50\n\t" \
  "v_mfma_f32_16x16x32_bf16 v[168:171], v[88:91], v[112:115], v[168:171]\n\t" \
  "ds_read_b128 v[80:83], v53 offset:8192\n\t" \
  "v_mul_f32 v46, v46, v51\n\t" \
  "v_mfma_f32_16x16x32_bf16 v[168:171], v[132:135], v[116:119], v[168:171]\n\t" \
  "ds_read_b128 v[84:87], v52 offset:8192\n\t" \
  "v_cvt_pk_bf16_f32 v42, v42, v46\n\t" \
  "v_mfma_f32_16x16x32_bf16 v[176:179], v[132:135], v[120:123], v[176:179]\n\t" \
  "ds_read_b64_tr_b16 v[132:133], v55 offset:34816\n\t" \
  "v_and_b32 v50, 0xffff0000, v38\n\t" \
  "v_mfma_f32_16x16x32_bf16 v[176:179], v[88:91], v[124:127], v[176:179]\n\t" \
  "ds_read_b64_tr_b16 v[134:135], v55 offset:35328\n\t" \
  "v_and_b32 v51, 0xffff0000, v40\n\t" \
  "v_mfma_f32_16x16x32_bf16 v[156:159], v[92:95], v[108:111], v[156:159]\n\t" \
  "ds_read_b64_tr_b16 v[88:89], v55 offset:38912\n\t" \
  "v_mul_f32 v43, v43, v50\n\t" \
  "v_mfma_f32_16x16x32_bf16 v[156:159], v[100:103], v[148:151], v[156:159]\n\t" \
  "ds_read_b64_tr_b16 v[90:91], v55 offset:39424\n\t" \
  "v_mul_f32 v47, v47, v51\n\t" \
  "v_mfma_f32_16x16x32_bf16 v[164:167], v[100:103], v[64:67], v[164:167]\n\t" \
  "ds_read_b64_tr_b16 v[108:109], v54 offset:26624\n\t" \
  "v_cvt_pk_bf16_f32 v43, v43, v47\n\t" \
  "v_mfma_f32_16x16x32_bf16 v[164:167], v[92:95], v[104:107], v[164:167]\n\t" \
  "ds_read_b64_tr_b16 v[110:111], v54 offset:27136\n\t" \
  "v_lshlrev_b32 v50, 16, v39\n\t" \
  "v_mfma_f32_16x16x32_bf16 v[172:175], v[92:95], v[112:115], v[172:175]\n\t" \
  "ds_read_b64_tr_b16 v[148:149], v54 offset:30720\n\t" \
  "v_lshlrev_b32 v51, 16, v41\n\t" \
  "v_mfma_f32_16x16x32_bf16 v[172:175], v[100:103], v[116:119], v[172:175]\n\t" \
  "ds_read_b64_tr_b16 v[150:151], v54 offset:31232\n\t" \
  "v_mul_f32 v44, v44, v50\n\t" \
  "v_mfma_f32_16x16x32_bf16 v[180:183], v[100:103], v[120:123], v[180:183]\n\t" \
  "ds_read_b128 v[64:67], v52 offset:0\n\t" \
  "v_mul_f32 v48, v48, v51\n\t" \
  "v_mfma_f32_16x16x32_bf16 v[180:183], v[92:95], v[124:127], v[180:183]\n\t" \
  "ds_read_b128 v[104:107], v53 offset:0\n\t" \
  "v_cvt_pk_bf16_f32 v44, v44, v48\n\t" \
  "s_waitcnt lgkmcnt(7)\n\t" \
  "v_mfma_f32_16x16x32_bf16 v[216:219], v[136:139], v[96:99], v[216:219]\n\t" \
  "ds_read_b64_tr_b16 v[112:113], v55 offset:26624\n\t" \
  "v_and_b32 v50, 0xffff0000, v39\n\t" \
  "v_mfma_f32_16x16x32_bf16 v[216:219], v[144:147], v[128:131], v[216:219]\n\t" \
  "ds_read_b64_tr_b16 v[114:115], v55 offset:27136\n\t" \
  "v_and_b32 v51, 0xffff0000, v41\n\t" \
  "v_mfma_f32_16x16x32_bf16 v[224:227], v[72:75], v[128:131], v[224:227]\n\t" \
  "ds_read_b64_tr_b16 v[116:117], v55 offset:30720\n\t" \
  "v_mul_f32 v45, v45, v50\n\t" \
  "v_mfma_f32_16x16x32_bf16 v[224:227], v[140:143], v[96:99], v[224:227]\n\t" \
  "ds_read_b64_tr_b16 v[118:119], v55 offset:31232\n\t" \
  "v_mul_f32 v49, v49, v51\n\t" \
  "v_mfma_f32_16x16x32_bf16 v[232:235], v[68:71], v[96:99], v[232:235]\n\t" \
  "ds_read_b64_tr_b16 v[100:101], v54 offset:18432\n\t" \
  "ds_read_b64_tr_b16 v[38:39], v1 offset:544\n\t" \
  "v_mfma_f32_16x16x32_bf16 v[232:235], v[76:79], v[128:131], v[232:235]\n\t" \
  "ds_read_b64_tr_b16 v[102:103], v54 offset:18944\n\t" \
  "ds_read_b64_tr_b16 v[40:41], v1 offset:32\n\t" \
  "v_mfma_f32_16x16x32_bf16 v[240:243], v[80:83], v[128:131], v[240:243]\n\t" \
  "ds_read_b64_tr_b16 v[120:121], v54 offset:22528\n\t" \
  "v_cvt_pk_bf16_f32 v45, v45, v49\n\t" \
  "v_mfma_f32_16x16x32_bf16 v[240:243], v[84:87], v[96:99], v[240:243]\n\t" \
  "ds_read_b64_tr_b16 v[122:123], v54 offset:23040\n\t" \
  "v_mfma_f32_16x16x32_bf16 v[220:223], v[136:139], v[132:135], v[220:223]\n\t" \
  "ds_read_b64_tr_b16 v[92:93], v55 offset:18432\n\t" \
  "s_waitcnt lgkmcnt(9)\n\t" \
  "v_mfma_f32_16x16x32_bf16 v[220:223], v[144:147], v[88:91], v[220:223]\n\t" \
  "ds_read_b64_tr_b16 v[94:95], v55 offset:18944\n\t" \
  "v_mfma_f32_16x16x32_bf16 v[228:231], v[72:75], v[88:91], v[228:231]\n\t" \
  "ds_read_b64_tr_b16 v[124:125], v55 offset:22528\n\t" \
  "v_mfma_f32_16x16x32_bf16 v[228:231], v[140:143], v[132:135], v[228:231]\n\t" \
  "ds_read_b64_tr_b16 v[126:127], v55 offset:23040\n\t" \
  "v_mfma_f32_16x16x32_bf16 v[236:239], v[68:71], v[132:135], v[236:239]\n\t" \
  "v_mfma_f32_16x16x32_bf16 v[236:239], v[76:79], v[88:91], v[236:239]\n\t" \
  "v_mfma_f32_16x16x32_bf16 v[244:247], v[80:83], v[88:91], v[244:247]\n\t" \
  "v_mfma_f32_16x16x32_bf16 v[244:247], v[84:87], v[132:135], v[244:247]\n\t" \
  "v_mfma_f32_16x16x32_bf16 v[184:187], v[108:111], v[136:139], v[184:187]\n\t" \
  "v_mfma_f32_16x16x32_bf16 v[184:187], v[148:151], v[144:147], v[184:187]\n\t" \
  "v_mfma_f32_16x16x32_bf16 v[192:195], v[148:151], v[72:75], v[192:195]\n\t" \
  "v_mfma_f32_16x16x32_bf16 v[192:195], v[108:111], v[140:143], v[192:195]\n\t" \
  "v_mfma_f32_16x16x32_bf16 v[200:203], v[108:111], v[68:71], v[200:203]\n\t" \
  "v_mfma_f32_16x16x32_bf16 v[200:203], v[148:151], v[76:79], v[200:203]\n\t" \
  "v_mfma_f32_16x16x32_bf16 v[208:211], v[148:151], v[80:83], v[208:211]\n\t" \
  "v_mfma_f32_16x16x32_bf16 v[208:211], v[108:111], v[84:87], v[208:211]\n\t" \
  "v_mfma_f32_16x16x32_bf16 v[248:251], v[108:111], v[64:67], v[248:251]\n\t" \
  "v_mfma_f32_16x16x32_bf16 v[248:251], v[148:151], v[104:107], v[248:251]\n\t" \
  "v_mfma_f32_16x16x32_bf16 v[252:255], v[112:115], v[64:67], v[252:255]\n\t" \
  "global_store_dwordx4 v2, v[42:45], %24 offset:1024 sc1\n\t" \
  "v_add_u32 v2, 0x10000, v2\n\t" \
  "s_waitcnt lgkmcnt(0)\n\t" \
  "v_mfma_f32_16x16x32_bf16 v[252:255], v[116:119], v[104:107], v[252:255]\n\t" \
  "v_mfma_f32_16x16x32_bf16 v[188:191], v[116:119], v[144:147], v[188:191]\n\t" \
  "v_mfma_f32_16x16x32_bf16 v[42:45], v[26:29], v[18:21], 0\n\t" \
  "v_mfma_f32_16x16x32_bf16 v[42:45], v[30:33], v[22:25], v[42:45]\n\t" \
  "v_mfma_f32_16x16x32_bf16 v[46:49], v[30:33], v[18:21], 0\n\t" \
  "v_mfma_f32_16x16x32_bf16 v[46:49], v[34:37], v[22:25], v[46:49]\n\t" \
  "ds_read_b128 v[26:29], v0 offset:1840\n\t" \
  "ds_read_b128 v[30:33], v0 offset:1328\n\t" \
  "v_mfma_f32_16x16x32_bf16 v[188:191], v[112:115], v[136:139], v[188:191]\n\t" \
  "ds_read_b128 v[34:37], v0 offset:800\n\t" \
  "s_waitcnt lgkmcnt(0)\n\t" \
  "s_barrier\n\t" \
  "v_mfma_f32_16x16x32_bf16 v[196:199], v[112:115], v[140:143], v[196:199]\n\t" \
  "ds_read_b64_tr_b16 v[128:129], v58 offset:34816\n\t" \
  "v_mfma_f32_16x16x32_bf16 v[196:199], v[116:119], v[72:75], v[196:199]\n\t" \
  "ds_read_b64_tr_b16 v[130:131], v58 offset:35328\n\t" \
  "v_lshlrev_b32 v50, 16, v38\n\t" \
  "v_mfma_f32_16x16x32_bf16 v[204:207], v[116:119], v[76:79], v[204:207]\n\t" \
  "ds_read_b128 v[96:99], v56 offset:2048\n\t" \
  "v_lshlrev_b32 v51, 16, v40\n\t" \
  "v_mfma_f32_16x16x32_bf16 v[204:207], v[112:115], v[68:71], v[204:207]\n\t" \
  "ds_read_b64_tr_b16 v[88:89], v58 offset:38912\n\t" \
  "v_mul_f32 v42, v42, v50\n\t" \
  "v_mfma_f32_16x16x32_bf16 v[212:215], v[112:115], v[84:87], v[212:215]\n\t" \
  "ds_read_b64_tr_b16 v[90:91], v58 offset:39424\n\t" \
  "v_mul_f32 v46, v46, v51\n\t" \
  "v_mfma_f32_16x16x32_bf16 v[212:215], v[116:119], v[80:83], v[212:215]\n\t" \
  "ds_read_b128 v[132:135], v57 offset:2048\n\t" \
  "v_cvt_pk_bf16_f32 v42, v42, v46\n\t" \
  "v_mfma_f32_16x16x32_bf16 v[152:155], v[100:103], v[136:139], v[152:155]\n\t" \
  "ds_read_b128 v[108:111], v57 offset:4096\n\t" \
  "v_and_b32 v50, 0xffff0000, v38\n\t" \
  "v_mfma_f32_16x16x32_bf16 v[152:155], v[120:123], v[144:147], v[152:155]\n\t" \
  "ds_read_b128 v[148:151], v56 offset:4096\n\t" \
  "v_and_b32 v51, 0xffff0000, v40\n\t" \
  "v_mfma_f32_16x16x32_bf16 v[160:163], v[120:123], v[72:75], v[160:163]\n\t" \
  "ds_read_b128 v[64:67], v56 offset:6144\n\t" \
  "v_mul_f32 v43, v43, v50\n\t" \
  "v_mfma_f32_16x16x32_bf16 v[160:163], v[100:103], v[140:143], v[160:163]\n\t" \
  "ds_read_b128 v[104:107], v57 offset:6144\n\t" \
  "v_mul_f32 v47, v47, v51\n\t" \
  "v_mfma_f32_16x16x32_bf16 v[168:171], v[100:103], v[68:71], v[168:171]\n\t" \
  "ds_read_b128 v[112:115], v57 offset:8192\n\t" \
  "v_cvt_pk_bf16_f32 v43, v43, v47\n\t" \
  "v_mfma_f32_16x16x32_bf16 v[168:171], v[120:123], v[76:79], v[168:171]\n\t" \
  "ds_read_b128 v[116:119], v56 offset:8192\n\t" \
  "v_lshlrev_b32 v50, 16, v39\n\t" \
  "v_mfma_f32_16x16x32_bf16 v[176:179], v[120:123], v[80:83], v[176:179]\n\t" \
  "ds_read_b64_tr_b16 v[120:121], v59 offset:34816\n\t" \
  "v_lshlrev_b32 v51, 16, v41\n\t" \
  "v_mfma_f32_16x16x32_bf16 v[176:179], v[100:103], v[84:87], v[176:179]\n\t" \
  "ds_read_b64_tr_b16 v[122:123], v59 offset:35328\n\t" \
  "v_mul_f32 v44, v44, v50\n\t" \
  "v_mfma_f32_16x16x32_bf16 v[156:159], v[92:95], v[136:139], v[156:159]\n\t" \
  "ds_read_b64_tr_b16 v[100:101], v59 offset:38912\n\t" \
  "v_mul_f32 v48, v48, v51\n\t" \
  "v_mfma_f32_16x16x32_bf16 v[156:159], v[124:127], v[144:147], v[156:159]\n\t" \
  "ds_read_b64_tr_b16 v[102:103], v59 offset:39424\n\t" \
  "v_cvt_pk_bf16_f32 v44, v44, v48\n\t" \
  "v_mfma_f32_16x16x32_bf16 v[164:167], v[124:127], v[72:75], v[164:167]\n\t" \
  "ds_read_b64_tr_b16 v[136:137], v58 offset:26624\n\t" \
  "v_and_b32 v50, 0xffff0000, v39\n\t" \
  "v_mfma_f32_16x16x32_bf16 v[164:167], v[92:95], v[140:143], v[164:167]\n\t" \
  "ds_read_b64_tr_b16 v[138:139], v58 offset:27136\n\t" \
  "v_and_b32 v51, 0xffff0000, v41\n\t" \
  "v_mfma_f32_16x16x32_bf16 v[172:175], v[92:95], v[68:71], v[172:175]\n\t" \
  "ds_read_b64_tr_b16 v[144:145], v58 offset:30720\n\t" \
  "v_mul_f32 v45, v45, v50\n\t" \
  "v_mfma_f32_16x16x32_bf16 v[172:175], v[124:127], v[76:79], v[172:175]\n\t" \
  "ds_read_b64_tr_b16 v[146:147], v58 offset:31232\n\t" \
  "v_mul_f32 v49, v49, v51\n\t" \
  "v_mfma_f32_16x16x32_bf16 v[180:183], v[124:127], v[80:83], v[180:183]\n\t" \
  "ds_read_b128 v[72:75], v56 offset:0\n\t" \
  "ds_read_b64_tr_b16 v[38:39], v1 offset:800\n\t" \
  "v_mfma_f32_16x16x32_bf16 v[180:183], v[92:95], v[84:87], v[180:183]\n\t" \
  "ds_read_b128 v[140:143], v57 offset:0\n\t" \
  "ds_read_b64_tr_b16 v[40:41], v1 offset:288\n\t" \
  "s_waitcnt lgkmcnt(9)\n\t" \
  "v_mfma_f32_16x16x32_bf16 v[216:219], v[96:99], v[128:131], v[216:219]\n\t" \
  "ds_read_b64_tr_b16 v[68:69], v59 offset:26624\n\t" \
  "v_cvt_pk_bf16_f32 v45, v45, v49\n\t" \
  "v_mfma_f32_16x16x32_bf16 v[216:219], v[132:135], v[88:91], v[216:219]\n\t" \
  "ds_read_b64_tr_b16 v[70:71], v59 offset:27136\n\t" \
  "v_mfma_f32_16x16x32_bf16 v[224:227], v[108:111], v[88:91], v[224:227]\n\t" \
  "ds_read_b64_tr_b16 v[76:77], v59 offset:30720\n\t" \
  "v_mfma_f32_16x16x32_bf16 v[224:227], v[148:151], v[128:131], v[224:227]\n\t" \
  "ds_read_b64_tr_b16 v[78:79], v59 offset:31232\n\t" \
  "v_mfma_f32_16x16x32_bf16 v[232:235], v[64:67], v[128:131], v[232:235]\n\t" \
  "ds_read_b64_tr_b16 v[80:81], v58 offset:18432\n\t" \
  "v_mfma_f32_16x16x32_bf16 v[232:235], v[104:107], v[88:91], v[232:235]\n\t" \
  "ds_read_b64_tr_b16 v[82:83], v58 offset:18944\n\t" \
  "v_mfma_f32_16x16x32_bf16 v[240:243], v[112:115], v[88:91], v[240:243]\n\t" \
  "ds_read_b64_tr_b16 v[124:125], v58 offset:22528\n\t" \
  "v_mfma_f32_16x16x32_bf16 v[240:243], v[116:119], v[128:131], v[240:243]\n\t" \
  "ds_read_b64_tr_b16 v[126:127], v58 offset:23040\n\t" \
  "v_mfma_f32_16x16x32_bf16 v[220:223], v[96:99], v[120:123], v[220:223]\n\t" \
  "ds_read_b64_tr_b16 v[84:85], v59 offset:18432\n\t" \
  "s_waitcnt lgkmcnt(7)\n\t" \
  "v_mfma_f32_16x16x32_bf16 v[220:223], v[132:135], v[100:103], v[220:223]\n\t" \
  "ds_read_b64_tr_b16 v[86:87], v59 offset:18944\n\t" \
  "v_mfma_f32_16x16x32_bf16 v[228:231], v[108:111], v[100:103], v[228:231]\n\t" \
  "ds_read_b64_tr_b16 v[92:93], v59 offset:22528\n\t" \
  "v_mfma_f32_16x16x32_bf16 v[228:231], v[148:151], v[120:123], v[228:231]\n\t" \
  "ds_read_b64_tr_b16 v[94:95], v59 offset:23040\n\t" \
  "v_mfma_f32_16x16x32_bf16 v[236:239], v[64:67], v[120:123], v[236:239]\n\t" \
  "v_mfma_f32_16x16x32_bf16 v[236:239], v[104:107], v[100:103], v[236:239]\n\t" \
  "v_mfma_f32_16x16x32_bf16 v[244:247], v[112:115], v[100:103], v[244:247]\n\t" \
  "v_mfma_f32_16x16x32_bf16 v[244:247], v[116:119], v[120:123], v[244:247]\n\t" \
  "v_mfma_f32_16x16x32_bf16 v[184:187], v[136:139], v[96:99], v[184:187]\n\t" \
  "v_mfma_f32_16x16x32_bf16 v[184:187], v[144:147], v[132:135], v[184:187]\n\t" \
  "v_mfma_f32_16x16x32_bf16 v[192:195], v[144:147], v[108:111], v[192:195]\n\t" \
  "v_mfma_f32_16x16x32_bf16 v[192:195], v[136:139], v[148:151], v[192:195]\n\t" \
  "v_mfma_f32_16x16x32_bf16 v[200:203], v[136:139], v[64:67], v[200:203]\n\t" \
  "v_mfma_f32_16x16x32_bf16 v[200:203], v[144:147], v[104:107], v[200:203]\n\t" \
  "v_mfma_f32_16x16x32_bf16 v[208:211], v[144:147], v[112:115], v[208:211]\n\t" \
  "v_mfma_f32_16x16x32_bf16 v[208:211], v[136:139], v[116:119], v[208:211]\n\t" \
  "v_mfma_f32_16x16x32_bf16 v[248:251], v[136:139], v[72:75], v[248:251]\n\t" \
  "v_mfma_f32_16x16x32_bf16 v[248:251], v[144:147], v[140:143], v[248:251]\n\t" \
  "v_mfma_f32_16x16x32_bf16 v[252:255], v[68:71], v[72:75], v[252:255]\n\t" \
  "global_store_dwordx4 v2, v[42:45], %24 offset:0 sc1\n\t" \
  "s_waitcnt lgkmcnt(0)\n\t" \
  "v_mfma_f32_16x16x32_bf16 v[252:255], v[76:79], v[140:143], v[252:255]\n\t" \
  "v_mfma_f32_16x16x32_bf16 v[42:45], v[26:29], v[18:21], 0\n\t" \
  "v_mfma_f32_16x16x32_bf16 v[42:45], v[30:33], v[22:25], v[42:45]\n\t" \
  "v_mfma_f32_16x16x32_bf16 v[46:49], v[30:33], v[18:21], 0\n\t" \
  "v_mfma_f32_16x16x32_bf16 v[46:49], v[34:37], v[22:25], v[46:49]\n\t" \
  "v_mfma_f32_16x16x32_bf16 v[188:191], v[76:79], v[132:135], v[188:191]\n\t" \
  "v_mfma_f32_16x16x32_bf16 v[188:191], v[68:71], v[96:99], v[188:191]\n\t" \
  "v_lshlrev_b32 v50, 16, v38\n\t" \
  "v_lshlrev_b32 v51, 16, v40\n\t" \
  "s_barrier\n\t" \
  "v_mfma_f32_16x16x32_bf16 v[196:199], v[68:71], v[148:151], v[196:199]\n\t" \
  "ds_read_b64_tr_b16 v[88:89], v62 offset:34816\n\t" \
  "v_mul_f32 v42, v42, v50\n\t" \
  "v_mfma_f32_16x16x32_bf16 v[196:199], v[76:79], v[108:111], v[196:199]\n\t" \
  "ds_read_b64_tr_b16 v[90:91], v62 offset:35328\n\t" \
  "v_mul_f32 v46, v46, v51\n\t" \
  "v_mfma_f32_16x16x32_bf16 v[204:207], v[76:79], v[104:107], v[204:207]\n\t" \
  "ds_read_b128 v[128:131], v60 offset:2048\n\t" \
  "v_cvt_pk_bf16_f32 v42, v42, v46\n\t" \
  "v_mfma_f32_16x16x32_bf16 v[204:207], v[68:71], v[64:67], v[204:207]\n\t" \
  "ds_read_b64_tr_b16 v[100:101], v62 offset:38912\n\t" \
  "v_and_b32 v50, 0xffff0000, v38\n\t" \
  "v_mfma_f32_16x16x32_bf16 v[212:215], v[68:71], v[116:119], v[212:215]\n\t" \
  "ds_read_b64_tr_b16 v[102:103], v62 offset:39424\n\t" \
  "v_and_b32 v51, 0xffff0000, v40\n\t" \
  "v_mfma_f32_16x16x32_bf16 v[212:215], v[76:79], v[112:115], v[212:215]\n\t" \
  "ds_read_b128 v[120:123], v61 offset:2048\n\t" \
  "v_mul_f32 v43, v43, v50\n\t" \
  "v_mfma_f32_16x16x32_bf16 v[152:155], v[80:83], v[96:99], v[152:155]\n\t" \
  "ds_read_b128 v[136:139], v61 offset:4096\n\t" \
  "v_mul_f32 v47, v47, v51\n\t" \
  "v_mfma_f32_16x16x32_bf16 v[152:155], v[124:127], v[132:135], v[152:155]\n\t" \
  "ds_read_b128 v[144:147], v60 offset:4096\n\t" \
  "v_cvt_pk_bf16_f32 v43, v43, v47\n\t" \
  "v_mfma_f32_16x16x32_bf16 v[160:163], v[124:127], v[108:111], v[160:163]\n\t" \
  "ds_read_b128 v[72:75], v60 offset:6144\n\t" \
  "v_lshlrev_b32 v50, 16, v39\n\t" \
  "v_mfma_f32_16x16x32_bf16 v[160:163], v[80:83], v[148:151], v[160:163]\n\t" \
  "ds_read_b128 v[140:143], v61 offset:6144\n\t" \
  "v_lshlrev_b32 v51, 16, v41\n\t" \
  "v_mfma_f32_16x16x32_bf16 v[168:171], v[80:83], v[64:67], v[168:171]\n\t" \
  "ds_read_b128 v[68:71], v61 offset:8192\n\t" \
  "v_mul_f32 v44, v44, v50\n\t" \
  "v_mfma_f32_16x16x32_bf16 v[168:171], v[124:127], v[104:107], v[168:171]\n\t" \
  "ds_read_b128 v[76:79], v60 offset:8192\n\t" \
  "v_mul_f32 v48, v48, v51\n\t" \
  "v_mfma_f32_16x16x32_bf16 v[176:179], v[124:127], v[112:115], v[176:179]\n\t" \
  "ds_read_b64_tr_b16 v[124:125], v63 offset:34816\n\t" \
  "v_cvt_pk_bf16_f32 v44, v44, v48\n\t" \
  "v_mfma_f32_16x16x32_bf16 v[176:179], v[80:83], v[116:119], v[176:179]\n\t" \
  "ds_read_b64_tr_b16 v[126:127], v63 offset:35328\n\t" \
  "v_and_b32 v50, 0xffff0000, v39\n\t" \
  "v_mfma_f32_16x16x32_bf16 v[156:159], v[84:87], v[96:99], v[156:159]\n\t" \
  "ds_read_b64_tr_b16 v[80:81], v63 offset:38912\n\t" \
  "v_and_b32 v51, 0xffff0000, v41\n\t" \
  "v_mfma_f32_16x16x32_bf16 v[156:159], v[92:95], v[132:135], v[156:159]\n\t" \
  "ds_read_b64_tr_b16 v[82:83], v63 offset:39424\n\t" \
  "v_mul_f32 v45, v45, v50\n\t" \
  "v_mfma_f32_16x16x32_bf16 v[164:167], v[92:95], v[108:111], v[164:167]\n\t" \
  "ds_read_b64_tr_b16 v[96:97], v62 offset:26624\n\t" \
  "v_mul_f32 v49, v49, v51\n\t" \
  "v_mfma_f32_16x16x32_bf16 v[164:167], v[84:87], v[148:151], v[164:167]\n\t" \
  "ds_read_b64_tr_b16 v[98:99], v62 offset:27136\n\t" \
  "v_cvt_pk_bf16_f32 v45, v45, v49\n\t" \
  "v_mfma_f32_16x16x32_bf16 v[172:175], v[84:87], v[64:67], v[172:175]\n\t" \
  "ds_read_b64_tr_b16 v[132:133], v62 offset:30720\n\t" \
  "v_mfma_f32_16x16x32_bf16 v[172:175], v[92:95], v[104:107], v[172:175]\n\t" \
  "ds_read_b64_tr_b16 v[134:135], v62 offset:31232\n\t" \
  "v_mfma_f32_16x16x32_bf16 v[180:183], v[92:95], v[112:115], v[180:183]\n\t" \
  "ds_read_b128 v[108:111], v60 offset:0\n\t" \
  "v_mfma_f32_16x16x32_bf16 v[180:183], v[84:87], v[116:119], v[180:183]\n\t" \
  "ds_read_b128 v[148:151], v61 offset:0\n\t" \
  "s_waitcnt lgkmcnt(7)\n\t" \
  "v_mfma_f32_16x16x32_bf16 v[216:219], v[128:131], v[88:91], v[216:219]\n\t" \
  "ds_read_b64_tr_b16 v[64:65], v63 offset:26624\n\t" \
  "v_mfma_f32_16x16x32_bf16 v[216:219], v[120:123], v[100:103], v[216:219]\n\t" \
  "ds_read_b64_tr_b16 v[66:67], v63 offset:27136\n\t" \
  "v_mfma_f32_16x16x32_bf16 v[224:227], v[136:139], v[100:103], v[224:227]\n\t" \
  "ds_read_b64_tr_b16 v[104:105], v63 offset:30720\n\t" \
  "v_mfma_f32_16x16x32_bf16 v[224:227], v[144:147], v[88:91], v[224:227]\n\t" \
  "ds_read_b64_tr_b16 v[106:107], v63 offset:31232\n\t" \
  "v_mfma_f32_16x16x32_bf16 v[232:235], v[72:75], v[88:91], v[232:235]\n\t" \
  "ds_read_b64_tr_b16 v[92:93], v62 offset:18432\n\t" \
  "v_mfma_f32_16x16x32_bf16 v[232:235], v[140:143], v[100:103], v[232:235]\n\t" \
  "ds_read_b64_tr_b16 v[94:95], v62 offset:18944\n\t" \
  "v_mfma_f32_16x16x32_bf16 v[240:243], v[68:71], v[100:103], v[240:243]\n\t" \
  "ds_read_b64_tr_b16 v[112:113], v62 offset:22528\n\t" \
  "v_mfma_f32_16x16x32_bf16 v[240:243], v[76:79], v[88:91], v[240:243]\n\t" \
  "ds_read_b64_tr_b16 v[114:115], v62 offset:23040\n\t" \
  "v_mfma_f32_16x16x32_bf16 v[220:223], v[128:131], v[124:127], v[220:223]\n\t" \
  "ds_read_b64_tr_b16 v[84:85], v63 offset:18432\n\t" \
  "s_waitcnt lgkmcnt(7)\n\t" \
  "v_mfma_f32_16x16x32_bf16 v[220:223], v[120:123], v[80:83], v[220:223]\n\t" \
  "ds_read_b64_tr_b16 v[86:87], v63 offset:18944\n\t" \
  "v_mfma_f32_16x16x32_bf16 v[228:231], v[136:139], v[80:83], v[228:231]\n\t" \
  "ds_read_b64_tr_b16 v[116:117], v63 offset:22528\n\t" \
  "v_mfma_f32_16x16x32_bf16 v[228:231], v[144:147], v[124:127], v[228:231]\n\t" \
  "ds_read_b64_tr_b16 v[118:119], v63 offset:23040\n\t" \
  "v_mfma_f32_16x16x32_bf16 v[236:239], v[72:75], v[124:127], v[236:239]\n\t" \
  "v_mfma_f32_16x16x32_bf16 v[236:239], v[140:143], v[80:83], v[236:239]\n\t" \
  "v_mfma_f32_16x16x32_bf16 v[244:247], v[68:71], v[80:83], v[244:247]\n\t" \
  "v_mfma_f32_16x16x32_bf16 v[244:247], v[76:79], v[124:127], v[244:247]\n\t" \
  "v_mfma_f32_16x16x32_bf16 v[184:187], v[96:99], v[128:131], v[184:187]\n\t" \
  "v_mfma_f32_16x16x32_bf16 v[184:187], v[132:135], v[120:123], v[184:187]\n\t" \
  "v_mfma_f32_16x16x32_bf16 v[192:195], v[132:135], v[136:139], v[192:195]\n\t" \
  "v_mfma_f32_16x16x32_bf16 v[192:195], v[96:99], v[144:147], v[192:195]\n\t" \
  "v_mfma_f32_16x16x32_bf16 v[200:203], v[96:99], v[72:75], v[200:203]\n\t" \
  "v_mfma_f32_16x16x32_bf16 v[200:203], v[132:135], v[140:143], v[200:203]\n\t" \
  "v_mfma_f32_16x16x32_bf16 v[208:211], v[132:135], v[68:71], v[208:211]\n\t" \
  "v_mfma_f32_16x16x32_bf16 v[208:211], v[96:99], v[76:79], v[208:211]\n\t" \
  "v_mfma_f32_16x16x32_bf16 v[248:251], v[96:99], v[108:111], v[248:251]\n\t" \
  "v_mfma_f32_16x16x32_bf16 v[248:251], v[132:135], v[148:151], v[248:251]\n\t" \
  "v_mfma_f32_16x16x32_bf16 v[252:255], v[64:67], v[108:111], v[252:255]\n\t" \
  "global_store_dwordx4 v2, v[42:45], %24 offset:1024 sc1\n\t" \
  "s_waitcnt lgkmcnt(0)\n\t" \
  "v_mfma_f32_16x16x32_bf16 v[252:255], v[104:107], v[148:151], v[252:255]\n\t" \
  "v_mfma_f32_16x16x32_bf16 v[188:191], v[104:107], v[120:123], v[188:191]\n\t" \
  "v_mfma_f32_16x16x32_bf16 v[188:191], v[64:67], v[128:131], v[188:191]\n\t" \
  "v_mov_b32 v30, 0xff61b1e6\n\t" \
  "v_bfe_i32 v34, v3, 0, 1\n\t" \
  "s_barrier\n\t" \
  "v_mfma_f32_16x16x32_bf16 v[196:199], v[64:67], v[144:147], v[196:199]\n\t" \
  "ds_read_b64_tr_b16 v[100:101], v54 offset:34816\n\t" \
  "v_and_b32 v34, v30, v34\n\t" \
  "v_mfma_f32_16x16x32_bf16 v[196:199], v[104:107], v[136:139], v[196:199]\n\t" \
  "ds_read_b64_tr_b16 v[102:103], v54 offset:35328\n\t" \
  "v_bfe_i32 v38, v3, 4, 1\n\t" \
  "v_mfma_f32_16x16x32_bf16 v[204:207], v[104:107], v[140:143], v[204:207]\n\t" \
  "ds_read_b128 v[88:91], v52 offset:2048\n\t" \
  "v_and_b32 v38, v30, v38\n\t" \
  "v_mfma_f32_16x16x32_bf16 v[204:207], v[64:67], v[72:75], v[204:207]\n\t" \
  "ds_read_b64_tr_b16 v[80:81], v54 offset:38912\n\t" \
  "v_bfe_i32 v42, v3, 8, 1\n\t" \
  "v_mfma_f32_16x16x32_bf16 v[212:215], v[64:67], v[76:79], v[212:215]\n\t" \
  "ds_read_b64_tr_b16 v[82:83], v54 offset:39424\n\t" \
  "v_bfi_b32 v42, v42, v30, %15\n\t" \
  "v_mfma_f32_16x16x32_bf16 v[212:215], v[104:107], v[68:71], v[212:215]\n\t" \
  "ds_read_b128 v[124:127], v53 offset:2048\n\t" \
  "v_mov_b32 v48, 0x3f803f80\n\t" \
  "v_mfma_f32_16x16x32_bf16 v[152:155], v[92:95], v[128:131], v[152:155]\n\t" \
  "ds_read_b128 v[96:99], v53 offset:4096\n\t" \
  "v_bfe_i32 v35, v3, 1, 1\n\t" \
  "v_mfma_f32_16x16x32_bf16 v[152:155], v[112:115], v[120:123], v[152:155]\n\t" \
  "ds_read_b128 v[132:135], v52 offset:4096\n\t" \
  "v_and_b32 v35, v30, v35\n\t" \
  "v_mfma_f32_16x16x32_bf16 v[160:163], v[112:115], v[136:139], v[160:163]\n\t" \
  "ds_read_b128 v[108:111], v52 offset:6144\n\t" \
  "v_bfe_i32 v39, v3, 5, 1\n\t" \
  "v_mfma_f32_16x16x32_bf16 v[160:163], v[92:95], v[144:147], v[160:163]\n\t" \
  "ds_read_b128 v[148:151], v53 offset:6144\n\t" \
  "v_and_b32 v39, v30, v39\n\t" \
  "v_mfma_f32_16x16x32_bf16 v[168:171], v[92:95], v[72:75], v[168:171]\n\t" \
  "ds_read_b128 v[64:67], v53 offset:8192\n\t" \
  "v_bfe_i32 v43, v3, 9, 1\n\t" \
  "504:\n\t" \
  "s_cbranch_execz 508f\n\t" \
  "v_mfma_f32_16x16x32_bf16 v[168:171], v[112:115], v[140:143], v[168:171]\n\t" \
  "ds_read_b128 v[104:107], v52 offset:8192\n\t" \
  "v_bfi_b32 v43, v43, v30, %15\n\t" \
  "v_mfma_f32_16x16x32_bf16 v[176:179], v[112:115], v[68:71], v[176:179]\n\t" \
  "ds_read_b64_tr_b16 v[112:113], v55 offset:34816\n\t" \
  "v_mov_b32 v49, 0x3f803f80\n\t" \
  "v_mfma_f32_16x16x32_bf16 v[176:179], v[92:95], v[76:79], v[176:179]\n\t" \
  "ds_read_b64_tr_b16 v[114:115], v55 offset:35328\n\t" \
  "v_bfe_i32 v36, v3, 2, 1\n\t" \
  "v_mfma_f32_16x16x32_bf16 v[156:159], v[84:87], v[128:131], v[156:159]\n\t" \
  "ds_read_b64_tr_b16 v[92:93], v55 offset:38912\n\t" \
  "v_and_b32 v36, v30, v36\n\t" \
  "v_mfma_f32_16x16x32_bf16 v[156:159], v[116:119], v[120:123], v[156:159]\n\t" \
  "ds_read_b64_tr_b16 v[94:95], v55 offset:39424\n\t" \
  "v_bfe_i32 v40, v3, 6, 1\n\t" \
  "v_mfma_f32_16x16x32_bf16 v[164:167], v[116:119], v[136:139], v[164:167]\n\t" \
  "ds_read_b64_tr_b16 v[128:129], v54 offset:26624\n\t" \
  "v_and_b32 v40, v30, v40\n\t" \
  "v_mfma_f32_16x16x32_bf16 v[164:167], v[84:87], v[144:147], v[164:167]\n\t" \
  "ds_read_b64_tr_b16 v[130:131], v54 offset:27136\n\t" \
  "v_bfe_i32 v44, v3, 10, 1\n\t" \
  "v_mfma_f32_16x16x32_bf16 v[172:175], v[84:87], v[72:75], v[172:175]\n\t" \
  "ds_read_b64_tr_b16 v[120:121], v54 offset:30720\n\t" \
  "v_bfi_b32 v44, v44, v30, %15\n\t" \
  "v_mfma_f32_16x16x32_bf16 v[172:175], v[116:119], v[140:143], v[172:175]\n\t" \
  "ds_read_b64_tr_b16 v[122:123], v54 offset:31232\n\t" \
  "v_mov_b32 v50, 0x3f803f80\n\t" \
  "v_mfma_f32_16x16x32_bf16 v[180:183], v[116:119], v[68:71], v[180:183]\n\t" \
  "ds_read_b128 v[136:139], v52 offset:0\n\t" \
  "v_bfe_i32 v37, v3, 3, 1\n\t" \
  "v_mfma_f32_16x16x32_bf16 v[180:183], v[84:87], v[76:79], v[180:183]\n\t" \
  "ds_read_b128 v[144:147], v53 offset:0\n\t" \
  "v_and_b32 v37, v30, v37\n\t" \
  "s_waitcnt lgkmcnt(7)\n\t" \
  "v_mfma_f32_16x16x32_bf16 v[216:219], v[88:91], v[100:103], v[216:219]\n\t" \
  "ds_read_b64_tr_b16 v[72:73], v55 offset:26624\n\t" \
  "v_bfe_i32 v41, v3, 7, 1\n\t" \
  "v_mfma_f32_16x16x32_bf16 v[216:219], v[124:127], v[80:83], v[216:219]\n\t" \
  "ds_read_b64_tr_b16 v[74:75], v55 offset:27136\n\t" \
  "v_and_b32 v41, v30, v41\n\t" \
  "v_mfma_f32_16x16x32_bf16 v[224:227], v[96:99], v[80:83], v[224:227]\n\t" \
  "ds_read_b64_tr_b16 v[140:141], v55 offset:30720\n\t" \
  "v_bfe_i32 v45, v3, 11, 1\n\t" \
  "v_mfma_f32_16x16x32_bf16 v[224:227], v[132:135], v[100:103], v[224:227]\n\t" \
  "ds_read_b64_tr_b16 v[142:143], v55 offset:31232\n\t" \
  "v_bfi_b32 v45, v45, v30, %15\n\t" \
  "v_mfma_f32_16x16x32_bf16 v[232:235], v[108:111], v[100:103], v[232:235]\n\t" \
  "ds_read_b64_tr_b16 v[68:69], v54 offset:18432\n\t" \
  "v_mov_b32 v51, 0x3f803f80\n\t" \
  "v_mfma_f32_16x16x32_bf16 v[232:235], v[148:151], v[80:83], v[232:235]\n\t" \
  "ds_read_b64_tr_b16 v[70:71], v54 offset:18944\n\t" \
  "v_bfe_i32 v46, v3, 12, 1\n\t" \
  "v_mfma_f32_16x16x32_bf16 v[240:243], v[64:67], v[80:83], v[240:243]\n\t" \
  "ds_read_b64_tr_b16 v[116:117], v54 offset:22528\n\t" \
  "v_bfe_i32 v31, v3, 13, 1\n\t" \
  "v_mfma_f32_16x16x32_bf16 v[240:243], v[104:107], v[100:103], v[240:243]\n\t" \
  "ds_read_b64_tr_b16 v[118:119], v54 offset:23040\n\t" \
  "v_perm_b32 v46, v31, v46, vcc_hi\n\t" \
  "v_mfma_f32_16x16x32_bf16 v[220:223], v[88:91], v[112:115], v[220:223]\n\t" \
  "ds_read_b64_tr_b16 v[76:77], v55 offset:18432\n\t" \
  "v_bfe_i32 v47, v3, 14, 1\n\t" \
  "s_waitcnt lgkmcnt(7)\n\t" \
  "v_mfma_f32_16x16x32_bf16 v[220:223], v[124:127], v[92:95], v[220:223]\n\t" \
  "ds_read_b64_tr_b16 v[78:79], v55 offset:18944\n\t" \
  "v_bfe_i32 v31, v3, 15, 1\n\t" \
  "v_mfma_f32_16x16x32_bf16 v[228:231], v[96:99], v[92:95], v[228:231]\n\t" \
  "ds_read_b64_tr_b16 v[84:85], v55 offset:22528\n\t" \
  "v_perm_b32 v47, v31, v47, vcc_hi\n\t" \
  "v_mfma_f32_16x16x32_bf16 v[228:231], v[132:135], v[112:115], v[228:231]\n\t" \
  "ds_read_b64_tr_b16 v[86:87], v55 offset:23040\n\t" \
  "v_add_u32 v30, 18432, v4\n\t" \
  "v_mfma_f32_16x16x32_bf16 v[236:239], v[108:111], v[112:115], v[236:239]\n\t" \
  "v_xor_b32 v31, 1024, v30\n\t" \
  "v_mov_b32 v32, %12\n\t" \
  "v_mfma_f32_16x16x32_bf16 v[236:239], v[148:151], v[92:95], v[236:239]\n\t" \
  "v_xor_b32 v33, 4, v32\n\t" \
  "v_mov_b32 v0, 1\n\t" \
  "v_mfma_f32_16x16x32_bf16 v[244:247], v[64:67], v[92:95], v[244:247]\n\t" \
  "v_and_b32 v4, 1023, v4\n\t" \
  "v_add_u32 v1, 8192, v4\n\t" \
  "v_mfma_f32_16x16x32_bf16 v[244:247], v[104:107], v[112:115], v[244:247]\n\t" \
  "v_mfma_f32_16x16x32_bf16 v[184:187], v[128:131], v[88:91], v[184:187]\n\t" \
  "v_mfma_f32_16x16x32_bf16 v[184:187], v[120:123], v[124:127], v[184:187]\n\t" \
  "v_mfma_f32_16x16x32_bf16 v[192:195], v[120:123], v[96:99], v[192:195]\n\t" \
  "v_mfma_f32_16x16x32_bf16 v[192:195], v[128:131], v[132:135], v[192:195]\n\t" \
  "v_mfma_f32_16x16x32_bf16 v[200:203], v[128:131], v[108:111], v[200:203]\n\t" \
  "v_mfma_f32_16x16x32_bf16 v[200:203], v[120:123], v[148:151], v[200:203]\n\t" \
  "v_mfma_f32_16x16x32_bf16 v[208:211], v[120:123], v[64:67], v[208:211]\n\t" \
  "v_mfma_f32_16x16x32_bf16 v[208:211], v[128:131], v[104:107], v[208:211]\n\t" \
  "v_mfma_f32_16x16x32_bf16 v[248:251], v[128:131], v[136:139], v[248:251]\n\t" \
  "v_mfma_f32_16x16x32_bf16 v[248:251], v[120:123], v[144:147], v[248:251]\n\t" \
  "v_mfma_f32_16x16x32_bf16 v[252:255], v[72:75], v[136:139], v[252:255]\n\t" \
  "s_waitcnt lgkmcnt(0)\n\t" \
  "v_mfma_f32_16x16x32_bf16 v[252:255], v[140:143], v[144:147], v[252:255]\n\t" \
  "v_mfma_f32_16x16x32_bf16 v[188:191], v[140:143], v[124:127], v[188:191]\n\t" \
  "v_mfma_f32_16x16x32_bf16 v[188:191], v[72:75], v[88:91], v[188:191]\n\t" \
  "s_barrier\n\t" \
  "v_mfma_f32_16x16x32_bf16 v[196:199], v[72:75], v[132:135], v[196:199]\n\t" \
  "ds_read_b64_tr_b16 v[80:81], v58 offset:34816\n\t" \
  "v_mfma_f32_16x16x32_bf16 v[196:199], v[140:143], v[96:99], v[196:199]\n\t" \
  "ds_read_b64_tr_b16 v[82:83], v58 offset:35328\n\t" \
  "v_mfma_f32_16x16x32_bf16 v[204:207], v[140:143], v[148:151], v[204:207]\n\t" \
  "ds_read_b128 v[100:103], v56 offset:2048\n\t" \
  "v_mfma_f32_16x16x32_bf16 v[204:207], v[72:75], v[108:111], v[204:207]\n\t" \
  "ds_read_b64_tr_b16 v[92:93], v58 offset:38912\n\t" \
  "v_mfma_f32_16x16x32_bf16 v[212:215], v[72:75], v[104:107], v[212:215]\n\t" \
  "ds_read_b64_tr_b16 v[94:95], v58 offset:39424\n\t" \
  "v_mfma_f32_16x16x32_bf16 v[212:215], v[140:143], v[64:67], v[212:215]\n\t" \
  "ds_read_b128 v[112:115], v57 offset:2048\n\t" \
  "v_mfma_f32_16x16x32_bf16 v[152:155], v[68:71], v[88:91], v[152:155]\n\t" \
  "ds_read_b128 v[128:131], v57 offset:4096\n\t" \
  "v_mfma_f32_16x16x32_bf16 v[152:155], v[116:119], v[124:127], v[152:155]\n\t" \
  "ds_read_b128 v[120:123], v56 offset:4096\n\t" \
  "v_mfma_f32_16x16x32_bf16 v[160:163], v[116:119], v[96:99], v[160:163]\n\t" \
  "ds_read_b128 v[136:139], v56 offset:6144\n\t" \
  "v_mfma_f32_16x16x32_bf16 v[160:163], v[68:71], v[132:135], v[160:163]\n\t" \
  "ds_read_b128 v[144:147], v57 offset:6144\n\t" \
  "v_mfma_f32_16x16x32_bf16 v[168:171], v[68:71], v[108:111], v[168:171]\n\t" \
  "ds_read_b128 v[72:75], v57 offset:8192\n\t" \
  "v_mfma_f32_16x16x32_bf16 v[168:171], v[116:119], v[148:151], v[168:171]\n\t" \
  "ds_read_b128 v[140:143], v56 offset:8192\n\t" \
  "v_mfma_f32_16x16x32_bf16 v[176:179], v[116:119], v[64:67], v[176:179]\n\t" \
  "ds_read_b64_tr_b16 v[116:117], v59 offset:34816\n\t" \
  "v_mfma_f32_16x16x32_bf16 v[176:179], v[68:71], v[104:107], v[176:179]\n\t" \
  "ds_read_b64_tr_b16 v[118:119], v59 offset:35328\n\t" \
  "v_mfma_f32_16x16x32_bf16 v[156:159], v[76:79], v[88:91], v[156:159]\n\t" \
  "ds_read_b64_tr_b16 v[68:69], v59 offset:38912\n\t" \
  "v_mfma_f32_16x16x32_bf16 v[156:159], v[84:87], v[124:127], v[156:159]\n\t" \
  "ds_read_b64_tr_b16 v[70:71], v59 offset:39424\n\t" \
  "v_mfma_f32_16x16x32_bf16 v[164:167], v[84:87], v[96:99], v[164:167]\n\t" \
  "ds_read_b64_tr_b16 v[88:89], v58 offset:26624\n\t" \
  "v_mfma_f32_16x16x32_bf16 v[164:167], v[76:79], v[132:135], v[164:167]\n\t" \
  "ds_read_b64_tr_b16 v[90:91], v58 offset:27136\n\t" \
  "v_mfma_f32_16x16x32_bf16 v[172:175], v[76:79], v[108:111], v[172:175]\n\t" \
  "ds_read_b64_tr_b16 v[124:125], v58 offset:30720\n\t" \
  "v_mfma_f32_16x16x32_bf16 v[172:175], v[84:87], v[148:151], v[172:175]\n\t" \
  "ds_read_b64_tr_b16 v[126:127], v58 offset:31232\n\t" \
  "v_mfma_f32_16x16x32_bf16 v[180:183], v[84:87], v[64:67], v[180:183]\n\t" \
  "ds_read_b128 v[96:99], v56 offset:0\n\t" \
  "v_mfma_f32_16x16x32_bf16 v[180:183], v[76:79], v[104:107], v[180:183]\n\t" \
  "ds_read_b128 v[132:135], v57 offset:0\n\t" \
  "s_waitcnt lgkmcnt(7)\n\t" \
  "v_mfma_f32_16x16x32_bf16 v[216:219], v[100:103], v[80:83], v[216:219]\n\t" \
  "ds_read_b64_tr_b16 v[108:109], v59 offset:26624\n\t" \
  "v_mfma_f32_16x16x32_bf16 v[216:219], v[112:115], v[92:95], v[216:219]\n\t" \
  "ds_read_b64_tr_b16 v[110:111], v59 offset:27136\n\t" \
  "v_mfma_f32_16x16x32_bf16 v[224:227], v[128:131], v[92:95], v[224:227]\n\t" \
  "ds_read_b64_tr_b16 v[148:149], v59 offset:30720\n\t" \
  "v_mfma_f32_16x16x32_bf16 v[224:227], v[120:123], v[80:83], v[224:227]\n\t" \
  "ds_read_b64_tr_b16 v[150:151], v59 offset:31232\n\t" \
  "v_mfma_f32_16x16x32_bf16 v[232:235], v[136:139], v[80:83], v[232:235]\n\t" \
  "ds_read_b64_tr_b16 v[64:65], v58 offset:18432\n\t" \
  "v_mfma_f32_16x16x32_bf16 v[232:235], v[144:147], v[92:95], v[232:235]\n\t" \
  "ds_read_b64_tr_b16 v[66:67], v58 offset:18944\n\t" \
  "v_mfma_f32_16x16x32_bf16 v[240:243], v[72:75], v[92:95], v[240:243]\n\t" \
  "ds_read_b64_tr_b16 v[84:85], v58 offset:22528\n\t" \
  "v_mfma_f32_16x16x32_bf16 v[240:243], v[140:143], v[80:83], v[240:243]\n\t" \
  "ds_read_b64_tr_b16 v[86:87], v58 offset:23040\n\t" \
  "v_mfma_f32_16x16x32_bf16 v[220:223], v[100:103], v[116:119], v[220:223]\n\t" \
  "ds_read_b64_tr_b16 v[76:77], v59 offset:18432\n\t" \
  "s_waitcnt lgkmcnt(7)\n\t" \
  "v_mfma_f32_16x16x32_bf16 v[220:223], v[112:115], v[68:71], v[220:223]\n\t" \
  "ds_read_b64_tr_b16 v[78:79], v59 offset:18944\n\t" \
  "v_mfma_f32_16x16x32_bf16 v[228:231], v[128:131], v[68:71], v[228:231]\n\t" \
  "ds_read_b64_tr_b16 v[104:105], v59 offset:22528\n\t" \
  "v_mfma_f32_16x16x32_bf16 v[228:231], v[120:123], v[116:119], v[228:231]\n\t" \
  "ds_read_b64_tr_b16 v[106:107], v59 offset:23040\n\t" \
  "v_mfma_f32_16x16x32_bf16 v[236:239], v[136:139], v[116:119], v[236:239]\n\t" \
  "v_mfma_f32_16x16x32_bf16 v[236:239], v[144:147], v[68:71], v[236:239]\n\t" \
  "v_mfma_f32_16x16x32_bf16 v[244:247], v[72:75], v[68:71], v[244:247]\n\t" \
  "v_mfma_f32_16x16x32_bf16 v[244:247], v[140:143], v[116:119], v[244:247]\n\t" \
  "v_mfma_f32_16x16x32_bf16 v[184:187], v[88:91], v[100:103], v[184:187]\n\t" \
  "v_mfma_f32_16x16x32_bf16 v[184:187], v[124:127], v[112:115], v[184:187]\n\t" \
  "v_mfma_f32_16x16x32_bf16 v[192:195], v[124:127], v[128:131], v[192:195]\n\t" \
  "v_mfma_f32_16x16x32_bf16 v[192:195], v[88:91], v[120:123], v[192:195]\n\t" \
  "v_mfma_f32_16x16x32_bf16 v[200:203], v[88:91], v[136:139], v[200:203]\n\t" \
  "v_mfma_f32_16x16x32_bf16 v[200:203], v[124:127], v[144:147], v[200:203]\n\t" \
  "v_mfma_f32_16x16x32_bf16 v[208:211], v[124:127], v[72:75], v[208:211]\n\t" \
  "v_mfma_f32_16x16x32_bf16 v[208:211], v[88:91], v[140:143], v[208:211]\n\t" \
  "v_mfma_f32_16x16x32_bf16 v[248:251], v[88:91], v[96:99], v[248:251]\n\t" \
  "v_mfma_f32_16x16x32_bf16 v[248:251], v[124:127], v[132:135], v[248:251]\n\t" \
  "v_mfma_f32_16x16x32_bf16 v[252:255], v[108:111], v[96:99], v[252:255]\n\t" \
  "s_waitcnt lgkmcnt(0)\n\t" \
  "v_mfma_f32_16x16x32_bf16 v[252:255], v[148:151], v[132:135], v[252:255]\n\t" \
  "v_mfma_f32_16x16x32_bf16 v[188:191], v[148:151], v[112:115], v[188:191]\n\t" \
  "v_mfma_f32_16x16x32_bf16 v[188:191], v[108:111], v[100:103], v[188:191]\n\t" \
  "s_waitcnt vmcnt(0)\n\t" \
  "s_barrier\n\t" \
  "v_mfma_f32_16x16x32_bf16 v[196:199], v[108:111], v[120:123], v[196:199]\n\t" \
  "ds_read_b64_tr_b16 v[92:93], v62 offset:34816\n\t" \
  "v_mfma_f32_16x16x32_bf16 v[196:199], v[148:151], v[128:131], v[196:199]\n\t" \
  "ds_read_b64_tr_b16 v[94:95], v62 offset:35328\n\t" \
  "v_mfma_f32_16x16x32_bf16 v[204:207], v[148:151], v[144:147], v[204:207]\n\t" \
  "ds_read_b128 v[80:83], v60 offset:2048\n\t" \
  "v_mfma_f32_16x16x32_bf16 v[204:207], v[108:111], v[136:139], v[204:207]\n\t" \
  "ds_read_b64_tr_b16 v[68:69], v62 offset:38912\n\t" \
  "v_mfma_f32_16x16x32_bf16 v[212:215], v[108:111], v[140:143], v[212:215]\n\t" \
  "ds_read_b64_tr_b16 v[70:71], v62 offset:39424\n\t" \
  "v_mfma_f32_16x16x32_bf16 v[212:215], v[148:151], v[72:75], v[212:215]\n\t" \
  "ds_read_b128 v[116:119], v61 offset:2048\n\t" \
  "v_mfma_f32_16x16x32_bf16 v[152:155], v[64:67], v[100:103], v[152:155]\n\t" \
  "ds_read_b128 v[88:91], v61 offset:4096\n\t" \
  "v_mfma_f32_16x16x32_bf16 v[152:155], v[84:87], v[112:115], v[152:155]\n\t" \
  "ds_read_b128 v[124:127], v60 offset:4096\n\t" \
  "v_mfma_f32_16x16x32_bf16 v[160:163], v[84:87], v[128:131], v[160:163]\n\t" \
  "ds_read_b128 v[96:99], v60 offset:6144\n\t" \
  "v_mfma_f32_16x16x32_bf16 v[160:163], v[64:67], v[120:123], v[160:163]\n\t" \
  "ds_read_b128 v[132:135], v61 offset:6144\n\t" \
  "v_mfma_f32_16x16x32_bf16 v[168:171], v[64:67], v[136:139], v[168:171]\n\t" \
  "ds_read_b128 v[108:111], v61 offset:8192\n\t" \
  "v_mfma_f32_16x16x32_bf16 v[168:171], v[84:87], v[144:147], v[168:171]\n\t" \
  "ds_read_b128 v[148:151], v60 offset:8192\n\t" \
  "v_mfma_f32_16x16x32_bf16 v[176:179], v[84:87], v[72:75], v[176:179]\n\t" \
  "ds_read_b64_tr_b16 v[84:85], v63 offset:34816\n\t" \
  "v_mfma_f32_16x16x32_bf16 v[176:179], v[64:67], v[140:143], v[176:179]\n\t" \
  "ds_read_b64_tr_b16 v[86:87], v63 offset:35328\n\t" \
  "v_mfma_f32_16x16x32_bf16 v[156:159], v[76:79], v[100:103], v[156:159]\n\t" \
  "ds_read_b64_tr_b16 v[64:65], v63 offset:38912\n\t" \
  "v_mfma_f32_16x16x32_bf16 v[156:159], v[104:107], v[112:115], v[156:159]\n\t" \
  "ds_read_b64_tr_b16 v[66:67], v63 offset:39424\n\t" \
  "v_mfma_f32_16x16x32_bf16 v[164:167], v[104:107], v[128:131], v[164:167]\n\t" \
  "ds_read_b64_tr_b16 v[100:101], v62 offset:26624\n\t" \
  "v_mfma_f32_16x16x32_bf16 v[164:167], v[76:79], v[120:123], v[164:167]\n\t" \
  "ds_read_b64_tr_b16 v[102:103], v62 offset:27136\n\t" \
  "v_mfma_f32_16x16x32_bf16 v[172:175], v[76:79], v[136:139], v[172:175]\n\t" \
  "ds_read_b64_tr_b16 v[112:113], v62 offset:30720\n\t" \
  "v_mfma_f32_16x16x32_bf16 v[172:175], v[104:107], v[144:147], v[172:175]\n\t" \
  "ds_read_b64_tr_b16 v[114:115], v62 offset:31232\n\t" \
  "v_mfma_f32_16x16x32_bf16 v[180:183], v[104:107], v[72:75], v[180:183]\n\t" \
  "ds_read_b128 v[128:131], v60 offset:0\n\t" \
  "v_mfma_f32_16x16x32_bf16 v[180:183], v[76:79], v[140:143], v[180:183]\n\t" \
  "ds_read_b128 v[120:123], v61 offset:0\n\t" \
  "s_waitcnt lgkmcnt(7)\n\t" \
  "v_mfma_f32_16x16x32_bf16 v[216:219], v[80:83], v[92:95], v[216:219]\n\t" \
  "ds_read_b64_tr_b16 v[136:137], v63 offset:26624\n\t" \
  "v_mfma_f32_16x16x32_bf16 v[216:219], v[116:119], v[68:71], v[216:219]\n\t" \
  "ds_read_b64_tr_b16 v[138:139], v63 offset:27136\n\t" \
  "v_mfma_f32_16x16x32_bf16 v[224:227], v[88:91], v[68:71], v[224:227]\n\t" \
  "ds_read_b64_tr_b16 v[144:145], v63 offset:30720\n\t" \
  "v_mfma_f32_16x16x32_bf16 v[224:227], v[124:127], v[92:95], v[224:227]\n\t" \
  "ds_read_b64_tr_b16 v[146:147], v63 offset:31232\n\t" \
  "v_mfma_f32_16x16x32_bf16 v[232:235], v[96:99], v[92:95], v[232:235]\n\t" \
  "ds_read_b64_tr_b16 v[72:73], v62 offset:18432\n\t" \
  "v_mfma_f32_16x16x32_bf16 v[232:235], v[132:135], v[68:71], v[232:235]\n\t" \
  "ds_read_b64_tr_b16 v[74:75], v62 offset:18944\n\t" \
  "v_mfma_f32_16x16x32_bf16 v[240:243], v[108:111], v[68:71], v[240:243]\n\t" \
  "ds_read_b64_tr_b16 v[104:105], v62 offset:22528\n\t" \
  "v_mfma_f32_16x16x32_bf16 v[240:243], v[148:151], v[92:95], v[240:243]\n\t" \
  "ds_read_b64_tr_b16 v[106:107], v62 offset:23040\n\t" \
  "v_mfma_f32_16x16x32_bf16 v[220:223], v[80:83], v[84:87], v[220:223]\n\t" \
  "ds_read_b64_tr_b16 v[76:77], v63 offset:18432\n\t" \
  "s_waitcnt lgkmcnt(7)\n\t" \
  "v_mfma_f32_16x16x32_bf16 v[220:223], v[116:119], v[64:67], v[220:223]\n\t" \
  "ds_read_b64_tr_b16 v[78:79], v63 offset:18944\n\t" \
  "v_mfma_f32_16x16x32_bf16 v[228:231], v[88:91], v[64:67], v[228:231]\n\t" \
  "ds_read_b64_tr_b16 v[140:141], v63 offset:22528\n\t" \
  "v_mfma_f32_16x16x32_bf16 v[228:231], v[124:127], v[84:87], v[228:231]\n\t" \
  "ds_read_b64_tr_b16 v[142:143], v63 offset:23040\n\t" \
  "v_mfma_f32_16x16x32_bf16 v[236:239], v[96:99], v[84:87], v[236:239]\n\t" \
  "v_mfma_f32_16x16x32_bf16 v[236:239], v[132:135], v[64:67], v[236:239]\n\t" \
  "v_mfma_f32_16x16x32_bf16 v[244:247], v[108:111], v[64:67], v[244:247]\n\t" \
  "v_mfma_f32_16x16x32_bf16 v[244:247], v[148:151], v[84:87], v[244:247]\n\t" \
  "v_mfma_f32_16x16x32_bf16 v[184:187], v[100:103], v[80:83], v[184:187]\n\t" \
  "ds_read_b32 v10, v32 offset:104\n\t" \
  "v_mfma_f32_16x16x32_bf16 v[184:187], v[112:115], v[116:119], v[184:187]\n\t" \
  "v_lshlrev_b32 v11, 3, v4\n\t" \
  "v_mfma_f32_16x16x32_bf16 v[192:195], v[112:115], v[88:91], v[192:195]\n\t" \
  "v_mfma_f32_16x16x32_bf16 v[192:195], v[100:103], v[124:127], v[192:195]\n\t" \
  "v_mfma_f32_16x16x32_bf16 v[200:203], v[100:103], v[96:99], v[200:203]\n\t" \
  "v_mfma_f32_16x16x32_bf16 v[200:203], v[112:115], v[132:135], v[200:203]\n\t" \
  "v_mfma_f32_16x16x32_bf16 v[208:211], v[112:115], v[108:111], v[208:211]\n\t" \
  "v_mfma_f32_16x16x32_bf16 v[208:211], v[100:103], v[148:151], v[208:211]\n\t" \
  "v_mfma_f32_16x16x32_bf16 v[248:251], v[100:103], v[128:131], v[248:251]\n\t" \
  "v_mfma_f32_16x16x32_bf16 v[248:251], v[112:115], v[120:123], v[248:251]\n\t" \
  "v_mfma_f32_16x16x32_bf16 v[252:255], v[136:139], v[128:131], v[252:255]\n\t" \
  "s_waitcnt lgkmcnt(0)\n\t" \
  "s_cmp_lg_u32 %13, 0\n\t" \
  "s_cbranch_scc1 27f\n\t" \
  "s_mov_b32 exec_hi, 0\n\t" \
  "global_store_dword v11, v10, %25 sc1\n\t" \
  "s_mov_b32 exec_hi, -1\n\t" \
  "27:\n\t" \
  "v_mfma_f32_16x16x32_bf16 v[252:255], v[144:147], v[120:123], v[252:255]\n\t" \
  "v_mfma_f32_16x16x32_bf16 v[188:191], v[144:147], v[116:119], v[188:191]\n\t" \
  "v_mfma_f32_16x16x32_bf16 v[188:191], v[136:139], v[80:83], v[188:191]\n\t" \
  "s_barrier\n\t" \
  "v_mfma_f32_16x16x32_bf16 v[196:199], v[136:139], v[124:127], v[196:199]\n\t" \
  "ds_read_b64_tr_b16 v[68:69], v54 offset:34816\n\t" \
  "v_mfma_f32_16x16x32_bf16 v[196:199], v[144:147], v[88:91], v[196:199]\n\t" \
  "ds_read_b64_tr_b16 v[70:71], v54 offset:35328\n\t" \
  "v_mfma_f32_16x16x32_bf16 v[204:207], v[144:147], v[132:135], v[204:207]\n\t" \
  "ds_read_b128 v[92:95], v52 offset:2048\n\t" \
  "v_mfma_f32_16x16x32_bf16 v[204:207], v[136:139], v[96:99], v[204:207]\n\t" \
  "ds_read_b64_tr_b16 v[64:65], v54 offset:38912\n\t" \
  "v_mfma_f32_16x16x32_bf16 v[212:215], v[136:139], v[148:151], v[212:215]\n\t" \
  "ds_read_b64_tr_b16 v[66:67], v54 offset:39424\n\t" \
  "v_mfma_f32_16x16x32_bf16 v[212:215], v[144:147], v[108:111], v[212:215]\n\t" \
  "ds_read_b128 v[84:87], v53 offset:2048\n\t" \
  "v_mfma_f32_16x16x32_bf16 v[152:155], v[72:75], v[80:83], v[152:155]\n\t" \
  "ds_read_b128 v[100:103], v53 offset:4096\n\t" \
  "v_mfma_f32_16x16x32_bf16 v[152:155], v[104:107], v[116:119], v[152:155]\n\t" \
  "ds_read_b128 v[112:115], v52 offset:4096\n\t" \
  "v_mfma_f32_16x16x32_bf16 v[160:163], v[104:107], v[88:91], v[160:163]\n\t" \
  "ds_read_b128 v[128:131], v52 offset:6144\n\t" \
  "v_mfma_f32_16x16x32_bf16 v[160:163], v[72:75], v[124:127], v[160:163]\n\t" \
  "ds_read_b128 v[120:123], v53 offset:6144\n\t" \
  "v_mfma_f32_16x16x32_bf16 v[168:171], v[72:75], v[96:99], v[168:171]\n\t" \
  "ds_read_b128 v[136:139], v53 offset:8192\n\t" \
  "v_mfma_f32_16x16x32_bf16 v[168:171], v[104:107], v[132:135], v[168:171]\n\t" \
  "ds_read_b128 v[144:147], v52 offset:8192\n\t" \
  "v_mfma_f32_16x16x32_bf16 v[176:179], v[104:107], v[108:111], v[176:179]\n\t" \
  "ds_read_b64_tr_b16 v[104:105], v55 offset:34816\n\t" \
  "v_mfma_f32_16x16x32_bf16 v[176:179], v[72:75], v[148:151], v[176:179]\n\t" \
  "ds_read_b64_tr_b16 v[106:107], v55 offset:35328\n\t" \
  "v_mfma_f32_16x16x32_bf16 v[156:159], v[76:79], v[80:83], v[156:159]\n\t" \
  "ds_read_b64_tr_b16 v[72:73], v55 offset:38912\n\t" \
  "v_mfma_f32_16x16x32_bf16 v[156:159], v[140:143], v[116:119], v[156:159]\n\t" \
  "ds_read_b64_tr_b16 v[74:75], v55 offset:39424\n\t" \
  "v_mfma_f32_16x16x32_bf16 v[164:167], v[140:143], v[88:91], v[164:167]\n\t" \
  "ds_read_b64_tr_b16 v[80:81], v54 offset:26624\n\t" \
  "v_mfma_f32_16x16x32_bf16 v[164:167], v[76:79], v[124:127], v[164:167]\n\t" \
  "ds_read_b64_tr_b16 v[82:83], v54 offset:27136\n\t" \
  "v_mfma_f32_16x16x32_bf16 v[172:175], v[76:79], v[96:99], v[172:175]\n\t" \
  "ds_read_b64_tr_b16 v[116:117], v54 offset:30720\n\t" \
  "v_mfma_f32_16x16x32_bf16 v[172:175], v[140:143], v[132:135], v[172:175]\n\t" \
  "ds_read_b64_tr_b16 v[118:119], v54 offset:31232\n\t" \
  "v_mfma_f32_16x16x32_bf16 v[180:183], v[140:143], v[108:111], v[180:183]\n\t" \
  "ds_read_b128 v[88:91], v52 offset:0\n\t" \
  "v_mfma_f32_16x16x32_bf16 v[180:183], v[76:79], v[148:151], v[180:183]\n\t" \
  "ds_read_b128 v[124:127], v53 offset:0\n\t" \
  "s_waitcnt lgkmcnt(7)\n\t" \
  "v_mfma_f32_16x16x32_bf16 v[216:219], v[92:95], v[68:71], v[216:219]\n\t" \
  "ds_read_b64_tr_b16 v[96:97], v55 offset:26624\n\t" \
  "v_mfma_f32_16x16x32_bf16 v[216:219], v[84:87], v[64:67], v[216:219]\n\t" \
  "ds_read_b64_tr_b16 v[98:99], v55 offset:27136\n\t" \
  "v_mfma_f32_16x16x32_bf16 v[224:227], v[100:103], v[64:67], v[224:227]\n\t" \
  "ds_read_b64_tr_b16 v[132:133], v55 offset:30720\n\t" \
  "v_mfma_f32_16x16x32_bf16 v[224:227], v[112:115], v[68:71], v[224:227]\n\t" \
  "ds_read_b64_tr_b16 v[134:135], v55 offset:31232\n\t" \
  "v_mfma_f32_16x16x32_bf16 v[232:235], v[128:131], v[68:71], v[232:235]\n\t" \
  "ds_read_b64_tr_b16 v[108:109], v54 offset:18432\n\t" \
  "v_mfma_f32_16x16x32_bf16 v[232:235], v[120:123], v[64:67], v[232:235]\n\t" \
  "ds_read_b64_tr_b16 v[110:111], v54 offset:18944\n\t" \
  "v_mfma_f32_16x16x32_bf16 v[240:243], v[136:139], v[64:67], v[240:243]\n\t" \
  "ds_read_b64_tr_b16 v[140:141], v54 offset:22528\n\t" \
  "v_mfma_f32_16x16x32_bf16 v[240:243], v[144:147], v[68:71], v[240:243]\n\t" \
  "ds_read_b64_tr_b16 v[142:143], v54 offset:23040\n\t" \
  "v_mfma_f32_16x16x32_bf16 v[220:223], v[92:95], v[104:107], v[220:223]\n\t" \
  "ds_read_b64_tr_b16 v[76:77], v55 offset:18432\n\t" \
  "s_waitcnt lgkmcnt(7)\n\t" \
  "v_mfma_f32_16x16x32_bf16 v[220:223], v[84:87], v[72:75], v[220:223]\n\t" \
  "ds_read_b64_tr_b16 v[78:79], v55 offset:18944\n\t" \
  "v_mfma_f32_16x16x32_bf16 v[228:231], v[100:103], v[72:75], v[228:231]\n\t" \
  "ds_read_b64_tr_b16 v[148:149], v55 offset:22528\n\t" \
  "v_mfma_f32_16x16x32_bf16 v[228:231], v[112:115], v[104:107], v[228:231]\n\t" \
  "ds_read_b64_tr_b16 v[150:151], v55 offset:23040\n\t" \
  "v_mfma_f32_16x16x32_bf16 v[236:239], v[128:131], v[104:107], v[236:239]\n\t" \
  "v_mfma_f32_16x16x32_bf16 v[236:239], v[120:123], v[72:75], v[236:239]\n\t" \
  "v_mfma_f32_16x16x32_bf16 v[244:247], v[136:139], v[72:75], v[244:247]\n\t" \
  "v_mfma_f32_16x16x32_bf16 v[244:247], v[144:147], v[104:107], v[244:247]\n\t" \
  "v_mfma_f32_16x16x32_bf16 v[184:187], v[80:83], v[92:95], v[184:187]\n\t" \
  "v_mfma_f32_16x16x32_bf16 v[184:187], v[116:119], v[84:87], v[184:187]\n\t" \
  "v_mfma_f32_16x16x32_bf16 v[192:195], v[116:119], v[100:103], v[192:195]\n\t" \
  "v_mfma_f32_16x16x32_bf16 v[192:195], v[80:83], v[112:115], v[192:195]\n\t" \
  "v_mfma_f32_16x16x32_bf16 v[200:203], v[80:83], v[128:131], v[200:203]\n\t" \
  "v_mfma_f32_16x16x32_bf16 v[200:203], v[116:119], v[120:123], v[200:203]\n\t" \
  "v_mfma_f32_16x16x32_bf16 v[208:211], v[116:119], v[136:139], v[208:211]\n\t" \
  "v_mfma_f32_16x16x32_bf16 v[208:211], v[80:83], v[144:147], v[208:211]\n\t" \
  "v_mfma_f32_16x16x32_bf16 v[248:251], v[80:83], v[88:91], v[248:251]\n\t" \
  "v_mfma_f32_16x16x32_bf16 v[248:251], v[116:119], v[124:127], v[248:251]\n\t" \
  "v_mfma_f32_16x16x32_bf16 v[252:255], v[96:99], v[88:91], v[252:255]\n\t" \
  "s_waitcnt lgkmcnt(0)\n\t" \
  "v_mfma_f32_16x16x32_bf16 v[252:255], v[132:135], v[124:127], v[252:255]\n\t" \
  "v_mfma_f32_16x16x32_bf16 v[188:191], v[132:135], v[84:87], v[188:191]\n\t" \
  "v_mfma_f32_16x16x32_bf16 v[188:191], v[96:99], v[92:95], v[188:191]\n\t" \
  "s_barrier\n\t" \
  "v_mfma_f32_16x16x32_bf16 v[196:199], v[96:99], v[112:115], v[196:199]\n\t" \
  "ds_read_b64_tr_b16 v[64:65], v58 offset:34816\n\t" \
  "v_mfma_f32_16x16x32_bf16 v[196:199], v[132:135], v[100:103], v[196:199]\n\t" \
  "ds_read_b64_tr_b16 v[66:67], v58 offset:35328\n\t" \
  "v_mfma_f32_16x16x32_bf16 v[204:207], v[132:135], v[120:123], v[204:207]\n\t" \
  "ds_read_b128 v[68:71], v56 offset:2048\n\t" \
  "v_mfma_f32_16x16x32_bf16 v[204:207], v[96:99], v[128:131], v[204:207]\n\t" \
  "ds_read_b64_tr_b16 v[72:73], v58 offset:38912\n\t" \
  "v_mfma_f32_16x16x32_bf16 v[212:215], v[96:99], v[144:147], v[212:215]\n\t" \
  "ds_read_b64_tr_b16 v[74:75], v58 offset:39424\n\t" \
  "v_mfma_f32_16x16x32_bf16 v[212:215], v[132:135], v[136:139], v[212:215]\n\t" \
  "ds_read_b128 v[104:107], v57 offset:2048\n\t" \
  "v_mfma_f32_16x16x32_bf16 v[152:155], v[108:111], v[92:95], v[152:155]\n\t" \
  "ds_read_b128 v[80:83], v57 offset:4096\n\t" \
  "v_mfma_f32_16x16x32_bf16 v[152:155], v[140:143], v[84:87], v[152:155]\n\t" \
  "ds_read_b128 v[116:119], v56 offset:4096\n\t" \
  "v_mfma_f32_16x16x32_bf16 v[160:163], v[140:143], v[100:103], v[160:163]\n\t" \
  "ds_read_b128 v[88:91], v56 offset:6144\n\t" \
  "v_mfma_f32_16x16x32_bf16 v[160:163], v[108:111], v[112:115], v[160:163]\n\t" \
  "ds_read_b128 v[124:127], v57 offset:6144\n\t" \
  "v_mfma_f32_16x16x32_bf16 v[168:171], v[108:111], v[128:131], v[168:171]\n\t" \
  "ds_read_b128 v[96:99], v57 offset:8192\n\t" \
  "v_mfma_f32_16x16x32_bf16 v[168:171], v[140:143], v[120:123], v[168:171]\n\t" \
  "ds_read_b128 v[132:135], v56 offset:8192\n\t" \
  "v_mfma_f32_16x16x32_bf16 v[176:179], v[140:143], v[136:139], v[176:179]\n\t" \
  "ds_read_b64_tr_b16 v[140:141], v59 offset:34816\n\t" \
  "v_mfma_f32_16x16x32_bf16 v[176:179], v[108:111], v[144:147], v[176:179]\n\t" \
  "ds_read_b64_tr_b16 v[142:143], v59 offset:35328\n\t" \
  "v_mfma_f32_16x16x32_bf16 v[156:159], v[76:79], v[92:95], v[156:159]\n\t" \
  "ds_read_b64_tr_b16 v[108:109], v59 offset:38912\n\t" \
  "v_mfma_f32_16x16x32_bf16 v[156:159], v[148:151], v[84:87], v[156:159]\n\t" \
  "ds_read_b64_tr_b16 v[110:111], v59 offset:39424\n\t" \
  "v_mfma_f32_16x16x32_bf16 v[164:167], v[148:151], v[100:103], v[164:167]\n\t" \
  "ds_read_b64_tr_b16 v[92:93], v58 offset:26624\n\t" \
  "v_mfma_f32_16x16x32_bf16 v[164:167], v[76:79], v[112:115], v[164:167]\n\t" \
  "ds_read_b64_tr_b16 v[94:95], v58 offset:27136\n\t" \
  "v_mfma_f32_16x16x32_bf16 v[172:175], v[76:79], v[128:131], v[172:175]\n\t" \
  "ds_read_b64_tr_b16 v[84:85], v58 offset:30720\n\t" \
  "v_mfma_f32_16x16x32_bf16 v[172:175], v[148:151], v[120:123], v[172:175]\n\t" \
  "ds_read_b64_tr_b16 v[86:87], v58 offset:31232\n\t" \
  "v_mfma_f32_16x16x32_bf16 v[180:183], v[148:151], v[136:139], v[180:183]\n\t" \
  "ds_read_b128 v[100:103], v56 offset:0\n\t" \
  "v_mfma_f32_16x16x32_bf16 v[180:183], v[76:79], v[144:147], v[180:183]\n\t" \
  "ds_read_b128 v[112:115], v57 offset:0\n\t" \
  "s_waitcnt lgkmcnt(7)\n\t" \
  "v_mfma_f32_16x16x32_bf16 v[216:219], v[68:71], v[64:67], v[216:219]\n\t" \
  "ds_read_b64_tr_b16 v[128:129], v59 offset:26624\n\t" \
  "v_mfma_f32_16x16x32_bf16 v[216:219], v[104:107], v[72:75], v[216:219]\n\t" \
  "ds_read_b64_tr_b16 v[130:131], v59 offset:27136\n\t" \
  "v_mfma_f32_16x16x32_bf16 v[224:227], v[80:83], v[72:75], v[224:227]\n\t" \
  "ds_read_b64_tr_b16 v[120:121], v59 offset:30720\n\t" \
  "v_mfma_f32_16x16x32_bf16 v[224:227], v[116:119], v[64:67], v[224:227]\n\t" \
  "ds_read_b64_tr_b16 v[122:123], v59 offset:31232\n\t" \
  "v_mfma_f32_16x16x32_bf16 v[232:235], v[88:91], v[64:67], v[232:235]\n\t" \
  "ds_read_b64_tr_b16 v[136:137], v58 offset:18432\n\t" \
  "v_mfma_f32_16x16x32_bf16 v[232:235], v[124:127], v[72:75], v[232:235]\n\t" \
  "ds_read_b64_tr_b16 v[138:139], v58 offset:18944\n\t" \
  "v_mfma_f32_16x16x32_bf16 v[240:243], v[96:99], v[72:75], v[240:243]\n\t" \
  "ds_read_b64_tr_b16 v[148:149], v58 offset:22528\n\t" \
  "v_mfma_f32_16x16x32_bf16 v[240:243], v[132:135], v[64:67], v[240:243]\n\t" \
  "ds_read_b64_tr_b16 v[150:151], v58 offset:23040\n\t" \
  "v_mfma_f32_16x16x32_bf16 v[220:223], v[68:71], v[140:143], v[220:223]\n\t" \
  "ds_read_b64_tr_b16 v[76:77], v59 offset:18432\n\t" \
  "s_waitcnt lgkmcnt(7)\n\t" \
  "v_mfma_f32_16x16x32_bf16 v[220:223], v[104:107], v[108:111], v[220:223]\n\t" \
  "ds_read_b64_tr_b16 v[78:79], v59 offset:18944\n\t" \
  "v_mfma_f32_16x16x32_bf16 v[228:231], v[80:83], v[108:111], v[228:231]\n\t" \
  "ds_read_b64_tr_b16 v[144:145], v59 offset:22528\n\t" \
  "v_mfma_f32_16x16x32_bf16 v[228:231], v[116:119], v[140:143], v[228:231]\n\t" \
  "ds_read_b64_tr_b16 v[146:147], v59 offset:23040\n\t" \
  "v_mfma_f32_16x16x32_bf16 v[236:239], v[88:91], v[140:143], v[236:239]\n\t" \
  "v_mfma_f32_16x16x32_bf16 v[236:239], v[124:127], v[108:111], v[236:239]\n\t" \
  "v_mfma_f32_16x16x32_bf16 v[244:247], v[96:99], v[108:111], v[244:247]\n\t" \
  "v_mfma_f32_16x16x32_bf16 v[244:247], v[132:135], v[140:143], v[244:247]\n\t" \
  "v_mfma_f32_16x16x32_bf16 v[184:187], v[92:95], v[68:71], v[184:187]\n\t" \
  "v_mfma_f32_16x16x32_bf16 v[184:187], v[84:87], v[104:107], v[184:187]\n\t" \
  "v_mfma_f32_16x16x32_bf16 v[192:195], v[84:87], v[80:83], v[192:195]\n\t" \
  "v_mfma_f32_16x16x32_bf16 v[192:195], v[92:95], v[116:119], v[192:195]\n\t" \
  "v_mfma_f32_16x16x32_bf16 v[200:203], v[92:95], v[88:91], v[200:203]\n\t" \
  "v_mfma_f32_16x16x32_bf16 v[200:203], v[84:87], v[124:127], v[200:203]\n\t" \
  "v_mfma_f32_16x16x32_bf16 v[208:211], v[84:87], v[96:99], v[208:211]\n\t" \
  "v_mfma_f32_16x16x32_bf16 v[208:211], v[92:95], v[132:135], v[208:211]\n\t" \
  "v_mfma_f32_16x16x32_bf16 v[248:251], v[92:95], v[100:103], v[248:251]\n\t" \
  "v_mfma_f32_16x16x32_bf16 v[248:251], v[84:87], v[112:115], v[248:251]\n\t" \
  "v_mfma_f32_16x16x32_bf16 v[252:255], v[128:131], v[100:103], v[252:255]\n\t" \
  "s_waitcnt lgkmcnt(0)\n\t" \
  "v_mfma_f32_16x16x32_bf16 v[252:255], v[120:123], v[112:115], v[252:255]\n\t" \
  "v_mfma_f32_16x16x32_bf16 v[188:191], v[120:123], v[104:107], v[188:191]\n\t" \
  "v_mfma_f32_16x16x32_bf16 v[188:191], v[128:131], v[68:71], v[188:191]\n\t" \
  "s_barrier\n\t" \
  "v_mfma_f32_16x16x32_bf16 v[196:199], v[128:131], v[116:119], v[196:199]\n\t" \
  "ds_read_b64_tr_b16 v[72:73], v62 offset:34816\n\t" \
  "v_mfma_f32_16x16x32_bf16 v[196:199], v[120:123], v[80:83], v[196:199]\n\t" \
  "ds_read_b64_tr_b16 v[74:75], v62 offset:35328\n\t" \
  "v_mfma_f32_16x16x32_bf16 v[204:207], v[120:123], v[124:127], v[204:207]\n\t" \
  "ds_read_b128 v[64:67], v60 offset:2048\n\t" \
  "v_mfma_f32_16x16x32_bf16 v[204:207], v[128:131], v[88:91], v[204:207]\n\t" \
  "ds_read_b64_tr_b16 v[108:109], v62 offset:38912\n\t" \
  "v_mfma_f32_16x16x32_bf16 v[212:215], v[128:131], v[132:135], v[212:215]\n\t" \
  "ds_read_b64_tr_b16 v[110:111], v62 offset:39424\n\t" \
  "v_mfma_f32_16x16x32_bf16 v[212:215], v[120:123], v[96:99], v[212:215]\n\t" \
  "ds_read_b128 v[140:143], v61 offset:2048\n\t" \
  "v_mfma_f32_16x16x32_bf16 v[152:155], v[136:139], v[68:71], v[152:155]\n\t" \
  "ds_read_b128 v[92:95], v61 offset:4096\n\t" \
  "v_mfma_f32_16x16x32_bf16 v[152:155], v[148:151], v[104:107], v[152:155]\n\t" \
  "ds_read_b128 v[84:87], v60 offset:4096\n\t" \
  "v_mfma_f32_16x16x32_bf16 v[160:163], v[148:151], v[80:83], v[160:163]\n\t" \
  "ds_read_b128 v[100:103], v60 offset:6144\n\t" \
  "v_mfma_f32_16x16x32_bf16 v[160:163], v[136:139], v[116:119], v[160:163]\n\t" \
  "ds_read_b128 v[112:115], v61 offset:6144\n\t" \
  "v_mfma_f32_16x16x32_bf16 v[168:171], v[136:139], v[88:91], v[168:171]\n\t" \
  "ds_read_b128 v[128:131], v61 offset:8192\n\t" \
  "v_mfma_f32_16x16x32_bf16 v[168:171], v[148:151], v[124:127], v[168:171]\n\t" \
  "ds_read_b128 v[120:123], v60 offset:8192\n\t" \
  "v_mfma_f32_16x16x32_bf16 v[176:179], v[148:151], v[96:99], v[176:179]\n\t" \
  "ds_read_b64_tr_b16 v[148:149], v63 offset:34816\n\t" \
  "v_mfma_f32_16x16x32_bf16 v[176:179], v[136:139], v[132:135], v[176:179]\n\t" \
  "ds_read_b64_tr_b16 v[150:151], v63 offset:35328\n\t" \
  "v_mfma_f32_16x16x32_bf16 v[156:159], v[76:79], v[68:71], v[156:159]\n\t" \
  "ds_read_b64_tr_b16 v[136:137], v63 offset:38912\n\t" \
  "v_mfma_f32_16x16x32_bf16 v[156:159], v[144:147], v[104:107], v[156:159]\n\t" \
  "ds_read_b64_tr_b16 v[138:139], v63 offset:39424\n\t" \
  "v_mfma_f32_16x16x32_bf16 v[164:167], v[144:147], v[80:83], v[164:167]\n\t" \
  "ds_read_b64_tr_b16 v[68:69], v62 offset:26624\n\t" \
  "v_mfma_f32_16x16x32_bf16 v[164:167], v[76:79], v[116:119], v[164:167]\n\t" \
  "ds_read_b64_tr_b16 v[70:71], v62 offset:27136\n\t" \
  "v_mfma_f32_16x16x32_bf16 v[172:175], v[76:79], v[88:91], v[172:175]\n\t" \
  "ds_read_b64_tr_b16 v[104:105], v62 offset:30720\n\t" \
  "v_mfma_f32_16x16x32_bf16 v[172:175], v[144:147], v[124:127], v[172:175]\n\t" \
  "ds_read_b64_tr_b16 v[106:107], v62 offset:31232\n\t" \
  "v_mfma_f32_16x16x32_bf16 v[180:183], v[144:147], v[96:99], v[180:183]\n\t" \
  "ds_read_b128 v[80:83], v60 offset:0\n\t" \
  "v_mfma_f32_16x16x32_bf16 v[180:183], v[76:79], v[132:135], v[180:183]\n\t" \
  "ds_read_b128 v[116:119], v61 offset:0\n\t" \
  "s_waitcnt lgkmcnt(7)\n\t" \
  "v_mfma_f32_16x16x32_bf16 v[216:219], v[64:67], v[72:75], v[216:219]\n\t" \
  "ds_read_b64_tr_b16 v[88:89], v63 offset:26624\n\t" \
  "v_mfma_f32_16x16x32_bf16 v[216:219], v[140:143], v[108:111], v[216:219]\n\t" \
  "ds_read_b64_tr_b16 v[90:91], v63 offset:27136\n\t" \
  "v_mfma_f32_16x16x32_bf16 v[224:227], v[92:95], v[108:111], v[224:227]\n\t" \
  "ds_read_b64_tr_b16 v[124:125], v63 offset:30720\n\t" \
  "v_mfma_f32_16x16x32_bf16 v[224:227], v[84:87], v[72:75], v[224:227]\n\t" \
  "ds_read_b64_tr_b16 v[126:127], v63 offset:31232\n\t" \
  "v_mfma_f32_16x16x32_bf16 v[232:235], v[100:103], v[72:75], v[232:235]\n\t" \
  "ds_read_b64_tr_b16 v[96:97], v62 offset:18432\n\t" \
  "v_mfma_f32_16x16x32_bf16 v[232:235], v[112:115], v[108:111], v[232:235]\n\t" \
  "ds_read_b64_tr_b16 v[98:99], v62 offset:18944\n\t" \
  "v_mfma_f32_16x16x32_bf16 v[240:243], v[128:131], v[108:111], v[240:243]\n\t" \
  "ds_read_b64_tr_b16 v[144:145], v62 offset:22528\n\t" \
  "v_mfma_f32_16x16x32_bf16 v[240:243], v[120:123], v[72:75], v[240:243]\n\t" \
  "ds_read_b64_tr_b16 v[146:147], v62 offset:23040\n\t" \
  "v_mfma_f32_16x16x32_bf16 v[220:223], v[64:67], v[148:151], v[220:223]\n\t" \
  "ds_read_b64_tr_b16 v[76:77], v63 offset:18432\n\t" \
  "s_waitcnt lgkmcnt(7)\n\t" \
  "505:\n\t" \
  "s_cbranch_execz 509f\n\t" \
  "v_mfma_f32_16x16x32_bf16 v[220:223], v[140:143], v[136:139], v[220:223]\n\t" \
  "ds_read_b64_tr_b16 v[78:79], v63 offset:18944\n\t" \
  "v_mfma_f32_16x16x32_bf16 v[228:231], v[92:95], v[136:139], v[228:231]\n\t" \
  "ds_read_b64_tr_b16 v[132:133], v63 offset:22528\n\t" \
  "v_mfma_f32_16x16x32_bf16 v[228:231], v[84:87], v[148:151], v[228:231]\n\t" \
  "ds_read_b64_tr_b16 v[134:135], v63 offset:23040\n\t" \
  "v_mfma_f32_16x16x32_bf16 v[236:239], v[100:103], v[148:151], v[236:239]\n\t" \
  "v_mfma_f32_16x16x32_bf16 v[236:239], v[112:115], v[136:139], v[236:239]\n\t" \
  "v_mfma_f32_16x16x32_bf16 v[244:247], v[128:131], v[136:139], v[244:247]\n\t" \
  "v_mfma_f32_16x16x32_bf16 v[244:247], v[120:123], v[148:151], v[244:247]\n\t" \
  "v_mfma_f32_16x16x32_bf16 v[184:187], v[68:71], v[64:67], v[184:187]\n\t" \
  "v_mfma_f32_16x16x32_bf16 v[184:187], v[104:107], v[140:143], v[184:187]\n\t" \
  "v_mfma_f32_16x16x32_bf16 v[192:195], v[104:107], v[92:95], v[192:195]\n\t" \
  "v_mfma_f32_16x16x32_bf16 v[192:195], v[68:71], v[84:87], v[192:195]\n\t" \
  "v_mfma_f32_16x16x32_bf16 v[200:203], v[68:71], v[100:103], v[200:203]\n\t" \
  "v_mfma_f32_16x16x32_bf16 v[200:203], v[104:107], v[112:115], v[200:203]\n\t" \
  "v_mfma_f32_16x16x32_bf16 v[208:211], v[104:107], v[128:131], v[208:211]\n\t" \
  "v_mfma_f32_16x16x32_bf16 v[208:211], v[68:71], v[120:123], v[208:211]\n\t" \
  "v_mfma_f32_16x16x32_bf16 v[248:251], v[68:71], v[80:83], v[248:251]\n\t" \
  "v_mfma_f32_16x16x32_bf16 v[248:251], v[104:107], v[116:119], v[248:251]\n\t" \
  "v_mfma_f32_16x16x32_bf16 v[252:255], v[88:91], v[80:83], v[252:255]\n\t" \
  "s_waitcnt lgkmcnt(0)\n\t" \
  "v_mfma_f32_16x16x32_bf16 v[252:255], v[124:127], v[116:119], v[252:255]\n\t" \
  "v_mfma_f32_16x16x32_bf16 v[188:191], v[124:127], v[140:143], v[188:191]\n\t" \
  "v_mfma_f32_16x16x32_bf16 v[188:191], v[88:91], v[64:67], v[188:191]\n\t" \
  "s_barrier\n\t" \
  "v_mfma_f32_16x16x32_bf16 v[196:199], v[88:91], v[84:87], v[196:199]\n\t" \
  "ds_read_b64_tr_b16 v[108:109], v54 offset:34816\n\t" \
  "v_mfma_f32_16x16x32_bf16 v[196:199], v[124:127], v[92:95], v[196:199]\n\t" \
  "ds_read_b64_tr_b16 v[110:111], v54 offset:35328\n\t" \
  "v_mfma_f32_16x16x32_bf16 v[204:207], v[124:127], v[112:115], v[204:207]\n\t" \
  "ds_read_b128 v[72:75], v52 offset:2048\n\t" \
  "v_mfma_f32_16x16x32_bf16 v[204:207], v[88:91], v[100:103], v[204:207]\n\t" \
  "ds_read_b64_tr_b16 v[136:137], v54 offset:38912\n\t" \
  "v_mfma_f32_16x16x32_bf16 v[212:215], v[88:91], v[120:123], v[212:215]\n\t" \
  "ds_read_b64_tr_b16 v[138:139], v54 offset:39424\n\t" \
  "v_mfma_f32_16x16x32_bf16 v[212:215], v[124:127], v[128:131], v[212:215]\n\t" \
  "ds_read_b128 v[148:151], v53 offset:2048\n\t" \
  "v_mfma_f32_16x16x32_bf16 v[152:155], v[96:99], v[64:67], v[152:155]\n\t" \
  "ds_read_b128 v[68:71], v53 offset:4096\n\t" \
  "v_mfma_f32_16x16x32_bf16 v[152:155], v[144:147], v[140:143], v[152:155]\n\t" \
  "ds_read_b128 v[104:107], v52 offset:4096\n\t" \
  "v_mfma_f32_16x16x32_bf16 v[160:163], v[144:147], v[92:95], v[160:163]\n\t" \
  "ds_read_b128 v[80:83], v52 offset:6144\n\t" \
  "v_mfma_f32_16x16x32_bf16 v[160:163], v[96:99], v[84:87], v[160:163]\n\t" \
  "ds_read_b128 v[116:119], v53 offset:6144\n\t" \
  "v_mfma_f32_16x16x32_bf16 v[168:171], v[96:99], v[100:103], v[168:171]\n\t" \
  "ds_read_b128 v[88:91], v53 offset:8192\n\t" \
  "v_mfma_f32_16x16x32_bf16 v[168:171], v[144:147], v[112:115], v[168:171]\n\t" \
  "ds_read_b128 v[124:127], v52 offset:8192\n\t" \
  "v_mfma_f32_16x16x32_bf16 v[176:179], v[144:147], v[128:131], v[176:179]\n\t" \
  "ds_read_b64_tr_b16 v[144:145], v55 offset:34816\n\t" \
  "v_mfma_f32_16x16x32_bf16 v[176:179], v[96:99], v[120:123], v[176:179]\n\t" \
  "ds_read_b64_tr_b16 v[146:147], v55 offset:35328\n\t" \
  "v_mfma_f32_16x16x32_bf16 v[156:159], v[76:79], v[64:67], v[156:159]\n\t" \
  "ds_read_b64_tr_b16 v[96:97], v55 offset:38912\n\t" \
  "v_mfma_f32_16x16x32_bf16 v[156:159], v[132:135], v[140:143], v[156:159]\n\t" \
  "ds_read_b64_tr_b16 v[98:99], v55 offset:39424\n\t" \
  "v_mfma_f32_16x16x32_bf16 v[164:167], v[132:135], v[92:95], v[164:167]\n\t" \
  "ds_read_b64_tr_b16 v[64:65], v54 offset:26624\n\t" \
  "v_mfma_f32_16x16x32_bf16 v[164:167], v[76:79], v[84:87], v[164:167]\n\t" \
  "ds_read_b64_tr_b16 v[66:67], v54 offset:27136\n\t" \
  "v_mfma_f32_16x16x32_bf16 v[172:175], v[76:79], v[100:103], v[172:175]\n\t" \
  "ds_read_b64_tr_b16 v[140:141], v54 offset:30720\n\t" \
  "v_mfma_f32_16x16x32_bf16 v[172:175], v[132:135], v[112:115], v[172:175]\n\t" \
  "ds_read_b64_tr_b16 v[142:143], v54 offset:31232\n\t" \
  "v_mfma_f32_16x16x32_bf16 v[180:183], v[132:135], v[128:131], v[180:183]\n\t" \
  "ds_read_b128 v[92:95], v52 offset:0\n\t" \
  "v_mfma_f32_16x16x32_bf16 v[180:183], v[76:79], v[120:123], v[180:183]\n\t" \
  "ds_read_b128 v[84:87], v53 offset:0\n\t" \
  "s_waitcnt lgkmcnt(7)\n\t" \
  "v_mfma_f32_16x16x32_bf16 v[216:219], v[72:75], v[108:111], v[216:219]\n\t" \
  "ds_read_b64_tr_b16 v[100:101], v55 offset:26624\n\t" \
  "v_mfma_f32_16x16x32_bf16 v[216:219], v[148:151], v[136:139], v[216:219]\n\t" \
  "ds_read_b64_tr_b16 v[102:103], v55 offset:27136\n\t" \
  "v_mfma_f32_16x16x32_bf16 v[224:227], v[68:71], v[136:139], v[224:227]\n\t" \
  "ds_read_b64_tr_b16 v[112:113], v55 offset:30720\n\t" \
  "v_mfma_f32_16x16x32_bf16 v[224:227], v[104:107], v[108:111], v[224:227]\n\t" \
  "ds_read_b64_tr_b16 v[114:115], v55 offset:31232\n\t" \
  "v_mfma_f32_16x16x32_bf16 v[232:235], v[80:83], v[108:111], v[232:235]\n\t" \
  "ds_read_b64_tr_b16 v[128:129], v54 offset:18432\n\t" \
  "v_mfma_f32_16x16x32_bf16 v[232:235], v[116:119], v[136:139], v[232:235]\n\t" \
  "ds_read_b64_tr_b16 v[130:131], v54 offset:18944\n\t" \
  "v_mfma_f32_16x16x32_bf16 v[240:243], v[88:91], v[136:139], v[240:243]\n\t" \
  "ds_read_b64_tr_b16 v[132:133], v54 offset:22528\n\t" \
  "v_mfma_f32_16x16x32_bf16 v[240:243], v[124:127], v[108:111], v[240:243]\n\t" \
  "ds_read_b64_tr_b16 v[134:135], v54 offset:23040\n\t" \
  "v_mfma_f32_16x16x32_bf16 v[220:223], v[72:75], v[144:147], v[220:223]\n\t" \
  "ds_read_b64_tr_b16 v[76:77], v55 offset:18432\n\t" \
  "s_waitcnt lgkmcnt(7)\n\t" \
  "v_mfma_f32_16x16x32_bf16 v[220:223], v[148:151], v[96:99], v[220:223]\n\t" \
  "ds_read_b64_tr_b16 v[78:79], v55 offset:18944\n\t" \
  "v_mfma_f32_16x16x32_bf16 v[228:231], v[68:71], v[96:99], v[228:231]\n\t" \
  "ds_read_b64_tr_b16 v[120:121], v55 offset:22528\n\t" \
  "v_mfma_f32_16x16x32_bf16 v[228:231], v[104:107], v[144:147], v[228:231]\n\t" \
  "ds_read_b64_tr_b16 v[122:123], v55 offset:23040\n\t" \
  "v_mfma_f32_16x16x32_bf16 v[236:239], v[80:83], v[144:147], v[236:239]\n\t" \
  "v_mfma_f32_16x16x32_bf16 v[236:239], v[116:119], v[96:99], v[236:239]\n\t" \
  "v_mfma_f32_16x16x32_bf16 v[244:247], v[88:91], v[96:99], v[244:247]\n\t" \
  "v_mfma_f32_16x16x32_bf16 v[244:247], v[124:127], v[144:147], v[244:247]\n\t" \
  "v_mfma_f32_16x16x32_bf16 v[184:187], v[64:67], v[72:75], v[184:187]\n\t" \
  "v_mfma_f32_16x16x32_bf16 v[184:187], v[140:143], v[148:151], v[184:187]\n\t" \
  "v_mfma_f32_16x16x32_bf16 v[192:195], v[140:143], v[68:71], v[192:195]\n\t" \
  "v_mfma_f32_16x16x32_bf16 v[192:195], v[64:67], v[104:107], v[192:195]\n\t" \
  "v_mfma_f32_16x16x32_bf16 v[200:203], v[64:67], v[80:83], v[200:203]\n\t" \
  "v_mfma_f32_16x16x32_bf16 v[200:203], v[140:143], v[116:119], v[200:203]\n\t" \
  "v_mfma_f32_16x16x32_bf16 v[208:211], v[140:143], v[88:91], v[208:211]\n\t" \
  "v_mfma_f32_16x16x32_bf16 v[208:211], v[64:67], v[124:127], v[208:211]\n\t" \
  "v_mfma_f32_16x16x32_bf16 v[248:251], v[64:67], v[92:95], v[248:251]\n\t" \
  "v_mfma_f32_16x16x32_bf16 v[248:251], v[140:143], v[84:87], v[248:251]\n\t" \
  "v_mfma_f32_16x16x32_bf16 v[252:255], v[100:103], v[92:95], v[252:255]\n\t" \
  "s_waitcnt lgkmcnt(0)\n\t" \
  "v_mfma_f32_16x16x32_bf16 v[252:255], v[112:115], v[84:87], v[252:255]\n\t" \
  "v_mfma_f32_16x16x32_bf16 v[188:191], v[112:115], v[148:151], v[188:191]\n\t" \
  "v_mfma_f32_16x16x32_bf16 v[188:191], v[100:103], v[72:75], v[188:191]\n\t" \
  "s_barrier\n\t" \
  "v_mfma_f32_16x16x32_bf16 v[196:199], v[100:103], v[104:107], v[196:199]\n\t" \
  "ds_read_b64_tr_b16 v[96:97], v58 offset:34816\n\t" \
  "v_mfma_f32_16x16x32_bf16 v[196:199], v[112:115], v[68:71], v[196:199]\n\t" \
  "ds_read_b64_tr_b16 v[98:99], v58 offset:35328\n\t" \
  "v_mfma_f32_16x16x32_bf16 v[204:207], v[112:115], v[116:119], v[204:207]\n\t" \
  "ds_read_b128 v[136:139], v56 offset:2048\n\t" \
  "v_mfma_f32_16x16x32_bf16 v[204:207], v[100:103], v[80:83], v[204:207]\n\t" \
  "ds_read_b64_tr_b16 v[64:65], v58 offset:38912\n\t" \
  "v_mfma_f32_16x16x32_bf16 v[212:215], v[100:103], v[124:127], v[212:215]\n\t" \
  "ds_read_b64_tr_b16 v[66:67], v58 offset:39424\n\t" \
  "v_mfma_f32_16x16x32_bf16 v[212:215], v[112:115], v[88:91], v[212:215]\n\t" \
  "ds_read_b128 v[108:111], v57 offset:2048\n\t" \
  "v_mfma_f32_16x16x32_bf16 v[152:155], v[128:131], v[72:75], v[152:155]\n\t" \
  "ds_read_b128 v[144:147], v57 offset:4096\n\t" \
  "v_mfma_f32_16x16x32_bf16 v[152:155], v[132:135], v[148:151], v[152:155]\n\t" \
  "ds_read_b128 v[140:143], v56 offset:4096\n\t" \
  "v_mfma_f32_16x16x32_bf16 v[160:163], v[132:135], v[68:71], v[160:163]\n\t" \
  "ds_read_b128 v[112:115], v56 offset:6144\n\t" \
  "v_mfma_f32_16x16x32_bf16 v[160:163], v[128:131], v[104:107], v[160:163]\n\t" \
  "ds_read_b64_tr_b16 v[92:93], v59 offset:34816\n\t" \
  "v_mfma_f32_16x16x32_bf16 v[168:171], v[128:131], v[80:83], v[168:171]\n\t" \
  "ds_read_b64_tr_b16 v[94:95], v59 offset:35328\n\t" \
  "v_mfma_f32_16x16x32_bf16 v[168:171], v[132:135], v[116:119], v[168:171]\n\t" \
  "ds_read_b64_tr_b16 v[84:85], v59 offset:38912\n\t" \
  "v_mfma_f32_16x16x32_bf16 v[176:179], v[132:135], v[88:91], v[176:179]\n\t" \
  "ds_read_b128 v[132:135], v57 offset:6144\n\t" \
  "v_mfma_f32_16x16x32_bf16 v[176:179], v[128:131], v[124:127], v[176:179]\n\t" \
  "ds_read_b128 v[128:131], v57 offset:8192\n\t" \
  "v_mfma_f32_16x16x32_bf16 v[156:159], v[76:79], v[72:75], v[156:159]\n\t" \
  "ds_read_b64_tr_b16 v[86:87], v59 offset:39424\n\t" \
  "v_mfma_f32_16x16x32_bf16 v[156:159], v[120:123], v[148:151], v[156:159]\n\t" \
  "ds_read_b128 v[148:151], v56 offset:8192\n\t" \
  "v_mfma_f32_16x16x32_bf16 v[164:167], v[120:123], v[68:71], v[164:167]\n\t" \
  "ds_read_b64_tr_b16 v[100:101], v58 offset:26624\n\t" \
  "v_mfma_f32_16x16x32_bf16 v[164:167], v[76:79], v[104:107], v[164:167]\n\t" \
  "ds_read_b64_tr_b16 v[102:103], v58 offset:27136\n\t" \
  "v_mfma_f32_16x16x32_bf16 v[172:175], v[76:79], v[80:83], v[172:175]\n\t" \
  "ds_read_b64_tr_b16 v[72:73], v58 offset:30720\n\t" \
  "v_mfma_f32_16x16x32_bf16 v[172:175], v[120:123], v[116:119], v[172:175]\n\t" \
  "ds_read_b64_tr_b16 v[74:75], v58 offset:31232\n\t" \
  "v_mfma_f32_16x16x32_bf16 v[180:183], v[120:123], v[88:91], v[180:183]\n\t" \
  "ds_read_b128 v[68:71], v56 offset:0\n\t" \
  "v_mfma_f32_16x16x32_bf16 v[180:183], v[76:79], v[124:127], v[180:183]\n\t" \
  "ds_read_b128 v[104:107], v57 offset:0\n\t" \
  "s_waitcnt lgkmcnt(7)\n\t" \
  "v_mfma_f32_16x16x32_bf16 v[216:219], v[136:139], v[96:99], v[216:219]\n\t" \
  "ds_read_b64_tr_b16 v[80:81], v59 offset:26624\n\t" \
  "v_mfma_f32_16x16x32_bf16 v[216:219], v[108:111], v[64:67], v[216:219]\n\t" \
  "ds_read_b64_tr_b16 v[82:83], v59 offset:27136\n\t" \
  "v_mfma_f32_16x16x32_bf16 v[224:227], v[144:147], v[64:67], v[224:227]\n\t" \
  "ds_read_b64_tr_b16 v[88:89], v59 offset:30720\n\t" \
  "v_mfma_f32_16x16x32_bf16 v[224:227], v[140:143], v[96:99], v[224:227]\n\t" \
  "ds_read_b64_tr_b16 v[90:91], v59 offset:31232\n\t" \
  "v_mfma_f32_16x16x32_bf16 v[232:235], v[112:115], v[96:99], v[232:235]\n\t" \
  "ds_read_b64_tr_b16 v[116:117], v58 offset:18432\n\t" \
  "v_mfma_f32_16x16x32_bf16 v[232:235], v[132:135], v[64:67], v[232:235]\n\t" \
  "ds_read_b64_tr_b16 v[118:119], v58 offset:18944\n\t" \
  "v_mfma_f32_16x16x32_bf16 v[240:243], v[128:131], v[64:67], v[240:243]\n\t" \
  "ds_read_b64_tr_b16 v[120:121], v58 offset:22528\n\t" \
  "s_waitcnt lgkmcnt(7)\n\t" \
  "v_mfma_f32_16x16x32_bf16 v[240:243], v[148:151], v[96:99], v[240:243]\n\t" \
  "ds_read_b64_tr_b16 v[122:123], v58 offset:23040\n\t" \
  "v_mfma_f32_16x16x32_bf16 v[220:223], v[136:139], v[92:95], v[220:223]\n\t" \
  "ds_read_b64_tr_b16 v[124:125], v59 offset:18432\n\t" \
  "v_mfma_f32_16x16x32_bf16 v[220:223], v[108:111], v[84:87], v[220:223]\n\t" \
  "ds_read_b64_tr_b16 v[126:127], v59 offset:18944\n\t" \
  "v_mfma_f32_16x16x32_bf16 v[228:231], v[144:147], v[84:87], v[228:231]\n\t" \
  "ds_read_b64_tr_b16 v[76:77], v59 offset:22528\n\t" \
  "v_mfma_f32_16x16x32_bf16 v[228:231], v[140:143], v[92:95], v[228:231]\n\t" \
  "ds_read_b64_tr_b16 v[78:79], v59 offset:23040\n\t" \
  "v_mfma_f32_16x16x32_bf16 v[236:239], v[112:115], v[92:95], v[236:239]\n\t" \
  "v_mfma_f32_16x16x32_bf16 v[236:239], v[132:135], v[84:87], v[236:239]\n\t" \
  "v_mfma_f32_16x16x32_bf16 v[244:247], v[128:131], v[84:87], v[244:247]\n\t" \
  "v_mfma_f32_16x16x32_bf16 v[244:247], v[148:151], v[92:95], v[244:247]\n\t" \
  "v_mfma_f32_16x16x32_bf16 v[184:187], v[100:103], v[136:139], v[184:187]\n\t" \
  "v_mfma_f32_16x16x32_bf16 v[184:187], v[72:75], v[108:111], v[184:187]\n\t" \
  "v_mfma_f32_16x16x32_bf16 v[192:195], v[72:75], v[144:147], v[192:195]\n\t" \
  "v_and_b32 v14, -16, v32\n\t" \
  "v_mfma_f32_16x16x32_bf16 v[192:195], v[100:103], v[140:143], v[192:195]\n\t" \
  "v_mfma_f32_16x16x32_bf16 v[200:203], v[100:103], v[112:115], v[200:203]\n\t" \
  "ds_read_b128 v[10:13], v14 offset:128\n\t" \
  "v_mfma_f32_16x16x32_bf16 v[200:203], v[72:75], v[132:135], v[200:203]\n\t" \
  "v_mfma_f32_16x16x32_bf16 v[208:211], v[72:75], v[128:131], v[208:211]\n\t" \
  "v_mfma_f32_16x16x32_bf16 v[208:211], v[100:103], v[148:151], v[208:211]\n\t" \
  "v_mfma_f32_16x16x32_bf16 v[248:251], v[100:103], v[68:71], v[248:251]\n\t" \
  "v_mfma_f32_16x16x32_bf16 v[248:251], v[72:75], v[104:107], v[248:251]\n\t" \
  "s_waitcnt lgkmcnt(1)\n\t" \
  "v_mfma_f32_16x16x32_bf16 v[252:255], v[80:83], v[68:71], v[252:255]\n\t" \
  "s_waitcnt lgkmcnt(0)\n\t" \
  "v_and_b32 v10, v10, v11\n\t" \
  "v_and_b32 v12, v12, v13\n\t" \
  "v_mfma_f32_16x16x32_bf16 v[252:255], v[88:91], v[104:107], v[252:255]\n\t" \
  "v_and_b32 v10, v10, v12\n\t" \
  "v_mfma_f32_16x16x32_bf16 v[188:191], v[88:91], v[108:111], v[188:191]\n\t" \
  "v_readfirstlane_b32 vcc_lo, v10\n\t" \
  "v_mfma_f32_16x16x32_bf16 v[188:191], v[80:83], v[136:139], v[188:191]\n\t" \
  "ds_write_b32 v32, v0 offset:144\n\t" \
  "s_cmp_eq_u32 vcc_lo, 0\n\t" \
  "s_cbranch_scc1 15f\n\t" \
  "14:\n\t" \
  "v_mfma_f32_16x16x32_bf16 v[196:199], v[80:83], v[140:143], v[196:199]\n\t" \
  "ds_read_b64_tr_b16 v[64:65], v62 offset:26624\n\t" \
  "v_mfma_f32_16x16x32_bf16 v[196:199], v[88:91], v[144:147], v[196:199]\n\t" \
  "ds_read_b64_tr_b16 v[66:67], v62 offset:27136\n\t" \
  "v_mfma_f32_16x16x32_bf16 v[204:207], v[88:91], v[132:135], v[204:207]\n\t" \
  "ds_read_b128 v[96:99], v60 offset:0\n\t" \
  "v_mfma_f32_16x16x32_bf16 v[204:207], v[80:83], v[112:115], v[204:207]\n\t" \
  "ds_read_b128 v[84:87], v60 offset:2048\n\t" \
  "v_mfma_f32_16x16x32_bf16 v[212:215], v[80:83], v[148:151], v[212:215]\n\t" \
  "ds_read_b128 v[92:95], v60 offset:4096\n\t" \
  "v_mfma_f32_16x16x32_bf16 v[212:215], v[88:91], v[128:131], v[212:215]\n\t" \
  "ds_read_b128 v[100:103], v60 offset:6144\n\t" \
  "v_mfma_f32_16x16x32_bf16 v[152:155], v[116:119], v[136:139], v[152:155]\n\t" \
  "ds_read_b128 v[72:75], v60 offset:8192\n\t" \
  "v_mfma_f32_16x16x32_bf16 v[152:155], v[120:123], v[108:111], v[152:155]\n\t" \
  "ds_read_b64_tr_b16 v[68:69], v63 offset:26624\n\t" \
  "v_mfma_f32_16x16x32_bf16 v[160:163], v[120:123], v[144:147], v[160:163]\n\t" \
  "ds_read_b64_tr_b16 v[70:71], v63 offset:27136\n\t" \
  "v_mfma_f32_16x16x32_bf16 v[160:163], v[116:119], v[140:143], v[160:163]\n\t" \
  "ds_read_b64_tr_b16 v[104:105], v62 offset:34816\n\t" \
  "v_mfma_f32_16x16x32_bf16 v[168:171], v[116:119], v[112:115], v[168:171]\n\t" \
  "ds_read_b64_tr_b16 v[106:107], v62 offset:35328\n\t" \
  "v_mfma_f32_16x16x32_bf16 v[168:171], v[120:123], v[132:135], v[168:171]\n\t" \
  "ds_read_b64_tr_b16 v[80:81], v63 offset:34816\n\t" \
  "v_mfma_f32_16x16x32_bf16 v[176:179], v[120:123], v[128:131], v[176:179]\n\t" \
  "ds_read_b64_tr_b16 v[82:83], v63 offset:35328\n\t" \
  "v_mfma_f32_16x16x32_bf16 v[176:179], v[116:119], v[148:151], v[176:179]\n\t" \
  "ds_read_b64_tr_b16 v[88:89], v62 offset:18432\n\t" \
  "v_mfma_f32_16x16x32_bf16 v[156:159], v[124:127], v[136:139], v[156:159]\n\t" \
  "ds_read_b64_tr_b16 v[90:91], v62 offset:18944\n\t" \
  "v_mfma_f32_16x16x32_bf16 v[156:159], v[76:79], v[108:111], v[156:159]\n\t" \
  "ds_read_b64_tr_b16 v[120:121], v62 offset:30720\n\t" \
  "v_mfma_f32_16x16x32_bf16 v[164:167], v[76:79], v[144:147], v[164:167]\n\t" \
  "ds_read_b64_tr_b16 v[122:123], v62 offset:31232\n\t" \
  "v_mfma_f32_16x16x32_bf16 v[164:167], v[124:127], v[140:143], v[164:167]\n\t" \
  "ds_read_b128 v[116:119], v61 offset:0\n\t" \
  "v_mfma_f32_16x16x32_bf16 v[172:175], v[124:127], v[112:115], v[172:175]\n\t" \
  "ds_read_b128 v[136:139], v61 offset:2048\n\t" \
  "v_mfma_f32_16x16x32_bf16 v[172:175], v[76:79], v[132:135], v[172:175]\n\t" \
  "ds_read_b128 v[108:111], v61 offset:4096\n\t" \
  "v_mfma_f32_16x16x32_bf16 v[180:183], v[76:79], v[128:131], v[180:183]\n\t" \
  "ds_read_b64_tr_b16 v[76:77], v63 offset:18432\n\t" \
  "v_mfma_f32_16x16x32_bf16 v[180:183], v[124:127], v[148:151], v[180:183]\n\t" \
  "ds_read_b64_tr_b16 v[78:79], v63 offset:18944\n\t" \
  "s_waitcnt lgkmcnt(7)\n\t" \
  "v_mfma_f32_16x16x32_bf16 v[248:251], v[64:67], v[96:99], v[248:251]\n\t" \
  "ds_read_b128 v[144:147], v61 offset:6144\n\t" \
  "v_mfma_f32_16x16x32_bf16 v[184:187], v[64:67], v[84:87], v[184:187]\n\t" \
  "ds_read_b128 v[140:143], v61 offset:8192\n\t" \
  "v_mfma_f32_16x16x32_bf16 v[192:195], v[64:67], v[92:95], v[192:195]\n\t" \
  "ds_read_b64_tr_b16 v[112:113], v63 offset:30720\n\t" \
  "v_mfma_f32_16x16x32_bf16 v[200:203], v[64:67], v[100:103], v[200:203]\n\t" \
  "ds_read_b64_tr_b16 v[114:115], v63 offset:31232\n\t" \
  "v_mfma_f32_16x16x32_bf16 v[208:211], v[64:67], v[72:75], v[208:211]\n\t" \
  "ds_read_b64_tr_b16 v[132:133], v62 offset:22528\n\t" \
  "v_mfma_f32_16x16x32_bf16 v[252:255], v[68:71], v[96:99], v[252:255]\n\t" \
  "ds_read_b64_tr_b16 v[134:135], v62 offset:23040\n\t" \
  "v_mfma_f32_16x16x32_bf16 v[188:191], v[68:71], v[84:87], v[188:191]\n\t" \
  "ds_read_b64_tr_b16 v[128:129], v63 offset:22528\n\t" \
  "v_mfma_f32_16x16x32_bf16 v[196:199], v[68:71], v[92:95], v[196:199]\n\t" \
  "ds_read_b64_tr_b16 v[130:131], v63 offset:23040\n\t" \
  "v_mfma_f32_16x16x32_bf16 v[204:207], v[68:71], v[100:103], v[204:207]\n\t" \
  "ds_read_b64_tr_b16 v[124:125], v62 offset:38912\n\t" \
  "v_mfma_f32_16x16x32_bf16 v[212:215], v[68:71], v[72:75], v[212:215]\n\t" \
  "ds_read_b64_tr_b16 v[126:127], v62 offset:39424\n\t" \
  "v_mfma_f32_16x16x32_bf16 v[216:219], v[84:87], v[104:107], v[216:219]\n\t" \
  "ds_read_b64_tr_b16 v[148:149], v63 offset:38912\n\t" \
  "v_mfma_f32_16x16x32_bf16 v[224:227], v[92:95], v[104:107], v[224:227]\n\t" \
  "ds_read_b64_tr_b16 v[150:151], v63 offset:39424\n\t" \
  "v_mfma_f32_16x16x32_bf16 v[232:235], v[100:103], v[104:107], v[232:235]\n\t" \
  "v_mfma_f32_16x16x32_bf16 v[240:243], v[72:75], v[104:107], v[240:243]\n\t" \
  "v_mfma_f32_16x16x32_bf16 v[220:223], v[84:87], v[80:83], v[220:223]\n\t" \
  "v_mfma_f32_16x16x32_bf16 v[228:231], v[92:95], v[80:83], v[228:231]\n\t" \
  "v_mfma_f32_16x16x32_bf16 v[236:239], v[100:103], v[80:83], v[236:239]\n\t" \
  "v_mfma_f32_16x16x32_bf16 v[244:247], v[72:75], v[80:83], v[244:247]\n\t" \
  "v_mfma_f32_16x16x32_bf16 v[152:155], v[88:91], v[84:87], v[152:155]\n\t" \
  "v_mfma_f32_16x16x32_bf16 v[160:163], v[88:91], v[92:95], v[160:163]\n\t" \
  "v_mfma_f32_16x16x32_bf16 v[168:171], v[88:91], v[100:103], v[168:171]\n\t" \
  "v_mfma_f32_16x16x32_bf16 v[176:179], v[88:91], v[72:75], v[176:179]\n\t" \
  "s_waitcnt lgkmcnt(0)\n\t" \
  "v_mfma_f32_16x16x32_bf16 v[156:159], v[76:79], v[84:87], v[156:159]\n\t" \
  "v_mfma_f32_16x16x32_bf16 v[164:167], v[76:79], v[92:95], v[164:167]\n\t" \
  "ds_write_b32 v32, v0\n\t" \
  "v_mfma_f32_16x16x32_bf16 v[172:175], v[76:79], v[100:103], v[172:175]\n\t" \
  "v_mfma_f32_16x16x32_bf16 v[180:183], v[76:79], v[72:75], v[180:183]\n\t" \
  "v_mfma_f32_16x16x32_bf16 v[248:251], v[120:123], v[116:119], v[248:251]\n\t" \
  "v_and_b32 v56, -16, v32\n\t" \
  "v_mfma_f32_16x16x32_bf16 v[184:187], v[120:123], v[136:139], v[184:187]\n\t" \
  "v_mfma_f32_16x16x32_bf16 v[192:195], v[120:123], v[108:111], v[192:195]\n\t" \
  "v_mfma_f32_16x16x32_bf16 v[200:203], v[120:123], v[144:147], v[200:203]\n\t" \
  "v_mfma_f32_16x16x32_bf16 v[208:211], v[120:123], v[140:143], v[208:211]\n\t" \
  "v_mfma_f32_16x16x32_bf16 v[252:255], v[112:115], v[116:119], v[252:255]\n\t" \
  "v_mfma_f32_16x16x32_bf16 v[188:191], v[112:115], v[136:139], v[188:191]\n\t" \
  "v_mfma_f32_16x16x32_bf16 v[196:199], v[112:115], v[108:111], v[196:199]\n\t" \
  "v_cvt_pk_bf16_f32 v10, v248, v249\n\t" \
  "v_cvt_pk_bf16_f32 v11, v250, v251\n\t" \
  "v_mfma_f32_16x16x32_bf16 v[204:207], v[112:115], v[144:147], v[204:207]\n\t" \
  "v_cvt_pk_bf16_f32 v14, v184, v185\n\t" \
  "v_cvt_pk_bf16_f32 v15, v186, v187\n\t" \
  "v_mfma_f32_16x16x32_bf16 v[212:215], v[112:115], v[140:143], v[212:215]\n\t" \
  "v_cvt_pk_bf16_f32 v12, v252, v253\n\t" \
  "v_cvt_pk_bf16_f32 v13, v254, v255\n\t" \
  "v_mfma_f32_16x16x32_bf16 v[152:155], v[132:135], v[136:139], v[152:155]\n\t" \
  "ds_write_b128 v30, v[10:13]\n\t" \
  "ds_write_b32 v32, v0 offset:48\n\t" \
  "v_mfma_f32_16x16x32_bf16 v[160:163], v[132:135], v[108:111], v[160:163]\n\t" \
  "v_cvt_pk_bf16_f32 v16, v188, v189\n\t" \
  "v_cvt_pk_bf16_f32 v17, v190, v191\n\t" \
  "v_mfma_f32_16x16x32_bf16 v[168:171], v[132:135], v[144:147], v[168:171]\n\t" \
  "v_cvt_pk_bf16_f32 v18, v192, v193\n\t" \
  "v_cvt_pk_bf16_f32 v19, v194, v195\n\t" \
  "v_mfma_f32_16x16x32_bf16 v[176:179], v[132:135], v[140:143], v[176:179]\n\t" \
  "v_cvt_pk_bf16_f32 v20, v196, v197\n\t" \
  "v_cvt_pk_bf16_f32 v21, v198, v199\n\t" \
  "v_mfma_f32_16x16x32_bf16 v[156:159], v[128:131], v[136:139], v[156:159]\n\t" \
  "v_cvt_pk_bf16_f32 v22, v200, v201\n\t" \
  "v_cvt_pk_bf16_f32 v23, v202, v203\n\t" \
  "v_mfma_f32_16x16x32_bf16 v[164:167], v[128:131], v[108:111], v[164:167]\n\t" \
  "v_cvt_pk_bf16_f32 v24, v204, v205\n\t" \
  "v_cvt_pk_bf16_f32 v25, v206, v207\n\t" \
  "v_mfma_f32_16x16x32_bf16 v[172:175], v[128:131], v[144:147], v[172:175]\n\t" \
  "v_cvt_pk_bf16_f32 v26, v208, v209\n\t" \
  "v_cvt_pk_bf16_f32 v27, v210, v211\n\t" \
  "v_mfma_f32_16x16x32_bf16 v[180:183], v[128:131], v[140:143], v[180:183]\n\t" \
  "v_cvt_pk_bf16_f32 v28, v212, v213\n\t" \
  "v_cvt_pk_bf16_f32 v29, v214, v215\n\t" \
  "v_mfma_f32_16x16x32_bf16 v[216:219], v[136:139], v[124:127], v[216:219]\n\t" \
  "v_cvt_pk_bf16_f32 v84, v152, v153\n\t" \
  "v_cvt_pk_bf16_f32 v85, v154, v155\n\t" \
  "v_mfma_f32_16x16x32_bf16 v[224:227], v[108:111], v[124:127], v[224:227]\n\t" \
  "v_cvt_pk_bf16_f32 v86, v156, v157\n\t" \
  "v_cvt_pk_bf16_f32 v87, v158, v159\n\t" \
  "v_mfma_f32_16x16x32_bf16 v[232:235], v[144:147], v[124:127], v[232:235]\n\t" \
  "v_cvt_pk_bf16_f32 v88, v160, v161\n\t" \
  "v_cvt_pk_bf16_f32 v89, v162, v163\n\t" \
  "v_mfma_f32_16x16x32_bf16 v[240:243], v[140:143], v[124:127], v[240:243]\n\t" \
  "v_cvt_pk_bf16_f32 v90, v164, v165\n\t" \
  "v_cvt_pk_bf16_f32 v91, v166, v167\n\t" \
  "v_mfma_f32_16x16x32_bf16 v[220:223], v[136:139], v[148:151], v[220:223]\n\t" \
  "v_cvt_pk_bf16_f32 v92, v168, v169\n\t" \
  "v_cvt_pk_bf16_f32 v93, v170, v171\n\t" \
  "v_mfma_f32_16x16x32_bf16 v[228:231], v[108:111], v[148:151], v[228:231]\n\t" \
  "v_cvt_pk_bf16_f32 v94, v172, v173\n\t" \
  "v_cvt_pk_bf16_f32 v95, v174, v175\n\t" \
  "v_mfma_f32_16x16x32_bf16 v[236:239], v[144:147], v[148:151], v[236:239]\n\t" \
  "v_cvt_pk_bf16_f32 v96, v176, v177\n\t" \
  "v_cvt_pk_bf16_f32 v97, v178, v179\n\t" \
  "v_mfma_f32_16x16x32_bf16 v[244:247], v[140:143], v[148:151], v[244:247]\n\t" \
  "v_cvt_pk_bf16_f32 v98, v180, v181\n\t" \
  "v_cvt_pk_bf16_f32 v99, v182, v183\n\t" \
  "v_mfma_f32_16x16x32_bf16 v[200:203], v[22:25], v[92:95], v[34:37]\n\t" \
  "v_cvt_pk_bf16_f32 v66, v232, v233\n\t" \
  "v_cvt_pk_bf16_f32 v67, v234, v235\n\t" \
  "v_mfma_f32_16x16x32_bf16 v[208:211], v[26:29], v[96:99], v[34:37]\n\t" \
  "v_cvt_pk_bf16_f32 v76, v236, v237\n\t" \
  "v_cvt_pk_bf16_f32 v77, v238, v239\n\t" \
  "v_mfma_f32_16x16x32_bf16 v[212:215], v[22:25], v[96:99], v[38:41]\n\t" \
  "v_cvt_pk_bf16_f32 v64, v240, v241\n\t" \
  "v_cvt_pk_bf16_f32 v65, v242, v243\n\t" \
  "v_mfma_f32_16x16x32_bf16 v[204:207], v[18:21], v[92:95], v[38:41]\n\t" \
  "v_cvt_pk_bf16_f32 v74, v244, v245\n\t" \
  "v_cvt_pk_bf16_f32 v75, v246, v247\n\t" \
  "v_mfma_f32_16x16x32_bf16 v[192:195], v[18:21], v[88:91], v[34:37]\n\t" \
  "v_cvt_pk_bf16_f32 v68, v224, v225\n\t" \
  "v_cvt_pk_bf16_f32 v69, v226, v227\n\t" \
  "v_mfma_f32_16x16x32_bf16 v[196:199], v[14:17], v[88:91], v[38:41]\n\t" \
  "v_max_f32 v208, v208, v212\n\t" \
  "v_max_f32 v209, v209, v213\n\t" \
  "v_mfma_f32_16x16x32_bf16 v[184:187], v[14:17], v[84:87], v[34:37]\n\t" \
  "v_max_f32 v210, v210, v214\n\t" \
  "v_max_f32 v200, v200, v204\n\t" \
  "v_mfma_f32_16x16x32_bf16 v[188:191], v[10:13], v[84:87], v[42:45]\n\t" \
  "v_max_f32 v201, v201, v205\n\t" \
  "v_max_f32 v202, v202, v206\n\t" \
  "v_max_f32 v192, v192, v196\n\t" \
  "v_max_f32 v193, v193, v197\n\t" \
  "v_max_f32 v194, v194, v198\n\t" \
  "v_max_f32 v211, v211, v215\n\t" \
  "ds_read_b32 v52, v33 offset:48\n\t" \
  "ds_read_b64 v[72:73], v31\n\t" \
  "ds_read_b64 v[82:83], v31 offset:8\n\t" \
  "v_max3_f32 v106, v208, v209, v210\n\t" \
  "v_max_f32 v184, v184, v188\n\t" \
  "v_max_f32 v185, v185, v189\n\t" \
  "v_max_f32 v186, v186, v190\n\t" \
  "v_max_f32 v203, v203, v207\n\t" \
  "v_max3_f32 v104, v200, v201, v202\n\t" \
  "v_max_f32 v195, v195, v199\n\t" \
  "v_max3_f32 v102, v192, v193, v194\n\t" \
  "v_max_f32 v187, v187, v191\n\t" \
  "v_max3_f32 v100, v184, v185, v186\n\t" \
  "v_max_f32 v106, v106, v211\n\t" \
  "v_max_f32 v104, v104, v203\n\t" \
  "v_max_f32 v102, v102, v195\n\t" \
  "v_max_f32 v100, v100, v187\n\t" \
  "v_permlane16_swap_b32 v104, v106\n\t" \
  "v_cvt_pk_bf16_f32 v78, v228, v229\n\t" \
  "v_permlane16_swap_b32 v100, v102\n\t" \
  "v_max_f32 v104, v104, v106\n\t" \
  "v_max_f32 v100, v100, v102\n\t" \
  "v_cvt_pk_bf16_f32 v79, v230, v231\n\t" \
  "v_cvt_pk_bf16_f32 v70, v216, v217\n\t" \
  "v_permlane32_swap_b32 v100, v104\n\t" \
  "v_cvt_pk_bf16_f32 v71, v218, v219\n\t" \
  "v_max_f32 v100, v100, v104\n\t" \
  "v_mul_f32 v100, 0xbe8293ee, v100\n\t" \
  "v_mov_b32 v104, v100\n\t" \
  "v_cvt_pk_bf16_f32 v80, v220, v221\n\t" \
  "v_cvt_pk_bf16_f32 v81, v222, v223\n\t" \
  "v_permlane32_swap_b32 v100, v104\n\t" \
  "s_waitcnt lgkmcnt(0)\n\t" \
  "v_readfirstlane_b32 vcc_lo, v52\n\t" \
  "s_nop 1\n\t" \
  "s_cmp_eq_u32 vcc_lo, 0\n\t" \
  "s_cbranch_scc1 5f\n\t" \
  "4:\n\t" \
  "v_mov_b32 v106, v104\n\t" \
  "v_mov_b32 v102, v100\n\t" \
  "s_nop 0\n\t" \
  "v_permlane16_swap_b32 v104, v106\n\t" \
  "v_permlane16_swap_b32 v100, v102\n\t" \
  "v_fma_f32 v208, v208, %16, v106\n\t" \
  "v_fma_f32 v209, v209, %16, v106\n\t" \
  "v_fma_f32 v210, v210, %16, v106\n\t" \
  "v_fma_f32 v211, v211, %16, v106\n\t" \
  "v_exp_f32 v208, v208\n\t" \
  "v_exp_f32 v209, v209\n\t" \
  "v_exp_f32 v210, v210\n\t" \
  "v_exp_f32 v211, v211\n\t" \
  "v_cvt_pk_bf16_f32 v212, v208, v209\n\t" \
  "v_cvt_pk_bf16_f32 v213, v210, v211\n\t" \
  "v_bfi_b32 v214, v46, 0, v212\n\t" \
  "v_bfi_b32 v215, v47, 0, v213\n\t" \
  "v_and_b32 v212, v46, v212\n\t" \
  "v_and_b32 v213, v47, v213\n\t" \
  "v_fma_f32 v200, v200, %16, v104\n\t" \
  "v_fma_f32 v201, v201, %16, v104\n\t" \
  "v_mfma_f32_16x16x32_bf16 v[208:211], v[48:51], v[212:215], 0\n\t" \
  "v_fma_f32 v202, v202, %16, v104\n\t" \
  "v_fma_f32 v203, v203, %16, v104\n\t" \
  "v_mfma_f32_16x16x32_bf16 v[176:179], v[64:67], v[212:215], 0\n\t" \
  "v_exp_f32 v200, v200\n\t" \
  "v_mfma_f32_16x16x32_bf16 v[180:183], v[74:77], v[212:215], 0\n\t" \
  "v_exp_f32 v201, v201\n\t" \
  "v_exp_f32 v202, v202\n\t" \
  "v_exp_f32 v203, v203\n\t" \
  "v_cvt_pk_bf16_f32 v204, v200, v201\n\t" \
  "v_cvt_pk_bf16_f32 v205, v202, v203\n\t" \
  "v_bfi_b32 v206, v46, 0, v204\n\t" \
  "v_bfi_b32 v207, v47, 0, v205\n\t" \
  "v_rcp_f32 v106, v208\n\t" \
  "v_and_b32 v204, v46, v204\n\t" \
  "v_and_b32 v205, v47, v205\n\t" \
  "v_pk_mul_f32 v[176:177], v[176:177], v[106:107] op_sel_hi:[1,0]\n\t" \
  "v_pk_mul_f32 v[178:179], v[178:179], v[106:107] op_sel_hi:[1,0]\n\t" \
  "v_mfma_f32_16x16x32_bf16 v[200:203], v[48:51], v[204:207], 0\n\t" \
  "v_pk_mul_f32 v[180:181], v[180:181], v[106:107] op_sel_hi:[1,0]\n\t" \
  "v_pk_mul_f32 v[182:183], v[182:183], v[106:107] op_sel_hi:[1,0]\n\t" \
  "v_mfma_f32_16x16x32_bf16 v[168:171], v[66:69], v[204:207], 0\n\t" \
  "v_cvt_pk_bf16_f32 v240, v176, v177\n\t" \
  "v_cvt_pk_bf16_f32 v241, v178, v179\n\t" \
  "v_mfma_f32_16x16x32_bf16 v[172:175], v[76:79], v[204:207], 0\n\t" \
  "v_cvt_pk_bf16_f32 v242, v180, v181\n\t" \
  "v_cvt_pk_bf16_f32 v243, v182, v183\n\t" \
  "global_store_dwordx4 v1, v[240:243], %14 offset:0 sc1\n\t" \
  "v_fma_f32 v192, v192, %16, v102\n\t" \
  "v_fma_f32 v193, v193, %16, v102\n\t" \
  "v_fma_f32 v194, v194, %16, v102\n\t" \
  "v_fma_f32 v195, v195, %16, v102\n\t" \
  "v_exp_f32 v192, v192\n\t" \
  "v_exp_f32 v193, v193\n\t" \
  "v_exp_f32 v194, v194\n\t" \
  "v_exp_f32 v195, v195\n\t" \
  "v_cvt_pk_bf16_f32 v196, v192, v193\n\t" \
  "v_cvt_pk_bf16_f32 v197, v194, v195\n\t" \
  "v_bfi_b32 v198, v46, 0, v196\n\t" \
  "v_bfi_b32 v199, v47, 0, v197\n\t" \
  "v_rcp_f32 v104, v200\n\t" \
  "v_and_b32 v196, v46, v196\n\t" \
  "v_and_b32 v197, v47, v197\n\t" \
  "v_pk_mul_f32 v[168:169], v[168:169], v[104:105] op_sel_hi:[1,0]\n\t" \
  "v_pk_mul_f32 v[170:171], v[170:171], v[104:105] op_sel_hi:[1,0]\n\t" \
  "v_mfma_f32_16x16x32_bf16 v[192:195], v[48:51], v[196:199], 0\n\t" \
  "v_pk_mul_f32 v[172:173], v[172:173], v[104:105] op_sel_hi:[1,0]\n\t" \
  "v_pk_mul_f32 v[174:175], v[174:175], v[104:105] op_sel_hi:[1,0]\n\t" \
  "v_mfma_f32_16x16x32_bf16 v[160:163], v[68:71], v[196:199], 0\n\t" \
  "v_cvt_pk_bf16_f32 v232, v168, v169\n\t" \
  "v_cvt_pk_bf16_f32 v233, v170, v171\n\t" \
  "v_mfma_f32_16x16x32_bf16 v[164:167], v[78:81], v[196:199], 0\n\t" \
  "v_cvt_pk_bf16_f32 v234, v172, v173\n\t" \
  "v_cvt_pk_bf16_f32 v235, v174, v175\n\t" \
  "global_store_dwordx4 v1, v[232:235], %14 offset:-4096 sc1\n\t" \
  "v_fma_f32 v184, v184, %16, v100\n\t" \
  "v_fma_f32 v185, v185, %16, v100\n\t" \
  "v_fma_f32 v186, v186, %16, v100\n\t" \
  "v_fma_f32 v187, v187, %16, v100\n\t" \
  "v_exp_f32 v184, v184\n\t" \
  "v_exp_f32 v185, v185\n\t" \
  "v_exp_f32 v186, v186\n\t" \
  "v_exp_f32 v187, v187\n\t" \
  "v_cvt_pk_bf16_f32 v188, v184, v185\n\t" \
  "v_cvt_pk_bf16_f32 v189, v186, v187\n\t" \
  "v_bfi_b32 v190, v46, 0, v188\n\t" \
  "v_bfi_b32 v191, v47, 0, v189\n\t" \
  "v_rcp_f32 v102, v192\n\t" \
  "v_and_b32 v188, v46, v188\n\t" \
  "v_and_b32 v189, v47, v189\n\t" \
  "v_pk_mul_f32 v[160:161], v[160:161], v[102:103] op_sel_hi:[1,0]\n\t" \
  "v_pk_mul_f32 v[162:163], v[162:163], v[102:103] op_sel_hi:[1,0]\n\t" \
  "v_mfma_f32_16x16x32_bf16 v[184:187], v[48:51], v[188:191], 0\n\t" \
  "v_pk_mul_f32 v[164:165], v[164:165], v[102:103] op_sel_hi:[1,0]\n\t" \
  "v_pk_mul_f32 v[166:167], v[166:167], v[102:103] op_sel_hi:[1,0]\n\t" \
  "v_mfma_f32_16x16x32_bf16 v[152:155], v[70:73], v[188:191], 0\n\t" \
  "v_cvt_pk_bf16_f32 v224, v160, v161\n\t" \
  "v_cvt_pk_bf16_f32 v225, v162, v163\n\t" \
  "v_mfma_f32_16x16x32_bf16 v[156:159], v[80:83], v[188:191], 0\n\t" \
  "v_cvt_pk_bf16_f32 v226, v164, v165\n\t" \
  "v_cvt_pk_bf16_f32 v227, v166, v167\n\t" \
  "global_store_dwordx4 v4, v[224:227], %14 offset:0 sc1\n\t" \
  "v_rcp_f32 v100, v184\n\t" \
  "ds_read_b128 v[60:63], v56 offset:64\n\t" \
  "ds_read_b64_tr_b16 v[108:109], v5 offset:0\n\t" \
  "ds_read_b64_tr_b16 v[110:111], v5 offset:16\n\t" \
  "ds_read_b128 v[112:115], v6 offset:16384\n\t" \
  "ds_read_b128 v[116:119], v6 offset:15360\n\t" \
  "ds_read_b64_tr_b16 v[120:121], v5 offset:512\n\t" \
  "ds_read_b64_tr_b16 v[122:123], v5 offset:528\n\t" \
  "ds_read_b128 v[124:127], v6 offset:14336\n\t" \
  "ds_read_b64_tr_b16 v[128:129], v5 offset:1024\n\t" \
  "ds_read_b64_tr_b16 v[130:131], v5 offset:1040\n\t" \
  "ds_read_b128 v[132:135], v6 offset:13312\n\t" \
  "ds_read_b64_tr_b16 v[136:137], v5 offset:1536\n\t" \
  "ds_read_b64_tr_b16 v[138:139], v5 offset:1552\n\t" \
  "ds_read_b128 v[140:143], v6 offset:12288\n\t" \
  "ds_read_b64_tr_b16 v[144:145], v5 offset:2048\n\t" \
  "v_pk_mul_f32 v[152:153], v[152:153], v[100:101] op_sel_hi:[1,0]\n\t" \
  "v_pk_mul_f32 v[154:155], v[154:155], v[100:101] op_sel_hi:[1,0]\n\t" \
  "v_cvt_pk_bf16_f32 v216, v152, v153\n\t" \
  "v_pk_mul_f32 v[156:157], v[156:157], v[100:101] op_sel_hi:[1,0]\n\t" \
  "v_pk_mul_f32 v[158:159], v[158:159], v[100:101] op_sel_hi:[1,0]\n\t" \
  "v_cvt_pk_bf16_f32 v217, v154, v155\n\t" \
  "506:\n\t" \
  "s_cbranch_execz 510f\n\t" \
  "v_cvt_pk_bf16_f32 v218, v156, v157\n\t" \
  "v_cvt_pk_bf16_f32 v219, v158, v159\n\t" \
  "global_store_dwordx4 v4, v[216:219], %14 offset:-4096 sc1\n\t" \
  "ds_write_b32 v32, v0 offset:112\n\t" \
  "s_branch 9f\n\t" \
  "5:\n\t" \
  "ds_read_b32 v52, v33 offset:48\n\t" \
  "ds_read_b64 v[72:73], v31\n\t" \
  "ds_read_b64 v[82:83], v31 offset:8\n\t" \
  "s_waitcnt lgkmcnt(0)\n\t" \
  "v_readfirstlane_b32 vcc_lo, v52\n\t" \
  "s_nop 1\n\t" \
  "s_cmp_eq_u32 vcc_lo, 0\n\t" \
  "s_cbranch_scc1 5b\n\t" \
  "s_branch 4b\n\t" \
  "15:\n\t" \
  "ds_read_b128 v[10:13], v14 offset:128\n\t" \
  "s_waitcnt lgkmcnt(0)\n\t" \
  "v_and_b32 v10, v10, v11\n\t" \
  "v_and_b32 v12, v12, v13\n\t" \
  "v_and_b32 v10, v10, v12\n\t" \
  "s_nop 1\n\t" \
  "v_readfirstlane_b32 vcc_lo, v10\n\t" \
  "s_nop 1\n\t" \
  "s_cmp_eq_u32 vcc_lo, 0\n\t" \
  "s_cbranch_scc1 15b\n\t" \
  "s_branch 14b\n\t" \
  "9:\n\t" \
  "507:\n\t" \
  "s_cbranch_execz 26b\n\t" \
  "s_waitcnt lgkmcnt(14)\n\t" \
  "v_and_b32 v60, v60, v61\n\t" \
  "v_and_b32 v62, v62, v63\n\t" \
  "v_mov_b32 v12, %13\n\t" \
  "v_and_b32 v60, v60, v62\n\t" \
  "s_nop 0\n\t" \
  "v_readfirstlane_b32 vcc_lo, v60\n\t" \
  "s_nop 1\n\t" \
  "s_cmp_eq_u32 vcc_lo, 0\n\t" \
  "s_cbranch_scc1 7f\n\t" \
  "6:\n\t" \
  "s_waitcnt lgkmcnt(11)\n\t" \
  "v_mfma_f32_16x16x32_bf16 v[192:195], v[108:111], v[112:115], 0\n\t" \
  "ds_read_b64_tr_b16 v[146:147], v5 offset:2064\n\t" \
  "ds_read_b128 v[148:151], v6 offset:11264\n\t" \
  "s_waitcnt lgkmcnt(12)\n\t" \
  "v_mfma_f32_16x16x32_bf16 v[196:199], v[108:111], v[116:119], 0\n\t" \
  "ds_read_b64_tr_b16 v[64:65], v5 offset:2560\n\t" \
  "ds_read_b64_tr_b16 v[66:67], v5 offset:2576\n\t" \
  "s_waitcnt lgkmcnt(12)\n\t" \
  "v_mfma_f32_16x16x32_bf16 v[196:199], v[120:123], v[112:115], v[196:199]\n\t" \
  "ds_read_b128 v[68:71], v6 offset:10240\n\t" \
  "ds_read_b64_tr_b16 v[72:73], v5 offset:3072\n\t" \
  "s_waitcnt lgkmcnt(13)\n\t" \
  "v_mfma_f32_16x16x32_bf16 v[200:203], v[108:111], v[124:127], 0\n\t" \
  "ds_read_b64_tr_b16 v[74:75], v5 offset:3088\n\t" \
  "ds_read_b128 v[76:79], v6 offset:9216\n\t" \
  "v_mfma_f32_16x16x32_bf16 v[200:203], v[120:123], v[116:119], v[200:203]\n\t" \
  "ds_read_b64_tr_b16 v[80:81], v5 offset:3584\n\t" \
  "ds_read_b64_tr_b16 v[82:83], v5 offset:3600\n\t" \
  "s_waitcnt lgkmcnt(15)\n\t" \
  "v_mfma_f32_16x16x32_bf16 v[200:203], v[128:131], v[112:115], v[200:203]\n\t" \
  "ds_read_b128 v[84:87], v6 offset:0\n\t" \
  "ds_read_b128 v[88:91], v6 offset:1024\n\t" \
  "s_waitcnt lgkmcnt(15)\n\t" \
  "v_mfma_f32_16x16x32_bf16 v[204:207], v[108:111], v[132:135], 0\n\t" \
  "ds_read_b128 v[92:95], v6 offset:2048\n\t" \
  "ds_read_b128 v[96:99], v6 offset:3072\n\t" \
  "v_mfma_f32_16x16x32_bf16 v[204:207], v[120:123], v[124:127], v[204:207]\n\t" \
  "ds_read_b128 v[100:103], v6 offset:4096\n\t" \
  "ds_read_b128 v[104:107], v6 offset:5120\n\t" \
  "v_mfma_f32_16x16x32_bf16 v[204:207], v[128:131], v[116:119], v[204:207]\n\t" \
  "ds_read_b128 v[152:155], v6 offset:6144\n\t" \
  "ds_read_b128 v[156:159], v6 offset:7168\n\t" \
  "s_waitcnt lgkmcnt(14)\n\t" \
  "v_mfma_f32_16x16x32_bf16 v[204:207], v[136:139], v[112:115], v[204:207]\n\t" \
  "v_lshl_add_u32 v12, v12, 12, v4\n\t" \
  "s_lshl_b32 s88, %13, 5\n\t" \
  "v_mfma_f32_16x16x32_bf16 v[208:211], v[108:111], v[140:143], 0\n\t" \
  "v_mfma_f32_16x16x32_bf16 v[208:211], v[120:123], v[132:135], v[208:211]\n\t" \
  "v_mfma_f32_16x16x32_bf16 v[208:211], v[128:131], v[124:127], v[208:211]\n\t" \
  "s_and_b32 s88, s88, 64\n\t" \
  "v_xor_b32 v13, s88, v54\n\t" \
  "v_mfma_f32_16x16x32_bf16 v[208:211], v[136:139], v[116:119], v[208:211]\n\t" \
  "v_mfma_f32_16x16x32_bf16 v[208:211], v[144:147], v[112:115], v[208:211]\n\t" \
  "v_mfma_f32_16x16x32_bf16 v[212:215], v[108:111], v[148:151], 0\n\t" \
  "ds_read_b32 v55, v56 offset:104\n\t" \
  "v_mfma_f32_16x16x32_bf16 v[212:215], v[120:123], v[140:143], v[212:215]\n\t" \
  "v_mfma_f32_16x16x32_bf16 v[212:215], v[128:131], v[132:135], v[212:215]\n\t" \
  "v_mfma_f32_16x16x32_bf16 v[212:215], v[136:139], v[124:127], v[212:215]\n\t" \
  "v_mfma_f32_16x16x32_bf16 v[212:215], v[144:147], v[116:119], v[212:215]\n\t" \
  "v_mfma_f32_16x16x32_bf16 v[212:215], v[64:67], v[112:115], v[212:215]\n\t" \
  "s_waitcnt lgkmcnt(1)\n\t" \
  "v_mfma_f32_16x16x32_bf16 v[216:219], v[108:111], v[68:71], 0\n\t" \
  "v_mfma_f32_16x16x32_bf16 v[216:219], v[120:123], v[148:151], v[216:219]\n\t" \
  "v_mfma_f32_16x16x32_bf16 v[216:219], v[128:131], v[140:143], v[216:219]\n\t" \
  "v_mfma_f32_16x16x32_bf16 v[216:219], v[136:139], v[132:135], v[216:219]\n\t" \
  "v_mfma_f32_16x16x32_bf16 v[216:219], v[144:147], v[124:127], v[216:219]\n\t" \
  "v_mfma_f32_16x16x32_bf16 v[216:219], v[64:67], v[116:119], v[216:219]\n\t" \
  "v_mfma_f32_16x16x32_bf16 v[216:219], v[72:75], v[112:115], v[216:219]\n\t" \
  "v_mfma_f32_16x16x32_bf16 v[220:223], v[108:111], v[76:79], 0\n\t" \
  "v_mfma_f32_16x16x32_bf16 v[220:223], v[120:123], v[68:71], v[220:223]\n\t" \
  "v_mfma_f32_16x16x32_bf16 v[220:223], v[128:131], v[148:151], v[220:223]\n\t" \
  "v_mfma_f32_16x16x32_bf16 v[220:223], v[136:139], v[140:143], v[220:223]\n\t" \
  "v_mfma_f32_16x16x32_bf16 v[220:223], v[144:147], v[132:135], v[220:223]\n\t" \
  "ds_read_b64_tr_b16 v[160:161], v5 offset:128\n\t" \
  "v_mfma_f32_16x16x32_bf16 v[220:223], v[64:67], v[124:127], v[220:223]\n\t" \
  "ds_read_b64_tr_b16 v[162:163], v5 offset:144\n\t" \
  "v_mfma_f32_16x16x32_bf16 v[220:223], v[72:75], v[116:119], v[220:223]\n\t" \
  "ds_read_b128 v[164:167], v7 offset:16384\n\t" \
  "v_mfma_f32_16x16x32_bf16 v[220:223], v[80:83], v[112:115], v[220:223]\n\t" \
  "ds_read_b128 v[168:171], v7 offset:15360\n\t" \
  "v_mfma_f32_16x16x32_bf16 v[252:255], v[108:111], v[84:87], 0\n\t" \
  "ds_read_b64_tr_b16 v[172:173], v5 offset:640\n\t" \
  "v_mfma_f32_16x16x32_bf16 v[252:255], v[120:123], v[88:91], v[252:255]\n\t" \
  "ds_read_b64_tr_b16 v[174:175], v5 offset:656\n\t" \
  "v_mfma_f32_16x16x32_bf16 v[252:255], v[128:131], v[92:95], v[252:255]\n\t" \
  "ds_read_b128 v[176:179], v7 offset:14336\n\t" \
  "v_mfma_f32_16x16x32_bf16 v[252:255], v[136:139], v[96:99], v[252:255]\n\t" \
  "ds_read_b64_tr_b16 v[180:181], v5 offset:1152\n\t" \
  "v_mfma_f32_16x16x32_bf16 v[252:255], v[144:147], v[100:103], v[252:255]\n\t" \
  "ds_read_b64_tr_b16 v[182:183], v5 offset:1168\n\t" \
  "v_mfma_f32_16x16x32_bf16 v[252:255], v[64:67], v[104:107], v[252:255]\n\t" \
  "ds_read_b128 v[184:187], v7 offset:13312\n\t" \
  "v_mfma_f32_16x16x32_bf16 v[252:255], v[72:75], v[152:155], v[252:255]\n\t" \
  "ds_read_b64_tr_b16 v[188:189], v5 offset:1664\n\t" \
  "v_mfma_f32_16x16x32_bf16 v[252:255], v[80:83], v[156:159], v[252:255]\n\t" \
  "ds_read_b64_tr_b16 v[190:191], v5 offset:1680\n\t" \
  "v_mfma_f32_16x16x32_bf16 v[248:251], v[108:111], v[88:91], 0\n\t" \
  "ds_read_b128 v[76:79], v7 offset:12288\n\t" \
  "v_mfma_f32_16x16x32_bf16 v[248:251], v[120:123], v[92:95], v[248:251]\n\t" \
  "v_mfma_f32_16x16x32_bf16 v[248:251], v[128:131], v[96:99], v[248:251]\n\t" \
  "ds_read_b64_tr_b16 v[68:69], v5 offset:2176\n\t" \
  "v_mfma_f32_16x16x32_bf16 v[248:251], v[136:139], v[100:103], v[248:251]\n\t" \
  "ds_read_b64_tr_b16 v[70:71], v5 offset:2192\n\t" \
  "v_mfma_f32_16x16x32_bf16 v[248:251], v[144:147], v[104:107], v[248:251]\n\t" \
  "ds_read_b128 v[148:151], v7 offset:11264\n\t" \
  "v_mfma_f32_16x16x32_bf16 v[248:251], v[64:67], v[152:155], v[248:251]\n\t" \
  "v_mfma_f32_16x16x32_bf16 v[248:251], v[72:75], v[156:159], v[248:251]\n\t" \
  "v_mfma_f32_16x16x32_bf16 v[244:247], v[108:111], v[92:95], 0\n\t" \
  "v_mfma_f32_16x16x32_bf16 v[244:247], v[120:123], v[96:99], v[244:247]\n\t" \
  "ds_read_b64_tr_b16 v[140:141], v5 offset:2688\n\t" \
  "v_mfma_f32_16x16x32_bf16 v[244:247], v[128:131], v[100:103], v[244:247]\n\t" \
  "ds_read_b64_tr_b16 v[142:143], v5 offset:2704\n\t" \
  "v_mfma_f32_16x16x32_bf16 v[244:247], v[136:139], v[104:107], v[244:247]\n\t" \
  "ds_read_b128 v[132:135], v7 offset:10240\n\t" \
  "v_mfma_f32_16x16x32_bf16 v[244:247], v[144:147], v[152:155], v[244:247]\n\t" \
  "v_mfma_f32_16x16x32_bf16 v[244:247], v[64:67], v[156:159], v[244:247]\n\t" \
  "v_mfma_f32_16x16x32_bf16 v[240:243], v[108:111], v[96:99], 0\n\t" \
  "v_mfma_f32_16x16x32_bf16 v[240:243], v[120:123], v[100:103], v[240:243]\n\t" \
  "v_mov_b32 v11, 1\n\t" \
  "v_mfma_f32_16x16x32_bf16 v[240:243], v[128:131], v[104:107], v[240:243]\n\t" \
  "ds_read_b64_tr_b16 v[124:125], v5 offset:3200\n\t" \
  "v_mfma_f32_16x16x32_bf16 v[240:243], v[136:139], v[152:155], v[240:243]\n\t" \
  "s_waitcnt vmcnt(0)\n\t" \
  "s_mov_b64 exec, 1\n\t" \
  "ds_add_rtn_u32 v10, v56, v11 offset:16\n\t" \
  "s_mov_b64 exec, -1\n\t" \
  "v_mfma_f32_16x16x32_bf16 v[240:243], v[144:147], v[156:159], v[240:243]\n\t" \
  "ds_read_b64_tr_b16 v[126:127], v5 offset:3216\n\t" \
  "v_mfma_f32_16x16x32_bf16 v[236:239], v[108:111], v[100:103], 0\n\t" \
  "ds_read_b128 v[116:119], v7 offset:9216\n\t" \
  "v_mfma_f32_16x16x32_bf16 v[236:239], v[120:123], v[104:107], v[236:239]\n\t" \
  "v_bfe_u32 v11, v12, 4, 4\n\t" \
  "v_mfma_f32_16x16x32_bf16 v[236:239], v[128:131], v[152:155], v[236:239]\n\t" \
  "v_lshlrev_b32 v11, 7, v11\n\t" \
  "v_mfma_f32_16x16x32_bf16 v[236:239], v[136:139], v[156:159], v[236:239]\n\t" \
  "v_mfma_f32_16x16x32_bf16 v[232:235], v[108:111], v[104:107], 0\n\t" \
  "v_mfma_f32_16x16x32_bf16 v[232:235], v[120:123], v[152:155], v[232:235]\n\t" \
  "s_waitcnt lgkmcnt(2)\n\t" \
  "v_readfirstlane_b32 vcc_lo, v10\n\t" \
  "v_mfma_f32_16x16x32_bf16 v[232:235], v[128:131], v[156:159], v[232:235]\n\t" \
  "s_cmp_eq_u32 vcc_lo, 3\n\t" \
  "s_cbranch_scc0 23f\n\t" \
  "s_mov_b64 exec, 0xffff\n\t" \
  "global_store_dword v11, v55, %19 sc1\n\t" \
  "s_mov_b64 exec, -1\n\t" \
  "23:\n\t" \
  "v_mfma_f32_16x16x32_bf16 v[228:231], v[108:111], v[152:155], 0\n\t" \
  "ds_read_b64_tr_b16 v[64:65], v5 offset:3712\n\t" \
  "v_mfma_f32_16x16x32_bf16 v[228:231], v[120:123], v[156:159], v[228:231]\n\t" \
  "ds_read_b64_tr_b16 v[66:67], v5 offset:3728\n\t" \
  "v_mfma_f32_16x16x32_bf16 v[224:227], v[108:111], v[156:159], 0\n\t" \
  "ds_read_b128 v[72:75], v7 offset:0\n\t" \
  "v_mfma_f32_16x16x32_bf16 v[192:195], v[160:163], v[164:167], v[192:195]\n\t" \
  "ds_read_b128 v[80:83], v7 offset:1024\n\t" \
  "v_mfma_f32_16x16x32_bf16 v[196:199], v[160:163], v[168:171], v[196:199]\n\t" \
  "ds_read_b128 v[84:87], v7 offset:2048\n\t" \
  "v_mfma_f32_16x16x32_bf16 v[196:199], v[172:175], v[164:167], v[196:199]\n\t" \
  "ds_read_b128 v[88:91], v7 offset:3072\n\t" \
  "v_mfma_f32_16x16x32_bf16 v[200:203], v[160:163], v[176:179], v[200:203]\n\t" \
  "ds_read_b128 v[92:95], v7 offset:4096\n\t" \
  "v_mfma_f32_16x16x32_bf16 v[200:203], v[172:175], v[168:171], v[200:203]\n\t" \
  "ds_read_b128 v[108:111], v7 offset:5120\n\t" \
  "v_mfma_f32_16x16x32_bf16 v[200:203], v[180:183], v[164:167], v[200:203]\n\t" \
  "ds_read_b128 v[96:99], v7 offset:6144\n\t" \
  "v_mfma_f32_16x16x32_bf16 v[204:207], v[160:163], v[184:187], v[204:207]\n\t" \
  "ds_read_b128 v[100:103], v7 offset:7168\n\t" \
  "v_mfma_f32_16x16x32_bf16 v[204:207], v[172:175], v[176:179], v[204:207]\n\t" \
  "v_mfma_f32_16x16x32_bf16 v[204:207], v[180:183], v[168:171], v[204:207]\n\t" \
  "v_mfma_f32_16x16x32_bf16 v[204:207], v[188:191], v[164:167], v[204:207]\n\t" \
  "v_mfma_f32_16x16x32_bf16 v[208:211], v[160:163], v[76:79], v[208:211]\n\t" \
  "v_mfma_f32_16x16x32_bf16 v[208:211], v[172:175], v[184:187], v[208:211]\n\t" \
  "v_mfma_f32_16x16x32_bf16 v[208:211], v[180:183], v[176:179], v[208:211]\n\t" \
  "v_mfma_f32_16x16x32_bf16 v[208:211], v[188:191], v[168:171], v[208:211]\n\t" \
  "v_mfma_f32_16x16x32_bf16 v[208:211], v[68:71], v[164:167], v[208:211]\n\t" \
  "s_getpc_b64 s[70:71]\n\t" \
  "24:\n\t" \
  "s_add_u32 s70, s70, 25f-24b\n\t" \
  "s_addc_u32 s71, s71, 0\n\t" \
  "v_mfma_f32_16x16x32_bf16 v[212:215], v[160:163], v[148:151], v[212:215]\n\t" \
  "v_mfma_f32_16x16x32_bf16 v[212:215], v[172:175], v[76:79], v[212:215]\n\t" \
  "v_mfma_f32_16x16x32_bf16 v[212:215], v[180:183], v[184:187], v[212:215]\n\t" \
  "v_mfma_f32_16x16x32_bf16 v[212:215], v[188:191], v[176:179], v[212:215]\n\t" \
  "v_lshrrev_b32 v14, 1, v4\n\t" \
  "v_mfma_f32_16x16x32_bf16 v[212:215], v[68:71], v[168:171], v[212:215]\n\t" \
  "v_mfma_f32_16x16x32_bf16 v[212:215], v[140:143], v[164:167], v[212:215]\n\t" \
  "v_lshrrev_b32 v15, 24, v3\n\t" \
  "v_mfma_f32_16x16x32_bf16 v[216:219], v[160:163], v[132:135], v[216:219]\n\t" \
  "v_mfma_f32_16x16x32_bf16 v[216:219], v[172:175], v[148:151], v[216:219]\n\t" \
  "v_add_u32 v14, %20, v14\n\t" \
  "v_mfma_f32_16x16x32_bf16 v[216:219], v[180:183], v[76:79], v[216:219]\n\t" \
  "v_mfma_f32_16x16x32_bf16 v[216:219], v[188:191], v[184:187], v[216:219]\n\t" \
  "v_lshrrev_b32 v18, 10, v12\n\t" \
  "v_mfma_f32_16x16x32_bf16 v[216:219], v[68:71], v[176:179], v[216:219]\n\t" \
  "v_mfma_f32_16x16x32_bf16 v[216:219], v[140:143], v[168:171], v[216:219]\n\t" \
  "v_and_b32 v18, 12, v18\n\t" \
  "s_waitcnt lgkmcnt(0)\n\t" \
  "v_mfma_f32_16x16x32_bf16 v[216:219], v[124:127], v[164:167], v[216:219]\n\t" \
  "v_mfma_f32_16x16x32_bf16 v[220:223], v[160:163], v[116:119], v[220:223]\n\t" \
  "v_mov_b32 v17, 16\n\t" \
  "v_mfma_f32_16x16x32_bf16 v[220:223], v[172:175], v[132:135], v[220:223]\n\t" \
  "v_mfma_f32_16x16x32_bf16 v[220:223], v[180:183], v[148:151], v[220:223]\n\t" \
  "v_add_u32 v18, v56, v18\n\t" \
  "v_mfma_f32_16x16x32_bf16 v[220:223], v[188:191], v[76:79], v[220:223]\n\t" \
  "v_mfma_f32_16x16x32_bf16 v[220:223], v[68:71], v[184:187], v[220:223]\n\t" \
  "s_mov_b32 s76, 0\n\t" \
  "ds_read_b64_tr_b16 v[76:77], v5 offset:256\n\t" \
  "v_mfma_f32_16x16x32_bf16 v[220:223], v[140:143], v[176:179], v[220:223]\n\t" \
  "ds_read_b64_tr_b16 v[78:79], v5 offset:272\n\t" \
  "v_mfma_f32_16x16x32_bf16 v[220:223], v[124:127], v[168:171], v[220:223]\n\t" \
  "s_brev_b32 s77, 1\n\t" \
  "ds_read_b128 v[104:107], v8 offset:16384\n\t" \
  "v_mfma_f32_16x16x32_bf16 v[220:223], v[64:67], v[164:167], v[220:223]\n\t" \
  "ds_read_b128 v[112:115], v8 offset:15360\n\t" \
  "v_mfma_f32_16x16x32_bf16 v[252:255], v[160:163], v[72:75], v[252:255]\n\t" \
  "s_mov_b32 s75, 0\n\t" \
  "ds_read_b64_tr_b16 v[120:121], v5 offset:768\n\t" \
  "v_mfma_f32_16x16x32_bf16 v[252:255], v[172:175], v[80:83], v[252:255]\n\t" \
  "ds_read_b64_tr_b16 v[122:123], v5 offset:784\n\t" \
  "v_mfma_f32_16x16x32_bf16 v[252:255], v[180:183], v[84:87], v[252:255]\n\t" \
  "v_add_u32 v28, 22528, v13\n\t" \
  "ds_read_b128 v[116:119], v8 offset:14336\n\t" \
  "v_mfma_f32_16x16x32_bf16 v[252:255], v[188:191], v[88:91], v[252:255]\n\t" \
  "ds_read_b64_tr_b16 v[72:73], v5 offset:1280\n\t" \
  "v_mfma_f32_16x16x32_bf16 v[252:255], v[68:71], v[92:95], v[252:255]\n\t" \
  "v_xor_b32 v29, 32, v28\n\t" \
  "ds_read_b64_tr_b16 v[74:75], v5 offset:1296\n\t" \
  "v_mfma_f32_16x16x32_bf16 v[252:255], v[140:143], v[108:111], v[252:255]\n\t" \
  "ds_read_b128 v[128:131], v8 offset:13312\n\t" \
  "v_mfma_f32_16x16x32_bf16 v[252:255], v[124:127], v[96:99], v[252:255]\n\t" \
  "v_xor_b32 v30, 64, v28\n\t" \
  "ds_read_b64_tr_b16 v[136:137], v5 offset:1792\n\t" \
  "v_mfma_f32_16x16x32_bf16 v[252:255], v[64:67], v[100:103], v[252:255]\n\t" \
  "ds_read_b64_tr_b16 v[138:139], v5 offset:1808\n\t" \
  "v_mfma_f32_16x16x32_bf16 v[248:251], v[160:163], v[80:83], v[248:251]\n\t" \
  "v_xor_b32 v31, 96, v28\n\t" \
  "ds_read_b128 v[144:147], v8 offset:12288\n\t" \
  "v_mfma_f32_16x16x32_bf16 v[248:251], v[172:175], v[84:87], v[248:251]\n\t" \
  "v_mfma_f32_16x16x32_bf16 v[248:251], v[180:183], v[88:91], v[248:251]\n\t" \
  "v_add_u32 v32, 88064, v13\n\t" \
  "ds_read_b64_tr_b16 v[64:65], v5 offset:2304\n\t" \
  "v_mfma_f32_16x16x32_bf16 v[248:251], v[188:191], v[92:95], v[248:251]\n\t" \
  "ds_read_b64_tr_b16 v[66:67], v5 offset:2320\n\t" \
  "v_mfma_f32_16x16x32_bf16 v[248:251], v[68:71], v[108:111], v[248:251]\n\t" \
  "v_xor_b32 v33, 32, v32\n\t" \
  "ds_read_b128 v[80:83], v8 offset:11264\n\t" \
  "v_mfma_f32_16x16x32_bf16 v[248:251], v[140:143], v[96:99], v[248:251]\n\t" \
  "v_mfma_f32_16x16x32_bf16 v[248:251], v[124:127], v[100:103], v[248:251]\n\t" \
  "v_xor_b32 v34, 64, v32\n\t" \
  "v_mfma_f32_16x16x32_bf16 v[244:247], v[160:163], v[84:87], v[244:247]\n\t" \
  "v_mfma_f32_16x16x32_bf16 v[244:247], v[172:175], v[88:91], v[244:247]\n\t" \
  "v_xor_b32 v35, 96, v32\n\t" \
  "ds_read_b64_tr_b16 v[84:85], v5 offset:2816\n\t" \
  "v_mfma_f32_16x16x32_bf16 v[244:247], v[180:183], v[92:95], v[244:247]\n\t" \
  "ds_read_b64_tr_b16 v[86:87], v5 offset:2832\n\t" \
  "v_mfma_f32_16x16x32_bf16 v[244:247], v[188:191], v[108:111], v[244:247]\n\t" \
  "v_add_u32 v36, 0, v13\n\t" \
  "ds_read_b128 v[124:127], v8 offset:10240\n\t" \
  "v_mfma_f32_16x16x32_bf16 v[244:247], v[68:71], v[96:99], v[244:247]\n\t" \
  "v_mfma_f32_16x16x32_bf16 v[244:247], v[140:143], v[100:103], v[244:247]\n\t" \
  "v_xor_b32 v37, 32, v36\n\t" \
  "v_mfma_f32_16x16x32_bf16 v[240:243], v[160:163], v[88:91], v[240:243]\n\t" \
  "v_mfma_f32_16x16x32_bf16 v[240:243], v[172:175], v[92:95], v[240:243]\n\t" \
  "v_xor_b32 v38, 64, v36\n\t" \
  "v_mfma_f32_16x16x32_bf16 v[240:243], v[180:183], v[108:111], v[240:243]\n\t" \
  "ds_read_b64_tr_b16 v[88:89], v5 offset:3328\n\t" \
  "v_mfma_f32_16x16x32_bf16 v[240:243], v[188:191], v[96:99], v[240:243]\n\t" \
  "v_xor_b32 v39, 96, v36\n\t" \
  "ds_read_b64_tr_b16 v[90:91], v5 offset:3344\n\t" \
  "v_mfma_f32_16x16x32_bf16 v[240:243], v[68:71], v[100:103], v[240:243]\n\t" \
  "ds_read_b128 v[152:155], v8 offset:9216\n\t" \
  "v_mfma_f32_16x16x32_bf16 v[236:239], v[160:163], v[92:95], v[236:239]\n\t" \
  "v_lshrrev_b32 v57, 5, v4\n\t" \
  "v_mfma_f32_16x16x32_bf16 v[236:239], v[172:175], v[108:111], v[236:239]\n\t" \
  "v_mfma_f32_16x16x32_bf16 v[236:239], v[180:183], v[96:99], v[236:239]\n\t" \
  "v_and_b32 v57, 0x18, v57\n\t" \
  "v_mfma_f32_16x16x32_bf16 v[236:239], v[188:191], v[100:103], v[236:239]\n\t" \
  "v_mfma_f32_16x16x32_bf16 v[232:235], v[160:163], v[108:111], v[232:235]\n\t" \
  "v_and_b32 v62, 0x70, v4\n\t" \
  "v_mfma_f32_16x16x32_bf16 v[232:235], v[172:175], v[96:99], v[232:235]\n\t" \
  "ds_read_b64_tr_b16 v[68:69], v5 offset:3840\n\t" \
  "v_mfma_f32_16x16x32_bf16 v[232:235], v[180:183], v[100:103], v[232:235]\n\t" \
  "v_xor_b32 v57, v57, v62\n\t" \
  "ds_read_b64_tr_b16 v[70:71], v5 offset:3856\n\t" \
  "v_mfma_f32_16x16x32_bf16 v[228:231], v[160:163], v[96:99], v[228:231]\n\t" \
  "ds_read_b128 v[92:95], v8 offset:0\n\t" \
  "v_mfma_f32_16x16x32_bf16 v[228:231], v[172:175], v[100:103], v[228:231]\n\t" \
  "v_and_b32 v58, 0xf0, v4\n\t" \
  "ds_read_b128 v[108:111], v8 offset:1024\n\t" \
  "v_mfma_f32_16x16x32_bf16 v[224:227], v[160:163], v[100:103], v[224:227]\n\t" \
  "ds_read_b128 v[96:99], v8 offset:2048\n\t" \
  "s_waitcnt lgkmcnt(5)\n\t" \
  "v_mfma_f32_16x16x32_bf16 v[192:195], v[76:79], v[104:107], v[192:195]\n\t" \
  "v_lshl_add_u32 v57, v58, 3, v57\n\t" \
  "ds_read_b128 v[100:103], v8 offset:3072\n\t" \
  "v_mfma_f32_16x16x32_bf16 v[196:199], v[76:79], v[112:115], v[196:199]\n\t" \
  "ds_read_b128 v[156:159], v8 offset:4096\n\t" \
  "v_mfma_f32_16x16x32_bf16 v[196:199], v[120:123], v[104:107], v[196:199]\n\t" \
  "v_add_u32 v57, %23, v57\n\t" \
  "ds_read_b128 v[160:163], v8 offset:5120\n\t" \
  "v_mfma_f32_16x16x32_bf16 v[200:203], v[76:79], v[116:119], v[200:203]\n\t" \
  "ds_read_b128 v[164:167], v8 offset:6144\n\t" \
  "v_mfma_f32_16x16x32_bf16 v[200:203], v[120:123], v[112:115], v[200:203]\n\t" \
  "v_xor_b32 v58, 32, v57\n\t" \
  "ds_read_b128 v[168:171], v8 offset:7168\n\t" \
  "v_mfma_f32_16x16x32_bf16 v[200:203], v[72:75], v[104:107], v[200:203]\n\t" \
  "v_mfma_f32_16x16x32_bf16 v[204:207], v[76:79], v[128:131], v[204:207]\n\t" \
  "v_xor_b32 v59, 64, v57\n\t" \
  "v_mfma_f32_16x16x32_bf16 v[204:207], v[120:123], v[116:119], v[204:207]\n\t" \
  "v_mfma_f32_16x16x32_bf16 v[204:207], v[72:75], v[112:115], v[204:207]\n\t" \
  "v_xor_b32 v60, 96, v57\n\t" \
  "v_mfma_f32_16x16x32_bf16 v[204:207], v[136:139], v[104:107], v[204:207]\n\t" \
  "v_mfma_f32_16x16x32_bf16 v[208:211], v[76:79], v[144:147], v[208:211]\n\t" \
  "v_lshrrev_b32 v61, 3, v4\n\t" \
  "v_mfma_f32_16x16x32_bf16 v[208:211], v[120:123], v[128:131], v[208:211]\n\t" \
  "v_mfma_f32_16x16x32_bf16 v[208:211], v[72:75], v[116:119], v[208:211]\n\t" \
  "v_and_b32 v61, 0x70, v61\n\t" \
  "v_mfma_f32_16x16x32_bf16 v[208:211], v[136:139], v[112:115], v[208:211]\n\t" \
  "v_mfma_f32_16x16x32_bf16 v[208:211], v[64:67], v[104:107], v[208:211]\n\t" \
  "v_xor_b32 v61, v61, v4\n\t" \
  "v_mfma_f32_16x16x32_bf16 v[212:215], v[76:79], v[80:83], v[212:215]\n\t" \
  "v_mfma_f32_16x16x32_bf16 v[212:215], v[120:123], v[144:147], v[212:215]\n\t" \
  "v_add_u32 v61, %23, v61\n\t" \
  "v_mfma_f32_16x16x32_bf16 v[212:215], v[72:75], v[128:131], v[212:215]\n\t" \
  "v_mfma_f32_16x16x32_bf16 v[212:215], v[136:139], v[116:119], v[212:215]\n\t" \
  "v_and_b32 v63, 0x380, v4\n\t" \
  "v_mfma_f32_16x16x32_bf16 v[212:215], v[64:67], v[112:115], v[212:215]\n\t" \
  "v_mfma_f32_16x16x32_bf16 v[212:215], v[84:87], v[104:107], v[212:215]\n\t" \
  "v_lshl_add_u32 v62, v63, 6, v62\n\t" \
  "v_mfma_f32_16x16x32_bf16 v[216:219], v[76:79], v[124:127], v[216:219]\n\t" \
  "v_mfma_f32_16x16x32_bf16 v[216:219], v[120:123], v[80:83], v[216:219]\n\t" \
  "v_add_u32 v63, 0x10000, v62\n\t" \
  "v_mfma_f32_16x16x32_bf16 v[216:219], v[72:75], v[144:147], v[216:219]\n\t" \
  "v_mfma_f32_16x16x32_bf16 v[216:219], v[136:139], v[128:131], v[216:219]\n\t" \
  "v_mfma_f32_16x16x32_bf16 v[216:219], v[64:67], v[116:119], v[216:219]\n\t" \
  "v_mfma_f32_16x16x32_bf16 v[216:219], v[84:87], v[112:115], v[216:219]\n\t" \
  "v_mfma_f32_16x16x32_bf16 v[216:219], v[88:91], v[104:107], v[216:219]\n\t" \
  "v_mfma_f32_16x16x32_bf16 v[220:223], v[76:79], v[152:155], v[220:223]\n\t" \
  "v_mfma_f32_16x16x32_bf16 v[220:223], v[120:123], v[124:127], v[220:223]\n\t" \
  "v_mfma_f32_16x16x32_bf16 v[220:223], v[72:75], v[80:83], v[220:223]\n\t" \
  "v_mfma_f32_16x16x32_bf16 v[220:223], v[136:139], v[144:147], v[220:223]\n\t" \
  "v_mfma_f32_16x16x32_bf16 v[220:223], v[64:67], v[128:131], v[220:223]\n\t" \
  "ds_read_b64_tr_b16 v[172:173], v5 offset:384\n\t" \
  "v_mfma_f32_16x16x32_bf16 v[220:223], v[84:87], v[116:119], v[220:223]\n\t" \
  "ds_read_b64_tr_b16 v[174:175], v5 offset:400\n\t" \
  "v_mfma_f32_16x16x32_bf16 v[220:223], v[88:91], v[112:115], v[220:223]\n\t" \
  "ds_read_b128 v[80:83], v9 offset:9216\n\t" \
  "s_waitcnt lgkmcnt(3)\n\t" \
  "v_mfma_f32_16x16x32_bf16 v[220:223], v[68:71], v[104:107], v[220:223]\n\t" \
  "ds_read_b64_tr_b16 v[176:177], v5 offset:896\n\t" \
  "v_mfma_f32_16x16x32_bf16 v[252:255], v[76:79], v[92:95], v[252:255]\n\t" \
  "ds_read_b64_tr_b16 v[178:179], v5 offset:912\n\t" \
  "v_mfma_f32_16x16x32_bf16 v[252:255], v[120:123], v[108:111], v[252:255]\n\t" \
  "ds_read_b128 v[116:119], v9 offset:10240\n\t" \
  "v_mfma_f32_16x16x32_bf16 v[252:255], v[72:75], v[96:99], v[252:255]\n\t" \
  "ds_read_b64_tr_b16 v[180:181], v5 offset:1408\n\t" \
  "v_mfma_f32_16x16x32_bf16 v[252:255], v[136:139], v[100:103], v[252:255]\n\t" \
  "ds_read_b64_tr_b16 v[182:183], v5 offset:1424\n\t" \
  "v_mfma_f32_16x16x32_bf16 v[252:255], v[64:67], v[156:159], v[252:255]\n\t" \
  "ds_read_b128 v[112:115], v9 offset:11264\n\t" \
  "v_mfma_f32_16x16x32_bf16 v[252:255], v[84:87], v[160:163], v[252:255]\n\t" \
  "ds_read_b64_tr_b16 v[184:185], v5 offset:1920\n\t" \
  "v_mfma_f32_16x16x32_bf16 v[252:255], v[88:91], v[164:167], v[252:255]\n\t" \
  "ds_read_b64_tr_b16 v[186:187], v5 offset:1936\n\t" \
  "v_mfma_f32_16x16x32_bf16 v[252:255], v[68:71], v[168:171], v[252:255]\n\t" \
  "ds_read_b128 v[104:107], v9 offset:12288\n\t" \
  "v_mfma_f32_16x16x32_bf16 v[248:251], v[76:79], v[108:111], v[248:251]\n\t" \
  "ds_read_b64_tr_b16 v[188:189], v5 offset:2432\n\t" \
  "v_mfma_f32_16x16x32_bf16 v[248:251], v[120:123], v[96:99], v[248:251]\n\t" \
  "ds_read_b64_tr_b16 v[190:191], v5 offset:2448\n\t" \
  "v_mfma_f32_16x16x32_bf16 v[248:251], v[72:75], v[100:103], v[248:251]\n\t" \
  "ds_read_b128 v[124:127], v9 offset:13312\n\t" \
  "v_mfma_f32_16x16x32_bf16 v[248:251], v[136:139], v[156:159], v[248:251]\n\t" \
  "ds_read_b64_tr_b16 v[148:149], v5 offset:2944\n\t" \
  "v_mfma_f32_16x16x32_bf16 v[248:251], v[64:67], v[160:163], v[248:251]\n\t" \
  "ds_read_b64_tr_b16 v[150:151], v5 offset:2960\n\t" \
  "v_mfma_f32_16x16x32_bf16 v[248:251], v[84:87], v[164:167], v[248:251]\n\t" \
  "ds_read_b128 v[140:143], v9 offset:14336\n\t" \
  "v_mfma_f32_16x16x32_bf16 v[248:251], v[88:91], v[168:171], v[248:251]\n\t" \
  "ds_read_b64_tr_b16 v[132:133], v5 offset:3456\n\t" \
  "v_mfma_f32_16x16x32_bf16 v[244:247], v[76:79], v[96:99], v[244:247]\n\t" \
  "ds_read_b64_tr_b16 v[134:135], v5 offset:3472\n\t" \
  "v_mfma_f32_16x16x32_bf16 v[244:247], v[120:123], v[100:103], v[244:247]\n\t" \
  "ds_read_b128 v[128:131], v9 offset:15360\n\t" \
  "v_mfma_f32_16x16x32_bf16 v[244:247], v[72:75], v[156:159], v[244:247]\n\t" \
  "ds_read_b64_tr_b16 v[144:145], v5 offset:3968\n\t" \
  "v_mfma_f32_16x16x32_bf16 v[244:247], v[136:139], v[160:163], v[244:247]\n\t" \
  "ds_read_b64_tr_b16 v[146:147], v5 offset:3984\n\t" \
  "v_mfma_f32_16x16x32_bf16 v[244:247], v[64:67], v[164:167], v[244:247]\n\t" \
  "ds_read_b128 v[152:155], v9 offset:16384\n\t" \
  "v_mfma_f32_16x16x32_bf16 v[244:247], v[84:87], v[168:171], v[244:247]\n\t" \
  "v_mfma_f32_16x16x32_bf16 v[240:243], v[76:79], v[100:103], v[240:243]\n\t" \
  "v_mfma_f32_16x16x32_bf16 v[240:243], v[120:123], v[156:159], v[240:243]\n\t" \
  "v_mfma_f32_16x16x32_bf16 v[240:243], v[72:75], v[160:163], v[240:243]\n\t" \
  "v_mfma_f32_16x16x32_bf16 v[240:243], v[136:139], v[164:167], v[240:243]\n\t" \
  "v_mfma_f32_16x16x32_bf16 v[240:243], v[64:67], v[168:171], v[240:243]\n\t" \
  "v_mfma_f32_16x16x32_bf16 v[236:239], v[76:79], v[156:159], v[236:239]\n\t" \
  "v_mfma_f32_16x16x32_bf16 v[236:239], v[120:123], v[160:163], v[236:239]\n\t" \
  "v_mfma_f32_16x16x32_bf16 v[236:239], v[72:75], v[164:167], v[236:239]\n\t" \
  "v_mfma_f32_16x16x32_bf16 v[236:239], v[136:139], v[168:171], v[236:239]\n\t" \
  "v_mfma_f32_16x16x32_bf16 v[232:235], v[76:79], v[160:163], v[232:235]\n\t" \
  "v_mfma_f32_16x16x32_bf16 v[232:235], v[120:123], v[164:167], v[232:235]\n\t" \
  "v_mfma_f32_16x16x32_bf16 v[232:235], v[72:75], v[168:171], v[232:235]\n\t" \
  "ds_read_b32 v16, v56 offset:96\n\t" \
  "ds_read_b128 v[136:139], v9 offset:7168\n\t" \
  "v_mfma_f32_16x16x32_bf16 v[228:231], v[76:79], v[164:167], v[228:231]\n\t" \
  "ds_read_b128 v[156:159], v9 offset:6144\n\t" \
  "v_mfma_f32_16x16x32_bf16 v[228:231], v[120:123], v[168:171], v[228:231]\n\t" \
  "v_mfma_f32_16x16x32_bf16 v[224:227], v[76:79], v[168:171], v[224:227]\n\t" \
  "ds_read_b128 v[160:163], v9 offset:5120\n\t" \
  "s_waitcnt lgkmcnt(4)\n\t" \
  "v_mfma_f32_16x16x32_bf16 v[220:223], v[172:175], v[80:83], v[220:223]\n\t" \
  "v_mfma_f32_16x16x32_bf16 v[220:223], v[176:179], v[116:119], v[220:223]\n\t" \
  "v_mfma_f32_16x16x32_bf16 v[220:223], v[180:183], v[112:115], v[220:223]\n\t" \
  "ds_read_b128 v[164:167], v9 offset:4096\n\t" \
  "v_mfma_f32_16x16x32_bf16 v[220:223], v[184:187], v[104:107], v[220:223]\n\t" \
  "s_waitcnt lgkmcnt(4)\n\t" \
  "v_readfirstlane_b32 vcc_lo, v16\n\t" \
  "v_mfma_f32_16x16x32_bf16 v[220:223], v[188:191], v[124:127], v[220:223]\n\t" \
  "s_cmp_eq_u32 vcc_lo, 0\n\t" \
  "s_cbranch_scc1 30f\n\t" \
  "s_lshr_b64 s[90:91], %18, s75\n\t" \
  "s_and_b32 s90, s90, 15\n\t" \
  "s_lshl_b32 s90, s90, 14\n\t" \
  "s_add_u32 s90, s90, s92\n\t" \
  "s_addc_u32 s91, s93, 0\n\t" \
  "s_add_u32 s75, s75, 4\n\t" \
  "s_swappc_b64 s[68:69], s[70:71]\n\t" \
  "30:\n\t" \
  "v_mfma_f32_16x16x32_bf16 v[220:223], v[148:151], v[140:143], v[220:223]\n\t" \
  "v_mfma_f32_16x16x32_bf16 v[220:223], v[132:135], v[128:131], v[220:223]\n\t" \
  "ds_read_b128 v[168:171], v9 offset:3072\n\t" \
  "v_mfma_f32_16x16x32_bf16 v[220:223], v[144:147], v[152:155], v[220:223]\n\t" \
  "v_mfma_f32_16x16x32_bf16 v[216:219], v[172:175], v[116:119], v[216:219]\n\t" \
  "v_mfma_f32_16x16x32_bf16 v[216:219], v[176:179], v[112:115], v[216:219]\n\t" \
  "v_mfma_f32_16x16x32_bf16 v[216:219], v[180:183], v[104:107], v[216:219]\n\t" \
  "ds_read_b32 v16, v56 offset:96\n\t" \
  "v_mfma_f32_16x16x32_bf16 v[216:219], v[184:187], v[124:127], v[216:219]\n\t" \
  "s_nop 2\n\t" \
  "v_add_f32 v253, v253, v220\n\t" \
  "v_add_f32 v254, v254, v221\n\t" \
  "v_mfma_f32_16x16x32_bf16 v[216:219], v[188:191], v[140:143], v[216:219]\n\t" \
  "v_add_f32 v255, v255, v222\n\t" \
  "v_mfma_f32_16x16x32_bf16 v[216:219], v[148:151], v[128:131], v[216:219]\n\t" \
  "v_mfma_f32_16x16x32_bf16 v[216:219], v[132:135], v[152:155], v[216:219]\n\t" \
  "v_mfma_f32_16x16x32_bf16 v[212:215], v[172:175], v[112:115], v[212:215]\n\t" \
  "v_mfma_f32_16x16x32_bf16 v[212:215], v[176:179], v[104:107], v[212:215]\n\t" \
  "v_mfma_f32_16x16x32_bf16 v[212:215], v[180:183], v[124:127], v[212:215]\n\t" \
  "s_waitcnt lgkmcnt(0)\n\t" \
  "v_readfirstlane_b32 vcc_lo, v16\n\t" \
  "v_mfma_f32_16x16x32_bf16 v[212:215], v[184:187], v[140:143], v[212:215]\n\t" \
  "s_cmp_eq_u32 vcc_lo, 0\n\t" \
  "s_cbranch_scc1 31f\n\t" \
  "s_lshr_b64 s[90:91], %18, s75\n\t" \
  "s_and_b32 s90, s90, 15\n\t" \
  "s_lshl_b32 s90, s90, 14\n\t" \
  "s_add_u32 s90, s90, s92\n\t" \
  "s_addc_u32 s91, s93, 0\n\t" \
  "s_add_u32 s75, s75, 4\n\t" \
  "s_swappc_b64 s[68:69], s[70:71]\n\t" \
  "31:\n\t" \
  "v_mfma_f32_16x16x32_bf16 v[212:215], v[188:191], v[128:131], v[212:215]\n\t" \
  "v_add_f32 v249, v249, v216\n\t" \
  "v_add_f32 v250, v250, v217\n\t" \
  "v_mfma_f32_16x16x32_bf16 v[212:215], v[148:151], v[152:155], v[212:215]\n\t" \
  "v_add_f32 v251, v251, v218\n\t" \
  "v_cndmask_b32 v27, v223, v219, s[76:77]\n\t" \
  "v_mfma_f32_16x16x32_bf16 v[208:211], v[172:175], v[104:107], v[208:211]\n\t" \
  "ds_bpermute_b32 v27, v15, v27\n\t" \
  "v_mfma_f32_16x16x32_bf16 v[208:211], v[176:179], v[124:127], v[208:211]\n\t" \
  "v_mfma_f32_16x16x32_bf16 v[208:211], v[180:183], v[140:143], v[208:211]\n\t" \
  "v_mfma_f32_16x16x32_bf16 v[208:211], v[184:187], v[128:131], v[208:211]\n\t" \
  "s_nop 0\n\t" \
  "v_add_f32 v245, v245, v212\n\t" \
  "v_add_f32 v246, v246, v213\n\t" \
  "v_mfma_f32_16x16x32_bf16 v[208:211], v[188:191], v[152:155], v[208:211]\n\t" \
  "v_add_f32 v247, v247, v214\n\t" \
  "v_cndmask_b32 v26, v219, v215, s[76:77]\n\t" \
  "v_mfma_f32_16x16x32_bf16 v[204:207], v[172:175], v[124:127], v[204:207]\n\t" \
  "ds_read_b32 v16, v56 offset:96\n\t" \
  "ds_bpermute_b32 v26, v15, v26\n\t" \
  "s_waitcnt lgkmcnt(2)\n\t" \
  "v_add_f32 v252, v252, v27\n\t" \
  "v_mfma_f32_16x16x32_bf16 v[204:207], v[176:179], v[140:143], v[204:207]\n\t" \
  "v_mfma_f32_16x16x32_bf16 v[204:207], v[180:183], v[128:131], v[204:207]\n\t" \
  "v_mfma_f32_16x16x32_bf16 v[204:207], v[184:187], v[152:155], v[204:207]\n\t" \
  "v_cndmask_b32 v25, v215, v211, s[76:77]\n\t" \
  "ds_bpermute_b32 v25, v15, v25\n\t" \
  "v_mfma_f32_16x16x32_bf16 v[200:203], v[172:175], v[140:143], v[200:203]\n\t" \
  "ds_read_b128 v[140:143], v9 offset:2048\n\t" \
  "s_waitcnt lgkmcnt(2)\n\t" \
  "v_add_f32 v248, v248, v26\n\t" \
  "v_mfma_f32_16x16x32_bf16 v[200:203], v[176:179], v[128:131], v[200:203]\n\t" \
  "v_mfma_f32_16x16x32_bf16 v[200:203], v[180:183], v[152:155], v[200:203]\n\t" \
  "v_mfma_f32_16x16x32_bf16 v[196:199], v[172:175], v[128:131], v[196:199]\n\t" \
  "v_readfirstlane_b32 vcc_lo, v16\n\t" \
  "ds_read_b128 v[128:131], v9 offset:1024\n\t" \
  "v_cndmask_b32 v24, v211, v207, s[76:77]\n\t" \
  "v_mfma_f32_16x16x32_bf16 v[196:199], v[176:179], v[152:155], v[196:199]\n\t" \
  "s_cmp_eq_u32 vcc_lo, 0\n\t" \
  "s_cbranch_scc1 32f\n\t" \
  "s_lshr_b64 s[90:91], %18, s75\n\t" \
  "s_and_b32 s90, s90, 15\n\t" \
  "s_lshl_b32 s90, s90, 14\n\t" \
  "s_add_u32 s90, s90, s92\n\t" \
  "s_addc_u32 s91, s93, 0\n\t" \
  "s_add_u32 s75, s75, 4\n\t" \
  "s_swappc_b64 s[68:69], s[70:71]\n\t" \
  "32:\n\t" \
  "v_mfma_f32_16x16x32_bf16 v[192:195], v[172:175], v[152:155], v[192:195]\n\t" \
  "ds_read_b128 v[152:155], v9 offset:0\n\t" \
  "ds_bpermute_b32 v24, v15, v24\n\t" \
  "s_waitcnt lgkmcnt(4)\n\t" \
  "v_add_f32 v244, v244, v25\n\t" \
  "v_mfma_f32_16x16x32_bf16 v[224:227], v[172:175], v[136:139], v[224:227]\n\t" \
  "v_cndmask_b32 v23, v207, v203, s[76:77]\n\t" \
  "ds_bpermute_b32 v23, v15, v23\n\t" \
  "v_mfma_f32_16x16x32_bf16 v[228:231], v[172:175], v[156:159], v[228:231]\n\t" \
  "v_mfma_f32_16x16x32_bf16 v[228:231], v[176:179], v[136:139], v[228:231]\n\t" \
  "v_cndmask_b32 v22, v203, v199, s[76:77]\n\t" \
  "ds_bpermute_b32 v22, v15, v22\n\t" \
  "v_mfma_f32_16x16x32_bf16 v[232:235], v[172:175], v[160:163], v[232:235]\n\t" \
  "v_cndmask_b32 v20, v195, 0, s[76:77]\n\t" \
  "ds_bpermute_b32 v20, v15, v20\n\t" \
  "v_mfma_f32_16x16x32_bf16 v[232:235], v[176:179], v[156:159], v[232:235]\n\t" \
  "v_cndmask_b32 v21, v199, v195, s[76:77]\n\t" \
  "ds_bpermute_b32 v21, v15, v21\n\t" \
  "v_mfma_f32_16x16x32_bf16 v[232:235], v[180:183], v[136:139], v[232:235]\n\t" \
  "v_add_f32 v225, v225, v192\n\t" \
  "v_add_f32 v226, v226, v193\n\t" \
  "v_mfma_f32_16x16x32_bf16 v[236:239], v[172:175], v[164:167], v[236:239]\n\t" \
  "ds_read_b32 v16, v56 offset:96\n\t" \
  "v_add_f32 v227, v227, v194\n\t" \
  "v_add_f32 v229, v229, v196\n\t" \
  "v_mfma_f32_16x16x32_bf16 v[236:239], v[176:179], v[160:163], v[236:239]\n\t" \
  "v_add_f32 v230, v230, v197\n\t" \
  "v_add_f32 v231, v231, v198\n\t" \
  "v_mfma_f32_16x16x32_bf16 v[236:239], v[180:183], v[156:159], v[236:239]\n\t" \
  "v_mfma_f32_16x16x32_bf16 v[236:239], v[184:187], v[136:139], v[236:239]\n\t" \
  "s_waitcnt lgkmcnt(2)\n\t" \
  "v_add_f32 v224, v224, v20\n\t" \
  "v_cvt_pk_bf16_f32 v45, v226, v227\n\t" \
  "v_mfma_f32_16x16x32_bf16 v[240:243], v[172:175], v[168:171], v[240:243]\n\t" \
  "v_cvt_pk_bf16_f32 v44, v224, v225\n\t" \
  "ds_write_b64 v14, v[44:45] offset:0\n\t" \
  "v_mfma_f32_16x16x32_bf16 v[240:243], v[176:179], v[164:167], v[240:243]\n\t" \
  "s_waitcnt lgkmcnt(2)\n\t" \
  "v_add_f32 v228, v228, v21\n\t" \
  "v_cvt_pk_bf16_f32 v47, v230, v231\n\t" \
  "v_mfma_f32_16x16x32_bf16 v[240:243], v[180:183], v[160:163], v[240:243]\n\t" \
  "v_cvt_pk_bf16_f32 v46, v228, v229\n\t" \
  "ds_write_b64 v14, v[46:47] offset:512\n\t" \
  "v_mfma_f32_16x16x32_bf16 v[240:243], v[184:187], v[156:159], v[240:243]\n\t" \
  "s_waitcnt lgkmcnt(2)\n\t" \
  "v_readfirstlane_b32 vcc_lo, v16\n\t" \
  "v_add_f32 v233, v233, v200\n\t" \
  "v_mfma_f32_16x16x32_bf16 v[240:243], v[188:191], v[136:139], v[240:243]\n\t" \
  "s_cmp_eq_u32 vcc_lo, 0\n\t" \
  "s_cbranch_scc1 33f\n\t" \
  "s_lshr_b64 s[90:91], %18, s75\n\t" \
  "s_and_b32 s90, s90, 15\n\t" \
  "s_lshl_b32 s90, s90, 14\n\t" \
  "s_add_u32 s90, s90, s92\n\t" \
  "s_addc_u32 s91, s93, 0\n\t" \
  "s_add_u32 s75, s75, 4\n\t" \
  "s_swappc_b64 s[68:69], s[70:71]\n\t" \
  "33:\n\t" \
  "508:\n\t" \
  "s_cbranch_execz 26b\n\t" \
  "v_mfma_f32_16x16x32_bf16 v[244:247], v[172:175], v[140:143], v[244:247]\n\t" \
  "ds_read_b128 v[40:43], v56 offset:80\n\t" \
  "v_add_f32 v234, v234, v201\n\t" \
  "v_add_f32 v235, v235, v202\n\t" \
  "v_mfma_f32_16x16x32_bf16 v[244:247], v[176:179], v[168:171], v[244:247]\n\t" \
  "v_add_f32 v232, v232, v22\n\t" \
  "v_cvt_pk_bf16_f32 v49, v234, v235\n\t" \
  "v_mfma_f32_16x16x32_bf16 v[244:247], v[180:183], v[164:167], v[244:247]\n\t" \
  "v_cvt_pk_bf16_f32 v48, v232, v233\n\t" \
  "ds_write_b64 v14, v[48:49] offset:1024\n\t" \
  "v_mfma_f32_16x16x32_bf16 v[244:247], v[184:187], v[160:163], v[244:247]\n\t" \
  "v_add_f32 v237, v237, v204\n\t" \
  "v_add_f32 v238, v238, v205\n\t" \
  "v_mfma_f32_16x16x32_bf16 v[244:247], v[188:191], v[156:159], v[244:247]\n\t" \
  "v_add_f32 v239, v239, v206\n\t" \
  "v_add_f32 v236, v236, v23\n\t" \
  "v_mfma_f32_16x16x32_bf16 v[244:247], v[148:151], v[136:139], v[244:247]\n\t" \
  "ds_read_b32 v16, v56 offset:96\n\t" \
  "v_cvt_pk_bf16_f32 v51, v238, v239\n\t" \
  "v_cvt_pk_bf16_f32 v50, v236, v237\n\t" \
  "v_mfma_f32_16x16x32_bf16 v[248:251], v[172:175], v[128:131], v[248:251]\n\t" \
  "ds_write_b64 v14, v[50:51] offset:1536\n\t" \
  "v_add_f32 v241, v241, v208\n\t" \
  "v_mfma_f32_16x16x32_bf16 v[248:251], v[176:179], v[140:143], v[248:251]\n\t" \
  "s_waitcnt lgkmcnt(3)\n\t" \
  "v_min_u32 v40, v40, v41\n\t" \
  "v_min3_u32 v40, v40, v42, v43\n\t" \
  "v_mfma_f32_16x16x32_bf16 v[248:251], v[180:183], v[168:171], v[248:251]\n\t" \
  "v_readfirstlane_b32 vcc_lo, v40\n\t" \
  "v_add_f32 v242, v242, v209\n\t" \
  "v_add_f32 v243, v243, v210\n\t" \
  "v_mfma_f32_16x16x32_bf16 v[248:251], v[184:187], v[164:167], v[248:251]\n\t" \
  "s_cmp_ge_u32 vcc_lo, 13\n\t" \
  "s_cbranch_scc0 35f\n\t" \
  "36:\n\t" \
  "v_mfma_f32_16x16x32_bf16 v[248:251], v[188:191], v[160:163], v[248:251]\n\t" \
  "v_add_f32 v240, v240, v24\n\t" \
  "v_cvt_pk_bf16_f32 v45, v242, v243\n\t" \
  "ds_read_b64_tr_b16 v[192:193], v28 offset:0\n\t" \
  "v_mfma_f32_16x16x32_bf16 v[248:251], v[148:151], v[156:159], v[248:251]\n\t" \
  "v_cvt_pk_bf16_f32 v44, v240, v241\n\t" \
  "ds_write_b64 v14, v[44:45] offset:2048\n\t" \
  "ds_read_b64_tr_b16 v[194:195], v28 offset:512\n\t" \
  "v_mfma_f32_16x16x32_bf16 v[248:251], v[132:135], v[136:139], v[248:251]\n\t" \
  "s_waitcnt lgkmcnt(4)\n\t" \
  "v_readfirstlane_b32 vcc_lo, v16\n\t" \
  "v_cvt_pk_bf16_f32 v47, v246, v247\n\t" \
  "v_mfma_f32_16x16x32_bf16 v[252:255], v[172:175], v[152:155], v[252:255]\n\t" \
  "s_cmp_eq_u32 vcc_lo, 0\n\t" \
  "s_cbranch_scc1 34f\n\t" \
  "s_cmp_ge_u32 s75, 16\n\t" \
  "s_cbranch_scc1 34f\n\t" \
  "s_lshr_b64 s[90:91], %18, s75\n\t" \
  "s_and_b32 s90, s90, 15\n\t" \
  "s_lshl_b32 s90, s90, 14\n\t" \
  "s_add_u32 s90, s90, s92\n\t" \
  "s_addc_u32 s91, s93, 0\n\t" \
  "s_add_u32 s75, s75, 4\n\t" \
  "s_swappc_b64 s[68:69], s[70:71]\n\t" \
  "34:\n\t" \
  "v_mfma_f32_16x16x32_bf16 v[252:255], v[176:179], v[128:131], v[252:255]\n\t" \
  "v_cvt_pk_bf16_f32 v46, v244, v245\n\t" \
  "ds_write_b64 v14, v[46:47] offset:2560\n\t" \
  "ds_read_b64_tr_b16 v[196:197], v28 offset:4096\n\t" \
  "v_mfma_f32_16x16x32_bf16 v[252:255], v[180:183], v[140:143], v[252:255]\n\t" \
  "ds_read_b64_tr_b16 v[198:199], v28 offset:4608\n\t" \
  "v_mfma_f32_16x16x32_bf16 v[252:255], v[184:187], v[168:171], v[252:255]\n\t" \
  "v_cvt_pk_bf16_f32 v49, v250, v251\n\t" \
  "v_cvt_pk_bf16_f32 v48, v248, v249\n\t" \
  "ds_read_b64_tr_b16 v[200:201], v29 offset:0\n\t" \
  "v_mfma_f32_16x16x32_bf16 v[252:255], v[188:191], v[164:167], v[252:255]\n\t" \
  "ds_write_b64 v14, v[48:49] offset:3072\n\t" \
  "ds_read_b64_tr_b16 v[202:203], v29 offset:512\n\t" \
  "v_mfma_f32_16x16x32_bf16 v[252:255], v[148:151], v[160:163], v[252:255]\n\t" \
  "ds_read_b64_tr_b16 v[204:205], v29 offset:4096\n\t" \
  "v_mfma_f32_16x16x32_bf16 v[252:255], v[132:135], v[156:159], v[252:255]\n\t" \
  "ds_read_b64_tr_b16 v[206:207], v29 offset:4608\n\t" \
  "v_mfma_f32_16x16x32_bf16 v[252:255], v[144:147], v[136:139], v[252:255]\n\t" \
  "ds_read_b64_tr_b16 v[208:209], v30 offset:0\n\t" \
  "ds_read_b32 v16, v56 offset:96\n\t" \
  "ds_read_b64_tr_b16 v[210:211], v30 offset:512\n\t" \
  "ds_read_b64_tr_b16 v[212:213], v30 offset:4096\n\t" \
  "ds_read_b64_tr_b16 v[214:215], v30 offset:4608\n\t" \
  "ds_read_b64_tr_b16 v[216:217], v31 offset:0\n\t" \
  "ds_read_b64_tr_b16 v[218:219], v31 offset:512\n\t" \
  "ds_read_b64_tr_b16 v[220:221], v31 offset:4096\n\t" \
  "ds_read_b64_tr_b16 v[222:223], v31 offset:4608\n\t" \
  "v_cvt_pk_bf16_f32 v51, v254, v255\n\t" \
  "v_cvt_pk_bf16_f32 v50, v252, v253\n\t" \
  "ds_write_b64 v14, v[50:51] offset:3584\n\t" \
  "ds_write_b32 v18, v17 offset:32\n\t" \
  "s_cmp_lg_u32 s75, 0\n\t" \
  "s_cbranch_scc1 41f\n\t" \
  "s_waitcnt lgkmcnt(9)\n\t" \
  "v_readfirstlane_b32 vcc_lo, v16\n\t" \
  "s_nop 1\n\t" \
  "s_cmp_ge_u32 vcc_lo, 1\n\t" \
  "s_cbranch_scc0 43f\n\t" \
  "44:\n\t" \
  "41:\n\t" \
  "s_cmp_ge_u32 s75, 16\n\t" \
  "s_cbranch_scc1 42f\n\t" \
  "s_lshr_b64 s[90:91], %18, s75\n\t" \
  "s_and_b32 s90, s90, 15\n\t" \
  "s_lshl_b32 s90, s90, 14\n\t" \
  "s_add_u32 s90, s90, s92\n\t" \
  "s_addc_u32 s91, s93, 0\n\t" \
  "s_add_u32 s75, s75, 4\n\t" \
  "s_swappc_b64 s[68:69], s[70:71]\n\t" \
  "s_branch 41b\n\t" \
  "42:\n\t" \
  "s_waitcnt lgkmcnt(15)\n\t" \
  "s_waitcnt vmcnt(14)\n\t" \
  "v_mfma_f32_16x16x32_bf16 v[224:227], v[192:195], v[64:67], 0\n\t" \
  "v_mfma_f32_16x16x32_bf16 v[224:227], v[196:199], v[68:71], v[224:227]\n\t" \
  "s_waitcnt vmcnt(12)\n\t" \
  "v_mfma_f32_16x16x32_bf16 v[228:231], v[196:199], v[76:79], 0\n\t" \
  "s_lshr_b64 s[90:91], %18, 16\n\t" \
  "s_and_b32 s90, s90, 15\n\t" \
  "s_lshl_b32 s90, s90, 14\n\t" \
  "s_add_u32 s90, s90, s92\n\t" \
  "s_addc_u32 s91, s93, 0\n\t" \
  "global_load_dwordx4 v[128:131], v12, s[90:91] offset:0 sc1\n\t" \
  "v_mfma_f32_16x16x32_bf16 v[228:231], v[192:195], v[72:75], v[228:231]\n\t" \
  "s_waitcnt lgkmcnt(12)\n\t" \
  "v_mfma_f32_16x16x32_bf16 v[232:235], v[200:203], v[64:67], 0\n\t" \
  "ds_read_b64_tr_b16 v[192:193], v28 offset:8192\n\t" \
  "s_waitcnt lgkmcnt(12)\n\t" \
  "v_mfma_f32_16x16x32_bf16 v[232:235], v[204:207], v[68:71], v[232:235]\n\t" \
  "ds_read_b64_tr_b16 v[194:195], v28 offset:8704\n\t" \
  "v_mfma_f32_16x16x32_bf16 v[236:239], v[204:207], v[76:79], 0\n\t" \
  "ds_read_b64_tr_b16 v[196:197], v28 offset:12288\n\t" \
  "global_load_dwordx4 v[132:135], v12, s[90:91] offset:1024 sc1\n\t" \
  "v_mfma_f32_16x16x32_bf16 v[236:239], v[200:203], v[72:75], v[236:239]\n\t" \
  "ds_read_b64_tr_b16 v[198:199], v28 offset:12800\n\t" \
  "s_waitcnt lgkmcnt(4)\n\t" \
  "v_mfma_f32_16x16x32_bf16 v[240:243], v[208:211], v[64:67], 0\n\t" \
  "ds_read_b64_tr_b16 v[200:201], v29 offset:8192\n\t" \
  "v_mfma_f32_16x16x32_bf16 v[240:243], v[212:215], v[68:71], v[240:243]\n\t" \
  "ds_read_b64_tr_b16 v[202:203], v29 offset:8704\n\t" \
  "v_mfma_f32_16x16x32_bf16 v[244:247], v[212:215], v[76:79], 0\n\t" \
  "ds_read_b64_tr_b16 v[204:205], v29 offset:12288\n\t" \
  "global_load_dwordx4 v[136:139], v12, s[90:91] offset:2048 sc1\n\t" \
  "v_mfma_f32_16x16x32_bf16 v[244:247], v[208:211], v[72:75], v[244:247]\n\t" \
  "ds_read_b64_tr_b16 v[206:207], v29 offset:12800\n\t" \
  "v_mfma_f32_16x16x32_bf16 v[248:251], v[216:219], v[64:67], 0\n\t" \
  "ds_read_b64_tr_b16 v[208:209], v30 offset:8192\n\t" \
  "v_mfma_f32_16x16x32_bf16 v[248:251], v[220:223], v[68:71], v[248:251]\n\t" \
  "ds_read_b64_tr_b16 v[210:211], v30 offset:8704\n\t" \
  "v_mfma_f32_16x16x32_bf16 v[252:255], v[220:223], v[76:79], 0\n\t" \
  "ds_read_b64_tr_b16 v[212:213], v30 offset:12288\n\t" \
  "global_load_dwordx4 v[140:143], v12, s[90:91] offset:3072 sc1\n\t" \
  "v_mfma_f32_16x16x32_bf16 v[252:255], v[216:219], v[72:75], v[252:255]\n\t" \
  "ds_read_b64_tr_b16 v[214:215], v30 offset:12800\n\t" \
  "s_waitcnt lgkmcnt(6)\n\t" \
  "s_waitcnt vmcnt(14)\n\t" \
  "v_mfma_f32_16x16x32_bf16 v[224:227], v[192:195], v[80:83], v[224:227]\n\t" \
  "ds_read_b64_tr_b16 v[216:217], v31 offset:8192\n\t" \
  "v_mfma_f32_16x16x32_bf16 v[224:227], v[196:199], v[84:87], v[224:227]\n\t" \
  "ds_read_b64_tr_b16 v[218:219], v31 offset:8704\n\t" \
  "s_waitcnt vmcnt(12)\n\t" \
  "v_mfma_f32_16x16x32_bf16 v[228:231], v[196:199], v[92:95], v[228:231]\n\t" \
  "ds_read_b64_tr_b16 v[220:221], v31 offset:12288\n\t" \
  "s_lshr_b64 s[90:91], %18, 20\n\t" \
  "s_and_b32 s90, s90, 15\n\t" \
  "s_lshl_b32 s90, s90, 14\n\t" \
  "s_add_u32 s90, s90, s92\n\t" \
  "s_addc_u32 s91, s93, 0\n\t" \
  "global_load_dwordx4 v[144:147], v12, s[90:91] offset:0 sc1\n\t" \
  "v_mfma_f32_16x16x32_bf16 v[228:231], v[192:195], v[88:91], v[228:231]\n\t" \
  "ds_read_b64_tr_b16 v[222:223], v31 offset:12800\n\t" \
  "v_mfma_f32_16x16x32_bf16 v[232:235], v[200:203], v[80:83], v[232:235]\n\t" \
  "ds_read_b64_tr_b16 v[192:193], v28 offset:16384\n\t" \
  "s_waitcnt lgkmcnt(6)\n\t" \
  "v_mfma_f32_16x16x32_bf16 v[232:235], v[204:207], v[84:87], v[232:235]\n\t" \
  "ds_read_b64_tr_b16 v[194:195], v28 offset:16896\n\t" \
  "ds_write_b32 v18, v17 offset:160\n\t" \
  "v_mfma_f32_16x16x32_bf16 v[236:239], v[204:207], v[92:95], v[236:239]\n\t" \
  "ds_read_b64_tr_b16 v[196:197], v28 offset:20480\n\t" \
  "global_load_dwordx4 v[148:151], v12, s[90:91] offset:1024 sc1\n\t" \
  "v_mfma_f32_16x16x32_bf16 v[236:239], v[200:203], v[88:91], v[236:239]\n\t" \
  "ds_read_b64_tr_b16 v[198:199], v28 offset:20992\n\t" \
  "v_mfma_f32_16x16x32_bf16 v[240:243], v[208:211], v[80:83], v[240:243]\n\t" \
  "ds_read_b64_tr_b16 v[200:201], v29 offset:16384\n\t" \
  "s_waitcnt lgkmcnt(7)\n\t" \
  "v_mfma_f32_16x16x32_bf16 v[240:243], v[212:215], v[84:87], v[240:243]\n\t" \
  "ds_read_b64_tr_b16 v[202:203], v29 offset:16896\n\t" \
  "v_mfma_f32_16x16x32_bf16 v[244:247], v[212:215], v[92:95], v[244:247]\n\t" \
  "ds_read_b64_tr_b16 v[204:205], v29 offset:20480\n\t" \
  "global_load_dwordx4 v[152:155], v12, s[90:91] offset:2048 sc1\n\t" \
  "v_mfma_f32_16x16x32_bf16 v[244:247], v[208:211], v[88:91], v[244:247]\n\t" \
  "ds_read_b64_tr_b16 v[206:207], v29 offset:20992\n\t" \
  "v_mfma_f32_16x16x32_bf16 v[248:251], v[216:219], v[80:83], v[248:251]\n\t" \
  "ds_read_b64_tr_b16 v[208:209], v30 offset:16384\n\t" \
  "s_waitcnt lgkmcnt(6)\n\t" \
  "v_mfma_f32_16x16x32_bf16 v[248:251], v[220:223], v[84:87], v[248:251]\n\t" \
  "ds_read_b64_tr_b16 v[210:211], v30 offset:16896\n\t" \
  "v_mfma_f32_16x16x32_bf16 v[252:255], v[220:223], v[92:95], v[252:255]\n\t" \
  "ds_read_b64_tr_b16 v[212:213], v30 offset:20480\n\t" \
  "global_load_dwordx4 v[156:159], v12, s[90:91] offset:3072 sc1\n\t" \
  "v_mfma_f32_16x16x32_bf16 v[252:255], v[216:219], v[88:91], v[252:255]\n\t" \
  "ds_read_b64_tr_b16 v[214:215], v30 offset:20992\n\t" \
  "s_waitcnt vmcnt(14)\n\t" \
  "v_mfma_f32_16x16x32_bf16 v[224:227], v[192:195], v[96:99], v[224:227]\n\t" \
  "ds_read_b64_tr_b16 v[216:217], v31 offset:16384\n\t" \
  "s_waitcnt lgkmcnt(6)\n\t" \
  "v_mfma_f32_16x16x32_bf16 v[224:227], v[196:199], v[100:103], v[224:227]\n\t" \
  "ds_read_b64_tr_b16 v[218:219], v31 offset:16896\n\t" \
  "s_lshr_b64 s[90:91], %18, 24\n\t" \
  "s_and_b32 s90, s90, 15\n\t" \
  "s_lshl_b32 s90, s90, 14\n\t" \
  "s_add_u32 s90, s90, s92\n\t" \
  "s_addc_u32 s91, s93, 0\n\t" \
  "global_load_dwordx4 v[160:163], v12, s[90:91] offset:0 sc1\n\t" \
  "s_waitcnt vmcnt(13)\n\t" \
  "v_mfma_f32_16x16x32_bf16 v[228:231], v[196:199], v[108:111], v[228:231]\n\t" \
  "ds_read_b64_tr_b16 v[220:221], v31 offset:20480\n\t" \
  "v_mfma_f32_16x16x32_bf16 v[228:231], v[192:195], v[104:107], v[228:231]\n\t" \
  "ds_read_b64_tr_b16 v[222:223], v31 offset:20992\n\t" \
  "global_load_dwordx4 v[164:167], v12, s[90:91] offset:1024 sc1\n\t" \
  "v_mfma_f32_16x16x32_bf16 v[232:235], v[200:203], v[96:99], v[232:235]\n\t" \
  "ds_read_b64_tr_b16 v[192:193], v28 offset:24576\n\t" \
  "s_waitcnt lgkmcnt(6)\n\t" \
  "v_mfma_f32_16x16x32_bf16 v[232:235], v[204:207], v[100:103], v[232:235]\n\t" \
  "ds_read_b64_tr_b16 v[194:195], v28 offset:25088\n\t" \
  "global_load_dwordx4 v[168:171], v12, s[90:91] offset:2048 sc1\n\t" \
  "v_mfma_f32_16x16x32_bf16 v[236:239], v[204:207], v[108:111], v[236:239]\n\t" \
  "ds_read_b64_tr_b16 v[196:197], v28 offset:28672\n\t" \
  "v_mfma_f32_16x16x32_bf16 v[236:239], v[200:203], v[104:107], v[236:239]\n\t" \
  "ds_read_b64_tr_b16 v[198:199], v28 offset:29184\n\t" \
  "global_load_dwordx4 v[172:175], v12, s[90:91] offset:3072 sc1\n\t" \
  "v_mfma_f32_16x16x32_bf16 v[240:243], v[208:211], v[96:99], v[240:243]\n\t" \
  "ds_read_b64_tr_b16 v[200:201], v29 offset:24576\n\t" \
  "s_waitcnt lgkmcnt(6)\n\t" \
  "v_mfma_f32_16x16x32_bf16 v[240:243], v[212:215], v[100:103], v[240:243]\n\t" \
  "ds_read_b64_tr_b16 v[202:203], v29 offset:25088\n\t" \
  "s_lshr_b64 s[90:91], %18, 28\n\t" \
  "s_and_b32 s90, s90, 15\n\t" \
  "s_lshl_b32 s90, s90, 14\n\t" \
  "s_add_u32 s90, s90, s92\n\t" \
  "s_addc_u32 s91, s93, 0\n\t" \
  "global_load_dwordx4 v[176:179], v12, s[90:91] offset:0 sc1\n\t" \
  "v_mfma_f32_16x16x32_bf16 v[244:247], v[212:215], v[108:111], v[244:247]\n\t" \
  "ds_read_b64_tr_b16 v[204:205], v29 offset:28672\n\t" \
  "v_mfma_f32_16x16x32_bf16 v[244:247], v[208:211], v[104:107], v[244:247]\n\t" \
  "ds_read_b64_tr_b16 v[206:207], v29 offset:29184\n\t" \
  "global_load_dwordx4 v[180:183], v12, s[90:91] offset:1024 sc1\n\t" \
  "v_mfma_f32_16x16x32_bf16 v[248:251], v[216:219], v[96:99], v[248:251]\n\t" \
  "ds_read_b64_tr_b16 v[208:209], v30 offset:24576\n\t" \
  "s_waitcnt lgkmcnt(6)\n\t" \
  "v_mfma_f32_16x16x32_bf16 v[248:251], v[220:223], v[100:103], v[248:251]\n\t" \
  "ds_read_b64_tr_b16 v[210:211], v30 offset:25088\n\t" \
  "global_load_dwordx4 v[184:187], v12, s[90:91] offset:2048 sc1\n\t" \
  "v_mfma_f32_16x16x32_bf16 v[252:255], v[220:223], v[108:111], v[252:255]\n\t" \
  "ds_read_b64_tr_b16 v[212:213], v30 offset:28672\n\t" \
  "v_mfma_f32_16x16x32_bf16 v[252:255], v[216:219], v[104:107], v[252:255]\n\t" \
  "ds_read_b64_tr_b16 v[214:215], v30 offset:29184\n\t" \
  "global_load_dwordx4 v[188:191], v12, s[90:91] offset:3072 sc1\n\t" \
  "s_waitcnt vmcnt(18)\n\t" \
  "v_mfma_f32_16x16x32_bf16 v[224:227], v[192:195], v[112:115], v[224:227]\n\t" \
  "ds_read_b64_tr_b16 v[216:217], v31 offset:24576\n\t" \
  "s_waitcnt lgkmcnt(6)\n\t" \
  "v_mfma_f32_16x16x32_bf16 v[224:227], v[196:199], v[116:119], v[224:227]\n\t" \
  "ds_read_b64_tr_b16 v[218:219], v31 offset:25088\n\t" \
  "s_waitcnt vmcnt(16)\n\t" \
  "v_mfma_f32_16x16x32_bf16 v[228:231], v[196:199], v[124:127], v[228:231]\n\t" \
  "ds_read_b64_tr_b16 v[220:221], v31 offset:28672\n\t" \
  "s_lshr_b64 s[90:91], %18, 32\n\t" \
  "s_and_b32 s90, s90, 15\n\t" \
  "s_lshl_b32 s90, s90, 14\n\t" \
  "s_add_u32 s90, s90, s92\n\t" \
  "s_addc_u32 s91, s93, 0\n\t" \
  "global_load_dwordx4 v[64:67], v12, s[90:91] offset:0 sc1\n\t" \
  "v_mfma_f32_16x16x32_bf16 v[228:231], v[192:195], v[120:123], v[228:231]\n\t" \
  "ds_read_b64_tr_b16 v[222:223], v31 offset:29184\n\t" \
  "v_mfma_f32_16x16x32_bf16 v[232:235], v[200:203], v[112:115], v[232:235]\n\t" \
  "ds_read_b64_tr_b16 v[192:193], v28 offset:32768\n\t" \
  "s_waitcnt lgkmcnt(6)\n\t" \
  "v_mfma_f32_16x16x32_bf16 v[232:235], v[204:207], v[116:119], v[232:235]\n\t" \
  "ds_read_b64_tr_b16 v[194:195], v28 offset:33280\n\t" \
  "v_mfma_f32_16x16x32_bf16 v[236:239], v[204:207], v[124:127], v[236:239]\n\t" \
  "ds_read_b64_tr_b16 v[196:197], v28 offset:36864\n\t" \
  "global_load_dwordx4 v[68:71], v12, s[90:91] offset:1024 sc1\n\t" \
  "v_mfma_f32_16x16x32_bf16 v[236:239], v[200:203], v[120:123], v[236:239]\n\t" \
  "ds_read_b64_tr_b16 v[198:199], v28 offset:37376\n\t" \
  "v_mfma_f32_16x16x32_bf16 v[240:243], v[208:211], v[112:115], v[240:243]\n\t" \
  "ds_read_b64_tr_b16 v[200:201], v29 offset:32768\n\t" \
  "s_waitcnt lgkmcnt(6)\n\t" \
  "v_mfma_f32_16x16x32_bf16 v[240:243], v[212:215], v[116:119], v[240:243]\n\t" \
  "ds_read_b64_tr_b16 v[202:203], v29 offset:33280\n\t" \
  "v_mfma_f32_16x16x32_bf16 v[244:247], v[212:215], v[124:127], v[244:247]\n\t" \
  "ds_read_b64_tr_b16 v[204:205], v29 offset:36864\n\t" \
  "global_load_dwordx4 v[72:75], v12, s[90:91] offset:2048 sc1\n\t" \
  "v_mfma_f32_16x16x32_bf16 v[244:247], v[208:211], v[120:123], v[244:247]\n\t" \
  "ds_read_b64_tr_b16 v[206:207], v29 offset:37376\n\t" \
  "v_mfma_f32_16x16x32_bf16 v[248:251], v[216:219], v[112:115], v[248:251]\n\t" \
  "ds_read_b64_tr_b16 v[208:209], v30 offset:32768\n\t" \
  "s_waitcnt lgkmcnt(6)\n\t" \
  "v_mfma_f32_16x16x32_bf16 v[248:251], v[220:223], v[116:119], v[248:251]\n\t" \
  "ds_read_b64_tr_b16 v[210:211], v30 offset:33280\n\t" \
  "v_mfma_f32_16x16x32_bf16 v[252:255], v[220:223], v[124:127], v[252:255]\n\t" \
  "ds_read_b64_tr_b16 v[212:213], v30 offset:36864\n\t" \
  "global_load_dwordx4 v[76:79], v12, s[90:91] offset:3072 sc1\n\t" \
  "v_mfma_f32_16x16x32_bf16 v[252:255], v[216:219], v[120:123], v[252:255]\n\t" \
  "ds_read_b64_tr_b16 v[214:215], v30 offset:37376\n\t" \
  "s_waitcnt vmcnt(18)\n\t" \
  "v_mfma_f32_16x16x32_bf16 v[224:227], v[192:195], v[128:131], v[224:227]\n\t" \
  "ds_read_b64_tr_b16 v[216:217], v31 offset:32768\n\t" \
  "s_waitcnt lgkmcnt(6)\n\t" \
  "v_mfma_f32_16x16x32_bf16 v[224:227], v[196:199], v[132:135], v[224:227]\n\t" \
  "ds_read_b64_tr_b16 v[218:219], v31 offset:33280\n\t" \
  "s_waitcnt vmcnt(16)\n\t" \
  "v_mfma_f32_16x16x32_bf16 v[228:231], v[196:199], v[140:143], v[228:231]\n\t" \
  "ds_read_b64_tr_b16 v[220:221], v31 offset:36864\n\t" \
  "s_lshr_b64 s[90:91], %18, 36\n\t" \
  "s_and_b32 s90, s90, 15\n\t" \
  "s_lshl_b32 s90, s90, 14\n\t" \
  "s_add_u32 s90, s90, s92\n\t" \
  "s_addc_u32 s91, s93, 0\n\t" \
  "global_load_dwordx4 v[80:83], v12, s[90:91] offset:0 sc1\n\t" \
  "v_mfma_f32_16x16x32_bf16 v[228:231], v[192:195], v[136:139], v[228:231]\n\t" \
  "ds_read_b64_tr_b16 v[222:223], v31 offset:37376\n\t" \
  "v_mfma_f32_16x16x32_bf16 v[232:235], v[200:203], v[128:131], v[232:235]\n\t" \
  "ds_read_b64_tr_b16 v[192:193], v28 offset:40960\n\t" \
  "s_waitcnt lgkmcnt(6)\n\t" \
  "v_mfma_f32_16x16x32_bf16 v[232:235], v[204:207], v[132:135], v[232:235]\n\t" \
  "ds_read_b64_tr_b16 v[194:195], v28 offset:41472\n\t" \
  "v_mfma_f32_16x16x32_bf16 v[236:239], v[204:207], v[140:143], v[236:239]\n\t" \
  "ds_read_b64_tr_b16 v[196:197], v28 offset:45056\n\t" \
  "global_load_dwordx4 v[84:87], v12, s[90:91] offset:1024 sc1\n\t" \
  "v_mfma_f32_16x16x32_bf16 v[236:239], v[200:203], v[136:139], v[236:239]\n\t" \
  "ds_read_b64_tr_b16 v[198:199], v28 offset:45568\n\t" \
  "v_mfma_f32_16x16x32_bf16 v[240:243], v[208:211], v[128:131], v[240:243]\n\t" \
  "ds_read_b64_tr_b16 v[200:201], v29 offset:40960\n\t" \
  "s_waitcnt lgkmcnt(6)\n\t" \
  "v_mfma_f32_16x16x32_bf16 v[240:243], v[212:215], v[132:135], v[240:243]\n\t" \
  "ds_read_b64_tr_b16 v[202:203], v29 offset:41472\n\t" \
  "v_mfma_f32_16x16x32_bf16 v[244:247], v[212:215], v[140:143], v[244:247]\n\t" \
  "ds_read_b64_tr_b16 v[204:205], v29 offset:45056\n\t" \
  "global_load_dwordx4 v[88:91], v12, s[90:91] offset:2048 sc1\n\t" \
  "v_mfma_f32_16x16x32_bf16 v[244:247], v[208:211], v[136:139], v[244:247]\n\t" \
  "ds_read_b64_tr_b16 v[206:207], v29 offset:45568\n\t" \
  "v_mfma_f32_16x16x32_bf16 v[248:251], v[216:219], v[128:131], v[248:251]\n\t" \
  "ds_read_b64_tr_b16 v[208:209], v30 offset:40960\n\t" \
  "s_waitcnt lgkmcnt(6)\n\t" \
  "v_mfma_f32_16x16x32_bf16 v[248:251], v[220:223], v[132:135], v[248:251]\n\t" \
  "ds_read_b64_tr_b16 v[210:211], v30 offset:41472\n\t" \
  "v_mfma_f32_16x16x32_bf16 v[252:255], v[220:223], v[140:143], v[252:255]\n\t" \
  "ds_read_b64_tr_b16 v[212:213], v30 offset:45056\n\t" \
  "global_load_dwordx4 v[92:95], v12, s[90:91] offset:3072 sc1\n\t" \
  "v_mfma_f32_16x16x32_bf16 v[252:255], v[216:219], v[136:139], v[252:255]\n\t" \
  "ds_read_b64_tr_b16 v[214:215], v30 offset:45568\n\t" \
  "s_waitcnt vmcnt(18)\n\t" \
  "v_mfma_f32_16x16x32_bf16 v[224:227], v[192:195], v[144:147], v[224:227]\n\t" \
  "ds_read_b64_tr_b16 v[216:217], v31 offset:40960\n\t" \
  "s_waitcnt lgkmcnt(6)\n\t" \
  "v_mfma_f32_16x16x32_bf16 v[224:227], v[196:199], v[148:151], v[224:227]\n\t" \
  "ds_read_b64_tr_b16 v[218:219], v31 offset:41472\n\t" \
  "s_waitcnt vmcnt(16)\n\t" \
  "v_mfma_f32_16x16x32_bf16 v[228:231], v[196:199], v[156:159], v[228:231]\n\t" \
  "ds_read_b64_tr_b16 v[220:221], v31 offset:45056\n\t" \
  "s_lshr_b64 s[90:91], %18, 40\n\t" \
  "s_and_b32 s90, s90, 15\n\t" \
  "s_lshl_b32 s90, s90, 14\n\t" \
  "s_add_u32 s90, s90, s92\n\t" \
  "s_addc_u32 s91, s93, 0\n\t" \
  "global_load_dwordx4 v[96:99], v12, s[90:91] offset:0 sc1\n\t" \
  "v_mfma_f32_16x16x32_bf16 v[228:231], v[192:195], v[152:155], v[228:231]\n\t" \
  "ds_read_b64_tr_b16 v[222:223], v31 offset:45568\n\t" \
  "v_mfma_f32_16x16x32_bf16 v[232:235], v[200:203], v[144:147], v[232:235]\n\t" \
  "ds_read_b64_tr_b16 v[192:193], v28 offset:49152\n\t" \
  "s_waitcnt lgkmcnt(6)\n\t" \
  "v_mfma_f32_16x16x32_bf16 v[232:235], v[204:207], v[148:151], v[232:235]\n\t" \
  "ds_read_b64_tr_b16 v[194:195], v28 offset:49664\n\t" \
  "v_mfma_f32_16x16x32_bf16 v[236:239], v[204:207], v[156:159], v[236:239]\n\t" \
  "ds_read_b64_tr_b16 v[196:197], v28 offset:53248\n\t" \
  "global_load_dwordx4 v[100:103], v12, s[90:91] offset:1024 sc1\n\t" \
  "v_mfma_f32_16x16x32_bf16 v[236:239], v[200:203], v[152:155], v[236:239]\n\t" \
  "ds_read_b64_tr_b16 v[198:199], v28 offset:53760\n\t" \
  "v_mfma_f32_16x16x32_bf16 v[240:243], v[208:211], v[144:147], v[240:243]\n\t" \
  "ds_read_b64_tr_b16 v[200:201], v29 offset:49152\n\t" \
  "s_waitcnt lgkmcnt(6)\n\t" \
  "v_mfma_f32_16x16x32_bf16 v[240:243], v[212:215], v[148:151], v[240:243]\n\t" \
  "ds_read_b64_tr_b16 v[202:203], v29 offset:49664\n\t" \
  "v_mfma_f32_16x16x32_bf16 v[244:247], v[212:215], v[156:159], v[244:247]\n\t" \
  "ds_read_b64_tr_b16 v[204:205], v29 offset:53248\n\t" \
  "global_load_dwordx4 v[104:107], v12, s[90:91] offset:2048 sc1\n\t" \
  "v_mfma_f32_16x16x32_bf16 v[244:247], v[208:211], v[152:155], v[244:247]\n\t" \
  "ds_read_b64_tr_b16 v[206:207], v29 offset:53760\n\t" \
  "v_mfma_f32_16x16x32_bf16 v[248:251], v[216:219], v[144:147], v[248:251]\n\t" \
  "ds_read_b64_tr_b16 v[208:209], v30 offset:49152\n\t" \
  "s_waitcnt lgkmcnt(6)\n\t" \
  "v_mfma_f32_16x16x32_bf16 v[248:251], v[220:223], v[148:151], v[248:251]\n\t" \
  "ds_read_b64_tr_b16 v[210:211], v30 offset:49664\n\t" \
  "v_mfma_f32_16x16x32_bf16 v[252:255], v[220:223], v[156:159], v[252:255]\n\t" \
  "ds_read_b64_tr_b16 v[212:213], v30 offset:53248\n\t" \
  "global_load_dwordx4 v[108:111], v12, s[90:91] offset:3072 sc1\n\t" \
  "ds_write_b32 v56, v56 offset:108\n\t" \
  "v_mfma_f32_16x16x32_bf16 v[252:255], v[216:219], v[152:155], v[252:255]\n\t" \
  "ds_read_b64_tr_b16 v[214:215], v30 offset:53760\n\t" \
  "s_waitcnt vmcnt(18)\n\t" \
  "v_mfma_f32_16x16x32_bf16 v[224:227], v[192:195], v[160:163], v[224:227]\n\t" \
  "ds_read_b64_tr_b16 v[216:217], v31 offset:49152\n\t" \
  "s_waitcnt lgkmcnt(7)\n\t" \
  "v_mfma_f32_16x16x32_bf16 v[224:227], v[196:199], v[164:167], v[224:227]\n\t" \
  "ds_read_b64_tr_b16 v[218:219], v31 offset:49664\n\t" \
  "s_waitcnt vmcnt(16)\n\t" \
  "v_mfma_f32_16x16x32_bf16 v[228:231], v[196:199], v[172:175], v[228:231]\n\t" \
  "ds_read_b64_tr_b16 v[220:221], v31 offset:53248\n\t" \
  "s_lshr_b64 s[90:91], %18, 44\n\t" \
  "s_and_b32 s90, s90, 15\n\t" \
  "s_lshl_b32 s90, s90, 14\n\t" \
  "s_add_u32 s90, s90, s92\n\t" \
  "s_addc_u32 s91, s93, 0\n\t" \
  "global_load_dwordx4 v[112:115], v12, s[90:91] offset:0 sc1\n\t" \
  "v_mfma_f32_16x16x32_bf16 v[228:231], v[192:195], v[168:171], v[228:231]\n\t" \
  "ds_read_b64_tr_b16 v[222:223], v31 offset:53760\n\t" \
  "v_mfma_f32_16x16x32_bf16 v[232:235], v[200:203], v[160:163], v[232:235]\n\t" \
  "ds_read_b64_tr_b16 v[192:193], v28 offset:57344\n\t" \
  "s_waitcnt lgkmcnt(6)\n\t" \
  "v_mfma_f32_16x16x32_bf16 v[232:235], v[204:207], v[164:167], v[232:235]\n\t" \
  "ds_read_b64_tr_b16 v[194:195], v28 offset:57856\n\t" \
  "v_mfma_f32_16x16x32_bf16 v[236:239], v[204:207], v[172:175], v[236:239]\n\t" \
  "ds_read_b64_tr_b16 v[196:197], v28 offset:61440\n\t" \
  "global_load_dwordx4 v[116:119], v12, s[90:91] offset:1024 sc1\n\t" \
  "v_mfma_f32_16x16x32_bf16 v[236:239], v[200:203], v[168:171], v[236:239]\n\t" \
  "ds_read_b64_tr_b16 v[198:199], v28 offset:61952\n\t" \
  "v_mfma_f32_16x16x32_bf16 v[240:243], v[208:211], v[160:163], v[240:243]\n\t" \
  "ds_read_b64_tr_b16 v[200:201], v29 offset:57344\n\t" \
  "s_waitcnt lgkmcnt(6)\n\t" \
  "v_mfma_f32_16x16x32_bf16 v[240:243], v[212:215], v[164:167], v[240:243]\n\t" \
  "ds_read_b64_tr_b16 v[202:203], v29 offset:57856\n\t" \
  "v_mfma_f32_16x16x32_bf16 v[244:247], v[212:215], v[172:175], v[244:247]\n\t" \
  "ds_read_b64_tr_b16 v[204:205], v29 offset:61440\n\t" \
  "global_load_dwordx4 v[120:123], v12, s[90:91] offset:2048 sc1\n\t" \
  "v_mfma_f32_16x16x32_bf16 v[244:247], v[208:211], v[168:171], v[244:247]\n\t" \
  "ds_read_b64_tr_b16 v[206:207], v29 offset:61952\n\t" \
  "v_mfma_f32_16x16x32_bf16 v[248:251], v[216:219], v[160:163], v[248:251]\n\t" \
  "ds_read_b64_tr_b16 v[208:209], v30 offset:57344\n\t" \
  "s_waitcnt lgkmcnt(6)\n\t" \
  "v_mfma_f32_16x16x32_bf16 v[248:251], v[220:223], v[164:167], v[248:251]\n\t" \
  "ds_read_b64_tr_b16 v[210:211], v30 offset:57856\n\t" \
  "v_mfma_f32_16x16x32_bf16 v[252:255], v[220:223], v[172:175], v[252:255]\n\t" \
  "ds_read_b64_tr_b16 v[212:213], v30 offset:61440\n\t" \
  "global_load_dwordx4 v[124:127], v12, s[90:91] offset:3072 sc1\n\t" \
  "v_mfma_f32_16x16x32_bf16 v[252:255], v[216:219], v[168:171], v[252:255]\n\t" \
  "ds_read_b64_tr_b16 v[214:215], v30 offset:61952\n\t" \
  "s_waitcnt vmcnt(18)\n\t" \
  "v_mfma_f32_16x16x32_bf16 v[224:227], v[192:195], v[176:179], v[224:227]\n\t" \
  "ds_read_b64_tr_b16 v[216:217], v31 offset:57344\n\t" \
  "s_waitcnt lgkmcnt(6)\n\t" \
  "v_mfma_f32_16x16x32_bf16 v[224:227], v[196:199], v[180:183], v[224:227]\n\t" \
  "ds_read_b64_tr_b16 v[218:219], v31 offset:57856\n\t" \
  "s_waitcnt vmcnt(16)\n\t" \
  "v_mfma_f32_16x16x32_bf16 v[228:231], v[196:199], v[188:191], v[228:231]\n\t" \
  "ds_read_b64_tr_b16 v[220:221], v31 offset:61440\n\t" \
  "s_lshr_b64 s[90:91], %18, 48\n\t" \
  "s_and_b32 s90, s90, 15\n\t" \
  "s_lshl_b32 s90, s90, 14\n\t" \
  "s_add_u32 s90, s90, s92\n\t" \
  "s_addc_u32 s91, s93, 0\n\t" \
  "global_load_dwordx4 v[128:131], v12, s[90:91] offset:0 sc1\n\t" \
  "v_mfma_f32_16x16x32_bf16 v[228:231], v[192:195], v[184:187], v[228:231]\n\t" \
  "ds_read_b64_tr_b16 v[222:223], v31 offset:61952\n\t" \
  "v_mfma_f32_16x16x32_bf16 v[232:235], v[200:203], v[176:179], v[232:235]\n\t" \
  "ds_read_b64_tr_b16 v[192:193], v32 offset:0\n\t" \
  "s_waitcnt lgkmcnt(6)\n\t" \
  "v_mfma_f32_16x16x32_bf16 v[232:235], v[204:207], v[180:183], v[232:235]\n\t" \
  "ds_read_b64_tr_b16 v[194:195], v32 offset:512\n\t" \
  "v_mfma_f32_16x16x32_bf16 v[236:239], v[204:207], v[188:191], v[236:239]\n\t" \
  "ds_read_b64_tr_b16 v[196:197], v32 offset:4096\n\t" \
  "global_load_dwordx4 v[132:135], v12, s[90:91] offset:1024 sc1\n\t" \
  "v_mfma_f32_16x16x32_bf16 v[236:239], v[200:203], v[184:187], v[236:239]\n\t" \
  "ds_read_b64_tr_b16 v[198:199], v32 offset:4608\n\t" \
  "v_mfma_f32_16x16x32_bf16 v[240:243], v[208:211], v[176:179], v[240:243]\n\t" \
  "ds_read_b64_tr_b16 v[200:201], v33 offset:0\n\t" \
  "s_waitcnt lgkmcnt(6)\n\t" \
  "v_mfma_f32_16x16x32_bf16 v[240:243], v[212:215], v[180:183], v[240:243]\n\t" \
  "ds_read_b64_tr_b16 v[202:203], v33 offset:512\n\t" \
  "v_mfma_f32_16x16x32_bf16 v[244:247], v[212:215], v[188:191], v[244:247]\n\t" \
  "ds_read_b64_tr_b16 v[204:205], v33 offset:4096\n\t" \
  "global_load_dwordx4 v[136:139], v12, s[90:91] offset:2048 sc1\n\t" \
  "v_mfma_f32_16x16x32_bf16 v[244:247], v[208:211], v[184:187], v[244:247]\n\t" \
  "ds_read_b64_tr_b16 v[206:207], v33 offset:4608\n\t" \
  "v_mfma_f32_16x16x32_bf16 v[248:251], v[216:219], v[176:179], v[248:251]\n\t" \
  "ds_read_b64_tr_b16 v[208:209], v34 offset:0\n\t" \
  "s_waitcnt lgkmcnt(6)\n\t" \
  "v_mfma_f32_16x16x32_bf16 v[248:251], v[220:223], v[180:183], v[248:251]\n\t" \
  "ds_read_b64_tr_b16 v[210:211], v34 offset:512\n\t" \
  "v_mfma_f32_16x16x32_bf16 v[252:255], v[220:223], v[188:191], v[252:255]\n\t" \
  "ds_read_b64_tr_b16 v[212:213], v34 offset:4096\n\t" \
  "global_load_dwordx4 v[140:143], v12, s[90:91] offset:3072 sc1\n\t" \
  "v_mfma_f32_16x16x32_bf16 v[252:255], v[216:219], v[184:187], v[252:255]\n\t" \
  "ds_read_b64_tr_b16 v[214:215], v34 offset:4608\n\t" \
  "s_waitcnt vmcnt(18)\n\t" \
  "v_mfma_f32_16x16x32_bf16 v[224:227], v[192:195], v[64:67], v[224:227]\n\t" \
  "ds_read_b64_tr_b16 v[216:217], v35 offset:0\n\t" \
  "s_waitcnt lgkmcnt(6)\n\t" \
  "v_mfma_f32_16x16x32_bf16 v[224:227], v[196:199], v[68:71], v[224:227]\n\t" \
  "ds_read_b64_tr_b16 v[218:219], v35 offset:512\n\t" \
  "s_waitcnt vmcnt(16)\n\t" \
  "v_mfma_f32_16x16x32_bf16 v[228:231], v[196:199], v[76:79], v[228:231]\n\t" \
  "ds_read_b64_tr_b16 v[220:221], v35 offset:4096\n\t" \
  "s_lshr_b64 s[90:91], %18, 52\n\t" \
  "s_and_b32 s90, s90, 15\n\t" \
  "s_lshl_b32 s90, s90, 14\n\t" \
  "s_add_u32 s90, s90, s92\n\t" \
  "s_addc_u32 s91, s93, 0\n\t" \
  "global_load_dwordx4 v[144:147], v12, s[90:91] offset:0 sc1\n\t" \
  "v_mfma_f32_16x16x32_bf16 v[228:231], v[192:195], v[72:75], v[228:231]\n\t" \
  "ds_read_b64_tr_b16 v[222:223], v35 offset:4608\n\t" \
  "v_mfma_f32_16x16x32_bf16 v[232:235], v[200:203], v[64:67], v[232:235]\n\t" \
  "ds_read_b64_tr_b16 v[192:193], v32 offset:8192\n\t" \
  "s_waitcnt lgkmcnt(6)\n\t" \
  "v_mfma_f32_16x16x32_bf16 v[232:235], v[204:207], v[68:71], v[232:235]\n\t" \
  "ds_read_b64_tr_b16 v[194:195], v32 offset:8704\n\t" \
  "v_mfma_f32_16x16x32_bf16 v[236:239], v[204:207], v[76:79], v[236:239]\n\t" \
  "ds_read_b64_tr_b16 v[196:197], v32 offset:12288\n\t" \
  "global_load_dwordx4 v[148:151], v12, s[90:91] offset:1024 sc1\n\t" \
  "ds_read_b32 v16, v56 offset:96\n\t" \
  "v_mfma_f32_16x16x32_bf16 v[236:239], v[200:203], v[72:75], v[236:239]\n\t" \
  "ds_read_b64_tr_b16 v[198:199], v32 offset:12800\n\t" \
  "v_mfma_f32_16x16x32_bf16 v[240:243], v[208:211], v[64:67], v[240:243]\n\t" \
  "ds_read_b64_tr_b16 v[200:201], v33 offset:8192\n\t" \
  "s_waitcnt lgkmcnt(7)\n\t" \
  "v_mfma_f32_16x16x32_bf16 v[240:243], v[212:215], v[68:71], v[240:243]\n\t" \
  "ds_read_b64_tr_b16 v[202:203], v33 offset:8704\n\t" \
  "v_mfma_f32_16x16x32_bf16 v[244:247], v[212:215], v[76:79], v[244:247]\n\t" \
  "ds_read_b64_tr_b16 v[204:205], v33 offset:12288\n\t" \
  "global_load_dwordx4 v[152:155], v12, s[90:91] offset:2048 sc1\n\t" \
  "v_mfma_f32_16x16x32_bf16 v[244:247], v[208:211], v[72:75], v[244:247]\n\t" \
  "ds_read_b64_tr_b16 v[206:207], v33 offset:12800\n\t" \
  "v_mfma_f32_16x16x32_bf16 v[248:251], v[216:219], v[64:67], v[248:251]\n\t" \
  "ds_read_b64_tr_b16 v[208:209], v34 offset:8192\n\t" \
  "s_waitcnt lgkmcnt(6)\n\t" \
  "v_mfma_f32_16x16x32_bf16 v[248:251], v[220:223], v[68:71], v[248:251]\n\t" \
  "ds_read_b64_tr_b16 v[210:211], v34 offset:8704\n\t" \
  "v_mfma_f32_16x16x32_bf16 v[252:255], v[220:223], v[76:79], v[252:255]\n\t" \
  "ds_read_b64_tr_b16 v[212:213], v34 offset:12288\n\t" \
  "global_load_dwordx4 v[156:159], v12, s[90:91] offset:3072 sc1\n\t" \
  "v_readfirstlane_b32 vcc_lo, v16\n\t" \
  "v_mfma_f32_16x16x32_bf16 v[252:255], v[216:219], v[72:75], v[252:255]\n\t" \
  "ds_read_b64_tr_b16 v[214:215], v34 offset:12800\n\t" \
  "s_cmp_ge_u32 vcc_lo, 2\n\t" \
  "s_cbranch_scc0 46f\n\t" \
  "47:\n\t" \
  "s_waitcnt vmcnt(18)\n\t" \
  "v_mfma_f32_16x16x32_bf16 v[224:227], v[192:195], v[80:83], v[224:227]\n\t" \
  "ds_read_b64_tr_b16 v[216:217], v35 offset:8192\n\t" \
  "s_waitcnt lgkmcnt(6)\n\t" \
  "v_mfma_f32_16x16x32_bf16 v[224:227], v[196:199], v[84:87], v[224:227]\n\t" \
  "ds_read_b64_tr_b16 v[218:219], v35 offset:8704\n\t" \
  "s_waitcnt vmcnt(16)\n\t" \
  "v_mfma_f32_16x16x32_bf16 v[228:231], v[196:199], v[92:95], v[228:231]\n\t" \
  "ds_read_b64_tr_b16 v[220:221], v35 offset:12288\n\t" \
  "s_lshr_b64 s[90:91], %18, 56\n\t" \
  "s_and_b32 s90, s90, 15\n\t" \
  "s_lshl_b32 s90, s90, 14\n\t" \
  "s_add_u32 s90, s90, s92\n\t" \
  "s_addc_u32 s91, s93, 0\n\t" \
  "global_load_dwordx4 v[160:163], v12, s[90:91] offset:0 sc1\n\t" \
  "v_mfma_f32_16x16x32_bf16 v[228:231], v[192:195], v[88:91], v[228:231]\n\t" \
  "ds_read_b64_tr_b16 v[222:223], v35 offset:12800\n\t" \
  "v_mfma_f32_16x16x32_bf16 v[232:235], v[200:203], v[80:83], v[232:235]\n\t" \
  "ds_read_b64_tr_b16 v[192:193], v32 offset:16384\n\t" \
  "s_waitcnt lgkmcnt(6)\n\t" \
  "v_mfma_f32_16x16x32_bf16 v[232:235], v[204:207], v[84:87], v[232:235]\n\t" \
  "ds_read_b64_tr_b16 v[194:195], v32 offset:16896\n\t" \
  "v_mfma_f32_16x16x32_bf16 v[236:239], v[204:207], v[92:95], v[236:239]\n\t" \
  "ds_read_b64_tr_b16 v[196:197], v32 offset:20480\n\t" \
  "global_load_dwordx4 v[164:167], v12, s[90:91] offset:1024 sc1\n\t" \
  "v_mfma_f32_16x16x32_bf16 v[236:239], v[200:203], v[88:91], v[236:239]\n\t" \
  "ds_read_b64_tr_b16 v[198:199], v32 offset:20992\n\t" \
  "v_mfma_f32_16x16x32_bf16 v[240:243], v[208:211], v[80:83], v[240:243]\n\t" \
  "ds_read_b64_tr_b16 v[200:201], v33 offset:16384\n\t" \
  "s_waitcnt lgkmcnt(6)\n\t" \
  "v_mfma_f32_16x16x32_bf16 v[240:243], v[212:215], v[84:87], v[240:243]\n\t" \
  "ds_read_b64_tr_b16 v[202:203], v33 offset:16896\n\t" \
  "v_mfma_f32_16x16x32_bf16 v[244:247], v[212:215], v[92:95], v[244:247]\n\t" \
  "ds_read_b64_tr_b16 v[204:205], v33 offset:20480\n\t" \
  "global_load_dwordx4 v[168:171], v12, s[90:91] offset:2048 sc1\n\t" \
  "v_mfma_f32_16x16x32_bf16 v[244:247], v[208:211], v[88:91], v[244:247]\n\t" \
  "ds_read_b64_tr_b16 v[206:207], v33 offset:20992\n\t" \
  "v_mfma_f32_16x16x32_bf16 v[248:251], v[216:219], v[80:83], v[248:251]\n\t" \
  "ds_read_b64_tr_b16 v[208:209], v34 offset:16384\n\t" \
  "s_waitcnt lgkmcnt(6)\n\t" \
  "v_mfma_f32_16x16x32_bf16 v[248:251], v[220:223], v[84:87], v[248:251]\n\t" \
  "ds_read_b64_tr_b16 v[210:211], v34 offset:16896\n\t" \
  "v_mfma_f32_16x16x32_bf16 v[252:255], v[220:223], v[92:95], v[252:255]\n\t" \
  "ds_read_b64_tr_b16 v[212:213], v34 offset:20480\n\t" \
  "global_load_dwordx4 v[172:175], v12, s[90:91] offset:3072 sc1\n\t" \
  "v_mfma_f32_16x16x32_bf16 v[252:255], v[216:219], v[88:91], v[252:255]\n\t" \
  "ds_read_b64_tr_b16 v[214:215], v34 offset:20992\n\t" \
  "s_waitcnt vmcnt(18)\n\t" \
  "v_mfma_f32_16x16x32_bf16 v[224:227], v[192:195], v[96:99], v[224:227]\n\t" \
  "ds_read_b64_tr_b16 v[216:217], v35 offset:16384\n\t" \
  "s_waitcnt lgkmcnt(6)\n\t" \
  "v_mfma_f32_16x16x32_bf16 v[224:227], v[196:199], v[100:103], v[224:227]\n\t" \
  "ds_read_b64_tr_b16 v[218:219], v35 offset:16896\n\t" \
  "s_waitcnt vmcnt(16)\n\t" \
  "v_mfma_f32_16x16x32_bf16 v[228:231], v[196:199], v[108:111], v[228:231]\n\t" \
  "509:\n\t" \
  "s_cbranch_execz 26b\n\t" \
  "ds_read_b64_tr_b16 v[220:221], v35 offset:20480\n\t" \
  "s_lshr_b64 s[90:91], %18, 60\n\t" \
  "s_and_b32 s90, s90, 15\n\t" \
  "s_lshl_b32 s90, s90, 14\n\t" \
  "s_add_u32 s90, s90, s92\n\t" \
  "s_addc_u32 s91, s93, 0\n\t" \
  "global_load_dwordx4 v[176:179], v12, s[90:91] offset:0 sc1\n\t" \
  "v_mfma_f32_16x16x32_bf16 v[228:231], v[192:195], v[104:107], v[228:231]\n\t" \
  "ds_read_b64_tr_b16 v[222:223], v35 offset:20992\n\t" \
  "v_mfma_f32_16x16x32_bf16 v[232:235], v[200:203], v[96:99], v[232:235]\n\t" \
  "ds_read_b64_tr_b16 v[192:193], v32 offset:24576\n\t" \
  "s_waitcnt lgkmcnt(6)\n\t" \
  "v_mfma_f32_16x16x32_bf16 v[232:235], v[204:207], v[100:103], v[232:235]\n\t" \
  "ds_read_b64_tr_b16 v[194:195], v32 offset:25088\n\t" \
  "v_mfma_f32_16x16x32_bf16 v[236:239], v[204:207], v[108:111], v[236:239]\n\t" \
  "ds_read_b64_tr_b16 v[196:197], v32 offset:28672\n\t" \
  "global_load_dwordx4 v[180:183], v12, s[90:91] offset:1024 sc1\n\t" \
  "v_mfma_f32_16x16x32_bf16 v[236:239], v[200:203], v[104:107], v[236:239]\n\t" \
  "ds_read_b64_tr_b16 v[198:199], v32 offset:29184\n\t" \
  "v_mfma_f32_16x16x32_bf16 v[240:243], v[208:211], v[96:99], v[240:243]\n\t" \
  "ds_read_b64_tr_b16 v[200:201], v33 offset:24576\n\t" \
  "s_waitcnt lgkmcnt(6)\n\t" \
  "v_mfma_f32_16x16x32_bf16 v[240:243], v[212:215], v[100:103], v[240:243]\n\t" \
  "ds_read_b64_tr_b16 v[202:203], v33 offset:25088\n\t" \
  "v_mfma_f32_16x16x32_bf16 v[244:247], v[212:215], v[108:111], v[244:247]\n\t" \
  "ds_read_b64_tr_b16 v[204:205], v33 offset:28672\n\t" \
  "global_load_dwordx4 v[184:187], v12, s[90:91] offset:2048 sc1\n\t" \
  "v_mfma_f32_16x16x32_bf16 v[244:247], v[208:211], v[104:107], v[244:247]\n\t" \
  "ds_read_b64_tr_b16 v[206:207], v33 offset:29184\n\t" \
  "v_mfma_f32_16x16x32_bf16 v[248:251], v[216:219], v[96:99], v[248:251]\n\t" \
  "ds_read_b64_tr_b16 v[208:209], v34 offset:24576\n\t" \
  "s_waitcnt lgkmcnt(6)\n\t" \
  "v_mfma_f32_16x16x32_bf16 v[248:251], v[220:223], v[100:103], v[248:251]\n\t" \
  "ds_read_b64_tr_b16 v[210:211], v34 offset:25088\n\t" \
  "v_mfma_f32_16x16x32_bf16 v[252:255], v[220:223], v[108:111], v[252:255]\n\t" \
  "ds_read_b64_tr_b16 v[212:213], v34 offset:28672\n\t" \
  "global_load_dwordx4 v[188:191], v12, s[90:91] offset:3072 sc1\n\t" \
  "v_mfma_f32_16x16x32_bf16 v[252:255], v[216:219], v[104:107], v[252:255]\n\t" \
  "ds_read_b64_tr_b16 v[214:215], v34 offset:29184\n\t" \
  "s_waitcnt vmcnt(18)\n\t" \
  "v_mfma_f32_16x16x32_bf16 v[224:227], v[192:195], v[112:115], v[224:227]\n\t" \
  "ds_read_b64_tr_b16 v[216:217], v35 offset:24576\n\t" \
  "s_waitcnt lgkmcnt(6)\n\t" \
  "v_mfma_f32_16x16x32_bf16 v[224:227], v[196:199], v[116:119], v[224:227]\n\t" \
  "ds_read_b64_tr_b16 v[218:219], v35 offset:25088\n\t" \
  "s_waitcnt vmcnt(16)\n\t" \
  "v_mfma_f32_16x16x32_bf16 v[228:231], v[196:199], v[124:127], v[228:231]\n\t" \
  "ds_read_b64_tr_b16 v[220:221], v35 offset:28672\n\t" \
  "v_mfma_f32_16x16x32_bf16 v[228:231], v[192:195], v[120:123], v[228:231]\n\t" \
  "ds_read_b64_tr_b16 v[222:223], v35 offset:29184\n\t" \
  "v_mfma_f32_16x16x32_bf16 v[232:235], v[200:203], v[112:115], v[232:235]\n\t" \
  "ds_read_b64_tr_b16 v[192:193], v32 offset:32768\n\t" \
  "s_waitcnt lgkmcnt(6)\n\t" \
  "v_mfma_f32_16x16x32_bf16 v[232:235], v[204:207], v[116:119], v[232:235]\n\t" \
  "ds_read_b64_tr_b16 v[194:195], v32 offset:33280\n\t" \
  "v_mfma_f32_16x16x32_bf16 v[236:239], v[204:207], v[124:127], v[236:239]\n\t" \
  "ds_read_b64_tr_b16 v[196:197], v32 offset:36864\n\t" \
  "v_mfma_f32_16x16x32_bf16 v[236:239], v[200:203], v[120:123], v[236:239]\n\t" \
  "ds_read_b64_tr_b16 v[198:199], v32 offset:37376\n\t" \
  "v_mfma_f32_16x16x32_bf16 v[240:243], v[208:211], v[112:115], v[240:243]\n\t" \
  "ds_read_b64_tr_b16 v[200:201], v33 offset:32768\n\t" \
  "s_waitcnt lgkmcnt(6)\n\t" \
  "v_mfma_f32_16x16x32_bf16 v[240:243], v[212:215], v[116:119], v[240:243]\n\t" \
  "ds_read_b128 v[40:43], v56 offset:80\n\t" \
  "ds_read_b64_tr_b16 v[202:203], v33 offset:33280\n\t" \
  "v_mfma_f32_16x16x32_bf16 v[244:247], v[212:215], v[124:127], v[244:247]\n\t" \
  "ds_read_b64_tr_b16 v[204:205], v33 offset:36864\n\t" \
  "v_mfma_f32_16x16x32_bf16 v[244:247], v[208:211], v[120:123], v[244:247]\n\t" \
  "ds_read_b64_tr_b16 v[206:207], v33 offset:37376\n\t" \
  "v_mfma_f32_16x16x32_bf16 v[248:251], v[216:219], v[112:115], v[248:251]\n\t" \
  "ds_read_b64_tr_b16 v[208:209], v34 offset:32768\n\t" \
  "s_waitcnt lgkmcnt(7)\n\t" \
  "v_mfma_f32_16x16x32_bf16 v[248:251], v[220:223], v[116:119], v[248:251]\n\t" \
  "ds_read_b64_tr_b16 v[210:211], v34 offset:33280\n\t" \
  "v_mfma_f32_16x16x32_bf16 v[252:255], v[220:223], v[124:127], v[252:255]\n\t" \
  "ds_read_b64_tr_b16 v[212:213], v34 offset:36864\n\t" \
  "v_mfma_f32_16x16x32_bf16 v[252:255], v[216:219], v[120:123], v[252:255]\n\t" \
  "ds_read_b64_tr_b16 v[214:215], v34 offset:37376\n\t" \
  "s_waitcnt vmcnt(14)\n\t" \
  "v_mfma_f32_16x16x32_bf16 v[224:227], v[192:195], v[128:131], v[224:227]\n\t" \
  "s_waitcnt lgkmcnt(7)\n\t" \
  "v_min_u32 v40, v40, v41\n\t" \
  "v_min3_u32 v40, v40, v42, v43\n\t" \
  "ds_read_b64_tr_b16 v[216:217], v35 offset:32768\n\t" \
  "v_mfma_f32_16x16x32_bf16 v[224:227], v[196:199], v[132:135], v[224:227]\n\t" \
  "v_readfirstlane_b32 vcc_lo, v40\n\t" \
  "ds_read_b64_tr_b16 v[218:219], v35 offset:33280\n\t" \
  "s_waitcnt vmcnt(12)\n\t" \
  "v_mfma_f32_16x16x32_bf16 v[228:231], v[196:199], v[140:143], v[228:231]\n\t" \
  "s_cmp_ge_u32 vcc_lo, 16\n\t" \
  "s_cbranch_scc0 37f\n\t" \
  "38:\n\t" \
  "ds_read_b64_tr_b16 v[220:221], v35 offset:36864\n\t" \
  "v_mfma_f32_16x16x32_bf16 v[228:231], v[192:195], v[136:139], v[228:231]\n\t" \
  "ds_read_b64_tr_b16 v[222:223], v35 offset:37376\n\t" \
  "s_waitcnt lgkmcnt(6)\n\t" \
  "v_mfma_f32_16x16x32_bf16 v[232:235], v[200:203], v[128:131], v[232:235]\n\t" \
  "ds_read_b64_tr_b16 v[192:193], v36 offset:0\n\t" \
  "v_mfma_f32_16x16x32_bf16 v[232:235], v[204:207], v[132:135], v[232:235]\n\t" \
  "ds_read_b64_tr_b16 v[194:195], v36 offset:512\n\t" \
  "v_mfma_f32_16x16x32_bf16 v[236:239], v[204:207], v[140:143], v[236:239]\n\t" \
  "ds_read_b64_tr_b16 v[196:197], v36 offset:4096\n\t" \
  "v_mfma_f32_16x16x32_bf16 v[236:239], v[200:203], v[136:139], v[236:239]\n\t" \
  "ds_read_b64_tr_b16 v[198:199], v36 offset:4608\n\t" \
  "v_mfma_f32_16x16x32_bf16 v[240:243], v[208:211], v[128:131], v[240:243]\n\t" \
  "ds_read_b64_tr_b16 v[200:201], v37 offset:0\n\t" \
  "s_waitcnt lgkmcnt(6)\n\t" \
  "v_mfma_f32_16x16x32_bf16 v[240:243], v[212:215], v[132:135], v[240:243]\n\t" \
  "ds_read_b64_tr_b16 v[202:203], v37 offset:512\n\t" \
  "v_mfma_f32_16x16x32_bf16 v[244:247], v[212:215], v[140:143], v[244:247]\n\t" \
  "ds_read_b64_tr_b16 v[204:205], v37 offset:4096\n\t" \
  "v_mfma_f32_16x16x32_bf16 v[244:247], v[208:211], v[136:139], v[244:247]\n\t" \
  "ds_read_b64_tr_b16 v[206:207], v37 offset:4608\n\t" \
  "v_mfma_f32_16x16x32_bf16 v[248:251], v[216:219], v[128:131], v[248:251]\n\t" \
  "ds_read_b64_tr_b16 v[208:209], v38 offset:0\n\t" \
  "s_waitcnt lgkmcnt(6)\n\t" \
  "v_mfma_f32_16x16x32_bf16 v[248:251], v[220:223], v[132:135], v[248:251]\n\t" \
  "ds_read_b64_tr_b16 v[210:211], v38 offset:512\n\t" \
  "v_mfma_f32_16x16x32_bf16 v[252:255], v[220:223], v[140:143], v[252:255]\n\t" \
  "ds_read_b64_tr_b16 v[212:213], v38 offset:4096\n\t" \
  "v_mfma_f32_16x16x32_bf16 v[252:255], v[216:219], v[136:139], v[252:255]\n\t" \
  "ds_read_b64_tr_b16 v[214:215], v38 offset:4608\n\t" \
  "s_waitcnt vmcnt(10)\n\t" \
  "v_mfma_f32_16x16x32_bf16 v[224:227], v[192:195], v[144:147], v[224:227]\n\t" \
  "ds_read_b64_tr_b16 v[216:217], v39 offset:0\n\t" \
  "s_waitcnt lgkmcnt(6)\n\t" \
  "v_mfma_f32_16x16x32_bf16 v[224:227], v[196:199], v[148:151], v[224:227]\n\t" \
  "ds_read_b64_tr_b16 v[218:219], v39 offset:512\n\t" \
  "s_waitcnt vmcnt(8)\n\t" \
  "v_mfma_f32_16x16x32_bf16 v[228:231], v[196:199], v[156:159], v[228:231]\n\t" \
  "ds_read_b64_tr_b16 v[220:221], v39 offset:4096\n\t" \
  "v_mfma_f32_16x16x32_bf16 v[228:231], v[192:195], v[152:155], v[228:231]\n\t" \
  "ds_read_b64_tr_b16 v[222:223], v39 offset:4608\n\t" \
  "v_mfma_f32_16x16x32_bf16 v[232:235], v[200:203], v[144:147], v[232:235]\n\t" \
  "ds_read_b64_tr_b16 v[192:193], v36 offset:8192\n\t" \
  "s_waitcnt lgkmcnt(6)\n\t" \
  "v_mfma_f32_16x16x32_bf16 v[232:235], v[204:207], v[148:151], v[232:235]\n\t" \
  "ds_read_b64_tr_b16 v[194:195], v36 offset:8704\n\t" \
  "v_mfma_f32_16x16x32_bf16 v[236:239], v[204:207], v[156:159], v[236:239]\n\t" \
  "ds_read_b64_tr_b16 v[196:197], v36 offset:12288\n\t" \
  "v_mfma_f32_16x16x32_bf16 v[236:239], v[200:203], v[152:155], v[236:239]\n\t" \
  "ds_read_b64_tr_b16 v[198:199], v36 offset:12800\n\t" \
  "v_mfma_f32_16x16x32_bf16 v[240:243], v[208:211], v[144:147], v[240:243]\n\t" \
  "ds_read_b64_tr_b16 v[200:201], v37 offset:8192\n\t" \
  "s_waitcnt lgkmcnt(6)\n\t" \
  "v_mfma_f32_16x16x32_bf16 v[240:243], v[212:215], v[148:151], v[240:243]\n\t" \
  "ds_read_b128 v[40:43], v56 offset:160\n\t" \
  "ds_read_b64_tr_b16 v[202:203], v37 offset:8704\n\t" \
  "v_mfma_f32_16x16x32_bf16 v[244:247], v[212:215], v[156:159], v[244:247]\n\t" \
  "ds_read_b64_tr_b16 v[204:205], v37 offset:12288\n\t" \
  "v_mfma_f32_16x16x32_bf16 v[244:247], v[208:211], v[152:155], v[244:247]\n\t" \
  "ds_read_b64_tr_b16 v[206:207], v37 offset:12800\n\t" \
  "v_mfma_f32_16x16x32_bf16 v[248:251], v[216:219], v[144:147], v[248:251]\n\t" \
  "ds_read_b64_tr_b16 v[208:209], v38 offset:8192\n\t" \
  "s_waitcnt lgkmcnt(7)\n\t" \
  "v_mfma_f32_16x16x32_bf16 v[248:251], v[220:223], v[148:151], v[248:251]\n\t" \
  "ds_read_b64_tr_b16 v[210:211], v38 offset:8704\n\t" \
  "v_mfma_f32_16x16x32_bf16 v[252:255], v[220:223], v[156:159], v[252:255]\n\t" \
  "ds_read_b64_tr_b16 v[212:213], v38 offset:12288\n\t" \
  "v_mfma_f32_16x16x32_bf16 v[252:255], v[216:219], v[152:155], v[252:255]\n\t" \
  "ds_read_b64_tr_b16 v[214:215], v38 offset:12800\n\t" \
  "s_waitcnt vmcnt(6)\n\t" \
  "v_mfma_f32_16x16x32_bf16 v[224:227], v[192:195], v[160:163], v[224:227]\n\t" \
  "s_waitcnt lgkmcnt(7)\n\t" \
  "v_min_u32 v40, v40, v41\n\t" \
  "v_min3_u32 v40, v40, v42, v43\n\t" \
  "ds_read_b64_tr_b16 v[216:217], v39 offset:8192\n\t" \
  "v_mfma_f32_16x16x32_bf16 v[224:227], v[196:199], v[164:167], v[224:227]\n\t" \
  "v_readfirstlane_b32 vcc_lo, v40\n\t" \
  "ds_read_b64_tr_b16 v[218:219], v39 offset:8704\n\t" \
  "s_waitcnt vmcnt(4)\n\t" \
  "v_mfma_f32_16x16x32_bf16 v[228:231], v[196:199], v[172:175], v[228:231]\n\t" \
  "s_cmp_ge_u32 vcc_lo, 16\n\t" \
  "s_cbranch_scc0 39f\n\t" \
  "40:\n\t" \
  "ds_read_b64_tr_b16 v[220:221], v39 offset:12288\n\t" \
  "v_mfma_f32_16x16x32_bf16 v[228:231], v[192:195], v[168:171], v[228:231]\n\t" \
  "ds_read_b64_tr_b16 v[222:223], v39 offset:12800\n\t" \
  "s_waitcnt lgkmcnt(6)\n\t" \
  "v_mfma_f32_16x16x32_bf16 v[232:235], v[200:203], v[160:163], v[232:235]\n\t" \
  "ds_read_b64_tr_b16 v[192:193], v32 offset:45312\n\t" \
  "v_mfma_f32_16x16x32_bf16 v[232:235], v[204:207], v[164:167], v[232:235]\n\t" \
  "ds_read_b64_tr_b16 v[194:195], v32 offset:45824\n\t" \
  "v_mfma_f32_16x16x32_bf16 v[236:239], v[204:207], v[172:175], v[236:239]\n\t" \
  "ds_read_b64_tr_b16 v[196:197], v32 offset:53760\n\t" \
  "v_mfma_f32_16x16x32_bf16 v[236:239], v[200:203], v[168:171], v[236:239]\n\t" \
  "ds_read_b64_tr_b16 v[198:199], v32 offset:54272\n\t" \
  "v_mfma_f32_16x16x32_bf16 v[240:243], v[208:211], v[160:163], v[240:243]\n\t" \
  "ds_read_b64_tr_b16 v[200:201], v33 offset:45312\n\t" \
  "s_waitcnt lgkmcnt(6)\n\t" \
  "v_mfma_f32_16x16x32_bf16 v[240:243], v[212:215], v[164:167], v[240:243]\n\t" \
  "ds_read_b64_tr_b16 v[202:203], v33 offset:45824\n\t" \
  "v_mfma_f32_16x16x32_bf16 v[244:247], v[212:215], v[172:175], v[244:247]\n\t" \
  "ds_read_b64_tr_b16 v[204:205], v33 offset:53760\n\t" \
  "v_mfma_f32_16x16x32_bf16 v[244:247], v[208:211], v[168:171], v[244:247]\n\t" \
  "ds_read_b64_tr_b16 v[206:207], v33 offset:54272\n\t" \
  "v_mfma_f32_16x16x32_bf16 v[248:251], v[216:219], v[160:163], v[248:251]\n\t" \
  "ds_read_b64_tr_b16 v[208:209], v34 offset:45312\n\t" \
  "s_waitcnt lgkmcnt(6)\n\t" \
  "v_mfma_f32_16x16x32_bf16 v[248:251], v[220:223], v[164:167], v[248:251]\n\t" \
  "ds_read_b64_tr_b16 v[210:211], v34 offset:45824\n\t" \
  "v_mfma_f32_16x16x32_bf16 v[252:255], v[220:223], v[172:175], v[252:255]\n\t" \
  "ds_read_b64_tr_b16 v[212:213], v34 offset:53760\n\t" \
  "v_mfma_f32_16x16x32_bf16 v[252:255], v[216:219], v[168:171], v[252:255]\n\t" \
  "ds_read_b64_tr_b16 v[214:215], v34 offset:54272\n\t" \
  "s_waitcnt vmcnt(2)\n\t" \
  "v_mfma_f32_16x16x32_bf16 v[224:227], v[192:195], v[176:179], v[224:227]\n\t" \
  "ds_read_b64_tr_b16 v[216:217], v35 offset:45312\n\t" \
  "s_waitcnt lgkmcnt(6)\n\t" \
  "v_mfma_f32_16x16x32_bf16 v[224:227], v[196:199], v[180:183], v[224:227]\n\t" \
  "ds_read_b64_tr_b16 v[218:219], v35 offset:45824\n\t" \
  "s_waitcnt vmcnt(0)\n\t" \
  "v_mfma_f32_16x16x32_bf16 v[228:231], v[196:199], v[188:191], v[228:231]\n\t" \
  "ds_read_b64_tr_b16 v[220:221], v35 offset:53760\n\t" \
  "v_mfma_f32_16x16x32_bf16 v[228:231], v[192:195], v[184:187], v[228:231]\n\t" \
  "ds_read_b64_tr_b16 v[222:223], v35 offset:54272\n\t" \
  "v_mfma_f32_16x16x32_bf16 v[232:235], v[200:203], v[176:179], v[232:235]\n\t" \
  "s_waitcnt lgkmcnt(5)\n\t" \
  "v_mfma_f32_16x16x32_bf16 v[232:235], v[204:207], v[180:183], v[232:235]\n\t" \
  "v_mfma_f32_16x16x32_bf16 v[236:239], v[204:207], v[188:191], v[236:239]\n\t" \
  "v_cvt_pk_bf16_f32 v224, v224, v225\n\t" \
  "v_cvt_pk_bf16_f32 v225, v226, v227\n\t" \
  "v_mfma_f32_16x16x32_bf16 v[236:239], v[200:203], v[184:187], v[236:239]\n\t" \
  "ds_write_b64 v57, v[224:225] offset:0\n\t" \
  "v_mfma_f32_16x16x32_bf16 v[240:243], v[208:211], v[176:179], v[240:243]\n\t" \
  "v_cvt_pk_bf16_f32 v228, v228, v229\n\t" \
  "v_cvt_pk_bf16_f32 v229, v230, v231\n\t" \
  "s_waitcnt lgkmcnt(2)\n\t" \
  "v_mfma_f32_16x16x32_bf16 v[240:243], v[212:215], v[180:183], v[240:243]\n\t" \
  "ds_write_b64 v57, v[228:229] offset:2048\n\t" \
  "v_mfma_f32_16x16x32_bf16 v[244:247], v[212:215], v[188:191], v[244:247]\n\t" \
  "v_cvt_pk_bf16_f32 v232, v232, v233\n\t" \
  "v_cvt_pk_bf16_f32 v233, v234, v235\n\t" \
  "v_mfma_f32_16x16x32_bf16 v[244:247], v[208:211], v[184:187], v[244:247]\n\t" \
  "ds_write_b64 v58, v[232:233] offset:0\n\t" \
  "v_mfma_f32_16x16x32_bf16 v[248:251], v[216:219], v[176:179], v[248:251]\n\t" \
  "v_cvt_pk_bf16_f32 v236, v236, v237\n\t" \
  "v_cvt_pk_bf16_f32 v237, v238, v239\n\t" \
  "s_waitcnt lgkmcnt(3)\n\t" \
  "v_mfma_f32_16x16x32_bf16 v[248:251], v[220:223], v[180:183], v[248:251]\n\t" \
  "ds_write_b64 v58, v[236:237] offset:2048\n\t" \
  "v_mfma_f32_16x16x32_bf16 v[252:255], v[220:223], v[188:191], v[252:255]\n\t" \
  "v_cvt_pk_bf16_f32 v240, v240, v241\n\t" \
  "v_cvt_pk_bf16_f32 v241, v242, v243\n\t" \
  "v_mfma_f32_16x16x32_bf16 v[252:255], v[216:219], v[184:187], v[252:255]\n\t" \
  "ds_write_b64 v59, v[240:241] offset:0\n\t" \
  "v_cvt_pk_bf16_f32 v244, v244, v245\n\t" \
  "v_cvt_pk_bf16_f32 v245, v246, v247\n\t" \
  "ds_write_b64 v59, v[244:245] offset:2048\n\t" \
  "v_cvt_pk_bf16_f32 v248, v248, v249\n\t" \
  "v_cvt_pk_bf16_f32 v249, v250, v251\n\t" \
  "ds_write_b64 v60, v[248:249] offset:0\n\t" \
  "s_nop 0\n\t" \
  "v_cvt_pk_bf16_f32 v252, v252, v253\n\t" \
  "v_cvt_pk_bf16_f32 v253, v254, v255\n\t" \
  "ds_write_b64 v60, v[252:253] offset:2048\n\t" \
  "ds_read_b128 v[64:67], v61 offset:0\n\t" \
  "ds_read_b128 v[68:71], v61 offset:1024\n\t" \
  "ds_read_b128 v[72:75], v61 offset:2048\n\t" \
  "ds_read_b128 v[76:79], v61 offset:3072\n\t" \
  "s_waitcnt lgkmcnt(3)\n\t" \
  "global_store_dwordx4 v62, v[64:67], %22 offset:-4096" OUTPOL "\n\t" \
  "s_waitcnt lgkmcnt(2)\n\t" \
  "global_store_dwordx4 v63, v[68:71], %22 offset:-4096" OUTPOL "\n\t" \
  "s_waitcnt lgkmcnt(1)\n\t" \
  "global_store_dwordx4 v62, v[72:75], %22 offset:0" OUTPOL "\n\t" \
  "s_waitcnt lgkmcnt(0)\n\t" \
  "global_store_dwordx4 v63, v[76:79], %22 offset:0" OUTPOL "\n\t" \
  "s_branch 19f\n\t" \
  "7:\n\t" \
  "s_sleep 1\n\t" \
  "ds_read_b128 v[60:63], v56 offset:64\n\t" \
  "ds_read_b64_tr_b16 v[108:109], v5 offset:0\n\t" \
  "ds_read_b64_tr_b16 v[110:111], v5 offset:16\n\t" \
  "ds_read_b128 v[112:115], v6 offset:16384\n\t" \
  "ds_read_b128 v[116:119], v6 offset:15360\n\t" \
  "ds_read_b64_tr_b16 v[120:121], v5 offset:512\n\t" \
  "ds_read_b64_tr_b16 v[122:123], v5 offset:528\n\t" \
  "ds_read_b128 v[124:127], v6 offset:14336\n\t" \
  "ds_read_b64_tr_b16 v[128:129], v5 offset:1024\n\t" \
  "ds_read_b64_tr_b16 v[130:131], v5 offset:1040\n\t" \
  "ds_read_b128 v[132:135], v6 offset:13312\n\t" \
  "ds_read_b64_tr_b16 v[136:137], v5 offset:1536\n\t" \
  "ds_read_b64_tr_b16 v[138:139], v5 offset:1552\n\t" \
  "ds_read_b128 v[140:143], v6 offset:12288\n\t" \
  "ds_read_b64_tr_b16 v[144:145], v5 offset:2048\n\t" \
  "s_waitcnt lgkmcnt(0)\n\t" \
  "v_and_b32 v60, v60, v61\n\t" \
  "v_and_b32 v62, v62, v63\n\t" \
  "v_mov_b32 v12, %13\n\t" \
  "v_and_b32 v60, v60, v62\n\t" \
  "s_nop 0\n\t" \
  "v_readfirstlane_b32 vcc_lo, v60\n\t" \
  "s_nop 1\n\t" \
  "s_cmp_eq_u32 vcc_lo, 0\n\t" \
  "s_cbranch_scc1 7b\n\t" \
  "s_branch 6b\n\t" \
  "25:\n\t" \
  "510:\n\t" \
  "s_cbranch_execz 26b\n\t" \
  "global_load_dwordx4 v[64:67], v12, s[90:91] offset:0 sc1\n\t" \
  "global_load_dwordx4 v[68:71], v12, s[90:91] offset:1024 sc1\n\t" \
  "global_load_dwordx4 v[72:75], v12, s[90:91] offset:2048 sc1\n\t" \
  "global_load_dwordx4 v[76:79], v12, s[90:91] offset:3072 sc1\n\t" \
  "s_swappc_b64 s[70:71], s[68:69]\n\t" \
  "global_load_dwordx4 v[80:83], v12, s[90:91] offset:0 sc1\n\t" \
  "global_load_dwordx4 v[84:87], v12, s[90:91] offset:1024 sc1\n\t" \
  "global_load_dwordx4 v[88:91], v12, s[90:91] offset:2048 sc1\n\t" \
  "global_load_dwordx4 v[92:95], v12, s[90:91] offset:3072 sc1\n\t" \
  "s_swappc_b64 s[70:71], s[68:69]\n\t" \
  "global_load_dwordx4 v[96:99], v12, s[90:91] offset:0 sc1\n\t" \
  "global_load_dwordx4 v[100:103], v12, s[90:91] offset:1024 sc1\n\t" \
  "global_load_dwordx4 v[104:107], v12, s[90:91] offset:2048 sc1\n\t" \
  "global_load_dwordx4 v[108:111], v12, s[90:91] offset:3072 sc1\n\t" \
  "s_swappc_b64 s[70:71], s[68:69]\n\t" \
  "global_load_dwordx4 v[112:115], v12, s[90:91] offset:0 sc1\n\t" \
  "global_load_dwordx4 v[116:119], v12, s[90:91] offset:1024 sc1\n\t" \
  "global_load_dwordx4 v[120:123], v12, s[90:91] offset:2048 sc1\n\t" \
  "global_load_dwordx4 v[124:127], v12, s[90:91] offset:3072 sc1\n\t" \
  "s_swappc_b64 s[70:71], s[68:69]\n\t" \
  "35:\n\t" \
  "ds_read_b128 v[40:43], v56 offset:80\n\t" \
  "s_waitcnt lgkmcnt(0)\n\t" \
  "v_min_u32 v40, v40, v41\n\t" \
  "v_min3_u32 v40, v40, v42, v43\n\t" \
  "s_nop 1\n\t" \
  "v_readfirstlane_b32 vcc_lo, v40\n\t" \
  "s_nop 1\n\t" \
  "s_cmp_ge_u32 vcc_lo, 13\n\t" \
  "s_cbranch_scc0 35b\n\t" \
  "s_branch 36b\n\t" \
  "37:\n\t" \
  "ds_read_b128 v[40:43], v56 offset:80\n\t" \
  "s_waitcnt lgkmcnt(0)\n\t" \
  "v_min_u32 v40, v40, v41\n\t" \
  "v_min3_u32 v40, v40, v42, v43\n\t" \
  "s_nop 1\n\t" \
  "v_readfirstlane_b32 vcc_lo, v40\n\t" \
  "s_nop 1\n\t" \
  "s_cmp_ge_u32 vcc_lo, 16\n\t" \
  "s_cbranch_scc0 37b\n\t" \
  "s_branch 38b\n\t" \
  "39:\n\t" \
  "ds_read_b128 v[40:43], v56 offset:160\n\t" \
  "s_waitcnt lgkmcnt(0)\n\t" \
  "v_min_u32 v40, v40, v41\n\t" \
  "v_min3_u32 v40, v40, v42, v43\n\t" \
  "s_nop 1\n\t" \
  "v_readfirstlane_b32 vcc_lo, v40\n\t" \
  "s_nop 1\n\t" \
  "s_cmp_ge_u32 vcc_lo, 16\n\t" \
  "s_cbranch_scc0 39b\n\t" \
  "s_branch 40b\n\t" \
  "43:\n\t" \
  "s_cmp_eq_u32 %13, 0\n\t" \
  "s_cbranch_scc1 45f\n\t" \
  "s_sleep 1\n\t" \
  "ds_read_b32 v16, v56 offset:96\n\t" \
  "s_waitcnt lgkmcnt(0)\n\t" \
  "v_readfirstlane_b32 vcc_lo, v16\n\t" \
  "s_nop 1\n\t" \
  "s_cmp_ge_u32 vcc_lo, 1\n\t" \
  "s_cbranch_scc0 43b\n\t" \
  "s_branch 44b\n\t" \
  "45:\n\t" \
  "v_bfe_u32 v19, v12, 4, 4\n\t" \
  "v_lshlrev_b32 v19, 2, v19\n\t" \
  "s_mov_b32 s90, 0\n\t" \
  "145:\n\t" \
  "global_load_dword v16, v19, %21 sc1\n\t" \
  "s_add_u32 s90, s90, 1\n\t" \
  "s_waitcnt vmcnt(0)\n\t" \
  "v_cmp_ne_u32 vcc, v55, v16\n\t" \
  "s_mov_b32 s88, 0xf7f7f7f7\n\t" \
  "s_mov_b32 s89, s88\n\t" \
  "s_and_b64 vcc, vcc, s[88:89]\n\t" \
  "s_cbranch_scc0 245f\n\t" \
  "s_sleep 2\n\t" \
  "s_cmp_lt_u32 s90, 0x400000\n\t" \
  "s_cbranch_scc1 145b\n\t" \
  "245:\n\t" \
  "v_mov_b32 v16, 1\n\t" \
  "ds_max_u32 v56, v16 offset:96\n\t" \
  "s_branch 44b\n\t" \
  "46:\n\t" \
  "s_cmp_eq_u32 %13, 0\n\t" \
  "s_cbranch_scc1 48f\n\t" \
  "s_sleep 1\n\t" \
  "ds_read_b32 v16, v56 offset:96\n\t" \
  "s_waitcnt lgkmcnt(0)\n\t" \
  "v_readfirstlane_b32 vcc_lo, v16\n\t" \
  "s_nop 1\n\t" \
  "s_cmp_ge_u32 vcc_lo, 2\n\t" \
  "s_cbranch_scc0 46b\n\t" \
  "s_branch 47b\n\t" \
  "48:\n\t" \
  "v_bfe_u32 v19, v12, 4, 4\n\t" \
  "v_lshlrev_b32 v19, 2, v19\n\t" \
  "s_mov_b32 s90, 0\n\t" \
  "148:\n\t" \
  "global_load_dword v16, v19, %21 sc1\n\t" \
  "s_add_u32 s90, s90, 1\n\t" \
  "s_waitcnt vmcnt(0)\n\t" \
  "v_cmp_ne_u32 vcc, v55, v16\n\t" \
  "s_mov_b32 s88, 0xf7f7f7f7\n\t" \
  "s_mov_b32 s89, s88\n\t" \
  "s_and_b64 vcc, vcc, -1\n\t" \
  "s_cbranch_scc0 248f\n\t" \
  "s_sleep 2\n\t" \
  "s_cmp_lt_u32 s90, 0x400000\n\t" \
  "s_cbranch_scc1 148b\n\t" \
  "248:\n\t" \
  "v_mov_b32 v16, 2\n\t" \
  "ds_max_u32 v56, v16 offset:96\n\t" \
  "s_branch 47b\n\t" \
  "19:\n\t"
#define C_CLOB "v10","v11","v12","v13","v18","v19","v20","v21","v22","v23","v24","v25","v26","v27","v28","v29","v30","v31","v32","v33","v34","v35","v36","v37","v38","v39","v40","v41","v42","v43","v44","v45","v46","v47","v48","v49","v50","v53","v55","v56","v57","v58","v59","v60","v61","v62","v63","v64","v65","v66","v67","v68","v69","v70","v71","v72","v73","v74","v75","v76","v77","v78","v79","v80","v81","v82","v83","v84","v85","v86","v87","v88","v89","v90","v91","v92","v93","v94","v95","v96","v97","v98","v99","v100","v101","v102","v103","v104","v105","v106","v107","v108","v109","v110","v111","v112","v113","v114","v115","v116","v117","v118","v119","v120","v121","v122","v123","v124","v125","v126","v127","v128","v129","v130","v131","v132","v133","v134","v135","v136","v137","v138","v139","v140","v141","v142","v143","v144","v145","v146","v147","v148","v149","v150","v151","v152","v153","v154","v155","v156","v157","v158","v159","v160","v161","v162","v163","v164","v165","v166","v167","v168","v169","v170","v171","v172","v173","v174","v175","v176","v177","v178","v179","v180","v181","v182","v183","v184","v185","v186","v187","v188","v189","v190","v191","v192","v193","v194","v195","v196","v197","v198","v199","v200","v201","v202","v203","v204","v205","v206","v207","v208","v209","v210","v211","v212","v213","v214","v215","v216","v217","v218","v219","v220","v221","v222","v223","v224","v225","v226","v227","v228","v229","v230","v231","v232","v233","v234","v235","v236","v237","v238","v239","v240","v241","v242","v243","v244","v245","v246","v247","v248","v249","v250","v251","v252","v253","v254","v255"

static_assert(KVP<13 && KVP>TGS+1, "");

#define TS(i)
#define TSDUMP()
#define TSB(i)
#define TSR(i)
#define TSH(i)
#define TSDECL()

typedef unsigned long long u64;
typedef u64 u64x8 __attribute__((ext_vector_type(8)));
typedef u64 u64x4 __attribute__((ext_vector_type(4)));
typedef unsigned u32x8 __attribute__((ext_vector_type(8)));
typedef unsigned u32x16 __attribute__((ext_vector_type(16)));
template<class T> __device__ __forceinline__ T sg(T v){
  if constexpr(sizeof(T)==4) return (T)__builtin_amdgcn_readfirstlane((unsigned)v); else return v; }
#define GP(T,v) ((T*)(__attribute__((address_space(1))) T*)(v))
__global__ __launch_bounds__(512,2) __attribute__((aligned(4096))) void fused_kernel(const u16* x_, const u16* wqkv_, const u16* kin_, const u16* vin_, const u16* shk_, const u16* hk_,
    const u16* wo_, const u16* qin_, const u16* sD_, u16* out_, u16* ws_, u16* unused_)
{
#define ST0()
#define ST1()
#define ARGS0() u64x8 ka; u64x4 kb; asm volatile("s_load_dwordx16 %0, %2, 0\n\ts_load_dwordx8 %1, %2, 64" : "=&s"(ka), "=&s"(kb) : "s"(__builtin_amdgcn_kernarg_segment_ptr()) : "memory")
#define ARGS() asm volatile("s_waitcnt lgkmcnt(0)" : "+s"(ka), "+s"(kb) :: "memory"); ST1() \
  const u16 *x=GP(const u16,ka[0]), *wqkv=GP(const u16,ka[1]), *kin=GP(const u16,ka[2]), *vin=GP(const u16,ka[3]), *shk=GP(const u16,ka[4]), *hk=GP(const u16,ka[5]), *wo=GP(const u16,ka[6]), *qin=GP(const u16,ka[7]), *sD=GP(const u16,kb[0]); \
  u16 *out=GP(u16,kb[1]), *o=GP(u16,kb[2]), *xrT=o+(size_t)S*DM, *yT=o+(size_t)2*S*DM; unsigned* flags=(unsigned*)(o+(size_t)6*S*DM); \
  (void)x; (void)wqkv; (void)kin; (void)vin; (void)shk; (void)hk; (void)wo; (void)qin; (void)sD; (void)out; (void)xrT; (void)yT; (void)flags
  ST0()
  __shared__ __attribute__((aligned(1024))) char smem[ALL_LDS];
  const int tid=threadIdx.x;
  const int w8=__builtin_amdgcn_readfirstlane(tid>>6);
  asm volatile("s_cmp_lg_u32 %0, 7\n\ts_cbranch_scc1 1f\n\ts_lshr_b32 vcc_lo, %2, 3\n\ts_mul_i32 vcc_lo, vcc_lo, %3\n\tv_and_b32 v254, 15, %1\n\tv_lshl_add_u32 v254, v254, 7, vcc_lo\n\ts_getpc_b64 vcc\n\tglobal_load_dword v255, v254, vcc\n1:"
      :: "s"(w8), "v"(tid), "s"(blockIdx.x), "n"(CODE_ST) : "memory", "scc", "vcc", "v254", "v255");
  LDS_AS unsigned* const ct=(LDS_AS unsigned*)(LDS_AS char*)(smem+CT_OFF);
  volatile LDS_AS unsigned* const vct=ct;
  auto all4=[&](int i)->bool{ u32x4 v=*(volatile LDS_AS u32x4*)(ct+i); return __builtin_amdgcn_readfirstlane(v[0]&v[1]&v[2]&v[3])!=0; };
#define ROTF(b) ((4+4*xr+4*xc+8*(b))&15)
#define BLOCK() int bid=blockIdx.x, w=w8&3; asm volatile("" : "+s"(bid), "+s"(w)); __builtin_assume(bid>=0 && bid<256 && w>=0 && w<4); \
  const int xcd=bid&7, idx=bid>>3, ra=idx&3, rbb=idx>>2, xr=xcd>>1, xc=xcd&1, rb=xr*4+ra, hp=xc*8+rbb, s0=rb*128, tm=rb, tn=hp^TNX; (void)s0; \
  auto korder=[&]()->unsigned long long{ const int r=KR; const unsigned long long N=0xfedca987654210ull, m=(1ull<<56)-1; const int sh=4*(r>=14? r-14 : r); \
    return (((N>>sh)|(N<<(56-sh)))&m) | ((idx&4)? 0xb3ull<<56 : 0x3bull<<56); }

  if(__builtin_expect(w8>=4,0)){
  BLOCK(); constexpr int WB=4; (void)WB;
  const int hd=w>>1, par=w&1;
  __builtin_amdgcn_s_setprio(CPRIO);
  {
    ARGS0(); LANES();
    if(w==0){ if(l<44) ct[l]=0u; if(idx==0) *(u32x4*)(smem+AUX+l*16)=u32x4{0,0,0,0}; }
    unsigned a0b=fr*128+((g^(fr&7))*16);
    const int r0=8*g+tq;
    unsigned bbb=r0*128+(((2*hd)^wswz(r0))*32)+8*tp;
    unsigned kA=AUX+(fr^3)*KVP_+w*64+(g&1)*16-(g>>1)*256, kB=AUX+(20+4*g-tq)*KVP_+w*64+8*tp;
    const int cch=127-4*idx-g;
    unsigned kvo=(xcd*32+8*w+tq)*16384+(cch>>4)*2048+(cch&15)*64+((tp^((cch>>2)&3))*16);
    unsigned sho=(xcd*128+32*w+fr)*8; const unsigned ctl=CT_OFF+4*w;
    u32x4 sel;
    { const unsigned b2=(g>>1)? 0x0302u : 0x0100u, sv=(fr&1)? (b2<<16|0x0c0cu) : (0x0c0c0000u|b2);
      #pragma unroll
      for(int i=0;i<4;i++) sel[i]= (fr>>1)==(g&1)*4+i ? sv : 0x0c0c0c0cu; }
    unsigned msk=0, lx=w*1024+l*16;
    #pragma unroll
    for(int e=0;e<4;e++){ const int d=4*g+e-fr; const unsigned pv=d<9, hv=par? pv : (unsigned)(d<1 || 4*g+e>7);
      msk|=((unsigned)(d+7)>7u?1u:0u)<<e | pv<<(4+e) | hv<<(8+e) | (d<=0?1u:0u)<<(12+e); }
    msk|=(unsigned)(g? l-16 : 48+((l-1)&15))<<26;
    const unsigned hb=__builtin_amdgcn_readfirstlane(rb==0 ? 0xcfa8965fu : 0u);
    unsigned ap=AUX+w*CV_W+(tq&1)*(CV_CP-4)+8+32*g+4*tq+8*tp;
    u32x4 bp;
    #pragma unroll
    for(int b=0;b<4;b++){ const int c=15+4*b-fr+g; bp[b]=c*64+((w^((c>>2)&3))*16); }
    asm volatile("" : "+v"(a0b), "+v"(bbb), "+v"(kA), "+v"(kB), "+v"(kvo), "+v"(sho), "+v"(msk), "+v"(lx), "+v"(ap), "+v"(bp), "+v"(sel));
    ARGS();
    const u16* ow=o+((size_t)(rb*16+hp)*16+par*2+hd)*512+2048;
    asm volatile("" : "+s"(shk));
    asm volatile(C_ASM : "+{v52}"(a0b), "+{v54}"(bbb), "+{v0}"(kA), "+{v1}"(kB), "+{v2}"(kvo), "+{v51}"(sho), "+{v3}"(msk), "+{v4}"(lx), "+{v5}"(ap), "+{v[6:9]}"(bp), "+{v[14:17]}"(sel)
       : "s"(shk), "s"(ctl), "s"(w), "s"(ow), "s"(hb), "s"(0.17677669529663687f*1.44269504088896341f), "{s[92:93]}"(o+(size_t)tm*16*8192), "s"(korder()),
         "s"(flags+FL_O+rb*512+hp), "s"(AUX+w*(CV_W+64)), "s"(flags+FL_O+(tm*16+tn)*32),
         "s"(out+(size_t)(tm*128+w*32+1)*2048+1024+tn*64), "s"(w<2? 16384+w*4096 : AUX+4352+w*CV_W), "s"(xrT), "s"(flags+FL_KV+xcd*1024+idx) : "memory", "scc", "vcc", "s68", "s69", "s70", "s71", "s75", "s76", "s77", "s88", "s89", "s90", "s91",
       C_CLOB);
    TSDECL();
    TS(20); TSDUMP();
  }
  return;
  }
  {
    ARGS0(); BLOCK(); constexpr int WB=0; (void)WB; LANES(); TSDECL();
    const unsigned vx=(l>>3)*4096+(((l&7)^(l>>3))*16), vxs=vx+(s0+32*w)*2048, vw=(l>>3)*6144+((((l&7)>>1)^((l>>4)&1))*32)+(l&1)*16+hp*128+w*98304, vw1=(vw^64)+49152-1024;
    u32x8 va; u32x2 vb; unsigned vsp;
    #pragma unroll
    for(int i=0;i<6;i++) va[i]=((i&1)? vw1 : vw)+(i>>1)*2048;
    va[6]=vxs; va[7]=vxs+32768-1024; vb[0]=vxs+2048; vb[1]=vxs+2048+32768-1024;
    vsp=vx+(rb? s0-16+w : 0)*2048;
    const unsigned sw1=w*1024, sw2=w*2048;
    unsigned rot=ROTF(rbb);
    asm volatile("" : "+v"(va), "+v"(vb), "+v"(vsp));
    asm volatile("" : "+s"(rot));
    unsigned vt=(w&1)? (xr*512+idx*16+(l>>1))*2048+(l&1)*1024 : (2*idx+(l&1))*6144+((l>>1)&3)*2048+xc*1024;
    const int kt0=idx*64; int kvs=KS0+KVSEL;
    unsigned kvl=(kt0+16*w+g)*2048+xcd*256+fr*16, l16=l*16;
    const bool hl= w==0 && kt0;
    unsigned c46=sg(w<2? 128u : w==2? 393216u : 0u), c49=sg(w<2? sw1 : (unsigned)ALL_LDS-96), c64=sg(hl? 8192u : 0u), c68=sg(hl? AUX : ALL_LDS-96);
    unsigned long long c50= w<2? ~0ull : w==2? 63ull : 1ull, c66= hl? ~0ull : 1ull;
    asm volatile("" : "+v"(vt), "+v"(kvl), "+v"(l16));
    asm volatile("" : "+s"(c46), "+s"(c49), "+s"(c64), "+s"(c68), "+s"(c50), "+s"(c66), "+s"(kvs));
    ARGS();
    TSB(16);
    asm volatile(H0_ASM : "+{s[40:41]}"(x), "+{s[42:43]}"(wqkv) : "{v[0:7]}"(va), "{v[8:9]}"(vb), "{v10}"(vsp), "{s52}"(rot), "{s53}"(sw2), "{s59}"(w)
      : "memory", "scc", "s76", "s77", "s78", "s79", "s82", "s83");
    const u16* spb= w==2? wqkv : x;
    const int hl3=(3-(int)((size_t)spb>>7))&7, roth=ROTF(hl3), xs=((hl3-ROTF(0))&7)-LEAD;
    unsigned i11=sg((w==2? roth+LEAD+1 : rot+1)&15), sbo, sst;
    if(w>=2){
      vsp=vt+hl3*128;
      touch(spb, vsp+((roth+(l>>3))&15)*c46, w==2? 0x3f3f3f3f3f3f3f3full>>(56-8*LEAD) : xs<=1? 0xffffffffull : 1ull);
    }
    TSB(17);
    unsigned gof=(((bid>>4)*128+64*(w&1)+4*(w>>1)+16*((l>>5)&1)+((l>>3)&3))*DM+(bid&15)*64)*2+(l16&127);
    unsigned wv[2];
    wv[0]=(((l>>5)*16+w*4+((l>>3)&3))*DM+tn*64)*2+((l16&127)^((l&16)*2)^((w&1)*64)); wv[1]=wv[0]+32*DM*2;
    unsigned zpa=(w>>1)*9216+(w&1)*512+l16, hda=AUX+w*CV_W+(l&1)*CV_CP+(l>>1)*16, yra=AUX+tq*(CV_W+64)+(g>>1)*128+(g&1)*32+tp*8+w*1024;
    constexpr unsigned FLB=12*S*DM;
    unsigned vc=(w&1)? FLB+(g? (1024+(g-1)*8192+(g==2? tm*16+tn : bid)*32)*4 : 0) : idx*CODE_ST+fr*128u;
    unsigned kfo=FLB+(FL_KV+bid*32+(l&31))*4, ofo= w==0? FLB+(FL_O+(tm*16+tn)*32+fr)*4 : FLB;
    asm volatile("" : "+v"(kfo), "+v"(ofo), "+v"(gof), "+v"(wv[0]), "+v"(wv[1]), "+v"(hda), "+v"(yra), "+v"(zpa), "+v"(vc));
    unsigned long long kt=korder(); asm volatile("" : "+s"(kt));
#define HDBG_O
#define HDBG_I
#define H_IN "{v[0:7]}"(va), "{v[8:9]}"(vb), "{v10}"(vsp), "{v11}"(kvl), "{s[40:41]}"(x), "{s[42:43]}"(wqkv), "{s[44:45]}"(spb), "{s46}"(c46), "{s49}"(c49), "{s[50:51]}"(c50), "{s53}"(sw2), \
      "{s58}"(kvs), "{s59}"(w), "{s[60:61]}"(kin), "{s[62:63]}"(vin), "{s64}"(c64), "{s[66:67]}"(c66), "{s68}"(c68) HDBG_I
#define H_CLOB "memory", "scc", "vcc", "s76", "s77", "s78", "s79", "s80", "s81", "s82", "s83", "s84", "s85", "v20", "v21", "v22", "v253", "v254", "v255"
    asm volatile(H1_ASM : "+{s47}"(i11), "+{s52}"(rot), "={s54}"(sbo), "={s55}"(sst) HDBG_O : H_IN : H_CLOB);
    const u16* hrow=hk+(size_t)(bid*4+w)*S;
    u32x4 h0; unsigned dvu;
    asm volatile("s_load_dwordx4 %0, %2, 0\n\ts_load_dword %1, %3, %4" : "=&s"(h0), "=&s"(dvu) : "s"(hrow), "s"(sD), "s"(bid*8+(w&2)*2) : "memory");
    unsigned long long pc; asm volatile("s_getpc_b64 %0" : "=s"(pc)); pc&=~4095ull;
    __builtin_amdgcn_s_setprio(HP1);
    asm volatile(H_ASM : "+{s47}"(i11), "+{s52}"(rot), "+{s54}"(sbo), "+{s55}"(sst) HDBG_O
      : H_IN, "{v14}"(vc), "{v16}"(kfo), "{s56}"(sg(3u<<(kvs-1) | 1u<<15)),
        "{s57}"(sg(((w&1)? (1u<<TGS | 1u<<KVP | 1u<<14 | ((1u<<(xs+8)>>8)&~3u)) : 1u<<CPS)&~(unsigned)(c50>>32))), "{s[70:71]}"(o), "{s[72:73]}"(pc) : H_CLOB);
    asm volatile("s_waitcnt lgkmcnt(0)" : "+s"(h0), "+s"(dvu) :: "memory");
    u32x4 qv[4], wr[6];
    BAR(); TSB(14);
    __builtin_amdgcn_s_setprio(HP2);
    { const unsigned fa=AUX+w*CV_W+32;
      asm volatile("s_mov_b64 exec, -2\n\ts_add_u32 m0, %3, %4\n\ts_nop 0\n\tglobal_load_lds_dwordx4 %0, %1" LDPOL "\n\ts_mov_b32 m0, %3\n\ts_nop 0\n\tglobal_load_lds_dwordx4 %0, %2" LDPOL "\n\ts_mov_b64 exec, -1"
        :: "v"(l16), "s"(hrow), "s"(hrow-2), "s"(fa), "n"(CV_CP) : "memory", "scc");
      #pragma unroll
      for(int i=1;i<4;i++){ gldsn(hrow+i*512, l16, fa, CV_CP+i*1024); gldsn(hrow-2+i*512, l16, fa, i*1024); } }
    const unsigned tag=__builtin_amdgcn_readfirstlane(vct[26]); const bool kvok=__builtin_amdgcn_readfirstlane(vct[5])!=0;
    u32x4 hv;
    { const unsigned d0=(h0[0]&0xffff0000u)|(pack2(bflo(h0[0])+((w&1)? bfhi(dvu) : bflo(dvu)),0.f)&0xffffu);
      hv= l==4? u32x4{0,d0,h0[1],h0[2]} : l==5? u32x4{d0,h0[1],h0[2],h0[3]} : u32x4{0,0,0,0}; }
    asm volatile("s_mov_b64 exec, 63\n\tds_write_b128 %0, %1\n\ts_mov_b64 exec, %3\n\tds_write_b128 %2, %1 offset:7680\n\ts_mov_b64 exec, -1"
      :: "v"(hda), "v"(hv), "v"(zpa), "s"(0xffffffff00000000ull) : "memory");
#define QLD() asm volatile("global_load_dwordx4 %0, %4, %5" LDPOL "\n\tglobal_load_dwordx4 %1, %4, %6" LDPOL "\n\tglobal_load_dwordx4 %2, %4, %7" LDPOL "\n\tglobal_load_dwordx4 %3, %4, %8" LDPOL \
        : "=&v"(qv[0]), "=&v"(qv[1]), "=&v"(qv[2]), "=&v"(qv[3]) : "v"(gof), "s"(qin), "s"(qin+8*DM), "s"(qin+32*DM), "s"(qin+40*DM) : "memory")
    auto kvt=[&](){
      #pragma unroll
      for(int i=0;i<4;i++) gldsc(xrT+(size_t)bid*8192+w*2048+i*512, l16, sw2, (i&1)*9216+(i>>1)*1024); };
    WAITV(8);
    vct[32+w]=1u;
    TSH(6);
    if(!kvok) wait_flag((const unsigned*)((const char*)o+kfo), tag);
    kvt();
    TSH(7);
    auto wos=[&](int st){ const u16* b=wo+(size_t)((kt>>(4*st))&15)*64*DM; glds(b,wv[0],sw1,WOB+st*8192); glds(b,wv[1],sw1,WOB+st*8192+4096); };
    wos(0); wos(1);
    if(QP==0) QLD();
    WAITV(QP==0? 8:4); WAITL();
    vct[16+w]=1u;
    TSH(8);
    __builtin_amdgcn_s_setprio(HP4);
    if(QP==1) QLD();
    while(!all4(36)) __builtin_amdgcn_s_sleep(1);
    #pragma unroll
    for(int st=2;st<7;st++){ wos(st); WAITV(W1); }
    while(!all4(0)) __builtin_amdgcn_s_sleep(1);
    #pragma unroll
    for(int st=7;st<13;st++){ wos(st); WAITV(W2); }
    #pragma unroll
    for(int st=0;st<3;st++){
      asm volatile("global_load_dwordx4 %0, %2, %4\n\tglobal_load_dwordx4 %1, %3, %4" : "=&v"(wr[2*st]), "=&v"(wr[2*st+1]) : "v"(wv[0]), "v"(wv[1]), "s"(wo+(size_t)((kt>>(52+4*st))&15)*64*DM) : "memory");
      WAITV(W3); }
    unsigned op;
    asm volatile("global_load_dword %0, %1, %2 sc1" : "=v"(op) : "v"(ofo), "s"(o) : "memory");
    if(QP==2) QLD();
    WAITV(QP==2? 11:7); vct[20+w]=13u;
    TSH(9);
    unsigned oseen=0;
    auto olevel=[&](unsigned v){ const unsigned long long ne=__ballot(v!=tag); const unsigned lv= ne==0 ? 2u : (ne&0xf7f7f7f7f7f7f7f7ull)==0 ? 1u : 0u;
      if(lv>oseen){ if(oseen==0){ TSH(10); } oseen=lv; if(l==0) __hip_atomic_fetch_max(ct+24, lv, __ATOMIC_RELAXED, __HIP_MEMORY_SCOPE_WORKGROUP); } };
    asm volatile("s_waitcnt vmcnt(0)" : "+v"(wr[0]), "+v"(wr[1]), "+v"(wr[2]), "+v"(wr[3]), "+v"(wr[4]), "+v"(wr[5]),
                                        "+v"(qv[0]), "+v"(qv[1]), "+v"(qv[2]), "+v"(qv[3]), "+v"(op) :: "memory");
    if(w==0) olevel(op);
    __builtin_amdgcn_s_setprio(HP3);
    bool gen= !(bid==0 && w==1);
    auto trygen=[&](){
      unsigned v[4];
      #pragma unroll
      for(int i=0;i<4;i++) v[i]=__hip_atomic_load(flags+FL_KV+(i*2+(l>>5))*1024+(l&31), __ATOMIC_RELAXED, __HIP_MEMORY_SCOPE_AGENT);
      if(__all(v[0]==tag && v[1]==tag && v[2]==tag && v[3]==tag)){ if(l==0) st4c(flags, tag); gen=true; } };
    if(!gen) trygen();
    float qf[32];
    #pragma unroll
    for(int i=0;i<16;i++){ qf[2*i]=bflo(qv[i>>2][i&3]); qf[2*i+1]=bfhi(qv[i>>2][i&3]); }
    #pragma unroll
    for(int i=0;i<32;i+=8) asm volatile("" : "+v"(qf[i]), "+v"(qf[i+1]), "+v"(qf[i+2]), "+v"(qf[i+3]), "+v"(qf[i+4]), "+v"(qf[i+5]), "+v"(qf[i+6]), "+v"(qf[i+7]));
    while(!all4(8)){
      if(w==0 && oseen<2) olevel(__hip_atomic_load((const unsigned*)((const char*)o+ofo), __ATOMIC_RELAXED, __HIP_MEMORY_SCOPE_AGENT));
      __builtin_amdgcn_s_sleep(OPOLLS);
    }
    TSH(11);
    __builtin_amdgcn_s_setprio(3);
    {
      const char* yb=smem+yra;
      s16x4 ya[4], yc[4];
      #pragma unroll
      for(int i=0;i<4;i++){ ya[i]=trread(yb+(i>>1)*512+(i&1)*64); yc[i]=trread(yb+(i>>1)*512+(i&1)*64+256); }
      #pragma unroll
      for(int i=0;i<4;i++) if(NOY!=1) st16c(yT+((size_t)(w*4+i)*256+bid)*512, l16, __builtin_bit_cast(u32x4,__builtin_shufflevector(ya[i],yc[i],0,1,2,3,4,5,6,7)));
    }
    #pragma unroll
    for(int i=0;i<6;i++) *(u32x4*)(smem+(i<4? i*4096 : AUX+4352+(i&1)*CV_W)+w*1024+l16)=wr[i];
    WAITL();
    vct[20+w]=16u;
    TSH(12);
    if(NOY){ for(int sp_=0; !gen && sp_<(1<<22); sp_++) trygen(); TS(20); TSDUMP(); return; }
    unsigned po=gof+(gof&~2047u);
    const u16* p0=yT+((size_t)(bid>>4)*256+(bid&15)*16)*512;
    unsigned y0=g*1024+w*256+((fr^(g&2))*16);
    asm volatile("" : "+v"(y0), "+v"(po));
    WAITV(0);
    if(l<4) st4c(flags+FL_Y+((w*4+l)*16+(bid>>4))*32+(bid&15), tag);
    TSH(13);
    if(w==0){ int sp_=0; while(oseen<2 && vct[24]<2u && ++sp_<(1<<22)){ olevel(__hip_atomic_load((const unsigned*)((const char*)o+ofo), __ATOMIC_RELAXED, __HIP_MEMORY_SCOPE_AGENT)); __builtin_amdgcn_s_sleep(8); } }
    if(w==0){
      const unsigned* fp=flags+FL_Y+bid*32+(l&15); int spins=0;
      while(vct[27]==0u) __builtin_amdgcn_s_sleep(2);
      while(true){
        unsigned v=__hip_atomic_load(fp, __ATOMIC_RELAXED, __HIP_MEMORY_SCOPE_AGENT);
        if(__all(v==tag) || ++spins>(1<<20)) break;
        __builtin_amdgcn_s_sleep(POLLS);
      }
      { u32x4 v; do{ v=*(volatile LDS_AS u32x4*)(ct+20); }while(__builtin_amdgcn_readfirstlane(v[0]&v[1]&v[2]&v[3]&16u)==0); }
      vct[25]=1u;
    } else { while(vct[25]==0u) __builtin_amdgcn_s_sleep(2); }
    TSH(14);
    {
      u32x4 yv[4];
      #pragma unroll
      for(int i=0;i<4;i++) gldsc(p0+i*2048, y0^(i*64), w*CV_W, AUX+i*1024);
      const char* ys=smem+AUX+w*CV_W+(l&7)*512;
      const int yi=((l>>3)^((l&7)*2))*16;
      WAITV(0);
      yv[0]=*(const u32x4*)(ys+yi); yv[1]=*(const u32x4*)(ys+yi+256); yv[2]=*(const u32x4*)(ys+(yi^128)); yv[3]=*(const u32x4*)(ys+(yi^128)+256);
      TSH(15);
      #pragma unroll
      for(int r=0;r<4;r++){
        u32x4 ov;
        #pragma unroll
        for(int e=0;e<4;e++){ const unsigned y2=yv[(r>>1)*2+(e>>1)][(r&1)*2+(e&1)]; ov[e]=pack2(qf[r*8+2*e]*bflo(y2), qf[r*8+2*e+1]*bfhi(y2)); }
        stos(out+((r&1)*8+(r>>1)*32)*2048, po, ov);
      }
    }
    for(int sp_=0; !gen && sp_<(1<<22); sp_++) trygen();
    TS(20); TSDUMP();
  }

}

extern "C" void kernel_launch(void* const* d_in, const int* in_sizes, int n_in, void* d_out, int out_size, void* d_ws, size_t ws_size, hipStream_t stream){
  const u16* q=(const u16*)d_in[0]; const u16* k=(const u16*)d_in[1]; const u16* v=(const u16*)d_in[2];
  const u16* shk=(const u16*)d_in[3]; const u16* hk=(const u16*)d_in[4]; const u16* sD=(const u16*)d_in[5];
  const u16* x=(const u16*)d_in[6]; const u16* wqkv=(const u16*)d_in[7]; const u16* wo=(const u16*)d_in[8];
  fused_kernel<<<256,512,0,stream>>>(x,wqkv,k,v,shk,hk,wo,q,sD,(u16*)d_out,(u16*)d_ws,(u16*)d_ws);
}
```

```cpp
#include <hip/hip_runtime.h>
#include <hip/hip_bf16.h>
#include <stdint.h>

typedef unsigned short u16;
typedef short s16x4 __attribute__((ext_vector_type(4)));
typedef float f32x32 __attribute__((ext_vector_type(32)));
typedef unsigned u32x4 __attribute__((ext_vector_type(4)));
typedef unsigned u32x2 __attribute__((ext_vector_type(2)));
#define LDS_AS __attribute__((address_space(3)))

__device__ __forceinline__ float bflo(unsigned x){ return __uint_as_float(x<<16); }
__device__ __forceinline__ float bfhi(unsigned x){ return __uint_as_float(x&0xffff0000u); }
typedef float f32x2 __attribute__((ext_vector_type(2)));
typedef __bf16 bf16x2_t __attribute__((ext_vector_type(2)));
__device__ __forceinline__ unsigned pack2(float lo, float hi){ return __builtin_bit_cast(unsigned, __builtin_convertvector(f32x2{lo,hi}, bf16x2_t)); }

#define GLDS(name,pol) __device__ __forceinline__ void name(const void* sb, unsigned voff, unsigned ldsb, int imm){ \
  asm volatile("s_add_u32 m0, %2, %3\n\ts_nop 0\n\tglobal_load_lds_dwordx4 %0, %1" pol :: "v"(voff), "s"(sb), "s"(ldsb), "n"(imm) : "memory", "scc"); }
#define LDPOL " nt"
GLDS(glds,"") GLDS(gldsn,LDPOL) GLDS(gldsc," sc1")
#define PACE 63
__device__ __forceinline__ void gldsp(const void* sb, unsigned voff, unsigned ldsb, int imm){
  asm volatile("s_add_u32 m0, %2, %3\n\ts_waitcnt vmcnt(%4)\n\tglobal_load_lds_dwordx4 %0, %1" :: "v"(voff), "s"(sb), "s"(ldsb), "n"(imm), "n"(PACE) : "memory", "scc"); }
__device__ __forceinline__ void touch(const void* sb, unsigned voff, unsigned long long mask){
  asm volatile("s_mov_b64 exec, %2\n\tglobal_load_dword v255, %0, %1\n\ts_mov_b64 exec, -1" :: "v"(voff), "s"(sb), "s"(mask) : "memory", "v255"); }
__device__ __forceinline__ void st16c(void* sb, unsigned vo, u32x4 v){ asm volatile("global_store_dwordx4 %0, %1, %2 sc1\n\ts_nop 1" :: "v"(vo), "v"(v), "s"(sb) : "memory"); }
#define OUTPOL " sc1 nt"
__device__ __forceinline__ void stos(void* sb, unsigned vo, u32x4 v){ asm volatile("global_store_dwordx4 %0, %1, %2" OUTPOL "\n\ts_nop 1" :: "v"(vo), "v"(v), "s"(sb) : "memory"); }
__device__ __forceinline__ void stoa(void* p, u32x4 v){ asm volatile("global_store_dwordx4 %0, %1, off" OUTPOL "\n\ts_nop 1" :: "v"(p), "v"(v) : "memory"); }
__device__ __forceinline__ void st4c(void* p, unsigned v){ asm volatile("global_store_dword %0, %1, off sc1" :: "v"(p), "v"(v) : "memory"); }
__device__ __forceinline__ void wait_flag(const unsigned* p, unsigned tag){
  int spins=0;
  while(true){
    unsigned v=__hip_atomic_load(p, __ATOMIC_RELAXED, __HIP_MEMORY_SCOPE_AGENT);
    if(__all(v==tag)) break;
    __builtin_amdgcn_s_sleep(2);
    if(++spins>(1<<22)) break;
  }
}
#define WAITV(n) asm volatile("s_waitcnt vmcnt(%0)"::"n"(n):"memory")
#define WAITL() asm volatile("s_waitcnt lgkmcnt(0)":::"memory")
#define BAR() do{ asm volatile("s_waitcnt lgkmcnt(0)":::"memory"); __builtin_amdgcn_s_barrier(); asm volatile("":::"memory"); }while(0)
#define LANES() int l; asm volatile("v_mbcnt_lo_u32_b32 %0, -1, 0\n\tv_mbcnt_hi_u32_b32 %0, -1, %0" : "=v"(l)); __builtin_assume(l>=0 && l<64); const int g=l>>4, fr=l&15, tq=(l&15)>>2, tp=l&3; (void)g; (void)fr; (void)tq; (void)tp;

__device__ __forceinline__ s16x4 trread(const char* p){ return __builtin_amdgcn_ds_read_tr16_b64_v4i16((LDS_AS s16x4*)p); }
__device__ __forceinline__ int wswz(int row){ return ((row>>1)&1) | (((row>>3)&1)<<1); }
__device__ __forceinline__ u32x4 widen(u32x2 a, u32x2 b){
  auto r0=__builtin_amdgcn_permlane16_swap(a[0],b[0],false,false);
  auto r1=__builtin_amdgcn_permlane16_swap(a[1],b[1],false,false);
  return u32x4{r0[0],r1[0],r0[1],r1[1]};
}

constexpr int S=2048, DM=1024;
constexpr int K1_XB=144*128, K1_WB=3*64*128, K1_ST=K1_XB+K1_WB;
constexpr int AUX=3*K1_ST, KVP_=1040;
constexpr int WOB=22528, CV_W=8448, CV_CP=4224;
constexpr int CT_OFF=AUX+33*KVP_;
constexpr int ALL_LDS=CT_OFF+176+96;
constexpr int FL_KV=1024, FL_O=FL_KV+8192, FL_Y=FL_O+8192;

#define CPRIO 2
#define CODE_ST 1590
#define LEAD 3
#define HP3 3
#define HP2 3
#define HP4 0
#define QP 1
#define TNX 1
#define HWS 99
#define HP1 0
#define HUNR 15
#define NE 2
#define WOC 0
#define W0 6
#define OPD 8
#define WT0 99
#define W1 2
#define W2 2
#define W3 4
#define OPOLLS 2
#define POLLS 3
#define HPRE 0
#define CPS2 99
#define KR ((((idx>>2)*7)>>2)+(((idx&3)*7)>>1))
#define NOY 0
#define YLDS 1
#define OLDS 0
#define KVSEL (rbb==3? 0 : idx%KSN)
#define KS0 1
#define KSN 3
#define KST 9
#define KVP 5
#define TGS 2
#define CPS 13
#define H0_ASM \
  "s_mul_i32 s76, s52, 0x60000\n\t" \
  "s_add_u32 s76, s42, s76\n\t" \
  "s_addc_u32 s77, s43, 0\n\t" \
  "s_add_u32 s82, s53, 86016\n\t" \
  "s_add_u32 m0, s82, 18432\n\t" \
  "s_lshl_b32 s78, s52, 7\n\t" \
  "global_load_lds_dwordx4 v0, s[76:77]\n\t" \
  "global_load_lds_dwordx4 v1, s[76:77] offset:1024\n\t" \
  "s_add_u32 s78, s40, s78\n\t" \
  "s_addc_u32 s79, s41, 0\n\t" \
  "s_add_u32 m0, s82, 2048\n\t" \
  "s_nop 0\n\t" \
  "global_load_lds_dwordx4 v6, s[78:79]\n\t" \
  "global_load_lds_dwordx4 v7, s[78:79] offset:1024\n\t" \
  "s_add_u32 m0, s82, 26624\n\t" \
  "s_nop 0\n\t" \
  "global_load_lds_dwordx4 v2, s[76:77]\n\t" \
  "global_load_lds_dwordx4 v3, s[76:77] offset:1024\n\t" \
  "s_add_u32 m0, s82, 10240\n\t" \
  "s_nop 0\n\t" \
  "global_load_lds_dwordx4 v8, s[78:79]\n\t" \
  "global_load_lds_dwordx4 v9, s[78:79] offset:1024\n\t" \
  "s_add_u32 m0, s82, 34816\n\t" \
  "s_nop 0\n\t" \
  "global_load_lds_dwordx4 v4, s[76:77]\n\t" \
  "global_load_lds_dwordx4 v5, s[76:77] offset:1024\n\t" \
  "s_cmp_gt_u32 s59, 1\n\t" \
  "s_cbranch_scc1 1f\n\t" \
  "s_lshr_b32 s83, s53, 1\n\t" \
  "s_add_u32 m0, s83, 86016\n\t" \
  "s_nop 0\n\t" \
  "global_load_lds_dwordx4 v10, s[78:79]\n\t" \
  "1:\n\t"
#define H1_ASM \
  "s_mov_b32 s55, 0\n\t" \
  "s_mov_b32 s54, 0\n\t" \
  "s_add_u32 s52, s52, 1\n\t" \
  "s_and_b32 s52, s52, 15\n\t" \
  "s_cmp_eq_u32 s58, 1\n\t" \
  "s_cbranch_scc1 1f\n\t" \
  "s_mul_i32 s76, s52, 0x60000\n\t" \
  "s_add_u32 s76, s42, s76\n\t" \
  "s_addc_u32 s77, s43, 0\n\t" \
  "s_lshl_b32 s78, s52, 7\n\t" \
  "s_add_u32 s78, s40, s78\n\t" \
  "s_addc_u32 s79, s41, 0\n\t" \
  "s_mul_i32 s80, s47, s46\n\t" \
  "s_add_u32 s80, s44, s80\n\t" \
  "s_addc_u32 s81, s45, 0\n\t" \
  "s_mov_b32 s82, s53\n\t" \
  "s_add_u32 m0, s82, 18432\n\t" \
  "s_add_u32 s52, s52, 1\n\t" \
  "global_load_lds_dwordx4 v0, s[76:77]\n\t" \
  "global_load_lds_dwordx4 v1, s[76:77] offset:1024\n\t" \
  "s_add_u32 m0, s82, 2048\n\t" \
  "s_and_b32 s52, s52, 15\n\t" \
  "global_load_lds_dwordx4 v6, s[78:79]\n\t" \
  "global_load_lds_dwordx4 v7, s[78:79] offset:1024\n\t" \
  "s_add_u32 m0, s82, 26624\n\t" \
  "s_add_u32 s47, s47, 1\n\t" \
  "global_load_lds_dwordx4 v2, s[76:77]\n\t" \
  "global_load_lds_dwordx4 v3, s[76:77] offset:1024\n\t" \
  "s_add_u32 m0, s82, 10240\n\t" \
  "s_and_b32 s47, s47, 15\n\t" \
  "global_load_lds_dwordx4 v8, s[78:79]\n\t" \
  "global_load_lds_dwordx4 v9, s[78:79] offset:1024\n\t" \
  "s_add_u32 m0, s82, 34816\n\t" \
  "s_and_b32 s83, s54, s51\n\t" \
  "global_load_lds_dwordx4 v4, s[76:77]\n\t" \
  "global_load_lds_dwordx4 v5, s[76:77] offset:1024\n\t" \
  "s_add_u32 m0, s83, s49\n\t" \
  "s_mov_b64 exec, s[50:51]\n\t" \
  "global_load_lds_dwordx4 v10, s[80:81]\n\t" \
  "s_mov_b64 exec, -1\n\t" \
  "s_mov_b32 s55, 1\n\t" \
  "s_mov_b32 s54, 43008\n\t" \
  "s_waitcnt vmcnt(11)\n\t" \
  "s_branch 2f\n\t" \
  "1:\n\t" \
  "s_mul_i32 s83, s59, 4160\n\t" \
  "v_add_u32 v20, 7152, v11\n\t" \
  "v_add_u32 v21, 14304, v11\n\t" \
  "v_add_u32 v22, 21456, v11\n\t" \
  "s_add_u32 m0, s83, 130064\n\t" \
  "s_nop 0\n\t" \
  "global_load_lds_dwordx4 v11, s[60:61]" LDPOL "\n\t" \
  "global_load_lds_dwordx4 v20, s[60:61] offset:1040" LDPOL "\n\t" \
  "global_load_lds_dwordx4 v21, s[60:61] offset:2080" LDPOL "\n\t" \
  "global_load_lds_dwordx4 v22, s[60:61] offset:3120" LDPOL "\n\t" \
  "s_add_u32 m0, s83, 146704\n\t" \
  "s_nop 0\n\t" \
  "global_load_lds_dwordx4 v11, s[62:63]" LDPOL "\n\t" \
  "global_load_lds_dwordx4 v20, s[62:63] offset:1040" LDPOL "\n\t" \
  "global_load_lds_dwordx4 v21, s[62:63] offset:2080" LDPOL "\n\t" \
  "global_load_lds_dwordx4 v22, s[62:63] offset:3120" LDPOL "\n\t" \
  "v_subrev_u32 v20, s64, v11\n\t" \
  "s_mov_b32 m0, s68\n\t" \
  "s_mov_b64 exec, s[66:67]\n\t" \
  "global_load_lds_dwordx4 v20, s[60:61]" LDPOL "\n\t" \
  "s_mov_b64 exec, -1\n\t" \
  "s_waitcnt vmcnt(9)\n\t" \
  "2:\n\t" \
  "s_waitcnt lgkmcnt(0)\n\t" \
  "s_barrier\n\t"
#define H_ASM \
  "s_mul_i32 s76, s52, 0x60000\n\t" \
  "s_add_u32 s76, s42, s76\n\t" \
  "s_addc_u32 s77, s43, 0\n\t" \
  "s_lshl_b32 s78, s52, 7\n\t" \
  "s_add_u32 s78, s40, s78\n\t" \
  "s_addc_u32 s79, s41, 0\n\t" \
  "s_mul_i32 s80, s47, s46\n\t" \
  "s_add_u32 s80, s44, s80\n\t" \
  "s_addc_u32 s81, s45, 0\n\t" \
  "s_add_u32 s82, s54, s53\n\t" \
  "s_cmp_eq_u32 s58, 2\n\t" \
  "s_cbranch_scc1 5f\n\t" \
  "s_branch 3f\n\t" \
  "1:\n\t" \
  "s_waitcnt vmcnt(11)\n\t" \
  "2:\n\t" \
  "s_barrier\n\t" \
  "3:\n\t" \
  "s_add_u32 s55, s55, 1\n\t" \
  "s_add_u32 m0, s82, 18432\n\t" \
  "s_add_u32 s52, s52, 1\n\t" \
  "global_load_lds_dwordx4 v0, s[76:77]\n\t" \
  "global_load_lds_dwordx4 v1, s[76:77] offset:1024\n\t" \
  "s_add_u32 m0, s82, 2048\n\t" \
  "s_and_b32 s52, s52, 15\n\t" \
  "global_load_lds_dwordx4 v6, s[78:79]\n\t" \
  "global_load_lds_dwordx4 v7, s[78:79] offset:1024\n\t" \
  "s_add_u32 m0, s82, 26624\n\t" \
  "s_add_u32 s47, s47, 1\n\t" \
  "global_load_lds_dwordx4 v2, s[76:77]\n\t" \
  "global_load_lds_dwordx4 v3, s[76:77] offset:1024\n\t" \
  "s_add_u32 m0, s82, 10240\n\t" \
  "s_and_b32 s47, s47, 15\n\t" \
  "global_load_lds_dwordx4 v8, s[78:79]\n\t" \
  "global_load_lds_dwordx4 v9, s[78:79] offset:1024\n\t" \
  "s_add_u32 m0, s82, 34816\n\t" \
  "s_and_b32 s83, s54, s51\n\t" \
  "global_load_lds_dwordx4 v4, s[76:77]\n\t" \
  "global_load_lds_dwordx4 v5, s[76:77] offset:1024\n\t" \
  "s_bitcmp1_b32 s57, s55\n\t" \
  "s_cbranch_scc1 10f\n\t" \
  "8:\n\t" \
  "s_add_u32 m0, s83, s49\n\t" \
  "s_mov_b64 exec, s[50:51]\n\t" \
  "global_load_lds_dwordx4 v10, s[80:81]\n\t" \
  "s_mov_b64 exec, -1\n\t" \
  "4:\n\t" \
  "s_mul_i32 s76, s52, 0x60000\n\t" \
  "s_add_u32 s76, s42, s76\n\t" \
  "s_addc_u32 s77, s43, 0\n\t" \
  "s_lshl_b32 s78, s52, 7\n\t" \
  "s_add_u32 s78, s40, s78\n\t" \
  "s_addc_u32 s79, s41, 0\n\t" \
  "s_mul_i32 s80, s47, s46\n\t" \
  "s_add_u32 s80, s44, s80\n\t" \
  "s_addc_u32 s81, s45, 0\n\t" \
  "s_add_u32 s54, s54, 43008\n\t" \
  "s_cmp_eq_u32 s54, 129024\n\t" \
  "s_cselect_b32 s54, 0, s54\n\t" \
  "s_add_u32 s82, s54, s53\n\t" \
  "s_bitcmp0_b32 s56, s55\n\t" \
  "s_cbranch_scc1 1b\n\t" \
  "s_cmp_eq_u32 s55, 15\n\t" \
  "s_cbranch_scc1 19f\n\t" \
  "s_cmp_eq_u32 s55, s58\n\t" \
  "s_cbranch_scc1 3b\n\t" \
  "s_waitcnt vmcnt(11)\n\t" \
  "s_barrier\n\t" \
  "5:\n\t" \
  "s_mul_i32 s83, s59, 4160\n\t" \
  "v_add_u32 v20, 7152, v11\n\t" \
  "v_add_u32 v21, 14304, v11\n\t" \
  "v_add_u32 v22, 21456, v11\n\t" \
  "s_add_u32 m0, s83, 130064\n\t" \
  "s_nop 0\n\t" \
  "global_load_lds_dwordx4 v11, s[60:61]" LDPOL "\n\t" \
  "global_load_lds_dwordx4 v20, s[60:61] offset:1040" LDPOL "\n\t" \
  "global_load_lds_dwordx4 v21, s[60:61] offset:2080" LDPOL "\n\t" \
  "global_load_lds_dwordx4 v22, s[60:61] offset:3120" LDPOL "\n\t" \
  "s_add_u32 m0, s83, 146704\n\t" \
  "s_nop 0\n\t" \
  "global_load_lds_dwordx4 v11, s[62:63]" LDPOL "\n\t" \
  "global_load_lds_dwordx4 v20, s[62:63] offset:1040" LDPOL "\n\t" \
  "global_load_lds_dwordx4 v21, s[62:63] offset:2080" LDPOL "\n\t" \
  "global_load_lds_dwordx4 v22, s[62:63] offset:3120" LDPOL "\n\t" \
  "v_subrev_u32 v20, s64, v11\n\t" \
  "s_mov_b32 m0, s68\n\t" \
  "s_mov_b64 exec, s[66:67]\n\t" \
  "global_load_lds_dwordx4 v20, s[60:61]" LDPOL "\n\t" \
  "s_mov_b64 exec, -1\n\t" \
  "s_waitcnt vmcnt(9)\n\t" \
  "s_branch 2b\n\t" \
  "10:\n\t" \
  "s_cmp_eq_u32 s59, 2\n\t" \
  "s_cbranch_scc0 11f\n\t" \
  "s_mov_b64 exec, 0xffff\n\t" \
  "global_load_dword v255, v14, s[72:73]\n\t" \
  "s_mov_b64 exec, -1\n\t" \
  "s_branch 4b\n\t" \
  "11:\n\t" \
  "s_cmp_eq_u32 s55, 2\n\t" \
  "s_cbranch_scc0 12f\n\t" \
  "global_load_dword v254, v14, s[70:71] sc1\n\t" \
  "s_branch 4b\n\t" \
  "12:\n\t" \
  "s_cmp_eq_u32 s55, 5\n\t" \
  "s_cbranch_scc0 13f\n\t" \
  "v_readfirstlane_b32 s84, v254\n\t" \
  "v_mov_b32 v253, 163448\n\t" \
  "s_nop 1\n\t" \
  "s_add_u32 s84, s84, 1\n\t" \
  "v_mov_b32 v254, s84\n\t" \
  "ds_write_b32 v253, v254\n\t" \
  "s_waitcnt lgkmcnt(0)\n\t" \
  "s_branch 8b\n\t" \
  "13:\n\t" \
  "s_cmp_eq_u32 s55, 14\n\t" \
  "s_cbranch_scc0 14f\n\t" \
  "global_load_dword v253, v16, s[70:71] sc1\n\t" \
  "s_branch 4b\n\t" \
  "14:\n\t" \
  "s_mov_b32 exec_hi, 0\n\t" \
  "global_load_dword v255, v10, s[44:45]\n\t" \
  "s_mov_b64 exec, -1\n\t" \
  "s_branch 4b\n\t" \
  "19:\n\t" \
  "s_waitcnt vmcnt(11)\n\t" \
  "s_cmp_lg_u32 s59, 3\n\t" \
  "s_cbranch_scc1 18f\n\t" \
  "v_cmp_ne_u32 vcc, v253, v254\n\t" \
  "v_mov_b32 v253, 163364\n\t" \
  "s_nop 1\n\t" \
  "s_cmp_eq_u64 vcc, 0\n\t" \
  "s_cselect_b32 s83, 1, 0\n\t" \
  "v_mov_b32 v254, s83\n\t" \
  "ds_write_b32 v253, v254\n\t" \
  "18:\n\t"
#define C_ASM \
  "s_branch 29f\n\t" \
  "28:\n\t" \
  "s_mov_b64 exec, 0\n\t" \
  "s_cmp_eq_u32 %13, 0\n\t" \
  "s_cbranch_scc1 500f\n\t" \
  "s_cmp_eq_u32 %13, 1\n\t" \
  "s_cbranch_scc1 501f\n\t" \
  "s_cmp_eq_u32 %13, 2\n\t" \
  "s_cbranch_scc1 502f\n\t" \
  "s_branch 503f\n\t" \
  "26:\n\t" \
  "s_mov_b64 exec, -1\n\t" \
  "s_add_u32 s88, s88, 4\n\t" \
  "s_addc_u32 s89, s89, 0\n\t" \
  "s_setpc_b64 s[88:89]\n\t" \
  ".p2align 12\n\t" \
  "29:\n\t" \
  "s_bitcmp0_b32 %13, 0\n\t" \
  "s_cbranch_scc1 8f\n\t" \
  "global_load_dwordx2 v[10:11], v51, %11\n\t" \
  "global_load_dwordx2 v[12:13], v51, %11 offset:128\n\t" \
  "v_xor_b32 v53, 64, v52\n\t" \
  "v_xor_b32 v55, 32, v54\n\t" \
  "v_add_u32 v56, 43008, v52\n\t" \
  "v_add_u32 v58, 43008, v54\n\t" \
  "v_xor_b32 v57, 64, v56\n\t" \
  "v_xor_b32 v59, 32, v58\n\t" \
  "v_add_u32 v60, 86016, v52\n\t" \
  "v_add_u32 v62, 86016, v54\n\t" \
  "v_xor_b32 v61, 64, v60\n\t" \
  "v_xor_b32 v63, 32, v62\n\t" \
  "s_mov_b32 vcc_hi, 0x05040100\n\t" \
  "s_waitcnt vmcnt(2)\n\t" \
  "s_barrier\n\t" \
  "ds_read_b64_tr_b16 v[64:65], v62 offset:26624\n\t" \
  "ds_read_b64_tr_b16 v[66:67], v62 offset:27136\n\t" \
  "ds_read_b128 v[68:71], v60 offset:10240\n\t" \
  "ds_read_b64_tr_b16 v[72:73], v62 offset:30720\n\t" \
  "ds_read_b64_tr_b16 v[74:75], v62 offset:31232\n\t" \
  "ds_read_b128 v[76:79], v61 offset:10240\n\t" \
  "ds_read_b128 v[80:83], v61 offset:12288\n\t" \
  "ds_read_b128 v[84:87], v60 offset:12288\n\t" \
  "ds_read_b128 v[88:91], v60 offset:14336\n\t" \
  "ds_read_b128 v[92:95], v61 offset:14336\n\t" \
  "ds_read_b128 v[96:99], v61 offset:16384\n\t" \
  "ds_read_b128 v[100:103], v60 offset:16384\n\t" \
  "ds_read_b64_tr_b16 v[104:105], v63 offset:26624\n\t" \
  "ds_read_b64_tr_b16 v[106:107], v63 offset:27136\n\t" \
  "ds_read_b64_tr_b16 v[108:109], v63 offset:30720\n\t" \
  "ds_read_b64_tr_b16 v[110:111], v63 offset:31232\n\t" \
  "s_waitcnt lgkmcnt(13)\n\t" \
  "v_mfma_f32_16x16x32_bf16 v[184:187], v[64:67], v[68:71], 0\n\t" \
  "ds_read_b64_tr_b16 v[112:113], v62 offset:34816\n\t" \
  "ds_read_b64_tr_b16 v[114:115], v62 offset:35328\n\t" \
  "s_waitcnt lgkmcnt(12)\n\t" \
  "v_mfma_f32_16x16x32_bf16 v[184:187], v[72:75], v[76:79], v[184:187]\n\t" \
  "ds_read_b64_tr_b16 v[116:117], v62 offset:38912\n\t" \
  "ds_read_b64_tr_b16 v[118:119], v62 offset:39424\n\t" \
  "s_waitcnt lgkmcnt(13)\n\t" \
  "v_mfma_f32_16x16x32_bf16 v[192:195], v[72:75], v[80:83], 0\n\t" \
  "ds_read_b128 v[120:123], v60 offset:0\n\t" \
  "s_waitcnt lgkmcnt(13)\n\t" \
  "v_mfma_f32_16x16x32_bf16 v[192:195], v[64:67], v[84:87], v[192:195]\n\t" \
  "ds_read_b128 v[124:127], v61 offset:0\n\t" \
  "s_waitcnt lgkmcnt(13)\n\t" \
  "v_mfma_f32_16x16x32_bf16 v[200:203], v[64:67], v[88:91], 0\n\t" \
  "ds_read_b64_tr_b16 v[128:129], v63 offset:34816\n\t" \
  "s_waitcnt lgkmcnt(13)\n\t" \
  "v_mfma_f32_16x16x32_bf16 v[200:203], v[72:75], v[92:95], v[200:203]\n\t" \
  "ds_read_b64_tr_b16 v[130:131], v63 offset:35328\n\t" \
  "s_waitcnt lgkmcnt(13)\n\t" \
  "v_mfma_f32_16x16x32_bf16 v[208:211], v[72:75], v[96:99], 0\n\t" \
  "ds_read_b64_tr_b16 v[132:133], v63 offset:38912\n\t" \
  "s_waitcnt lgkmcnt(9)\n\t" \
  "v_mfma_f32_16x16x32_bf16 v[208:211], v[64:67], v[100:103], v[208:211]\n\t" \
  "ds_read_b64_tr_b16 v[134:135], v63 offset:39424\n\t" \
  "v_mfma_f32_16x16x32_bf16 v[188:191], v[104:107], v[68:71], 0\n\t" \
  "ds_read_b64_tr_b16 v[136:137], v62 offset:18432\n\t" \
  "v_mfma_f32_16x16x32_bf16 v[188:191], v[108:111], v[76:79], v[188:191]\n\t" \
  "ds_read_b64_tr_b16 v[138:139], v62 offset:18944\n\t" \
  "v_mfma_f32_16x16x32_bf16 v[196:199], v[108:111], v[80:83], 0\n\t" \
  "ds_read_b64_tr_b16 v[140:141], v62 offset:22528\n\t" \
  "v_mfma_f32_16x16x32_bf16 v[196:199], v[104:107], v[84:87], v[196:199]\n\t" \
  "ds_read_b64_tr_b16 v[142:143], v62 offset:23040\n\t" \
  "v_mfma_f32_16x16x32_bf16 v[204:207], v[104:107], v[88:91], 0\n\t" \
  "ds_read_b64_tr_b16 v[144:145], v63 offset:18432\n\t" \
  "v_mfma_f32_16x16x32_bf16 v[204:207], v[108:111], v[92:95], v[204:207]\n\t" \
  "ds_read_b64_tr_b16 v[146:147], v63 offset:18944\n\t" \
  "v_mfma_f32_16x16x32_bf16 v[212:215], v[108:111], v[96:99], 0\n\t" \
  "ds_read_b64_tr_b16 v[148:149], v63 offset:22528\n\t" \
  "v_mfma_f32_16x16x32_bf16 v[212:215], v[104:107], v[100:103], v[212:215]\n\t" \
  "ds_read_b64_tr_b16 v[150:151], v63 offset:23040\n\t" \
  "s_waitcnt lgkmcnt(7)\n\t" \
  "v_mfma_f32_16x16x32_bf16 v[216:219], v[68:71], v[112:115], 0\n\t" \
  "v_mfma_f32_16x16x32_bf16 v[216:219], v[76:79], v[116:119], v[216:219]\n\t" \
  "v_mfma_f32_16x16x32_bf16 v[224:227], v[80:83], v[116:119], 0\n\t" \
  "v_mfma_f32_16x16x32_bf16 v[224:227], v[84:87], v[112:115], v[224:227]\n\t" \
  "v_mfma_f32_16x16x32_bf16 v[232:235], v[88:91], v[112:115], 0\n\t" \
  "v_mfma_f32_16x16x32_bf16 v[232:235], v[92:95], v[116:119], v[232:235]\n\t" \
  "v_mfma_f32_16x16x32_bf16 v[240:243], v[96:99], v[116:119], 0\n\t" \
  "v_mfma_f32_16x16x32_bf16 v[240:243], v[100:103], v[112:115], v[240:243]\n\t" \
  "v_mfma_f32_16x16x32_bf16 v[248:251], v[120:123], v[112:115], 0\n\t" \
  "v_mfma_f32_16x16x32_bf16 v[248:251], v[124:127], v[116:119], v[248:251]\n\t" \
  "v_mfma_f32_16x16x32_bf16 v[252:255], v[120:123], v[128:131], 0\n\t" \
  "v_mfma_f32_16x16x32_bf16 v[252:255], v[124:127], v[132:135], v[252:255]\n\t" \
  "v_mfma_f32_16x16x32_bf16 v[220:223], v[76:79], v[132:135], 0\n\t" \
  "v_mfma_f32_16x16x32_bf16 v[220:223], v[68:71], v[128:131], v[220:223]\n\t" \
  "s_waitcnt lgkmcnt(0)\n\t" \
  "s_getpc_b64 s[88:89]\n\t" \
  "s_branch 28b\n\t" \
  "s_barrier\n\t" \
  "v_mfma_f32_16x16x32_bf16 v[228:231], v[84:87], v[128:131], 0\n\t" \
  "ds_read_b64_tr_b16 v[72:73], v54 offset:26624\n\t" \
  "v_mfma_f32_16x16x32_bf16 v[228:231], v[80:83], v[132:135], v[228:231]\n\t" \
  "ds_read_b64_tr_b16 v[74:75], v54 offset:27136\n\t" \
  "v_mfma_f32_16x16x32_bf16 v[236:239], v[92:95], v[132:135], 0\n\t" \
  "ds_read_b128 v[64:67], v52 offset:10240\n\t" \
  "v_mfma_f32_16x16x32_bf16 v[236:239], v[88:91], v[128:131], v[236:239]\n\t" \
  "ds_read_b64_tr_b16 v[108:109], v54 offset:30720\n\t" \
  "v_mfma_f32_16x16x32_bf16 v[244:247], v[100:103], v[128:131], 0\n\t" \
  "ds_read_b64_tr_b16 v[110:111], v54 offset:31232\n\t" \
  "v_mfma_f32_16x16x32_bf16 v[244:247], v[96:99], v[132:135], v[244:247]\n\t" \
  "ds_read_b128 v[104:107], v53 offset:10240\n\t" \
  "v_mfma_f32_16x16x32_bf16 v[152:155], v[136:139], v[68:71], 0\n\t" \
  "ds_read_b128 v[112:115], v53 offset:12288\n\t" \
  "v_mfma_f32_16x16x32_bf16 v[152:155], v[140:143], v[76:79], v[152:155]\n\t" \
  "ds_read_b128 v[116:119], v52 offset:12288\n\t" \
  "v_mfma_f32_16x16x32_bf16 v[160:163], v[140:143], v[80:83], 0\n\t" \
  "ds_read_b128 v[120:123], v52 offset:14336\n\t" \
  "v_mfma_f32_16x16x32_bf16 v[160:163], v[136:139], v[84:87], v[160:163]\n\t" \
  "ds_read_b128 v[124:127], v53 offset:14336\n\t" \
  "v_mfma_f32_16x16x32_bf16 v[168:171], v[136:139], v[88:91], 0\n\t" \
  "ds_read_b128 v[128:131], v53 offset:16384\n\t" \
  "v_mfma_f32_16x16x32_bf16 v[168:171], v[140:143], v[92:95], v[168:171]\n\t" \
  "ds_read_b128 v[132:135], v52 offset:16384\n\t" \
  "v_mfma_f32_16x16x32_bf16 v[176:179], v[140:143], v[96:99], 0\n\t" \
  "ds_read_b64_tr_b16 v[140:141], v55 offset:26624\n\t" \
  "v_mfma_f32_16x16x32_bf16 v[176:179], v[136:139], v[100:103], v[176:179]\n\t" \
  "ds_read_b64_tr_b16 v[142:143], v55 offset:27136\n\t" \
  "v_mfma_f32_16x16x32_bf16 v[156:159], v[144:147], v[68:71], 0\n\t" \
  "ds_read_b64_tr_b16 v[136:137], v55 offset:30720\n\t" \
  "v_mfma_f32_16x16x32_bf16 v[156:159], v[148:151], v[76:79], v[156:159]\n\t" \
  "ds_read_b64_tr_b16 v[138:139], v55 offset:31232\n\t" \
  "v_mfma_f32_16x16x32_bf16 v[164:167], v[148:151], v[80:83], 0\n\t" \
  "ds_read_b64_tr_b16 v[68:69], v54 offset:34816\n\t" \
  "v_mfma_f32_16x16x32_bf16 v[164:167], v[144:147], v[84:87], v[164:167]\n\t" \
  "ds_read_b64_tr_b16 v[70:71], v54 offset:35328\n\t" \
  "v_mfma_f32_16x16x32_bf16 v[172:175], v[144:147], v[88:91], 0\n\t" \
  "ds_read_b64_tr_b16 v[76:77], v54 offset:38912\n\t" \
  "v_mfma_f32_16x16x32_bf16 v[172:175], v[148:151], v[92:95], v[172:175]\n\t" \
  "ds_read_b64_tr_b16 v[78:79], v54 offset:39424\n\t" \
  "v_mfma_f32_16x16x32_bf16 v[180:183], v[148:151], v[96:99], 0\n\t" \
  "ds_read_b128 v[80:83], v52 offset:0\n\t" \
  "v_mfma_f32_16x16x32_bf16 v[180:183], v[144:147], v[100:103], v[180:183]\n\t" \
  "ds_read_b128 v[84:87], v53 offset:0\n\t" \
  "s_waitcnt lgkmcnt(7)\n\t" \
  "v_mfma_f32_16x16x32_bf16 v[184:187], v[72:75], v[64:67], v[184:187]\n\t" \
  "ds_read_b64_tr_b16 v[88:89], v55 offset:34816\n\t" \
  "v_mfma_f32_16x16x32_bf16 v[184:187], v[108:111], v[104:107], v[184:187]\n\t" \
  "ds_read_b64_tr_b16 v[90:91], v55 offset:35328\n\t" \
  "v_mfma_f32_16x16x32_bf16 v[192:195], v[108:111], v[112:115], v[192:195]\n\t" \
  "ds_read_b64_tr_b16 v[92:93], v55 offset:38912\n\t" \
  "v_mfma_f32_16x16x32_bf16 v[192:195], v[72:75], v[116:119], v[192:195]\n\t" \
  "ds_read_b64_tr_b16 v[94:95], v55 offset:39424\n\t" \
  "v_mfma_f32_16x16x32_bf16 v[200:203], v[72:75], v[120:123], v[200:203]\n\t" \
  "ds_read_b64_tr_b16 v[96:97], v54 offset:18432\n\t" \
  "v_mfma_f32_16x16x32_bf16 v[200:203], v[108:111], v[124:127], v[200:203]\n\t" \
  "ds_read_b64_tr_b16 v[98:99], v54 offset:18944\n\t" \
  "v_mfma_f32_16x16x32_bf16 v[208:211], v[108:111], v[128:131], v[208:211]\n\t" \
  "ds_read_b64_tr_b16 v[148:149], v54 offset:22528\n\t" \
  "v_mfma_f32_16x16x32_bf16 v[208:211], v[72:75], v[132:135], v[208:211]\n\t" \
  "ds_read_b64_tr_b16 v[150:151], v54 offset:23040\n\t" \
  "v_mfma_f32_16x16x32_bf16 v[188:191], v[140:143], v[64:67], v[188:191]\n\t" \
  "ds_read_b64_tr_b16 v[100:101], v55 offset:18432\n\t" \
  "s_waitcnt lgkmcnt(7)\n\t" \
  "v_mfma_f32_16x16x32_bf16 v[188:191], v[136:139], v[104:107], v[188:191]\n\t" \
  "ds_read_b64_tr_b16 v[102:103], v55 offset:18944\n\t" \
  "v_mfma_f32_16x16x32_bf16 v[196:199], v[136:139], v[112:115], v[196:199]\n\t" \
  "ds_read_b64_tr_b16 v[144:145], v55 offset:22528\n\t" \
  "v_mfma_f32_16x16x32_bf16 v[196:199], v[140:143], v[116:119], v[196:199]\n\t" \
  "ds_read_b64_tr_b16 v[146:147], v55 offset:23040\n\t" \
  "v_mfma_f32_16x16x32_bf16 v[204:207], v[140:143], v[120:123], v[204:207]\n\t" \
  "v_mfma_f32_16x16x32_bf16 v[204:207], v[136:139], v[124:127], v[204:207]\n\t" \
  "v_mfma_f32_16x16x32_bf16 v[212:215], v[136:139], v[128:131], v[212:215]\n\t" \
  "v_mfma_f32_16x16x32_bf16 v[212:215], v[140:143], v[132:135], v[212:215]\n\t" \
  "v_mfma_f32_16x16x32_bf16 v[216:219], v[64:67], v[68:71], v[216:219]\n\t" \
  "v_mfma_f32_16x16x32_bf16 v[216:219], v[104:107], v[76:79], v[216:219]\n\t" \
  "v_mfma_f32_16x16x32_bf16 v[224:227], v[112:115], v[76:79], v[224:227]\n\t" \
  "v_mfma_f32_16x16x32_bf16 v[224:227], v[116:119], v[68:71], v[224:227]\n\t" \
  "v_mfma_f32_16x16x32_bf16 v[232:235], v[120:123], v[68:71], v[232:235]\n\t" \
  "v_mfma_f32_16x16x32_bf16 v[232:235], v[124:127], v[76:79], v[232:235]\n\t" \
  "v_mfma_f32_16x16x32_bf16 v[240:243], v[128:131], v[76:79], v[240:243]\n\t" \
  "v_mfma_f32_16x16x32_bf16 v[240:243], v[132:135], v[68:71], v[240:243]\n\t" \
  "v_mfma_f32_16x16x32_bf16 v[248:251], v[80:83], v[68:71], v[248:251]\n\t" \
  "v_mfma_f32_16x16x32_bf16 v[248:251], v[84:87], v[76:79], v[248:251]\n\t" \
  "v_mfma_f32_16x16x32_bf16 v[252:255], v[80:83], v[88:91], v[252:255]\n\t" \
  "s_waitcnt lgkmcnt(0)\n\t" \
  "v_mfma_f32_16x16x32_bf16 v[252:255], v[84:87], v[92:95], v[252:255]\n\t" \
  "v_mfma_f32_16x16x32_bf16 v[220:223], v[104:107], v[92:95], v[220:223]\n\t" \
  "v_mfma_f32_16x16x32_bf16 v[220:223], v[64:67], v[88:91], v[220:223]\n\t" \
  "s_barrier\n\t" \
  "v_mfma_f32_16x16x32_bf16 v[228:231], v[116:119], v[88:91], v[228:231]\n\t" \
  "ds_read_b64_tr_b16 v[108:109], v58 offset:26624\n\t" \
  "v_mfma_f32_16x16x32_bf16 v[228:231], v[112:115], v[92:95], v[228:231]\n\t" \
  "ds_read_b64_tr_b16 v[110:111], v58 offset:27136\n\t" \
  "v_mfma_f32_16x16x32_bf16 v[236:239], v[124:127], v[92:95], v[236:239]\n\t" \
  "ds_read_b128 v[72:75], v56 offset:10240\n\t" \
  "v_mfma_f32_16x16x32_bf16 v[236:239], v[120:123], v[88:91], v[236:239]\n\t" \
  "ds_read_b64_tr_b16 v[136:137], v58 offset:30720\n\t" \
  "v_mfma_f32_16x16x32_bf16 v[244:247], v[132:135], v[88:91], v[244:247]\n\t" \
  "ds_read_b64_tr_b16 v[138:139], v58 offset:31232\n\t" \
  "v_mfma_f32_16x16x32_bf16 v[244:247], v[128:131], v[92:95], v[244:247]\n\t" \
  "ds_read_b128 v[140:143], v57 offset:10240\n\t" \
  "v_mfma_f32_16x16x32_bf16 v[152:155], v[96:99], v[64:67], v[152:155]\n\t" \
  "ds_read_b128 v[68:71], v57 offset:12288\n\t" \
  "v_mfma_f32_16x16x32_bf16 v[152:155], v[148:151], v[104:107], v[152:155]\n\t" \
  "ds_read_b128 v[76:79], v56 offset:12288\n\t" \
  "v_mfma_f32_16x16x32_bf16 v[160:163], v[148:151], v[112:115], v[160:163]\n\t" \
  "ds_read_b128 v[80:83], v56 offset:14336\n\t" \
  "v_mfma_f32_16x16x32_bf16 v[160:163], v[96:99], v[116:119], v[160:163]\n\t" \
  "ds_read_b128 v[84:87], v57 offset:14336\n\t" \
  "v_mfma_f32_16x16x32_bf16 v[168:171], v[96:99], v[120:123], v[168:171]\n\t" \
  "ds_read_b128 v[88:91], v57 offset:16384\n\t" \
  "v_mfma_f32_16x16x32_bf16 v[168:171], v[148:151], v[124:127], v[168:171]\n\t" \
  "ds_read_b128 v[92:95], v56 offset:16384\n\t" \
  "v_mfma_f32_16x16x32_bf16 v[176:179], v[148:151], v[128:131], v[176:179]\n\t" \
  "ds_read_b64_tr_b16 v[148:149], v59 offset:26624\n\t" \
  "v_mfma_f32_16x16x32_bf16 v[176:179], v[96:99], v[132:135], v[176:179]\n\t" \
  "ds_read_b64_tr_b16 v[150:151], v59 offset:27136\n\t" \
  "v_mfma_f32_16x16x32_bf16 v[156:159], v[100:103], v[64:67], v[156:159]\n\t" \
  "ds_read_b64_tr_b16 v[96:97], v59 offset:30720\n\t" \
  "v_mfma_f32_16x16x32_bf16 v[156:159], v[144:147], v[104:107], v[156:159]\n\t" \
  "ds_read_b64_tr_b16 v[98:99], v59 offset:31232\n\t" \
  "v_mfma_f32_16x16x32_bf16 v[164:167], v[144:147], v[112:115], v[164:167]\n\t" \
  "ds_read_b64_tr_b16 v[64:65], v58 offset:34816\n\t" \
  "v_mfma_f32_16x16x32_bf16 v[164:167], v[100:103], v[116:119], v[164:167]\n\t" \
  "ds_read_b64_tr_b16 v[66:67], v58 offset:35328\n\t" \
  "v_mfma_f32_16x16x32_bf16 v[172:175], v[100:103], v[120:123], v[172:175]\n\t" \
  "ds_read_b64_tr_b16 v[104:105], v58 offset:38912\n\t" \
  "v_mfma_f32_16x16x32_bf16 v[172:175], v[144:147], v[124:127], v[172:175]\n\t" \
  "ds_read_b64_tr_b16 v[106:107], v58 offset:39424\n\t" \
  "v_mfma_f32_16x16x32_bf16 v[180:183], v[144:147], v[128:131], v[180:183]\n\t" \
  "ds_read_b128 v[112:115], v56 offset:0\n\t" \
  "v_mfma_f32_16x16x32_bf16 v[180:183], v[100:103], v[132:135], v[180:183]\n\t" \
  "ds_read_b128 v[116:119], v57 offset:0\n\t" \
  "s_waitcnt lgkmcnt(7)\n\t" \
  "v_mfma_f32_16x16x32_bf16 v[184:187], v[108:111], v[72:75], v[184:187]\n\t" \
  "ds_read_b64_tr_b16 v[120:121], v59 offset:34816\n\t" \
  "v_mfma_f32_16x16x32_bf16 v[184:187], v[136:139], v[140:143], v[184:187]\n\t" \
  "ds_read_b64_tr_b16 v[122:123], v59 offset:35328\n\t" \
  "v_mfma_f32_16x16x32_bf16 v[192:195], v[136:139], v[68:71], v[192:195]\n\t" \
  "ds_read_b64_tr_b16 v[124:125], v59 offset:38912\n\t" \
  "v_mfma_f32_16x16x32_bf16 v[192:195], v[108:111], v[76:79], v[192:195]\n\t" \
  "ds_read_b64_tr_b16 v[126:127], v59 offset:39424\n\t" \
  "v_mfma_f32_16x16x32_bf16 v[200:203], v[108:111], v[80:83], v[200:203]\n\t" \
  "ds_read_b64_tr_b16 v[128:129], v58 offset:18432\n\t" \
  "v_mfma_f32_16x16x32_bf16 v[200:203], v[136:139], v[84:87], v[200:203]\n\t" \
  "ds_read_b64_tr_b16 v[130:131], v58 offset:18944\n\t" \
  "v_mfma_f32_16x16x32_bf16 v[208:211], v[136:139], v[88:91], v[208:211]\n\t" \
  "ds_read_b64_tr_b16 v[144:145], v58 offset:22528\n\t" \
  "v_mfma_f32_16x16x32_bf16 v[208:211], v[108:111], v[92:95], v[208:211]\n\t" \
  "ds_read_b64_tr_b16 v[146:147], v58 offset:23040\n\t" \
  "v_mfma_f32_16x16x32_bf16 v[188:191], v[148:151], v[72:75], v[188:191]\n\t" \
  "ds_read_b64_tr_b16 v[100:101], v59 offset:18432\n\t" \
  "s_waitcnt lgkmcnt(7)\n\t" \
  "v_mfma_f32_16x16x32_bf16 v[188:191], v[96:99], v[140:143], v[188:191]\n\t" \
  "ds_read_b64_tr_b16 v[102:103], v59 offset:18944\n\t" \
  "v_mfma_f32_16x16x32_bf16 v[196:199], v[96:99], v[68:71], v[196:199]\n\t" \
  "ds_read_b64_tr_b16 v[132:133], v59 offset:22528\n\t" \
  "v_mfma_f32_16x16x32_bf16 v[196:199], v[148:151], v[76:79], v[196:199]\n\t" \
  "ds_read_b64_tr_b16 v[134:135], v59 offset:23040\n\t" \
  "v_mfma_f32_16x16x32_bf16 v[204:207], v[148:151], v[80:83], v[204:207]\n\t" \
  "v_mfma_f32_16x16x32_bf16 v[204:207], v[96:99], v[84:87], v[204:207]\n\t" \
  "v_mfma_f32_16x16x32_bf16 v[212:215], v[96:99], v[88:91], v[212:215]\n\t" \
  "v_mfma_f32_16x16x32_bf16 v[212:215], v[148:151], v[92:95], v[212:215]\n\t" \
  "v_mfma_f32_16x16x32_bf16 v[216:219], v[72:75], v[64:67], v[216:219]\n\t" \
  "v_mfma_f32_16x16x32_bf16 v[216:219], v[140:143], v[104:107], v[216:219]\n\t" \
  "v_mfma_f32_16x16x32_bf16 v[224:227], v[68:71], v[104:107], v[224:227]\n\t" \
  "v_mfma_f32_16x16x32_bf16 v[224:227], v[76:79], v[64:67], v[224:227]\n\t" \
  "v_mfma_f32_16x16x32_bf16 v[232:235], v[80:83], v[64:67], v[232:235]\n\t" \
  "v_mfma_f32_16x16x32_bf16 v[232:235], v[84:87], v[104:107], v[232:235]\n\t" \
  "v_mfma_f32_16x16x32_bf16 v[240:243], v[88:91], v[104:107], v[240:243]\n\t" \
  "v_mfma_f32_16x16x32_bf16 v[240:243], v[92:95], v[64:67], v[240:243]\n\t" \
  "v_mfma_f32_16x16x32_bf16 v[248:251], v[112:115], v[64:67], v[248:251]\n\t" \
  "v_mfma_f32_16x16x32_bf16 v[248:251], v[116:119], v[104:107], v[248:251]\n\t" \
  "v_mfma_f32_16x16x32_bf16 v[252:255], v[112:115], v[120:123], v[252:255]\n\t" \
  "s_waitcnt lgkmcnt(0)\n\t" \
  "v_mfma_f32_16x16x32_bf16 v[252:255], v[116:119], v[124:127], v[252:255]\n\t" \
  "v_mfma_f32_16x16x32_bf16 v[220:223], v[140:143], v[124:127], v[220:223]\n\t" \
  "v_mfma_f32_16x16x32_bf16 v[220:223], v[72:75], v[120:123], v[220:223]\n\t" \
  "s_barrier\n\t" \
  "v_mfma_f32_16x16x32_bf16 v[228:231], v[76:79], v[120:123], v[228:231]\n\t" \
  "ds_read_b64_tr_b16 v[136:137], v62 offset:26624\n\t" \
  "v_bfe_u32 v30, v4, 9, 1\n\t" \
  "v_mfma_f32_16x16x32_bf16 v[228:231], v[68:71], v[124:127], v[228:231]\n\t" \
  "ds_read_b64_tr_b16 v[138:139], v62 offset:27136\n\t" \
  "v_mad_i32_i24 v30, v30, -16, v0\n\t" \
  "v_mfma_f32_16x16x32_bf16 v[236:239], v[84:87], v[124:127], v[236:239]\n\t" \
  "ds_read_b128 v[108:111], v60 offset:10240\n\t" \
  "ds_read_b128 v[26:29], v0 offset:1552\n\t" \
  "v_mfma_f32_16x16x32_bf16 v[236:239], v[80:83], v[120:123], v[236:239]\n\t" \
  "ds_read_b64_tr_b16 v[96:97], v62 offset:30720\n\t" \
  "ds_read_b128 v[30:33], v30 offset:1040\n\t" \
  "v_mfma_f32_16x16x32_bf16 v[244:247], v[92:95], v[120:123], v[244:247]\n\t" \
  "ds_read_b64_tr_b16 v[98:99], v62 offset:31232\n\t" \
  "ds_read_b128 v[34:37], v0 offset:512\n\t" \
  "v_mfma_f32_16x16x32_bf16 v[244:247], v[88:91], v[124:127], v[244:247]\n\t" \
  "ds_read_b128 v[148:151], v61 offset:10240\n\t" \
  "ds_read_b64_tr_b16 v[38:39], v1 offset:512\n\t" \
  "v_mfma_f32_16x16x32_bf16 v[152:155], v[128:131], v[72:75], v[152:155]\n\t" \
  "ds_read_b128 v[64:67], v61 offset:12288\n\t" \
  "ds_read_b64_tr_b16 v[40:41], v1 offset:0\n\t" \
  "v_mfma_f32_16x16x32_bf16 v[152:155], v[144:147], v[140:143], v[152:155]\n\t" \
  "ds_read_b128 v[104:107], v60 offset:12288\n\t" \
  "s_waitcnt vmcnt(0)\n\t" \
  "v_mfma_f32_16x16x32_bf16 v[160:163], v[144:147], v[68:71], v[160:163]\n\t" \
  "ds_read_b128 v[112:115], v60 offset:14336\n\t" \
  "v_perm_b32 v18, v10, v10, v14\n\t" \
  "v_mfma_f32_16x16x32_bf16 v[160:163], v[128:131], v[76:79], v[160:163]\n\t" \
  "ds_read_b128 v[116:119], v61 offset:14336\n\t" \
  "v_perm_b32 v19, v10, v10, v15\n\t" \
  "v_mfma_f32_16x16x32_bf16 v[168:171], v[128:131], v[80:83], v[168:171]\n\t" \
  "ds_read_b128 v[120:123], v61 offset:16384\n\t" \
  "v_perm_b32 v20, v10, v10, v16\n\t" \
  "v_mfma_f32_16x16x32_bf16 v[168:171], v[144:147], v[84:87], v[168:171]\n\t" \
  "ds_read_b128 v[124:127], v60 offset:16384\n\t" \
  "v_perm_b32 v21, v10, v10, v17\n\t" \
  "v_mfma_f32_16x16x32_bf16 v[176:179], v[144:147], v[88:91], v[176:179]\n\t" \
  "ds_read_b64_tr_b16 v[144:145], v63 offset:26624\n\t" \
  "v_perm_b32 v22, v11, v11, v14\n\t" \
  "v_mfma_f32_16x16x32_bf16 v[176:179], v[128:131], v[92:95], v[176:179]\n\t" \
  "ds_read_b64_tr_b16 v[146:147], v63 offset:27136\n\t" \
  "v_perm_b32 v23, v11, v11, v15\n\t" \
  "v_mfma_f32_16x16x32_bf16 v[156:159], v[100:103], v[72:75], v[156:159]\n\t" \
  "ds_read_b64_tr_b16 v[128:129], v63 offset:30720\n\t" \
  "v_perm_b32 v24, v11, v11, v16\n\t" \
  "v_mfma_f32_16x16x32_bf16 v[156:159], v[132:135], v[140:143], v[156:159]\n\t" \
  "ds_read_b64_tr_b16 v[130:131], v63 offset:31232\n\t" \
  "v_perm_b32 v25, v11, v11, v17\n\t" \
  "v_mfma_f32_16x16x32_bf16 v[164:167], v[132:135], v[68:71], v[164:167]\n\t" \
  "ds_read_b64_tr_b16 v[72:73], v62 offset:34816\n\t" \
  "v_mfma_f32_16x16x32_bf16 v[164:167], v[100:103], v[76:79], v[164:167]\n\t" \
  "ds_read_b64_tr_b16 v[74:75], v62 offset:35328\n\t" \
  "v_mfma_f32_16x16x32_bf16 v[172:175], v[100:103], v[80:83], v[172:175]\n\t" \
  "ds_read_b64_tr_b16 v[140:141], v62 offset:38912\n\t" \
  "s_waitcnt lgkmcnt(6)\n\t" \
  "v_mfma_f32_16x16x32_bf16 v[42:45], v[26:29], v[18:21], 0\n\t" \
  "v_mfma_f32_16x16x32_bf16 v[42:45], v[30:33], v[22:25], v[42:45]\n\t" \
  "v_mfma_f32_16x16x32_bf16 v[46:49], v[30:33], v[18:21], 0\n\t" \
  "v_mfma_f32_16x16x32_bf16 v[46:49], v[34:37], v[22:25], v[46:49]\n\t" \
  "ds_read_b128 v[26:29], v0 offset:1808\n\t" \
  "ds_read_b128 v[30:33], v0 offset:1296\n\t" \
  "v_mfma_f32_16x16x32_bf16 v[172:175], v[132:135], v[84:87], v[172:175]\n\t" \
  "ds_read_b64_tr_b16 v[142:143], v62 offset:39424\n\t" \
  "ds_read_b128 v[34:37], v0 offset:768\n\t" \
  "v_mfma_f32_16x16x32_bf16 v[180:183], v[132:135], v[88:91], v[180:183]\n\t" \
  "ds_read_b128 v[68:71], v60 offset:0\n\t" \
  "v_mfma_f32_16x16x32_bf16 v[180:183], v[100:103], v[92:95], v[180:183]\n\t" \
  "ds_read_b128 v[76:79], v61 offset:0\n\t" \
  "v_mfma_f32_16x16x32_bf16 v[184:187], v[136:139], v[108:111], v[184:187]\n\t" \
  "ds_read_b64_tr_b16 v[80:81], v63 offset:34816\n\t" \
  "v_lshlrev_b32 v50, 16, v38\n\t" \
  "v_mfma_f32_16x16x32_bf16 v[184:187], v[96:99], v[148:151], v[184:187]\n\t" \
  "ds_read_b64_tr_b16 v[82:83], v63 offset:35328\n\t" \
  "v_lshlrev_b32 v51, 16, v40\n\t" \
  "v_mfma_f32_16x16x32_bf16 v[192:195], v[96:99], v[64:67], v[192:195]\n\t" \
  "ds_read_b64_tr_b16 v[84:85], v63 offset:38912\n\t" \
  "v_mul_f32 v42, v42, v50\n\t" \
  "v_mfma_f32_16x16x32_bf16 v[192:195], v[136:139], v[104:107], v[192:195]\n\t" \
  "ds_read_b64_tr_b16 v[86:87], v63 offset:39424\n\t" \
  "v_mul_f32 v46, v46, v51\n\t" \
  "v_mfma_f32_16x16x32_bf16 v[200:203], v[136:139], v[112:115], v[200:203]\n\t" \
  "ds_read_b64_tr_b16 v[88:89], v62 offset:18432\n\t" \
  "v_cvt_pk_bf16_f32 v42, v42, v46\n\t" \
  "v_mfma_f32_16x16x32_bf16 v[200:203], v[96:99], v[116:119], v[200:203]\n\t" \
  "ds_read_b64_tr_b16 v[90:91], v62 offset:18944\n\t" \
  "v_and_b32 v50, 0xffff0000, v38\n\t" \
  "v_mfma_f32_16x16x32_bf16 v[208:211], v[96:99], v[120:123], v[208:211]\n\t" \
  "ds_read_b64_tr_b16 v[132:133], v62 offset:22528\n\t" \
  "v_and_b32 v51, 0xffff0000, v40\n\t" \
  "v_mfma_f32_16x16x32_bf16 v[208:211], v[136:139], v[124:127], v[208:211]\n\t" \
  "ds_read_b64_tr_b16 v[134:135], v62 offset:23040\n\t" \
  "v_mul_f32 v43, v43, v50\n\t" \
  "s_waitcnt lgkmcnt(7)\n\t" \
  "v_mfma_f32_16x16x32_bf16 v[188:191], v[144:147], v[108:111], v[188:191]\n\t" \
  "ds_read_b64_tr_b16 v[92:93], v63 offset:18432\n\t" \
  "v_mul_f32 v47, v47, v51\n\t" \
  "v_mfma_f32_16x16x32_bf16 v[188:191], v[128:131], v[148:151], v[188:191]\n\t" \
  "ds_read_b64_tr_b16 v[94:95], v63 offset:18944\n\t" \
  "v_cvt_pk_bf16_f32 v43, v43, v47\n\t" \
  "v_mfma_f32_16x16x32_bf16 v[196:199], v[128:131], v[64:67], v[196:199]\n\t" \
  "ds_read_b64_tr_b16 v[100:101], v63 offset:22528\n\t" \
  "v_lshlrev_b32 v50, 16, v39\n\t" \
  "v_mfma_f32_16x16x32_bf16 v[196:199], v[144:147], v[104:107], v[196:199]\n\t" \
  "ds_read_b64_tr_b16 v[102:103], v63 offset:23040\n\t" \
  "v_lshlrev_b32 v51, 16, v41\n\t" \
  "v_mfma_f32_16x16x32_bf16 v[204:207], v[144:147], v[112:115], v[204:207]\n\t" \
  "v_mul_f32 v44, v44, v50\n\t" \
  "v_mul_f32 v48, v48, v51\n\t" \
  "v_mfma_f32_16x16x32_bf16 v[204:207], v[128:131], v[116:119], v[204:207]\n\t" \
  "v_cvt_pk_bf16_f32 v44, v44, v48\n\t" \
  "v_and_b32 v50, 0xffff0000, v39\n\t" \
  "v_mfma_f32_16x16x32_bf16 v[212:215], v[128:131], v[120:123], v[212:215]\n\t" \
  "v_and_b32 v51, 0xffff0000, v41\n\t" \
  "v_mul_f32 v45, v45, v50\n\t" \
  "v_mfma_f32_16x16x32_bf16 v[212:215], v[144:147], v[124:127], v[212:215]\n\t" \
  "v_mul_f32 v49, v49, v51\n\t" \
  "ds_read_b64_tr_b16 v[38:39], v1 offset:768\n\t" \
  "v_mfma_f32_16x16x32_bf16 v[216:219], v[108:111], v[72:75], v[216:219]\n\t" \
  "ds_read_b64_tr_b16 v[40:41], v1 offset:256\n\t" \
  "v_cvt_pk_bf16_f32 v45, v45, v49\n\t" \
  "v_mfma_f32_16x16x32_bf16 v[216:219], v[148:151], v[140:143], v[216:219]\n\t" \
  "v_mfma_f32_16x16x32_bf16 v[224:227], v[64:67], v[140:143], v[224:227]\n\t" \
  "v_mfma_f32_16x16x32_bf16 v[224:227], v[104:107], v[72:75], v[224:227]\n\t" \
  "v_mfma_f32_16x16x32_bf16 v[232:235], v[112:115], v[72:75], v[232:235]\n\t" \
  "v_mfma_f32_16x16x32_bf16 v[232:235], v[116:119], v[140:143], v[232:235]\n\t" \
  "v_mfma_f32_16x16x32_bf16 v[240:243], v[120:123], v[140:143], v[240:243]\n\t" \
  "v_mfma_f32_16x16x32_bf16 v[240:243], v[124:127], v[72:75], v[240:243]\n\t" \
  "v_mfma_f32_16x16x32_bf16 v[248:251], v[68:71], v[72:75], v[248:251]\n\t" \
  "v_mfma_f32_16x16x32_bf16 v[248:251], v[76:79], v[140:143], v[248:251]\n\t" \
  "s_waitcnt lgkmcnt(0)\n\t" \
  "v_mfma_f32_16x16x32_bf16 v[252:255], v[68:71], v[80:83], v[252:255]\n\t" \
  "global_store_dwordx4 v2, v[42:45], %24 offset:0 sc1\n\t" \
  "v_mfma_f32_16x16x32_bf16 v[252:255], v[76:79], v[84:87], v[252:255]\n\t" \
  "s_nop 0\n\t" \
  "v_mfma_f32_16x16x32_bf16 v[42:45], v[26:29], v[18:21], 0\n\t" \
  "v_mfma_f32_16x16x32_bf16 v[42:45], v[30:33], v[22:25], v[42:45]\n\t" \
  "v_mfma_f32_16x16x32_bf16 v[46:49], v[30:33], v[18:21], 0\n\t" \
  "v_mfma_f32_16x16x32_bf16 v[46:49], v[34:37], v[22:25], v[46:49]\n\t" \
  "v_bfe_u32 v30, v4, 9, 1\n\t" \
  "v_mad_i32_i24 v30, v30, -16, v0\n\t" \
  "v_mfma_f32_16x16x32_bf16 v[220:223], v[148:151], v[84:87], v[220:223]\n\t" \
  "ds_read_b128 v[26:29], v0 offset:1584\n\t" \
  "ds_read_b128 v[30:33], v30 offset:1072\n\t" \
  "v_mfma_f32_16x16x32_bf16 v[220:223], v[108:111], v[80:83], v[220:223]\n\t" \
  "ds_read_b128 v[34:37], v0 offset:544\n\t" \
  "v_perm_b32 v18, v12, v12, v14\n\t" \
  "s_waitcnt lgkmcnt(0)\n\t" \
  "s_barrier\n\t" \
  "v_mfma_f32_16x16x32_bf16 v[228:231], v[104:107], v[80:83], v[228:231]\n\t" \
  "ds_read_b64_tr_b16 v[96:97], v54 offset:26624\n\t" \
  "v_perm_b32 v19, v12, v12, v15\n\t" \
  "v_mfma_f32_16x16x32_bf16 v[228:231], v[64:67], v[84:87], v[228:231]\n\t" \
  "ds_read_b64_tr_b16 v[98:99], v54 offset:27136\n\t" \
  "v_perm_b32 v20, v12, v12, v16\n\t" \
  "v_mfma_f32_16x16x32_bf16 v[236:239], v[116:119], v[84:87], v[236:239]\n\t" \
  "ds_read_b128 v[136:139], v52 offset:10240\n\t" \
  "v_perm_b32 v21, v12, v12, v17\n\t" \
  "v_mfma_f32_16x16x32_bf16 v[236:239], v[112:115], v[80:83], v[236:239]\n\t" \
  "ds_read_b64_tr_b16 v[128:129], v54 offset:30720\n\t" \
  "v_perm_b32 v22, v13, v13, v14\n\t" \
  "v_mfma_f32_16x16x32_bf16 v[244:247], v[124:127], v[80:83], v[244:247]\n\t" \
  "ds_read_b64_tr_b16 v[130:131], v54 offset:31232\n\t" \
  "v_perm_b32 v23, v13, v13, v15\n\t" \
  "v_mfma_f32_16x16x32_bf16 v[244:247], v[120:123], v[84:87], v[244:247]\n\t" \
  "ds_read_b128 v[144:147], v53 offset:10240\n\t" \
  "v_perm_b32 v24, v13, v13, v16\n\t" \
  "v_mfma_f32_16x16x32_bf16 v[152:155], v[88:91], v[108:111], v[152:155]\n\t" \
  "ds_read_b128 v[72:75], v53 offset:12288\n\t" \
  "v_perm_b32 v25, v13, v13, v17\n\t" \
  "v_mfma_f32_16x16x32_bf16 v[152:155], v[132:135], v[148:151], v[152:155]\n\t" \
  "ds_read_b128 v[140:143], v52 offset:12288\n\t" \
  "v_lshlrev_b32 v50, 16, v38\n\t" \
  "v_mfma_f32_16x16x32_bf16 v[160:163], v[132:135], v[64:67], v[160:163]\n\t" \
  "ds_read_b128 v[68:71], v52 offset:14336\n\t" \
  "v_lshlrev_b32 v51, 16, v40\n\t" \
  "v_mfma_f32_16x16x32_bf16 v[160:163], v[88:91], v[104:107], v[160:163]\n\t" \
  "ds_read_b128 v[76:79], v53 offset:14336\n\t" \
  "v_mul_f32 v42, v42, v50\n\t" \
  "v_mfma_f32_16x16x32_bf16 v[168:171], v[88:91], v[112:115], v[168:171]\n\t" \
  "ds_read_b128 v[80:83], v53 offset:16384\n\t" \
  "v_mul_f32 v46, v46, v51\n\t" \
  "v_mfma_f32_16x16x32_bf16 v[168:171], v[132:135], v[116:119], v[168:171]\n\t" \
  "ds_read_b128 v[84:87], v52 offset:16384\n\t" \
  "v_cvt_pk_bf16_f32 v42, v42, v46\n\t" \
  "v_mfma_f32_16x16x32_bf16 v[176:179], v[132:135], v[120:123], v[176:179]\n\t" \
  "ds_read_b64_tr_b16 v[132:133], v55 offset:26624\n\t" \
  "v_and_b32 v50, 0xffff0000, v38\n\t" \
  "v_mfma_f32_16x16x32_bf16 v[176:179], v[88:91], v[124:127], v[176:179]\n\t" \
  "ds_read_b64_tr_b16 v[134:135], v55 offset:27136\n\t" \
  "v_and_b32 v51, 0xffff0000, v40\n\t" \
  "v_mfma_f32_16x16x32_bf16 v[156:159], v[92:95], v[108:111], v[156:159]\n\t" \
  "ds_read_b64_tr_b16 v[88:89], v55 offset:30720\n\t" \
  "v_mul_f32 v43, v43, v50\n\t" \
  "v_mfma_f32_16x16x32_bf16 v[156:159], v[100:103], v[148:151], v[156:159]\n\t" \
  "ds_read_b64_tr_b16 v[90:91], v55 offset:31232\n\t" \
  "v_mul_f32 v47, v47, v51\n\t" \
  "v_mfma_f32_16x16x32_bf16 v[164:167], v[100:103], v[64:67], v[164:167]\n\t" \
  "ds_read_b64_tr_b16 v[108:109], v54 offset:34816\n\t" \
  "v_cvt_pk_bf16_f32 v43, v43, v47\n\t" \
  "v_mfma_f32_16x16x32_bf16 v[164:167], v[92:95], v[104:107], v[164:167]\n\t" \
  "ds_read_b64_tr_b16 v[110:111], v54 offset:35328\n\t" \
  "v_lshlrev_b32 v50, 16, v39\n\t" \
  "v_mfma_f32_16x16x32_bf16 v[172:175], v[92:95], v[112:115], v[172:175]\n\t" \
  "ds_read_b64_tr_b16 v[148:149], v54 offset:38912\n\t" \
  "v_lshlrev_b32 v51, 16, v41\n\t" \
  "v_mfma_f32_16x16x32_bf16 v[172:175], v[100:103], v[116:119], v[172:175]\n\t" \
  "ds_read_b64_tr_b16 v[150:151], v54 offset:39424\n\t" \
  "v_mul_f32 v44, v44, v50\n\t" \
  "v_mfma_f32_16x16x32_bf16 v[180:183], v[100:103], v[120:123], v[180:183]\n\t" \
  "ds_read_b128 v[64:67], v52 offset:0\n\t" \
  "v_mul_f32 v48, v48, v51\n\t" \
  "v_mfma_f32_16x16x32_bf16 v[180:183], v[92:95], v[124:127], v[180:183]\n\t" \
  "ds_read_b128 v[104:107], v53 offset:0\n\t" \
  "v_cvt_pk_bf16_f32 v44, v44, v48\n\t" \
  "s_waitcnt lgkmcnt(7)\n\t" \
  "v_mfma_f32_16x16x32_bf16 v[184:187], v[96:99], v[136:139], v[184:187]\n\t" \
  "ds_read_b64_tr_b16 v[112:113], v55 offset:34816\n\t" \
  "v_and_b32 v50, 0xffff0000, v39\n\t" \
  "v_mfma_f32_16x16x32_bf16 v[184:187], v[128:131], v[144:147], v[184:187]\n\t" \
  "ds_read_b64_tr_b16 v[114:115], v55 offset:35328\n\t" \
  "v_and_b32 v51, 0xffff0000, v41\n\t" \
  "v_mfma_f32_16x16x32_bf16 v[192:195], v[128:131], v[72:75], v[192:195]\n\t" \
  "ds_read_b64_tr_b16 v[116:117], v55 offset:38912\n\t" \
  "v_mul_f32 v45, v45, v50\n\t" \
  "v_mfma_f32_16x16x32_bf16 v[192:195], v[96:99], v[140:143], v[192:195]\n\t" \
  "ds_read_b64_tr_b16 v[118:119], v55 offset:39424\n\t" \
  "v_mul_f32 v49, v49, v51\n\t" \
  "v_mfma_f32_16x16x32_bf16 v[200:203], v[96:99], v[68:71], v[200:203]\n\t" \
  "ds_read_b64_tr_b16 v[100:101], v54 offset:18432\n\t" \
  "ds_read_b64_tr_b16 v[38:39], v1 offset:544\n\t" \
  "v_mfma_f32_16x16x32_bf16 v[200:203], v[128:131], v[76:79], v[200:203]\n\t" \
  "ds_read_b64_tr_b16 v[102:103], v54 offset:18944\n\t" \
  "ds_read_b64_tr_b16 v[40:41], v1 offset:32\n\t" \
  "v_mfma_f32_16x16x32_bf16 v[208:211], v[128:131], v[80:83], v[208:211]\n\t" \
  "ds_read_b64_tr_b16 v[120:121], v54 offset:22528\n\t" \
  "v_cvt_pk_bf16_f32 v45, v45, v49\n\t" \
  "v_mfma_f32_16x16x32_bf16 v[208:211], v[96:99], v[84:87], v[208:211]\n\t" \
  "ds_read_b64_tr_b16 v[122:123], v54 offset:23040\n\t" \
  "v_mfma_f32_16x16x32_bf16 v[188:191], v[132:135], v[136:139], v[188:191]\n\t" \
  "ds_read_b64_tr_b16 v[92:93], v55 offset:18432\n\t" \
  "s_waitcnt lgkmcnt(9)\n\t" \
  "v_mfma_f32_16x16x32_bf16 v[188:191], v[88:91], v[144:147], v[188:191]\n\t" \
  "ds_read_b64_tr_b16 v[94:95], v55 offset:18944\n\t" \
  "v_mfma_f32_16x16x32_bf16 v[196:199], v[88:91], v[72:75], v[196:199]\n\t" \
  "ds_read_b64_tr_b16 v[124:125], v55 offset:22528\n\t" \
  "v_mfma_f32_16x16x32_bf16 v[196:199], v[132:135], v[140:143], v[196:199]\n\t" \
  "ds_read_b64_tr_b16 v[126:127], v55 offset:23040\n\t" \
  "v_mfma_f32_16x16x32_bf16 v[204:207], v[132:135], v[68:71], v[204:207]\n\t" \
  "v_mfma_f32_16x16x32_bf16 v[204:207], v[88:91], v[76:79], v[204:207]\n\t" \
  "v_mfma_f32_16x16x32_bf16 v[212:215], v[88:91], v[80:83], v[212:215]\n\t" \
  "v_mfma_f32_16x16x32_bf16 v[212:215], v[132:135], v[84:87], v[212:215]\n\t" \
  "v_mfma_f32_16x16x32_bf16 v[216:219], v[136:139], v[108:111], v[216:219]\n\t" \
  "v_mfma_f32_16x16x32_bf16 v[216:219], v[144:147], v[148:151], v[216:219]\n\t" \
  "v_mfma_f32_16x16x32_bf16 v[224:227], v[72:75], v[148:151], v[224:227]\n\t" \
  "v_mfma_f32_16x16x32_bf16 v[224:227], v[140:143], v[108:111], v[224:227]\n\t" \
  "v_mfma_f32_16x16x32_bf16 v[232:235], v[68:71], v[108:111], v[232:235]\n\t" \
  "v_mfma_f32_16x16x32_bf16 v[232:235], v[76:79], v[148:151], v[232:235]\n\t" \
  "v_mfma_f32_16x16x32_bf16 v[240:243], v[80:83], v[148:151], v[240:243]\n\t" \
  "v_mfma_f32_16x16x32_bf16 v[240:243], v[84:87], v[108:111], v[240:243]\n\t" \
  "v_mfma_f32_16x16x32_bf16 v[248:251], v[64:67], v[108:111], v[248:251]\n\t" \
  "v_mfma_f32_16x16x32_bf16 v[248:251], v[104:107], v[148:151], v[248:251]\n\t" \
  "v_mfma_f32_16x16x32_bf16 v[252:255], v[64:67], v[112:115], v[252:255]\n\t" \
  "global_store_dwordx4 v2, v[42:45], %24 offset:1024 sc1\n\t" \
  "v_add_u32 v2, 0x10000, v2\n\t" \
  "s_waitcnt lgkmcnt(0)\n\t" \
  "v_mfma_f32_16x16x32_bf16 v[252:255], v[104:107], v[116:119], v[252:255]\n\t" \
  "v_mfma_f32_16x16x32_bf16 v[220:223], v[144:147], v[116:119], v[220:223]\n\t" \
  "v_mfma_f32_16x16x32_bf16 v[42:45], v[26:29], v[18:21], 0\n\t" \
  "v_mfma_f32_16x16x32_bf16 v[42:45], v[30:33], v[22:25], v[42:45]\n\t" \
  "v_mfma_f32_16x16x32_bf16 v[46:49], v[30:33], v[18:21], 0\n\t" \
  "v_mfma_f32_16x16x32_bf16 v[46:49], v[34:37], v[22:25], v[46:49]\n\t" \
  "500:\n\t" \
  "s_cbranch_execz 504f\n\t" \
  "ds_read_b128 v[26:29], v0 offset:1840\n\t" \
  "ds_read_b128 v[30:33], v0 offset:1328\n\t" \
  "v_mfma_f32_16x16x32_bf16 v[220:223], v[136:139], v[112:115], v[220:223]\n\t" \
  "ds_read_b128 v[34:37], v0 offset:800\n\t" \
  "s_waitcnt lgkmcnt(0)\n\t" \
  "s_barrier\n\t" \
  "v_mfma_f32_16x16x32_bf16 v[228:231], v[140:143], v[112:115], v[228:231]\n\t" \
  "ds_read_b64_tr_b16 v[128:129], v58 offset:26624\n\t" \
  "v_mfma_f32_16x16x32_bf16 v[228:231], v[72:75], v[116:119], v[228:231]\n\t" \
  "ds_read_b64_tr_b16 v[130:131], v58 offset:27136\n\t" \
  "v_lshlrev_b32 v50, 16, v38\n\t" \
  "v_mfma_f32_16x16x32_bf16 v[236:239], v[76:79], v[116:119], v[236:239]\n\t" \
  "ds_read_b128 v[96:99], v56 offset:10240\n\t" \
  "v_lshlrev_b32 v51, 16, v40\n\t" \
  "v_mfma_f32_16x16x32_bf16 v[236:239], v[68:71], v[112:115], v[236:239]\n\t" \
  "ds_read_b64_tr_b16 v[88:89], v58 offset:30720\n\t" \
  "v_mul_f32 v42, v42, v50\n\t" \
  "v_mfma_f32_16x16x32_bf16 v[244:247], v[84:87], v[112:115], v[244:247]\n\t" \
  "ds_read_b64_tr_b16 v[90:91], v58 offset:31232\n\t" \
  "v_mul_f32 v46, v46, v51\n\t" \
  "v_mfma_f32_16x16x32_bf16 v[244:247], v[80:83], v[116:119], v[244:247]\n\t" \
  "ds_read_b128 v[132:135], v57 offset:10240\n\t" \
  "v_cvt_pk_bf16_f32 v42, v42, v46\n\t" \
  "v_mfma_f32_16x16x32_bf16 v[152:155], v[100:103], v[136:139], v[152:155]\n\t" \
  "ds_read_b128 v[108:111], v57 offset:12288\n\t" \
  "v_and_b32 v50, 0xffff0000, v38\n\t" \
  "v_mfma_f32_16x16x32_bf16 v[152:155], v[120:123], v[144:147], v[152:155]\n\t" \
  "ds_read_b128 v[148:151], v56 offset:12288\n\t" \
  "v_and_b32 v51, 0xffff0000, v40\n\t" \
  "v_mfma_f32_16x16x32_bf16 v[160:163], v[120:123], v[72:75], v[160:163]\n\t" \
  "ds_read_b128 v[64:67], v56 offset:14336\n\t" \
  "v_mul_f32 v43, v43, v50\n\t" \
  "v_mfma_f32_16x16x32_bf16 v[160:163], v[100:103], v[140:143], v[160:163]\n\t" \
  "ds_read_b128 v[104:107], v57 offset:14336\n\t" \
  "v_mul_f32 v47, v47, v51\n\t" \
  "v_mfma_f32_16x16x32_bf16 v[168:171], v[100:103], v[68:71], v[168:171]\n\t" \
  "ds_read_b128 v[112:115], v57 offset:16384\n\t" \
  "v_cvt_pk_bf16_f32 v43, v43, v47\n\t" \
  "v_mfma_f32_16x16x32_bf16 v[168:171], v[120:123], v[76:79], v[168:171]\n\t" \
  "ds_read_b128 v[116:119], v56 offset:16384\n\t" \
  "v_lshlrev_b32 v50, 16, v39\n\t" \
  "v_mfma_f32_16x16x32_bf16 v[176:179], v[120:123], v[80:83], v[176:179]\n\t" \
  "ds_read_b64_tr_b16 v[120:121], v59 offset:26624\n\t" \
  "v_lshlrev_b32 v51, 16, v41\n\t" \
  "v_mfma_f32_16x16x32_bf16 v[176:179], v[100:103], v[84:87], v[176:179]\n\t" \
  "ds_read_b64_tr_b16 v[122:123], v59 offset:27136\n\t" \
  "v_mul_f32 v44, v44, v50\n\t" \
  "v_mfma_f32_16x16x32_bf16 v[156:159], v[92:95], v[136:139], v[156:159]\n\t" \
  "ds_read_b64_tr_b16 v[100:101], v59 offset:30720\n\t" \
  "v_mul_f32 v48, v48, v51\n\t" \
  "v_mfma_f32_16x16x32_bf16 v[156:159], v[124:127], v[144:147], v[156:159]\n\t" \
  "ds_read_b64_tr_b16 v[102:103], v59 offset:31232\n\t" \
  "v_cvt_pk_bf16_f32 v44, v44, v48\n\t" \
  "v_mfma_f32_16x16x32_bf16 v[164:167], v[124:127], v[72:75], v[164:167]\n\t" \
  "ds_read_b64_tr_b16 v[136:137], v58 offset:34816\n\t" \
  "v_and_b32 v50, 0xffff0000, v39\n\t" \
  "v_mfma_f32_16x16x32_bf16 v[164:167], v[92:95], v[140:143], v[164:167]\n\t" \
  "ds_read_b64_tr_b16 v[138:139], v58 offset:35328\n\t" \
  "v_and_b32 v51, 0xffff0000, v41\n\t" \
  "v_mfma_f32_16x16x32_bf16 v[172:175], v[92:95], v[68:71], v[172:175]\n\t" \
  "ds_read_b64_tr_b16 v[144:145], v58 offset:38912\n\t" \
  "v_mul_f32 v45, v45, v50\n\t" \
  "v_mfma_f32_16x16x32_bf16 v[172:175], v[124:127], v[76:79], v[172:175]\n\t" \
  "ds_read_b64_tr_b16 v[146:147], v58 offset:39424\n\t" \
  "v_mul_f32 v49, v49, v51\n\t" \
  "v_mfma_f32_16x16x32_bf16 v[180:183], v[124:127], v[80:83], v[180:183]\n\t" \
  "ds_read_b128 v[72:75], v56 offset:0\n\t" \
  "ds_read_b64_tr_b16 v[38:39], v1 offset:800\n\t" \
  "v_mfma_f32_16x16x32_bf16 v[180:183], v[92:95], v[84:87], v[180:183]\n\t" \
  "ds_read_b128 v[140:143], v57 offset:0\n\t" \
  "ds_read_b64_tr_b16 v[40:41], v1 offset:288\n\t" \
  "s_waitcnt lgkmcnt(9)\n\t" \
  "v_mfma_f32_16x16x32_bf16 v[184:187], v[128:131], v[96:99], v[184:187]\n\t" \
  "ds_read_b64_tr_b16 v[68:69], v59 offset:34816\n\t" \
  "v_cvt_pk_bf16_f32 v45, v45, v49\n\t" \
  "v_mfma_f32_16x16x32_bf16 v[184:187], v[88:91], v[132:135], v[184:187]\n\t" \
  "ds_read_b64_tr_b16 v[70:71], v59 offset:35328\n\t" \
  "v_mfma_f32_16x16x32_bf16 v[192:195], v[88:91], v[108:111], v[192:195]\n\t" \
  "ds_read_b64_tr_b16 v[76:77], v59 offset:38912\n\t" \
  "v_mfma_f32_16x16x32_bf16 v[192:195], v[128:131], v[148:151], v[192:195]\n\t" \
  "ds_read_b64_tr_b16 v[78:79], v59 offset:39424\n\t" \
  "v_mfma_f32_16x16x32_bf16 v[200:203], v[128:131], v[64:67], v[200:203]\n\t" \
  "ds_read_b64_tr_b16 v[80:81], v58 offset:18432\n\t" \
  "v_mfma_f32_16x16x32_bf16 v[200:203], v[88:91], v[104:107], v[200:203]\n\t" \
  "ds_read_b64_tr_b16 v[82:83], v58 offset:18944\n\t" \
  "v_mfma_f32_16x16x32_bf16 v[208:211], v[88:91], v[112:115], v[208:211]\n\t" \
  "ds_read_b64_tr_b16 v[124:125], v58 offset:22528\n\t" \
  "v_mfma_f32_16x16x32_bf16 v[208:211], v[128:131], v[116:119], v[208:211]\n\t" \
  "ds_read_b64_tr_b16 v[126:127], v58 offset:23040\n\t" \
  "v_mfma_f32_16x16x32_bf16 v[188:191], v[120:123], v[96:99], v[188:191]\n\t" \
  "ds_read_b64_tr_b16 v[84:85], v59 offset:18432\n\t" \
  "s_waitcnt lgkmcnt(7)\n\t" \
  "v_mfma_f32_16x16x32_bf16 v[188:191], v[100:103], v[132:135], v[188:191]\n\t" \
  "ds_read_b64_tr_b16 v[86:87], v59 offset:18944\n\t" \
  "v_mfma_f32_16x16x32_bf16 v[196:199], v[100:103], v[108:111], v[196:199]\n\t" \
  "ds_read_b64_tr_b16 v[92:93], v59 offset:22528\n\t" \
  "v_mfma_f32_16x16x32_bf16 v[196:199], v[120:123], v[148:151], v[196:199]\n\t" \
  "ds_read_b64_tr_b16 v[94:95], v59 offset:23040\n\t" \
  "v_mfma_f32_16x16x32_bf16 v[204:207], v[120:123], v[64:67], v[204:207]\n\t" \
  "v_mfma_f32_16x16x32_bf16 v[204:207], v[100:103], v[104:107], v[204:207]\n\t" \
  "v_mfma_f32_16x16x32_bf16 v[212:215], v[100:103], v[112:115], v[212:215]\n\t" \
  "v_mfma_f32_16x16x32_bf16 v[212:215], v[120:123], v[116:119], v[212:215]\n\t" \
  "v_mfma_f32_16x16x32_bf16 v[216:219], v[96:99], v[136:139], v[216:219]\n\t" \
  "v_mfma_f32_16x16x32_bf16 v[216:219], v[132:135], v[144:147], v[216:219]\n\t" \
  "v_mfma_f32_16x16x32_bf16 v[224:227], v[108:111], v[144:147], v[224:227]\n\t" \
  "v_mfma_f32_16x16x32_bf16 v[224:227], v[148:151], v[136:139], v[224:227]\n\t" \
  "v_mfma_f32_16x16x32_bf16 v[232:235], v[64:67], v[136:139], v[232:235]\n\t" \
  "v_mfma_f32_16x16x32_bf16 v[232:235], v[104:107], v[144:147], v[232:235]\n\t" \
  "v_mfma_f32_16x16x32_bf16 v[240:243], v[112:115], v[144:147], v[240:243]\n\t" \
  "v_mfma_f32_16x16x32_bf16 v[240:243], v[116:119], v[136:139], v[240:243]\n\t" \
  "v_mfma_f32_16x16x32_bf16 v[248:251], v[72:75], v[136:139], v[248:251]\n\t" \
  "v_mfma_f32_16x16x32_bf16 v[248:251], v[140:143], v[144:147], v[248:251]\n\t" \
  "v_mfma_f32_16x16x32_bf16 v[252:255], v[72:75], v[68:71], v[252:255]\n\t" \
  "global_store_dwordx4 v2, v[42:45], %24 offset:0 sc1\n\t" \
  "s_waitcnt lgkmcnt(0)\n\t" \
  "v_mfma_f32_16x16x32_bf16 v[252:255], v[140:143], v[76:79], v[252:255]\n\t" \
  "v_mfma_f32_16x16x32_bf16 v[42:45], v[26:29], v[18:21], 0\n\t" \
  "v_mfma_f32_16x16x32_bf16 v[42:45], v[30:33], v[22:25], v[42:45]\n\t" \
  "v_mfma_f32_16x16x32_bf16 v[46:49], v[30:33], v[18:21], 0\n\t" \
  "v_mfma_f32_16x16x32_bf16 v[46:49], v[34:37], v[22:25], v[46:49]\n\t" \
  "v_mfma_f32_16x16x32_bf16 v[220:223], v[132:135], v[76:79], v[220:223]\n\t" \
  "v_mfma_f32_16x16x32_bf16 v[220:223], v[96:99], v[68:71], v[220:223]\n\t" \
  "v_lshlrev_b32 v50, 16, v38\n\t" \
  "v_lshlrev_b32 v51, 16, v40\n\t" \
  "s_barrier\n\t" \
  "v_mfma_f32_16x16x32_bf16 v[228:231], v[148:151], v[68:71], v[228:231]\n\t" \
  "ds_read_b64_tr_b16 v[88:89], v62 offset:26624\n\t" \
  "v_mul_f32 v42, v42, v50\n\t" \
  "v_mfma_f32_16x16x32_bf16 v[228:231], v[108:111], v[76:79], v[228:231]\n\t" \
  "ds_read_b64_tr_b16 v[90:91], v62 offset:27136\n\t" \
  "v_mul_f32 v46, v46, v51\n\t" \
  "v_mfma_f32_16x16x32_bf16 v[236:239], v[104:107], v[76:79], v[236:239]\n\t" \
  "ds_read_b128 v[128:131], v60 offset:10240\n\t" \
  "v_cvt_pk_bf16_f32 v42, v42, v46\n\t" \
  "v_mfma_f32_16x16x32_bf16 v[236:239], v[64:67], v[68:71], v[236:239]\n\t" \
  "ds_read_b64_tr_b16 v[100:101], v62 offset:30720\n\t" \
  "v_and_b32 v50, 0xffff0000, v38\n\t" \
  "v_mfma_f32_16x16x32_bf16 v[244:247], v[116:119], v[68:71], v[244:247]\n\t" \
  "ds_read_b64_tr_b16 v[102:103], v62 offset:31232\n\t" \
  "v_and_b32 v51, 0xffff0000, v40\n\t" \
  "v_mfma_f32_16x16x32_bf16 v[244:247], v[112:115], v[76:79], v[244:247]\n\t" \
  "ds_read_b128 v[120:123], v61 offset:10240\n\t" \
  "v_mul_f32 v43, v43, v50\n\t" \
  "v_mfma_f32_16x16x32_bf16 v[152:155], v[80:83], v[96:99], v[152:155]\n\t" \
  "ds_read_b128 v[136:139], v61 offset:12288\n\t" \
  "v_mul_f32 v47, v47, v51\n\t" \
  "v_mfma_f32_16x16x32_bf16 v[152:155], v[124:127], v[132:135], v[152:155]\n\t" \
  "ds_read_b128 v[144:147], v60 offset:12288\n\t" \
  "v_cvt_pk_bf16_f32 v43, v43, v47\n\t" \
  "v_mfma_f32_16x16x32_bf16 v[160:163], v[124:127], v[108:111], v[160:163]\n\t" \
  "ds_read_b128 v[72:75], v60 offset:14336\n\t" \
  "v_lshlrev_b32 v50, 16, v39\n\t" \
  "v_mfma_f32_16x16x32_bf16 v[160:163], v[80:83], v[148:151], v[160:163]\n\t" \
  "ds_read_b128 v[140:143], v61 offset:14336\n\t" \
  "v_lshlrev_b32 v51, 16, v41\n\t" \
  "v_mfma_f32_16x16x32_bf16 v[168:171], v[80:83], v[64:67], v[168:171]\n\t" \
  "ds_read_b128 v[68:71], v61 offset:16384\n\t" \
  "v_mul_f32 v44, v44, v50\n\t" \
  "v_mfma_f32_16x16x32_bf16 v[168:171], v[124:127], v[104:107], v[168:171]\n\t" \
  "ds_read_b128 v[76:79], v60 offset:16384\n\t" \
  "v_mul_f32 v48, v48, v51\n\t" \
  "v_mfma_f32_16x16x32_bf16 v[176:179], v[124:127], v[112:115], v[176:179]\n\t" \
  "ds_read_b64_tr_b16 v[124:125], v63 offset:26624\n\t" \
  "v_cvt_pk_bf16_f32 v44, v44, v48\n\t" \
  "v_mfma_f32_16x16x32_bf16 v[176:179], v[80:83], v[116:119], v[176:179]\n\t" \
  "ds_read_b64_tr_b16 v[126:127], v63 offset:27136\n\t" \
  "v_and_b32 v50, 0xffff0000, v39\n\t" \
  "v_mfma_f32_16x16x32_bf16 v[156:159], v[84:87], v[96:99], v[156:159]\n\t" \
  "ds_read_b64_tr_b16 v[80:81], v63 offset:30720\n\t" \
  "v_and_b32 v51, 0xffff0000, v41\n\t" \
  "v_mfma_f32_16x16x32_bf16 v[156:159], v[92:95], v[132:135], v[156:159]\n\t" \
  "ds_read_b64_tr_b16 v[82:83], v63 offset:31232\n\t" \
  "v_mul_f32 v45, v45, v50\n\t" \
  "v_mfma_f32_16x16x32_bf16 v[164:167], v[92:95], v[108:111], v[164:167]\n\t" \
  "ds_read_b64_tr_b16 v[96:97], v62 offset:34816\n\t" \
  "v_mul_f32 v49, v49, v51\n\t" \
  "v_mfma_f32_16x16x32_bf16 v[164:167], v[84:87], v[148:151], v[164:167]\n\t" \
  "ds_read_b64_tr_b16 v[98:99], v62 offset:35328\n\t" \
  "v_cvt_pk_bf16_f32 v45, v45, v49\n\t" \
  "v_mfma_f32_16x16x32_bf16 v[172:175], v[84:87], v[64:67], v[172:175]\n\t" \
  "ds_read_b64_tr_b16 v[132:133], v62 offset:38912\n\t" \
  "v_mfma_f32_16x16x32_bf16 v[172:175], v[92:95], v[104:107], v[172:175]\n\t" \
  "ds_read_b64_tr_b16 v[134:135], v62 offset:39424\n\t" \
  "v_mfma_f32_16x16x32_bf16 v[180:183], v[92:95], v[112:115], v[180:183]\n\t" \
  "ds_read_b128 v[108:111], v60 offset:0\n\t" \
  "v_mfma_f32_16x16x32_bf16 v[180:183], v[84:87], v[116:119], v[180:183]\n\t" \
  "ds_read_b128 v[148:151], v61 offset:0\n\t" \
  "s_waitcnt lgkmcnt(7)\n\t" \
  "v_mfma_f32_16x16x32_bf16 v[184:187], v[88:91], v[128:131], v[184:187]\n\t" \
  "ds_read_b64_tr_b16 v[64:65], v63 offset:34816\n\t" \
  "v_mfma_f32_16x16x32_bf16 v[184:187], v[100:103], v[120:123], v[184:187]\n\t" \
  "ds_read_b64_tr_b16 v[66:67], v63 offset:35328\n\t" \
  "v_mfma_f32_16x16x32_bf16 v[192:195], v[100:103], v[136:139], v[192:195]\n\t" \
  "ds_read_b64_tr_b16 v[104:105], v63 offset:38912\n\t" \
  "v_mfma_f32_16x16x32_bf16 v[192:195], v[88:91], v[144:147], v[192:195]\n\t" \
  "ds_read_b64_tr_b16 v[106:107], v63 offset:39424\n\t" \
  "v_mfma_f32_16x16x32_bf16 v[200:203], v[88:91], v[72:75], v[200:203]\n\t" \
  "ds_read_b64_tr_b16 v[92:93], v62 offset:18432\n\t" \
  "v_mfma_f32_16x16x32_bf16 v[200:203], v[100:103], v[140:143], v[200:203]\n\t" \
  "ds_read_b64_tr_b16 v[94:95], v62 offset:18944\n\t" \
  "v_mfma_f32_16x16x32_bf16 v[208:211], v[100:103], v[68:71], v[208:211]\n\t" \
  "ds_read_b64_tr_b16 v[112:113], v62 offset:22528\n\t" \
  "v_mfma_f32_16x16x32_bf16 v[208:211], v[88:91], v[76:79], v[208:211]\n\t" \
  "ds_read_b64_tr_b16 v[114:115], v62 offset:23040\n\t" \
  "v_mfma_f32_16x16x32_bf16 v[188:191], v[124:127], v[128:131], v[188:191]\n\t" \
  "ds_read_b64_tr_b16 v[84:85], v63 offset:18432\n\t" \
  "s_waitcnt lgkmcnt(7)\n\t" \
  "v_mfma_f32_16x16x32_bf16 v[188:191], v[80:83], v[120:123], v[188:191]\n\t" \
  "ds_read_b64_tr_b16 v[86:87], v63 offset:18944\n\t" \
  "v_mfma_f32_16x16x32_bf16 v[196:199], v[80:83], v[136:139], v[196:199]\n\t" \
  "ds_read_b64_tr_b16 v[116:117], v63 offset:22528\n\t" \
  "v_mfma_f32_16x16x32_bf16 v[196:199], v[124:127], v[144:147], v[196:199]\n\t" \
  "ds_read_b64_tr_b16 v[118:119], v63 offset:23040\n\t" \
  "v_mfma_f32_16x16x32_bf16 v[204:207], v[124:127], v[72:75], v[204:207]\n\t" \
  "v_mfma_f32_16x16x32_bf16 v[204:207], v[80:83], v[140:143], v[204:207]\n\t" \
  "v_mfma_f32_16x16x32_bf16 v[212:215], v[80:83], v[68:71], v[212:215]\n\t" \
  "v_mfma_f32_16x16x32_bf16 v[212:215], v[124:127], v[76:79], v[212:215]\n\t" \
  "v_mfma_f32_16x16x32_bf16 v[216:219], v[128:131], v[96:99], v[216:219]\n\t" \
  "v_mfma_f32_16x16x32_bf16 v[216:219], v[120:123], v[132:135], v[216:219]\n\t" \
  "v_mfma_f32_16x16x32_bf16 v[224:227], v[136:139], v[132:135], v[224:227]\n\t" \
  "v_mfma_f32_16x16x32_bf16 v[224:227], v[144:147], v[96:99], v[224:227]\n\t" \
  "v_mfma_f32_16x16x32_bf16 v[232:235], v[72:75], v[96:99], v[232:235]\n\t" \
  "v_mfma_f32_16x16x32_bf16 v[232:235], v[140:143], v[132:135], v[232:235]\n\t" \
  "v_mfma_f32_16x16x32_bf16 v[240:243], v[68:71], v[132:135], v[240:243]\n\t" \
  "v_mfma_f32_16x16x32_bf16 v[240:243], v[76:79], v[96:99], v[240:243]\n\t" \
  "v_mfma_f32_16x16x32_bf16 v[248:251], v[108:111], v[96:99], v[248:251]\n\t" \
  "v_mfma_f32_16x16x32_bf16 v[248:251], v[148:151], v[132:135], v[248:251]\n\t" \
  "v_mfma_f32_16x16x32_bf16 v[252:255], v[108:111], v[64:67], v[252:255]\n\t" \
  "global_store_dwordx4 v2, v[42:45], %24 offset:1024 sc1\n\t" \
  "s_waitcnt lgkmcnt(0)\n\t" \
  "v_mfma_f32_16x16x32_bf16 v[252:255], v[148:151], v[104:107], v[252:255]\n\t" \
  "v_mfma_f32_16x16x32_bf16 v[220:223], v[120:123], v[104:107], v[220:223]\n\t" \
  "v_mfma_f32_16x16x32_bf16 v[220:223], v[128:131], v[64:67], v[220:223]\n\t" \
  "v_mov_b32 v30, 0xff61b1e6\n\t" \
  "v_bfe_i32 v34, v3, 0, 1\n\t" \
  "s_barrier\n\t" \
  "v_mfma_f32_16x16x32_bf16 v[228:231], v[144:147], v[64:67], v[228:231]\n\t" \
  "ds_read_b64_tr_b16 v[100:101], v54 offset:26624\n\t" \
  "v_and_b32 v34, v30, v34\n\t" \
  "v_mfma_f32_16x16x32_bf16 v[228:231], v[136:139], v[104:107], v[228:231]\n\t" \
  "ds_read_b64_tr_b16 v[102:103], v54 offset:27136\n\t" \
  "v_bfe_i32 v38, v3, 4, 1\n\t" \
  "v_mfma_f32_16x16x32_bf16 v[236:239], v[140:143], v[104:107], v[236:239]\n\t" \
  "ds_read_b128 v[88:91], v52 offset:10240\n\t" \
  "v_and_b32 v38, v30, v38\n\t" \
  "v_mfma_f32_16x16x32_bf16 v[236:239], v[72:75], v[64:67], v[236:239]\n\t" \
  "ds_read_b64_tr_b16 v[80:81], v54 offset:30720\n\t" \
  "v_bfe_i32 v42, v3, 8, 1\n\t" \
  "v_mfma_f32_16x16x32_bf16 v[244:247], v[76:79], v[64:67], v[244:247]\n\t" \
  "ds_read_b64_tr_b16 v[82:83], v54 offset:31232\n\t" \
  "v_bfi_b32 v42, v42, v30, %15\n\t" \
  "v_mfma_f32_16x16x32_bf16 v[244:247], v[68:71], v[104:107], v[244:247]\n\t" \
  "ds_read_b128 v[124:127], v53 offset:10240\n\t" \
  "v_mov_b32 v48, 0x3f803f80\n\t" \
  "v_mfma_f32_16x16x32_bf16 v[152:155], v[92:95], v[128:131], v[152:155]\n\t" \
  "ds_read_b128 v[96:99], v53 offset:12288\n\t" \
  "v_bfe_i32 v35, v3, 1, 1\n\t" \
  "v_mfma_f32_16x16x32_bf16 v[152:155], v[112:115], v[120:123], v[152:155]\n\t" \
  "ds_read_b128 v[132:135], v52 offset:12288\n\t" \
  "v_and_b32 v35, v30, v35\n\t" \
  "v_mfma_f32_16x16x32_bf16 v[160:163], v[112:115], v[136:139], v[160:163]\n\t" \
  "ds_read_b128 v[108:111], v52 offset:14336\n\t" \
  "v_bfe_i32 v39, v3, 5, 1\n\t" \
  "v_mfma_f32_16x16x32_bf16 v[160:163], v[92:95], v[144:147], v[160:163]\n\t" \
  "ds_read_b128 v[148:151], v53 offset:14336\n\t" \
  "v_and_b32 v39, v30, v39\n\t" \
  "v_mfma_f32_16x16x32_bf16 v[168:171], v[92:95], v[72:75], v[168:171]\n\t" \
  "ds_read_b128 v[64:67], v53 offset:16384\n\t" \
  "v_bfe_i32 v43, v3, 9, 1\n\t" \
  "v_mfma_f32_16x16x32_bf16 v[168:171], v[112:115], v[140:143], v[168:171]\n\t" \
  "ds_read_b128 v[104:107], v52 offset:16384\n\t" \
  "v_bfi_b32 v43, v43, v30, %15\n\t" \
  "v_mfma_f32_16x16x32_bf16 v[176:179], v[112:115], v[68:71], v[176:179]\n\t" \
  "ds_read_b64_tr_b16 v[112:113], v55 offset:26624\n\t" \
  "v_mov_b32 v49, 0x3f803f80\n\t" \
  "v_mfma_f32_16x16x32_bf16 v[176:179], v[92:95], v[76:79], v[176:179]\n\t" \
  "ds_read_b64_tr_b16 v[114:115], v55 offset:27136\n\t" \
  "v_bfe_i32 v36, v3, 2, 1\n\t" \
  "v_mfma_f32_16x16x32_bf16 v[156:159], v[84:87], v[128:131], v[156:159]\n\t" \
  "ds_read_b64_tr_b16 v[92:93], v55 offset:30720\n\t" \
  "v_and_b32 v36, v30, v36\n\t" \
  "v_mfma_f32_16x16x32_bf16 v[156:159], v[116:119], v[120:123], v[156:159]\n\t" \
  "ds_read_b64_tr_b16 v[94:95], v55 offset:31232\n\t" \
  "v_bfe_i32 v40, v3, 6, 1\n\t" \
  "v_mfma_f32_16x16x32_bf16 v[164:167], v[116:119], v[136:139], v[164:167]\n\t" \
  "ds_read_b64_tr_b16 v[128:129], v54 offset:34816\n\t" \
  "v_and_b32 v40, v30, v40\n\t" \
  "v_mfma_f32_16x16x32_bf16 v[164:167], v[84:87], v[144:147], v[164:167]\n\t" \
  "ds_read_b64_tr_b16 v[130:131], v54 offset:35328\n\t" \
  "v_bfe_i32 v44, v3, 10, 1\n\t" \
  "v_mfma_f32_16x16x32_bf16 v[172:175], v[84:87], v[72:75], v[172:175]\n\t" \
  "ds_read_b64_tr_b16 v[120:121], v54 offset:38912\n\t" \
  "v_bfi_b32 v44, v44, v30, %15\n\t" \
  "v_mfma_f32_16x16x32_bf16 v[172:175], v[116:119], v[140:143], v[172:175]\n\t" \
  "ds_read_b64_tr_b16 v[122:123], v54 offset:39424\n\t" \
  "v_mov_b32 v50, 0x3f803f80\n\t" \
  "v_mfma_f32_16x16x32_bf16 v[180:183], v[116:119], v[68:71], v[180:183]\n\t" \
  "ds_read_b128 v[136:139], v52 offset:0\n\t" \
  "v_bfe_i32 v37, v3, 3, 1\n\t" \
  "v_mfma_f32_16x16x32_bf16 v[180:183], v[84:87], v[76:79], v[180:183]\n\t" \
  "ds_read_b128 v[144:147], v53 offset:0\n\t" \
  "v_and_b32 v37, v30, v37\n\t" \
  "s_waitcnt lgkmcnt(7)\n\t" \
  "v_mfma_f32_16x16x32_bf16 v[184:187], v[100:103], v[88:91], v[184:187]\n\t" \
  "ds_read_b64_tr_b16 v[72:73], v55 offset:34816\n\t" \
  "v_bfe_i32 v41, v3, 7, 1\n\t" \
  "v_mfma_f32_16x16x32_bf16 v[184:187], v[80:83], v[124:127], v[184:187]\n\t" \
  "ds_read_b64_tr_b16 v[74:75], v55 offset:35328\n\t" \
  "v_and_b32 v41, v30, v41\n\t" \
  "v_mfma_f32_16x16x32_bf16 v[192:195], v[80:83], v[96:99], v[192:195]\n\t" \
  "ds_read_b64_tr_b16 v[140:141], v55 offset:38912\n\t" \
  "v_bfe_i32 v45, v3, 11, 1\n\t" \
  "v_mfma_f32_16x16x32_bf16 v[192:195], v[100:103], v[132:135], v[192:195]\n\t" \
  "ds_read_b64_tr_b16 v[142:143], v55 offset:39424\n\t" \
  "v_bfi_b32 v45, v45, v30, %15\n\t" \
  "v_mfma_f32_16x16x32_bf16 v[200:203], v[100:103], v[108:111], v[200:203]\n\t" \
  "ds_read_b64_tr_b16 v[68:69], v54 offset:18432\n\t" \
  "v_mov_b32 v51, 0x3f803f80\n\t" \
  "v_mfma_f32_16x16x32_bf16 v[200:203], v[80:83], v[148:151], v[200:203]\n\t" \
  "ds_read_b64_tr_b16 v[70:71], v54 offset:18944\n\t" \
  "v_bfe_i32 v46, v3, 12, 1\n\t" \
  "v_mfma_f32_16x16x32_bf16 v[208:211], v[80:83], v[64:67], v[208:211]\n\t" \
  "ds_read_b64_tr_b16 v[116:117], v54 offset:22528\n\t" \
  "v_bfe_i32 v31, v3, 13, 1\n\t" \
  "v_mfma_f32_16x16x32_bf16 v[208:211], v[100:103], v[104:107], v[208:211]\n\t" \
  "ds_read_b64_tr_b16 v[118:119], v54 offset:23040\n\t" \
  "v_perm_b32 v46, v31, v46, vcc_hi\n\t" \
  "v_mfma_f32_16x16x32_bf16 v[188:191], v[112:115], v[88:91], v[188:191]\n\t" \
  "ds_read_b64_tr_b16 v[76:77], v55 offset:18432\n\t" \
  "v_bfe_i32 v47, v3, 14, 1\n\t" \
  "s_waitcnt lgkmcnt(7)\n\t" \
  "v_mfma_f32_16x16x32_bf16 v[188:191], v[92:95], v[124:127], v[188:191]\n\t" \
  "ds_read_b64_tr_b16 v[78:79], v55 offset:18944\n\t" \
  "v_bfe_i32 v31, v3, 15, 1\n\t" \
  "v_mfma_f32_16x16x32_bf16 v[196:199], v[92:95], v[96:99], v[196:199]\n\t" \
  "ds_read_b64_tr_b16 v[84:85], v55 offset:22528\n\t" \
  "v_perm_b32 v47, v31, v47, vcc_hi\n\t" \
  "v_mfma_f32_16x16x32_bf16 v[196:199], v[112:115], v[132:135], v[196:199]\n\t" \
  "ds_read_b64_tr_b16 v[86:87], v55 offset:23040\n\t" \
  "v_add_u32 v30, 18432, v4\n\t" \
  "v_mfma_f32_16x16x32_bf16 v[204:207], v[112:115], v[108:111], v[204:207]\n\t" \
  "v_xor_b32 v31, 1024, v30\n\t" \
  "v_mov_b32 v32, %12\n\t" \
  "v_mfma_f32_16x16x32_bf16 v[204:207], v[92:95], v[148:151], v[204:207]\n\t" \
  "v_xor_b32 v33, 4, v32\n\t" \
  "v_mov_b32 v0, 1\n\t" \
  "v_mfma_f32_16x16x32_bf16 v[212:215], v[92:95], v[64:67], v[212:215]\n\t" \
  "v_and_b32 v4, 1023, v4\n\t" \
  "v_add_u32 v1, 8192, v4\n\t" \
  "v_mfma_f32_16x16x32_bf16 v[212:215], v[112:115], v[104:107], v[212:215]\n\t" \
  "v_mfma_f32_16x16x32_bf16 v[216:219], v[88:91], v[128:131], v[216:219]\n\t" \
  "v_mfma_f32_16x16x32_bf16 v[216:219], v[124:127], v[120:123], v[216:219]\n\t" \
  "v_mfma_f32_16x16x32_bf16 v[224:227], v[96:99], v[120:123], v[224:227]\n\t" \
  "v_mfma_f32_16x16x32_bf16 v[224:227], v[132:135], v[128:131], v[224:227]\n\t" \
  "v_mfma_f32_16x16x32_bf16 v[232:235], v[108:111], v[128:131], v[232:235]\n\t" \
  "v_mfma_f32_16x16x32_bf16 v[232:235], v[148:151], v[120:123], v[232:235]\n\t" \
  "v_mfma_f32_16x16x32_bf16 v[240:243], v[64:67], v[120:123], v[240:243]\n\t" \
  "v_mfma_f32_16x16x32_bf16 v[240:243], v[104:107], v[128:131], v[240:243]\n\t" \
  "v_mfma_f32_16x16x32_bf16 v[248:251], v[136:139], v[128:131], v[248:251]\n\t" \
  "v_mfma_f32_16x16x32_bf16 v[248:251], v[144:147], v[120:123], v[248:251]\n\t" \
  "v_mfma_f32_16x16x32_bf16 v[252:255], v[136:139], v[72:75], v[252:255]\n\t" \
  "s_waitcnt lgkmcnt(0)\n\t" \
  "v_mfma_f32_16x16x32_bf16 v[252:255], v[144:147], v[140:143], v[252:255]\n\t" \
  "v_mfma_f32_16x16x32_bf16 v[220:223], v[124:127], v[140:143], v[220:223]\n\t" \
  "v_mfma_f32_16x16x32_bf16 v[220:223], v[88:91], v[72:75], v[220:223]\n\t" \
  "s_barrier\n\t" \
  "v_mfma_f32_16x16x32_bf16 v[228:231], v[132:135], v[72:75], v[228:231]\n\t" \
  "ds_read_b64_tr_b16 v[80:81], v58 offset:26624\n\t" \
  "v_mfma_f32_16x16x32_bf16 v[228:231], v[96:99], v[140:143], v[228:231]\n\t" \
  "ds_read_b64_tr_b16 v[82:83], v58 offset:27136\n\t" \
  "v_mfma_f32_16x16x32_bf16 v[236:239], v[148:151], v[140:143], v[236:239]\n\t" \
  "ds_read_b128 v[100:103], v56 offset:10240\n\t" \
  "v_mfma_f32_16x16x32_bf16 v[236:239], v[108:111], v[72:75], v[236:239]\n\t" \
  "ds_read_b64_tr_b16 v[92:93], v58 offset:30720\n\t" \
  "v_mfma_f32_16x16x32_bf16 v[244:247], v[104:107], v[72:75], v[244:247]\n\t" \
  "ds_read_b64_tr_b16 v[94:95], v58 offset:31232\n\t" \
  "v_mfma_f32_16x16x32_bf16 v[244:247], v[64:67], v[140:143], v[244:247]\n\t" \
  "ds_read_b128 v[112:115], v57 offset:10240\n\t" \
  "v_mfma_f32_16x16x32_bf16 v[152:155], v[68:71], v[88:91], v[152:155]\n\t" \
  "ds_read_b128 v[128:131], v57 offset:12288\n\t" \
  "v_mfma_f32_16x16x32_bf16 v[152:155], v[116:119], v[124:127], v[152:155]\n\t" \
  "ds_read_b128 v[120:123], v56 offset:12288\n\t" \
  "v_mfma_f32_16x16x32_bf16 v[160:163], v[116:119], v[96:99], v[160:163]\n\t" \
  "ds_read_b128 v[136:139], v56 offset:14336\n\t" \
  "v_mfma_f32_16x16x32_bf16 v[160:163], v[68:71], v[132:135], v[160:163]\n\t" \
  "ds_read_b128 v[144:147], v57 offset:14336\n\t" \
  "v_mfma_f32_16x16x32_bf16 v[168:171], v[68:71], v[108:111], v[168:171]\n\t" \
  "ds_read_b128 v[72:75], v57 offset:16384\n\t" \
  "v_mfma_f32_16x16x32_bf16 v[168:171], v[116:119], v[148:151], v[168:171]\n\t" \
  "ds_read_b128 v[140:143], v56 offset:16384\n\t" \
  "v_mfma_f32_16x16x32_bf16 v[176:179], v[116:119], v[64:67], v[176:179]\n\t" \
  "ds_read_b64_tr_b16 v[116:117], v59 offset:26624\n\t" \
  "v_mfma_f32_16x16x32_bf16 v[176:179], v[68:71], v[104:107], v[176:179]\n\t" \
  "ds_read_b64_tr_b16 v[118:119], v59 offset:27136\n\t" \
  "v_mfma_f32_16x16x32_bf16 v[156:159], v[76:79], v[88:91], v[156:159]\n\t" \
  "ds_read_b64_tr_b16 v[68:69], v59 offset:30720\n\t" \
  "v_mfma_f32_16x16x32_bf16 v[156:159], v[84:87], v[124:127], v[156:159]\n\t" \
  "ds_read_b64_tr_b16 v[70:71], v59 offset:31232\n\t" \
  "v_mfma_f32_16x16x32_bf16 v[164:167], v[84:87], v[96:99], v[164:167]\n\t" \
  "ds_read_b64_tr_b16 v[88:89], v58 offset:34816\n\t" \
  "v_mfma_f32_16x16x32_bf16 v[164:167], v[76:79], v[132:135], v[164:167]\n\t" \
  "ds_read_b64_tr_b16 v[90:91], v58 offset:35328\n\t" \
  "v_mfma_f32_16x16x32_bf16 v[172:175], v[76:79], v[108:111], v[172:175]\n\t" \
  "ds_read_b64_tr_b16 v[124:125], v58 offset:38912\n\t" \
  "v_mfma_f32_16x16x32_bf16 v[172:175], v[84:87], v[148:151], v[172:175]\n\t" \
  "ds_read_b64_tr_b16 v[126:127], v58 offset:39424\n\t" \
  "v_mfma_f32_16x16x32_bf16 v[180:183], v[84:87], v[64:67], v[180:183]\n\t" \
  "ds_read_b128 v[96:99], v56 offset:0\n\t" \
  "v_mfma_f32_16x16x32_bf16 v[180:183], v[76:79], v[104:107], v[180:183]\n\t" \
  "ds_read_b128 v[132:135], v57 offset:0\n\t" \
  "s_waitcnt lgkmcnt(7)\n\t" \
  "v_mfma_f32_16x16x32_bf16 v[184:187], v[80:83], v[100:103], v[184:187]\n\t" \
  "ds_read_b64_tr_b16 v[108:109], v59 offset:34816\n\t" \
  "v_mfma_f32_16x16x32_bf16 v[184:187], v[92:95], v[112:115], v[184:187]\n\t" \
  "ds_read_b64_tr_b16 v[110:111], v59 offset:35328\n\t" \
  "v_mfma_f32_16x16x32_bf16 v[192:195], v[92:95], v[128:131], v[192:195]\n\t" \
  "ds_read_b64_tr_b16 v[148:149], v59 offset:38912\n\t" \
  "v_mfma_f32_16x16x32_bf16 v[192:195], v[80:83], v[120:123], v[192:195]\n\t" \
  "ds_read_b64_tr_b16 v[150:151], v59 offset:39424\n\t" \
  "v_mfma_f32_16x16x32_bf16 v[200:203], v[80:83], v[136:139], v[200:203]\n\t" \
  "ds_read_b64_tr_b16 v[64:65], v58 offset:18432\n\t" \
  "v_mfma_f32_16x16x32_bf16 v[200:203], v[92:95], v[144:147], v[200:203]\n\t" \
  "ds_read_b64_tr_b16 v[66:67], v58 offset:18944\n\t" \
  "v_mfma_f32_16x16x32_bf16 v[208:211], v[92:95], v[72:75], v[208:211]\n\t" \
  "ds_read_b64_tr_b16 v[84:85], v58 offset:22528\n\t" \
  "v_mfma_f32_16x16x32_bf16 v[208:211], v[80:83], v[140:143], v[208:211]\n\t" \
  "ds_read_b64_tr_b16 v[86:87], v58 offset:23040\n\t" \
  "v_mfma_f32_16x16x32_bf16 v[188:191], v[116:119], v[100:103], v[188:191]\n\t" \
  "ds_read_b64_tr_b16 v[76:77], v59 offset:18432\n\t" \
  "s_waitcnt lgkmcnt(7)\n\t" \
  "v_mfma_f32_16x16x32_bf16 v[188:191], v[68:71], v[112:115], v[188:191]\n\t" \
  "ds_read_b64_tr_b16 v[78:79], v59 offset:18944\n\t" \
  "v_mfma_f32_16x16x32_bf16 v[196:199], v[68:71], v[128:131], v[196:199]\n\t" \
  "ds_read_b64_tr_b16 v[104:105], v59 offset:22528\n\t" \
  "v_mfma_f32_16x16x32_bf16 v[196:199], v[116:119], v[120:123], v[196:199]\n\t" \
  "ds_read_b64_tr_b16 v[106:107], v59 offset:23040\n\t" \
  "v_mfma_f32_16x16x32_bf16 v[204:207], v[116:119], v[136:139], v[204:207]\n\t" \
  "v_mfma_f32_16x16x32_bf16 v[204:207], v[68:71], v[144:147], v[204:207]\n\t" \
  "v_mfma_f32_16x16x32_bf16 v[212:215], v[68:71], v[72:75], v[212:215]\n\t" \
  "v_mfma_f32_16x16x32_bf16 v[212:215], v[116:119], v[140:143], v[212:215]\n\t" \
  "v_mfma_f32_16x16x32_bf16 v[216:219], v[100:103], v[88:91], v[216:219]\n\t" \
  "v_mfma_f32_16x16x32_bf16 v[216:219], v[112:115], v[124:127], v[216:219]\n\t" \
  "v_mfma_f32_16x16x32_bf16 v[224:227], v[128:131], v[124:127], v[224:227]\n\t" \
  "v_mfma_f32_16x16x32_bf16 v[224:227], v[120:123], v[88:91], v[224:227]\n\t" \
  "v_mfma_f32_16x16x32_bf16 v[232:235], v[136:139], v[88:91], v[232:235]\n\t" \
  "v_mfma_f32_16x16x32_bf16 v[232:235], v[144:147], v[124:127], v[232:235]\n\t" \
  "v_mfma_f32_16x16x32_bf16 v[240:243], v[72:75], v[124:127], v[240:243]\n\t" \
  "v_mfma_f32_16x16x32_bf16 v[240:243], v[140:143], v[88:91], v[240:243]\n\t" \
  "v_mfma_f32_16x16x32_bf16 v[248:251], v[96:99], v[88:91], v[248:251]\n\t" \
  "v_mfma_f32_16x16x32_bf16 v[248:251], v[132:135], v[124:127], v[248:251]\n\t" \
  "v_mfma_f32_16x16x32_bf16 v[252:255], v[96:99], v[108:111], v[252:255]\n\t" \
  "s_waitcnt lgkmcnt(0)\n\t" \
  "v_mfma_f32_16x16x32_bf16 v[252:255], v[132:135], v[148:151], v[252:255]\n\t" \
  "v_mfma_f32_16x16x32_bf16 v[220:223], v[112:115], v[148:151], v[220:223]\n\t" \
  "v_mfma_f32_16x16x32_bf16 v[220:223], v[100:103], v[108:111], v[220:223]\n\t" \
  "s_waitcnt vmcnt(0)\n\t" \
  "s_barrier\n\t" \
  "v_mfma_f32_16x16x32_bf16 v[228:231], v[120:123], v[108:111], v[228:231]\n\t" \
  "ds_read_b64_tr_b16 v[92:93], v62 offset:26624\n\t" \
  "v_mfma_f32_16x16x32_bf16 v[228:231], v[128:131], v[148:151], v[228:231]\n\t" \
  "ds_read_b64_tr_b16 v[94:95], v62 offset:27136\n\t" \
  "v_mfma_f32_16x16x32_bf16 v[236:239], v[144:147], v[148:151], v[236:239]\n\t" \
  "ds_read_b128 v[80:83], v60 offset:10240\n\t" \
  "v_mfma_f32_16x16x32_bf16 v[236:239], v[136:139], v[108:111], v[236:239]\n\t" \
  "ds_read_b64_tr_b16 v[68:69], v62 offset:30720\n\t" \
  "v_mfma_f32_16x16x32_bf16 v[244:247], v[140:143], v[108:111], v[244:247]\n\t" \
  "ds_read_b64_tr_b16 v[70:71], v62 offset:31232\n\t" \
  "v_mfma_f32_16x16x32_bf16 v[244:247], v[72:75], v[148:151], v[244:247]\n\t" \
  "ds_read_b128 v[116:119], v61 offset:10240\n\t" \
  "v_mfma_f32_16x16x32_bf16 v[152:155], v[64:67], v[100:103], v[152:155]\n\t" \
  "ds_read_b128 v[88:91], v61 offset:12288\n\t" \
  "v_mfma_f32_16x16x32_bf16 v[152:155], v[84:87], v[112:115], v[152:155]\n\t" \
  "ds_read_b128 v[124:127], v60 offset:12288\n\t" \
  "v_mfma_f32_16x16x32_bf16 v[160:163], v[84:87], v[128:131], v[160:163]\n\t" \
  "ds_read_b128 v[96:99], v60 offset:14336\n\t" \
  "v_mfma_f32_16x16x32_bf16 v[160:163], v[64:67], v[120:123], v[160:163]\n\t" \
  "ds_read_b128 v[132:135], v61 offset:14336\n\t" \
  "v_mfma_f32_16x16x32_bf16 v[168:171], v[64:67], v[136:139], v[168:171]\n\t" \
  "ds_read_b128 v[108:111], v61 offset:16384\n\t" \
  "v_mfma_f32_16x16x32_bf16 v[168:171], v[84:87], v[144:147], v[168:171]\n\t" \
  "ds_read_b128 v[148:151], v60 offset:16384\n\t" \
  "v_mfma_f32_16x16x32_bf16 v[176:179], v[84:87], v[72:75], v[176:179]\n\t" \
  "ds_read_b64_tr_b16 v[84:85], v63 offset:26624\n\t" \
  "v_mfma_f32_16x16x32_bf16 v[176:179], v[64:67], v[140:143], v[176:179]\n\t" \
  "ds_read_b64_tr_b16 v[86:87], v63 offset:27136\n\t" \
  "v_mfma_f32_16x16x32_bf16 v[156:159], v[76:79], v[100:103], v[156:159]\n\t" \
  "ds_read_b64_tr_b16 v[64:65], v63 offset:30720\n\t" \
  "v_mfma_f32_16x16x32_bf16 v[156:159], v[104:107], v[112:115], v[156:159]\n\t" \
  "ds_read_b64_tr_b16 v[66:67], v63 offset:31232\n\t" \
  "v_mfma_f32_16x16x32_bf16 v[164:167], v[104:107], v[128:131], v[164:167]\n\t" \
  "ds_read_b64_tr_b16 v[100:101], v62 offset:34816\n\t" \
  "v_mfma_f32_16x16x32_bf16 v[164:167], v[76:79], v[120:123], v[164:167]\n\t" \
  "ds_read_b64_tr_b16 v[102:103], v62 offset:35328\n\t" \
  "v_mfma_f32_16x16x32_bf16 v[172:175], v[76:79], v[136:139], v[172:175]\n\t" \
  "ds_read_b64_tr_b16 v[112:113], v62 offset:38912\n\t" \
  "v_mfma_f32_16x16x32_bf16 v[172:175], v[104:107], v[144:147], v[172:175]\n\t" \
  "ds_read_b64_tr_b16 v[114:115], v62 offset:39424\n\t" \
  "v_mfma_f32_16x16x32_bf16 v[180:183], v[104:107], v[72:75], v[180:183]\n\t" \
  "ds_read_b128 v[128:131], v60 offset:0\n\t" \
  "v_mfma_f32_16x16x32_bf16 v[180:183], v[76:79], v[140:143], v[180:183]\n\t" \
  "ds_read_b128 v[120:123], v61 offset:0\n\t" \
  "s_waitcnt lgkmcnt(7)\n\t" \
  "v_mfma_f32_16x16x32_bf16 v[184:187], v[92:95], v[80:83], v[184:187]\n\t" \
  "ds_read_b64_tr_b16 v[136:137], v63 offset:34816\n\t" \
  "v_mfma_f32_16x16x32_bf16 v[184:187], v[68:71], v[116:119], v[184:187]\n\t" \
  "ds_read_b64_tr_b16 v[138:139], v63 offset:35328\n\t" \
  "v_mfma_f32_16x16x32_bf16 v[192:195], v[68:71], v[88:91], v[192:195]\n\t" \
  "ds_read_b64_tr_b16 v[144:145], v63 offset:38912\n\t" \
  "v_mfma_f32_16x16x32_bf16 v[192:195], v[92:95], v[124:127], v[192:195]\n\t" \
  "ds_read_b64_tr_b16 v[146:147], v63 offset:39424\n\t" \
  "v_mfma_f32_16x16x32_bf16 v[200:203], v[92:95], v[96:99], v[200:203]\n\t" \
  "ds_read_b64_tr_b16 v[72:73], v62 offset:18432\n\t" \
  "v_mfma_f32_16x16x32_bf16 v[200:203], v[68:71], v[132:135], v[200:203]\n\t" \
  "ds_read_b64_tr_b16 v[74:75], v62 offset:18944\n\t" \
  "v_mfma_f32_16x16x32_bf16 v[208:211], v[68:71], v[108:111], v[208:211]\n\t" \
  "ds_read_b64_tr_b16 v[104:105], v62 offset:22528\n\t" \
  "v_mfma_f32_16x16x32_bf16 v[208:211], v[92:95], v[148:151], v[208:211]\n\t" \
  "ds_read_b64_tr_b16 v[106:107], v62 offset:23040\n\t" \
  "v_mfma_f32_16x16x32_bf16 v[188:191], v[84:87], v[80:83], v[188:191]\n\t" \
  "ds_read_b64_tr_b16 v[76:77], v63 offset:18432\n\t" \
  "s_waitcnt lgkmcnt(7)\n\t" \
  "v_mfma_f32_16x16x32_bf16 v[188:191], v[64:67], v[116:119], v[188:191]\n\t" \
  "ds_read_b64_tr_b16 v[78:79], v63 offset:18944\n\t" \
  "v_mfma_f32_16x16x32_bf16 v[196:199], v[64:67], v[88:91], v[196:199]\n\t" \
  "ds_read_b64_tr_b16 v[140:141], v63 offset:22528\n\t" \
  "v_mfma_f32_16x16x32_bf16 v[196:199], v[84:87], v[124:127], v[196:199]\n\t" \
  "ds_read_b64_tr_b16 v[142:143], v63 offset:23040\n\t" \
  "v_mfma_f32_16x16x32_bf16 v[204:207], v[84:87], v[96:99], v[204:207]\n\t" \
  "v_mfma_f32_16x16x32_bf16 v[204:207], v[64:67], v[132:135], v[204:207]\n\t" \
  "v_mfma_f32_16x16x32_bf16 v[212:215], v[64:67], v[108:111], v[212:215]\n\t" \
  "v_mfma_f32_16x16x32_bf16 v[212:215], v[84:87], v[148:151], v[212:215]\n\t" \
  "v_mfma_f32_16x16x32_bf16 v[216:219], v[80:83], v[100:103], v[216:219]\n\t" \
  "v_mfma_f32_16x16x32_bf16 v[216:219], v[116:119], v[112:115], v[216:219]\n\t" \
  "v_mfma_f32_16x16x32_bf16 v[224:227], v[88:91], v[112:115], v[224:227]\n\t" \
  "v_mfma_f32_16x16x32_bf16 v[224:227], v[124:127], v[100:103], v[224:227]\n\t" \
  "v_mfma_f32_16x16x32_bf16 v[232:235], v[96:99], v[100:103], v[232:235]\n\t" \
  "v_mfma_f32_16x16x32_bf16 v[232:235], v[132:135], v[112:115], v[232:235]\n\t" \
  "v_mfma_f32_16x16x32_bf16 v[240:243], v[108:111], v[112:115], v[240:243]\n\t" \
  "v_mfma_f32_16x16x32_bf16 v[240:243], v[148:151], v[100:103], v[240:243]\n\t" \
  "501:\n\t" \
  "s_cbranch_execz 505f\n\t" \
  "v_mfma_f32_16x16x32_bf16 v[248:251], v[128:131], v[100:103], v[248:251]\n\t" \
  "v_mfma_f32_16x16x32_bf16 v[248:251], v[120:123], v[112:115], v[248:251]\n\t" \
  "v_mfma_f32_16x16x32_bf16 v[252:255], v[128:131], v[136:139], v[252:255]\n\t" \
  "s_waitcnt lgkmcnt(0)\n\t" \
  "v_mfma_f32_16x16x32_bf16 v[252:255], v[120:123], v[144:147], v[252:255]\n\t" \
  "v_mfma_f32_16x16x32_bf16 v[220:223], v[116:119], v[144:147], v[220:223]\n\t" \
  "v_mfma_f32_16x16x32_bf16 v[220:223], v[80:83], v[136:139], v[220:223]\n\t" \
  "s_barrier\n\t" \
  "v_mfma_f32_16x16x32_bf16 v[228:231], v[124:127], v[136:139], v[228:231]\n\t" \
  "ds_read_b64_tr_b16 v[68:69], v54 offset:26624\n\t" \
  "v_mfma_f32_16x16x32_bf16 v[228:231], v[88:91], v[144:147], v[228:231]\n\t" \
  "ds_read_b64_tr_b16 v[70:71], v54 offset:27136\n\t" \
  "v_mfma_f32_16x16x32_bf16 v[236:239], v[132:135], v[144:147], v[236:239]\n\t" \
  "ds_read_b128 v[92:95], v52 offset:10240\n\t" \
  "v_mfma_f32_16x16x32_bf16 v[236:239], v[96:99], v[136:139], v[236:239]\n\t" \
  "ds_read_b64_tr_b16 v[64:65], v54 offset:30720\n\t" \
  "v_mfma_f32_16x16x32_bf16 v[244:247], v[148:151], v[136:139], v[244:247]\n\t" \
  "ds_read_b64_tr_b16 v[66:67], v54 offset:31232\n\t" \
  "v_mfma_f32_16x16x32_bf16 v[244:247], v[108:111], v[144:147], v[244:247]\n\t" \
  "ds_read_b128 v[84:87], v53 offset:10240\n\t" \
  "v_mfma_f32_16x16x32_bf16 v[152:155], v[72:75], v[80:83], v[152:155]\n\t" \
  "ds_read_b128 v[100:103], v53 offset:12288\n\t" \
  "v_mfma_f32_16x16x32_bf16 v[152:155], v[104:107], v[116:119], v[152:155]\n\t" \
  "ds_read_b128 v[112:115], v52 offset:12288\n\t" \
  "v_mfma_f32_16x16x32_bf16 v[160:163], v[104:107], v[88:91], v[160:163]\n\t" \
  "ds_read_b128 v[128:131], v52 offset:14336\n\t" \
  "v_mfma_f32_16x16x32_bf16 v[160:163], v[72:75], v[124:127], v[160:163]\n\t" \
  "ds_read_b128 v[120:123], v53 offset:14336\n\t" \
  "v_mfma_f32_16x16x32_bf16 v[168:171], v[72:75], v[96:99], v[168:171]\n\t" \
  "ds_read_b128 v[136:139], v53 offset:16384\n\t" \
  "v_mfma_f32_16x16x32_bf16 v[168:171], v[104:107], v[132:135], v[168:171]\n\t" \
  "ds_read_b128 v[144:147], v52 offset:16384\n\t" \
  "v_mfma_f32_16x16x32_bf16 v[176:179], v[104:107], v[108:111], v[176:179]\n\t" \
  "ds_read_b64_tr_b16 v[104:105], v55 offset:26624\n\t" \
  "v_mfma_f32_16x16x32_bf16 v[176:179], v[72:75], v[148:151], v[176:179]\n\t" \
  "ds_read_b64_tr_b16 v[106:107], v55 offset:27136\n\t" \
  "v_mfma_f32_16x16x32_bf16 v[156:159], v[76:79], v[80:83], v[156:159]\n\t" \
  "ds_read_b64_tr_b16 v[72:73], v55 offset:30720\n\t" \
  "v_mfma_f32_16x16x32_bf16 v[156:159], v[140:143], v[116:119], v[156:159]\n\t" \
  "ds_read_b64_tr_b16 v[74:75], v55 offset:31232\n\t" \
  "v_mfma_f32_16x16x32_bf16 v[164:167], v[140:143], v[88:91], v[164:167]\n\t" \
  "ds_read_b64_tr_b16 v[80:81], v54 offset:34816\n\t" \
  "v_mfma_f32_16x16x32_bf16 v[164:167], v[76:79], v[124:127], v[164:167]\n\t" \
  "ds_read_b64_tr_b16 v[82:83], v54 offset:35328\n\t" \
  "v_mfma_f32_16x16x32_bf16 v[172:175], v[76:79], v[96:99], v[172:175]\n\t" \
  "ds_read_b64_tr_b16 v[116:117], v54 offset:38912\n\t" \
  "v_mfma_f32_16x16x32_bf16 v[172:175], v[140:143], v[132:135], v[172:175]\n\t" \
  "ds_read_b64_tr_b16 v[118:119], v54 offset:39424\n\t" \
  "v_mfma_f32_16x16x32_bf16 v[180:183], v[140:143], v[108:111], v[180:183]\n\t" \
  "ds_read_b128 v[88:91], v52 offset:0\n\t" \
  "v_mfma_f32_16x16x32_bf16 v[180:183], v[76:79], v[148:151], v[180:183]\n\t" \
  "ds_read_b128 v[124:127], v53 offset:0\n\t" \
  "s_waitcnt lgkmcnt(7)\n\t" \
  "v_mfma_f32_16x16x32_bf16 v[184:187], v[68:71], v[92:95], v[184:187]\n\t" \
  "ds_read_b64_tr_b16 v[96:97], v55 offset:34816\n\t" \
  "v_mfma_f32_16x16x32_bf16 v[184:187], v[64:67], v[84:87], v[184:187]\n\t" \
  "ds_read_b64_tr_b16 v[98:99], v55 offset:35328\n\t" \
  "v_mfma_f32_16x16x32_bf16 v[192:195], v[64:67], v[100:103], v[192:195]\n\t" \
  "ds_read_b64_tr_b16 v[132:133], v55 offset:38912\n\t" \
  "v_mfma_f32_16x16x32_bf16 v[192:195], v[68:71], v[112:115], v[192:195]\n\t" \
  "ds_read_b64_tr_b16 v[134:135], v55 offset:39424\n\t" \
  "v_mfma_f32_16x16x32_bf16 v[200:203], v[68:71], v[128:131], v[200:203]\n\t" \
  "ds_read_b64_tr_b16 v[108:109], v54 offset:18432\n\t" \
  "v_mfma_f32_16x16x32_bf16 v[200:203], v[64:67], v[120:123], v[200:203]\n\t" \
  "ds_read_b64_tr_b16 v[110:111], v54 offset:18944\n\t" \
  "v_mfma_f32_16x16x32_bf16 v[208:211], v[64:67], v[136:139], v[208:211]\n\t" \
  "ds_read_b64_tr_b16 v[140:141], v54 offset:22528\n\t" \
  "v_mfma_f32_16x16x32_bf16 v[208:211], v[68:71], v[144:147], v[208:211]\n\t" \
  "ds_read_b64_tr_b16 v[142:143], v54 offset:23040\n\t" \
  "v_mfma_f32_16x16x32_bf16 v[188:191], v[104:107], v[92:95], v[188:191]\n\t" \
  "ds_read_b64_tr_b16 v[76:77], v55 offset:18432\n\t" \
  "s_waitcnt lgkmcnt(7)\n\t" \
  "v_mfma_f32_16x16x32_bf16 v[188:191], v[72:75], v[84:87], v[188:191]\n\t" \
  "ds_read_b64_tr_b16 v[78:79], v55 offset:18944\n\t" \
  "v_mfma_f32_16x16x32_bf16 v[196:199], v[72:75], v[100:103], v[196:199]\n\t" \
  "ds_read_b64_tr_b16 v[148:149], v55 offset:22528\n\t" \
  "v_mfma_f32_16x16x32_bf16 v[196:199], v[104:107], v[112:115], v[196:199]\n\t" \
  "ds_read_b64_tr_b16 v[150:151], v55 offset:23040\n\t" \
  "v_mfma_f32_16x16x32_bf16 v[204:207], v[104:107], v[128:131], v[204:207]\n\t" \
  "v_mfma_f32_16x16x32_bf16 v[204:207], v[72:75], v[120:123], v[204:207]\n\t" \
  "v_mfma_f32_16x16x32_bf16 v[212:215], v[72:75], v[136:139], v[212:215]\n\t" \
  "v_mfma_f32_16x16x32_bf16 v[212:215], v[104:107], v[144:147], v[212:215]\n\t" \
  "v_mfma_f32_16x16x32_bf16 v[216:219], v[92:95], v[80:83], v[216:219]\n\t" \
  "v_mfma_f32_16x16x32_bf16 v[216:219], v[84:87], v[116:119], v[216:219]\n\t" \
  "v_mfma_f32_16x16x32_bf16 v[224:227], v[100:103], v[116:119], v[224:227]\n\t" \
  "v_mfma_f32_16x16x32_bf16 v[224:227], v[112:115], v[80:83], v[224:227]\n\t" \
  "v_mfma_f32_16x16x32_bf16 v[232:235], v[128:131], v[80:83], v[232:235]\n\t" \
  "v_mfma_f32_16x16x32_bf16 v[232:235], v[120:123], v[116:119], v[232:235]\n\t" \
  "v_mfma_f32_16x16x32_bf16 v[240:243], v[136:139], v[116:119], v[240:243]\n\t" \
  "v_mfma_f32_16x16x32_bf16 v[240:243], v[144:147], v[80:83], v[240:243]\n\t" \
  "v_mfma_f32_16x16x32_bf16 v[248:251], v[88:91], v[80:83], v[248:251]\n\t" \
  "v_mfma_f32_16x16x32_bf16 v[248:251], v[124:127], v[116:119], v[248:251]\n\t" \
  "v_mfma_f32_16x16x32_bf16 v[252:255], v[88:91], v[96:99], v[252:255]\n\t" \
  "s_waitcnt lgkmcnt(0)\n\t" \
  "v_mfma_f32_16x16x32_bf16 v[252:255], v[124:127], v[132:135], v[252:255]\n\t" \
  "v_mfma_f32_16x16x32_bf16 v[220:223], v[84:87], v[132:135], v[220:223]\n\t" \
  "v_mfma_f32_16x16x32_bf16 v[220:223], v[92:95], v[96:99], v[220:223]\n\t" \
  "s_barrier\n\t" \
  "v_mfma_f32_16x16x32_bf16 v[228:231], v[112:115], v[96:99], v[228:231]\n\t" \
  "ds_read_b64_tr_b16 v[64:65], v58 offset:26624\n\t" \
  "v_mfma_f32_16x16x32_bf16 v[228:231], v[100:103], v[132:135], v[228:231]\n\t" \
  "ds_read_b64_tr_b16 v[66:67], v58 offset:27136\n\t" \
  "v_mfma_f32_16x16x32_bf16 v[236:239], v[120:123], v[132:135], v[236:239]\n\t" \
  "ds_read_b128 v[68:71], v56 offset:10240\n\t" \
  "v_mfma_f32_16x16x32_bf16 v[236:239], v[128:131], v[96:99], v[236:239]\n\t" \
  "ds_read_b64_tr_b16 v[72:73], v58 offset:30720\n\t" \
  "v_mfma_f32_16x16x32_bf16 v[244:247], v[144:147], v[96:99], v[244:247]\n\t" \
  "ds_read_b64_tr_b16 v[74:75], v58 offset:31232\n\t" \
  "v_mfma_f32_16x16x32_bf16 v[244:247], v[136:139], v[132:135], v[244:247]\n\t" \
  "ds_read_b128 v[104:107], v57 offset:10240\n\t" \
  "v_mfma_f32_16x16x32_bf16 v[152:155], v[108:111], v[92:95], v[152:155]\n\t" \
  "ds_read_b128 v[80:83], v57 offset:12288\n\t" \
  "v_mfma_f32_16x16x32_bf16 v[152:155], v[140:143], v[84:87], v[152:155]\n\t" \
  "ds_read_b128 v[116:119], v56 offset:12288\n\t" \
  "v_mfma_f32_16x16x32_bf16 v[160:163], v[140:143], v[100:103], v[160:163]\n\t" \
  "ds_read_b128 v[88:91], v56 offset:14336\n\t" \
  "v_mfma_f32_16x16x32_bf16 v[160:163], v[108:111], v[112:115], v[160:163]\n\t" \
  "ds_read_b128 v[124:127], v57 offset:14336\n\t" \
  "v_mfma_f32_16x16x32_bf16 v[168:171], v[108:111], v[128:131], v[168:171]\n\t" \
  "ds_read_b128 v[96:99], v57 offset:16384\n\t" \
  "v_mfma_f32_16x16x32_bf16 v[168:171], v[140:143], v[120:123], v[168:171]\n\t" \
  "ds_read_b128 v[132:135], v56 offset:16384\n\t" \
  "v_mfma_f32_16x16x32_bf16 v[176:179], v[140:143], v[136:139], v[176:179]\n\t" \
  "ds_read_b64_tr_b16 v[140:141], v59 offset:26624\n\t" \
  "v_mfma_f32_16x16x32_bf16 v[176:179], v[108:111], v[144:147], v[176:179]\n\t" \
  "ds_read_b64_tr_b16 v[142:143], v59 offset:27136\n\t" \
  "v_mfma_f32_16x16x32_bf16 v[156:159], v[76:79], v[92:95], v[156:159]\n\t" \
  "ds_read_b64_tr_b16 v[108:109], v59 offset:30720\n\t" \
  "v_mfma_f32_16x16x32_bf16 v[156:159], v[148:151], v[84:87], v[156:159]\n\t" \
  "ds_read_b64_tr_b16 v[110:111], v59 offset:31232\n\t" \
  "v_mfma_f32_16x16x32_bf16 v[164:167], v[148:151], v[100:103], v[164:167]\n\t" \
  "ds_read_b64_tr_b16 v[92:93], v58 offset:34816\n\t" \
  "v_mfma_f32_16x16x32_bf16 v[164:167], v[76:79], v[112:115], v[164:167]\n\t" \
  "ds_read_b64_tr_b16 v[94:95], v58 offset:35328\n\t" \
  "v_mfma_f32_16x16x32_bf16 v[172:175], v[76:79], v[128:131], v[172:175]\n\t" \
  "ds_read_b64_tr_b16 v[84:85], v58 offset:38912\n\t" \
  "v_mfma_f32_16x16x32_bf16 v[172:175], v[148:151], v[120:123], v[172:175]\n\t" \
  "ds_read_b64_tr_b16 v[86:87], v58 offset:39424\n\t" \
  "v_mfma_f32_16x16x32_bf16 v[180:183], v[148:151], v[136:139], v[180:183]\n\t" \
  "ds_read_b128 v[100:103], v56 offset:0\n\t" \
  "v_mfma_f32_16x16x32_bf16 v[180:183], v[76:79], v[144:147], v[180:183]\n\t" \
  "ds_read_b128 v[112:115], v57 offset:0\n\t" \
  "s_waitcnt lgkmcnt(7)\n\t" \
  "v_mfma_f32_16x16x32_bf16 v[184:187], v[64:67], v[68:71], v[184:187]\n\t" \
  "ds_read_b64_tr_b16 v[128:129], v59 offset:34816\n\t" \
  "v_mfma_f32_16x16x32_bf16 v[184:187], v[72:75], v[104:107], v[184:187]\n\t" \
  "ds_read_b64_tr_b16 v[130:131], v59 offset:35328\n\t" \
  "v_mfma_f32_16x16x32_bf16 v[192:195], v[72:75], v[80:83], v[192:195]\n\t" \
  "ds_read_b64_tr_b16 v[120:121], v59 offset:38912\n\t" \
  "v_mfma_f32_16x16x32_bf16 v[192:195], v[64:67], v[116:119], v[192:195]\n\t" \
  "ds_read_b64_tr_b16 v[122:123], v59 offset:39424\n\t" \
  "v_mfma_f32_16x16x32_bf16 v[200:203], v[64:67], v[88:91], v[200:203]\n\t" \
  "ds_read_b64_tr_b16 v[136:137], v58 offset:18432\n\t" \
  "v_mfma_f32_16x16x32_bf16 v[200:203], v[72:75], v[124:127], v[200:203]\n\t" \
  "ds_read_b64_tr_b16 v[138:139], v58 offset:18944\n\t" \
  "v_mfma_f32_16x16x32_bf16 v[208:211], v[72:75], v[96:99], v[208:211]\n\t" \
  "ds_read_b64_tr_b16 v[148:149], v58 offset:22528\n\t" \
  "v_mfma_f32_16x16x32_bf16 v[208:211], v[64:67], v[132:135], v[208:211]\n\t" \
  "ds_read_b64_tr_b16 v[150:151], v58 offset:23040\n\t" \
  "v_mfma_f32_16x16x32_bf16 v[188:191], v[140:143], v[68:71], v[188:191]\n\t" \
  "ds_read_b64_tr_b16 v[76:77], v59 offset:18432\n\t" \
  "s_waitcnt lgkmcnt(7)\n\t" \
  "v_mfma_f32_16x16x32_bf16 v[188:191], v[108:111], v[104:107], v[188:191]\n\t" \
  "ds_read_b64_tr_b16 v[78:79], v59 offset:18944\n\t" \
  "v_mfma_f32_16x16x32_bf16 v[196:199], v[108:111], v[80:83], v[196:199]\n\t" \
  "ds_read_b64_tr_b16 v[144:145], v59 offset:22528\n\t" \
  "v_mfma_f32_16x16x32_bf16 v[196:199], v[140:143], v[116:119], v[196:199]\n\t" \
  "ds_read_b64_tr_b16 v[146:147], v59 offset:23040\n\t" \
  "v_mfma_f32_16x16x32_bf16 v[204:207], v[140:143], v[88:91], v[204:207]\n\t" \
  "v_mfma_f32_16x16x32_bf16 v[204:207], v[108:111], v[124:127], v[204:207]\n\t" \
  "v_mfma_f32_16x16x32_bf16 v[212:215], v[108:111], v[96:99], v[212:215]\n\t" \
  "v_mfma_f32_16x16x32_bf16 v[212:215], v[140:143], v[132:135], v[212:215]\n\t" \
  "v_mfma_f32_16x16x32_bf16 v[216:219], v[68:71], v[92:95], v[216:219]\n\t" \
  "v_mfma_f32_16x16x32_bf16 v[216:219], v[104:107], v[84:87], v[216:219]\n\t" \
  "v_mfma_f32_16x16x32_bf16 v[224:227], v[80:83], v[84:87], v[224:227]\n\t" \
  "v_mfma_f32_16x16x32_bf16 v[224:227], v[116:119], v[92:95], v[224:227]\n\t" \
  "v_mfma_f32_16x16x32_bf16 v[232:235], v[88:91], v[92:95], v[232:235]\n\t" \
  "v_mfma_f32_16x16x32_bf16 v[232:235], v[124:127], v[84:87], v[232:235]\n\t" \
  "v_mfma_f32_16x16x32_bf16 v[240:243], v[96:99], v[84:87], v[240:243]\n\t" \
  "v_mfma_f32_16x16x32_bf16 v[240:243], v[132:135], v[92:95], v[240:243]\n\t" \
  "v_mfma_f32_16x16x32_bf16 v[248:251], v[100:103], v[92:95], v[248:251]\n\t" \
  "v_mfma_f32_16x16x32_bf16 v[248:251], v[112:115], v[84:87], v[248:251]\n\t" \
  "v_mfma_f32_16x16x32_bf16 v[252:255], v[100:103], v[128:131], v[252:255]\n\t" \
  "s_waitcnt lgkmcnt(0)\n\t" \
  "v_mfma_f32_16x16x32_bf16 v[252:255], v[112:115], v[120:123], v[252:255]\n\t" \
  "v_mfma_f32_16x16x32_bf16 v[220:223], v[104:107], v[120:123], v[220:223]\n\t" \
  "v_mfma_f32_16x16x32_bf16 v[220:223], v[68:71], v[128:131], v[220:223]\n\t" \
  "s_barrier\n\t" \
  "v_mfma_f32_16x16x32_bf16 v[228:231], v[116:119], v[128:131], v[228:231]\n\t" \
  "ds_read_b64_tr_b16 v[72:73], v62 offset:26624\n\t" \
  "v_mfma_f32_16x16x32_bf16 v[228:231], v[80:83], v[120:123], v[228:231]\n\t" \
  "ds_read_b64_tr_b16 v[74:75], v62 offset:27136\n\t" \
  "v_mfma_f32_16x16x32_bf16 v[236:239], v[124:127], v[120:123], v[236:239]\n\t" \
  "ds_read_b128 v[64:67], v60 offset:10240\n\t" \
  "v_mfma_f32_16x16x32_bf16 v[236:239], v[88:91], v[128:131], v[236:239]\n\t" \
  "ds_read_b64_tr_b16 v[108:109], v62 offset:30720\n\t" \
  "v_mfma_f32_16x16x32_bf16 v[244:247], v[132:135], v[128:131], v[244:247]\n\t" \
  "ds_read_b64_tr_b16 v[110:111], v62 offset:31232\n\t" \
  "v_mfma_f32_16x16x32_bf16 v[244:247], v[96:99], v[120:123], v[244:247]\n\t" \
  "ds_read_b128 v[140:143], v61 offset:10240\n\t" \
  "v_mfma_f32_16x16x32_bf16 v[152:155], v[136:139], v[68:71], v[152:155]\n\t" \
  "ds_read_b128 v[92:95], v61 offset:12288\n\t" \
  "v_mfma_f32_16x16x32_bf16 v[152:155], v[148:151], v[104:107], v[152:155]\n\t" \
  "ds_read_b128 v[84:87], v60 offset:12288\n\t" \
  "v_mfma_f32_16x16x32_bf16 v[160:163], v[148:151], v[80:83], v[160:163]\n\t" \
  "ds_read_b128 v[100:103], v60 offset:14336\n\t" \
  "v_mfma_f32_16x16x32_bf16 v[160:163], v[136:139], v[116:119], v[160:163]\n\t" \
  "ds_read_b128 v[112:115], v61 offset:14336\n\t" \
  "v_mfma_f32_16x16x32_bf16 v[168:171], v[136:139], v[88:91], v[168:171]\n\t" \
  "ds_read_b128 v[128:131], v61 offset:16384\n\t" \
  "v_mfma_f32_16x16x32_bf16 v[168:171], v[148:151], v[124:127], v[168:171]\n\t" \
  "ds_read_b128 v[120:123], v60 offset:16384\n\t" \
  "v_mfma_f32_16x16x32_bf16 v[176:179], v[148:151], v[96:99], v[176:179]\n\t" \
  "ds_read_b64_tr_b16 v[148:149], v63 offset:26624\n\t" \
  "v_mfma_f32_16x16x32_bf16 v[176:179], v[136:139], v[132:135], v[176:179]\n\t" \
  "ds_read_b64_tr_b16 v[150:151], v63 offset:27136\n\t" \
  "v_mfma_f32_16x16x32_bf16 v[156:159], v[76:79], v[68:71], v[156:159]\n\t" \
  "ds_read_b64_tr_b16 v[136:137], v63 offset:30720\n\t" \
  "v_mfma_f32_16x16x32_bf16 v[156:159], v[144:147], v[104:107], v[156:159]\n\t" \
  "ds_read_b64_tr_b16 v[138:139], v63 offset:31232\n\t" \
  "v_mfma_f32_16x16x32_bf16 v[164:167], v[144:147], v[80:83], v[164:167]\n\t" \
  "ds_read_b64_tr_b16 v[68:69], v62 offset:34816\n\t" \
  "v_mfma_f32_16x16x32_bf16 v[164:167], v[76:79], v[116:119], v[164:167]\n\t" \
  "ds_read_b64_tr_b16 v[70:71], v62 offset:35328\n\t" \
  "v_mfma_f32_16x16x32_bf16 v[172:175], v[76:79], v[88:91], v[172:175]\n\t" \
  "ds_read_b64_tr_b16 v[104:105], v62 offset:38912\n\t" \
  "v_mfma_f32_16x16x32_bf16 v[172:175], v[144:147], v[124:127], v[172:175]\n\t" \
  "ds_read_b64_tr_b16 v[106:107], v62 offset:39424\n\t" \
  "v_mfma_f32_16x16x32_bf16 v[180:183], v[144:147], v[96:99], v[180:183]\n\t" \
  "ds_read_b128 v[80:83], v60 offset:0\n\t" \
  "v_mfma_f32_16x16x32_bf16 v[180:183], v[76:79], v[132:135], v[180:183]\n\t" \
  "ds_read_b128 v[116:119], v61 offset:0\n\t" \
  "s_waitcnt lgkmcnt(7)\n\t" \
  "v_mfma_f32_16x16x32_bf16 v[184:187], v[72:75], v[64:67], v[184:187]\n\t" \
  "ds_read_b64_tr_b16 v[88:89], v63 offset:34816\n\t" \
  "v_mfma_f32_16x16x32_bf16 v[184:187], v[108:111], v[140:143], v[184:187]\n\t" \
  "ds_read_b64_tr_b16 v[90:91], v63 offset:35328\n\t" \
  "v_mfma_f32_16x16x32_bf16 v[192:195], v[108:111], v[92:95], v[192:195]\n\t" \
  "ds_read_b64_tr_b16 v[124:125], v63 offset:38912\n\t" \
  "v_mfma_f32_16x16x32_bf16 v[192:195], v[72:75], v[84:87], v[192:195]\n\t" \
  "ds_read_b64_tr_b16 v[126:127], v63 offset:39424\n\t" \
  "v_mfma_f32_16x16x32_bf16 v[200:203], v[72:75], v[100:103], v[200:203]\n\t" \
  "ds_read_b64_tr_b16 v[96:97], v62 offset:18432\n\t" \
  "v_mfma_f32_16x16x32_bf16 v[200:203], v[108:111], v[112:115], v[200:203]\n\t" \
  "ds_read_b64_tr_b16 v[98:99], v62 offset:18944\n\t" \
  "v_mfma_f32_16x16x32_bf16 v[208:211], v[108:111], v[128:131], v[208:211]\n\t" \
  "ds_read_b64_tr_b16 v[144:145], v62 offset:22528\n\t" \
  "v_mfma_f32_16x16x32_bf16 v[208:211], v[72:75], v[120:123], v[208:211]\n\t" \
  "ds_read_b64_tr_b16 v[146:147], v62 offset:23040\n\t" \
  "v_mfma_f32_16x16x32_bf16 v[188:191], v[148:151], v[64:67], v[188:191]\n\t" \
  "ds_read_b64_tr_b16 v[76:77], v63 offset:18432\n\t" \
  "s_waitcnt lgkmcnt(7)\n\t" \
  "v_mfma_f32_16x16x32_bf16 v[188:191], v[136:139], v[140:143], v[188:191]\n\t" \
  "ds_read_b64_tr_b16 v[78:79], v63 offset:18944\n\t" \
  "v_mfma_f32_16x16x32_bf16 v[196:199], v[136:139], v[92:95], v[196:199]\n\t" \
  "ds_read_b64_tr_b16 v[132:133], v63 offset:22528\n\t" \
  "v_mfma_f32_16x16x32_bf16 v[196:199], v[148:151], v[84:87], v[196:199]\n\t" \
  "ds_read_b64_tr_b16 v[134:135], v63 offset:23040\n\t" \
  "v_mfma_f32_16x16x32_bf16 v[204:207], v[148:151], v[100:103], v[204:207]\n\t" \
  "v_mfma_f32_16x16x32_bf16 v[204:207], v[136:139], v[112:115], v[204:207]\n\t" \
  "v_mfma_f32_16x16x32_bf16 v[212:215], v[136:139], v[128:131], v[212:215]\n\t" \
  "v_mfma_f32_16x16x32_bf16 v[212:215], v[148:151], v[120:123], v[212:215]\n\t" \
  "v_mfma_f32_16x16x32_bf16 v[216:219], v[64:67], v[68:71], v[216:219]\n\t" \
  "v_mfma_f32_16x16x32_bf16 v[216:219], v[140:143], v[104:107], v[216:219]\n\t" \
  "v_mfma_f32_16x16x32_bf16 v[224:227], v[92:95], v[104:107], v[224:227]\n\t" \
  "v_mfma_f32_16x16x32_bf16 v[224:227], v[84:87], v[68:71], v[224:227]\n\t" \
  "v_mfma_f32_16x16x32_bf16 v[232:235], v[100:103], v[68:71], v[232:235]\n\t" \
  "v_mfma_f32_16x16x32_bf16 v[232:235], v[112:115], v[104:107], v[232:235]\n\t" \
  "v_mfma_f32_16x16x32_bf16 v[240:243], v[128:131], v[104:107], v[240:243]\n\t" \
  "v_mfma_f32_16x16x32_bf16 v[240:243], v[120:123], v[68:71], v[240:243]\n\t" \
  "v_mfma_f32_16x16x32_bf16 v[248:251], v[80:83], v[68:71], v[248:251]\n\t" \
  "v_mfma_f32_16x16x32_bf16 v[248:251], v[116:119], v[104:107], v[248:251]\n\t" \
  "v_mfma_f32_16x16x32_bf16 v[252:255], v[80:83], v[88:91], v[252:255]\n\t" \
  "s_waitcnt lgkmcnt(0)\n\t" \
  "v_mfma_f32_16x16x32_bf16 v[252:255], v[116:119], v[124:127], v[252:255]\n\t" \
  "v_mfma_f32_16x16x32_bf16 v[220:223], v[140:143], v[124:127], v[220:223]\n\t" \
  "v_mfma_f32_16x16x32_bf16 v[220:223], v[64:67], v[88:91], v[220:223]\n\t" \
  "s_barrier\n\t" \
  "v_mfma_f32_16x16x32_bf16 v[228:231], v[84:87], v[88:91], v[228:231]\n\t" \
  "ds_read_b64_tr_b16 v[108:109], v54 offset:26624\n\t" \
  "v_mfma_f32_16x16x32_bf16 v[228:231], v[92:95], v[124:127], v[228:231]\n\t" \
  "ds_read_b64_tr_b16 v[110:111], v54 offset:27136\n\t" \
  "v_mfma_f32_16x16x32_bf16 v[236:239], v[112:115], v[124:127], v[236:239]\n\t" \
  "ds_read_b128 v[72:75], v52 offset:10240\n\t" \
  "v_mfma_f32_16x16x32_bf16 v[236:239], v[100:103], v[88:91], v[236:239]\n\t" \
  "ds_read_b64_tr_b16 v[136:137], v54 offset:30720\n\t" \
  "v_mfma_f32_16x16x32_bf16 v[244:247], v[120:123], v[88:91], v[244:247]\n\t" \
  "ds_read_b64_tr_b16 v[138:139], v54 offset:31232\n\t" \
  "v_mfma_f32_16x16x32_bf16 v[244:247], v[128:131], v[124:127], v[244:247]\n\t" \
  "ds_read_b128 v[148:151], v53 offset:10240\n\t" \
  "v_mfma_f32_16x16x32_bf16 v[152:155], v[96:99], v[64:67], v[152:155]\n\t" \
  "ds_read_b128 v[68:71], v53 offset:12288\n\t" \
  "v_mfma_f32_16x16x32_bf16 v[152:155], v[144:147], v[140:143], v[152:155]\n\t" \
  "ds_read_b128 v[104:107], v52 offset:12288\n\t" \
  "v_mfma_f32_16x16x32_bf16 v[160:163], v[144:147], v[92:95], v[160:163]\n\t" \
  "ds_read_b128 v[80:83], v52 offset:14336\n\t" \
  "v_mfma_f32_16x16x32_bf16 v[160:163], v[96:99], v[84:87], v[160:163]\n\t" \
  "ds_read_b128 v[116:119], v53 offset:14336\n\t" \
  "v_mfma_f32_16x16x32_bf16 v[168:171], v[96:99], v[100:103], v[168:171]\n\t" \
  "ds_read_b128 v[88:91], v53 offset:16384\n\t" \
  "v_mfma_f32_16x16x32_bf16 v[168:171], v[144:147], v[112:115], v[168:171]\n\t" \
  "ds_read_b128 v[124:127], v52 offset:16384\n\t" \
  "v_mfma_f32_16x16x32_bf16 v[176:179], v[144:147], v[128:131], v[176:179]\n\t" \
  "ds_read_b64_tr_b16 v[144:145], v55 offset:26624\n\t" \
  "v_mfma_f32_16x16x32_bf16 v[176:179], v[96:99], v[120:123], v[176:179]\n\t" \
  "ds_read_b64_tr_b16 v[146:147], v55 offset:27136\n\t" \
  "v_mfma_f32_16x16x32_bf16 v[156:159], v[76:79], v[64:67], v[156:159]\n\t" \
  "ds_read_b64_tr_b16 v[96:97], v55 offset:30720\n\t" \
  "v_mfma_f32_16x16x32_bf16 v[156:159], v[132:135], v[140:143], v[156:159]\n\t" \
  "ds_read_b64_tr_b16 v[98:99], v55 offset:31232\n\t" \
  "v_mfma_f32_16x16x32_bf16 v[164:167], v[132:135], v[92:95], v[164:167]\n\t" \
  "ds_read_b64_tr_b16 v[64:65], v54 offset:34816\n\t" \
  "v_mfma_f32_16x16x32_bf16 v[164:167], v[76:79], v[84:87], v[164:167]\n\t" \
  "ds_read_b64_tr_b16 v[66:67], v54 offset:35328\n\t" \
  "v_mfma_f32_16x16x32_bf16 v[172:175], v[76:79], v[100:103], v[172:175]\n\t" \
  "ds_read_b64_tr_b16 v[140:141], v54 offset:38912\n\t" \
  "v_mfma_f32_16x16x32_bf16 v[172:175], v[132:135], v[112:115], v[172:175]\n\t" \
  "ds_read_b64_tr_b16 v[142:143], v54 offset:39424\n\t" \
  "v_mfma_f32_16x16x32_bf16 v[180:183], v[132:135], v[128:131], v[180:183]\n\t" \
  "ds_read_b128 v[92:95], v52 offset:0\n\t" \
  "v_mfma_f32_16x16x32_bf16 v[180:183], v[76:79], v[120:123], v[180:183]\n\t" \
  "ds_read_b128 v[84:87], v53 offset:0\n\t" \
  "s_waitcnt lgkmcnt(7)\n\t" \
  "v_mfma_f32_16x16x32_bf16 v[184:187], v[108:111], v[72:75], v[184:187]\n\t" \
  "ds_read_b64_tr_b16 v[100:101], v55 offset:34816\n\t" \
  "v_mfma_f32_16x16x32_bf16 v[184:187], v[136:139], v[148:151], v[184:187]\n\t" \
  "ds_read_b64_tr_b16 v[102:103], v55 offset:35328\n\t" \
  "v_mfma_f32_16x16x32_bf16 v[192:195], v[136:139], v[68:71], v[192:195]\n\t" \
  "ds_read_b64_tr_b16 v[112:113], v55 offset:38912\n\t" \
  "v_mfma_f32_16x16x32_bf16 v[192:195], v[108:111], v[104:107], v[192:195]\n\t" \
  "ds_read_b64_tr_b16 v[114:115], v55 offset:39424\n\t" \
  "v_mfma_f32_16x16x32_bf16 v[200:203], v[108:111], v[80:83], v[200:203]\n\t" \
  "ds_read_b64_tr_b16 v[128:129], v54 offset:18432\n\t" \
  "v_mfma_f32_16x16x32_bf16 v[200:203], v[136:139], v[116:119], v[200:203]\n\t" \
  "ds_read_b64_tr_b16 v[130:131], v54 offset:18944\n\t" \
  "v_mfma_f32_16x16x32_bf16 v[208:211], v[136:139], v[88:91], v[208:211]\n\t" \
  "ds_read_b64_tr_b16 v[132:133], v54 offset:22528\n\t" \
  "v_mfma_f32_16x16x32_bf16 v[208:211], v[108:111], v[124:127], v[208:211]\n\t" \
  "ds_read_b64_tr_b16 v[134:135], v54 offset:23040\n\t" \
  "v_mfma_f32_16x16x32_bf16 v[188:191], v[144:147], v[72:75], v[188:191]\n\t" \
  "ds_read_b64_tr_b16 v[76:77], v55 offset:18432\n\t" \
  "s_waitcnt lgkmcnt(7)\n\t" \
  "v_mfma_f32_16x16x32_bf16 v[188:191], v[96:99], v[148:151], v[188:191]\n\t" \
  "ds_read_b64_tr_b16 v[78:79], v55 offset:18944\n\t" \
  "v_mfma_f32_16x16x32_bf16 v[196:199], v[96:99], v[68:71], v[196:199]\n\t" \
  "ds_read_b64_tr_b16 v[120:121], v55 offset:22528\n\t" \
  "v_mfma_f32_16x16x32_bf16 v[196:199], v[144:147], v[104:107], v[196:199]\n\t" \
  "ds_read_b64_tr_b16 v[122:123], v55 offset:23040\n\t" \
  "v_mfma_f32_16x16x32_bf16 v[204:207], v[144:147], v[80:83], v[204:207]\n\t" \
  "v_mfma_f32_16x16x32_bf16 v[204:207], v[96:99], v[116:119], v[204:207]\n\t" \
  "v_mfma_f32_16x16x32_bf16 v[212:215], v[96:99], v[88:91], v[212:215]\n\t" \
  "v_mfma_f32_16x16x32_bf16 v[212:215], v[144:147], v[124:127], v[212:215]\n\t" \
  "v_mfma_f32_16x16x32_bf16 v[216:219], v[72:75], v[64:67], v[216:219]\n\t" \
  "v_mfma_f32_16x16x32_bf16 v[216:219], v[148:151], v[140:143], v[216:219]\n\t" \
  "v_mfma_f32_16x16x32_bf16 v[224:227], v[68:71], v[140:143], v[224:227]\n\t" \
  "v_mfma_f32_16x16x32_bf16 v[224:227], v[104:107], v[64:67], v[224:227]\n\t" \
  "v_mfma_f32_16x16x32_bf16 v[232:235], v[80:83], v[64:67], v[232:235]\n\t" \
  "v_mfma_f32_16x16x32_bf16 v[232:235], v[116:119], v[140:143], v[232:235]\n\t" \
  "v_mfma_f32_16x16x32_bf16 v[240:243], v[88:91], v[140:143], v[240:243]\n\t" \
  "v_mfma_f32_16x16x32_bf16 v[240:243], v[124:127], v[64:67], v[240:243]\n\t" \
  "v_mfma_f32_16x16x32_bf16 v[248:251], v[92:95], v[64:67], v[248:251]\n\t" \
  "v_mfma_f32_16x16x32_bf16 v[248:251], v[84:87], v[140:143], v[248:251]\n\t" \
  "v_mfma_f32_16x16x32_bf16 v[252:255], v[92:95], v[100:103], v[252:255]\n\t" \
  "s_waitcnt lgkmcnt(0)\n\t" \
  "v_mfma_f32_16x16x32_bf16 v[252:255], v[84:87], v[112:115], v[252:255]\n\t" \
  "v_mfma_f32_16x16x32_bf16 v[220:223], v[148:151], v[112:115], v[220:223]\n\t" \
  "v_mfma_f32_16x16x32_bf16 v[220:223], v[72:75], v[100:103], v[220:223]\n\t" \
  "s_barrier\n\t" \
  "v_mfma_f32_16x16x32_bf16 v[228:231], v[104:107], v[100:103], v[228:231]\n\t" \
  "ds_read_b64_tr_b16 v[96:97], v58 offset:26624\n\t" \
  "v_mfma_f32_16x16x32_bf16 v[228:231], v[68:71], v[112:115], v[228:231]\n\t" \
  "ds_read_b64_tr_b16 v[98:99], v58 offset:27136\n\t" \
  "v_mfma_f32_16x16x32_bf16 v[236:239], v[116:119], v[112:115], v[236:239]\n\t" \
  "ds_read_b128 v[136:139], v56 offset:10240\n\t" \
  "v_mfma_f32_16x16x32_bf16 v[236:239], v[80:83], v[100:103], v[236:239]\n\t" \
  "ds_read_b64_tr_b16 v[64:65], v58 offset:30720\n\t" \
  "v_mfma_f32_16x16x32_bf16 v[244:247], v[124:127], v[100:103], v[244:247]\n\t" \
  "ds_read_b64_tr_b16 v[66:67], v58 offset:31232\n\t" \
  "v_mfma_f32_16x16x32_bf16 v[244:247], v[88:91], v[112:115], v[244:247]\n\t" \
  "ds_read_b128 v[108:111], v57 offset:10240\n\t" \
  "v_mfma_f32_16x16x32_bf16 v[152:155], v[128:131], v[72:75], v[152:155]\n\t" \
  "ds_read_b128 v[144:147], v57 offset:12288\n\t" \
  "v_mfma_f32_16x16x32_bf16 v[152:155], v[132:135], v[148:151], v[152:155]\n\t" \
  "ds_read_b128 v[140:143], v56 offset:12288\n\t" \
  "v_mfma_f32_16x16x32_bf16 v[160:163], v[132:135], v[68:71], v[160:163]\n\t" \
  "ds_read_b128 v[112:115], v56 offset:14336\n\t" \
  "v_mfma_f32_16x16x32_bf16 v[160:163], v[128:131], v[104:107], v[160:163]\n\t" \
  "ds_read_b64_tr_b16 v[92:93], v59 offset:26624\n\t" \
  "v_mfma_f32_16x16x32_bf16 v[168:171], v[128:131], v[80:83], v[168:171]\n\t" \
  "ds_read_b64_tr_b16 v[94:95], v59 offset:27136\n\t" \
  "v_mfma_f32_16x16x32_bf16 v[168:171], v[132:135], v[116:119], v[168:171]\n\t" \
  "ds_read_b64_tr_b16 v[84:85], v59 offset:30720\n\t" \
  "v_mfma_f32_16x16x32_bf16 v[176:179], v[132:135], v[88:91], v[176:179]\n\t" \
  "ds_read_b128 v[132:135], v57 offset:14336\n\t" \
  "v_mfma_f32_16x16x32_bf16 v[176:179], v[128:131], v[124:127], v[176:179]\n\t" \
  "ds_read_b128 v[128:131], v57 offset:16384\n\t" \
  "v_mfma_f32_16x16x32_bf16 v[156:159], v[76:79], v[72:75], v[156:159]\n\t" \
  "ds_read_b64_tr_b16 v[86:87], v59 offset:31232\n\t" \
  "v_mfma_f32_16x16x32_bf16 v[156:159], v[120:123], v[148:151], v[156:159]\n\t" \
  "ds_read_b128 v[148:151], v56 offset:16384\n\t" \
  "v_mfma_f32_16x16x32_bf16 v[164:167], v[120:123], v[68:71], v[164:167]\n\t" \
  "ds_read_b64_tr_b16 v[100:101], v58 offset:34816\n\t" \
  "v_mfma_f32_16x16x32_bf16 v[164:167], v[76:79], v[104:107], v[164:167]\n\t" \
  "ds_read_b64_tr_b16 v[102:103], v58 offset:35328\n\t" \
  "v_mfma_f32_16x16x32_bf16 v[172:175], v[76:79], v[80:83], v[172:175]\n\t" \
  "ds_read_b64_tr_b16 v[72:73], v58 offset:38912\n\t" \
  "v_mfma_f32_16x16x32_bf16 v[172:175], v[120:123], v[116:119], v[172:175]\n\t" \
  "ds_read_b64_tr_b16 v[74:75], v58 offset:39424\n\t" \
  "v_mfma_f32_16x16x32_bf16 v[180:183], v[120:123], v[88:91], v[180:183]\n\t" \
  "ds_read_b128 v[68:71], v56 offset:0\n\t" \
  "v_mfma_f32_16x16x32_bf16 v[180:183], v[76:79], v[124:127], v[180:183]\n\t" \
  "ds_read_b128 v[104:107], v57 offset:0\n\t" \
  "s_waitcnt lgkmcnt(7)\n\t" \
  "v_mfma_f32_16x16x32_bf16 v[184:187], v[96:99], v[136:139], v[184:187]\n\t" \
  "ds_read_b64_tr_b16 v[80:81], v59 offset:34816\n\t" \
  "v_mfma_f32_16x16x32_bf16 v[184:187], v[64:67], v[108:111], v[184:187]\n\t" \
  "ds_read_b64_tr_b16 v[82:83], v59 offset:35328\n\t" \
  "v_mfma_f32_16x16x32_bf16 v[192:195], v[64:67], v[144:147], v[192:195]\n\t" \
  "ds_read_b64_tr_b16 v[88:89], v59 offset:38912\n\t" \
  "v_mfma_f32_16x16x32_bf16 v[192:195], v[96:99], v[140:143], v[192:195]\n\t" \
  "ds_read_b64_tr_b16 v[90:91], v59 offset:39424\n\t" \
  "v_mfma_f32_16x16x32_bf16 v[200:203], v[96:99], v[112:115], v[200:203]\n\t" \
  "ds_read_b64_tr_b16 v[116:117], v58 offset:18432\n\t" \
  "v_mfma_f32_16x16x32_bf16 v[200:203], v[64:67], v[132:135], v[200:203]\n\t" \
  "ds_read_b64_tr_b16 v[118:119], v58 offset:18944\n\t" \
  "v_mfma_f32_16x16x32_bf16 v[208:211], v[64:67], v[128:131], v[208:211]\n\t" \
  "ds_read_b64_tr_b16 v[120:121], v58 offset:22528\n\t" \
  "s_waitcnt lgkmcnt(7)\n\t" \
  "v_mfma_f32_16x16x32_bf16 v[208:211], v[96:99], v[148:151], v[208:211]\n\t" \
  "ds_read_b64_tr_b16 v[122:123], v58 offset:23040\n\t" \
  "v_mfma_f32_16x16x32_bf16 v[188:191], v[92:95], v[136:139], v[188:191]\n\t" \
  "ds_read_b64_tr_b16 v[124:125], v59 offset:18432\n\t" \
  "v_mfma_f32_16x16x32_bf16 v[188:191], v[84:87], v[108:111], v[188:191]\n\t" \
  "ds_read_b64_tr_b16 v[126:127], v59 offset:18944\n\t" \
  "v_mfma_f32_16x16x32_bf16 v[196:199], v[84:87], v[144:147], v[196:199]\n\t" \
  "ds_read_b64_tr_b16 v[76:77], v59 offset:22528\n\t" \
  "v_mfma_f32_16x16x32_bf16 v[196:199], v[92:95], v[140:143], v[196:199]\n\t" \
  "ds_read_b64_tr_b16 v[78:79], v59 offset:23040\n\t" \
  "v_mfma_f32_16x16x32_bf16 v[204:207], v[92:95], v[112:115], v[204:207]\n\t" \
  "v_mfma_f32_16x16x32_bf16 v[204:207], v[84:87], v[132:135], v[204:207]\n\t" \
  "v_mfma_f32_16x16x32_bf16 v[212:215], v[84:87], v[128:131], v[212:215]\n\t" \
  "v_mfma_f32_16x16x32_bf16 v[212:215], v[92:95], v[148:151], v[212:215]\n\t" \
  "v_mfma_f32_16x16x32_bf16 v[216:219], v[136:139], v[100:103], v[216:219]\n\t" \
  "v_mfma_f32_16x16x32_bf16 v[216:219], v[108:111], v[72:75], v[216:219]\n\t" \
  "v_mfma_f32_16x16x32_bf16 v[224:227], v[144:147], v[72:75], v[224:227]\n\t" \
  "v_and_b32 v14, -16, v32\n\t" \
  "v_mfma_f32_16x16x32_bf16 v[224:227], v[140:143], v[100:103], v[224:227]\n\t" \
  "v_mfma_f32_16x16x32_bf16 v[232:235], v[112:115], v[100:103], v[232:235]\n\t" \
  "ds_read_b128 v[10:13], v14 offset:128\n\t" \
  "v_mfma_f32_16x16x32_bf16 v[232:235], v[132:135], v[72:75], v[232:235]\n\t" \
  "v_mfma_f32_16x16x32_bf16 v[240:243], v[128:131], v[72:75], v[240:243]\n\t" \
  "v_mfma_f32_16x16x32_bf16 v[240:243], v[148:151], v[100:103], v[240:243]\n\t" \
  "v_mfma_f32_16x16x32_bf16 v[248:251], v[68:71], v[100:103], v[248:251]\n\t" \
  "v_mfma_f32_16x16x32_bf16 v[248:251], v[104:107], v[72:75], v[248:251]\n\t" \
  "s_waitcnt lgkmcnt(1)\n\t" \
  "v_mfma_f32_16x16x32_bf16 v[252:255], v[68:71], v[80:83], v[252:255]\n\t" \
  "s_waitcnt lgkmcnt(0)\n\t" \
  "v_and_b32 v10, v10, v11\n\t" \
  "v_and_b32 v12, v12, v13\n\t" \
  "v_mfma_f32_16x16x32_bf16 v[252:255], v[104:107], v[88:91], v[252:255]\n\t" \
  "v_and_b32 v10, v10, v12\n\t" \
  "v_mfma_f32_16x16x32_bf16 v[220:223], v[108:111], v[88:91], v[220:223]\n\t" \
  "v_readfirstlane_b32 vcc_lo, v10\n\t" \
  "v_mfma_f32_16x16x32_bf16 v[220:223], v[136:139], v[80:83], v[220:223]\n\t" \
  "ds_write_b32 v32, v0 offset:144\n\t" \
  "s_cmp_eq_u32 vcc_lo, 0\n\t" \
  "s_cbranch_scc1 15f\n\t" \
  "14:\n\t" \
  "v_mfma_f32_16x16x32_bf16 v[228:231], v[140:143], v[80:83], v[228:231]\n\t" \
  "ds_read_b64_tr_b16 v[64:65], v62 offset:26624\n\t" \
  "v_mfma_f32_16x16x32_bf16 v[228:231], v[144:147], v[88:91], v[228:231]\n\t" \
  "ds_read_b64_tr_b16 v[66:67], v62 offset:27136\n\t" \
  "v_mfma_f32_16x16x32_bf16 v[236:239], v[132:135], v[88:91], v[236:239]\n\t" \
  "ds_read_b128 v[96:99], v60 offset:10240\n\t" \
  "v_mfma_f32_16x16x32_bf16 v[236:239], v[112:115], v[80:83], v[236:239]\n\t" \
  "ds_read_b128 v[84:87], v60 offset:12288\n\t" \
  "v_mfma_f32_16x16x32_bf16 v[244:247], v[148:151], v[80:83], v[244:247]\n\t" \
  "ds_read_b128 v[92:95], v60 offset:14336\n\t" \
  "v_mfma_f32_16x16x32_bf16 v[244:247], v[128:131], v[88:91], v[244:247]\n\t" \
  "ds_read_b128 v[100:103], v60 offset:16384\n\t" \
  "v_mfma_f32_16x16x32_bf16 v[152:155], v[116:119], v[136:139], v[152:155]\n\t" \
  "ds_read_b64_tr_b16 v[72:73], v63 offset:26624\n\t" \
  "v_mfma_f32_16x16x32_bf16 v[152:155], v[120:123], v[108:111], v[152:155]\n\t" \
  "ds_read_b64_tr_b16 v[74:75], v63 offset:27136\n\t" \
  "v_mfma_f32_16x16x32_bf16 v[160:163], v[120:123], v[144:147], v[160:163]\n\t" \
  "ds_read_b64_tr_b16 v[68:69], v62 offset:34816\n\t" \
  "v_mfma_f32_16x16x32_bf16 v[160:163], v[116:119], v[140:143], v[160:163]\n\t" \
  "ds_read_b64_tr_b16 v[70:71], v62 offset:35328\n\t" \
  "v_mfma_f32_16x16x32_bf16 v[168:171], v[116:119], v[112:115], v[168:171]\n\t" \
  "ds_read_b128 v[104:107], v60 offset:0\n\t" \
  "v_mfma_f32_16x16x32_bf16 v[168:171], v[120:123], v[132:135], v[168:171]\n\t" \
  "ds_read_b64_tr_b16 v[80:81], v63 offset:34816\n\t" \
  "v_mfma_f32_16x16x32_bf16 v[176:179], v[120:123], v[128:131], v[176:179]\n\t" \
  "ds_read_b64_tr_b16 v[82:83], v63 offset:35328\n\t" \
  "v_mfma_f32_16x16x32_bf16 v[176:179], v[116:119], v[148:151], v[176:179]\n\t" \
  "ds_read_b64_tr_b16 v[88:89], v62 offset:18432\n\t" \
  "v_mfma_f32_16x16x32_bf16 v[156:159], v[124:127], v[136:139], v[156:159]\n\t" \
  "ds_read_b64_tr_b16 v[90:91], v62 offset:18944\n\t" \
  "v_mfma_f32_16x16x32_bf16 v[156:159], v[76:79], v[108:111], v[156:159]\n\t" \
  "ds_read_b64_tr_b16 v[120:121], v62 offset:30720\n\t" \
  "v_mfma_f32_16x16x32_bf16 v[164:167], v[76:79], v[144:147], v[164:167]\n\t" \
  "ds_read_b64_tr_b16 v[122:123], v62 offset:31232\n\t" \
  "v_mfma_f32_16x16x32_bf16 v[164:167], v[124:127], v[140:143], v[164:167]\n\t" \
  "ds_read_b128 v[116:119], v61 offset:10240\n\t" \
  "v_mfma_f32_16x16x32_bf16 v[172:175], v[124:127], v[112:115], v[172:175]\n\t" \
  "ds_read_b128 v[136:139], v61 offset:12288\n\t" \
  "v_mfma_f32_16x16x32_bf16 v[172:175], v[76:79], v[132:135], v[172:175]\n\t" \
  "ds_read_b128 v[108:111], v61 offset:14336\n\t" \
  "v_mfma_f32_16x16x32_bf16 v[180:183], v[76:79], v[128:131], v[180:183]\n\t" \
  "ds_read_b64_tr_b16 v[76:77], v63 offset:18432\n\t" \
  "v_mfma_f32_16x16x32_bf16 v[180:183], v[124:127], v[148:151], v[180:183]\n\t" \
  "ds_read_b64_tr_b16 v[78:79], v63 offset:18944\n\t" \
  "s_waitcnt lgkmcnt(7)\n\t" \
  "v_mfma_f32_16x16x32_bf16 v[184:187], v[64:67], v[96:99], v[184:187]\n\t" \
  "ds_read_b128 v[144:147], v61 offset:16384\n\t" \
  "v_mfma_f32_16x16x32_bf16 v[192:195], v[64:67], v[84:87], v[192:195]\n\t" \
  "ds_read_b64_tr_b16 v[140:141], v63 offset:30720\n\t" \
  "v_mfma_f32_16x16x32_bf16 v[200:203], v[64:67], v[92:95], v[200:203]\n\t" \
  "ds_read_b64_tr_b16 v[142:143], v63 offset:31232\n\t" \
  "v_mfma_f32_16x16x32_bf16 v[208:211], v[64:67], v[100:103], v[208:211]\n\t" \
  "ds_read_b64_tr_b16 v[112:113], v62 offset:22528\n\t" \
  "v_mfma_f32_16x16x32_bf16 v[188:191], v[72:75], v[96:99], v[188:191]\n\t" \
  "ds_read_b64_tr_b16 v[114:115], v62 offset:23040\n\t" \
  "v_mfma_f32_16x16x32_bf16 v[196:199], v[72:75], v[84:87], v[196:199]\n\t" \
  "ds_read_b64_tr_b16 v[132:133], v63 offset:22528\n\t" \
  "v_mfma_f32_16x16x32_bf16 v[204:207], v[72:75], v[92:95], v[204:207]\n\t" \
  "ds_read_b64_tr_b16 v[134:135], v63 offset:23040\n\t" \
  "v_mfma_f32_16x16x32_bf16 v[212:215], v[72:75], v[100:103], v[212:215]\n\t" \
  "502:\n\t" \
  "s_cbranch_execz 506f\n\t" \
  "ds_read_b64_tr_b16 v[128:129], v62 offset:38912\n\t" \
  "v_mfma_f32_16x16x32_bf16 v[248:251], v[104:107], v[68:71], v[248:251]\n\t" \
  "ds_read_b64_tr_b16 v[130:131], v62 offset:39424\n\t" \
  "v_mfma_f32_16x16x32_bf16 v[216:219], v[96:99], v[68:71], v[216:219]\n\t" \
  "ds_read_b128 v[124:127], v61 offset:0\n\t" \
  "v_mfma_f32_16x16x32_bf16 v[224:227], v[84:87], v[68:71], v[224:227]\n\t" \
  "ds_read_b64_tr_b16 v[148:149], v63 offset:38912\n\t" \
  "v_mfma_f32_16x16x32_bf16 v[232:235], v[92:95], v[68:71], v[232:235]\n\t" \
  "ds_read_b64_tr_b16 v[150:151], v63 offset:39424\n\t" \
  "v_mfma_f32_16x16x32_bf16 v[240:243], v[100:103], v[68:71], v[240:243]\n\t" \
  "v_mfma_f32_16x16x32_bf16 v[252:255], v[104:107], v[80:83], v[252:255]\n\t" \
  "v_mfma_f32_16x16x32_bf16 v[220:223], v[96:99], v[80:83], v[220:223]\n\t" \
  "v_mfma_f32_16x16x32_bf16 v[228:231], v[84:87], v[80:83], v[228:231]\n\t" \
  "v_mfma_f32_16x16x32_bf16 v[236:239], v[92:95], v[80:83], v[236:239]\n\t" \
  "v_mfma_f32_16x16x32_bf16 v[244:247], v[100:103], v[80:83], v[244:247]\n\t" \
  "v_mfma_f32_16x16x32_bf16 v[152:155], v[88:91], v[96:99], v[152:155]\n\t" \
  "v_mfma_f32_16x16x32_bf16 v[160:163], v[88:91], v[84:87], v[160:163]\n\t" \
  "v_mfma_f32_16x16x32_bf16 v[168:171], v[88:91], v[92:95], v[168:171]\n\t" \
  "v_mfma_f32_16x16x32_bf16 v[176:179], v[88:91], v[100:103], v[176:179]\n\t" \
  "s_waitcnt lgkmcnt(0)\n\t" \
  "v_mfma_f32_16x16x32_bf16 v[156:159], v[76:79], v[96:99], v[156:159]\n\t" \
  "v_mfma_f32_16x16x32_bf16 v[164:167], v[76:79], v[84:87], v[164:167]\n\t" \
  "ds_write_b32 v32, v0\n\t" \
  "v_mfma_f32_16x16x32_bf16 v[172:175], v[76:79], v[92:95], v[172:175]\n\t" \
  "v_mfma_f32_16x16x32_bf16 v[180:183], v[76:79], v[100:103], v[180:183]\n\t" \
  "v_mfma_f32_16x16x32_bf16 v[184:187], v[120:123], v[116:119], v[184:187]\n\t" \
  "v_and_b32 v56, -16, v32\n\t" \
  "v_mfma_f32_16x16x32_bf16 v[192:195], v[120:123], v[136:139], v[192:195]\n\t" \
  "v_mfma_f32_16x16x32_bf16 v[200:203], v[120:123], v[108:111], v[200:203]\n\t" \
  "v_mfma_f32_16x16x32_bf16 v[208:211], v[120:123], v[144:147], v[208:211]\n\t" \
  "v_mfma_f32_16x16x32_bf16 v[188:191], v[140:143], v[116:119], v[188:191]\n\t" \
  "v_mfma_f32_16x16x32_bf16 v[196:199], v[140:143], v[136:139], v[196:199]\n\t" \
  "v_mfma_f32_16x16x32_bf16 v[204:207], v[140:143], v[108:111], v[204:207]\n\t" \
  "v_mfma_f32_16x16x32_bf16 v[212:215], v[140:143], v[144:147], v[212:215]\n\t" \
  "v_cvt_pk_bf16_f32 v14, v184, v185\n\t" \
  "v_cvt_pk_bf16_f32 v15, v186, v187\n\t" \
  "v_mfma_f32_16x16x32_bf16 v[152:155], v[112:115], v[116:119], v[152:155]\n\t" \
  "v_cvt_pk_bf16_f32 v18, v192, v193\n\t" \
  "v_cvt_pk_bf16_f32 v19, v194, v195\n\t" \
  "v_mfma_f32_16x16x32_bf16 v[160:163], v[112:115], v[136:139], v[160:163]\n\t" \
  "v_cvt_pk_bf16_f32 v16, v188, v189\n\t" \
  "v_cvt_pk_bf16_f32 v17, v190, v191\n\t" \
  "v_mfma_f32_16x16x32_bf16 v[168:171], v[112:115], v[108:111], v[168:171]\n\t" \
  "v_cvt_pk_bf16_f32 v20, v196, v197\n\t" \
  "v_cvt_pk_bf16_f32 v21, v198, v199\n\t" \
  "v_mfma_f32_16x16x32_bf16 v[176:179], v[112:115], v[144:147], v[176:179]\n\t" \
  "v_cvt_pk_bf16_f32 v22, v200, v201\n\t" \
  "v_cvt_pk_bf16_f32 v23, v202, v203\n\t" \
  "v_mfma_f32_16x16x32_bf16 v[156:159], v[132:135], v[116:119], v[156:159]\n\t" \
  "v_cvt_pk_bf16_f32 v24, v204, v205\n\t" \
  "v_cvt_pk_bf16_f32 v25, v206, v207\n\t" \
  "v_mfma_f32_16x16x32_bf16 v[164:167], v[132:135], v[136:139], v[164:167]\n\t" \
  "v_cvt_pk_bf16_f32 v26, v208, v209\n\t" \
  "v_cvt_pk_bf16_f32 v27, v210, v211\n\t" \
  "v_mfma_f32_16x16x32_bf16 v[172:175], v[132:135], v[108:111], v[172:175]\n\t" \
  "v_cvt_pk_bf16_f32 v28, v212, v213\n\t" \
  "v_cvt_pk_bf16_f32 v29, v214, v215\n\t" \
  "v_mfma_f32_16x16x32_bf16 v[180:183], v[132:135], v[144:147], v[180:183]\n\t" \
  "v_cvt_pk_bf16_f32 v84, v152, v153\n\t" \
  "v_cvt_pk_bf16_f32 v85, v154, v155\n\t" \
  "v_mfma_f32_16x16x32_bf16 v[248:251], v[124:127], v[128:131], v[248:251]\n\t" \
  "v_cvt_pk_bf16_f32 v86, v156, v157\n\t" \
  "v_cvt_pk_bf16_f32 v87, v158, v159\n\t" \
  "v_mfma_f32_16x16x32_bf16 v[216:219], v[116:119], v[128:131], v[216:219]\n\t" \
  "v_cvt_pk_bf16_f32 v88, v160, v161\n\t" \
  "v_cvt_pk_bf16_f32 v89, v162, v163\n\t" \
  "v_mfma_f32_16x16x32_bf16 v[224:227], v[136:139], v[128:131], v[224:227]\n\t" \
  "v_cvt_pk_bf16_f32 v90, v164, v165\n\t" \
  "v_cvt_pk_bf16_f32 v91, v166, v167\n\t" \
  "v_mfma_f32_16x16x32_bf16 v[232:235], v[108:111], v[128:131], v[232:235]\n\t" \
  "v_cvt_pk_bf16_f32 v72, v248, v249\n\t" \
  "v_cvt_pk_bf16_f32 v73, v250, v251\n\t" \
  "v_mfma_f32_16x16x32_bf16 v[240:243], v[144:147], v[128:131], v[240:243]\n\t" \
  "ds_read_b32 v52, v33 offset:48\n\t" \
  "ds_read_b128 v[10:13], v31\n\t" \
  "v_cvt_pk_bf16_f32 v92, v168, v169\n\t" \
  "v_mfma_f32_16x16x32_bf16 v[252:255], v[124:127], v[148:151], v[252:255]\n\t" \
  "v_cvt_pk_bf16_f32 v93, v170, v171\n\t" \
  "v_cvt_pk_bf16_f32 v94, v172, v173\n\t" \
  "v_mfma_f32_16x16x32_bf16 v[220:223], v[116:119], v[148:151], v[220:223]\n\t" \
  "v_cvt_pk_bf16_f32 v95, v174, v175\n\t" \
  "v_cvt_pk_bf16_f32 v96, v176, v177\n\t" \
  "v_mfma_f32_16x16x32_bf16 v[228:231], v[136:139], v[148:151], v[228:231]\n\t" \
  "v_cvt_pk_bf16_f32 v97, v178, v179\n\t" \
  "v_cvt_pk_bf16_f32 v98, v180, v181\n\t" \
  "v_mfma_f32_16x16x32_bf16 v[236:239], v[108:111], v[148:151], v[236:239]\n\t" \
  "v_cvt_pk_bf16_f32 v82, v252, v253\n\t" \
  "v_cvt_pk_bf16_f32 v83, v254, v255\n\t" \
  "v_mfma_f32_16x16x32_bf16 v[244:247], v[144:147], v[148:151], v[244:247]\n\t" \
  "ds_write_b64 v30, v[72:73]\n\t" \
  "ds_write_b64 v30, v[82:83] offset:8\n\t" \
  "ds_write_b32 v32, v0 offset:48\n\t" \
  "v_mfma_f32_16x16x32_bf16 v[200:203], v[22:25], v[92:95], v[34:37]\n\t" \
  "v_cvt_pk_bf16_f32 v99, v182, v183\n\t" \
  "s_waitcnt lgkmcnt(0)\n\t" \
  "v_readfirstlane_b32 vcc_lo, v52\n\t" \
  "s_nop 1\n\t" \
  "s_cmp_eq_u32 vcc_lo, 0\n\t" \
  "s_cbranch_scc1 5f\n\t" \
  "4:\n\t" \
  "v_mfma_f32_16x16x32_bf16 v[204:207], v[18:21], v[92:95], v[38:41]\n\t" \
  "v_cvt_pk_bf16_f32 v66, v232, v233\n\t" \
  "v_cvt_pk_bf16_f32 v67, v234, v235\n\t" \
  "v_mfma_f32_16x16x32_bf16 v[208:211], v[26:29], v[96:99], v[34:37]\n\t" \
  "v_cvt_pk_bf16_f32 v76, v236, v237\n\t" \
  "v_cvt_pk_bf16_f32 v77, v238, v239\n\t" \
  "v_mfma_f32_16x16x32_bf16 v[212:215], v[22:25], v[96:99], v[38:41]\n\t" \
  "v_cvt_pk_bf16_f32 v64, v240, v241\n\t" \
  "v_cvt_pk_bf16_f32 v65, v242, v243\n\t" \
  "v_mfma_f32_16x16x32_bf16 v[192:195], v[18:21], v[88:91], v[34:37]\n\t" \
  "v_max_f32 v200, v200, v204\n\t" \
  "v_max_f32 v201, v201, v205\n\t" \
  "v_mfma_f32_16x16x32_bf16 v[196:199], v[14:17], v[88:91], v[38:41]\n\t" \
  "v_max_f32 v202, v202, v206\n\t" \
  "v_max_f32 v203, v203, v207\n\t" \
  "v_mfma_f32_16x16x32_bf16 v[184:187], v[14:17], v[84:87], v[34:37]\n\t" \
  "v_max_f32 v208, v208, v212\n\t" \
  "v_max_f32 v209, v209, v213\n\t" \
  "v_mfma_f32_16x16x32_bf16 v[188:191], v[10:13], v[84:87], v[42:45]\n\t" \
  "v_max_f32 v210, v210, v214\n\t" \
  "v_max_f32 v211, v211, v215\n\t" \
  "v_max_f32 v192, v192, v196\n\t" \
  "v_max_f32 v193, v193, v197\n\t" \
  "v_max_f32 v194, v194, v198\n\t" \
  "v_max3_f32 v106, v208, v209, v210\n\t" \
  "v_max3_f32 v104, v200, v201, v202\n\t" \
  "v_max_f32 v195, v195, v199\n\t" \
  "v_max_f32 v184, v184, v188\n\t" \
  "v_max_f32 v185, v185, v189\n\t" \
  "v_max_f32 v186, v186, v190\n\t" \
  "v_max3_f32 v102, v192, v193, v194\n\t" \
  "v_max_f32 v187, v187, v191\n\t" \
  "v_max3_f32 v100, v184, v185, v186\n\t" \
  "v_max_f32 v106, v106, v211\n\t" \
  "v_max_f32 v104, v104, v203\n\t" \
  "v_max_f32 v102, v102, v195\n\t" \
  "v_max_f32 v100, v100, v187\n\t" \
  "v_permlane16_swap_b32 v104, v106\n\t" \
  "v_cvt_pk_bf16_f32 v74, v244, v245\n\t" \
  "v_permlane16_swap_b32 v100, v102\n\t" \
  "v_max_f32 v104, v104, v106\n\t" \
  "v_max_f32 v100, v100, v102\n\t" \
  "v_cvt_pk_bf16_f32 v75, v246, v247\n\t" \
  "v_cvt_pk_bf16_f32 v68, v224, v225\n\t" \
  "v_permlane32_swap_b32 v100, v104\n\t" \
  "v_cvt_pk_bf16_f32 v69, v226, v227\n\t" \
  "v_max_f32 v100, v100, v104\n\t" \
  "v_mul_f32 v100, 0xbe8293ee, v100\n\t" \
  "v_mov_b32 v104, v100\n\t" \
  "v_cvt_pk_bf16_f32 v78, v228, v229\n\t" \
  "v_cvt_pk_bf16_f32 v79, v230, v231\n\t" \
  "v_permlane32_swap_b32 v100, v104\n\t" \
  "v_cvt_pk_bf16_f32 v70, v216, v217\n\t" \
  "v_mov_b32 v106, v104\n\t" \
  "v_mov_b32 v102, v100\n\t" \
  "v_cvt_pk_bf16_f32 v71, v218, v219\n\t" \
  "v_permlane16_swap_b32 v104, v106\n\t" \
  "v_permlane16_swap_b32 v100, v102\n\t" \
  "v_fma_f32 v208, v208, %16, v106\n\t" \
  "v_fma_f32 v209, v209, %16, v106\n\t" \
  "v_fma_f32 v210, v210, %16, v106\n\t" \
  "v_fma_f32 v211, v211, %16, v106\n\t" \
  "v_exp_f32 v208, v208\n\t" \
  "v_exp_f32 v209, v209\n\t" \
  "v_exp_f32 v210, v210\n\t" \
  "v_exp_f32 v211, v211\n\t" \
  "v_cvt_pk_bf16_f32 v212, v208, v209\n\t" \
  "v_cvt_pk_bf16_f32 v213, v210, v211\n\t" \
  "v_bfi_b32 v214, v46, 0, v212\n\t" \
  "v_bfi_b32 v215, v47, 0, v213\n\t" \
  "v_and_b32 v212, v46, v212\n\t" \
  "v_and_b32 v213, v47, v213\n\t" \
  "v_fma_f32 v200, v200, %16, v104\n\t" \
  "v_fma_f32 v201, v201, %16, v104\n\t" \
  "v_mfma_f32_16x16x32_bf16 v[208:211], v[48:51], v[212:215], 0\n\t" \
  "v_fma_f32 v202, v202, %16, v104\n\t" \
  "v_fma_f32 v203, v203, %16, v104\n\t" \
  "v_mfma_f32_16x16x32_bf16 v[176:179], v[64:67], v[212:215], 0\n\t" \
  "v_exp_f32 v200, v200\n\t" \
  "v_mfma_f32_16x16x32_bf16 v[180:183], v[74:77], v[212:215], 0\n\t" \
  "v_exp_f32 v201, v201\n\t" \
  "v_exp_f32 v202, v202\n\t" \
  "v_exp_f32 v203, v203\n\t" \
  "v_cvt_pk_bf16_f32 v204, v200, v201\n\t" \
  "v_cvt_pk_bf16_f32 v205, v202, v203\n\t" \
  "v_bfi_b32 v206, v46, 0, v204\n\t" \
  "v_bfi_b32 v207, v47, 0, v205\n\t" \
  "v_rcp_f32 v106, v208\n\t" \
  "v_and_b32 v204, v46, v204\n\t" \
  "v_and_b32 v205, v47, v205\n\t" \
  "v_pk_mul_f32 v[176:177], v[176:177], v[106:107] op_sel_hi:[1,0]\n\t" \
  "v_pk_mul_f32 v[178:179], v[178:179], v[106:107] op_sel_hi:[1,0]\n\t" \
  "v_mfma_f32_16x16x32_bf16 v[200:203], v[48:51], v[204:207], 0\n\t" \
  "v_pk_mul_f32 v[180:181], v[180:181], v[106:107] op_sel_hi:[1,0]\n\t" \
  "v_pk_mul_f32 v[182:183], v[182:183], v[106:107] op_sel_hi:[1,0]\n\t" \
  "v_mfma_f32_16x16x32_bf16 v[168:171], v[66:69], v[204:207], 0\n\t" \
  "v_cvt_pk_bf16_f32 v240, v176, v177\n\t" \
  "v_cvt_pk_bf16_f32 v241, v178, v179\n\t" \
  "v_mfma_f32_16x16x32_bf16 v[172:175], v[76:79], v[204:207], 0\n\t" \
  "v_cvt_pk_bf16_f32 v242, v180, v181\n\t" \
  "v_cvt_pk_bf16_f32 v243, v182, v183\n\t" \
  "global_store_dwordx4 v1, v[240:243], %14 offset:0 sc1\n\t" \
  "v_fma_f32 v192, v192, %16, v102\n\t" \
  "v_fma_f32 v193, v193, %16, v102\n\t" \
  "v_fma_f32 v194, v194, %16, v102\n\t" \
  "v_fma_f32 v195, v195, %16, v102\n\t" \
  "v_exp_f32 v192, v192\n\t" \
  "v_exp_f32 v193, v193\n\t" \
  "v_exp_f32 v194, v194\n\t" \
  "v_exp_f32 v195, v195\n\t" \
  "v_cvt_pk_bf16_f32 v196, v192, v193\n\t" \
  "v_cvt_pk_bf16_f32 v197, v194, v195\n\t" \
  "v_bfi_b32 v198, v46, 0, v196\n\t" \
  "v_bfi_b32 v199, v47, 0, v197\n\t" \
  "v_rcp_f32 v104, v200\n\t" \
  "v_and_b32 v196, v46, v196\n\t" \
  "v_and_b32 v197, v47, v197\n\t" \
  "v_cvt_pk_bf16_f32 v80, v220, v221\n\t" \
  "v_cvt_pk_bf16_f32 v81, v222, v223\n\t" \
  "v_mfma_f32_16x16x32_bf16 v[192:195], v[48:51], v[196:199], 0\n\t" \
  "v_pk_mul_f32 v[168:169], v[168:169], v[104:105] op_sel_hi:[1,0]\n\t" \
  "v_pk_mul_f32 v[170:171], v[170:171], v[104:105] op_sel_hi:[1,0]\n\t" \
  "v_mfma_f32_16x16x32_bf16 v[160:163], v[68:71], v[196:199], 0\n\t" \
  "v_pk_mul_f32 v[172:173], v[172:173], v[104:105] op_sel_hi:[1,0]\n\t" \
  "v_pk_mul_f32 v[174:175], v[174:175], v[104:105] op_sel_hi:[1,0]\n\t" \
  "v_mfma_f32_16x16x32_bf16 v[164:167], v[78:81], v[196:199], 0\n\t" \
  "v_cvt_pk_bf16_f32 v232, v168, v169\n\t" \
  "v_cvt_pk_bf16_f32 v233, v170, v171\n\t" \
  "v_cvt_pk_bf16_f32 v234, v172, v173\n\t" \
  "v_cvt_pk_bf16_f32 v235, v174, v175\n\t" \
  "global_store_dwordx4 v1, v[232:235], %14 offset:-4096 sc1\n\t" \
  "v_fma_f32 v184, v184, %16, v100\n\t" \
  "v_fma_f32 v185, v185, %16, v100\n\t" \
  "v_fma_f32 v186, v186, %16, v100\n\t" \
  "v_fma_f32 v187, v187, %16, v100\n\t" \
  "v_exp_f32 v184, v184\n\t" \
  "v_exp_f32 v185, v185\n\t" \
  "v_exp_f32 v186, v186\n\t" \
  "v_exp_f32 v187, v187\n\t" \
  "v_cvt_pk_bf16_f32 v188, v184, v185\n\t" \
  "v_cvt_pk_bf16_f32 v189, v186, v187\n\t" \
  "v_bfi_b32 v190, v46, 0, v188\n\t" \
  "v_bfi_b32 v191, v47, 0, v189\n\t" \
  "v_rcp_f32 v102, v192\n\t" \
  "v_and_b32 v188, v46, v188\n\t" \
  "v_and_b32 v189, v47, v189\n\t" \
  "v_pk_mul_f32 v[160:161], v[160:161], v[102:103] op_sel_hi:[1,0]\n\t" \
  "v_pk_mul_f32 v[162:163], v[162:163], v[102:103] op_sel_hi:[1,0]\n\t" \
  "v_mfma_f32_16x16x32_bf16 v[184:187], v[48:51], v[188:191], 0\n\t" \
  "v_pk_mul_f32 v[164:165], v[164:165], v[102:103] op_sel_hi:[1,0]\n\t" \
  "v_pk_mul_f32 v[166:167], v[166:167], v[102:103] op_sel_hi:[1,0]\n\t" \
  "v_mfma_f32_16x16x32_bf16 v[152:155], v[70:73], v[188:191], 0\n\t" \
  "v_cvt_pk_bf16_f32 v224, v160, v161\n\t" \
  "v_cvt_pk_bf16_f32 v225, v162, v163\n\t" \
  "v_mfma_f32_16x16x32_bf16 v[156:159], v[80:83], v[188:191], 0\n\t" \
  "v_cvt_pk_bf16_f32 v226, v164, v165\n\t" \
  "v_cvt_pk_bf16_f32 v227, v166, v167\n\t" \
  "global_store_dwordx4 v4, v[224:227], %14 offset:0 sc1\n\t" \
  "v_rcp_f32 v100, v184\n\t" \
  "ds_read_b128 v[60:63], v56 offset:64\n\t" \
  "ds_read_b64_tr_b16 v[108:109], v5 offset:0\n\t" \
  "ds_read_b64_tr_b16 v[110:111], v5 offset:16\n\t" \
  "ds_read_b128 v[112:115], v6 offset:16384\n\t" \
  "ds_read_b128 v[116:119], v6 offset:15360\n\t" \
  "ds_read_b64_tr_b16 v[120:121], v5 offset:512\n\t" \
  "ds_read_b64_tr_b16 v[122:123], v5 offset:528\n\t" \
  "ds_read_b128 v[124:127], v6 offset:14336\n\t" \
  "ds_read_b64_tr_b16 v[128:129], v5 offset:1024\n\t" \
  "ds_read_b64_tr_b16 v[130:131], v5 offset:1040\n\t" \
  "ds_read_b128 v[132:135], v6 offset:13312\n\t" \
  "ds_read_b64_tr_b16 v[136:137], v5 offset:1536\n\t" \
  "ds_read_b64_tr_b16 v[138:139], v5 offset:1552\n\t" \
  "ds_read_b128 v[140:143], v6 offset:12288\n\t" \
  "ds_read_b64_tr_b16 v[144:145], v5 offset:2048\n\t" \
  "v_pk_mul_f32 v[152:153], v[152:153], v[100:101] op_sel_hi:[1,0]\n\t" \
  "v_pk_mul_f32 v[154:155], v[154:155], v[100:101] op_sel_hi:[1,0]\n\t" \
  "v_cvt_pk_bf16_f32 v216, v152, v153\n\t" \
  "v_pk_mul_f32 v[156:157], v[156:157], v[100:101] op_sel_hi:[1,0]\n\t" \
  "v_pk_mul_f32 v[158:159], v[158:159], v[100:101] op_sel_hi:[1,0]\n\t" \
  "v_cvt_pk_bf16_f32 v217, v154, v155\n\t" \
  "v_cvt_pk_bf16_f32 v218, v156, v157\n\t" \
  "v_cvt_pk_bf16_f32 v219, v158, v159\n\t" \
  "global_store_dwordx4 v4, v[216:219], %14 offset:-4096 sc1\n\t" \
  "ds_write_b32 v32, v0 offset:112\n\t" \
  "s_branch 9f\n\t" \
  "5:\n\t" \
  "ds_read_b32 v52, v33 offset:48\n\t" \
  "ds_read_b128 v[10:13], v31\n\t" \
  "s_waitcnt lgkmcnt(0)\n\t" \
  "v_readfirstlane_b32 vcc_lo, v52\n\t" \
  "s_nop 1\n\t" \
  "s_cmp_eq_u32 vcc_lo, 0\n\t" \
  "s_cbranch_scc1 5b\n\t" \
  "s_branch 4b\n\t" \
  "15:\n\t" \
  "ds_read_b128 v[10:13], v14 offset:128\n\t" \
  "s_waitcnt lgkmcnt(0)\n\t" \
  "v_and_b32 v10, v10, v11\n\t" \
  "v_and_b32 v12, v12, v13\n\t" \
  "v_and_b32 v10, v10, v12\n\t" \
  "s_nop 1\n\t" \
  "v_readfirstlane_b32 vcc_lo, v10\n\t" \
  "s_nop 1\n\t" \
  "s_cmp_eq_u32 vcc_lo, 0\n\t" \
  "s_cbranch_scc1 15b\n\t" \
  "s_branch 14b\n\t" \
  "8:\n\t" \
  "global_load_dwordx2 v[10:11], v51, %11\n\t" \
  "global_load_dwordx2 v[12:13], v51, %11 offset:128\n\t" \
  "v_xor_b32 v53, 64, v52\n\t" \
  "v_xor_b32 v55, 32, v54\n\t" \
  "v_add_u32 v56, 43008, v52\n\t" \
  "v_add_u32 v58, 43008, v54\n\t" \
  "v_xor_b32 v57, 64, v56\n\t" \
  "v_xor_b32 v59, 32, v58\n\t" \
  "v_add_u32 v60, 86016, v52\n\t" \
  "v_add_u32 v62, 86016, v54\n\t" \
  "v_xor_b32 v61, 64, v60\n\t" \
  "v_xor_b32 v63, 32, v62\n\t" \
  "s_mov_b32 vcc_hi, 0x05040100\n\t" \
  "s_waitcnt vmcnt(2)\n\t" \
  "s_barrier\n\t" \
  "ds_read_b64_tr_b16 v[64:65], v62 offset:34816\n\t" \
  "ds_read_b64_tr_b16 v[66:67], v62 offset:35328\n\t" \
  "ds_read_b128 v[68:71], v60 offset:2048\n\t" \
  "ds_read_b64_tr_b16 v[72:73], v62 offset:38912\n\t" \
  "ds_read_b64_tr_b16 v[74:75], v62 offset:39424\n\t" \
  "ds_read_b128 v[76:79], v61 offset:2048\n\t" \
  "ds_read_b128 v[80:83], v61 offset:4096\n\t" \
  "ds_read_b128 v[84:87], v60 offset:4096\n\t" \
  "ds_read_b128 v[88:91], v60 offset:6144\n\t" \
  "ds_read_b128 v[92:95], v61 offset:6144\n\t" \
  "ds_read_b128 v[96:99], v61 offset:8192\n\t" \
  "ds_read_b128 v[100:103], v60 offset:8192\n\t" \
  "ds_read_b64_tr_b16 v[104:105], v63 offset:34816\n\t" \
  "ds_read_b64_tr_b16 v[106:107], v63 offset:35328\n\t" \
  "ds_read_b64_tr_b16 v[108:109], v63 offset:38912\n\t" \
  "ds_read_b64_tr_b16 v[110:111], v63 offset:39424\n\t" \
  "s_waitcnt lgkmcnt(13)\n\t" \
  "v_mfma_f32_16x16x32_bf16 v[216:219], v[68:71], v[64:67], 0\n\t" \
  "ds_read_b64_tr_b16 v[112:113], v62 offset:26624\n\t" \
  "ds_read_b64_tr_b16 v[114:115], v62 offset:27136\n\t" \
  "s_waitcnt lgkmcnt(12)\n\t" \
  "v_mfma_f32_16x16x32_bf16 v[216:219], v[76:79], v[72:75], v[216:219]\n\t" \
  "ds_read_b64_tr_b16 v[116:117], v62 offset:30720\n\t" \
  "ds_read_b64_tr_b16 v[118:119], v62 offset:31232\n\t" \
  "s_waitcnt lgkmcnt(13)\n\t" \
  "v_mfma_f32_16x16x32_bf16 v[224:227], v[80:83], v[72:75], 0\n\t" \
  "ds_read_b128 v[120:123], v60 offset:0\n\t" \
  "s_waitcnt lgkmcnt(13)\n\t" \
  "v_mfma_f32_16x16x32_bf16 v[224:227], v[84:87], v[64:67], v[224:227]\n\t" \
  "ds_read_b128 v[124:127], v61 offset:0\n\t" \
  "s_waitcnt lgkmcnt(13)\n\t" \
  "v_mfma_f32_16x16x32_bf16 v[232:235], v[88:91], v[64:67], 0\n\t" \
  "ds_read_b64_tr_b16 v[128:129], v63 offset:26624\n\t" \
  "s_waitcnt lgkmcnt(13)\n\t" \
  "v_mfma_f32_16x16x32_bf16 v[232:235], v[92:95], v[72:75], v[232:235]\n\t" \
  "ds_read_b64_tr_b16 v[130:131], v63 offset:27136\n\t" \
  "s_waitcnt lgkmcnt(13)\n\t" \
  "v_mfma_f32_16x16x32_bf16 v[240:243], v[96:99], v[72:75], 0\n\t" \
  "ds_read_b64_tr_b16 v[132:133], v63 offset:30720\n\t" \
  "s_waitcnt lgkmcnt(9)\n\t" \
  "v_mfma_f32_16x16x32_bf16 v[240:243], v[100:103], v[64:67], v[240:243]\n\t" \
  "ds_read_b64_tr_b16 v[134:135], v63 offset:31232\n\t" \
  "v_mfma_f32_16x16x32_bf16 v[220:223], v[68:71], v[104:107], 0\n\t" \
  "ds_read_b64_tr_b16 v[136:137], v62 offset:18432\n\t" \
  "v_mfma_f32_16x16x32_bf16 v[220:223], v[76:79], v[108:111], v[220:223]\n\t" \
  "ds_read_b64_tr_b16 v[138:139], v62 offset:18944\n\t" \
  "v_mfma_f32_16x16x32_bf16 v[228:231], v[80:83], v[108:111], 0\n\t" \
  "ds_read_b64_tr_b16 v[140:141], v62 offset:22528\n\t" \
  "v_mfma_f32_16x16x32_bf16 v[228:231], v[84:87], v[104:107], v[228:231]\n\t" \
  "ds_read_b64_tr_b16 v[142:143], v62 offset:23040\n\t" \
  "v_mfma_f32_16x16x32_bf16 v[236:239], v[88:91], v[104:107], 0\n\t" \
  "ds_read_b64_tr_b16 v[144:145], v63 offset:18432\n\t" \
  "v_mfma_f32_16x16x32_bf16 v[236:239], v[92:95], v[108:111], v[236:239]\n\t" \
  "ds_read_b64_tr_b16 v[146:147], v63 offset:18944\n\t" \
  "v_mfma_f32_16x16x32_bf16 v[244:247], v[96:99], v[108:111], 0\n\t" \
  "ds_read_b64_tr_b16 v[148:149], v63 offset:22528\n\t" \
  "v_mfma_f32_16x16x32_bf16 v[244:247], v[100:103], v[104:107], v[244:247]\n\t" \
  "ds_read_b64_tr_b16 v[150:151], v63 offset:23040\n\t" \
  "s_waitcnt lgkmcnt(7)\n\t" \
  "v_mfma_f32_16x16x32_bf16 v[184:187], v[112:115], v[68:71], 0\n\t" \
  "v_mfma_f32_16x16x32_bf16 v[184:187], v[116:119], v[76:79], v[184:187]\n\t" \
  "v_mfma_f32_16x16x32_bf16 v[192:195], v[116:119], v[80:83], 0\n\t" \
  "v_mfma_f32_16x16x32_bf16 v[192:195], v[112:115], v[84:87], v[192:195]\n\t" \
  "v_mfma_f32_16x16x32_bf16 v[200:203], v[112:115], v[88:91], 0\n\t" \
  "v_mfma_f32_16x16x32_bf16 v[200:203], v[116:119], v[92:95], v[200:203]\n\t" \
  "v_mfma_f32_16x16x32_bf16 v[208:211], v[116:119], v[96:99], 0\n\t" \
  "v_mfma_f32_16x16x32_bf16 v[208:211], v[112:115], v[100:103], v[208:211]\n\t" \
  "v_mfma_f32_16x16x32_bf16 v[248:251], v[112:115], v[120:123], 0\n\t" \
  "v_mfma_f32_16x16x32_bf16 v[248:251], v[116:119], v[124:127], v[248:251]\n\t" \
  "v_mfma_f32_16x16x32_bf16 v[252:255], v[128:131], v[120:123], 0\n\t" \
  "v_mfma_f32_16x16x32_bf16 v[252:255], v[132:135], v[124:127], v[252:255]\n\t" \
  "v_mfma_f32_16x16x32_bf16 v[188:191], v[132:135], v[76:79], 0\n\t" \
  "v_mfma_f32_16x16x32_bf16 v[188:191], v[128:131], v[68:71], v[188:191]\n\t" \
  "s_waitcnt lgkmcnt(0)\n\t" \
  "s_getpc_b64 s[88:89]\n\t" \
  "s_branch 28b\n\t" \
  "s_barrier\n\t" \
  "v_mfma_f32_16x16x32_bf16 v[196:199], v[128:131], v[84:87], 0\n\t" \
  "ds_read_b64_tr_b16 v[72:73], v54 offset:34816\n\t" \
  "v_mfma_f32_16x16x32_bf16 v[196:199], v[132:135], v[80:83], v[196:199]\n\t" \
  "ds_read_b64_tr_b16 v[74:75], v54 offset:35328\n\t" \
  "v_mfma_f32_16x16x32_bf16 v[204:207], v[132:135], v[92:95], 0\n\t" \
  "ds_read_b128 v[64:67], v52 offset:2048\n\t" \
  "v_mfma_f32_16x16x32_bf16 v[204:207], v[128:131], v[88:91], v[204:207]\n\t" \
  "ds_read_b64_tr_b16 v[108:109], v54 offset:38912\n\t" \
  "v_mfma_f32_16x16x32_bf16 v[212:215], v[128:131], v[100:103], 0\n\t" \
  "ds_read_b64_tr_b16 v[110:111], v54 offset:39424\n\t" \
  "v_mfma_f32_16x16x32_bf16 v[212:215], v[132:135], v[96:99], v[212:215]\n\t" \
  "ds_read_b128 v[104:107], v53 offset:2048\n\t" \
  "v_mfma_f32_16x16x32_bf16 v[152:155], v[136:139], v[68:71], 0\n\t" \
  "ds_read_b128 v[112:115], v53 offset:4096\n\t" \
  "v_mfma_f32_16x16x32_bf16 v[152:155], v[140:143], v[76:79], v[152:155]\n\t" \
  "ds_read_b128 v[116:119], v52 offset:4096\n\t" \
  "v_mfma_f32_16x16x32_bf16 v[160:163], v[140:143], v[80:83], 0\n\t" \
  "ds_read_b128 v[120:123], v52 offset:6144\n\t" \
  "v_mfma_f32_16x16x32_bf16 v[160:163], v[136:139], v[84:87], v[160:163]\n\t" \
  "ds_read_b128 v[124:127], v53 offset:6144\n\t" \
  "v_mfma_f32_16x16x32_bf16 v[168:171], v[136:139], v[88:91], 0\n\t" \
  "ds_read_b128 v[128:131], v53 offset:8192\n\t" \
  "v_mfma_f32_16x16x32_bf16 v[168:171], v[140:143], v[92:95], v[168:171]\n\t" \
  "ds_read_b128 v[132:135], v52 offset:8192\n\t" \
  "v_mfma_f32_16x16x32_bf16 v[176:179], v[140:143], v[96:99], 0\n\t" \
  "ds_read_b64_tr_b16 v[140:141], v55 offset:34816\n\t" \
  "v_mfma_f32_16x16x32_bf16 v[176:179], v[136:139], v[100:103], v[176:179]\n\t" \
  "ds_read_b64_tr_b16 v[142:143], v55 offset:35328\n\t" \
  "v_mfma_f32_16x16x32_bf16 v[156:159], v[144:147], v[68:71], 0\n\t" \
  "ds_read_b64_tr_b16 v[136:137], v55 offset:38912\n\t" \
  "v_mfma_f32_16x16x32_bf16 v[156:159], v[148:151], v[76:79], v[156:159]\n\t" \
  "ds_read_b64_tr_b16 v[138:139], v55 offset:39424\n\t" \
  "v_mfma_f32_16x16x32_bf16 v[164:167], v[148:151], v[80:83], 0\n\t" \
  "ds_read_b64_tr_b16 v[68:69], v54 offset:26624\n\t" \
  "v_mfma_f32_16x16x32_bf16 v[164:167], v[144:147], v[84:87], v[164:167]\n\t" \
  "ds_read_b64_tr_b16 v[70:71], v54 offset:27136\n\t" \
  "v_mfma_f32_16x16x32_bf16 v[172:175], v[144:147], v[88:91], 0\n\t" \
  "ds_read_b64_tr_b16 v[76:77], v54 offset:30720\n\t" \
  "v_mfma_f32_16x16x32_bf16 v[172:175], v[148:151], v[92:95], v[172:175]\n\t" \
  "ds_read_b64_tr_b16 v[78:79], v54 offset:31232\n\t" \
  "v_mfma_f32_16x16x32_bf16 v[180:183], v[148:151], v[96:99], 0\n\t" \
  "ds_read_b128 v[80:83], v52 offset:0\n\t" \
  "v_mfma_f32_16x16x32_bf16 v[180:183], v[144:147], v[100:103], v[180:183]\n\t" \
  "ds_read_b128 v[84:87], v53 offset:0\n\t" \
  "s_waitcnt lgkmcnt(7)\n\t" \
  "v_mfma_f32_16x16x32_bf16 v[216:219], v[64:67], v[72:75], v[216:219]\n\t" \
  "ds_read_b64_tr_b16 v[88:89], v55 offset:26624\n\t" \
  "v_mfma_f32_16x16x32_bf16 v[216:219], v[104:107], v[108:111], v[216:219]\n\t" \
  "ds_read_b64_tr_b16 v[90:91], v55 offset:27136\n\t" \
  "v_mfma_f32_16x16x32_bf16 v[224:227], v[112:115], v[108:111], v[224:227]\n\t" \
  "ds_read_b64_tr_b16 v[92:93], v55 offset:30720\n\t" \
  "v_mfma_f32_16x16x32_bf16 v[224:227], v[116:119], v[72:75], v[224:227]\n\t" \
  "ds_read_b64_tr_b16 v[94:95], v55 offset:31232\n\t" \
  "v_mfma_f32_16x16x32_bf16 v[232:235], v[120:123], v[72:75], v[232:235]\n\t" \
  "ds_read_b64_tr_b16 v[96:97], v54 offset:18432\n\t" \
  "v_mfma_f32_16x16x32_bf16 v[232:235], v[124:127], v[108:111], v[232:235]\n\t" \
  "ds_read_b64_tr_b16 v[98:99], v54 offset:18944\n\t" \
  "v_mfma_f32_16x16x32_bf16 v[240:243], v[128:131], v[108:111], v[240:243]\n\t" \
  "ds_read_b64_tr_b16 v[148:149], v54 offset:22528\n\t" \
  "v_mfma_f32_16x16x32_bf16 v[240:243], v[132:135], v[72:75], v[240:243]\n\t" \
  "ds_read_b64_tr_b16 v[150:151], v54 offset:23040\n\t" \
  "v_mfma_f32_16x16x32_bf16 v[220:223], v[64:67], v[140:143], v[220:223]\n\t" \
  "ds_read_b64_tr_b16 v[100:101], v55 offset:18432\n\t" \
  "s_waitcnt lgkmcnt(7)\n\t" \
  "v_mfma_f32_16x16x32_bf16 v[220:223], v[104:107], v[136:139], v[220:223]\n\t" \
  "ds_read_b64_tr_b16 v[102:103], v55 offset:18944\n\t" \
  "v_mfma_f32_16x16x32_bf16 v[228:231], v[112:115], v[136:139], v[228:231]\n\t" \
  "ds_read_b64_tr_b16 v[144:145], v55 offset:22528\n\t" \
  "v_mfma_f32_16x16x32_bf16 v[228:231], v[116:119], v[140:143], v[228:231]\n\t" \
  "ds_read_b64_tr_b16 v[146:147], v55 offset:23040\n\t" \
  "v_mfma_f32_16x16x32_bf16 v[236:239], v[120:123], v[140:143], v[236:239]\n\t" \
  "v_mfma_f32_16x16x32_bf16 v[236:239], v[124:127], v[136:139], v[236:239]\n\t" \
  "v_mfma_f32_16x16x32_bf16 v[244:247], v[128:131], v[136:139], v[244:247]\n\t" \
  "v_mfma_f32_16x16x32_bf16 v[244:247], v[132:135], v[140:143], v[244:247]\n\t" \
  "v_mfma_f32_16x16x32_bf16 v[184:187], v[68:71], v[64:67], v[184:187]\n\t" \
  "v_mfma_f32_16x16x32_bf16 v[184:187], v[76:79], v[104:107], v[184:187]\n\t" \
  "v_mfma_f32_16x16x32_bf16 v[192:195], v[76:79], v[112:115], v[192:195]\n\t" \
  "v_mfma_f32_16x16x32_bf16 v[192:195], v[68:71], v[116:119], v[192:195]\n\t" \
  "v_mfma_f32_16x16x32_bf16 v[200:203], v[68:71], v[120:123], v[200:203]\n\t" \
  "v_mfma_f32_16x16x32_bf16 v[200:203], v[76:79], v[124:127], v[200:203]\n\t" \
  "v_mfma_f32_16x16x32_bf16 v[208:211], v[76:79], v[128:131], v[208:211]\n\t" \
  "v_mfma_f32_16x16x32_bf16 v[208:211], v[68:71], v[132:135], v[208:211]\n\t" \
  "v_mfma_f32_16x16x32_bf16 v[248:251], v[68:71], v[80:83], v[248:251]\n\t" \
  "v_mfma_f32_16x16x32_bf16 v[248:251], v[76:79], v[84:87], v[248:251]\n\t" \
  "v_mfma_f32_16x16x32_bf16 v[252:255], v[88:91], v[80:83], v[252:255]\n\t" \
  "s_waitcnt lgkmcnt(0)\n\t" \
  "v_mfma_f32_16x16x32_bf16 v[252:255], v[92:95], v[84:87], v[252:255]\n\t" \
  "v_mfma_f32_16x16x32_bf16 v[188:191], v[92:95], v[104:107], v[188:191]\n\t" \
  "v_mfma_f32_16x16x32_bf16 v[188:191], v[88:91], v[64:67], v[188:191]\n\t" \
  "s_barrier\n\t" \
  "v_mfma_f32_16x16x32_bf16 v[196:199], v[88:91], v[116:119], v[196:199]\n\t" \
  "ds_read_b64_tr_b16 v[108:109], v58 offset:34816\n\t" \
  "v_mfma_f32_16x16x32_bf16 v[196:199], v[92:95], v[112:115], v[196:199]\n\t" \
  "ds_read_b64_tr_b16 v[110:111], v58 offset:35328\n\t" \
  "v_mfma_f32_16x16x32_bf16 v[204:207], v[92:95], v[124:127], v[204:207]\n\t" \
  "ds_read_b128 v[72:75], v56 offset:2048\n\t" \
  "v_mfma_f32_16x16x32_bf16 v[204:207], v[88:91], v[120:123], v[204:207]\n\t" \
  "ds_read_b64_tr_b16 v[136:137], v58 offset:38912\n\t" \
  "v_mfma_f32_16x16x32_bf16 v[212:215], v[88:91], v[132:135], v[212:215]\n\t" \
  "ds_read_b64_tr_b16 v[138:139], v58 offset:39424\n\t" \
  "v_mfma_f32_16x16x32_bf16 v[212:215], v[92:95], v[128:131], v[212:215]\n\t" \
  "ds_read_b128 v[140:143], v57 offset:2048\n\t" \
  "v_mfma_f32_16x16x32_bf16 v[152:155], v[96:99], v[64:67], v[152:155]\n\t" \
  "ds_read_b128 v[68:71], v57 offset:4096\n\t" \
  "v_mfma_f32_16x16x32_bf16 v[152:155], v[148:151], v[104:107], v[152:155]\n\t" \
  "ds_read_b128 v[76:79], v56 offset:4096\n\t" \
  "v_mfma_f32_16x16x32_bf16 v[160:163], v[148:151], v[112:115], v[160:163]\n\t" \
  "ds_read_b128 v[80:83], v56 offset:6144\n\t" \
  "v_mfma_f32_16x16x32_bf16 v[160:163], v[96:99], v[116:119], v[160:163]\n\t" \
  "ds_read_b128 v[84:87], v57 offset:6144\n\t" \
  "v_mfma_f32_16x16x32_bf16 v[168:171], v[96:99], v[120:123], v[168:171]\n\t" \
  "ds_read_b128 v[88:91], v57 offset:8192\n\t" \
  "v_mfma_f32_16x16x32_bf16 v[168:171], v[148:151], v[124:127], v[168:171]\n\t" \
  "ds_read_b128 v[92:95], v56 offset:8192\n\t" \
  "v_mfma_f32_16x16x32_bf16 v[176:179], v[148:151], v[128:131], v[176:179]\n\t" \
  "ds_read_b64_tr_b16 v[148:149], v59 offset:34816\n\t" \
  "v_mfma_f32_16x16x32_bf16 v[176:179], v[96:99], v[132:135], v[176:179]\n\t" \
  "ds_read_b64_tr_b16 v[150:151], v59 offset:35328\n\t" \
  "v_mfma_f32_16x16x32_bf16 v[156:159], v[100:103], v[64:67], v[156:159]\n\t" \
  "ds_read_b64_tr_b16 v[96:97], v59 offset:38912\n\t" \
  "v_mfma_f32_16x16x32_bf16 v[156:159], v[144:147], v[104:107], v[156:159]\n\t" \
  "ds_read_b64_tr_b16 v[98:99], v59 offset:39424\n\t" \
  "v_mfma_f32_16x16x32_bf16 v[164:167], v[144:147], v[112:115], v[164:167]\n\t" \
  "ds_read_b64_tr_b16 v[64:65], v58 offset:26624\n\t" \
  "v_mfma_f32_16x16x32_bf16 v[164:167], v[100:103], v[116:119], v[164:167]\n\t" \
  "ds_read_b64_tr_b16 v[66:67], v58 offset:27136\n\t" \
  "v_mfma_f32_16x16x32_bf16 v[172:175], v[100:103], v[120:123], v[172:175]\n\t" \
  "ds_read_b64_tr_b16 v[104:105], v58 offset:30720\n\t" \
  "v_mfma_f32_16x16x32_bf16 v[172:175], v[144:147], v[124:127], v[172:175]\n\t" \
  "ds_read_b64_tr_b16 v[106:107], v58 offset:31232\n\t" \
  "v_mfma_f32_16x16x32_bf16 v[180:183], v[144:147], v[128:131], v[180:183]\n\t" \
  "ds_read_b128 v[112:115], v56 offset:0\n\t" \
  "v_mfma_f32_16x16x32_bf16 v[180:183], v[100:103], v[132:135], v[180:183]\n\t" \
  "ds_read_b128 v[116:119], v57 offset:0\n\t" \
  "s_waitcnt lgkmcnt(7)\n\t" \
  "v_mfma_f32_16x16x32_bf16 v[216:219], v[72:75], v[108:111], v[216:219]\n\t" \
  "ds_read_b64_tr_b16 v[120:121], v59 offset:26624\n\t" \
  "v_mfma_f32_16x16x32_bf16 v[216:219], v[140:143], v[136:139], v[216:219]\n\t" \
  "ds_read_b64_tr_b16 v[122:123], v59 offset:27136\n\t" \
  "v_mfma_f32_16x16x32_bf16 v[224:227], v[68:71], v[136:139], v[224:227]\n\t" \
  "ds_read_b64_tr_b16 v[124:125], v59 offset:30720\n\t" \
  "v_mfma_f32_16x16x32_bf16 v[224:227], v[76:79], v[108:111], v[224:227]\n\t" \
  "ds_read_b64_tr_b16 v[126:127], v59 offset:31232\n\t" \
  "v_mfma_f32_16x16x32_bf16 v[232:235], v[80:83], v[108:111], v[232:235]\n\t" \
  "ds_read_b64_tr_b16 v[128:129], v58 offset:18432\n\t" \
  "v_mfma_f32_16x16x32_bf16 v[232:235], v[84:87], v[136:139], v[232:235]\n\t" \
  "ds_read_b64_tr_b16 v[130:131], v58 offset:18944\n\t" \
  "v_mfma_f32_16x16x32_bf16 v[240:243], v[88:91], v[136:139], v[240:243]\n\t" \
  "ds_read_b64_tr_b16 v[144:145], v58 offset:22528\n\t" \
  "v_mfma_f32_16x16x32_bf16 v[240:243], v[92:95], v[108:111], v[240:243]\n\t" \
  "ds_read_b64_tr_b16 v[146:147], v58 offset:23040\n\t" \
  "v_mfma_f32_16x16x32_bf16 v[220:223], v[72:75], v[148:151], v[220:223]\n\t" \
  "ds_read_b64_tr_b16 v[100:101], v59 offset:18432\n\t" \
  "s_waitcnt lgkmcnt(7)\n\t" \
  "v_mfma_f32_16x16x32_bf16 v[220:223], v[140:143], v[96:99], v[220:223]\n\t" \
  "ds_read_b64_tr_b16 v[102:103], v59 offset:18944\n\t" \
  "v_mfma_f32_16x16x32_bf16 v[228:231], v[68:71], v[96:99], v[228:231]\n\t" \
  "ds_read_b64_tr_b16 v[132:133], v59 offset:22528\n\t" \
  "v_mfma_f32_16x16x32_bf16 v[228:231], v[76:79], v[148:151], v[228:231]\n\t" \
  "ds_read_b64_tr_b16 v[134:135], v59 offset:23040\n\t" \
  "v_mfma_f32_16x16x32_bf16 v[236:239], v[80:83], v[148:151], v[236:239]\n\t" \
  "v_mfma_f32_16x16x32_bf16 v[236:239], v[84:87], v[96:99], v[236:239]\n\t" \
  "v_mfma_f32_16x16x32_bf16 v[244:247], v[88:91], v[96:99], v[244:247]\n\t" \
  "v_mfma_f32_16x16x32_bf16 v[244:247], v[92:95], v[148:151], v[244:247]\n\t" \
  "v_mfma_f32_16x16x32_bf16 v[184:187], v[64:67], v[72:75], v[184:187]\n\t" \
  "v_mfma_f32_16x16x32_bf16 v[184:187], v[104:107], v[140:143], v[184:187]\n\t" \
  "v_mfma_f32_16x16x32_bf16 v[192:195], v[104:107], v[68:71], v[192:195]\n\t" \
  "v_mfma_f32_16x16x32_bf16 v[192:195], v[64:67], v[76:79], v[192:195]\n\t" \
  "v_mfma_f32_16x16x32_bf16 v[200:203], v[64:67], v[80:83], v[200:203]\n\t" \
  "v_mfma_f32_16x16x32_bf16 v[200:203], v[104:107], v[84:87], v[200:203]\n\t" \
  "v_mfma_f32_16x16x32_bf16 v[208:211], v[104:107], v[88:91], v[208:211]\n\t" \
  "v_mfma_f32_16x16x32_bf16 v[208:211], v[64:67], v[92:95], v[208:211]\n\t" \
  "503:\n\t" \
  "s_cbranch_execz 507f\n\t" \
  "v_mfma_f32_16x16x32_bf16 v[248:251], v[64:67], v[112:115], v[248:251]\n\t" \
  "v_mfma_f32_16x16x32_bf16 v[248:251], v[104:107], v[116:119], v[248:251]\n\t" \
  "v_mfma_f32_16x16x32_bf16 v[252:255], v[120:123], v[112:115], v[252:255]\n\t" \
  "s_waitcnt lgkmcnt(0)\n\t" \
  "v_mfma_f32_16x16x32_bf16 v[252:255], v[124:127], v[116:119], v[252:255]\n\t" \
  "v_mfma_f32_16x16x32_bf16 v[188:191], v[124:127], v[140:143], v[188:191]\n\t" \
  "v_mfma_f32_16x16x32_bf16 v[188:191], v[120:123], v[72:75], v[188:191]\n\t" \
  "s_barrier\n\t" \
  "v_mfma_f32_16x16x32_bf16 v[196:199], v[120:123], v[76:79], v[196:199]\n\t" \
  "ds_read_b64_tr_b16 v[136:137], v62 offset:34816\n\t" \
  "v_bfe_u32 v30, v4, 9, 1\n\t" \
  "v_mfma_f32_16x16x32_bf16 v[196:199], v[124:127], v[68:71], v[196:199]\n\t" \
  "ds_read_b64_tr_b16 v[138:139], v62 offset:35328\n\t" \
  "v_mad_i32_i24 v30, v30, -16, v0\n\t" \
  "v_mfma_f32_16x16x32_bf16 v[204:207], v[124:127], v[84:87], v[204:207]\n\t" \
  "ds_read_b128 v[108:111], v60 offset:2048\n\t" \
  "ds_read_b128 v[26:29], v0 offset:1552\n\t" \
  "v_mfma_f32_16x16x32_bf16 v[204:207], v[120:123], v[80:83], v[204:207]\n\t" \
  "ds_read_b64_tr_b16 v[96:97], v62 offset:38912\n\t" \
  "ds_read_b128 v[30:33], v30 offset:1040\n\t" \
  "v_mfma_f32_16x16x32_bf16 v[212:215], v[120:123], v[92:95], v[212:215]\n\t" \
  "ds_read_b64_tr_b16 v[98:99], v62 offset:39424\n\t" \
  "ds_read_b128 v[34:37], v0 offset:512\n\t" \
  "v_mfma_f32_16x16x32_bf16 v[212:215], v[124:127], v[88:91], v[212:215]\n\t" \
  "ds_read_b128 v[148:151], v61 offset:2048\n\t" \
  "ds_read_b64_tr_b16 v[38:39], v1 offset:512\n\t" \
  "v_mfma_f32_16x16x32_bf16 v[152:155], v[128:131], v[72:75], v[152:155]\n\t" \
  "ds_read_b128 v[64:67], v61 offset:4096\n\t" \
  "ds_read_b64_tr_b16 v[40:41], v1 offset:0\n\t" \
  "v_mfma_f32_16x16x32_bf16 v[152:155], v[144:147], v[140:143], v[152:155]\n\t" \
  "ds_read_b128 v[104:107], v60 offset:4096\n\t" \
  "s_waitcnt vmcnt(0)\n\t" \
  "v_mfma_f32_16x16x32_bf16 v[160:163], v[144:147], v[68:71], v[160:163]\n\t" \
  "ds_read_b128 v[112:115], v60 offset:6144\n\t" \
  "v_perm_b32 v18, v10, v10, v14\n\t" \
  "v_mfma_f32_16x16x32_bf16 v[160:163], v[128:131], v[76:79], v[160:163]\n\t" \
  "ds_read_b128 v[116:119], v61 offset:6144\n\t" \
  "v_perm_b32 v19, v10, v10, v15\n\t" \
  "v_mfma_f32_16x16x32_bf16 v[168:171], v[128:131], v[80:83], v[168:171]\n\t" \
  "ds_read_b128 v[120:123], v61 offset:8192\n\t" \
  "v_perm_b32 v20, v10, v10, v16\n\t" \
  "v_mfma_f32_16x16x32_bf16 v[168:171], v[144:147], v[84:87], v[168:171]\n\t" \
  "ds_read_b128 v[124:127], v60 offset:8192\n\t" \
  "v_perm_b32 v21, v10, v10, v17\n\t" \
  "v_mfma_f32_16x16x32_bf16 v[176:179], v[144:147], v[88:91], v[176:179]\n\t" \
  "ds_read_b64_tr_b16 v[144:145], v63 offset:34816\n\t" \
  "v_perm_b32 v22, v11, v11, v14\n\t" \
  "v_mfma_f32_16x16x32_bf16 v[176:179], v[128:131], v[92:95], v[176:179]\n\t" \
  "ds_read_b64_tr_b16 v[146:147], v63 offset:35328\n\t" \
  "v_perm_b32 v23, v11, v11, v15\n\t" \
  "v_mfma_f32_16x16x32_bf16 v[156:159], v[100:103], v[72:75], v[156:159]\n\t" \
  "ds_read_b64_tr_b16 v[128:129], v63 offset:38912\n\t" \
  "v_perm_b32 v24, v11, v11, v16\n\t" \
  "v_mfma_f32_16x16x32_bf16 v[156:159], v[132:135], v[140:143], v[156:159]\n\t" \
  "ds_read_b64_tr_b16 v[130:131], v63 offset:39424\n\t" \
  "v_perm_b32 v25, v11, v11, v17\n\t" \
  "v_mfma_f32_16x16x32_bf16 v[164:167], v[132:135], v[68:71], v[164:167]\n\t" \
  "ds_read_b64_tr_b16 v[72:73], v62 offset:26624\n\t" \
  "v_mfma_f32_16x16x32_bf16 v[164:167], v[100:103], v[76:79], v[164:167]\n\t" \
  "ds_read_b64_tr_b16 v[74:75], v62 offset:27136\n\t" \
  "v_mfma_f32_16x16x32_bf16 v[172:175], v[100:103], v[80:83], v[172:175]\n\t" \
  "ds_read_b64_tr_b16 v[140:141], v62 offset:30720\n\t" \
  "s_waitcnt lgkmcnt(6)\n\t" \
  "v_mfma_f32_16x16x32_bf16 v[42:45], v[26:29], v[18:21], 0\n\t" \
  "v_mfma_f32_16x16x32_bf16 v[42:45], v[30:33], v[22:25], v[42:45]\n\t" \
  "v_mfma_f32_16x16x32_bf16 v[46:49], v[30:33], v[18:21], 0\n\t" \
  "v_mfma_f32_16x16x32_bf16 v[46:49], v[34:37], v[22:25], v[46:49]\n\t" \
  "ds_read_b128 v[26:29], v0 offset:1808\n\t" \
  "ds_read_b128 v[30:33], v0 offset:1296\n\t" \
  "v_mfma_f32_16x16x32_bf16 v[172:175], v[132:135], v[84:87], v[172:175]\n\t" \
  "ds_read_b64_tr_b16 v[142:143], v62 offset:31232\n\t" \
  "ds_read_b128 v[34:37], v0 offset:768\n\t" \
  "v_mfma_f32_16x16x32_bf16 v[180:183], v[132:135], v[88:91], v[180:183]\n\t" \
  "ds_read_b128 v[68:71], v60 offset:0\n\t" \
  "v_mfma_f32_16x16x32_bf16 v[180:183], v[100:103], v[92:95], v[180:183]\n\t" \
  "ds_read_b128 v[76:79], v61 offset:0\n\t" \
  "v_mfma_f32_16x16x32_bf16 v[216:219], v[108:111], v[136:139], v[216:219]\n\t" \
  "ds_read_b64_tr_b16 v[80:81], v63 offset:26624\n\t" \
  "v_lshlrev_b32 v50, 16, v38\n\t" \
  "v_mfma_f32_16x16x32_bf16 v[216:219], v[148:151], v[96:99], v[216:219]\n\t" \
  "ds_read_b64_tr_b16 v[82:83], v63 offset:27136\n\t" \
  "v_lshlrev_b32 v51, 16, v40\n\t" \
  "v_mfma_f32_16x16x32_bf16 v[224:227], v[64:67], v[96:99], v[224:227]\n\t" \
  "ds_read_b64_tr_b16 v[84:85], v63 offset:30720\n\t" \
  "v_mul_f32 v42, v42, v50\n\t" \
  "v_mfma_f32_16x16x32_bf16 v[224:227], v[104:107], v[136:139], v[224:227]\n\t" \
  "ds_read_b64_tr_b16 v[86:87], v63 offset:31232\n\t" \
  "v_mul_f32 v46, v46, v51\n\t" \
  "v_mfma_f32_16x16x32_bf16 v[232:235], v[112:115], v[136:139], v[232:235]\n\t" \
  "ds_read_b64_tr_b16 v[88:89], v62 offset:18432\n\t" \
  "v_cvt_pk_bf16_f32 v42, v42, v46\n\t" \
  "v_mfma_f32_16x16x32_bf16 v[232:235], v[116:119], v[96:99], v[232:235]\n\t" \
  "ds_read_b64_tr_b16 v[90:91], v62 offset:18944\n\t" \
  "v_and_b32 v50, 0xffff0000, v38\n\t" \
  "v_mfma_f32_16x16x32_bf16 v[240:243], v[120:123], v[96:99], v[240:243]\n\t" \
  "ds_read_b64_tr_b16 v[132:133], v62 offset:22528\n\t" \
  "v_and_b32 v51, 0xffff0000, v40\n\t" \
  "v_mfma_f32_16x16x32_bf16 v[240:243], v[124:127], v[136:139], v[240:243]\n\t" \
  "ds_read_b64_tr_b16 v[134:135], v62 offset:23040\n\t" \
  "v_mul_f32 v43, v43, v50\n\t" \
  "s_waitcnt lgkmcnt(7)\n\t" \
  "v_mfma_f32_16x16x32_bf16 v[220:223], v[108:111], v[144:147], v[220:223]\n\t" \
  "ds_read_b64_tr_b16 v[92:93], v63 offset:18432\n\t" \
  "v_mul_f32 v47, v47, v51\n\t" \
  "v_mfma_f32_16x16x32_bf16 v[220:223], v[148:151], v[128:131], v[220:223]\n\t" \
  "ds_read_b64_tr_b16 v[94:95], v63 offset:18944\n\t" \
  "v_cvt_pk_bf16_f32 v43, v43, v47\n\t" \
  "v_mfma_f32_16x16x32_bf16 v[228:231], v[64:67], v[128:131], v[228:231]\n\t" \
  "ds_read_b64_tr_b16 v[100:101], v63 offset:22528\n\t" \
  "v_lshlrev_b32 v50, 16, v39\n\t" \
  "v_mfma_f32_16x16x32_bf16 v[228:231], v[104:107], v[144:147], v[228:231]\n\t" \
  "ds_read_b64_tr_b16 v[102:103], v63 offset:23040\n\t" \
  "v_lshlrev_b32 v51, 16, v41\n\t" \
  "v_mfma_f32_16x16x32_bf16 v[236:239], v[112:115], v[144:147], v[236:239]\n\t" \
  "v_mul_f32 v44, v44, v50\n\t" \
  "v_mul_f32 v48, v48, v51\n\t" \
  "v_mfma_f32_16x16x32_bf16 v[236:239], v[116:119], v[128:131], v[236:239]\n\t" \
  "v_cvt_pk_bf16_f32 v44, v44, v48\n\t" \
  "v_and_b32 v50, 0xffff0000, v39\n\t" \
  "v_mfma_f32_16x16x32_bf16 v[244:247], v[120:123], v[128:131], v[244:247]\n\t" \
  "v_and_b32 v51, 0xffff0000, v41\n\t" \
  "v_mul_f32 v45, v45, v50\n\t" \
  "v_mfma_f32_16x16x32_bf16 v[244:247], v[124:127], v[144:147], v[244:247]\n\t" \
  "v_mul_f32 v49, v49, v51\n\t" \
  "ds_read_b64_tr_b16 v[38:39], v1 offset:768\n\t" \
  "v_mfma_f32_16x16x32_bf16 v[184:187], v[72:75], v[108:111], v[184:187]\n\t" \
  "ds_read_b64_tr_b16 v[40:41], v1 offset:256\n\t" \
  "v_cvt_pk_bf16_f32 v45, v45, v49\n\t" \
  "v_mfma_f32_16x16x32_bf16 v[184:187], v[140:143], v[148:151], v[184:187]\n\t" \
  "v_mfma_f32_16x16x32_bf16 v[192:195], v[140:143], v[64:67], v[192:195]\n\t" \
  "v_mfma_f32_16x16x32_bf16 v[192:195], v[72:75], v[104:107], v[192:195]\n\t" \
  "v_mfma_f32_16x16x32_bf16 v[200:203], v[72:75], v[112:115], v[200:203]\n\t" \
  "v_mfma_f32_16x16x32_bf16 v[200:203], v[140:143], v[116:119], v[200:203]\n\t" \
  "v_mfma_f32_16x16x32_bf16 v[208:211], v[140:143], v[120:123], v[208:211]\n\t" \
  "v_mfma_f32_16x16x32_bf16 v[208:211], v[72:75], v[124:127], v[208:211]\n\t" \
  "v_mfma_f32_16x16x32_bf16 v[248:251], v[72:75], v[68:71], v[248:251]\n\t" \
  "v_mfma_f32_16x16x32_bf16 v[248:251], v[140:143], v[76:79], v[248:251]\n\t" \
  "s_waitcnt lgkmcnt(0)\n\t" \
  "v_mfma_f32_16x16x32_bf16 v[252:255], v[80:83], v[68:71], v[252:255]\n\t" \
  "global_store_dwordx4 v2, v[42:45], %24 offset:0 sc1\n\t" \
  "v_mfma_f32_16x16x32_bf16 v[252:255], v[84:87], v[76:79], v[252:255]\n\t" \
  "s_nop 0\n\t" \
  "v_mfma_f32_16x16x32_bf16 v[42:45], v[26:29], v[18:21], 0\n\t" \
  "v_mfma_f32_16x16x32_bf16 v[42:45], v[30:33], v[22:25], v[42:45]\n\t" \
  "v_mfma_f32_16x16x32_bf16 v[46:49], v[30:33], v[18:21], 0\n\t" \
  "v_mfma_f32_16x16x32_bf16 v[46:49], v[34:37], v[22:25], v[46:49]\n\t" \
  "v_bfe_u32 v30, v4, 9, 1\n\t" \
  "v_mad_i32_i24 v30, v30, -16, v0\n\t" \
  "v_mfma_f32_16x16x32_bf16 v[188:191], v[84:87], v[148:151], v[188:191]\n\t" \
  "ds_read_b128 v[26:29], v0 offset:1584\n\t" \
  "ds_read_b128 v[30:33], v30 offset:1072\n\t" \
  "v_mfma_f32_16x16x32_bf16 v[188:191], v[80:83], v[108:111], v[188:191]\n\t" \
  "ds_read_b128 v[34:37], v0 offset:544\n\t" \
  "v_perm_b32 v18, v12, v12, v14\n\t" \
  "s_waitcnt lgkmcnt(0)\n\t" \
  "s_barrier\n\t" \
  "v_mfma_f32_16x16x32_bf16 v[196:199], v[80:83], v[104:107], v[196:199]\n\t" \
  "ds_read_b64_tr_b16 v[96:97], v54 offset:34816\n\t" \
  "v_perm_b32 v19, v12, v12, v15\n\t" \
  "v_mfma_f32_16x16x32_bf16 v[196:199], v[84:87], v[64:67], v[196:199]\n\t" \
  "ds_read_b64_tr_b16 v[98:99], v54 offset:35328\n\t" \
  "v_perm_b32 v20, v12, v12, v16\n\t" \
  "v_mfma_f32_16x16x32_bf16 v[204:207], v[84:87], v[116:119], v[204:207]\n\t" \
  "ds_read_b128 v[136:139], v52 offset:2048\n\t" \
  "v_perm_b32 v21, v12, v12, v17\n\t" \
  "v_mfma_f32_16x16x32_bf16 v[204:207], v[80:83], v[112:115], v[204:207]\n\t" \
  "ds_read_b64_tr_b16 v[128:129], v54 offset:38912\n\t" \
  "v_perm_b32 v22, v13, v13, v14\n\t" \
  "v_mfma_f32_16x16x32_bf16 v[212:215], v[80:83], v[124:127], v[212:215]\n\t" \
  "ds_read_b64_tr_b16 v[130:131], v54 offset:39424\n\t" \
  "v_perm_b32 v23, v13, v13, v15\n\t" \
  "v_mfma_f32_16x16x32_bf16 v[212:215], v[84:87], v[120:123], v[212:215]\n\t" \
  "ds_read_b128 v[144:147], v53 offset:2048\n\t" \
  "v_perm_b32 v24, v13, v13, v16\n\t" \
  "v_mfma_f32_16x16x32_bf16 v[152:155], v[88:91], v[108:111], v[152:155]\n\t" \
  "ds_read_b128 v[72:75], v53 offset:4096\n\t" \
  "v_perm_b32 v25, v13, v13, v17\n\t" \
  "v_mfma_f32_16x16x32_bf16 v[152:155], v[132:135], v[148:151], v[152:155]\n\t" \
  "ds_read_b128 v[140:143], v52 offset:4096\n\t" \
  "v_lshlrev_b32 v50, 16, v38\n\t" \
  "v_mfma_f32_16x16x32_bf16 v[160:163], v[132:135], v[64:67], v[160:163]\n\t" \
  "ds_read_b128 v[68:71], v52 offset:6144\n\t" \
  "v_lshlrev_b32 v51, 16, v40\n\t" \
  "v_mfma_f32_16x16x32_bf16 v[160:163], v[88:91], v[104:107], v[160:163]\n\t" \
  "ds_read_b128 v[76:79], v53 offset:6144\n\t" \
  "v_mul_f32 v42, v42, v50\n\t" \
  "v_mfma_f32_16x16x32_bf16 v[168:171], v[88:91], v[112:115], v[168:171]\n\t" \
  "ds_read_b128 v[80:83], v53 offset:8192\n\t" \
  "v_mul_f32 v46, v46, v51\n\t" \
  "v_mfma_f32_16x16x32_bf16 v[168:171], v[132:135], v[116:119], v[168:171]\n\t" \
  "ds_read_b128 v[84:87], v52 offset:8192\n\t" \
  "v_cvt_pk_bf16_f32 v42, v42, v46\n\t" \
  "v_mfma_f32_16x16x32_bf16 v[176:179], v[132:135], v[120:123], v[176:179]\n\t" \
  "ds_read_b64_tr_b16 v[132:133], v55 offset:34816\n\t" \
  "v_and_b32 v50, 0xffff0000, v38\n\t" \
  "v_mfma_f32_16x16x32_bf16 v[176:179], v[88:91], v[124:127], v[176:179]\n\t" \
  "ds_read_b64_tr_b16 v[134:135], v55 offset:35328\n\t" \
  "v_and_b32 v51, 0xffff0000, v40\n\t" \
  "v_mfma_f32_16x16x32_bf16 v[156:159], v[92:95], v[108:111], v[156:159]\n\t" \
  "ds_read_b64_tr_b16 v[88:89], v55 offset:38912\n\t" \
  "v_mul_f32 v43, v43, v50\n\t" \
  "v_mfma_f32_16x16x32_bf16 v[156:159], v[100:103], v[148:151], v[156:159]\n\t" \
  "ds_read_b64_tr_b16 v[90:91], v55 offset:39424\n\t" \
  "v_mul_f32 v47, v47, v51\n\t" \
  "v_mfma_f32_16x16x32_bf16 v[164:167], v[100:103], v[64:67], v[164:167]\n\t" \
  "ds_read_b64_tr_b16 v[108:109], v54 offset:26624\n\t" \
  "v_cvt_pk_bf16_f32 v43, v43, v47\n\t" \
  "v_mfma_f32_16x16x32_bf16 v[164:167], v[92:95], v[104:107], v[164:167]\n\t" \
  "ds_read_b64_tr_b16 v[110:111], v54 offset:27136\n\t" \
  "v_lshlrev_b32 v50, 16, v39\n\t" \
  "v_mfma_f32_16x16x32_bf16 v[172:175], v[92:95], v[112:115], v[172:175]\n\t" \
  "ds_read_b64_tr_b16 v[148:149], v54 offset:30720\n\t" \
  "v_lshlrev_b32 v51, 16, v41\n\t" \
  "v_mfma_f32_16x16x32_bf16 v[172:175], v[100:103], v[116:119], v[172:175]\n\t" \
  "ds_read_b64_tr_b16 v[150:151], v54 offset:31232\n\t" \
  "v_mul_f32 v44, v44, v50\n\t" \
  "v_mfma_f32_16x16x32_bf16 v[180:183], v[100:103], v[120:123], v[180:183]\n\t" \
  "ds_read_b128 v[64:67], v52 offset:0\n\t" \
  "v_mul_f32 v48, v48, v51\n\t" \
  "v_mfma_f32_16x16x32_bf16 v[180:183], v[92:95], v[124:127], v[180:183]\n\t" \
  "ds_read_b128 v[104:107], v53 offset:0\n\t" \
  "v_cvt_pk_bf16_f32 v44, v44, v48\n\t" \
  "s_waitcnt lgkmcnt(7)\n\t" \
  "v_mfma_f32_16x16x32_bf16 v[216:219], v[136:139], v[96:99], v[216:219]\n\t" \
  "ds_read_b64_tr_b16 v[112:113], v55 offset:26624\n\t" \
  "v_and_b32 v50, 0xffff0000, v39\n\t" \
  "v_mfma_f32_16x16x32_bf16 v[216:219], v[144:147], v[128:131], v[216:219]\n\t" \
  "ds_read_b64_tr_b16 v[114:115], v55 offset:27136\n\t" \
  "v_and_b32 v51, 0xffff0000, v41\n\t" \
  "v_mfma_f32_16x16x32_bf16 v[224:227], v[72:75], v[128:131], v[224:227]\n\t" \
  "ds_read_b64_tr_b16 v[116:117], v55 offset:30720\n\t" \
  "v_mul_f32 v45, v45, v50\n\t" \
  "v_mfma_f32_16x16x32_bf16 v[224:227], v[140:143], v[96:99], v[224:227]\n\t" \
  "ds_read_b64_tr_b16 v[118:119], v55 offset:31232\n\t" \
  "v_mul_f32 v49, v49, v51\n\t" \
  "v_mfma_f32_16x16x32_bf16 v[232:235], v[68:71], v[96:99], v[232:235]\n\t" \
  "ds_read_b64_tr_b16 v[100:101], v54 offset:18432\n\t" \
  "ds_read_b64_tr_b16 v[38:39], v1 offset:544\n\t" \
  "v_mfma_f32_16x16x32_bf16 v[232:235], v[76:79], v[128:131], v[232:235]\n\t" \
  "ds_read_b64_tr_b16 v[102:103], v54 offset:18944\n\t" \
  "ds_read_b64_tr_b16 v[40:41], v1 offset:32\n\t" \
  "v_mfma_f32_16x16x32_bf16 v[240:243], v[80:83], v[128:131], v[240:243]\n\t" \
  "ds_read_b64_tr_b16 v[120:121], v54 offset:22528\n\t" \
  "v_cvt_pk_bf16_f32 v45, v45, v49\n\t" \
  "v_mfma_f32_16x16x32_bf16 v[240:243], v[84:87], v[96:99], v[240:243]\n\t" \
  "ds_read_b64_tr_b16 v[122:123], v54 offset:23040\n\t" \
  "v_mfma_f32_16x16x32_bf16 v[220:223], v[136:139], v[132:135], v[220:223]\n\t" \
  "ds_read_b64_tr_b16 v[92:93], v55 offset:18432\n\t" \
  "s_waitcnt lgkmcnt(9)\n\t" \
  "v_mfma_f32_16x16x32_bf16 v[220:223], v[144:147], v[88:91], v[220:223]\n\t" \
  "ds_read_b64_tr_b16 v[94:95], v55 offset:18944\n\t" \
  "v_mfma_f32_16x16x32_bf16 v[228:231], v[72:75], v[88:91], v[228:231]\n\t" \
  "ds_read_b64_tr_b16 v[124:125], v55 offset:22528\n\t" \
  "v_mfma_f32_16x16x32_bf16 v[228:231], v[140:143], v[132:135], v[228:231]\n\t" \
  "ds_read_b64_tr_b16 v[126:127], v55 offset:23040\n\t" \
  "v_mfma_f32_16x16x32_bf16 v[236:239], v[68:71], v[132:135], v[236:239]\n\t" \
  "v_mfma_f32_16x16x32_bf16 v[236:239], v[76:79], v[88:91], v[236:239]\n\t" \
  "v_mfma_f32_16x16x32_bf16 v[244:247], v[80:83], v[88:91], v[244:247]\n\t" \
  "v_mfma_f32_16x16x32_bf16 v[244:247], v[84:87], v[132:135], v[244:247]\n\t" \
  "v_mfma_f32_16x16x32_bf16 v[184:187], v[108:111], v[136:139], v[184:187]\n\t" \
  "v_mfma_f32_16x16x32_bf16 v[184:187], v[148:151], v[144:147], v[184:187]\n\t" \
  "v_mfma_f32_16x16x32_bf16 v[192:195], v[148:151], v[72:75], v[192:195]\n\t" \
  "v_mfma_f32_16x16x32_bf16 v[192:195], v[108:111], v[140:143], v[192:195]\n\t" \
  "v_mfma_f32_16x16x32_bf16 v[200:203], v[108:111], v[68:71], v[200:203]\n\t" \
  "v_mfma_f32_16x16x32_bf16 v[200:203], v[148:151], v[76:79], v[200:203]\n\t" \
  "v_mfma_f32_16x16x32_bf16 v[208:211], v[148:151], v[80:83], v[208:211]\n\t" \
  "v_mfma_f32_16x16x32_bf16 v[208:211], v[108:111], v[84:87], v[208:211]\n\t" \
  "v_mfma_f32_16x16x32_bf16 v[248:251], v[108:111], v[64:67], v[248:251]\n\t" \
  "v_mfma_f32_16x16x32_bf16 v[248:251], v[148:151], v[104:107], v[248:251]\n\t" \
  "v_mfma_f32_16x16x32_bf16 v[252:255], v[112:115], v[64:67], v[252:255]\n\t" \
  "global_store_dwordx4 v2, v[42:45], %24 offset:1024 sc1\n\t" \
  "v_add_u32 v2, 0x10000, v2\n\t" \
  "s_waitcnt lgkmcnt(0)\n\t" \
  "v_mfma_f32_16x16x32_bf16 v[252:255], v[116:119], v[104:107], v[252:255]\n\t" \
  "v_mfma_f32_16x16x32_bf16 v[188:191], v[116:119], v[144:147], v[188:191]\n\t" \
  "v_mfma_f32_16x16x32_bf16 v[42:45], v[26:29], v[18:21], 0\n\t" \
  "v_mfma_f32_16x16x32_bf16 v[42:45], v[30:33], v[22:25], v[42:45]\n\t" \
  "v_mfma_f32_16x16x32_bf16 v[46:49], v[30:33], v[18:21], 0\n\t" \
  "v_mfma_f32_16x16x32_bf16 v[46:49], v[34:37], v[22:25], v[46:49]\n\t" \
  "ds_read_b128 v[26:29], v0 offset:1840\n\t" \
  "ds_read_b128 v[30:33], v0 offset:1328\n\t" \
  "v_mfma_f32_16x16x32_bf16 v[188:191], v[112:115], v[136:139], v[188:191]\n\t" \
  "ds_read_b128 v[34:37], v0 offset:800\n\t" \
  "s_waitcnt lgkmcnt(0)\n\t" \
  "s_barrier\n\t" \
  "v_mfma_f32_16x16x32_bf16 v[196:199], v[112:115], v[140:143], v[196:199]\n\t" \
  "ds_read_b64_tr_b16 v[128:129], v58 offset:34816\n\t" \
  "v_mfma_f32_16x16x32_bf16 v[196:199], v[116:119], v[72:75], v[196:199]\n\t" \
  "ds_read_b64_tr_b16 v[130:131], v58 offset:35328\n\t" \
  "v_lshlrev_b32 v50, 16, v38\n\t" \
  "v_mfma_f32_16x16x32_bf16 v[204:207], v[116:119], v[76:79], v[204:207]\n\t" \
  "ds_read_b128 v[96:99], v56 offset:2048\n\t" \
  "v_lshlrev_b32 v51, 16, v40\n\t" \
  "v_mfma_f32_16x16x32_bf16 v[204:207], v[112:115], v[68:71], v[204:207]\n\t" \
  "ds_read_b64_tr_b16 v[88:89], v58 offset:38912\n\t" \
  "v_mul_f32 v42, v42, v50\n\t" \
  "v_mfma_f32_16x16x32_bf16 v[212:215], v[112:115], v[84:87], v[212:215]\n\t" \
  "ds_read_b64_tr_b16 v[90:91], v58 offset:39424\n\t" \
  "v_mul_f32 v46, v46, v51\n\t" \
  "v_mfma_f32_16x16x32_bf16 v[212:215], v[116:119], v[80:83], v[212:215]\n\t" \
  "ds_read_b128 v[132:135], v57 offset:2048\n\t" \
  "v_cvt_pk_bf16_f32 v42, v42, v46\n\t" \
  "v_mfma_f32_16x16x32_bf16 v[152:155], v[100:103], v[136:139], v[152:155]\n\t" \
  "ds_read_b128 v[108:111], v57 offset:4096\n\t" \
  "v_and_b32 v50, 0xffff0000, v38\n\t" \
  "v_mfma_f32_16x16x32_bf16 v[152:155], v[120:123], v[144:147], v[152:155]\n\t" \
  "ds_read_b128 v[148:151], v56 offset:4096\n\t" \
  "v_and_b32 v51, 0xffff0000, v40\n\t" \
  "v_mfma_f32_16x16x32_bf16 v[160:163], v[120:123], v[72:75], v[160:163]\n\t" \
  "ds_read_b128 v[64:67], v56 offset:6144\n\t" \
  "v_mul_f32 v43, v43, v50\n\t" \
  "v_mfma_f32_16x16x32_bf16 v[160:163], v[100:103], v[140:143], v[160:163]\n\t" \
  "ds_read_b128 v[104:107], v57 offset:6144\n\t" \
  "v_mul_f32 v47, v47, v51\n\t" \
  "v_mfma_f32_16x16x32_bf16 v[168:171], v[100:103], v[68:71], v[168:171]\n\t" \
  "ds_read_b128 v[112:115], v57 offset:8192\n\t" \
  "v_cvt_pk_bf16_f32 v43, v43, v47\n\t" \
  "v_mfma_f32_16x16x32_bf16 v[168:171], v[120:123], v[76:79], v[168:171]\n\t" \
  "ds_read_b128 v[116:119], v56 offset:8192\n\t" \
  "v_lshlrev_b32 v50, 16, v39\n\t" \
  "v_mfma_f32_16x16x32_bf16 v[176:179], v[120:123], v[80:83], v[176:179]\n\t" \
  "ds_read_b64_tr_b16 v[120:121], v59 offset:34816\n\t" \
  "v_lshlrev_b32 v51, 16, v41\n\t" \
  "v_mfma_f32_16x16x32_bf16 v[176:179], v[100:103], v[84:87], v[176:179]\n\t" \
  "ds_read_b64_tr_b16 v[122:123], v59 offset:35328\n\t" \
  "v_mul_f32 v44, v44, v50\n\t" \
  "v_mfma_f32_16x16x32_bf16 v[156:159], v[92:95], v[136:139], v[156:159]\n\t" \
  "ds_read_b64_tr_b16 v[100:101], v59 offset:38912\n\t" \
  "v_mul_f32 v48, v48, v51\n\t" \
  "v_mfma_f32_16x16x32_bf16 v[156:159], v[124:127], v[144:147], v[156:159]\n\t" \
  "ds_read_b64_tr_b16 v[102:103], v59 offset:39424\n\t" \
  "v_cvt_pk_bf16_f32 v44, v44, v48\n\t" \
  "v_mfma_f32_16x16x32_bf16 v[164:167], v[124:127], v[72:75], v[164:167]\n\t" \
  "ds_read_b64_tr_b16 v[136:137], v58 offset:26624\n\t" \
  "v_and_b32 v50, 0xffff0000, v39\n\t" \
  "v_mfma_f32_16x16x32_bf16 v[164:167], v[92:95], v[140:143], v[164:167]\n\t" \
  "ds_read_b64_tr_b16 v[138:139], v58 offset:27136\n\t" \
  "v_and_b32 v51, 0xffff0000, v41\n\t" \
  "v_mfma_f32_16x16x32_bf16 v[172:175], v[92:95], v[68:71], v[172:175]\n\t" \
  "ds_read_b64_tr_b16 v[144:145], v58 offset:30720\n\t" \
  "v_mul_f32 v45, v45, v50\n\t" \
  "v_mfma_f32_16x16x32_bf16 v[172:175], v[124:127], v[76:79], v[172:175]\n\t" \
  "ds_read_b64_tr_b16 v[146:147], v58 offset:31232\n\t" \
  "v_mul_f32 v49, v49, v51\n\t" \
  "v_mfma_f32_16x16x32_bf16 v[180:183], v[124:127], v[80:83], v[180:183]\n\t" \
  "ds_read_b128 v[72:75], v56 offset:0\n\t" \
  "ds_read_b64_tr_b16 v[38:39], v1 offset:800\n\t" \
  "v_mfma_f32_16x16x32_bf16 v[180:183], v[92:95], v[84:87], v[180:183]\n\t" \
  "ds_read_b128 v[140:143], v57 offset:0\n\t" \
  "ds_read_b64_tr_b16 v[40:41], v1 offset:288\n\t" \
  "s_waitcnt lgkmcnt(9)\n\t" \
  "v_mfma_f32_16x16x32_bf16 v[216:219], v[96:99], v[128:131], v[216:219]\n\t" \
  "ds_read_b64_tr_b16 v[68:69], v59 offset:26624\n\t" \
  "v_cvt_pk_bf16_f32 v45, v45, v49\n\t" \
  "v_mfma_f32_16x16x32_bf16 v[216:219], v[132:135], v[88:91], v[216:219]\n\t" \
  "ds_read_b64_tr_b16 v[70:71], v59 offset:27136\n\t" \
  "v_mfma_f32_16x16x32_bf16 v[224:227], v[108:111], v[88:91], v[224:227]\n\t" \
  "ds_read_b64_tr_b16 v[76:77], v59 offset:30720\n\t" \
  "v_mfma_f32_16x16x32_bf16 v[224:227], v[148:151], v[128:131], v[224:227]\n\t" \
  "ds_read_b64_tr_b16 v[78:79], v59 offset:31232\n\t" \
  "v_mfma_f32_16x16x32_bf16 v[232:235], v[64:67], v[128:131], v[232:235]\n\t" \
  "ds_read_b64_tr_b16 v[80:81], v58 offset:18432\n\t" \
  "v_mfma_f32_16x16x32_bf16 v[232:235], v[104:107], v[88:91], v[232:235]\n\t" \
  "ds_read_b64_tr_b16 v[82:83], v58 offset:18944\n\t" \
  "v_mfma_f32_16x16x32_bf16 v[240:243], v[112:115], v[88:91], v[240:243]\n\t" \
  "ds_read_b64_tr_b16 v[124:125], v58 offset:22528\n\t" \
  "v_mfma_f32_16x16x32_bf16 v[240:243], v[116:119], v[128:131], v[240:243]\n\t" \
  "ds_read_b64_tr_b16 v[126:127], v58 offset:23040\n\t" \
  "v_mfma_f32_16x16x32_bf16 v[220:223], v[96:99], v[120:123], v[220:223]\n\t" \
  "ds_read_b64_tr_b16 v[84:85], v59 offset:18432\n\t" \
  "s_waitcnt lgkmcnt(7)\n\t" \
  "v_mfma_f32_16x16x32_bf16 v[220:223], v[132:135], v[100:103], v[220:223]\n\t" \
  "ds_read_b64_tr_b16 v[86:87], v59 offset:18944\n\t" \
  "v_mfma_f32_16x16x32_bf16 v[228:231], v[108:111], v[100:103], v[228:231]\n\t" \
  "ds_read_b64_tr_b16 v[92:93], v59 offset:22528\n\t" \
  "v_mfma_f32_16x16x32_bf16 v[228:231], v[148:151], v[120:123], v[228:231]\n\t" \
  "ds_read_b64_tr_b16 v[94:95], v59 offset:23040\n\t" \
  "v_mfma_f32_16x16x32_bf16 v[236:239], v[64:67], v[120:123], v[236:239]\n\t" \
  "v_mfma_f32_16x16x32_bf16 v[236:239], v[104:107], v[100:103], v[236:239]\n\t" \
  "v_mfma_f32_16x16x32_bf16 v[244:247], v[112:115], v[100:103], v[244:247]\n\t" \
  "v_mfma_f32_16x16x32_bf16 v[244:247], v[116:119], v[120:123], v[244:247]\n\t" \
  "v_mfma_f32_16x16x32_bf16 v[184:187], v[136:139], v[96:99], v[184:187]\n\t" \
  "v_mfma_f32_16x16x32_bf16 v[184:187], v[144:147], v[132:135], v[184:187]\n\t" \
  "v_mfma_f32_16x16x32_bf16 v[192:195], v[144:147], v[108:111], v[192:195]\n\t" \
  "v_mfma_f32_16x16x32_bf16 v[192:195], v[136:139], v[148:151], v[192:195]\n\t" \
  "v_mfma_f32_16x16x32_bf16 v[200:203], v[136:139], v[64:67], v[200:203]\n\t" \
  "v_mfma_f32_16x16x32_bf16 v[200:203], v[144:147], v[104:107], v[200:203]\n\t" \
  "v_mfma_f32_16x16x32_bf16 v[208:211], v[144:147], v[112:115], v[208:211]\n\t" \
  "v_mfma_f32_16x16x32_bf16 v[208:211], v[136:139], v[116:119], v[208:211]\n\t" \
  "v_mfma_f32_16x16x32_bf16 v[248:251], v[136:139], v[72:75], v[248:251]\n\t" \
  "v_mfma_f32_16x16x32_bf16 v[248:251], v[144:147], v[140:143], v[248:251]\n\t" \
  "v_mfma_f32_16x16x32_bf16 v[252:255], v[68:71], v[72:75], v[252:255]\n\t" \
  "global_store_dwordx4 v2, v[42:45], %24 offset:0 sc1\n\t" \
  "s_waitcnt lgkmcnt(0)\n\t" \
  "v_mfma_f32_16x16x32_bf16 v[252:255], v[76:79], v[140:143], v[252:255]\n\t" \
  "v_mfma_f32_16x16x32_bf16 v[42:45], v[26:29], v[18:21], 0\n\t" \
  "v_mfma_f32_16x16x32_bf16 v[42:45], v[30:33], v[22:25], v[42:45]\n\t" \
  "v_mfma_f32_16x16x32_bf16 v[46:49], v[30:33], v[18:21], 0\n\t" \
  "v_mfma_f32_16x16x32_bf16 v[46:49], v[34:37], v[22:25], v[46:49]\n\t" \
  "v_mfma_f32_16x16x32_bf16 v[188:191], v[76:79], v[132:135], v[188:191]\n\t" \
  "v_mfma_f32_16x16x32_bf16 v[188:191], v[68:71], v[96:99], v[188:191]\n\t" \
  "v_lshlrev_b32 v50, 16, v38\n\t" \
  "v_lshlrev_b32 v51, 16, v40\n\t" \
  "s_barrier\n\t" \
  "v_mfma_f32_16x16x32_bf16 v[196:199], v[68:71], v[148:151], v[196:199]\n\t" \
  "ds_read_b64_tr_b16 v[88:89], v62 offset:34816\n\t" \
  "v_mul_f32 v42, v42, v50\n\t" \
  "v_mfma_f32_16x16x32_bf16 v[196:199], v[76:79], v[108:111], v[196:199]\n\t" \
  "ds_read_b64_tr_b16 v[90:91], v62 offset:35328\n\t" \
  "v_mul_f32 v46, v46, v51\n\t" \
  "v_mfma_f32_16x16x32_bf16 v[204:207], v[76:79], v[104:107], v[204:207]\n\t" \
  "ds_read_b128 v[128:131], v60 offset:2048\n\t" \
  "v_cvt_pk_bf16_f32 v42, v42, v46\n\t" \
  "v_mfma_f32_16x16x32_bf16 v[204:207], v[68:71], v[64:67], v[204:207]\n\t" \
  "ds_read_b64_tr_b16 v[100:101], v62 offset:38912\n\t" \
  "v_and_b32 v50, 0xffff0000, v38\n\t" \
  "v_mfma_f32_16x16x32_bf16 v[212:215], v[68:71], v[116:119], v[212:215]\n\t" \
  "ds_read_b64_tr_b16 v[102:103], v62 offset:39424\n\t" \
  "v_and_b32 v51, 0xffff0000, v40\n\t" \
  "v_mfma_f32_16x16x32_bf16 v[212:215], v[76:79], v[112:115], v[212:215]\n\t" \
  "ds_read_b128 v[120:123], v61 offset:2048\n\t" \
  "v_mul_f32 v43, v43, v50\n\t" \
  "v_mfma_f32_16x16x32_bf16 v[152:155], v[80:83], v[96:99], v[152:155]\n\t" \
  "ds_read_b128 v[136:139], v61 offset:4096\n\t" \
  "v_mul_f32 v47, v47, v51\n\t" \
  "v_mfma_f32_16x16x32_bf16 v[152:155], v[124:127], v[132:135], v[152:155]\n\t" \
  "ds_read_b128 v[144:147], v60 offset:4096\n\t" \
  "v_cvt_pk_bf16_f32 v43, v43, v47\n\t" \
  "v_mfma_f32_16x16x32_bf16 v[160:163], v[124:127], v[108:111], v[160:163]\n\t" \
  "ds_read_b128 v[72:75], v60 offset:6144\n\t" \
  "v_lshlrev_b32 v50, 16, v39\n\t" \
  "v_mfma_f32_16x16x32_bf16 v[160:163], v[80:83], v[148:151], v[160:163]\n\t" \
  "ds_read_b128 v[140:143], v61 offset:6144\n\t" \
  "v_lshlrev_b32 v51, 16, v41\n\t" \
  "v_mfma_f32_16x16x32_bf16 v[168:171], v[80:83], v[64:67], v[168:171]\n\t" \
  "ds_read_b128 v[68:71], v61 offset:8192\n\t" \
  "v_mul_f32 v44, v44, v50\n\t" \
  "v_mfma_f32_16x16x32_bf16 v[168:171], v[124:127], v[104:107], v[168:171]\n\t" \
  "ds_read_b128 v[76:79], v60 offset:8192\n\t" \
  "v_mul_f32 v48, v48, v51\n\t" \
  "v_mfma_f32_16x16x32_bf16 v[176:179], v[124:127], v[112:115], v[176:179]\n\t" \
  "ds_read_b64_tr_b16 v[124:125], v63 offset:34816\n\t" \
  "v_cvt_pk_bf16_f32 v44, v44, v48\n\t" \
  "v_mfma_f32_16x16x32_bf16 v[176:179], v[80:83], v[116:119], v[176:179]\n\t" \
  "ds_read_b64_tr_b16 v[126:127], v63 offset:35328\n\t" \
  "v_and_b32 v50, 0xffff0000, v39\n\t" \
  "v_mfma_f32_16x16x32_bf16 v[156:159], v[84:87], v[96:99], v[156:159]\n\t" \
  "ds_read_b64_tr_b16 v[80:81], v63 offset:38912\n\t" \
  "v_and_b32 v51, 0xffff0000, v41\n\t" \
  "v_mfma_f32_16x16x32_bf16 v[156:159], v[92:95], v[132:135], v[156:159]\n\t" \
  "ds_read_b64_tr_b16 v[82:83], v63 offset:39424\n\t" \
  "v_mul_f32 v45, v45, v50\n\t" \
  "v_mfma_f32_16x16x32_bf16 v[164:167], v[92:95], v[108:111], v[164:167]\n\t" \
  "ds_read_b64_tr_b16 v[96:97], v62 offset:26624\n\t" \
  "v_mul_f32 v49, v49, v51\n\t" \
  "v_mfma_f32_16x16x32_bf16 v[164:167], v[84:87], v[148:151], v[164:167]\n\t" \
  "ds_read_b64_tr_b16 v[98:99], v62 offset:27136\n\t" \
  "v_cvt_pk_bf16_f32 v45, v45, v49\n\t" \
  "v_mfma_f32_16x16x32_bf16 v[172:175], v[84:87], v[64:67], v[172:175]\n\t" \
  "ds_read_b64_tr_b16 v[132:133], v62 offset:30720\n\t" \
  "v_mfma_f32_16x16x32_bf16 v[172:175], v[92:95], v[104:107], v[172:175]\n\t" \
  "ds_read_b64_tr_b16 v[134:135], v62 offset:31232\n\t" \
  "v_mfma_f32_16x16x32_bf16 v[180:183], v[92:95], v[112:115], v[180:183]\n\t" \
  "ds_read_b128 v[108:111], v60 offset:0\n\t" \
  "v_mfma_f32_16x16x32_bf16 v[180:183], v[84:87], v[116:119], v[180:183]\n\t" \
  "ds_read_b128 v[148:151], v61 offset:0\n\t" \
  "s_waitcnt lgkmcnt(7)\n\t" \
  "v_mfma_f32_16x16x32_bf16 v[216:219], v[128:131], v[88:91], v[216:219]\n\t" \
  "ds_read_b64_tr_b16 v[64:65], v63 offset:26624\n\t" \
  "v_mfma_f32_16x16x32_bf16 v[216:219], v[120:123], v[100:103], v[216:219]\n\t" \
  "ds_read_b64_tr_b16 v[66:67], v63 offset:27136\n\t" \
  "v_mfma_f32_16x16x32_bf16 v[224:227], v[136:139], v[100:103], v[224:227]\n\t" \
  "ds_read_b64_tr_b16 v[104:105], v63 offset:30720\n\t" \
  "v_mfma_f32_16x16x32_bf16 v[224:227], v[144:147], v[88:91], v[224:227]\n\t" \
  "ds_read_b64_tr_b16 v[106:107], v63 offset:31232\n\t" \
  "v_mfma_f32_16x16x32_bf16 v[232:235], v[72:75], v[88:91], v[232:235]\n\t" \
  "ds_read_b64_tr_b16 v[92:93], v62 offset:18432\n\t" \
  "v_mfma_f32_16x16x32_bf16 v[232:235], v[140:143], v[100:103], v[232:235]\n\t" \
  "ds_read_b64_tr_b16 v[94:95], v62 offset:18944\n\t" \
  "v_mfma_f32_16x16x32_bf16 v[240:243], v[68:71], v[100:103], v[240:243]\n\t" \
  "ds_read_b64_tr_b16 v[112:113], v62 offset:22528\n\t" \
  "v_mfma_f32_16x16x32_bf16 v[240:243], v[76:79], v[88:91], v[240:243]\n\t" \
  "ds_read_b64_tr_b16 v[114:115], v62 offset:23040\n\t" \
  "v_mfma_f32_16x16x32_bf16 v[220:223], v[128:131], v[124:127], v[220:223]\n\t" \
  "ds_read_b64_tr_b16 v[84:85], v63 offset:18432\n\t" \
  "s_waitcnt lgkmcnt(7)\n\t" \
  "v_mfma_f32_16x16x32_bf16 v[220:223], v[120:123], v[80:83], v[220:223]\n\t" \
  "ds_read_b64_tr_b16 v[86:87], v63 offset:18944\n\t" \
  "v_mfma_f32_16x16x32_bf16 v[228:231], v[136:139], v[80:83], v[228:231]\n\t" \
  "ds_read_b64_tr_b16 v[116:117], v63 offset:22528\n\t" \
  "v_mfma_f32_16x16x32_bf16 v[228:231], v[144:147], v[124:127], v[228:231]\n\t" \
  "ds_read_b64_tr_b16 v[118:119], v63 offset:23040\n\t" \
  "v_mfma_f32_16x16x32_bf16 v[236:239], v[72:75], v[124:127], v[236:239]\n\t" \
  "v_mfma_f32_16x16x32_bf16 v[236:239], v[140:143], v[80:83], v[236:239]\n\t" \
  "v_mfma_f32_16x16x32_bf16 v[244:247], v[68:71], v[80:83], v[244:247]\n\t" \
  "v_mfma_f32_16x16x32_bf16 v[244:247], v[76:79], v[124:127], v[244:247]\n\t" \
  "v_mfma_f32_16x16x32_bf16 v[184:187], v[96:99], v[128:131], v[184:187]\n\t" \
  "v_mfma_f32_16x16x32_bf16 v[184:187], v[132:135], v[120:123], v[184:187]\n\t" \
  "v_mfma_f32_16x16x32_bf16 v[192:195], v[132:135], v[136:139], v[192:195]\n\t" \
  "v_mfma_f32_16x16x32_bf16 v[192:195], v[96:99], v[144:147], v[192:195]\n\t" \
  "v_mfma_f32_16x16x32_bf16 v[200:203], v[96:99], v[72:75], v[200:203]\n\t" \
  "v_mfma_f32_16x16x32_bf16 v[200:203], v[132:135], v[140:143], v[200:203]\n\t" \
  "v_mfma_f32_16x16x32_bf16 v[208:211], v[132:135], v[68:71], v[208:211]\n\t" \
  "v_mfma_f32_16x16x32_bf16 v[208:211], v[96:99], v[76:79], v[208:211]\n\t" \
  "v_mfma_f32_16x16x32_bf16 v[248:251], v[96:99], v[108:111], v[248:251]\n\t" \
  "v_mfma_f32_16x16x32_bf16 v[248:251], v[132:135], v[148:151], v[248:251]\n\t" \
  "v_mfma_f32_16x16x32_bf16 v[252:255], v[64:67], v[108:111], v[252:255]\n\t" \
  "global_store_dwordx4 v2, v[42:45], %24 offset:1024 sc1\n\t" \
  "s_waitcnt lgkmcnt(0)\n\t" \
  "v_mfma_f32_16x16x32_bf16 v[252:255], v[104:107], v[148:151], v[252:255]\n\t" \
  "v_mfma_f32_16x16x32_bf16 v[188:191], v[104:107], v[120:123], v[188:191]\n\t" \
  "v_mfma_f32_16x16x32_bf16 v[188:191], v[64:67], v[128:131], v[188:191]\n\t" \
  "v_mov_b32 v30, 0xff61b1e6\n\t" \
  "v_bfe_i32 v34, v3, 0, 1\n\t" \
  "s_barrier\n\t" \
  "v_mfma_f32_16x16x32_bf16 v[196:199], v[64:67], v[144:147], v[196:199]\n\t" \
  "ds_read_b64_tr_b16 v[100:101], v54 offset:34816\n\t" \
  "v_and_b32 v34, v30, v34\n\t" \
  "v_mfma_f32_16x16x32_bf16 v[196:199], v[104:107], v[136:139], v[196:199]\n\t" \
  "ds_read_b64_tr_b16 v[102:103], v54 offset:35328\n\t" \
  "v_bfe_i32 v38, v3, 4, 1\n\t" \
  "v_mfma_f32_16x16x32_bf16 v[204:207], v[104:107], v[140:143], v[204:207]\n\t" \
  "ds_read_b128 v[88:91], v52 offset:2048\n\t" \
  "v_and_b32 v38, v30, v38\n\t" \
  "v_mfma_f32_16x16x32_bf16 v[204:207], v[64:67], v[72:75], v[204:207]\n\t" \
  "ds_read_b64_tr_b16 v[80:81], v54 offset:38912\n\t" \
  "v_bfe_i32 v42, v3, 8, 1\n\t" \
  "v_mfma_f32_16x16x32_bf16 v[212:215], v[64:67], v[76:79], v[212:215]\n\t" \
  "ds_read_b64_tr_b16 v[82:83], v54 offset:39424\n\t" \
  "v_bfi_b32 v42, v42, v30, %15\n\t" \
  "v_mfma_f32_16x16x32_bf16 v[212:215], v[104:107], v[68:71], v[212:215]\n\t" \
  "ds_read_b128 v[124:127], v53 offset:2048\n\t" \
  "v_mov_b32 v48, 0x3f803f80\n\t" \
  "v_mfma_f32_16x16x32_bf16 v[152:155], v[92:95], v[128:131], v[152:155]\n\t" \
  "ds_read_b128 v[96:99], v53 offset:4096\n\t" \
  "v_bfe_i32 v35, v3, 1, 1\n\t" \
  "v_mfma_f32_16x16x32_bf16 v[152:155], v[112:115], v[120:123], v[152:155]\n\t" \
  "ds_read_b128 v[132:135], v52 offset:4096\n\t" \
  "v_and_b32 v35, v30, v35\n\t" \
  "v_mfma_f32_16x16x32_bf16 v[160:163], v[112:115], v[136:139], v[160:163]\n\t" \
  "ds_read_b128 v[108:111], v52 offset:6144\n\t" \
  "v_bfe_i32 v39, v3, 5, 1\n\t" \
  "v_mfma_f32_16x16x32_bf16 v[160:163], v[92:95], v[144:147], v[160:163]\n\t" \
  "ds_read_b128 v[148:151], v53 offset:6144\n\t" \
  "v_and_b32 v39, v30, v39\n\t" \
  "v_mfma_f32_16x16x32_bf16 v[168:171], v[92:95], v[72:75], v[168:171]\n\t" \
  "ds_read_b128 v[64:67], v53 offset:8192\n\t" \
  "v_bfe_i32 v43, v3, 9, 1\n\t" \
  "504:\n\t" \
  "s_cbranch_execz 508f\n\t" \
  "v_mfma_f32_16x16x32_bf16 v[168:171], v[112:115], v[140:143], v[168:171]\n\t" \
  "ds_read_b128 v[104:107], v52 offset:8192\n\t" \
  "v_bfi_b32 v43, v43, v30, %15\n\t" \
  "v_mfma_f32_16x16x32_bf16 v[176:179], v[112:115], v[68:71], v[176:179]\n\t" \
  "ds_read_b64_tr_b16 v[112:113], v55 offset:34816\n\t" \
  "v_mov_b32 v49, 0x3f803f80\n\t" \
  "v_mfma_f32_16x16x32_bf16 v[176:179], v[92:95], v[76:79], v[176:179]\n\t" \
  "ds_read_b64_tr_b16 v[114:115], v55 offset:35328\n\t" \
  "v_bfe_i32 v36, v3, 2, 1\n\t" \
  "v_mfma_f32_16x16x32_bf16 v[156:159], v[84:87], v[128:131], v[156:159]\n\t" \
  "ds_read_b64_tr_b16 v[92:93], v55 offset:38912\n\t" \
  "v_and_b32 v36, v30, v36\n\t" \
  "v_mfma_f32_16x16x32_bf16 v[156:159], v[116:119], v[120:123], v[156:159]\n\t" \
  "ds_read_b64_tr_b16 v[94:95], v55 offset:39424\n\t" \
  "v_bfe_i32 v40, v3, 6, 1\n\t" \
  "v_mfma_f32_16x16x32_bf16 v[164:167], v[116:119], v[136:139], v[164:167]\n\t" \
  "ds_read_b64_tr_b16 v[128:129], v54 offset:26624\n\t" \
  "v_and_b32 v40, v30, v40\n\t" \
  "v_mfma_f32_16x16x32_bf16 v[164:167], v[84:87], v[144:147], v[164:167]\n\t" \
  "ds_read_b64_tr_b16 v[130:131], v54 offset:27136\n\t" \
  "v_bfe_i32 v44, v3, 10, 1\n\t" \
  "v_mfma_f32_16x16x32_bf16 v[172:175], v[84:87], v[72:75], v[172:175]\n\t" \
  "ds_read_b64_tr_b16 v[120:121], v54 offset:30720\n\t" \
  "v_bfi_b32 v44, v44, v30, %15\n\t" \
  "v_mfma_f32_16x16x32_bf16 v[172:175], v[116:119], v[140:143], v[172:175]\n\t" \
  "ds_read_b64_tr_b16 v[122:123], v54 offset:31232\n\t" \
  "v_mov_b32 v50, 0x3f803f80\n\t" \
  "v_mfma_f32_16x16x32_bf16 v[180:183], v[116:119], v[68:71], v[180:183]\n\t" \
  "ds_read_b128 v[136:139], v52 offset:0\n\t" \
  "v_bfe_i32 v37, v3, 3, 1\n\t" \
  "v_mfma_f32_16x16x32_bf16 v[180:183], v[84:87], v[76:79], v[180:183]\n\t" \
  "ds_read_b128 v[144:147], v53 offset:0\n\t" \
  "v_and_b32 v37, v30, v37\n\t" \
  "s_waitcnt lgkmcnt(7)\n\t" \
  "v_mfma_f32_16x16x32_bf16 v[216:219], v[88:91], v[100:103], v[216:219]\n\t" \
  "ds_read_b64_tr_b16 v[72:73], v55 offset:26624\n\t" \
  "v_bfe_i32 v41, v3, 7, 1\n\t" \
  "v_mfma_f32_16x16x32_bf16 v[216:219], v[124:127], v[80:83], v[216:219]\n\t" \
  "ds_read_b64_tr_b16 v[74:75], v55 offset:27136\n\t" \
  "v_and_b32 v41, v30, v41\n\t" \
  "v_mfma_f32_16x16x32_bf16 v[224:227], v[96:99], v[80:83], v[224:227]\n\t" \
  "ds_read_b64_tr_b16 v[140:141], v55 offset:30720\n\t" \
  "v_bfe_i32 v45, v3, 11, 1\n\t" \
  "v_mfma_f32_16x16x32_bf16 v[224:227], v[132:135], v[100:103], v[224:227]\n\t" \
  "ds_read_b64_tr_b16 v[142:143], v55 offset:31232\n\t" \
  "v_bfi_b32 v45, v45, v30, %15\n\t" \
  "v_mfma_f32_16x16x32_bf16 v[232:235], v[108:111], v[100:103], v[232:235]\n\t" \
  "ds_read_b64_tr_b16 v[68:69], v54 offset:18432\n\t" \
  "v_mov_b32 v51, 0x3f803f80\n\t" \
  "v_mfma_f32_16x16x32_bf16 v[232:235], v[148:151], v[80:83], v[232:235]\n\t" \
  "ds_read_b64_tr_b16 v[70:71], v54 offset:18944\n\t" \
  "v_bfe_i32 v46, v3, 12, 1\n\t" \
  "v_mfma_f32_16x16x32_bf16 v[240:243], v[64:67], v[80:83], v[240:243]\n\t" \
  "ds_read_b64_tr_b16 v[116:117], v54 offset:22528\n\t" \
  "v_bfe_i32 v31, v3, 13, 1\n\t" \
  "v_mfma_f32_16x16x32_bf16 v[240:243], v[104:107], v[100:103], v[240:243]\n\t" \
  "ds_read_b64_tr_b16 v[118:119], v54 offset:23040\n\t" \
  "v_perm_b32 v46, v31, v46, vcc_hi\n\t" \
  "v_mfma_f32_16x16x32_bf16 v[220:223], v[88:91], v[112:115], v[220:223]\n\t" \
  "ds_read_b64_tr_b16 v[76:77], v55 offset:18432\n\t" \
  "v_bfe_i32 v47, v3, 14, 1\n\t" \
  "s_waitcnt lgkmcnt(7)\n\t" \
  "v_mfma_f32_16x16x32_bf16 v[220:223], v[124:127], v[92:95], v[220:223]\n\t" \
  "ds_read_b64_tr_b16 v[78:79], v55 offset:18944\n\t" \
  "v_bfe_i32 v31, v3, 15, 1\n\t" \
  "v_mfma_f32_16x16x32_bf16 v[228:231], v[96:99], v[92:95], v[228:231]\n\t" \
  "ds_read_b64_tr_b16 v[84:85], v55 offset:22528\n\t" \
  "v_perm_b32 v47, v31, v47, vcc_hi\n\t" \
  "v_mfma_f32_16x16x32_bf16 v[228:231], v[132:135], v[112:115], v[228:231]\n\t" \
  "ds_read_b64_tr_b16 v[86:87], v55 offset:23040\n\t" \
  "v_add_u32 v30, 18432, v4\n\t" \
  "v_mfma_f32_16x16x32_bf16 v[236:239], v[108:111], v[112:115], v[236:239]\n\t" \
  "v_xor_b32 v31, 1024, v30\n\t" \
  "v_mov_b32 v32, %12\n\t" \
  "v_mfma_f32_16x16x32_bf16 v[236:239], v[148:151], v[92:95], v[236:239]\n\t" \
  "v_xor_b32 v33, 4, v32\n\t" \
  "v_mov_b32 v0, 1\n\t" \
  "v_mfma_f32_16x16x32_bf16 v[244:247], v[64:67], v[92:95], v[244:247]\n\t" \
  "v_and_b32 v4, 1023, v4\n\t" \
  "v_add_u32 v1, 8192, v4\n\t" \
  "v_mfma_f32_16x16x32_bf16 v[244:247], v[104:107], v[112:115], v[244:247]\n\t" \
  "v_mfma_f32_16x16x32_bf16 v[184:187], v[128:131], v[88:91], v[184:187]\n\t" \
  "v_mfma_f32_16x16x32_bf16 v[184:187], v[120:123], v[124:127], v[184:187]\n\t" \
  "v_mfma_f32_16x16x32_bf16 v[192:195], v[120:123], v[96:99], v[192:195]\n\t" \
  "v_mfma_f32_16x16x32_bf16 v[192:195], v[128:131], v[132:135], v[192:195]\n\t" \
  "v_mfma_f32_16x16x32_bf16 v[200:203], v[128:131], v[108:111], v[200:203]\n\t" \
  "v_mfma_f32_16x16x32_bf16 v[200:203], v[120:123], v[148:151], v[200:203]\n\t" \
  "v_mfma_f32_16x16x32_bf16 v[208:211], v[120:123], v[64:67], v[208:211]\n\t" \
  "v_mfma_f32_16x16x32_bf16 v[208:211], v[128:131], v[104:107], v[208:211]\n\t" \
  "v_mfma_f32_16x16x32_bf16 v[248:251], v[128:131], v[136:139], v[248:251]\n\t" \
  "v_mfma_f32_16x16x32_bf16 v[248:251], v[120:123], v[144:147], v[248:251]\n\t" \
  "v_mfma_f32_16x16x32_bf16 v[252:255], v[72:75], v[136:139], v[252:255]\n\t" \
  "s_waitcnt lgkmcnt(0)\n\t" \
  "v_mfma_f32_16x16x32_bf16 v[252:255], v[140:143], v[144:147], v[252:255]\n\t" \
  "v_mfma_f32_16x16x32_bf16 v[188:191], v[140:143], v[124:127], v[188:191]\n\t" \
  "v_mfma_f32_16x16x32_bf16 v[188:191], v[72:75], v[88:91], v[188:191]\n\t" \
  "s_barrier\n\t" \
  "v_mfma_f32_16x16x32_bf16 v[196:199], v[72:75], v[132:135], v[196:199]\n\t" \
  "ds_read_b64_tr_b16 v[80:81], v58 offset:34816\n\t" \
  "v_mfma_f32_16x16x32_bf16 v[196:199], v[140:143], v[96:99], v[196:199]\n\t" \
  "ds_read_b64_tr_b16 v[82:83], v58 offset:35328\n\t" \
  "v_mfma_f32_16x16x32_bf16 v[204:207], v[140:143], v[148:151], v[204:207]\n\t" \
  "ds_read_b128 v[100:103], v56 offset:2048\n\t" \
  "v_mfma_f32_16x16x32_bf16 v[204:207], v[72:75], v[108:111], v[204:207]\n\t" \
  "ds_read_b64_tr_b16 v[92:93], v58 offset:38912\n\t" \
  "v_mfma_f32_16x16x32_bf16 v[212:215], v[72:75], v[104:107], v[212:215]\n\t" \
  "ds_read_b64_tr_b16 v[94:95], v58 offset:39424\n\t" \
  "v_mfma_f32_16x16x32_bf16 v[212:215], v[140:143], v[64:67], v[212:215]\n\t" \
  "ds_read_b128 v[112:115], v57 offset:2048\n\t" \
  "v_mfma_f32_16x16x32_bf16 v[152:155], v[68:71], v[88:91], v[152:155]\n\t" \
  "ds_read_b128 v[128:131], v57 offset:4096\n\t" \
  "v_mfma_f32_16x16x32_bf16 v[152:155], v[116:119], v[124:127], v[152:155]\n\t" \
  "ds_read_b128 v[120:123], v56 offset:4096\n\t" \
  "v_mfma_f32_16x16x32_bf16 v[160:163], v[116:119], v[96:99], v[160:163]\n\t" \
  "ds_read_b128 v[136:139], v56 offset:6144\n\t" \
  "v_mfma_f32_16x16x32_bf16 v[160:163], v[68:71], v[132:135], v[160:163]\n\t" \
  "ds_read_b128 v[144:147], v57 offset:6144\n\t" \
  "v_mfma_f32_16x16x32_bf16 v[168:171], v[68:71], v[108:111], v[168:171]\n\t" \
  "ds_read_b128 v[72:75], v57 offset:8192\n\t" \
  "v_mfma_f32_16x16x32_bf16 v[168:171], v[116:119], v[148:151], v[168:171]\n\t" \
  "ds_read_b128 v[140:143], v56 offset:8192\n\t" \
  "v_mfma_f32_16x16x32_bf16 v[176:179], v[116:119], v[64:67], v[176:179]\n\t" \
  "ds_read_b64_tr_b16 v[116:117], v59 offset:34816\n\t" \
  "v_mfma_f32_16x16x32_bf16 v[176:179], v[68:71], v[104:107], v[176:179]\n\t" \
  "ds_read_b64_tr_b16 v[118:119], v59 offset:35328\n\t" \
  "v_mfma_f32_16x16x32_bf16 v[156:159], v[76:79], v[88:91], v[156:159]\n\t" \
  "ds_read_b64_tr_b16 v[68:69], v59 offset:38912\n\t" \
  "v_mfma_f32_16x16x32_bf16 v[156:159], v[84:87], v[124:127], v[156:159]\n\t" \
  "ds_read_b64_tr_b16 v[70:71], v59 offset:39424\n\t" \
  "v_mfma_f32_16x16x32_bf16 v[164:167], v[84:87], v[96:99], v[164:167]\n\t" \
  "ds_read_b64_tr_b16 v[88:89], v58 offset:26624\n\t" \
  "v_mfma_f32_16x16x32_bf16 v[164:167], v[76:79], v[132:135], v[164:167]\n\t" \
  "ds_read_b64_tr_b16 v[90:91], v58 offset:27136\n\t" \
  "v_mfma_f32_16x16x32_bf16 v[172:175], v[76:79], v[108:111], v[172:175]\n\t" \
  "ds_read_b64_tr_b16 v[124:125], v58 offset:30720\n\t" \
  "v_mfma_f32_16x16x32_bf16 v[172:175], v[84:87], v[148:151], v[172:175]\n\t" \
  "ds_read_b64_tr_b16 v[126:127], v58 offset:31232\n\t" \
  "v_mfma_f32_16x16x32_bf16 v[180:183], v[84:87], v[64:67], v[180:183]\n\t" \
  "ds_read_b128 v[96:99], v56 offset:0\n\t" \
  "v_mfma_f32_16x16x32_bf16 v[180:183], v[76:79], v[104:107], v[180:183]\n\t" \
  "ds_read_b128 v[132:135], v57 offset:0\n\t" \
  "s_waitcnt lgkmcnt(7)\n\t" \
  "v_mfma_f32_16x16x32_bf16 v[216:219], v[100:103], v[80:83], v[216:219]\n\t" \
  "ds_read_b64_tr_b16 v[108:109], v59 offset:26624\n\t" \
  "v_mfma_f32_16x16x32_bf16 v[216:219], v[112:115], v[92:95], v[216:219]\n\t" \
  "ds_read_b64_tr_b16 v[110:111], v59 offset:27136\n\t" \
  "v_mfma_f32_16x16x32_bf16 v[224:227], v[128:131], v[92:95], v[224:227]\n\t" \
  "ds_read_b64_tr_b16 v[148:149], v59 offset:30720\n\t" \
  "v_mfma_f32_16x16x32_bf16 v[224:227], v[120:123], v[80:83], v[224:227]\n\t" \
  "ds_read_b64_tr_b16 v[150:151], v59 offset:31232\n\t" \
  "v_mfma_f32_16x16x32_bf16 v[232:235], v[136:139], v[80:83], v[232:235]\n\t" \
  "ds_read_b64_tr_b16 v[64:65], v58 offset:18432\n\t" \
  "v_mfma_f32_16x16x32_bf16 v[232:235], v[144:147], v[92:95], v[232:235]\n\t" \
  "ds_read_b64_tr_b16 v[66:67], v58 offset:18944\n\t" \
  "v_mfma_f32_16x16x32_bf16 v[240:243], v[72:75], v[92:95], v[240:243]\n\t" \
  "ds_read_b64_tr_b16 v[84:85], v58 offset:22528\n\t" \
  "v_mfma_f32_16x16x32_bf16 v[240:243], v[140:143], v[80:83], v[240:243]\n\t" \
  "ds_read_b64_tr_b16 v[86:87], v58 offset:23040\n\t" \
  "v_mfma_f32_16x16x32_bf16 v[220:223], v[100:103], v[116:119], v[220:223]\n\t" \
  "ds_read_b64_tr_b16 v[76:77], v59 offset:18432\n\t" \
  "s_waitcnt lgkmcnt(7)\n\t" \
  "v_mfma_f32_16x16x32_bf16 v[220:223], v[112:115], v[68:71], v[220:223]\n\t" \
  "ds_read_b64_tr_b16 v[78:79], v59 offset:18944\n\t" \
  "v_mfma_f32_16x16x32_bf16 v[228:231], v[128:131], v[68:71], v[228:231]\n\t" \
  "ds_read_b64_tr_b16 v[104:105], v59 offset:22528\n\t" \
  "v_mfma_f32_16x16x32_bf16 v[228:231], v[120:123], v[116:119], v[228:231]\n\t" \
  "ds_read_b64_tr_b16 v[106:107], v59 offset:23040\n\t" \
  "v_mfma_f32_16x16x32_bf16 v[236:239], v[136:139], v[116:119], v[236:239]\n\t" \
  "v_mfma_f32_16x16x32_bf16 v[236:239], v[144:147], v[68:71], v[236:239]\n\t" \
  "v_mfma_f32_16x16x32_bf16 v[244:247], v[72:75], v[68:71], v[244:247]\n\t" \
  "v_mfma_f32_16x16x32_bf16 v[244:247], v[140:143], v[116:119], v[244:247]\n\t" \
  "v_mfma_f32_16x16x32_bf16 v[184:187], v[88:91], v[100:103], v[184:187]\n\t" \
  "v_mfma_f32_16x16x32_bf16 v[184:187], v[124:127], v[112:115], v[184:187]\n\t" \
  "v_mfma_f32_16x16x32_bf16 v[192:195], v[124:127], v[128:131], v[192:195]\n\t" \
  "v_mfma_f32_16x16x32_bf16 v[192:195], v[88:91], v[120:123], v[192:195]\n\t" \
  "v_mfma_f32_16x16x32_bf16 v[200:203], v[88:91], v[136:139], v[200:203]\n\t" \
  "v_mfma_f32_16x16x32_bf16 v[200:203], v[124:127], v[144:147], v[200:203]\n\t" \
  "v_mfma_f32_16x16x32_bf16 v[208:211], v[124:127], v[72:75], v[208:211]\n\t" \
  "v_mfma_f32_16x16x32_bf16 v[208:211], v[88:91], v[140:143], v[208:211]\n\t" \
  "v_mfma_f32_16x16x32_bf16 v[248:251], v[88:91], v[96:99], v[248:251]\n\t" \
  "v_mfma_f32_16x16x32_bf16 v[248:251], v[124:127], v[132:135], v[248:251]\n\t" \
  "v_mfma_f32_16x16x32_bf16 v[252:255], v[108:111], v[96:99], v[252:255]\n\t" \
  "s_waitcnt lgkmcnt(0)\n\t" \
  "v_mfma_f32_16x16x32_bf16 v[252:255], v[148:151], v[132:135], v[252:255]\n\t" \
  "v_mfma_f32_16x16x32_bf16 v[188:191], v[148:151], v[112:115], v[188:191]\n\t" \
  "v_mfma_f32_16x16x32_bf16 v[188:191], v[108:111], v[100:103], v[188:191]\n\t" \
  "s_waitcnt vmcnt(0)\n\t" \
  "s_barrier\n\t" \
  "v_mfma_f32_16x16x32_bf16 v[196:199], v[108:111], v[120:123], v[196:199]\n\t" \
  "ds_read_b64_tr_b16 v[92:93], v62 offset:34816\n\t" \
  "v_mfma_f32_16x16x32_bf16 v[196:199], v[148:151], v[128:131], v[196:199]\n\t" \
  "ds_read_b64_tr_b16 v[94:95], v62 offset:35328\n\t" \
  "v_mfma_f32_16x16x32_bf16 v[204:207], v[148:151], v[144:147], v[204:207]\n\t" \
  "ds_read_b128 v[80:83], v60 offset:2048\n\t" \
  "v_mfma_f32_16x16x32_bf16 v[204:207], v[108:111], v[136:139], v[204:207]\n\t" \
  "ds_read_b64_tr_b16 v[68:69], v62 offset:38912\n\t" \
  "v_mfma_f32_16x16x32_bf16 v[212:215], v[108:111], v[140:143], v[212:215]\n\t" \
  "ds_read_b64_tr_b16 v[70:71], v62 offset:39424\n\t" \
  "v_mfma_f32_16x16x32_bf16 v[212:215], v[148:151], v[72:75], v[212:215]\n\t" \
  "ds_read_b128 v[116:119], v61 offset:2048\n\t" \
  "v_mfma_f32_16x16x32_bf16 v[152:155], v[64:67], v[100:103], v[152:155]\n\t" \
  "ds_read_b128 v[88:91], v61 offset:4096\n\t" \
  "v_mfma_f32_16x16x32_bf16 v[152:155], v[84:87], v[112:115], v[152:155]\n\t" \
  "ds_read_b128 v[124:127], v60 offset:4096\n\t" \
  "v_mfma_f32_16x16x32_bf16 v[160:163], v[84:87], v[128:131], v[160:163]\n\t" \
  "ds_read_b128 v[96:99], v60 offset:6144\n\t" \
  "v_mfma_f32_16x16x32_bf16 v[160:163], v[64:67], v[120:123], v[160:163]\n\t" \
  "ds_read_b128 v[132:135], v61 offset:6144\n\t" \
  "v_mfma_f32_16x16x32_bf16 v[168:171], v[64:67], v[136:139], v[168:171]\n\t" \
  "ds_read_b128 v[108:111], v61 offset:8192\n\t" \
  "v_mfma_f32_16x16x32_bf16 v[168:171], v[84:87], v[144:147], v[168:171]\n\t" \
  "ds_read_b128 v[148:151], v60 offset:8192\n\t" \
  "v_mfma_f32_16x16x32_bf16 v[176:179], v[84:87], v[72:75], v[176:179]\n\t" \
  "ds_read_b64_tr_b16 v[84:85], v63 offset:34816\n\t" \
  "v_mfma_f32_16x16x32_bf16 v[176:179], v[64:67], v[140:143], v[176:179]\n\t" \
  "ds_read_b64_tr_b16 v[86:87], v63 offset:35328\n\t" \
  "v_mfma_f32_16x16x32_bf16 v[156:159], v[76:79], v[100:103], v[156:159]\n\t" \
  "ds_read_b64_tr_b16 v[64:65], v63 offset:38912\n\t" \
  "v_mfma_f32_16x16x32_bf16 v[156:159], v[104:107], v[112:115], v[156:159]\n\t" \
  "ds_read_b64_tr_b16 v[66:67], v63 offset:39424\n\t" \
  "v_mfma_f32_16x16x32_bf16 v[164:167], v[104:107], v[128:131], v[164:167]\n\t" \
  "ds_read_b64_tr_b16 v[100:101], v62 offset:26624\n\t" \
  "v_mfma_f32_16x16x32_bf16 v[164:167], v[76:79], v[120:123], v[164:167]\n\t" \
  "ds_read_b64_tr_b16 v[102:103], v62 offset:27136\n\t" \
  "v_mfma_f32_16x16x32_bf16 v[172:175], v[76:79], v[136:139], v[172:175]\n\t" \
  "ds_read_b64_tr_b16 v[112:113], v62 offset:30720\n\t" \
  "v_mfma_f32_16x16x32_bf16 v[172:175], v[104:107], v[144:147], v[172:175]\n\t" \
  "ds_read_b64_tr_b16 v[114:115], v62 offset:31232\n\t" \
  "v_mfma_f32_16x16x32_bf16 v[180:183], v[104:107], v[72:75], v[180:183]\n\t" \
  "ds_read_b128 v[128:131], v60 offset:0\n\t" \
  "v_mfma_f32_16x16x32_bf16 v[180:183], v[76:79], v[140:143], v[180:183]\n\t" \
  "ds_read_b128 v[120:123], v61 offset:0\n\t" \
  "s_waitcnt lgkmcnt(7)\n\t" \
  "v_mfma_f32_16x16x32_bf16 v[216:219], v[80:83], v[92:95], v[216:219]\n\t" \
  "ds_read_b64_tr_b16 v[136:137], v63 offset:26624\n\t" \
  "v_mfma_f32_16x16x32_bf16 v[216:219], v[116:119], v[68:71], v[216:219]\n\t" \
  "ds_read_b64_tr_b16 v[138:139], v63 offset:27136\n\t" \
  "v_mfma_f32_16x16x32_bf16 v[224:227], v[88:91], v[68:71], v[224:227]\n\t" \
  "ds_read_b64_tr_b16 v[144:145], v63 offset:30720\n\t" \
  "v_mfma_f32_16x16x32_bf16 v[224:227], v[124:127], v[92:95], v[224:227]\n\t" \
  "ds_read_b64_tr_b16 v[146:147], v63 offset:31232\n\t" \
  "v_mfma_f32_16x16x32_bf16 v[232:235], v[96:99], v[92:95], v[232:235]\n\t" \
  "ds_read_b64_tr_b16 v[72:73], v62 offset:18432\n\t" \
  "v_mfma_f32_16x16x32_bf16 v[232:235], v[132:135], v[68:71], v[232:235]\n\t" \
  "ds_read_b64_tr_b16 v[74:75], v62 offset:18944\n\t" \
  "v_mfma_f32_16x16x32_bf16 v[240:243], v[108:111], v[68:71], v[240:243]\n\t" \
  "ds_read_b64_tr_b16 v[104:105], v62 offset:22528\n\t" \
  "v_mfma_f32_16x16x32_bf16 v[240:243], v[148:151], v[92:95], v[240:243]\n\t" \
  "ds_read_b64_tr_b16 v[106:107], v62 offset:23040\n\t" \
  "v_mfma_f32_16x16x32_bf16 v[220:223], v[80:83], v[84:87], v[220:223]\n\t" \
  "ds_read_b64_tr_b16 v[76:77], v63 offset:18432\n\t" \
  "s_waitcnt lgkmcnt(7)\n\t" \
  "v_mfma_f32_16x16x32_bf16 v[220:223], v[116:119], v[64:67], v[220:223]\n\t" \
  "ds_read_b64_tr_b16 v[78:79], v63 offset:18944\n\t" \
  "v_mfma_f32_16x16x32_bf16 v[228:231], v[88:91], v[64:67], v[228:231]\n\t" \
  "ds_read_b64_tr_b16 v[140:141], v63 offset:22528\n\t" \
  "v_mfma_f32_16x16x32_bf16 v[228:231], v[124:127], v[84:87], v[228:231]\n\t" \
  "ds_read_b64_tr_b16 v[142:143], v63 offset:23040\n\t" \
  "v_mfma_f32_16x16x32_bf16 v[236:239], v[96:99], v[84:87], v[236:239]\n\t" \
  "v_mfma_f32_16x16x32_bf16 v[236:239], v[132:135], v[64:67], v[236:239]\n\t" \
  "v_mfma_f32_16x16x32_bf16 v[244:247], v[108:111], v[64:67], v[244:247]\n\t" \
  "v_mfma_f32_16x16x32_bf16 v[244:247], v[148:151], v[84:87], v[244:247]\n\t" \
  "v_mfma_f32_16x16x32_bf16 v[184:187], v[100:103], v[80:83], v[184:187]\n\t" \
  "ds_read_b32 v10, v32 offset:104\n\t" \
  "v_mfma_f32_16x16x32_bf16 v[184:187], v[112:115], v[116:119], v[184:187]\n\t" \
  "v_lshlrev_b32 v11, 3, v4\n\t" \
  "v_mfma_f32_16x16x32_bf16 v[192:195], v[112:115], v[88:91], v[192:195]\n\t" \
  "v_mfma_f32_16x16x32_bf16 v[192:195], v[100:103], v[124:127], v[192:195]\n\t" \
  "v_mfma_f32_16x16x32_bf16 v[200:203], v[100:103], v[96:99], v[200:203]\n\t" \
  "v_mfma_f32_16x16x32_bf16 v[200:203], v[112:115], v[132:135], v[200:203]\n\t" \
  "v_mfma_f32_16x16x32_bf16 v[208:211], v[112:115], v[108:111], v[208:211]\n\t" \
  "v_mfma_f32_16x16x32_bf16 v[208:211], v[100:103], v[148:151], v[208:211]\n\t" \
  "v_mfma_f32_16x16x32_bf16 v[248:251], v[100:103], v[128:131], v[248:251]\n\t" \
  "v_mfma_f32_16x16x32_bf16 v[248:251], v[112:115], v[120:123], v[248:251]\n\t" \
  "v_mfma_f32_16x16x32_bf16 v[252:255], v[136:139], v[128:131], v[252:255]\n\t" \
  "s_waitcnt lgkmcnt(0)\n\t" \
  "s_cmp_lg_u32 %13, 0\n\t" \
  "s_cbranch_scc1 27f\n\t" \
  "s_mov_b32 exec_hi, 0\n\t" \
  "global_store_dword v11, v10, %25 sc1\n\t" \
  "s_mov_b32 exec_hi, -1\n\t" \
  "27:\n\t" \
  "v_mfma_f32_16x16x32_bf16 v[252:255], v[144:147], v[120:123], v[252:255]\n\t" \
  "v_mfma_f32_16x16x32_bf16 v[188:191], v[144:147], v[116:119], v[188:191]\n\t" \
  "v_mfma_f32_16x16x32_bf16 v[188:191], v[136:139], v[80:83], v[188:191]\n\t" \
  "s_barrier\n\t" \
  "v_mfma_f32_16x16x32_bf16 v[196:199], v[136:139], v[124:127], v[196:199]\n\t" \
  "ds_read_b64_tr_b16 v[68:69], v54 offset:34816\n\t" \
  "v_mfma_f32_16x16x32_bf16 v[196:199], v[144:147], v[88:91], v[196:199]\n\t" \
  "ds_read_b64_tr_b16 v[70:71], v54 offset:35328\n\t" \
  "v_mfma_f32_16x16x32_bf16 v[204:207], v[144:147], v[132:135], v[204:207]\n\t" \
  "ds_read_b128 v[92:95], v52 offset:2048\n\t" \
  "v_mfma_f32_16x16x32_bf16 v[204:207], v[136:139], v[96:99], v[204:207]\n\t" \
  "ds_read_b64_tr_b16 v[64:65], v54 offset:38912\n\t" \
  "v_mfma_f32_16x16x32_bf16 v[212:215], v[136:139], v[148:151], v[212:215]\n\t" \
  "ds_read_b64_tr_b16 v[66:67], v54 offset:39424\n\t" \
  "v_mfma_f32_16x16x32_bf16 v[212:215], v[144:147], v[108:111], v[212:215]\n\t" \
  "ds_read_b128 v[84:87], v53 offset:2048\n\t" \
  "v_mfma_f32_16x16x32_bf16 v[152:155], v[72:75], v[80:83], v[152:155]\n\t" \
  "ds_read_b128 v[100:103], v53 offset:4096\n\t" \
  "v_mfma_f32_16x16x32_bf16 v[152:155], v[104:107], v[116:119], v[152:155]\n\t" \
  "ds_read_b128 v[112:115], v52 offset:4096\n\t" \
  "v_mfma_f32_16x16x32_bf16 v[160:163], v[104:107], v[88:91], v[160:163]\n\t" \
  "ds_read_b128 v[128:131], v52 offset:6144\n\t" \
  "v_mfma_f32_16x16x32_bf16 v[160:163], v[72:75], v[124:127], v[160:163]\n\t" \
  "ds_read_b128 v[120:123], v53 offset:6144\n\t" \
  "v_mfma_f32_16x16x32_bf16 v[168:171], v[72:75], v[96:99], v[168:171]\n\t" \
  "ds_read_b128 v[136:139], v53 offset:8192\n\t" \
  "v_mfma_f32_16x16x32_bf16 v[168:171], v[104:107], v[132:135], v[168:171]\n\t" \
  "ds_read_b128 v[144:147], v52 offset:8192\n\t" \
  "v_mfma_f32_16x16x32_bf16 v[176:179], v[104:107], v[108:111], v[176:179]\n\t" \
  "ds_read_b64_tr_b16 v[104:105], v55 offset:34816\n\t" \
  "v_mfma_f32_16x16x32_bf16 v[176:179], v[72:75], v[148:151], v[176:179]\n\t" \
  "ds_read_b64_tr_b16 v[106:107], v55 offset:35328\n\t" \
  "v_mfma_f32_16x16x32_bf16 v[156:159], v[76:79], v[80:83], v[156:159]\n\t" \
  "ds_read_b64_tr_b16 v[72:73], v55 offset:38912\n\t" \
  "v_mfma_f32_16x16x32_bf16 v[156:159], v[140:143], v[116:119], v[156:159]\n\t" \
  "ds_read_b64_tr_b16 v[74:75], v55 offset:39424\n\t" \
  "v_mfma_f32_16x16x32_bf16 v[164:167], v[140:143], v[88:91], v[164:167]\n\t" \
  "ds_read_b64_tr_b16 v[80:81], v54 offset:26624\n\t" \
  "v_mfma_f32_16x16x32_bf16 v[164:167], v[76:79], v[124:127], v[164:167]\n\t" \
  "ds_read_b64_tr_b16 v[82:83], v54 offset:27136\n\t" \
  "v_mfma_f32_16x16x32_bf16 v[172:175], v[76:79], v[96:99], v[172:175]\n\t" \
  "ds_read_b64_tr_b16 v[116:117], v54 offset:30720\n\t" \
  "v_mfma_f32_16x16x32_bf16 v[172:175], v[140:143], v[132:135], v[172:175]\n\t" \
  "ds_read_b64_tr_b16 v[118:119], v54 offset:31232\n\t" \
  "v_mfma_f32_16x16x32_bf16 v[180:183], v[140:143], v[108:111], v[180:183]\n\t" \
  "ds_read_b128 v[88:91], v52 offset:0\n\t" \
  "v_mfma_f32_16x16x32_bf16 v[180:183], v[76:79], v[148:151], v[180:183]\n\t" \
  "ds_read_b128 v[124:127], v53 offset:0\n\t" \
  "s_waitcnt lgkmcnt(7)\n\t" \
  "v_mfma_f32_16x16x32_bf16 v[216:219], v[92:95], v[68:71], v[216:219]\n\t" \
  "ds_read_b64_tr_b16 v[96:97], v55 offset:26624\n\t" \
  "v_mfma_f32_16x16x32_bf16 v[216:219], v[84:87], v[64:67], v[216:219]\n\t" \
  "ds_read_b64_tr_b16 v[98:99], v55 offset:27136\n\t" \
  "v_mfma_f32_16x16x32_bf16 v[224:227], v[100:103], v[64:67], v[224:227]\n\t" \
  "ds_read_b64_tr_b16 v[132:133], v55 offset:30720\n\t" \
  "v_mfma_f32_16x16x32_bf16 v[224:227], v[112:115], v[68:71], v[224:227]\n\t" \
  "ds_read_b64_tr_b16 v[134:135], v55 offset:31232\n\t" \
  "v_mfma_f32_16x16x32_bf16 v[232:235], v[128:131], v[68:71], v[232:235]\n\t" \
  "ds_read_b64_tr_b16 v[108:109], v54 offset:18432\n\t" \
  "v_mfma_f32_16x16x32_bf16 v[232:235], v[120:123], v[64:67], v[232:235]\n\t" \
  "ds_read_b64_tr_b16 v[110:111], v54 offset:18944\n\t" \
  "v_mfma_f32_16x16x32_bf16 v[240:243], v[136:139], v[64:67], v[240:243]\n\t" \
  "ds_read_b64_tr_b16 v[140:141], v54 offset:22528\n\t" \
  "v_mfma_f32_16x16x32_bf16 v[240:243], v[144:147], v[68:71], v[240:243]\n\t" \
  "ds_read_b64_tr_b16 v[142:143], v54 offset:23040\n\t" \
  "v_mfma_f32_16x16x32_bf16 v[220:223], v[92:95], v[104:107], v[220:223]\n\t" \
  "ds_read_b64_tr_b16 v[76:77], v55 offset:18432\n\t" \
  "s_waitcnt lgkmcnt(7)\n\t" \
  "v_mfma_f32_16x16x32_bf16 v[220:223], v[84:87], v[72:75], v[220:223]\n\t" \
  "ds_read_b64_tr_b16 v[78:79], v55 offset:18944\n\t" \
  "v_mfma_f32_16x16x32_bf16 v[228:231], v[100:103], v[72:75], v[228:231]\n\t" \
  "ds_read_b64_tr_b16 v[148:149], v55 offset:22528\n\t" \
  "v_mfma_f32_16x16x32_bf16 v[228:231], v[112:115], v[104:107], v[228:231]\n\t" \
  "ds_read_b64_tr_b16 v[150:151], v55 offset:23040\n\t" \
  "v_mfma_f32_16x16x32_bf16 v[236:239], v[128:131], v[104:107], v[236:239]\n\t" \
  "v_mfma_f32_16x16x32_bf16 v[236:239], v[120:123], v[72:75], v[236:239]\n\t" \
  "v_mfma_f32_16x16x32_bf16 v[244:247], v[136:139], v[72:75], v[244:247]\n\t" \
  "v_mfma_f32_16x16x32_bf16 v[244:247], v[144:147], v[104:107], v[244:247]\n\t" \
  "v_mfma_f32_16x16x32_bf16 v[184:187], v[80:83], v[92:95], v[184:187]\n\t" \
  "v_mfma_f32_16x16x32_bf16 v[184:187], v[116:119], v[84:87], v[184:187]\n\t" \
  "v_mfma_f32_16x16x32_bf16 v[192:195], v[116:119], v[100:103], v[192:195]\n\t" \
  "v_mfma_f32_16x16x32_bf16 v[192:195], v[80:83], v[112:115], v[192:195]\n\t" \
  "v_mfma_f32_16x16x32_bf16 v[200:203], v[80:83], v[128:131], v[200:203]\n\t" \
  "v_mfma_f32_16x16x32_bf16 v[200:203], v[116:119], v[120:123], v[200:203]\n\t" \
  "v_mfma_f32_16x16x32_bf16 v[208:211], v[116:119], v[136:139], v[208:211]\n\t" \
  "v_mfma_f32_16x16x32_bf16 v[208:211], v[80:83], v[144:147], v[208:211]\n\t" \
  "v_mfma_f32_16x16x32_bf16 v[248:251], v[80:83], v[88:91], v[248:251]\n\t" \
  "v_mfma_f32_16x16x32_bf16 v[248:251], v[116:119], v[124:127], v[248:251]\n\t" \
  "v_mfma_f32_16x16x32_bf16 v[252:255], v[96:99], v[88:91], v[252:255]\n\t" \
  "s_waitcnt lgkmcnt(0)\n\t" \
  "v_mfma_f32_16x16x32_bf16 v[252:255], v[132:135], v[124:127], v[252:255]\n\t" \
  "v_mfma_f32_16x16x32_bf16 v[188:191], v[132:135], v[84:87], v[188:191]\n\t" \
  "v_mfma_f32_16x16x32_bf16 v[188:191], v[96:99], v[92:95], v[188:191]\n\t" \
  "s_barrier\n\t" \
  "v_mfma_f32_16x16x32_bf16 v[196:199], v[96:99], v[112:115], v[196:199]\n\t" \
  "ds_read_b64_tr_b16 v[64:65], v58 offset:34816\n\t" \
  "v_mfma_f32_16x16x32_bf16 v[196:199], v[132:135], v[100:103], v[196:199]\n\t" \
  "ds_read_b64_tr_b16 v[66:67], v58 offset:35328\n\t" \
  "v_mfma_f32_16x16x32_bf16 v[204:207], v[132:135], v[120:123], v[204:207]\n\t" \
  "ds_read_b128 v[68:71], v56 offset:2048\n\t" \
  "v_mfma_f32_16x16x32_bf16 v[204:207], v[96:99], v[128:131], v[204:207]\n\t" \
  "ds_read_b64_tr_b16 v[72:73], v58 offset:38912\n\t" \
  "v_mfma_f32_16x16x32_bf16 v[212:215], v[96:99], v[144:147], v[212:215]\n\t" \
  "ds_read_b64_tr_b16 v[74:75], v58 offset:39424\n\t" \
  "v_mfma_f32_16x16x32_bf16 v[212:215], v[132:135], v[136:139], v[212:215]\n\t" \
  "ds_read_b128 v[104:107], v57 offset:2048\n\t" \
  "v_mfma_f32_16x16x32_bf16 v[152:155], v[108:111], v[92:95], v[152:155]\n\t" \
  "ds_read_b128 v[80:83], v57 offset:4096\n\t" \
  "v_mfma_f32_16x16x32_bf16 v[152:155], v[140:143], v[84:87], v[152:155]\n\t" \
  "ds_read_b128 v[116:119], v56 offset:4096\n\t" \
  "v_mfma_f32_16x16x32_bf16 v[160:163], v[140:143], v[100:103], v[160:163]\n\t" \
  "ds_read_b128 v[88:91], v56 offset:6144\n\t" \
  "v_mfma_f32_16x16x32_bf16 v[160:163], v[108:111], v[112:115], v[160:163]\n\t" \
  "ds_read_b128 v[124:127], v57 offset:6144\n\t" \
  "v_mfma_f32_16x16x32_bf16 v[168:171], v[108:111], v[128:131], v[168:171]\n\t" \
  "ds_read_b128 v[96:99], v57 offset:8192\n\t" \
  "v_mfma_f32_16x16x32_bf16 v[168:171], v[140:143], v[120:123], v[168:171]\n\t" \
  "ds_read_b128 v[132:135], v56 offset:8192\n\t" \
  "v_mfma_f32_16x16x32_bf16 v[176:179], v[140:143], v[136:139], v[176:179]\n\t" \
  "ds_read_b64_tr_b16 v[140:141], v59 offset:34816\n\t" \
  "v_mfma_f32_16x16x32_bf16 v[176:179], v[108:111], v[144:147], v[176:179]\n\t" \
  "ds_read_b64_tr_b16 v[142:143], v59 offset:35328\n\t" \
  "v_mfma_f32_16x16x32_bf16 v[156:159], v[76:79], v[92:95], v[156:159]\n\t" \
  "ds_read_b64_tr_b16 v[108:109], v59 offset:38912\n\t" \
  "v_mfma_f32_16x16x32_bf16 v[156:159], v[148:151], v[84:87], v[156:159]\n\t" \
  "ds_read_b64_tr_b16 v[110:111], v59 offset:39424\n\t" \
  "v_mfma_f32_16x16x32_bf16 v[164:167], v[148:151], v[100:103], v[164:167]\n\t" \
  "ds_read_b64_tr_b16 v[92:93], v58 offset:26624\n\t" \
  "v_mfma_f32_16x16x32_bf16 v[164:167], v[76:79], v[112:115], v[164:167]\n\t" \
  "ds_read_b64_tr_b16 v[94:95], v58 offset:27136\n\t" \
  "v_mfma_f32_16x16x32_bf16 v[172:175], v[76:79], v[128:131], v[172:175]\n\t" \
  "ds_read_b64_tr_b16 v[84:85], v58 offset:30720\n\t" \
  "v_mfma_f32_16x16x32_bf16 v[172:175], v[148:151], v[120:123], v[172:175]\n\t" \
  "ds_read_b64_tr_b16 v[86:87], v58 offset:31232\n\t" \
  "v_mfma_f32_16x16x32_bf16 v[180:183], v[148:151], v[136:139], v[180:183]\n\t" \
  "ds_read_b128 v[100:103], v56 offset:0\n\t" \
  "v_mfma_f32_16x16x32_bf16 v[180:183], v[76:79], v[144:147], v[180:183]\n\t" \
  "ds_read_b128 v[112:115], v57 offset:0\n\t" \
  "s_waitcnt lgkmcnt(7)\n\t" \
  "v_mfma_f32_16x16x32_bf16 v[216:219], v[68:71], v[64:67], v[216:219]\n\t" \
  "ds_read_b64_tr_b16 v[128:129], v59 offset:26624\n\t" \
  "v_mfma_f32_16x16x32_bf16 v[216:219], v[104:107], v[72:75], v[216:219]\n\t" \
  "ds_read_b64_tr_b16 v[130:131], v59 offset:27136\n\t" \
  "v_mfma_f32_16x16x32_bf16 v[224:227], v[80:83], v[72:75], v[224:227]\n\t" \
  "ds_read_b64_tr_b16 v[120:121], v59 offset:30720\n\t" \
  "v_mfma_f32_16x16x32_bf16 v[224:227], v[116:119], v[64:67], v[224:227]\n\t" \
  "ds_read_b64_tr_b16 v[122:123], v59 offset:31232\n\t" \
  "v_mfma_f32_16x16x32_bf16 v[232:235], v[88:91], v[64:67], v[232:235]\n\t" \
  "ds_read_b64_tr_b16 v[136:137], v58 offset:18432\n\t" \
  "v_mfma_f32_16x16x32_bf16 v[232:235], v[124:127], v[72:75], v[232:235]\n\t" \
  "ds_read_b64_tr_b16 v[138:139], v58 offset:18944\n\t" \
  "v_mfma_f32_16x16x32_bf16 v[240:243], v[96:99], v[72:75], v[240:243]\n\t" \
  "ds_read_b64_tr_b16 v[148:149], v58 offset:22528\n\t" \
  "v_mfma_f32_16x16x32_bf16 v[240:243], v[132:135], v[64:67], v[240:243]\n\t" \
  "ds_read_b64_tr_b16 v[150:151], v58 offset:23040\n\t" \
  "v_mfma_f32_16x16x32_bf16 v[220:223], v[68:71], v[140:143], v[220:223]\n\t" \
  "ds_read_b64_tr_b16 v[76:77], v59 offset:18432\n\t" \
  "s_waitcnt lgkmcnt(7)\n\t" \
  "v_mfma_f32_16x16x32_bf16 v[220:223], v[104:107], v[108:111], v[220:223]\n\t" \
  "ds_read_b64_tr_b16 v[78:79], v59 offset:18944\n\t" \
  "v_mfma_f32_16x16x32_bf16 v[228:231], v[80:83], v[108:111], v[228:231]\n\t" \
  "ds_read_b64_tr_b16 v[144:145], v59 offset:22528\n\t" \
  "v_mfma_f32_16x16x32_bf16 v[228:231], v[116:119], v[140:143], v[228:231]\n\t" \
  "ds_read_b64_tr_b16 v[146:147], v59 offset:23040\n\t" \
  "v_mfma_f32_16x16x32_bf16 v[236:239], v[88:91], v[140:143], v[236:239]\n\t" \
  "v_mfma_f32_16x16x32_bf16 v[236:239], v[124:127], v[108:111], v[236:239]\n\t" \
  "v_mfma_f32_16x16x32_bf16 v[244:247], v[96:99], v[108:111], v[244:247]\n\t" \
  "v_mfma_f32_16x16x32_bf16 v[244:247], v[132:135], v[140:143], v[244:247]\n\t" \
  "v_mfma_f32_16x16x32_bf16 v[184:187], v[92:95], v[68:71], v[184:187]\n\t" \
  "v_mfma_f32_16x16x32_bf16 v[184:187], v[84:87], v[104:107], v[184:187]\n\t" \
  "v_mfma_f32_16x16x32_bf16 v[192:195], v[84:87], v[80:83], v[192:195]\n\t" \
  "v_mfma_f32_16x16x32_bf16 v[192:195], v[92:95], v[116:119], v[192:195]\n\t" \
  "v_mfma_f32_16x16x32_bf16 v[200:203], v[92:95], v[88:91], v[200:203]\n\t" \
  "v_mfma_f32_16x16x32_bf16 v[200:203], v[84:87], v[124:127], v[200:203]\n\t" \
  "v_mfma_f32_16x16x32_bf16 v[208:211], v[84:87], v[96:99], v[208:211]\n\t" \
  "v_mfma_f32_16x16x32_bf16 v[208:211], v[92:95], v[132:135], v[208:211]\n\t" \
  "v_mfma_f32_16x16x32_bf16 v[248:251], v[92:95], v[100:103], v[248:251]\n\t" \
  "v_mfma_f32_16x16x32_bf16 v[248:251], v[84:87], v[112:115], v[248:251]\n\t" \
  "v_mfma_f32_16x16x32_bf16 v[252:255], v[128:131], v[100:103], v[252:255]\n\t" \
  "s_waitcnt lgkmcnt(0)\n\t" \
  "v_mfma_f32_16x16x32_bf16 v[252:255], v[120:123], v[112:115], v[252:255]\n\t" \
  "v_mfma_f32_16x16x32_bf16 v[188:191], v[120:123], v[104:107], v[188:191]\n\t" \
  "v_mfma_f32_16x16x32_bf16 v[188:191], v[128:131], v[68:71], v[188:191]\n\t" \
  "s_barrier\n\t" \
  "v_mfma_f32_16x16x32_bf16 v[196:199], v[128:131], v[116:119], v[196:199]\n\t" \
  "ds_read_b64_tr_b16 v[72:73], v62 offset:34816\n\t" \
  "v_mfma_f32_16x16x32_bf16 v[196:199], v[120:123], v[80:83], v[196:199]\n\t" \
  "ds_read_b64_tr_b16 v[74:75], v62 offset:35328\n\t" \
  "v_mfma_f32_16x16x32_bf16 v[204:207], v[120:123], v[124:127], v[204:207]\n\t" \
  "ds_read_b128 v[64:67], v60 offset:2048\n\t" \
  "v_mfma_f32_16x16x32_bf16 v[204:207], v[128:131], v[88:91], v[204:207]\n\t" \
  "ds_read_b64_tr_b16 v[108:109], v62 offset:38912\n\t" \
  "v_mfma_f32_16x16x32_bf16 v[212:215], v[128:131], v[132:135], v[212:215]\n\t" \
  "ds_read_b64_tr_b16 v[110:111], v62 offset:39424\n\t" \
  "v_mfma_f32_16x16x32_bf16 v[212:215], v[120:123], v[96:99], v[212:215]\n\t" \
  "ds_read_b128 v[140:143], v61 offset:2048\n\t" \
  "v_mfma_f32_16x16x32_bf16 v[152:155], v[136:139], v[68:71], v[152:155]\n\t" \
  "ds_read_b128 v[92:95], v61 offset:4096\n\t" \
  "v_mfma_f32_16x16x32_bf16 v[152:155], v[148:151], v[104:107], v[152:155]\n\t" \
  "ds_read_b128 v[84:87], v60 offset:4096\n\t" \
  "v_mfma_f32_16x16x32_bf16 v[160:163], v[148:151], v[80:83], v[160:163]\n\t" \
  "ds_read_b128 v[100:103], v60 offset:6144\n\t" \
  "v_mfma_f32_16x16x32_bf16 v[160:163], v[136:139], v[116:119], v[160:163]\n\t" \
  "ds_read_b128 v[112:115], v61 offset:6144\n\t" \
  "v_mfma_f32_16x16x32_bf16 v[168:171], v[136:139], v[88:91], v[168:171]\n\t" \
  "ds_read_b128 v[128:131], v61 offset:8192\n\t" \
  "v_mfma_f32_16x16x32_bf16 v[168:171], v[148:151], v[124:127], v[168:171]\n\t" \
  "ds_read_b128 v[120:123], v60 offset:8192\n\t" \
  "v_mfma_f32_16x16x32_bf16 v[176:179], v[148:151], v[96:99], v[176:179]\n\t" \
  "ds_read_b64_tr_b16 v[148:149], v63 offset:34816\n\t" \
  "v_mfma_f32_16x16x32_bf16 v[176:179], v[136:139], v[132:135], v[176:179]\n\t" \
  "ds_read_b64_tr_b16 v[150:151], v63 offset:35328\n\t" \
  "v_mfma_f32_16x16x32_bf16 v[156:159], v[76:79], v[68:71], v[156:159]\n\t" \
  "ds_read_b64_tr_b16 v[136:137], v63 offset:38912\n\t" \
  "v_mfma_f32_16x16x32_bf16 v[156:159], v[144:147], v[104:107], v[156:159]\n\t" \
  "ds_read_b64_tr_b16 v[138:139], v63 offset:39424\n\t" \
  "v_mfma_f32_16x16x32_bf16 v[164:167], v[144:147], v[80:83], v[164:167]\n\t" \
  "ds_read_b64_tr_b16 v[68:69], v62 offset:26624\n\t" \
  "v_mfma_f32_16x16x32_bf16 v[164:167], v[76:79], v[116:119], v[164:167]\n\t" \
  "ds_read_b64_tr_b16 v[70:71], v62 offset:27136\n\t" \
  "v_mfma_f32_16x16x32_bf16 v[172:175], v[76:79], v[88:91], v[172:175]\n\t" \
  "ds_read_b64_tr_b16 v[104:105], v62 offset:30720\n\t" \
  "v_mfma_f32_16x16x32_bf16 v[172:175], v[144:147], v[124:127], v[172:175]\n\t" \
  "ds_read_b64_tr_b16 v[106:107], v62 offset:31232\n\t" \
  "v_mfma_f32_16x16x32_bf16 v[180:183], v[144:147], v[96:99], v[180:183]\n\t" \
  "ds_read_b128 v[80:83], v60 offset:0\n\t" \
  "v_mfma_f32_16x16x32_bf16 v[180:183], v[76:79], v[132:135], v[180:183]\n\t" \
  "ds_read_b128 v[116:119], v61 offset:0\n\t" \
  "s_waitcnt lgkmcnt(7)\n\t" \
  "v_mfma_f32_16x16x32_bf16 v[216:219], v[64:67], v[72:75], v[216:219]\n\t" \
  "ds_read_b64_tr_b16 v[88:89], v63 offset:26624\n\t" \
  "v_mfma_f32_16x16x32_bf16 v[216:219], v[140:143], v[108:111], v[216:219]\n\t" \
  "ds_read_b64_tr_b16 v[90:91], v63 offset:27136\n\t" \
  "v_mfma_f32_16x16x32_bf16 v[224:227], v[92:95], v[108:111], v[224:227]\n\t" \
  "ds_read_b64_tr_b16 v[124:125], v63 offset:30720\n\t" \
  "v_mfma_f32_16x16x32_bf16 v[224:227], v[84:87], v[72:75], v[224:227]\n\t" \
  "ds_read_b64_tr_b16 v[126:127], v63 offset:31232\n\t" \
  "v_mfma_f32_16x16x32_bf16 v[232:235], v[100:103], v[72:75], v[232:235]\n\t" \
  "ds_read_b64_tr_b16 v[96:97], v62 offset:18432\n\t" \
  "v_mfma_f32_16x16x32_bf16 v[232:235], v[112:115], v[108:111], v[232:235]\n\t" \
  "ds_read_b64_tr_b16 v[98:99], v62 offset:18944\n\t" \
  "v_mfma_f32_16x16x32_bf16 v[240:243], v[128:131], v[108:111], v[240:243]\n\t" \
  "ds_read_b64_tr_b16 v[144:145], v62 offset:22528\n\t" \
  "v_mfma_f32_16x16x32_bf16 v[240:243], v[120:123], v[72:75], v[240:243]\n\t" \
  "ds_read_b64_tr_b16 v[146:147], v62 offset:23040\n\t" \
  "v_mfma_f32_16x16x32_bf16 v[220:223], v[64:67], v[148:151], v[220:223]\n\t" \
  "ds_read_b64_tr_b16 v[76:77], v63 offset:18432\n\t" \
  "s_waitcnt lgkmcnt(7)\n\t" \
  "505:\n\t" \
  "s_cbranch_execz 509f\n\t" \
  "v_mfma_f32_16x16x32_bf16 v[220:223], v[140:143], v[136:139], v[220:223]\n\t" \
  "ds_read_b64_tr_b16 v[78:79], v63 offset:18944\n\t" \
  "v_mfma_f32_16x16x32_bf16 v[228:231], v[92:95], v[136:139], v[228:231]\n\t" \
  "ds_read_b64_tr_b16 v[132:133], v63 offset:22528\n\t" \
  "v_mfma_f32_16x16x32_bf16 v[228:231], v[84:87], v[148:151], v[228:231]\n\t" \
  "ds_read_b64_tr_b16 v[134:135], v63 offset:23040\n\t" \
  "v_mfma_f32_16x16x32_bf16 v[236:239], v[100:103], v[148:151], v[236:239]\n\t" \
  "v_mfma_f32_16x16x32_bf16 v[236:239], v[112:115], v[136:139], v[236:239]\n\t" \
  "v_mfma_f32_16x16x32_bf16 v[244:247], v[128:131], v[136:139], v[244:247]\n\t" \
  "v_mfma_f32_16x16x32_bf16 v[244:247], v[120:123], v[148:151], v[244:247]\n\t" \
  "v_mfma_f32_16x16x32_bf16 v[184:187], v[68:71], v[64:67], v[184:187]\n\t" \
  "v_mfma_f32_16x16x32_bf16 v[184:187], v[104:107], v[140:143], v[184:187]\n\t" \
  "v_mfma_f32_16x16x32_bf16 v[192:195], v[104:107], v[92:95], v[192:195]\n\t" \
  "v_mfma_f32_16x16x32_bf16 v[192:195], v[68:71], v[84:87], v[192:195]\n\t" \
  "v_mfma_f32_16x16x32_bf16 v[200:203], v[68:71], v[100:103], v[200:203]\n\t" \
  "v_mfma_f32_16x16x32_bf16 v[200:203], v[104:107], v[112:115], v[200:203]\n\t" \
  "v_mfma_f32_16x16x32_bf16 v[208:211], v[104:107], v[128:131], v[208:211]\n\t" \
  "v_mfma_f32_16x16x32_bf16 v[208:211], v[68:71], v[120:123], v[208:211]\n\t" \
  "v_mfma_f32_16x16x32_bf16 v[248:251], v[68:71], v[80:83], v[248:251]\n\t" \
  "v_mfma_f32_16x16x32_bf16 v[248:251], v[104:107], v[116:119], v[248:251]\n\t" \
  "v_mfma_f32_16x16x32_bf16 v[252:255], v[88:91], v[80:83], v[252:255]\n\t" \
  "s_waitcnt lgkmcnt(0)\n\t" \
  "v_mfma_f32_16x16x32_bf16 v[252:255], v[124:127], v[116:119], v[252:255]\n\t" \
  "v_mfma_f32_16x16x32_bf16 v[188:191], v[124:127], v[140:143], v[188:191]\n\t" \
  "v_mfma_f32_16x16x32_bf16 v[188:191], v[88:91], v[64:67], v[188:191]\n\t" \
  "s_barrier\n\t" \
  "v_mfma_f32_16x16x32_bf16 v[196:199], v[88:91], v[84:87], v[196:199]\n\t" \
  "ds_read_b64_tr_b16 v[108:109], v54 offset:34816\n\t" \
  "v_mfma_f32_16x16x32_bf16 v[196:199], v[124:127], v[92:95], v[196:199]\n\t" \
  "ds_read_b64_tr_b16 v[110:111], v54 offset:35328\n\t" \
  "v_mfma_f32_16x16x32_bf16 v[204:207], v[124:127], v[112:115], v[204:207]\n\t" \
  "ds_read_b128 v[72:75], v52 offset:2048\n\t" \
  "v_mfma_f32_16x16x32_bf16 v[204:207], v[88:91], v[100:103], v[204:207]\n\t" \
  "ds_read_b64_tr_b16 v[136:137], v54 offset:38912\n\t" \
  "v_mfma_f32_16x16x32_bf16 v[212:215], v[88:91], v[120:123], v[212:215]\n\t" \
  "ds_read_b64_tr_b16 v[138:139], v54 offset:39424\n\t" \
  "v_mfma_f32_16x16x32_bf16 v[212:215], v[124:127], v[128:131], v[212:215]\n\t" \
  "ds_read_b128 v[148:151], v53 offset:2048\n\t" \
  "v_mfma_f32_16x16x32_bf16 v[152:155], v[96:99], v[64:67], v[152:155]\n\t" \
  "ds_read_b128 v[68:71], v53 offset:4096\n\t" \
  "v_mfma_f32_16x16x32_bf16 v[152:155], v[144:147], v[140:143], v[152:155]\n\t" \
  "ds_read_b128 v[104:107], v52 offset:4096\n\t" \
  "v_mfma_f32_16x16x32_bf16 v[160:163], v[144:147], v[92:95], v[160:163]\n\t" \
  "ds_read_b128 v[80:83], v52 offset:6144\n\t" \
  "v_mfma_f32_16x16x32_bf16 v[160:163], v[96:99], v[84:87], v[160:163]\n\t" \
  "ds_read_b128 v[116:119], v53 offset:6144\n\t" \
  "v_mfma_f32_16x16x32_bf16 v[168:171], v[96:99], v[100:103], v[168:171]\n\t" \
  "ds_read_b128 v[88:91], v53 offset:8192\n\t" \
  "v_mfma_f32_16x16x32_bf16 v[168:171], v[144:147], v[112:115], v[168:171]\n\t" \
  "ds_read_b128 v[124:127], v52 offset:8192\n\t" \
  "v_mfma_f32_16x16x32_bf16 v[176:179], v[144:147], v[128:131], v[176:179]\n\t" \
  "ds_read_b64_tr_b16 v[144:145], v55 offset:34816\n\t" \
  "v_mfma_f32_16x16x32_bf16 v[176:179], v[96:99], v[120:123], v[176:179]\n\t" \
  "ds_read_b64_tr_b16 v[146:147], v55 offset:35328\n\t" \
  "v_mfma_f32_16x16x32_bf16 v[156:159], v[76:79], v[64:67], v[156:159]\n\t" \
  "ds_read_b64_tr_b16 v[96:97], v55 offset:38912\n\t" \
  "v_mfma_f32_16x16x32_bf16 v[156:159], v[132:135], v[140:143], v[156:159]\n\t" \
  "ds_read_b64_tr_b16 v[98:99], v55 offset:39424\n\t" \
  "v_mfma_f32_16x16x32_bf16 v[164:167], v[132:135], v[92:95], v[164:167]\n\t" \
  "ds_read_b64_tr_b16 v[64:65], v54 offset:26624\n\t" \
  "v_mfma_f32_16x16x32_bf16 v[164:167], v[76:79], v[84:87], v[164:167]\n\t" \
  "ds_read_b64_tr_b16 v[66:67], v54 offset:27136\n\t" \
  "v_mfma_f32_16x16x32_bf16 v[172:175], v[76:79], v[100:103], v[172:175]\n\t" \
  "ds_read_b64_tr_b16 v[140:141], v54 offset:30720\n\t" \
  "v_mfma_f32_16x16x32_bf16 v[172:175], v[132:135], v[112:115], v[172:175]\n\t" \
  "ds_read_b64_tr_b16 v[142:143], v54 offset:31232\n\t" \
  "v_mfma_f32_16x16x32_bf16 v[180:183], v[132:135], v[128:131], v[180:183]\n\t" \
  "ds_read_b128 v[92:95], v52 offset:0\n\t" \
  "v_mfma_f32_16x16x32_bf16 v[180:183], v[76:79], v[120:123], v[180:183]\n\t" \
  "ds_read_b128 v[84:87], v53 offset:0\n\t" \
  "s_waitcnt lgkmcnt(7)\n\t" \
  "v_mfma_f32_16x16x32_bf16 v[216:219], v[72:75], v[108:111], v[216:219]\n\t" \
  "ds_read_b64_tr_b16 v[100:101], v55 offset:26624\n\t" \
  "v_mfma_f32_16x16x32_bf16 v[216:219], v[148:151], v[136:139], v[216:219]\n\t" \
  "ds_read_b64_tr_b16 v[102:103], v55 offset:27136\n\t" \
  "v_mfma_f32_16x16x32_bf16 v[224:227], v[68:71], v[136:139], v[224:227]\n\t" \
  "ds_read_b64_tr_b16 v[112:113], v55 offset:30720\n\t" \
  "v_mfma_f32_16x16x32_bf16 v[224:227], v[104:107], v[108:111], v[224:227]\n\t" \
  "ds_read_b64_tr_b16 v[114:115], v55 offset:31232\n\t" \
  "v_mfma_f32_16x16x32_bf16 v[232:235], v[80:83], v[108:111], v[232:235]\n\t" \
  "ds_read_b64_tr_b16 v[128:129], v54 offset:18432\n\t" \
  "v_mfma_f32_16x16x32_bf16 v[232:235], v[116:119], v[136:139], v[232:235]\n\t" \
  "ds_read_b64_tr_b16 v[130:131], v54 offset:18944\n\t" \
  "v_mfma_f32_16x16x32_bf16 v[240:243], v[88:91], v[136:139], v[240:243]\n\t" \
  "ds_read_b64_tr_b16 v[132:133], v54 offset:22528\n\t" \
  "v_mfma_f32_16x16x32_bf16 v[240:243], v[124:127], v[108:111], v[240:243]\n\t" \
  "ds_read_b64_tr_b16 v[134:135], v54 offset:23040\n\t" \
  "v_mfma_f32_16x16x32_bf16 v[220:223], v[72:75], v[144:147], v[220:223]\n\t" \
  "ds_read_b64_tr_b16 v[76:77], v55 offset:18432\n\t" \
  "s_waitcnt lgkmcnt(7)\n\t" \
  "v_mfma_f32_16x16x32_bf16 v[220:223], v[148:151], v[96:99], v[220:223]\n\t" \
  "ds_read_b64_tr_b16 v[78:79], v55 offset:18944\n\t" \
  "v_mfma_f32_16x16x32_bf16 v[228:231], v[68:71], v[96:99], v[228:231]\n\t" \
  "ds_read_b64_tr_b16 v[120:121], v55 offset:22528\n\t" \
  "v_mfma_f32_16x16x32_bf16 v[228:231], v[104:107], v[144:147], v[228:231]\n\t" \
  "ds_read_b64_tr_b16 v[122:123], v55 offset:23040\n\t" \
  "v_mfma_f32_16x16x32_bf16 v[236:239], v[80:83], v[144:147], v[236:239]\n\t" \
  "v_mfma_f32_16x16x32_bf16 v[236:239], v[116:119], v[96:99], v[236:239]\n\t" \
  "v_mfma_f32_16x16x32_bf16 v[244:247], v[88:91], v[96:99], v[244:247]\n\t" \
  "v_mfma_f32_16x16x32_bf16 v[244:247], v[124:127], v[144:147], v[244:247]\n\t" \
  "v_mfma_f32_16x16x32_bf16 v[184:187], v[64:67], v[72:75], v[184:187]\n\t" \
  "v_mfma_f32_16x16x32_bf16 v[184:187], v[140:143], v[148:151], v[184:187]\n\t" \
  "v_mfma_f32_16x16x32_bf16 v[192:195], v[140:143], v[68:71], v[192:195]\n\t" \
  "v_mfma_f32_16x16x32_bf16 v[192:195], v[64:67], v[104:107], v[192:195]\n\t" \
  "v_mfma_f32_16x16x32_bf16 v[200:203], v[64:67], v[80:83], v[200:203]\n\t" \
  "v_mfma_f32_16x16x32_bf16 v[200:203], v[140:143], v[116:119], v[200:203]\n\t" \
  "v_mfma_f32_16x16x32_bf16 v[208:211], v[140:143], v[88:91], v[208:211]\n\t" \
  "v_mfma_f32_16x16x32_bf16 v[208:211], v[64:67], v[124:127], v[208:211]\n\t" \
  "v_mfma_f32_16x16x32_bf16 v[248:251], v[64:67], v[92:95], v[248:251]\n\t" \
  "v_mfma_f32_16x16x32_bf16 v[248:251], v[140:143], v[84:87], v[248:251]\n\t" \
  "v_mfma_f32_16x16x32_bf16 v[252:255], v[100:103], v[92:95], v[252:255]\n\t" \
  "s_waitcnt lgkmcnt(0)\n\t" \
  "v_mfma_f32_16x16x32_bf16 v[252:255], v[112:115], v[84:87], v[252:255]\n\t" \
  "v_mfma_f32_16x16x32_bf16 v[188:191], v[112:115], v[148:151], v[188:191]\n\t" \
  "v_mfma_f32_16x16x32_bf16 v[188:191], v[100:103], v[72:75], v[188:191]\n\t" \
  "s_barrier\n\t" \
  "v_mfma_f32_16x16x32_bf16 v[196:199], v[100:103], v[104:107], v[196:199]\n\t" \
  "ds_read_b64_tr_b16 v[96:97], v58 offset:34816\n\t" \
  "v_mfma_f32_16x16x32_bf16 v[196:199], v[112:115], v[68:71], v[196:199]\n\t" \
  "ds_read_b64_tr_b16 v[98:99], v58 offset:35328\n\t" \
  "v_mfma_f32_16x16x32_bf16 v[204:207], v[112:115], v[116:119], v[204:207]\n\t" \
  "ds_read_b128 v[136:139], v56 offset:2048\n\t" \
  "v_mfma_f32_16x16x32_bf16 v[204:207], v[100:103], v[80:83], v[204:207]\n\t" \
  "ds_read_b64_tr_b16 v[64:65], v58 offset:38912\n\t" \
  "v_mfma_f32_16x16x32_bf16 v[212:215], v[100:103], v[124:127], v[212:215]\n\t" \
  "ds_read_b64_tr_b16 v[66:67], v58 offset:39424\n\t" \
  "v_mfma_f32_16x16x32_bf16 v[212:215], v[112:115], v[88:91], v[212:215]\n\t" \
  "ds_read_b128 v[108:111], v57 offset:2048\n\t" \
  "v_mfma_f32_16x16x32_bf16 v[152:155], v[128:131], v[72:75], v[152:155]\n\t" \
  "ds_read_b128 v[144:147], v57 offset:4096\n\t" \
  "v_mfma_f32_16x16x32_bf16 v[152:155], v[132:135], v[148:151], v[152:155]\n\t" \
  "ds_read_b128 v[140:143], v56 offset:4096\n\t" \
  "v_mfma_f32_16x16x32_bf16 v[160:163], v[132:135], v[68:71], v[160:163]\n\t" \
  "ds_read_b128 v[112:115], v56 offset:6144\n\t" \
  "v_mfma_f32_16x16x32_bf16 v[160:163], v[128:131], v[104:107], v[160:163]\n\t" \
  "ds_read_b64_tr_b16 v[92:93], v59 offset:34816\n\t" \
  "v_mfma_f32_16x16x32_bf16 v[168:171], v[128:131], v[80:83], v[168:171]\n\t" \
  "ds_read_b64_tr_b16 v[94:95], v59 offset:35328\n\t" \
  "v_mfma_f32_16x16x32_bf16 v[168:171], v[132:135], v[116:119], v[168:171]\n\t" \
  "ds_read_b64_tr_b16 v[84:85], v59 offset:38912\n\t" \
  "v_mfma_f32_16x16x32_bf16 v[176:179], v[132:135], v[88:91], v[176:179]\n\t" \
  "ds_read_b128 v[132:135], v57 offset:6144\n\t" \
  "v_mfma_f32_16x16x32_bf16 v[176:179], v[128:131], v[124:127], v[176:179]\n\t" \
  "ds_read_b128 v[128:131], v57 offset:8192\n\t" \
  "v_mfma_f32_16x16x32_bf16 v[156:159], v[76:79], v[72:75], v[156:159]\n\t" \
  "ds_read_b64_tr_b16 v[86:87], v59 offset:39424\n\t" \
  "v_mfma_f32_16x16x32_bf16 v[156:159], v[120:123], v[148:151], v[156:159]\n\t" \
  "ds_read_b128 v[148:151], v56 offset:8192\n\t" \
  "v_mfma_f32_16x16x32_bf16 v[164:167], v[120:123], v[68:71], v[164:167]\n\t" \
  "ds_read_b64_tr_b16 v[100:101], v58 offset:26624\n\t" \
  "v_mfma_f32_16x16x32_bf16 v[164:167], v[76:79], v[104:107], v[164:167]\n\t" \
  "ds_read_b64_tr_b16 v[102:103], v58 offset:27136\n\t" \
  "v_mfma_f32_16x16x32_bf16 v[172:175], v[76:79], v[80:83], v[172:175]\n\t" \
  "ds_read_b64_tr_b16 v[72:73], v58 offset:30720\n\t" \
  "v_mfma_f32_16x16x32_bf16 v[172:175], v[120:123], v[116:119], v[172:175]\n\t" \
  "ds_read_b64_tr_b16 v[74:75], v58 offset:31232\n\t" \
  "v_mfma_f32_16x16x32_bf16 v[180:183], v[120:123], v[88:91], v[180:183]\n\t" \
  "ds_read_b128 v[68:71], v56 offset:0\n\t" \
  "v_mfma_f32_16x16x32_bf16 v[180:183], v[76:79], v[124:127], v[180:183]\n\t" \
  "ds_read_b128 v[104:107], v57 offset:0\n\t" \
  "s_waitcnt lgkmcnt(7)\n\t" \
  "v_mfma_f32_16x16x32_bf16 v[216:219], v[136:139], v[96:99], v[216:219]\n\t" \
  "ds_read_b64_tr_b16 v[80:81], v59 offset:26624\n\t" \
  "v_mfma_f32_16x16x32_bf16 v[216:219], v[108:111], v[64:67], v[216:219]\n\t" \
  "ds_read_b64_tr_b16 v[82:83], v59 offset:27136\n\t" \
  "v_mfma_f32_16x16x32_bf16 v[224:227], v[144:147], v[64:67], v[224:227]\n\t" \
  "ds_read_b64_tr_b16 v[88:89], v59 offset:30720\n\t" \
  "v_mfma_f32_16x16x32_bf16 v[224:227], v[140:143], v[96:99], v[224:227]\n\t" \
  "ds_read_b64_tr_b16 v[90:91], v59 offset:31232\n\t" \
  "v_mfma_f32_16x16x32_bf16 v[232:235], v[112:115], v[96:99], v[232:235]\n\t" \
  "ds_read_b64_tr_b16 v[116:117], v58 offset:18432\n\t" \
  "v_mfma_f32_16x16x32_bf16 v[232:235], v[132:135], v[64:67], v[232:235]\n\t" \
  "ds_read_b64_tr_b16 v[118:119], v58 offset:18944\n\t" \
  "v_mfma_f32_16x16x32_bf16 v[240:243], v[128:131], v[64:67], v[240:243]\n\t" \
  "ds_read_b64_tr_b16 v[120:121], v58 offset:22528\n\t" \
  "s_waitcnt lgkmcnt(7)\n\t" \
  "v_mfma_f32_16x16x32_bf16 v[240:243], v[148:151], v[96:99], v[240:243]\n\t" \
  "ds_read_b64_tr_b16 v[122:123], v58 offset:23040\n\t" \
  "v_mfma_f32_16x16x32_bf16 v[220:223], v[136:139], v[92:95], v[220:223]\n\t" \
  "ds_read_b64_tr_b16 v[124:125], v59 offset:18432\n\t" \
  "v_mfma_f32_16x16x32_bf16 v[220:223], v[108:111], v[84:87], v[220:223]\n\t" \
  "ds_read_b64_tr_b16 v[126:127], v59 offset:18944\n\t" \
  "v_mfma_f32_16x16x32_bf16 v[228:231], v[144:147], v[84:87], v[228:231]\n\t" \
  "ds_read_b64_tr_b16 v[76:77], v59 offset:22528\n\t" \
  "v_mfma_f32_16x16x32_bf16 v[228:231], v[140:143], v[92:95], v[228:231]\n\t" \
  "ds_read_b64_tr_b16 v[78:79], v59 offset:23040\n\t" \
  "v_mfma_f32_16x16x32_bf16 v[236:239], v[112:115], v[92:95], v[236:239]\n\t" \
  "v_mfma_f32_16x16x32_bf16 v[236:239], v[132:135], v[84:87], v[236:239]\n\t" \
  "v_mfma_f32_16x16x32_bf16 v[244:247], v[128:131], v[84:87], v[244:247]\n\t" \
  "v_mfma_f32_16x16x32_bf16 v[244:247], v[148:151], v[92:95], v[244:247]\n\t" \
  "v_mfma_f32_16x16x32_bf16 v[184:187], v[100:103], v[136:139], v[184:187]\n\t" \
  "v_mfma_f32_16x16x32_bf16 v[184:187], v[72:75], v[108:111], v[184:187]\n\t" \
  "v_mfma_f32_16x16x32_bf16 v[192:195], v[72:75], v[144:147], v[192:195]\n\t" \
  "v_and_b32 v14, -16, v32\n\t" \
  "v_mfma_f32_16x16x32_bf16 v[192:195], v[100:103], v[140:143], v[192:195]\n\t" \
  "v_mfma_f32_16x16x32_bf16 v[200:203], v[100:103], v[112:115], v[200:203]\n\t" \
  "ds_read_b128 v[10:13], v14 offset:128\n\t" \
  "v_mfma_f32_16x16x32_bf16 v[200:203], v[72:75], v[132:135], v[200:203]\n\t" \
  "v_mfma_f32_16x16x32_bf16 v[208:211], v[72:75], v[128:131], v[208:211]\n\t" \
  "v_mfma_f32_16x16x32_bf16 v[208:211], v[100:103], v[148:151], v[208:211]\n\t" \
  "v_mfma_f32_16x16x32_bf16 v[248:251], v[100:103], v[68:71], v[248:251]\n\t" \
  "v_mfma_f32_16x16x32_bf16 v[248:251], v[72:75], v[104:107], v[248:251]\n\t" \
  "s_waitcnt lgkmcnt(1)\n\t" \
  "v_mfma_f32_16x16x32_bf16 v[252:255], v[80:83], v[68:71], v[252:255]\n\t" \
  "s_waitcnt lgkmcnt(0)\n\t" \
  "v_and_b32 v10, v10, v11\n\t" \
  "v_and_b32 v12, v12, v13\n\t" \
  "v_mfma_f32_16x16x32_bf16 v[252:255], v[88:91], v[104:107], v[252:255]\n\t" \
  "v_and_b32 v10, v10, v12\n\t" \
  "v_mfma_f32_16x16x32_bf16 v[188:191], v[88:91], v[108:111], v[188:191]\n\t" \
  "v_readfirstlane_b32 vcc_lo, v10\n\t" \
  "v_mfma_f32_16x16x32_bf16 v[188:191], v[80:83], v[136:139], v[188:191]\n\t" \
  "ds_write_b32 v32, v0 offset:144\n\t" \
  "s_cmp_eq_u32 vcc_lo, 0\n\t" \
  "s_cbranch_scc1 15f\n\t" \
  "14:\n\t" \
  "v_mfma_f32_16x16x32_bf16 v[196:199], v[80:83], v[140:143], v[196:199]\n\t" \
  "ds_read_b64_tr_b16 v[64:65], v62 offset:26624\n\t" \
  "v_mfma_f32_16x16x32_bf16 v[196:199], v[88:91], v[144:147], v[196:199]\n\t" \
  "ds_read_b64_tr_b16 v[66:67], v62 offset:27136\n\t" \
  "v_mfma_f32_16x16x32_bf16 v[204:207], v[88:91], v[132:135], v[204:207]\n\t" \
  "ds_read_b128 v[96:99], v60 offset:0\n\t" \
  "v_mfma_f32_16x16x32_bf16 v[204:207], v[80:83], v[112:115], v[204:207]\n\t" \
  "ds_read_b128 v[84:87], v60 offset:2048\n\t" \
  "v_mfma_f32_16x16x32_bf16 v[212:215], v[80:83], v[148:151], v[212:215]\n\t" \
  "ds_read_b128 v[92:95], v60 offset:4096\n\t" \
  "v_mfma_f32_16x16x32_bf16 v[212:215], v[88:91], v[128:131], v[212:215]\n\t" \
  "ds_read_b128 v[100:103], v60 offset:6144\n\t" \
  "v_mfma_f32_16x16x32_bf16 v[152:155], v[116:119], v[136:139], v[152:155]\n\t" \
  "ds_read_b128 v[72:75], v60 offset:8192\n\t" \
  "v_mfma_f32_16x16x32_bf16 v[152:155], v[120:123], v[108:111], v[152:155]\n\t" \
  "ds_read_b64_tr_b16 v[68:69], v63 offset:26624\n\t" \
  "v_mfma_f32_16x16x32_bf16 v[160:163], v[120:123], v[144:147], v[160:163]\n\t" \
  "ds_read_b64_tr_b16 v[70:71], v63 offset:27136\n\t" \
  "v_mfma_f32_16x16x32_bf16 v[160:163], v[116:119], v[140:143], v[160:163]\n\t" \
  "ds_read_b64_tr_b16 v[104:105], v62 offset:34816\n\t" \
  "v_mfma_f32_16x16x32_bf16 v[168:171], v[116:119], v[112:115], v[168:171]\n\t" \
  "ds_read_b64_tr_b16 v[106:107], v62 offset:35328\n\t" \
  "v_mfma_f32_16x16x32_bf16 v[168:171], v[120:123], v[132:135], v[168:171]\n\t" \
  "ds_read_b64_tr_b16 v[80:81], v63 offset:34816\n\t" \
  "v_mfma_f32_16x16x32_bf16 v[176:179], v[120:123], v[128:131], v[176:179]\n\t" \
  "ds_read_b64_tr_b16 v[82:83], v63 offset:35328\n\t" \
  "v_mfma_f32_16x16x32_bf16 v[176:179], v[116:119], v[148:151], v[176:179]\n\t" \
  "ds_read_b64_tr_b16 v[88:89], v62 offset:18432\n\t" \
  "v_mfma_f32_16x16x32_bf16 v[156:159], v[124:127], v[136:139], v[156:159]\n\t" \
  "ds_read_b64_tr_b16 v[90:91], v62 offset:18944\n\t" \
  "v_mfma_f32_16x16x32_bf16 v[156:159], v[76:79], v[108:111], v[156:159]\n\t" \
  "ds_read_b64_tr_b16 v[120:121], v62 offset:30720\n\t" \
  "v_mfma_f32_16x16x32_bf16 v[164:167], v[76:79], v[144:147], v[164:167]\n\t" \
  "ds_read_b64_tr_b16 v[122:123], v62 offset:31232\n\t" \
  "v_mfma_f32_16x16x32_bf16 v[164:167], v[124:127], v[140:143], v[164:167]\n\t" \
  "ds_read_b128 v[116:119], v61 offset:0\n\t" \
  "v_mfma_f32_16x16x32_bf16 v[172:175], v[124:127], v[112:115], v[172:175]\n\t" \
  "ds_read_b128 v[136:139], v61 offset:2048\n\t" \
  "v_mfma_f32_16x16x32_bf16 v[172:175], v[76:79], v[132:135], v[172:175]\n\t" \
  "ds_read_b128 v[108:111], v61 offset:4096\n\t" \
  "v_mfma_f32_16x16x32_bf16 v[180:183], v[76:79], v[128:131], v[180:183]\n\t" \
  "ds_read_b64_tr_b16 v[76:77], v63 offset:18432\n\t" \
  "v_mfma_f32_16x16x32_bf16 v[180:183], v[124:127], v[148:151], v[180:183]\n\t" \
  "ds_read_b64_tr_b16 v[78:79], v63 offset:18944\n\t" \
  "s_waitcnt lgkmcnt(7)\n\t" \
  "v_mfma_f32_16x16x32_bf16 v[248:251], v[64:67], v[96:99], v[248:251]\n\t" \
  "ds_read_b128 v[144:147], v61 offset:6144\n\t" \
  "v_mfma_f32_16x16x32_bf16 v[184:187], v[64:67], v[84:87], v[184:187]\n\t" \
  "ds_read_b128 v[140:143], v61 offset:8192\n\t" \
  "v_mfma_f32_16x16x32_bf16 v[192:195], v[64:67], v[92:95], v[192:195]\n\t" \
  "ds_read_b64_tr_b16 v[112:113], v63 offset:30720\n\t" \
  "v_mfma_f32_16x16x32_bf16 v[200:203], v[64:67], v[100:103], v[200:203]\n\t" \
  "ds_read_b64_tr_b16 v[114:115], v63 offset:31232\n\t" \
  "v_mfma_f32_16x16x32_bf16 v[208:211], v[64:67], v[72:75], v[208:211]\n\t" \
  "ds_read_b64_tr_b16 v[132:133], v62 offset:22528\n\t" \
  "v_mfma_f32_16x16x32_bf16 v[252:255], v[68:71], v[96:99], v[252:255]\n\t" \
  "ds_read_b64_tr_b16 v[134:135], v62 offset:23040\n\t" \
  "v_mfma_f32_16x16x32_bf16 v[188:191], v[68:71], v[84:87], v[188:191]\n\t" \
  "ds_read_b64_tr_b16 v[128:129], v63 offset:22528\n\t" \
  "v_mfma_f32_16x16x32_bf16 v[196:199], v[68:71], v[92:95], v[196:199]\n\t" \
  "ds_read_b64_tr_b16 v[130:131], v63 offset:23040\n\t" \
  "v_mfma_f32_16x16x32_bf16 v[204:207], v[68:71], v[100:103], v[204:207]\n\t" \
  "ds_read_b64_tr_b16 v[124:125], v62 offset:38912\n\t" \
  "v_mfma_f32_16x16x32_bf16 v[212:215], v[68:71], v[72:75], v[212:215]\n\t" \
  "ds_read_b64_tr_b16 v[126:127], v62 offset:39424\n\t" \
  "v_mfma_f32_16x16x32_bf16 v[216:219], v[84:87], v[104:107], v[216:219]\n\t" \
  "ds_read_b64_tr_b16 v[148:149], v63 offset:38912\n\t" \
  "v_mfma_f32_16x16x32_bf16 v[224:227], v[92:95], v[104:107], v[224:227]\n\t" \
  "ds_read_b64_tr_b16 v[150:151], v63 offset:39424\n\t" \
  "v_mfma_f32_16x16x32_bf16 v[232:235], v[100:103], v[104:107], v[232:235]\n\t" \
  "v_mfma_f32_16x16x32_bf16 v[240:243], v[72:75], v[104:107], v[240:243]\n\t" \
  "v_mfma_f32_16x16x32_bf16 v[220:223], v[84:87], v[80:83], v[220:223]\n\t" \
  "v_mfma_f32_16x16x32_bf16 v[228:231], v[92:95], v[80:83], v[228:231]\n\t" \
  "v_mfma_f32_16x16x32_bf16 v[236:239], v[100:103], v[80:83], v[236:239]\n\t" \
  "v_mfma_f32_16x16x32_bf16 v[244:247], v[72:75], v[80:83], v[244:247]\n\t" \
  "v_mfma_f32_16x16x32_bf16 v[152:155], v[88:91], v[84:87], v[152:155]\n\t" \
  "v_mfma_f32_16x16x32_bf16 v[160:163], v[88:91], v[92:95], v[160:163]\n\t" \
  "v_mfma_f32_16x16x32_bf16 v[168:171], v[88:91], v[100:103], v[168:171]\n\t" \
  "v_mfma_f32_16x16x32_bf16 v[176:179], v[88:91], v[72:75], v[176:179]\n\t" \
  "s_waitcnt lgkmcnt(0)\n\t" \
  "v_mfma_f32_16x16x32_bf16 v[156:159], v[76:79], v[84:87], v[156:159]\n\t" \
  "v_mfma_f32_16x16x32_bf16 v[164:167], v[76:79], v[92:95], v[164:167]\n\t" \
  "ds_write_b32 v32, v0\n\t" \
  "v_mfma_f32_16x16x32_bf16 v[172:175], v[76:79], v[100:103], v[172:175]\n\t" \
  "v_mfma_f32_16x16x32_bf16 v[180:183], v[76:79], v[72:75], v[180:183]\n\t" \
  "v_mfma_f32_16x16x32_bf16 v[248:251], v[120:123], v[116:119], v[248:251]\n\t" \
  "v_and_b32 v56, -16, v32\n\t" \
  "v_mfma_f32_16x16x32_bf16 v[184:187], v[120:123], v[136:139], v[184:187]\n\t" \
  "v_mfma_f32_16x16x32_bf16 v[192:195], v[120:123], v[108:111], v[192:195]\n\t" \
  "v_mfma_f32_16x16x32_bf16 v[200:203], v[120:123], v[144:147], v[200:203]\n\t" \
  "v_mfma_f32_16x16x32_bf16 v[208:211], v[120:123], v[140:143], v[208:211]\n\t" \
  "v_mfma_f32_16x16x32_bf16 v[252:255], v[112:115], v[116:119], v[252:255]\n\t" \
  "v_mfma_f32_16x16x32_bf16 v[188:191], v[112:115], v[136:139], v[188:191]\n\t" \
  "v_mfma_f32_16x16x32_bf16 v[196:199], v[112:115], v[108:111], v[196:199]\n\t" \
  "v_cvt_pk_bf16_f32 v10, v248, v249\n\t" \
  "v_cvt_pk_bf16_f32 v11, v250, v251\n\t" \
  "v_mfma_f32_16x16x32_bf16 v[204:207], v[112:115], v[144:147], v[204:207]\n\t" \
  "v_cvt_pk_bf16_f32 v14, v184, v185\n\t" \
  "v_cvt_pk_bf16_f32 v15, v186, v187\n\t" \
  "v_mfma_f32_16x16x32_bf16 v[212:215], v[112:115], v[140:143], v[212:215]\n\t" \
  "v_cvt_pk_bf16_f32 v12, v252, v253\n\t" \
  "v_cvt_pk_bf16_f32 v13, v254, v255\n\t" \
  "v_mfma_f32_16x16x32_bf16 v[152:155], v[132:135], v[136:139], v[152:155]\n\t" \
  "ds_write_b128 v30, v[10:13]\n\t" \
  "ds_write_b32 v32, v0 offset:48\n\t" \
  "v_mfma_f32_16x16x32_bf16 v[160:163], v[132:135], v[108:111], v[160:163]\n\t" \
  "v_cvt_pk_bf16_f32 v16, v188, v189\n\t" \
  "v_cvt_pk_bf16_f32 v17, v190, v191\n\t" \
  "v_mfma_f32_16x16x32_bf16 v[168:171], v[132:135], v[144:147], v[168:171]\n\t" \
  "v_cvt_pk_bf16_f32 v18, v192, v193\n\t" \
  "v_cvt_pk_bf16_f32 v19, v194, v195\n\t" \
  "v_mfma_f32_16x16x32_bf16 v[176:179], v[132:135], v[140:143], v[176:179]\n\t" \
  "v_cvt_pk_bf16_f32 v20, v196, v197\n\t" \
  "v_cvt_pk_bf16_f32 v21, v198, v199\n\t" \
  "v_mfma_f32_16x16x32_bf16 v[156:159], v[128:131], v[136:139], v[156:159]\n\t" \
  "v_cvt_pk_bf16_f32 v22, v200, v201\n\t" \
  "v_cvt_pk_bf16_f32 v23, v202, v203\n\t" \
  "v_mfma_f32_16x16x32_bf16 v[164:167], v[128:131], v[108:111], v[164:167]\n\t" \
  "v_cvt_pk_bf16_f32 v24, v204, v205\n\t" \
  "v_cvt_pk_bf16_f32 v25, v206, v207\n\t" \
  "v_mfma_f32_16x16x32_bf16 v[172:175], v[128:131], v[144:147], v[172:175]\n\t" \
  "v_cvt_pk_bf16_f32 v26, v208, v209\n\t" \
  "v_cvt_pk_bf16_f32 v27, v210, v211\n\t" \
  "v_mfma_f32_16x16x32_bf16 v[180:183], v[128:131], v[140:143], v[180:183]\n\t" \
  "v_cvt_pk_bf16_f32 v28, v212, v213\n\t" \
  "v_cvt_pk_bf16_f32 v29, v214, v215\n\t" \
  "v_mfma_f32_16x16x32_bf16 v[216:219], v[136:139], v[124:127], v[216:219]\n\t" \
  "v_cvt_pk_bf16_f32 v84, v152, v153\n\t" \
  "v_cvt_pk_bf16_f32 v85, v154, v155\n\t" \
  "v_mfma_f32_16x16x32_bf16 v[224:227], v[108:111], v[124:127], v[224:227]\n\t" \
  "v_cvt_pk_bf16_f32 v86, v156, v157\n\t" \
  "v_cvt_pk_bf16_f32 v87, v158, v159\n\t" \
  "v_mfma_f32_16x16x32_bf16 v[232:235], v[144:147], v[124:127], v[232:235]\n\t" \
  "v_cvt_pk_bf16_f32 v88, v160, v161\n\t" \
  "v_cvt_pk_bf16_f32 v89, v162, v163\n\t" \
  "v_mfma_f32_16x16x32_bf16 v[240:243], v[140:143], v[124:127], v[240:243]\n\t" \
  "v_cvt_pk_bf16_f32 v90, v164, v165\n\t" \
  "v_cvt_pk_bf16_f32 v91, v166, v167\n\t" \
  "v_mfma_f32_16x16x32_bf16 v[220:223], v[136:139], v[148:151], v[220:223]\n\t" \
  "v_cvt_pk_bf16_f32 v92, v168, v169\n\t" \
  "v_cvt_pk_bf16_f32 v93, v170, v171\n\t" \
  "v_mfma_f32_16x16x32_bf16 v[228:231], v[108:111], v[148:151], v[228:231]\n\t" \
  "v_cvt_pk_bf16_f32 v94, v172, v173\n\t" \
  "v_cvt_pk_bf16_f32 v95, v174, v175\n\t" \
  "v_mfma_f32_16x16x32_bf16 v[236:239], v[144:147], v[148:151], v[236:239]\n\t" \
  "v_cvt_pk_bf16_f32 v96, v176, v177\n\t" \
  "v_cvt_pk_bf16_f32 v97, v178, v179\n\t" \
  "v_mfma_f32_16x16x32_bf16 v[244:247], v[140:143], v[148:151], v[244:247]\n\t" \
  "v_cvt_pk_bf16_f32 v98, v180, v181\n\t" \
  "v_cvt_pk_bf16_f32 v99, v182, v183\n\t" \
  "v_mfma_f32_16x16x32_bf16 v[200:203], v[22:25], v[92:95], v[34:37]\n\t" \
  "v_cvt_pk_bf16_f32 v66, v232, v233\n\t" \
  "v_cvt_pk_bf16_f32 v67, v234, v235\n\t" \
  "v_mfma_f32_16x16x32_bf16 v[208:211], v[26:29], v[96:99], v[34:37]\n\t" \
  "v_cvt_pk_bf16_f32 v76, v236, v237\n\t" \
  "v_cvt_pk_bf16_f32 v77, v238, v239\n\t" \
  "v_mfma_f32_16x16x32_bf16 v[212:215], v[22:25], v[96:99], v[38:41]\n\t" \
  "v_cvt_pk_bf16_f32 v64, v240, v241\n\t" \
  "v_cvt_pk_bf16_f32 v65, v242, v243\n\t" \
  "v_mfma_f32_16x16x32_bf16 v[204:207], v[18:21], v[92:95], v[38:41]\n\t" \
  "v_cvt_pk_bf16_f32 v74, v244, v245\n\t" \
  "v_cvt_pk_bf16_f32 v75, v246, v247\n\t" \
  "v_mfma_f32_16x16x32_bf16 v[192:195], v[18:21], v[88:91], v[34:37]\n\t" \
  "v_cvt_pk_bf16_f32 v68, v224, v225\n\t" \
  "v_cvt_pk_bf16_f32 v69, v226, v227\n\t" \
  "v_mfma_f32_16x16x32_bf16 v[196:199], v[14:17], v[88:91], v[38:41]\n\t" \
  "v_max_f32 v208, v208, v212\n\t" \
  "v_max_f32 v209, v209, v213\n\t" \
  "v_mfma_f32_16x16x32_bf16 v[184:187], v[14:17], v[84:87], v[34:37]\n\t" \
  "v_max_f32 v210, v210, v214\n\t" \
  "v_max_f32 v200, v200, v204\n\t" \
  "v_mfma_f32_16x16x32_bf16 v[188:191], v[10:13], v[84:87], v[42:45]\n\t" \
  "v_max_f32 v201, v201, v205\n\t" \
  "v_max_f32 v202, v202, v206\n\t" \
  "v_max_f32 v192, v192, v196\n\t" \
  "v_max_f32 v193, v193, v197\n\t" \
  "v_max_f32 v194, v194, v198\n\t" \
  "v_max_f32 v211, v211, v215\n\t" \
  "ds_read_b32 v52, v33 offset:48\n\t" \
  "ds_read_b64 v[72:73], v31\n\t" \
  "ds_read_b64 v[82:83], v31 offset:8\n\t" \
  "v_max3_f32 v106, v208, v209, v210\n\t" \
  "v_max_f32 v184, v184, v188\n\t" \
  "v_max_f32 v185, v185, v189\n\t" \
  "v_max_f32 v186, v186, v190\n\t" \
  "v_max_f32 v203, v203, v207\n\t" \
  "v_max3_f32 v104, v200, v201, v202\n\t" \
  "v_max_f32 v195, v195, v199\n\t" \
  "v_max3_f32 v102, v192, v193, v194\n\t" \
  "v_max_f32 v187, v187, v191\n\t" \
  "v_max3_f32 v100, v184, v185, v186\n\t" \
  "v_max_f32 v106, v106, v211\n\t" \
  "v_max_f32 v104, v104, v203\n\t" \
  "v_max_f32 v102, v102, v195\n\t" \
  "v_max_f32 v100, v100, v187\n\t" \
  "v_permlane16_swap_b32 v104, v106\n\t" \
  "v_cvt_pk_bf16_f32 v78, v228, v229\n\t" \
  "v_permlane16_swap_b32 v100, v102\n\t" \
  "v_max_f32 v104, v104, v106\n\t" \
  "v_max_f32 v100, v100, v102\n\t" \
  "v_cvt_pk_bf16_f32 v79, v230, v231\n\t" \
  "v_cvt_pk_bf16_f32 v70, v216, v217\n\t" \
  "v_permlane32_swap_b32 v100, v104\n\t" \
  "v_cvt_pk_bf16_f32 v71, v218, v219\n\t" \
  "v_max_f32 v100, v100, v104\n\t" \
  "v_mul_f32 v100, 0xbe8293ee, v100\n\t" \
  "v_mov_b32 v104, v100\n\t" \
  "v_cvt_pk_bf16_f32 v80, v220, v221\n\t" \
  "v_cvt_pk_bf16_f32 v81, v222, v223\n\t" \
  "v_permlane32_swap_b32 v100, v104\n\t" \
  "s_waitcnt lgkmcnt(0)\n\t" \
  "v_readfirstlane_b32 vcc_lo, v52\n\t" \
  "s_nop 1\n\t" \
  "s_cmp_eq_u32 vcc_lo, 0\n\t" \
  "s_cbranch_scc1 5f\n\t" \
  "4:\n\t" \
  "v_mov_b32 v106, v104\n\t" \
  "v_mov_b32 v102, v100\n\t" \
  "s_nop 0\n\t" \
  "v_permlane16_swap_b32 v104, v106\n\t" \
  "v_permlane16_swap_b32 v100, v102\n\t" \
  "v_fma_f32 v208, v208, %16, v106\n\t" \
  "v_fma_f32 v209, v209, %16, v106\n\t" \
  "v_fma_f32 v210, v210, %16, v106\n\t" \
  "v_fma_f32 v211, v211, %16, v106\n\t" \
  "v_exp_f32 v208, v208\n\t" \
  "v_exp_f32 v209, v209\n\t" \
  "v_exp_f32 v210, v210\n\t" \
  "v_exp_f32 v211, v211\n\t" \
  "v_cvt_pk_bf16_f32 v212, v208, v209\n\t" \
  "v_cvt_pk_bf16_f32 v213, v210, v211\n\t" \
  "v_bfi_b32 v214, v46, 0, v212\n\t" \
  "v_bfi_b32 v215, v47, 0, v213\n\t" \
  "v_and_b32 v212, v46, v212\n\t" \
  "v_and_b32 v213, v47, v213\n\t" \
  "v_fma_f32 v200, v200, %16, v104\n\t" \
  "v_fma_f32 v201, v201, %16, v104\n\t" \
  "v_mfma_f32_16x16x32_bf16 v[208:211], v[48:51], v[212:215], 0\n\t" \
  "v_fma_f32 v202, v202, %16, v104\n\t" \
  "v_fma_f32 v203, v203, %16, v104\n\t" \
  "v_mfma_f32_16x16x32_bf16 v[176:179], v[64:67], v[212:215], 0\n\t" \
  "v_exp_f32 v200, v200\n\t" \
  "v_mfma_f32_16x16x32_bf16 v[180:183], v[74:77], v[212:215], 0\n\t" \
  "v_exp_f32 v201, v201\n\t" \
  "v_exp_f32 v202, v202\n\t" \
  "v_exp_f32 v203, v203\n\t" \
  "v_cvt_pk_bf16_f32 v204, v200, v201\n\t" \
  "v_cvt_pk_bf16_f32 v205, v202, v203\n\t" \
  "v_bfi_b32 v206, v46, 0, v204\n\t" \
  "v_bfi_b32 v207, v47, 0, v205\n\t" \
  "v_rcp_f32 v106, v208\n\t" \
  "v_and_b32 v204, v46, v204\n\t" \
  "v_and_b32 v205, v47, v205\n\t" \
  "v_pk_mul_f32 v[176:177], v[176:177], v[106:107] op_sel_hi:[1,0]\n\t" \
  "v_pk_mul_f32 v[178:179], v[178:179], v[106:107] op_sel_hi:[1,0]\n\t" \
  "v_mfma_f32_16x16x32_bf16 v[200:203], v[48:51], v[204:207], 0\n\t" \
  "v_pk_mul_f32 v[180:181], v[180:181], v[106:107] op_sel_hi:[1,0]\n\t" \
  "v_pk_mul_f32 v[182:183], v[182:183], v[106:107] op_sel_hi:[1,0]\n\t" \
  "v_mfma_f32_16x16x32_bf16 v[168:171], v[66:69], v[204:207], 0\n\t" \
  "v_cvt_pk_bf16_f32 v240, v176, v177\n\t" \
  "v_cvt_pk_bf16_f32 v241, v178, v179\n\t" \
  "v_mfma_f32_16x16x32_bf16 v[172:175], v[76:79], v[204:207], 0\n\t" \
  "v_cvt_pk_bf16_f32 v242, v180, v181\n\t" \
  "v_cvt_pk_bf16_f32 v243, v182, v183\n\t" \
  "global_store_dwordx4 v1, v[240:243], %14 offset:0 sc1\n\t" \
  "v_fma_f32 v192, v192, %16, v102\n\t" \
  "v_fma_f32 v193, v193, %16, v102\n\t" \
  "v_fma_f32 v194, v194, %16, v102\n\t" \
  "v_fma_f32 v195, v195, %16, v102\n\t" \
  "v_exp_f32 v192, v192\n\t" \
  "v_exp_f32 v193, v193\n\t" \
  "v_exp_f32 v194, v194\n\t" \
  "v_exp_f32 v195, v195\n\t" \
  "v_cvt_pk_bf16_f32 v196, v192, v193\n\t" \
  "v_cvt_pk_bf16_f32 v197, v194, v195\n\t" \
  "v_bfi_b32 v198, v46, 0, v196\n\t" \
  "v_bfi_b32 v199, v47, 0, v197\n\t" \
  "v_rcp_f32 v104, v200\n\t" \
  "v_and_b32 v196, v46, v196\n\t" \
  "v_and_b32 v197, v47, v197\n\t" \
  "v_pk_mul_f32 v[168:169], v[168:169], v[104:105] op_sel_hi:[1,0]\n\t" \
  "v_pk_mul_f32 v[170:171], v[170:171], v[104:105] op_sel_hi:[1,0]\n\t" \
  "v_mfma_f32_16x16x32_bf16 v[192:195], v[48:51], v[196:199], 0\n\t" \
  "v_pk_mul_f32 v[172:173], v[172:173], v[104:105] op_sel_hi:[1,0]\n\t" \
  "v_pk_mul_f32 v[174:175], v[174:175], v[104:105] op_sel_hi:[1,0]\n\t" \
  "v_mfma_f32_16x16x32_bf16 v[160:163], v[68:71], v[196:199], 0\n\t" \
  "v_cvt_pk_bf16_f32 v232, v168, v169\n\t" \
  "v_cvt_pk_bf16_f32 v233, v170, v171\n\t" \
  "v_mfma_f32_16x16x32_bf16 v[164:167], v[78:81], v[196:199], 0\n\t" \
  "v_cvt_pk_bf16_f32 v234, v172, v173\n\t" \
  "v_cvt_pk_bf16_f32 v235, v174, v175\n\t" \
  "global_store_dwordx4 v1, v[232:235], %14 offset:-4096 sc1\n\t" \
  "v_fma_f32 v184, v184, %16, v100\n\t" \
  "v_fma_f32 v185, v185, %16, v100\n\t" \
  "v_fma_f32 v186, v186, %16, v100\n\t" \
  "v_fma_f32 v187, v187, %16, v100\n\t" \
  "v_exp_f32 v184, v184\n\t" \
  "v_exp_f32 v185, v185\n\t" \
  "v_exp_f32 v186, v186\n\t" \
  "v_exp_f32 v187, v187\n\t" \
  "v_cvt_pk_bf16_f32 v188, v184, v185\n\t" \
  "v_cvt_pk_bf16_f32 v189, v186, v187\n\t" \
  "v_bfi_b32 v190, v46, 0, v188\n\t" \
  "v_bfi_b32 v191, v47, 0, v189\n\t" \
  "v_rcp_f32 v102, v192\n\t" \
  "v_and_b32 v188, v46, v188\n\t" \
  "v_and_b32 v189, v47, v189\n\t" \
  "v_pk_mul_f32 v[160:161], v[160:161], v[102:103] op_sel_hi:[1,0]\n\t" \
  "v_pk_mul_f32 v[162:163], v[162:163], v[102:103] op_sel_hi:[1,0]\n\t" \
  "v_mfma_f32_16x16x32_bf16 v[184:187], v[48:51], v[188:191], 0\n\t" \
  "v_pk_mul_f32 v[164:165], v[164:165], v[102:103] op_sel_hi:[1,0]\n\t" \
  "v_pk_mul_f32 v[166:167], v[166:167], v[102:103] op_sel_hi:[1,0]\n\t" \
  "v_mfma_f32_16x16x32_bf16 v[152:155], v[70:73], v[188:191], 0\n\t" \
  "v_cvt_pk_bf16_f32 v224, v160, v161\n\t" \
  "v_cvt_pk_bf16_f32 v225, v162, v163\n\t" \
  "v_mfma_f32_16x16x32_bf16 v[156:159], v[80:83], v[188:191], 0\n\t" \
  "v_cvt_pk_bf16_f32 v226, v164, v165\n\t" \
  "v_cvt_pk_bf16_f32 v227, v166, v167\n\t" \
  "global_store_dwordx4 v4, v[224:227], %14 offset:0 sc1\n\t" \
  "v_rcp_f32 v100, v184\n\t" \
  "ds_read_b128 v[60:63], v56 offset:64\n\t" \
  "ds_read_b64_tr_b16 v[108:109], v5 offset:0\n\t" \
  "ds_read_b64_tr_b16 v[110:111], v5 offset:16\n\t" \
  "ds_read_b128 v[112:115], v6 offset:16384\n\t" \
  "ds_read_b128 v[116:119], v6 offset:15360\n\t" \
  "ds_read_b64_tr_b16 v[120:121], v5 offset:512\n\t" \
  "ds_read_b64_tr_b16 v[122:123], v5 offset:528\n\t" \
  "ds_read_b128 v[124:127], v6 offset:14336\n\t" \
  "ds_read_b64_tr_b16 v[128:129], v5 offset:1024\n\t" \
  "ds_read_b64_tr_b16 v[130:131], v5 offset:1040\n\t" \
  "ds_read_b128 v[132:135], v6 offset:13312\n\t" \
  "ds_read_b64_tr_b16 v[136:137], v5 offset:1536\n\t" \
  "ds_read_b64_tr_b16 v[138:139], v5 offset:1552\n\t" \
  "ds_read_b128 v[140:143], v6 offset:12288\n\t" \
  "ds_read_b64_tr_b16 v[144:145], v5 offset:2048\n\t" \
  "v_pk_mul_f32 v[152:153], v[152:153], v[100:101] op_sel_hi:[1,0]\n\t" \
  "v_pk_mul_f32 v[154:155], v[154:155], v[100:101] op_sel_hi:[1,0]\n\t" \
  "v_cvt_pk_bf16_f32 v216, v152, v153\n\t" \
  "v_pk_mul_f32 v[156:157], v[156:157], v[100:101] op_sel_hi:[1,0]\n\t" \
  "v_pk_mul_f32 v[158:159], v[158:159], v[100:101] op_sel_hi:[1,0]\n\t" \
  "v_cvt_pk_bf16_f32 v217, v154, v155\n\t" \
  "506:\n\t" \
  "s_cbranch_execz 510f\n\t" \
  "v_cvt_pk_bf16_f32 v218, v156, v157\n\t" \
  "v_cvt_pk_bf16_f32 v219, v158, v159\n\t" \
  "global_store_dwordx4 v4, v[216:219], %14 offset:-4096 sc1\n\t" \
  "ds_write_b32 v32, v0 offset:112\n\t" \
  "s_branch 9f\n\t" \
  "5:\n\t" \
  "ds_read_b32 v52, v33 offset:48\n\t" \
  "ds_read_b64 v[72:73], v31\n\t" \
  "ds_read_b64 v[82:83], v31 offset:8\n\t" \
  "s_waitcnt lgkmcnt(0)\n\t" \
  "v_readfirstlane_b32 vcc_lo, v52\n\t" \
  "s_nop 1\n\t" \
  "s_cmp_eq_u32 vcc_lo, 0\n\t" \
  "s_cbranch_scc1 5b\n\t" \
  "s_branch 4b\n\t" \
  "15:\n\t" \
  "ds_read_b128 v[10:13], v14 offset:128\n\t" \
  "s_waitcnt lgkmcnt(0)\n\t" \
  "v_and_b32 v10, v10, v11\n\t" \
  "v_and_b32 v12, v12, v13\n\t" \
  "v_and_b32 v10, v10, v12\n\t" \
  "s_nop 1\n\t" \
  "v_readfirstlane_b32 vcc_lo, v10\n\t" \
  "s_nop 1\n\t" \
  "s_cmp_eq_u32 vcc_lo, 0\n\t" \
  "s_cbranch_scc1 15b\n\t" \
  "s_branch 14b\n\t" \
  "9:\n\t" \
  "507:\n\t" \
  "s_cbranch_execz 511f\n\t" \
  "s_waitcnt lgkmcnt(14)\n\t" \
  "v_and_b32 v60, v60, v61\n\t" \
  "v_and_b32 v62, v62, v63\n\t" \
  "v_mov_b32 v12, %13\n\t" \
  "v_and_b32 v60, v60, v62\n\t" \
  "s_nop 0\n\t" \
  "v_readfirstlane_b32 vcc_lo, v60\n\t" \
  "s_nop 1\n\t" \
  "s_cmp_eq_u32 vcc_lo, 0\n\t" \
  "s_cbranch_scc1 7f\n\t" \
  "6:\n\t" \
  "s_waitcnt lgkmcnt(11)\n\t" \
  "v_mfma_f32_16x16x32_bf16 v[192:195], v[108:111], v[112:115], 0\n\t" \
  "ds_read_b64_tr_b16 v[146:147], v5 offset:2064\n\t" \
  "ds_read_b128 v[148:151], v6 offset:11264\n\t" \
  "s_waitcnt lgkmcnt(12)\n\t" \
  "v_mfma_f32_16x16x32_bf16 v[196:199], v[108:111], v[116:119], 0\n\t" \
  "ds_read_b64_tr_b16 v[64:65], v5 offset:2560\n\t" \
  "ds_read_b64_tr_b16 v[66:67], v5 offset:2576\n\t" \
  "s_waitcnt lgkmcnt(12)\n\t" \
  "v_mfma_f32_16x16x32_bf16 v[196:199], v[120:123], v[112:115], v[196:199]\n\t" \
  "ds_read_b128 v[68:71], v6 offset:10240\n\t" \
  "ds_read_b64_tr_b16 v[72:73], v5 offset:3072\n\t" \
  "s_waitcnt lgkmcnt(13)\n\t" \
  "v_mfma_f32_16x16x32_bf16 v[200:203], v[108:111], v[124:127], 0\n\t" \
  "ds_read_b64_tr_b16 v[74:75], v5 offset:3088\n\t" \
  "ds_read_b128 v[76:79], v6 offset:9216\n\t" \
  "v_mfma_f32_16x16x32_bf16 v[200:203], v[120:123], v[116:119], v[200:203]\n\t" \
  "ds_read_b64_tr_b16 v[80:81], v5 offset:3584\n\t" \
  "ds_read_b64_tr_b16 v[82:83], v5 offset:3600\n\t" \
  "s_waitcnt lgkmcnt(15)\n\t" \
  "v_mfma_f32_16x16x32_bf16 v[200:203], v[128:131], v[112:115], v[200:203]\n\t" \
  "ds_read_b128 v[84:87], v6 offset:0\n\t" \
  "ds_read_b128 v[88:91], v6 offset:1024\n\t" \
  "s_waitcnt lgkmcnt(15)\n\t" \
  "v_mfma_f32_16x16x32_bf16 v[204:207], v[108:111], v[132:135], 0\n\t" \
  "ds_read_b128 v[92:95], v6 offset:2048\n\t" \
  "ds_read_b128 v[96:99], v6 offset:3072\n\t" \
  "v_mfma_f32_16x16x32_bf16 v[204:207], v[120:123], v[124:127], v[204:207]\n\t" \
  "ds_read_b128 v[100:103], v6 offset:4096\n\t" \
  "ds_read_b128 v[104:107], v6 offset:5120\n\t" \
  "v_mfma_f32_16x16x32_bf16 v[204:207], v[128:131], v[116:119], v[204:207]\n\t" \
  "ds_read_b128 v[152:155], v6 offset:6144\n\t" \
  "ds_read_b128 v[156:159], v6 offset:7168\n\t" \
  "s_waitcnt lgkmcnt(14)\n\t" \
  "v_mfma_f32_16x16x32_bf16 v[204:207], v[136:139], v[112:115], v[204:207]\n\t" \
  "v_lshl_add_u32 v12, v12, 12, v4\n\t" \
  "s_lshl_b32 s88, %13, 5\n\t" \
  "v_mfma_f32_16x16x32_bf16 v[208:211], v[108:111], v[140:143], 0\n\t" \
  "v_mfma_f32_16x16x32_bf16 v[208:211], v[120:123], v[132:135], v[208:211]\n\t" \
  "v_mfma_f32_16x16x32_bf16 v[208:211], v[128:131], v[124:127], v[208:211]\n\t" \
  "s_and_b32 s88, s88, 64\n\t" \
  "v_xor_b32 v13, s88, v54\n\t" \
  "v_mfma_f32_16x16x32_bf16 v[208:211], v[136:139], v[116:119], v[208:211]\n\t" \
  "v_mfma_f32_16x16x32_bf16 v[208:211], v[144:147], v[112:115], v[208:211]\n\t" \
  "v_mfma_f32_16x16x32_bf16 v[212:215], v[108:111], v[148:151], 0\n\t" \
  "ds_read_b32 v55, v56 offset:104\n\t" \
  "v_mfma_f32_16x16x32_bf16 v[212:215], v[120:123], v[140:143], v[212:215]\n\t" \
  "v_mfma_f32_16x16x32_bf16 v[212:215], v[128:131], v[132:135], v[212:215]\n\t" \
  "v_bfe_u32 v11, v12, 4, 4\n\t" \
  "s_mov_b32 s74, 0\n\t" \
  "v_mfma_f32_16x16x32_bf16 v[212:215], v[136:139], v[124:127], v[212:215]\n\t" \
  "v_lshlrev_b32 v11, 7, v11\n\t" \
  "v_mfma_f32_16x16x32_bf16 v[212:215], v[144:147], v[116:119], v[212:215]\n\t" \
  "v_mov_b32 v10, 1\n\t" \
  "v_mfma_f32_16x16x32_bf16 v[212:215], v[64:67], v[112:115], v[212:215]\n\t" \
  "s_getpc_b64 s[70:71]\n\t" \
  "24:\n\t" \
  "s_add_u32 s70, s70, 23f-24b\n\t" \
  "s_addc_u32 s71, s71, 0\n\t" \
  "s_waitcnt lgkmcnt(1)\n\t" \
  "v_mfma_f32_16x16x32_bf16 v[216:219], v[108:111], v[68:71], 0\n\t" \
  "v_mfma_f32_16x16x32_bf16 v[216:219], v[120:123], v[148:151], v[216:219]\n\t" \
  "v_mfma_f32_16x16x32_bf16 v[216:219], v[128:131], v[140:143], v[216:219]\n\t" \
  "v_mfma_f32_16x16x32_bf16 v[216:219], v[136:139], v[132:135], v[216:219]\n\t" \
  "v_mfma_f32_16x16x32_bf16 v[216:219], v[144:147], v[124:127], v[216:219]\n\t" \
  "v_mfma_f32_16x16x32_bf16 v[216:219], v[64:67], v[116:119], v[216:219]\n\t" \
  "v_mfma_f32_16x16x32_bf16 v[216:219], v[72:75], v[112:115], v[216:219]\n\t" \
  "v_mfma_f32_16x16x32_bf16 v[220:223], v[108:111], v[76:79], 0\n\t" \
  "v_mfma_f32_16x16x32_bf16 v[220:223], v[120:123], v[68:71], v[220:223]\n\t" \
  "v_mfma_f32_16x16x32_bf16 v[220:223], v[128:131], v[148:151], v[220:223]\n\t" \
  "v_mfma_f32_16x16x32_bf16 v[220:223], v[136:139], v[140:143], v[220:223]\n\t" \
  "v_mfma_f32_16x16x32_bf16 v[220:223], v[144:147], v[132:135], v[220:223]\n\t" \
  "ds_read_b64_tr_b16 v[160:161], v5 offset:128\n\t" \
  "v_mfma_f32_16x16x32_bf16 v[220:223], v[64:67], v[124:127], v[220:223]\n\t" \
  "ds_read_b64_tr_b16 v[162:163], v5 offset:144\n\t" \
  "v_mfma_f32_16x16x32_bf16 v[220:223], v[72:75], v[116:119], v[220:223]\n\t" \
  "ds_read_b128 v[164:167], v7 offset:16384\n\t" \
  "v_mfma_f32_16x16x32_bf16 v[220:223], v[80:83], v[112:115], v[220:223]\n\t" \
  "ds_read_b128 v[168:171], v7 offset:15360\n\t" \
  "v_mfma_f32_16x16x32_bf16 v[252:255], v[108:111], v[84:87], 0\n\t" \
  "ds_read_b64_tr_b16 v[172:173], v5 offset:640\n\t" \
  "v_mfma_f32_16x16x32_bf16 v[252:255], v[120:123], v[88:91], v[252:255]\n\t" \
  "ds_read_b64_tr_b16 v[174:175], v5 offset:656\n\t" \
  "v_mfma_f32_16x16x32_bf16 v[252:255], v[128:131], v[92:95], v[252:255]\n\t" \
  "ds_read_b128 v[176:179], v7 offset:14336\n\t" \
  "v_mfma_f32_16x16x32_bf16 v[252:255], v[136:139], v[96:99], v[252:255]\n\t" \
  "ds_read_b64_tr_b16 v[180:181], v5 offset:1152\n\t" \
  "v_mfma_f32_16x16x32_bf16 v[252:255], v[144:147], v[100:103], v[252:255]\n\t" \
  "ds_read_b64_tr_b16 v[182:183], v5 offset:1168\n\t" \
  "v_mfma_f32_16x16x32_bf16 v[252:255], v[64:67], v[104:107], v[252:255]\n\t" \
  "ds_read_b128 v[184:187], v7 offset:13312\n\t" \
  "v_mfma_f32_16x16x32_bf16 v[252:255], v[72:75], v[152:155], v[252:255]\n\t" \
  "ds_read_b64_tr_b16 v[188:189], v5 offset:1664\n\t" \
  "v_mfma_f32_16x16x32_bf16 v[252:255], v[80:83], v[156:159], v[252:255]\n\t" \
  "ds_read_b64_tr_b16 v[190:191], v5 offset:1680\n\t" \
  "v_mfma_f32_16x16x32_bf16 v[248:251], v[108:111], v[88:91], 0\n\t" \
  "ds_read_b128 v[76:79], v7 offset:12288\n\t" \
  "v_mfma_f32_16x16x32_bf16 v[248:251], v[120:123], v[92:95], v[248:251]\n\t" \
  "v_mfma_f32_16x16x32_bf16 v[248:251], v[128:131], v[96:99], v[248:251]\n\t" \
  "ds_read_b64_tr_b16 v[68:69], v5 offset:2176\n\t" \
  "v_mfma_f32_16x16x32_bf16 v[248:251], v[136:139], v[100:103], v[248:251]\n\t" \
  "ds_read_b64_tr_b16 v[70:71], v5 offset:2192\n\t" \
  "v_mfma_f32_16x16x32_bf16 v[248:251], v[144:147], v[104:107], v[248:251]\n\t" \
  "ds_read_b128 v[148:151], v7 offset:11264\n\t" \
  "v_mfma_f32_16x16x32_bf16 v[248:251], v[64:67], v[152:155], v[248:251]\n\t" \
  "v_mfma_f32_16x16x32_bf16 v[248:251], v[72:75], v[156:159], v[248:251]\n\t" \
  "v_mfma_f32_16x16x32_bf16 v[244:247], v[108:111], v[92:95], 0\n\t" \
  "v_mfma_f32_16x16x32_bf16 v[244:247], v[120:123], v[96:99], v[244:247]\n\t" \
  "ds_read_b64_tr_b16 v[140:141], v5 offset:2688\n\t" \
  "v_mfma_f32_16x16x32_bf16 v[244:247], v[128:131], v[100:103], v[244:247]\n\t" \
  "ds_read_b64_tr_b16 v[142:143], v5 offset:2704\n\t" \
  "v_mfma_f32_16x16x32_bf16 v[244:247], v[136:139], v[104:107], v[244:247]\n\t" \
  "ds_read_b128 v[132:135], v7 offset:10240\n\t" \
  "v_mfma_f32_16x16x32_bf16 v[244:247], v[144:147], v[152:155], v[244:247]\n\t" \
  "v_mfma_f32_16x16x32_bf16 v[244:247], v[64:67], v[156:159], v[244:247]\n\t" \
  "v_mfma_f32_16x16x32_bf16 v[240:243], v[108:111], v[96:99], 0\n\t" \
  "v_mfma_f32_16x16x32_bf16 v[240:243], v[120:123], v[100:103], v[240:243]\n\t" \
  "v_mfma_f32_16x16x32_bf16 v[240:243], v[128:131], v[104:107], v[240:243]\n\t" \
  "ds_read_b64_tr_b16 v[124:125], v5 offset:3200\n\t" \
  "v_mfma_f32_16x16x32_bf16 v[240:243], v[136:139], v[152:155], v[240:243]\n\t" \
  "ds_read_b64_tr_b16 v[126:127], v5 offset:3216\n\t" \
  "v_mfma_f32_16x16x32_bf16 v[240:243], v[144:147], v[156:159], v[240:243]\n\t" \
  "ds_read_b128 v[116:119], v7 offset:9216\n\t" \
  "v_mfma_f32_16x16x32_bf16 v[236:239], v[108:111], v[100:103], 0\n\t" \
  "v_mfma_f32_16x16x32_bf16 v[236:239], v[120:123], v[104:107], v[236:239]\n\t" \
  "s_getreg_b32 vcc_lo, hwreg(HW_REG_IB_STS, 0, 4)\n\t" \
  "v_mfma_f32_16x16x32_bf16 v[236:239], v[128:131], v[152:155], v[236:239]\n\t" \
  "s_or_b32 vcc_lo, vcc_lo, s74\n\t" \
  "s_cbranch_scc1 30f\n\t" \
  "s_swappc_b64 s[68:69], s[70:71]\n\t" \
  "30:\n\t" \
  "v_mfma_f32_16x16x32_bf16 v[236:239], v[136:139], v[156:159], v[236:239]\n\t" \
  "v_mfma_f32_16x16x32_bf16 v[232:235], v[108:111], v[104:107], 0\n\t" \
  "v_mfma_f32_16x16x32_bf16 v[232:235], v[120:123], v[152:155], v[232:235]\n\t" \
  "ds_read_b64_tr_b16 v[64:65], v5 offset:3712\n\t" \
  "v_mfma_f32_16x16x32_bf16 v[232:235], v[128:131], v[156:159], v[232:235]\n\t" \
  "ds_read_b64_tr_b16 v[66:67], v5 offset:3728\n\t" \
  "v_mfma_f32_16x16x32_bf16 v[228:231], v[108:111], v[152:155], 0\n\t" \
  "ds_read_b128 v[72:75], v7 offset:0\n\t" \
  "v_mfma_f32_16x16x32_bf16 v[228:231], v[120:123], v[156:159], v[228:231]\n\t" \
  "ds_read_b128 v[80:83], v7 offset:1024\n\t" \
  "v_mfma_f32_16x16x32_bf16 v[224:227], v[108:111], v[156:159], 0\n\t" \
  "ds_read_b128 v[84:87], v7 offset:2048\n\t" \
  "s_waitcnt lgkmcnt(5)\n\t" \
  "v_mfma_f32_16x16x32_bf16 v[192:195], v[160:163], v[164:167], v[192:195]\n\t" \
  "ds_read_b128 v[88:91], v7 offset:3072\n\t" \
  "v_mfma_f32_16x16x32_bf16 v[196:199], v[160:163], v[168:171], v[196:199]\n\t" \
  "ds_read_b128 v[92:95], v7 offset:4096\n\t" \
  "v_mfma_f32_16x16x32_bf16 v[196:199], v[172:175], v[164:167], v[196:199]\n\t" \
  "ds_read_b128 v[108:111], v7 offset:5120\n\t" \
  "v_mfma_f32_16x16x32_bf16 v[200:203], v[160:163], v[176:179], v[200:203]\n\t" \
  "ds_read_b128 v[112:115], v7 offset:6144\n\t" \
  "v_mfma_f32_16x16x32_bf16 v[200:203], v[172:175], v[168:171], v[200:203]\n\t" \
  "ds_read_b128 v[120:123], v7 offset:7168\n\t" \
  "v_mfma_f32_16x16x32_bf16 v[200:203], v[180:183], v[164:167], v[200:203]\n\t" \
  "v_mfma_f32_16x16x32_bf16 v[204:207], v[160:163], v[184:187], v[204:207]\n\t" \
  "v_mfma_f32_16x16x32_bf16 v[204:207], v[172:175], v[176:179], v[204:207]\n\t" \
  "v_mfma_f32_16x16x32_bf16 v[204:207], v[180:183], v[168:171], v[204:207]\n\t" \
  "v_mfma_f32_16x16x32_bf16 v[204:207], v[188:191], v[164:167], v[204:207]\n\t" \
  "v_mfma_f32_16x16x32_bf16 v[208:211], v[160:163], v[76:79], v[208:211]\n\t" \
  "v_mfma_f32_16x16x32_bf16 v[208:211], v[172:175], v[184:187], v[208:211]\n\t" \
  "s_getreg_b32 vcc_lo, hwreg(HW_REG_IB_STS, 0, 4)\n\t" \
  "v_mfma_f32_16x16x32_bf16 v[208:211], v[180:183], v[176:179], v[208:211]\n\t" \
  "s_or_b32 vcc_lo, vcc_lo, s74\n\t" \
  "s_cbranch_scc1 31f\n\t" \
  "s_swappc_b64 s[68:69], s[70:71]\n\t" \
  "31:\n\t" \
  "v_mfma_f32_16x16x32_bf16 v[208:211], v[188:191], v[168:171], v[208:211]\n\t" \
  "v_mfma_f32_16x16x32_bf16 v[208:211], v[68:71], v[164:167], v[208:211]\n\t" \
  "v_mfma_f32_16x16x32_bf16 v[212:215], v[160:163], v[148:151], v[212:215]\n\t" \
  "v_mfma_f32_16x16x32_bf16 v[212:215], v[172:175], v[76:79], v[212:215]\n\t" \
  "v_mfma_f32_16x16x32_bf16 v[212:215], v[180:183], v[184:187], v[212:215]\n\t" \
  "v_mfma_f32_16x16x32_bf16 v[212:215], v[188:191], v[176:179], v[212:215]\n\t" \
  "v_mfma_f32_16x16x32_bf16 v[212:215], v[68:71], v[168:171], v[212:215]\n\t" \
  "v_mfma_f32_16x16x32_bf16 v[212:215], v[140:143], v[164:167], v[212:215]\n\t" \
  "v_mfma_f32_16x16x32_bf16 v[216:219], v[160:163], v[132:135], v[216:219]\n\t" \
  "v_mfma_f32_16x16x32_bf16 v[216:219], v[172:175], v[148:151], v[216:219]\n\t" \
  "v_mfma_f32_16x16x32_bf16 v[216:219], v[180:183], v[76:79], v[216:219]\n\t" \
  "v_mfma_f32_16x16x32_bf16 v[216:219], v[188:191], v[184:187], v[216:219]\n\t" \
  "v_mfma_f32_16x16x32_bf16 v[216:219], v[68:71], v[176:179], v[216:219]\n\t" \
  "v_mfma_f32_16x16x32_bf16 v[216:219], v[140:143], v[168:171], v[216:219]\n\t" \
  "v_mfma_f32_16x16x32_bf16 v[216:219], v[124:127], v[164:167], v[216:219]\n\t" \
  "v_mfma_f32_16x16x32_bf16 v[220:223], v[160:163], v[116:119], v[220:223]\n\t" \
  "v_mfma_f32_16x16x32_bf16 v[220:223], v[172:175], v[132:135], v[220:223]\n\t" \
  "v_mfma_f32_16x16x32_bf16 v[220:223], v[180:183], v[148:151], v[220:223]\n\t" \
  "v_mfma_f32_16x16x32_bf16 v[220:223], v[188:191], v[76:79], v[220:223]\n\t" \
  "v_mfma_f32_16x16x32_bf16 v[220:223], v[68:71], v[184:187], v[220:223]\n\t" \
  "s_cmp_lg_u32 s74, 0\n\t" \
  "s_cbranch_scc1 32f\n\t" \
  "s_swappc_b64 s[68:69], s[70:71]\n\t" \
  "32:\n\t" \
  "v_mfma_f32_16x16x32_bf16 v[220:223], v[140:143], v[176:179], v[220:223]\n\t" \
  "ds_read_b64_tr_b16 v[76:77], v5 offset:256\n\t" \
  "v_mfma_f32_16x16x32_bf16 v[220:223], v[124:127], v[168:171], v[220:223]\n\t" \
  "ds_read_b64_tr_b16 v[78:79], v5 offset:272\n\t" \
  "s_waitcnt lgkmcnt(2)\n\t" \
  "v_mfma_f32_16x16x32_bf16 v[220:223], v[64:67], v[164:167], v[220:223]\n\t" \
  "s_getpc_b64 s[70:71]\n\t" \
  "24:\n\t" \
  "s_add_u32 s70, s70, 25f-24b\n\t" \
  "s_addc_u32 s71, s71, 0\n\t" \
  "v_mfma_f32_16x16x32_bf16 v[252:255], v[160:163], v[72:75], v[252:255]\n\t" \
  "ds_read_b128 v[128:131], v8 offset:16384\n\t" \
  "v_mfma_f32_16x16x32_bf16 v[252:255], v[172:175], v[80:83], v[252:255]\n\t" \
  "ds_read_b128 v[136:139], v8 offset:15360\n\t" \
  "v_mfma_f32_16x16x32_bf16 v[252:255], v[180:183], v[84:87], v[252:255]\n\t" \
  "v_lshrrev_b32 v14, 1, v4\n\t" \
  "ds_read_b64_tr_b16 v[144:145], v5 offset:768\n\t" \
  "v_mfma_f32_16x16x32_bf16 v[252:255], v[188:191], v[88:91], v[252:255]\n\t" \
  "ds_read_b64_tr_b16 v[146:147], v5 offset:784\n\t" \
  "v_mfma_f32_16x16x32_bf16 v[252:255], v[68:71], v[92:95], v[252:255]\n\t" \
  "v_lshrrev_b32 v15, 24, v3\n\t" \
  "ds_read_b128 v[96:99], v8 offset:14336\n\t" \
  "v_mfma_f32_16x16x32_bf16 v[252:255], v[140:143], v[108:111], v[252:255]\n\t" \
  "ds_read_b64_tr_b16 v[72:73], v5 offset:1280\n\t" \
  "v_mfma_f32_16x16x32_bf16 v[252:255], v[124:127], v[112:115], v[252:255]\n\t" \
  "v_add_u32 v14, %20, v14\n\t" \
  "ds_read_b64_tr_b16 v[74:75], v5 offset:1296\n\t" \
  "v_mfma_f32_16x16x32_bf16 v[252:255], v[64:67], v[120:123], v[252:255]\n\t" \
  "ds_read_b128 v[100:103], v8 offset:13312\n\t" \
  "v_mfma_f32_16x16x32_bf16 v[248:251], v[160:163], v[80:83], v[248:251]\n\t" \
  "v_lshrrev_b32 v18, 10, v12\n\t" \
  "ds_read_b64_tr_b16 v[104:105], v5 offset:1792\n\t" \
  "v_mfma_f32_16x16x32_bf16 v[248:251], v[172:175], v[84:87], v[248:251]\n\t" \
  "ds_read_b64_tr_b16 v[106:107], v5 offset:1808\n\t" \
  "v_mfma_f32_16x16x32_bf16 v[248:251], v[180:183], v[88:91], v[248:251]\n\t" \
  "v_and_b32 v18, 12, v18\n\t" \
  "ds_read_b128 v[152:155], v8 offset:12288\n\t" \
  "v_mfma_f32_16x16x32_bf16 v[248:251], v[188:191], v[92:95], v[248:251]\n\t" \
  "ds_read_b64_tr_b16 v[64:65], v5 offset:2304\n\t" \
  "v_mfma_f32_16x16x32_bf16 v[248:251], v[68:71], v[108:111], v[248:251]\n\t" \
  "v_mov_b32 v17, 16\n\t" \
  "ds_read_b64_tr_b16 v[66:67], v5 offset:2320\n\t" \
  "v_mfma_f32_16x16x32_bf16 v[248:251], v[140:143], v[112:115], v[248:251]\n\t" \
  "ds_read_b128 v[80:83], v8 offset:11264\n\t" \
  "v_mfma_f32_16x16x32_bf16 v[248:251], v[124:127], v[120:123], v[248:251]\n\t" \
  "v_add_u32 v18, v56, v18\n\t" \
  "v_mfma_f32_16x16x32_bf16 v[244:247], v[160:163], v[84:87], v[244:247]\n\t" \
  "v_mfma_f32_16x16x32_bf16 v[244:247], v[172:175], v[88:91], v[244:247]\n\t" \
  "s_mov_b32 s76, 0\n\t" \
  "ds_read_b64_tr_b16 v[84:85], v5 offset:2816\n\t" \
  "v_mfma_f32_16x16x32_bf16 v[244:247], v[180:183], v[92:95], v[244:247]\n\t" \
  "ds_read_b64_tr_b16 v[86:87], v5 offset:2832\n\t" \
  "v_mfma_f32_16x16x32_bf16 v[244:247], v[188:191], v[108:111], v[244:247]\n\t" \
  "s_brev_b32 s77, 1\n\t" \
  "ds_read_b128 v[156:159], v8 offset:10240\n\t" \
  "v_mfma_f32_16x16x32_bf16 v[244:247], v[68:71], v[112:115], v[244:247]\n\t" \
  "v_mfma_f32_16x16x32_bf16 v[244:247], v[140:143], v[120:123], v[244:247]\n\t" \
  "s_mov_b32 s75, 0\n\t" \
  "v_mfma_f32_16x16x32_bf16 v[240:243], v[160:163], v[88:91], v[240:243]\n\t" \
  "v_mfma_f32_16x16x32_bf16 v[240:243], v[172:175], v[92:95], v[240:243]\n\t" \
  "v_add_u32 v28, 22528, v13\n\t" \
  "v_mfma_f32_16x16x32_bf16 v[240:243], v[180:183], v[108:111], v[240:243]\n\t" \
  "ds_read_b64_tr_b16 v[88:89], v5 offset:3328\n\t" \
  "v_mfma_f32_16x16x32_bf16 v[240:243], v[188:191], v[112:115], v[240:243]\n\t" \
  "v_xor_b32 v29, 32, v28\n\t" \
  "ds_read_b64_tr_b16 v[90:91], v5 offset:3344\n\t" \
  "v_mfma_f32_16x16x32_bf16 v[240:243], v[68:71], v[120:123], v[240:243]\n\t" \
  "ds_read_b128 v[164:167], v8 offset:9216\n\t" \
  "v_mfma_f32_16x16x32_bf16 v[236:239], v[160:163], v[92:95], v[236:239]\n\t" \
  "v_xor_b32 v30, 64, v28\n\t" \
  "v_mfma_f32_16x16x32_bf16 v[236:239], v[172:175], v[108:111], v[236:239]\n\t" \
  "v_mfma_f32_16x16x32_bf16 v[236:239], v[180:183], v[112:115], v[236:239]\n\t" \
  "v_xor_b32 v31, 96, v28\n\t" \
  "v_mfma_f32_16x16x32_bf16 v[236:239], v[188:191], v[120:123], v[236:239]\n\t" \
  "v_mfma_f32_16x16x32_bf16 v[232:235], v[160:163], v[108:111], v[232:235]\n\t" \
  "v_add_u32 v32, 88064, v13\n\t" \
  "v_mfma_f32_16x16x32_bf16 v[232:235], v[172:175], v[112:115], v[232:235]\n\t" \
  "ds_read_b64_tr_b16 v[68:69], v5 offset:3840\n\t" \
  "v_mfma_f32_16x16x32_bf16 v[232:235], v[180:183], v[120:123], v[232:235]\n\t" \
  "v_xor_b32 v33, 32, v32\n\t" \
  "ds_read_b64_tr_b16 v[70:71], v5 offset:3856\n\t" \
  "v_mfma_f32_16x16x32_bf16 v[228:231], v[160:163], v[112:115], v[228:231]\n\t" \
  "ds_read_b128 v[92:95], v8 offset:0\n\t" \
  "v_mfma_f32_16x16x32_bf16 v[228:231], v[172:175], v[120:123], v[228:231]\n\t" \
  "v_xor_b32 v34, 64, v32\n\t" \
  "ds_read_b128 v[168:171], v8 offset:1024\n\t" \
  "v_mfma_f32_16x16x32_bf16 v[224:227], v[160:163], v[120:123], v[224:227]\n\t" \
  "ds_read_b128 v[172:175], v8 offset:2048\n\t" \
  "s_waitcnt lgkmcnt(5)\n\t" \
  "v_mfma_f32_16x16x32_bf16 v[192:195], v[76:79], v[128:131], v[192:195]\n\t" \
  "v_xor_b32 v35, 96, v32\n\t" \
  "ds_read_b128 v[160:163], v8 offset:3072\n\t" \
  "v_mfma_f32_16x16x32_bf16 v[196:199], v[76:79], v[136:139], v[196:199]\n\t" \
  "ds_read_b128 v[176:179], v8 offset:4096\n\t" \
  "v_mfma_f32_16x16x32_bf16 v[196:199], v[144:147], v[128:131], v[196:199]\n\t" \
  "v_add_u32 v36, 0, v13\n\t" \
  "ds_read_b128 v[180:183], v8 offset:5120\n\t" \
  "v_mfma_f32_16x16x32_bf16 v[200:203], v[76:79], v[96:99], v[200:203]\n\t" \
  "ds_read_b128 v[184:187], v8 offset:6144\n\t" \
  "v_mfma_f32_16x16x32_bf16 v[200:203], v[144:147], v[136:139], v[200:203]\n\t" \
  "v_xor_b32 v37, 32, v36\n\t" \
  "ds_read_b128 v[188:191], v8 offset:7168\n\t" \
  "v_mfma_f32_16x16x32_bf16 v[200:203], v[72:75], v[128:131], v[200:203]\n\t" \
  "v_mfma_f32_16x16x32_bf16 v[204:207], v[76:79], v[100:103], v[204:207]\n\t" \
  "v_xor_b32 v38, 64, v36\n\t" \
  "v_mfma_f32_16x16x32_bf16 v[204:207], v[144:147], v[96:99], v[204:207]\n\t" \
  "v_mfma_f32_16x16x32_bf16 v[204:207], v[72:75], v[136:139], v[204:207]\n\t" \
  "v_xor_b32 v39, 96, v36\n\t" \
  "v_mfma_f32_16x16x32_bf16 v[204:207], v[104:107], v[128:131], v[204:207]\n\t" \
  "v_mfma_f32_16x16x32_bf16 v[208:211], v[76:79], v[152:155], v[208:211]\n\t" \
  "v_lshrrev_b32 v57, 5, v4\n\t" \
  "v_mfma_f32_16x16x32_bf16 v[208:211], v[144:147], v[100:103], v[208:211]\n\t" \
  "v_mfma_f32_16x16x32_bf16 v[208:211], v[72:75], v[96:99], v[208:211]\n\t" \
  "v_and_b32 v57, 0x18, v57\n\t" \
  "v_mfma_f32_16x16x32_bf16 v[208:211], v[104:107], v[136:139], v[208:211]\n\t" \
  "v_mfma_f32_16x16x32_bf16 v[208:211], v[64:67], v[128:131], v[208:211]\n\t" \
  "v_and_b32 v62, 0x70, v4\n\t" \
  "v_mfma_f32_16x16x32_bf16 v[212:215], v[76:79], v[80:83], v[212:215]\n\t" \
  "v_mfma_f32_16x16x32_bf16 v[212:215], v[144:147], v[152:155], v[212:215]\n\t" \
  "v_xor_b32 v57, v57, v62\n\t" \
  "v_mfma_f32_16x16x32_bf16 v[212:215], v[72:75], v[100:103], v[212:215]\n\t" \
  "v_mfma_f32_16x16x32_bf16 v[212:215], v[104:107], v[96:99], v[212:215]\n\t" \
  "v_and_b32 v58, 0xf0, v4\n\t" \
  "v_mfma_f32_16x16x32_bf16 v[212:215], v[64:67], v[136:139], v[212:215]\n\t" \
  "v_mfma_f32_16x16x32_bf16 v[212:215], v[84:87], v[128:131], v[212:215]\n\t" \
  "v_lshl_add_u32 v57, v58, 3, v57\n\t" \
  "v_mfma_f32_16x16x32_bf16 v[216:219], v[76:79], v[156:159], v[216:219]\n\t" \
  "v_mfma_f32_16x16x32_bf16 v[216:219], v[144:147], v[80:83], v[216:219]\n\t" \
  "v_add_u32 v57, %23, v57\n\t" \
  "v_mfma_f32_16x16x32_bf16 v[216:219], v[72:75], v[152:155], v[216:219]\n\t" \
  "v_mfma_f32_16x16x32_bf16 v[216:219], v[104:107], v[100:103], v[216:219]\n\t" \
  "v_xor_b32 v58, 32, v57\n\t" \
  "v_mfma_f32_16x16x32_bf16 v[216:219], v[64:67], v[96:99], v[216:219]\n\t" \
  "v_mfma_f32_16x16x32_bf16 v[216:219], v[84:87], v[136:139], v[216:219]\n\t" \
  "v_xor_b32 v59, 64, v57\n\t" \
  "v_mfma_f32_16x16x32_bf16 v[216:219], v[88:91], v[128:131], v[216:219]\n\t" \
  "v_mfma_f32_16x16x32_bf16 v[220:223], v[76:79], v[164:167], v[220:223]\n\t" \
  "v_xor_b32 v60, 96, v57\n\t" \
  "v_mfma_f32_16x16x32_bf16 v[220:223], v[144:147], v[156:159], v[220:223]\n\t" \
  "v_mfma_f32_16x16x32_bf16 v[220:223], v[72:75], v[80:83], v[220:223]\n\t" \
  "v_lshrrev_b32 v61, 3, v4\n\t" \
  "v_mfma_f32_16x16x32_bf16 v[220:223], v[104:107], v[152:155], v[220:223]\n\t" \
  "v_mfma_f32_16x16x32_bf16 v[220:223], v[64:67], v[100:103], v[220:223]\n\t" \
  "v_and_b32 v61, 0x70, v61\n\t" \
  "ds_read_b64_tr_b16 v[148:149], v5 offset:384\n\t" \
  "v_mfma_f32_16x16x32_bf16 v[220:223], v[84:87], v[96:99], v[220:223]\n\t" \
  "ds_read_b64_tr_b16 v[150:151], v5 offset:400\n\t" \
  "v_mfma_f32_16x16x32_bf16 v[220:223], v[88:91], v[136:139], v[220:223]\n\t" \
  "v_xor_b32 v61, v61, v4\n\t" \
  "ds_read_b128 v[80:83], v9 offset:16384\n\t" \
  "s_waitcnt lgkmcnt(3)\n\t" \
  "v_mfma_f32_16x16x32_bf16 v[220:223], v[68:71], v[128:131], v[220:223]\n\t" \
  "ds_read_b128 v[140:143], v9 offset:15360\n\t" \
  "v_mfma_f32_16x16x32_bf16 v[252:255], v[76:79], v[92:95], v[252:255]\n\t" \
  "v_add_u32 v61, %23, v61\n\t" \
  "ds_read_b64_tr_b16 v[132:133], v5 offset:896\n\t" \
  "v_mfma_f32_16x16x32_bf16 v[252:255], v[144:147], v[168:171], v[252:255]\n\t" \
  "ds_read_b64_tr_b16 v[134:135], v5 offset:912\n\t" \
  "v_mfma_f32_16x16x32_bf16 v[252:255], v[72:75], v[172:175], v[252:255]\n\t" \
  "v_and_b32 v63, 0x380, v4\n\t" \
  "ds_read_b128 v[124:127], v9 offset:14336\n\t" \
  "v_mfma_f32_16x16x32_bf16 v[252:255], v[104:107], v[160:163], v[252:255]\n\t" \
  "ds_read_b64_tr_b16 v[116:117], v5 offset:1408\n\t" \
  "v_mfma_f32_16x16x32_bf16 v[252:255], v[64:67], v[176:179], v[252:255]\n\t" \
  "v_lshl_add_u32 v62, v63, 6, v62\n\t" \
  "ds_read_b64_tr_b16 v[118:119], v5 offset:1424\n\t" \
  "v_mfma_f32_16x16x32_bf16 v[252:255], v[84:87], v[180:183], v[252:255]\n\t" \
  "ds_read_b128 v[92:95], v9 offset:13312\n\t" \
  "v_mfma_f32_16x16x32_bf16 v[252:255], v[88:91], v[184:187], v[252:255]\n\t" \
  "v_add_u32 v63, 0x10000, v62\n\t" \
  "ds_read_b64_tr_b16 v[108:109], v5 offset:1920\n\t" \
  "v_mfma_f32_16x16x32_bf16 v[252:255], v[68:71], v[188:191], v[252:255]\n\t" \
  "ds_read_b64_tr_b16 v[110:111], v5 offset:1936\n\t" \
  "v_mfma_f32_16x16x32_bf16 v[248:251], v[76:79], v[168:171], v[248:251]\n\t" \
  "ds_read_b128 v[112:115], v9 offset:12288\n\t" \
  "v_mfma_f32_16x16x32_bf16 v[248:251], v[144:147], v[172:175], v[248:251]\n\t" \
  "v_mfma_f32_16x16x32_bf16 v[248:251], v[72:75], v[160:163], v[248:251]\n\t" \
  "ds_read_b64_tr_b16 v[120:121], v5 offset:2432\n\t" \
  "v_mfma_f32_16x16x32_bf16 v[248:251], v[104:107], v[176:179], v[248:251]\n\t" \
  "ds_read_b64_tr_b16 v[122:123], v5 offset:2448\n\t" \
  "v_mfma_f32_16x16x32_bf16 v[248:251], v[64:67], v[180:183], v[248:251]\n\t" \
  "ds_read_b128 v[68:71], v9 offset:11264\n\t" \
  "v_mfma_f32_16x16x32_bf16 v[248:251], v[84:87], v[184:187], v[248:251]\n\t" \
  "v_mfma_f32_16x16x32_bf16 v[248:251], v[88:91], v[188:191], v[248:251]\n\t" \
  "v_mfma_f32_16x16x32_bf16 v[244:247], v[76:79], v[172:175], v[244:247]\n\t" \
  "v_mfma_f32_16x16x32_bf16 v[244:247], v[144:147], v[160:163], v[244:247]\n\t" \
  "ds_read_b64_tr_b16 v[128:129], v5 offset:2944\n\t" \
  "v_mfma_f32_16x16x32_bf16 v[244:247], v[72:75], v[176:179], v[244:247]\n\t" \
  "ds_read_b64_tr_b16 v[130:131], v5 offset:2960\n\t" \
  "v_mfma_f32_16x16x32_bf16 v[244:247], v[104:107], v[180:183], v[244:247]\n\t" \
  "ds_read_b128 v[88:91], v9 offset:10240\n\t" \
  "v_mfma_f32_16x16x32_bf16 v[244:247], v[64:67], v[184:187], v[244:247]\n\t" \
  "v_mfma_f32_16x16x32_bf16 v[244:247], v[84:87], v[188:191], v[244:247]\n\t" \
  "v_mfma_f32_16x16x32_bf16 v[240:243], v[76:79], v[160:163], v[240:243]\n\t" \
  "v_mfma_f32_16x16x32_bf16 v[240:243], v[144:147], v[176:179], v[240:243]\n\t" \
  "v_mfma_f32_16x16x32_bf16 v[240:243], v[72:75], v[180:183], v[240:243]\n\t" \
  "ds_read_b64_tr_b16 v[136:137], v5 offset:3456\n\t" \
  "v_mfma_f32_16x16x32_bf16 v[240:243], v[104:107], v[184:187], v[240:243]\n\t" \
  "ds_read_b64_tr_b16 v[138:139], v5 offset:3472\n\t" \
  "v_mfma_f32_16x16x32_bf16 v[240:243], v[64:67], v[188:191], v[240:243]\n\t" \
  "ds_read_b128 v[84:87], v9 offset:9216\n\t" \
  "v_mfma_f32_16x16x32_bf16 v[236:239], v[76:79], v[176:179], v[236:239]\n\t" \
  "v_mfma_f32_16x16x32_bf16 v[236:239], v[144:147], v[180:183], v[236:239]\n\t" \
  "v_mfma_f32_16x16x32_bf16 v[236:239], v[72:75], v[184:187], v[236:239]\n\t" \
  "v_mfma_f32_16x16x32_bf16 v[236:239], v[104:107], v[188:191], v[236:239]\n\t" \
  "v_mfma_f32_16x16x32_bf16 v[232:235], v[76:79], v[180:183], v[232:235]\n\t" \
  "v_mfma_f32_16x16x32_bf16 v[232:235], v[144:147], v[184:187], v[232:235]\n\t" \
  "ds_read_b64_tr_b16 v[96:97], v5 offset:3968\n\t" \
  "v_mfma_f32_16x16x32_bf16 v[232:235], v[72:75], v[188:191], v[232:235]\n\t" \
  "ds_read_b64_tr_b16 v[98:99], v5 offset:3984\n\t" \
  "v_mfma_f32_16x16x32_bf16 v[228:231], v[76:79], v[184:187], v[228:231]\n\t" \
  "ds_read_b128 v[100:103], v9 offset:7168\n\t" \
  "v_mfma_f32_16x16x32_bf16 v[228:231], v[144:147], v[188:191], v[228:231]\n\t" \
  "ds_read_b128 v[104:107], v9 offset:6144\n\t" \
  "v_mfma_f32_16x16x32_bf16 v[224:227], v[76:79], v[188:191], v[224:227]\n\t" \
  "ds_read_b128 v[144:147], v9 offset:5120\n\t" \
  "s_waitcnt lgkmcnt(5)\n\t" \
  "v_mfma_f32_16x16x32_bf16 v[192:195], v[148:151], v[80:83], v[192:195]\n\t" \
  "v_mfma_f32_16x16x32_bf16 v[196:199], v[148:151], v[140:143], v[196:199]\n\t" \
  "v_mfma_f32_16x16x32_bf16 v[196:199], v[132:135], v[80:83], v[196:199]\n\t" \
  "ds_read_b128 v[152:155], v9 offset:4096\n\t" \
  "v_mfma_f32_16x16x32_bf16 v[200:203], v[148:151], v[124:127], v[200:203]\n\t" \
  "v_mfma_f32_16x16x32_bf16 v[200:203], v[132:135], v[140:143], v[200:203]\n\t" \
  "v_mfma_f32_16x16x32_bf16 v[200:203], v[116:119], v[80:83], v[200:203]\n\t" \
  "v_mfma_f32_16x16x32_bf16 v[204:207], v[148:151], v[92:95], v[204:207]\n\t" \
  "ds_read_b128 v[156:159], v9 offset:3072\n\t" \
  "v_cndmask_b32 v20, v195, 0, s[76:77]\n\t" \
  "v_mfma_f32_16x16x32_bf16 v[204:207], v[132:135], v[124:127], v[204:207]\n\t" \
  "ds_bpermute_b32 v20, v15, v20\n\t" \
  "v_add_f32 v225, v225, v192\n\t" \
  "v_mfma_f32_16x16x32_bf16 v[204:207], v[116:119], v[140:143], v[204:207]\n\t" \
  "v_add_f32 v226, v226, v193\n\t" \
  "v_add_f32 v227, v227, v194\n\t" \
  "v_mfma_f32_16x16x32_bf16 v[204:207], v[108:111], v[80:83], v[204:207]\n\t" \
  "v_cndmask_b32 v21, v199, v195, s[76:77]\n\t" \
  "ds_bpermute_b32 v21, v15, v21\n\t" \
  "v_mfma_f32_16x16x32_bf16 v[208:211], v[148:151], v[112:115], v[208:211]\n\t" \
  "v_add_f32 v229, v229, v196\n\t" \
  "v_add_f32 v230, v230, v197\n\t" \
  "v_mfma_f32_16x16x32_bf16 v[208:211], v[132:135], v[92:95], v[208:211]\n\t" \
  "ds_read_b128 v[164:167], v9 offset:2048\n\t" \
  "v_add_f32 v231, v231, v198\n\t" \
  "v_mfma_f32_16x16x32_bf16 v[208:211], v[116:119], v[124:127], v[208:211]\n\t" \
  "v_cndmask_b32 v22, v203, v199, s[76:77]\n\t" \
  "ds_bpermute_b32 v22, v15, v22\n\t" \
  "v_mfma_f32_16x16x32_bf16 v[208:211], v[108:111], v[140:143], v[208:211]\n\t" \
  "v_add_f32 v233, v233, v200\n\t" \
  "v_add_f32 v234, v234, v201\n\t" \
  "v_mfma_f32_16x16x32_bf16 v[208:211], v[120:123], v[80:83], v[208:211]\n\t" \
  "v_add_f32 v235, v235, v202\n\t" \
  "s_waitcnt lgkmcnt(3)\n\t" \
  "v_add_f32 v224, v224, v20\n\t" \
  "v_mfma_f32_16x16x32_bf16 v[212:215], v[148:151], v[68:71], v[212:215]\n\t" \
  "v_cndmask_b32 v23, v207, v203, s[76:77]\n\t" \
  "ds_bpermute_b32 v23, v15, v23\n\t" \
  "v_mfma_f32_16x16x32_bf16 v[212:215], v[132:135], v[112:115], v[212:215]\n\t" \
  "v_add_f32 v237, v237, v204\n\t" \
  "v_add_f32 v238, v238, v205\n\t" \
  "v_mfma_f32_16x16x32_bf16 v[212:215], v[116:119], v[92:95], v[212:215]\n\t" \
  "ds_read_b128 v[168:171], v9 offset:1024\n\t" \
  "v_add_f32 v239, v239, v206\n\t" \
  "v_mfma_f32_16x16x32_bf16 v[212:215], v[108:111], v[124:127], v[212:215]\n\t" \
  "s_waitcnt lgkmcnt(4)\n\t" \
  "v_add_f32 v228, v228, v21\n\t" \
  "v_mfma_f32_16x16x32_bf16 v[212:215], v[120:123], v[140:143], v[212:215]\n\t" \
  "s_waitcnt lgkmcnt(2)\n\t" \
  "v_add_f32 v232, v232, v22\n\t" \
  "v_mfma_f32_16x16x32_bf16 v[212:215], v[128:131], v[80:83], v[212:215]\n\t" \
  "v_cndmask_b32 v24, v211, v207, s[76:77]\n\t" \
  "ds_bpermute_b32 v24, v15, v24\n\t" \
  "v_mfma_f32_16x16x32_bf16 v[216:219], v[148:151], v[88:91], v[216:219]\n\t" \
  "v_add_f32 v241, v241, v208\n\t" \
  "v_add_f32 v242, v242, v209\n\t" \
  "v_mfma_f32_16x16x32_bf16 v[216:219], v[132:135], v[68:71], v[216:219]\n\t" \
  "v_add_f32 v243, v243, v210\n\t" \
  "v_mfma_f32_16x16x32_bf16 v[216:219], v[116:119], v[112:115], v[216:219]\n\t" \
  "s_waitcnt lgkmcnt(2)\n\t" \
  "v_add_f32 v236, v236, v23\n\t" \
  "v_mfma_f32_16x16x32_bf16 v[216:219], v[108:111], v[92:95], v[216:219]\n\t" \
  "ds_read_b128 v[172:175], v9 offset:0\n\t" \
  "v_mfma_f32_16x16x32_bf16 v[216:219], v[120:123], v[124:127], v[216:219]\n\t" \
  "v_mfma_f32_16x16x32_bf16 v[216:219], v[128:131], v[140:143], v[216:219]\n\t" \
  "ds_read_b32 v16, v56 offset:96\n\t" \
  "v_cndmask_b32 v25, v215, v211, s[76:77]\n\t" \
  "v_mfma_f32_16x16x32_bf16 v[216:219], v[136:139], v[80:83], v[216:219]\n\t" \
  "ds_bpermute_b32 v25, v15, v25\n\t" \
  "v_add_f32 v245, v245, v212\n\t" \
  "v_mfma_f32_16x16x32_bf16 v[220:223], v[148:151], v[84:87], v[220:223]\n\t" \
  "v_add_f32 v246, v246, v213\n\t" \
  "v_add_f32 v247, v247, v214\n\t" \
  "v_mfma_f32_16x16x32_bf16 v[220:223], v[132:135], v[88:91], v[220:223]\n\t" \
  "s_waitcnt lgkmcnt(3)\n\t" \
  "v_add_f32 v240, v240, v24\n\t" \
  "v_mfma_f32_16x16x32_bf16 v[220:223], v[116:119], v[68:71], v[220:223]\n\t" \
  "v_mfma_f32_16x16x32_bf16 v[220:223], v[108:111], v[112:115], v[220:223]\n\t" \
  "v_mfma_f32_16x16x32_bf16 v[220:223], v[120:123], v[92:95], v[220:223]\n\t" \
  "v_mfma_f32_16x16x32_bf16 v[220:223], v[128:131], v[124:127], v[220:223]\n\t" \
  "s_waitcnt lgkmcnt(1)\n\t" \
  "v_readfirstlane_b32 vcc_lo, v16\n\t" \
  "v_mfma_f32_16x16x32_bf16 v[220:223], v[136:139], v[140:143], v[220:223]\n\t" \
  "s_cmp_eq_u32 vcc_lo, 0\n\t" \
  "s_cbranch_scc1 33f\n\t" \
  "s_lshr_b64 s[90:91], %18, s75\n\t" \
  "s_and_b32 s90, s90, 15\n\t" \
  "s_lshl_b32 s90, s90, 14\n\t" \
  "s_add_u32 s90, s90, s92\n\t" \
  "s_addc_u32 s91, s93, 0\n\t" \
  "s_add_u32 s75, s75, 4\n\t" \
  "s_swappc_b64 s[68:69], s[70:71]\n\t" \
  "33:\n\t" \
  "v_mfma_f32_16x16x32_bf16 v[220:223], v[96:99], v[80:83], v[220:223]\n\t" \
  "v_cndmask_b32 v26, v219, v215, s[76:77]\n\t" \
  "ds_bpermute_b32 v26, v15, v26\n\t" \
  "v_mfma_f32_16x16x32_bf16 v[224:227], v[148:151], v[100:103], v[224:227]\n\t" \
  "v_add_f32 v249, v249, v216\n\t" \
  "v_add_f32 v250, v250, v217\n\t" \
  "v_mfma_f32_16x16x32_bf16 v[228:231], v[148:151], v[104:107], v[228:231]\n\t" \
  "ds_read_b128 v[40:43], v56 offset:80\n\t" \
  "v_add_f32 v251, v251, v218\n\t" \
  "v_mfma_f32_16x16x32_bf16 v[228:231], v[132:135], v[100:103], v[228:231]\n\t" \
  "s_waitcnt lgkmcnt(2)\n\t" \
  "v_add_f32 v244, v244, v25\n\t" \
  "v_mfma_f32_16x16x32_bf16 v[232:235], v[148:151], v[144:147], v[232:235]\n\t" \
  "v_mfma_f32_16x16x32_bf16 v[232:235], v[132:135], v[104:107], v[232:235]\n\t" \
  "v_mfma_f32_16x16x32_bf16 v[232:235], v[116:119], v[100:103], v[232:235]\n\t" \
  "s_waitcnt lgkmcnt(1)\n\t" \
  "v_add_f32 v248, v248, v26\n\t" \
  "v_cndmask_b32 v27, v223, v219, s[76:77]\n\t" \
  "v_mfma_f32_16x16x32_bf16 v[236:239], v[148:151], v[152:155], v[236:239]\n\t" \
  "ds_bpermute_b32 v27, v15, v27\n\t" \
  "v_add_f32 v253, v253, v220\n\t" \
  "v_mfma_f32_16x16x32_bf16 v[236:239], v[132:135], v[144:147], v[236:239]\n\t" \
  "v_add_f32 v254, v254, v221\n\t" \
  "v_add_f32 v255, v255, v222\n\t" \
  "v_mfma_f32_16x16x32_bf16 v[236:239], v[116:119], v[104:107], v[236:239]\n\t" \
  "s_waitcnt lgkmcnt(1)\n\t" \
  "v_min_u32 v40, v40, v41\n\t" \
  "v_min3_u32 v40, v40, v42, v43\n\t" \
  "v_mfma_f32_16x16x32_bf16 v[236:239], v[108:111], v[100:103], v[236:239]\n\t" \
  "v_readfirstlane_b32 vcc_lo, v40\n\t" \
  "v_cvt_pk_bf16_f32 v45, v226, v227\n\t" \
  "v_mfma_f32_16x16x32_bf16 v[240:243], v[148:151], v[156:159], v[240:243]\n\t" \
  "ds_read_b32 v16, v56 offset:96\n\t" \
  "s_cmp_ge_u32 vcc_lo, 7\n\t" \
  "s_cbranch_scc0 36f\n\t" \
  "37:\n\t" \
  "v_mfma_f32_16x16x32_bf16 v[240:243], v[132:135], v[152:155], v[240:243]\n\t" \
  "v_cvt_pk_bf16_f32 v44, v224, v225\n\t" \
  "ds_write_b64 v14, v[44:45] offset:0\n\t" \
  "v_mfma_f32_16x16x32_bf16 v[240:243], v[116:119], v[144:147], v[240:243]\n\t" \
  "v_cvt_pk_bf16_f32 v47, v230, v231\n\t" \
  "v_cvt_pk_bf16_f32 v46, v228, v229\n\t" \
  "v_mfma_f32_16x16x32_bf16 v[240:243], v[108:111], v[104:107], v[240:243]\n\t" \
  "ds_write_b64 v14, v[46:47] offset:512\n\t" \
  "v_cvt_pk_bf16_f32 v49, v234, v235\n\t" \
  "v_mfma_f32_16x16x32_bf16 v[240:243], v[120:123], v[100:103], v[240:243]\n\t" \
  "v_cvt_pk_bf16_f32 v48, v232, v233\n\t" \
  "ds_write_b64 v14, v[48:49] offset:1024\n\t" \
  "v_mfma_f32_16x16x32_bf16 v[244:247], v[148:151], v[164:167], v[244:247]\n\t" \
  "s_waitcnt lgkmcnt(4)\n\t" \
  "v_add_f32 v252, v252, v27\n\t" \
  "v_cvt_pk_bf16_f32 v51, v238, v239\n\t" \
  "v_mfma_f32_16x16x32_bf16 v[244:247], v[132:135], v[156:159], v[244:247]\n\t" \
  "508:\n\t" \
  "s_cbranch_execz 26b\n\t" \
  "v_cvt_pk_bf16_f32 v50, v236, v237\n\t" \
  "ds_write_b64 v14, v[50:51] offset:1536\n\t" \
  "v_mfma_f32_16x16x32_bf16 v[244:247], v[116:119], v[152:155], v[244:247]\n\t" \
  "s_waitcnt lgkmcnt(4)\n\t" \
  "v_readfirstlane_b32 vcc_lo, v16\n\t" \
  "v_mfma_f32_16x16x32_bf16 v[244:247], v[108:111], v[144:147], v[244:247]\n\t" \
  "s_cmp_eq_u32 vcc_lo, 0\n\t" \
  "s_cbranch_scc1 34f\n\t" \
  "s_lshr_b64 s[90:91], %18, s75\n\t" \
  "s_and_b32 s90, s90, 15\n\t" \
  "s_lshl_b32 s90, s90, 14\n\t" \
  "s_add_u32 s90, s90, s92\n\t" \
  "s_addc_u32 s91, s93, 0\n\t" \
  "s_add_u32 s75, s75, 4\n\t" \
  "s_swappc_b64 s[68:69], s[70:71]\n\t" \
  "34:\n\t" \
  "v_mfma_f32_16x16x32_bf16 v[244:247], v[120:123], v[104:107], v[244:247]\n\t" \
  "ds_read_b64_tr_b16 v[192:193], v28 offset:0\n\t" \
  "v_mfma_f32_16x16x32_bf16 v[244:247], v[128:131], v[100:103], v[244:247]\n\t" \
  "v_cvt_pk_bf16_f32 v45, v242, v243\n\t" \
  "v_cvt_pk_bf16_f32 v44, v240, v241\n\t" \
  "v_mfma_f32_16x16x32_bf16 v[248:251], v[148:151], v[168:171], v[248:251]\n\t" \
  "ds_write_b64 v14, v[44:45] offset:2048\n\t" \
  "ds_read_b64_tr_b16 v[194:195], v28 offset:512\n\t" \
  "v_mfma_f32_16x16x32_bf16 v[248:251], v[132:135], v[164:167], v[248:251]\n\t" \
  "ds_read_b32 v16, v56 offset:96\n\t" \
  "ds_read_b64_tr_b16 v[196:197], v28 offset:4096\n\t" \
  "v_mfma_f32_16x16x32_bf16 v[248:251], v[116:119], v[156:159], v[248:251]\n\t" \
  "ds_read_b64_tr_b16 v[198:199], v28 offset:4608\n\t" \
  "v_mfma_f32_16x16x32_bf16 v[248:251], v[108:111], v[152:155], v[248:251]\n\t" \
  "ds_read_b64_tr_b16 v[200:201], v29 offset:0\n\t" \
  "v_mfma_f32_16x16x32_bf16 v[248:251], v[120:123], v[144:147], v[248:251]\n\t" \
  "ds_read_b64_tr_b16 v[202:203], v29 offset:512\n\t" \
  "v_mfma_f32_16x16x32_bf16 v[248:251], v[128:131], v[104:107], v[248:251]\n\t" \
  "v_cvt_pk_bf16_f32 v47, v246, v247\n\t" \
  "v_cvt_pk_bf16_f32 v46, v244, v245\n\t" \
  "v_mfma_f32_16x16x32_bf16 v[248:251], v[136:139], v[100:103], v[248:251]\n\t" \
  "ds_write_b64 v14, v[46:47] offset:2560\n\t" \
  "ds_read_b64_tr_b16 v[204:205], v29 offset:4096\n\t" \
  "v_mfma_f32_16x16x32_bf16 v[252:255], v[148:151], v[172:175], v[252:255]\n\t" \
  "ds_read_b64_tr_b16 v[206:207], v29 offset:4608\n\t" \
  "v_mfma_f32_16x16x32_bf16 v[252:255], v[132:135], v[168:171], v[252:255]\n\t" \
  "s_waitcnt lgkmcnt(7)\n\t" \
  "v_readfirstlane_b32 vcc_lo, v16\n\t" \
  "v_mfma_f32_16x16x32_bf16 v[252:255], v[116:119], v[164:167], v[252:255]\n\t" \
  "s_cmp_eq_u32 vcc_lo, 0\n\t" \
  "s_cbranch_scc1 35f\n\t" \
  "s_cmp_ge_u32 s75, 8\n\t" \
  "s_cbranch_scc1 35f\n\t" \
  "s_lshr_b64 s[90:91], %18, s75\n\t" \
  "s_and_b32 s90, s90, 15\n\t" \
  "s_lshl_b32 s90, s90, 14\n\t" \
  "s_add_u32 s90, s90, s92\n\t" \
  "s_addc_u32 s91, s93, 0\n\t" \
  "s_add_u32 s75, s75, 4\n\t" \
  "s_swappc_b64 s[68:69], s[70:71]\n\t" \
  "35:\n\t" \
  "v_mfma_f32_16x16x32_bf16 v[252:255], v[108:111], v[156:159], v[252:255]\n\t" \
  "ds_read_b64_tr_b16 v[208:209], v30 offset:0\n\t" \
  "v_mfma_f32_16x16x32_bf16 v[252:255], v[120:123], v[152:155], v[252:255]\n\t" \
  "ds_read_b64_tr_b16 v[210:211], v30 offset:512\n\t" \
  "v_mfma_f32_16x16x32_bf16 v[252:255], v[128:131], v[144:147], v[252:255]\n\t" \
  "v_cvt_pk_bf16_f32 v49, v250, v251\n\t" \
  "v_cvt_pk_bf16_f32 v48, v248, v249\n\t" \
  "v_mfma_f32_16x16x32_bf16 v[252:255], v[136:139], v[104:107], v[252:255]\n\t" \
  "ds_write_b64 v14, v[48:49] offset:3072\n\t" \
  "ds_read_b64_tr_b16 v[212:213], v30 offset:4096\n\t" \
  "v_mfma_f32_16x16x32_bf16 v[252:255], v[96:99], v[100:103], v[252:255]\n\t" \
  "ds_read_b64_tr_b16 v[214:215], v30 offset:4608\n\t" \
  "ds_read_b32 v16, v56 offset:96\n\t" \
  "ds_read_b64_tr_b16 v[216:217], v31 offset:0\n\t" \
  "ds_read_b64_tr_b16 v[218:219], v31 offset:512\n\t" \
  "ds_read_b64_tr_b16 v[220:221], v31 offset:4096\n\t" \
  "ds_read_b64_tr_b16 v[222:223], v31 offset:4608\n\t" \
  "s_nop 1\n\t" \
  "v_cvt_pk_bf16_f32 v51, v254, v255\n\t" \
  "v_cvt_pk_bf16_f32 v50, v252, v253\n\t" \
  "ds_write_b64 v14, v[50:51] offset:3584\n\t" \
  "ds_write_b32 v18, v17 offset:32\n\t" \
  "s_cmp_lg_u32 s75, 0\n\t" \
  "s_cbranch_scc1 44f\n\t" \
  "s_waitcnt lgkmcnt(6)\n\t" \
  "v_readfirstlane_b32 vcc_lo, v16\n\t" \
  "s_nop 1\n\t" \
  "s_cmp_ge_u32 vcc_lo, 1\n\t" \
  "s_cbranch_scc0 46f\n\t" \
  "47:\n\t" \
  "44:\n\t" \
  "s_cmp_ge_u32 s75, 8\n\t" \
  "s_cbranch_scc1 45f\n\t" \
  "s_lshr_b64 s[90:91], %18, s75\n\t" \
  "s_and_b32 s90, s90, 15\n\t" \
  "s_lshl_b32 s90, s90, 14\n\t" \
  "s_add_u32 s90, s90, s92\n\t" \
  "s_addc_u32 s91, s93, 0\n\t" \
  "s_add_u32 s75, s75, 4\n\t" \
  "s_swappc_b64 s[68:69], s[70:71]\n\t" \
  "s_branch 44b\n\t" \
  "45:\n\t" \
  "s_waitcnt vmcnt(6)\n\t" \
  "v_mfma_f32_16x16x32_bf16 v[224:227], v[192:195], v[64:67], 0\n\t" \
  "s_waitcnt lgkmcnt(12)\n\t" \
  "v_mfma_f32_16x16x32_bf16 v[224:227], v[196:199], v[68:71], v[224:227]\n\t" \
  "s_waitcnt vmcnt(4)\n\t" \
  "v_mfma_f32_16x16x32_bf16 v[228:231], v[196:199], v[76:79], 0\n\t" \
  "s_lshr_b64 s[90:91], %18, 8\n\t" \
  "s_and_b32 s90, s90, 15\n\t" \
  "s_lshl_b32 s90, s90, 14\n\t" \
  "s_add_u32 s90, s90, s92\n\t" \
  "s_addc_u32 s91, s93, 0\n\t" \
  "global_load_dwordx4 v[96:99], v12, s[90:91] offset:0 sc1\n\t" \
  "v_mfma_f32_16x16x32_bf16 v[228:231], v[192:195], v[72:75], v[228:231]\n\t" \
  "v_mfma_f32_16x16x32_bf16 v[232:235], v[200:203], v[64:67], 0\n\t" \
  "ds_read_b64_tr_b16 v[192:193], v28 offset:8192\n\t" \
  "v_mfma_f32_16x16x32_bf16 v[232:235], v[204:207], v[68:71], v[232:235]\n\t" \
  "ds_read_b64_tr_b16 v[194:195], v28 offset:8704\n\t" \
  "v_mfma_f32_16x16x32_bf16 v[236:239], v[204:207], v[76:79], 0\n\t" \
  "ds_read_b64_tr_b16 v[196:197], v28 offset:12288\n\t" \
  "global_load_dwordx4 v[100:103], v12, s[90:91] offset:1024 sc1\n\t" \
  "v_mfma_f32_16x16x32_bf16 v[236:239], v[200:203], v[72:75], v[236:239]\n\t" \
  "ds_read_b64_tr_b16 v[198:199], v28 offset:12800\n\t" \
  "s_waitcnt lgkmcnt(4)\n\t" \
  "v_mfma_f32_16x16x32_bf16 v[240:243], v[208:211], v[64:67], 0\n\t" \
  "ds_read_b64_tr_b16 v[200:201], v29 offset:8192\n\t" \
  "v_mfma_f32_16x16x32_bf16 v[240:243], v[212:215], v[68:71], v[240:243]\n\t" \
  "ds_read_b64_tr_b16 v[202:203], v29 offset:8704\n\t" \
  "v_mfma_f32_16x16x32_bf16 v[244:247], v[212:215], v[76:79], 0\n\t" \
  "ds_read_b64_tr_b16 v[204:205], v29 offset:12288\n\t" \
  "global_load_dwordx4 v[104:107], v12, s[90:91] offset:2048 sc1\n\t" \
  "v_mfma_f32_16x16x32_bf16 v[244:247], v[208:211], v[72:75], v[244:247]\n\t" \
  "ds_read_b64_tr_b16 v[206:207], v29 offset:12800\n\t" \
  "v_mfma_f32_16x16x32_bf16 v[248:251], v[216:219], v[64:67], 0\n\t" \
  "ds_read_b64_tr_b16 v[208:209], v30 offset:8192\n\t" \
  "v_mfma_f32_16x16x32_bf16 v[248:251], v[220:223], v[68:71], v[248:251]\n\t" \
  "ds_read_b64_tr_b16 v[210:211], v30 offset:8704\n\t" \
  "v_mfma_f32_16x16x32_bf16 v[252:255], v[220:223], v[76:79], 0\n\t" \
  "ds_read_b64_tr_b16 v[212:213], v30 offset:12288\n\t" \
  "global_load_dwordx4 v[108:111], v12, s[90:91] offset:3072 sc1\n\t" \
  "v_mfma_f32_16x16x32_bf16 v[252:255], v[216:219], v[72:75], v[252:255]\n\t" \
  "ds_read_b64_tr_b16 v[214:215], v30 offset:12800\n\t" \
  "s_waitcnt lgkmcnt(6)\n\t" \
  "s_waitcnt vmcnt(6)\n\t" \
  "v_mfma_f32_16x16x32_bf16 v[224:227], v[192:195], v[80:83], v[224:227]\n\t" \
  "ds_read_b64_tr_b16 v[216:217], v31 offset:8192\n\t" \
  "v_mfma_f32_16x16x32_bf16 v[224:227], v[196:199], v[84:87], v[224:227]\n\t" \
  "ds_read_b64_tr_b16 v[218:219], v31 offset:8704\n\t" \
  "s_waitcnt vmcnt(4)\n\t" \
  "v_mfma_f32_16x16x32_bf16 v[228:231], v[196:199], v[92:95], v[228:231]\n\t" \
  "ds_read_b64_tr_b16 v[220:221], v31 offset:12288\n\t" \
  "s_lshr_b64 s[90:91], %18, 12\n\t" \
  "s_and_b32 s90, s90, 15\n\t" \
  "s_lshl_b32 s90, s90, 14\n\t" \
  "s_add_u32 s90, s90, s92\n\t" \
  "s_addc_u32 s91, s93, 0\n\t" \
  "global_load_dwordx4 v[112:115], v12, s[90:91] offset:0 sc1\n\t" \
  "v_mfma_f32_16x16x32_bf16 v[228:231], v[192:195], v[88:91], v[228:231]\n\t" \
  "ds_read_b64_tr_b16 v[222:223], v31 offset:12800\n\t" \
  "v_mfma_f32_16x16x32_bf16 v[232:235], v[200:203], v[80:83], v[232:235]\n\t" \
  "ds_read_b64_tr_b16 v[192:193], v28 offset:16384\n\t" \
  "s_waitcnt lgkmcnt(6)\n\t" \
  "v_mfma_f32_16x16x32_bf16 v[232:235], v[204:207], v[84:87], v[232:235]\n\t" \
  "ds_read_b64_tr_b16 v[194:195], v28 offset:16896\n\t" \
  "ds_write_b32 v18, v17 offset:160\n\t" \
  "v_mfma_f32_16x16x32_bf16 v[236:239], v[204:207], v[92:95], v[236:239]\n\t" \
  "ds_read_b64_tr_b16 v[196:197], v28 offset:20480\n\t" \
  "global_load_dwordx4 v[116:119], v12, s[90:91] offset:1024 sc1\n\t" \
  "v_mfma_f32_16x16x32_bf16 v[236:239], v[200:203], v[88:91], v[236:239]\n\t" \
  "ds_read_b64_tr_b16 v[198:199], v28 offset:20992\n\t" \
  "v_mfma_f32_16x16x32_bf16 v[240:243], v[208:211], v[80:83], v[240:243]\n\t" \
  "ds_read_b64_tr_b16 v[200:201], v29 offset:16384\n\t" \
  "s_waitcnt lgkmcnt(7)\n\t" \
  "v_mfma_f32_16x16x32_bf16 v[240:243], v[212:215], v[84:87], v[240:243]\n\t" \
  "ds_read_b64_tr_b16 v[202:203], v29 offset:16896\n\t" \
  "v_mfma_f32_16x16x32_bf16 v[244:247], v[212:215], v[92:95], v[244:247]\n\t" \
  "ds_read_b64_tr_b16 v[204:205], v29 offset:20480\n\t" \
  "global_load_dwordx4 v[120:123], v12, s[90:91] offset:2048 sc1\n\t" \
  "v_mfma_f32_16x16x32_bf16 v[244:247], v[208:211], v[88:91], v[244:247]\n\t" \
  "ds_read_b64_tr_b16 v[206:207], v29 offset:20992\n\t" \
  "v_mfma_f32_16x16x32_bf16 v[248:251], v[216:219], v[80:83], v[248:251]\n\t" \
  "ds_read_b64_tr_b16 v[208:209], v30 offset:16384\n\t" \
  "s_waitcnt lgkmcnt(6)\n\t" \
  "v_mfma_f32_16x16x32_bf16 v[248:251], v[220:223], v[84:87], v[248:251]\n\t" \
  "ds_read_b64_tr_b16 v[210:211], v30 offset:16896\n\t" \
  "v_mfma_f32_16x16x32_bf16 v[252:255], v[220:223], v[92:95], v[252:255]\n\t" \
  "ds_read_b64_tr_b16 v[212:213], v30 offset:20480\n\t" \
  "global_load_dwordx4 v[124:127], v12, s[90:91] offset:3072 sc1\n\t" \
  "v_mfma_f32_16x16x32_bf16 v[252:255], v[216:219], v[88:91], v[252:255]\n\t" \
  "ds_read_b64_tr_b16 v[214:215], v30 offset:20992\n\t" \
  "s_waitcnt vmcnt(6)\n\t" \
  "v_mfma_f32_16x16x32_bf16 v[224:227], v[192:195], v[96:99], v[224:227]\n\t" \
  "ds_read_b64_tr_b16 v[216:217], v31 offset:16384\n\t" \
  "s_waitcnt lgkmcnt(6)\n\t" \
  "v_mfma_f32_16x16x32_bf16 v[224:227], v[196:199], v[100:103], v[224:227]\n\t" \
  "ds_read_b64_tr_b16 v[218:219], v31 offset:16896\n\t" \
  "s_lshr_b64 s[90:91], %18, 16\n\t" \
  "s_and_b32 s90, s90, 15\n\t" \
  "s_lshl_b32 s90, s90, 14\n\t" \
  "s_add_u32 s90, s90, s92\n\t" \
  "s_addc_u32 s91, s93, 0\n\t" \
  "global_load_dwordx4 v[128:131], v12, s[90:91] offset:0 sc1\n\t" \
  "s_waitcnt vmcnt(5)\n\t" \
  "v_mfma_f32_16x16x32_bf16 v[228:231], v[196:199], v[108:111], v[228:231]\n\t" \
  "ds_read_b64_tr_b16 v[220:221], v31 offset:20480\n\t" \
  "v_mfma_f32_16x16x32_bf16 v[228:231], v[192:195], v[104:107], v[228:231]\n\t" \
  "ds_read_b64_tr_b16 v[222:223], v31 offset:20992\n\t" \
  "global_load_dwordx4 v[132:135], v12, s[90:91] offset:1024 sc1\n\t" \
  "v_mfma_f32_16x16x32_bf16 v[232:235], v[200:203], v[96:99], v[232:235]\n\t" \
  "ds_read_b64_tr_b16 v[192:193], v28 offset:24576\n\t" \
  "s_waitcnt lgkmcnt(6)\n\t" \
  "v_mfma_f32_16x16x32_bf16 v[232:235], v[204:207], v[100:103], v[232:235]\n\t" \
  "ds_read_b64_tr_b16 v[194:195], v28 offset:25088\n\t" \
  "global_load_dwordx4 v[136:139], v12, s[90:91] offset:2048 sc1\n\t" \
  "v_mfma_f32_16x16x32_bf16 v[236:239], v[204:207], v[108:111], v[236:239]\n\t" \
  "ds_read_b64_tr_b16 v[196:197], v28 offset:28672\n\t" \
  "v_mfma_f32_16x16x32_bf16 v[236:239], v[200:203], v[104:107], v[236:239]\n\t" \
  "ds_read_b64_tr_b16 v[198:199], v28 offset:29184\n\t" \
  "global_load_dwordx4 v[140:143], v12, s[90:91] offset:3072 sc1\n\t" \
  "v_mfma_f32_16x16x32_bf16 v[240:243], v[208:211], v[96:99], v[240:243]\n\t" \
  "ds_read_b64_tr_b16 v[200:201], v29 offset:24576\n\t" \
  "s_waitcnt lgkmcnt(6)\n\t" \
  "v_mfma_f32_16x16x32_bf16 v[240:243], v[212:215], v[100:103], v[240:243]\n\t" \
  "ds_read_b64_tr_b16 v[202:203], v29 offset:25088\n\t" \
  "s_lshr_b64 s[90:91], %18, 20\n\t" \
  "s_and_b32 s90, s90, 15\n\t" \
  "s_lshl_b32 s90, s90, 14\n\t" \
  "s_add_u32 s90, s90, s92\n\t" \
  "s_addc_u32 s91, s93, 0\n\t" \
  "global_load_dwordx4 v[144:147], v12, s[90:91] offset:0 sc1\n\t" \
  "v_mfma_f32_16x16x32_bf16 v[244:247], v[212:215], v[108:111], v[244:247]\n\t" \
  "ds_read_b64_tr_b16 v[204:205], v29 offset:28672\n\t" \
  "v_mfma_f32_16x16x32_bf16 v[244:247], v[208:211], v[104:107], v[244:247]\n\t" \
  "ds_read_b64_tr_b16 v[206:207], v29 offset:29184\n\t" \
  "global_load_dwordx4 v[148:151], v12, s[90:91] offset:1024 sc1\n\t" \
  "v_mfma_f32_16x16x32_bf16 v[248:251], v[216:219], v[96:99], v[248:251]\n\t" \
  "ds_read_b64_tr_b16 v[208:209], v30 offset:24576\n\t" \
  "s_waitcnt lgkmcnt(6)\n\t" \
  "v_mfma_f32_16x16x32_bf16 v[248:251], v[220:223], v[100:103], v[248:251]\n\t" \
  "ds_read_b64_tr_b16 v[210:211], v30 offset:25088\n\t" \
  "global_load_dwordx4 v[152:155], v12, s[90:91] offset:2048 sc1\n\t" \
  "v_mfma_f32_16x16x32_bf16 v[252:255], v[220:223], v[108:111], v[252:255]\n\t" \
  "ds_read_b64_tr_b16 v[212:213], v30 offset:28672\n\t" \
  "v_mfma_f32_16x16x32_bf16 v[252:255], v[216:219], v[104:107], v[252:255]\n\t" \
  "ds_read_b64_tr_b16 v[214:215], v30 offset:29184\n\t" \
  "global_load_dwordx4 v[156:159], v12, s[90:91] offset:3072 sc1\n\t" \
  "s_waitcnt vmcnt(10)\n\t" \
  "v_mfma_f32_16x16x32_bf16 v[224:227], v[192:195], v[112:115], v[224:227]\n\t" \
  "ds_read_b64_tr_b16 v[216:217], v31 offset:24576\n\t" \
  "s_waitcnt lgkmcnt(6)\n\t" \
  "v_mfma_f32_16x16x32_bf16 v[224:227], v[196:199], v[116:119], v[224:227]\n\t" \
  "ds_read_b64_tr_b16 v[218:219], v31 offset:25088\n\t" \
  "s_waitcnt vmcnt(8)\n\t" \
  "v_mfma_f32_16x16x32_bf16 v[228:231], v[196:199], v[124:127], v[228:231]\n\t" \
  "ds_read_b64_tr_b16 v[220:221], v31 offset:28672\n\t" \
  "s_lshr_b64 s[90:91], %18, 24\n\t" \
  "s_and_b32 s90, s90, 15\n\t" \
  "s_lshl_b32 s90, s90, 14\n\t" \
  "s_add_u32 s90, s90, s92\n\t" \
  "s_addc_u32 s91, s93, 0\n\t" \
  "global_load_dwordx4 v[160:163], v12, s[90:91] offset:0 sc1\n\t" \
  "v_mfma_f32_16x16x32_bf16 v[228:231], v[192:195], v[120:123], v[228:231]\n\t" \
  "ds_read_b64_tr_b16 v[222:223], v31 offset:29184\n\t" \
  "v_mfma_f32_16x16x32_bf16 v[232:235], v[200:203], v[112:115], v[232:235]\n\t" \
  "ds_read_b64_tr_b16 v[192:193], v28 offset:32768\n\t" \
  "s_waitcnt lgkmcnt(6)\n\t" \
  "v_mfma_f32_16x16x32_bf16 v[232:235], v[204:207], v[116:119], v[232:235]\n\t" \
  "ds_read_b64_tr_b16 v[194:195], v28 offset:33280\n\t" \
  "v_mfma_f32_16x16x32_bf16 v[236:239], v[204:207], v[124:127], v[236:239]\n\t" \
  "ds_read_b64_tr_b16 v[196:197], v28 offset:36864\n\t" \
  "global_load_dwordx4 v[164:167], v12, s[90:91] offset:1024 sc1\n\t" \
  "v_mfma_f32_16x16x32_bf16 v[236:239], v[200:203], v[120:123], v[236:239]\n\t" \
  "ds_read_b64_tr_b16 v[198:199], v28 offset:37376\n\t" \
  "v_mfma_f32_16x16x32_bf16 v[240:243], v[208:211], v[112:115], v[240:243]\n\t" \
  "ds_read_b64_tr_b16 v[200:201], v29 offset:32768\n\t" \
  "s_waitcnt lgkmcnt(6)\n\t" \
  "v_mfma_f32_16x16x32_bf16 v[240:243], v[212:215], v[116:119], v[240:243]\n\t" \
  "ds_read_b64_tr_b16 v[202:203], v29 offset:33280\n\t" \
  "v_mfma_f32_16x16x32_bf16 v[244:247], v[212:215], v[124:127], v[244:247]\n\t" \
  "ds_read_b64_tr_b16 v[204:205], v29 offset:36864\n\t" \
  "global_load_dwordx4 v[168:171], v12, s[90:91] offset:2048 sc1\n\t" \
  "v_mfma_f32_16x16x32_bf16 v[244:247], v[208:211], v[120:123], v[244:247]\n\t" \
  "ds_read_b64_tr_b16 v[206:207], v29 offset:37376\n\t" \
  "v_mfma_f32_16x16x32_bf16 v[248:251], v[216:219], v[112:115], v[248:251]\n\t" \
  "ds_read_b64_tr_b16 v[208:209], v30 offset:32768\n\t" \
  "s_waitcnt lgkmcnt(6)\n\t" \
  "v_mfma_f32_16x16x32_bf16 v[248:251], v[220:223], v[116:119], v[248:251]\n\t" \
  "ds_read_b64_tr_b16 v[210:211], v30 offset:33280\n\t" \
  "v_mfma_f32_16x16x32_bf16 v[252:255], v[220:223], v[124:127], v[252:255]\n\t" \
  "ds_read_b64_tr_b16 v[212:213], v30 offset:36864\n\t" \
  "global_load_dwordx4 v[172:175], v12, s[90:91] offset:3072 sc1\n\t" \
  "v_mfma_f32_16x16x32_bf16 v[252:255], v[216:219], v[120:123], v[252:255]\n\t" \
  "ds_read_b64_tr_b16 v[214:215], v30 offset:37376\n\t" \
  "s_waitcnt vmcnt(10)\n\t" \
  "v_mfma_f32_16x16x32_bf16 v[224:227], v[192:195], v[128:131], v[224:227]\n\t" \
  "ds_read_b64_tr_b16 v[216:217], v31 offset:32768\n\t" \
  "s_waitcnt lgkmcnt(6)\n\t" \
  "v_mfma_f32_16x16x32_bf16 v[224:227], v[196:199], v[132:135], v[224:227]\n\t" \
  "ds_read_b64_tr_b16 v[218:219], v31 offset:33280\n\t" \
  "s_waitcnt vmcnt(8)\n\t" \
  "v_mfma_f32_16x16x32_bf16 v[228:231], v[196:199], v[140:143], v[228:231]\n\t" \
  "ds_read_b64_tr_b16 v[220:221], v31 offset:36864\n\t" \
  "s_lshr_b64 s[90:91], %18, 28\n\t" \
  "s_and_b32 s90, s90, 15\n\t" \
  "s_lshl_b32 s90, s90, 14\n\t" \
  "s_add_u32 s90, s90, s92\n\t" \
  "s_addc_u32 s91, s93, 0\n\t" \
  "global_load_dwordx4 v[176:179], v12, s[90:91] offset:0 sc1\n\t" \
  "v_mfma_f32_16x16x32_bf16 v[228:231], v[192:195], v[136:139], v[228:231]\n\t" \
  "ds_read_b64_tr_b16 v[222:223], v31 offset:37376\n\t" \
  "v_mfma_f32_16x16x32_bf16 v[232:235], v[200:203], v[128:131], v[232:235]\n\t" \
  "ds_read_b64_tr_b16 v[192:193], v28 offset:40960\n\t" \
  "s_waitcnt lgkmcnt(6)\n\t" \
  "v_mfma_f32_16x16x32_bf16 v[232:235], v[204:207], v[132:135], v[232:235]\n\t" \
  "ds_read_b64_tr_b16 v[194:195], v28 offset:41472\n\t" \
  "v_mfma_f32_16x16x32_bf16 v[236:239], v[204:207], v[140:143], v[236:239]\n\t" \
  "ds_read_b64_tr_b16 v[196:197], v28 offset:45056\n\t" \
  "global_load_dwordx4 v[180:183], v12, s[90:91] offset:1024 sc1\n\t" \
  "v_mfma_f32_16x16x32_bf16 v[236:239], v[200:203], v[136:139], v[236:239]\n\t" \
  "ds_read_b64_tr_b16 v[198:199], v28 offset:45568\n\t" \
  "v_mfma_f32_16x16x32_bf16 v[240:243], v[208:211], v[128:131], v[240:243]\n\t" \
  "ds_read_b64_tr_b16 v[200:201], v29 offset:40960\n\t" \
  "s_waitcnt lgkmcnt(6)\n\t" \
  "v_mfma_f32_16x16x32_bf16 v[240:243], v[212:215], v[132:135], v[240:243]\n\t" \
  "ds_read_b64_tr_b16 v[202:203], v29 offset:41472\n\t" \
  "v_mfma_f32_16x16x32_bf16 v[244:247], v[212:215], v[140:143], v[244:247]\n\t" \
  "ds_read_b64_tr_b16 v[204:205], v29 offset:45056\n\t" \
  "global_load_dwordx4 v[184:187], v12, s[90:91] offset:2048 sc1\n\t" \
  "v_mfma_f32_16x16x32_bf16 v[244:247], v[208:211], v[136:139], v[244:247]\n\t" \
  "ds_read_b64_tr_b16 v[206:207], v29 offset:45568\n\t" \
  "v_mfma_f32_16x16x32_bf16 v[248:251], v[216:219], v[128:131], v[248:251]\n\t" \
  "ds_read_b64_tr_b16 v[208:209], v30 offset:40960\n\t" \
  "s_waitcnt lgkmcnt(6)\n\t" \
  "v_mfma_f32_16x16x32_bf16 v[248:251], v[220:223], v[132:135], v[248:251]\n\t" \
  "ds_read_b64_tr_b16 v[210:211], v30 offset:41472\n\t" \
  "v_mfma_f32_16x16x32_bf16 v[252:255], v[220:223], v[140:143], v[252:255]\n\t" \
  "ds_read_b64_tr_b16 v[212:213], v30 offset:45056\n\t" \
  "global_load_dwordx4 v[188:191], v12, s[90:91] offset:3072 sc1\n\t" \
  "v_mfma_f32_16x16x32_bf16 v[252:255], v[216:219], v[136:139], v[252:255]\n\t" \
  "ds_read_b64_tr_b16 v[214:215], v30 offset:45568\n\t" \
  "s_waitcnt vmcnt(10)\n\t" \
  "v_mfma_f32_16x16x32_bf16 v[224:227], v[192:195], v[144:147], v[224:227]\n\t" \
  "ds_read_b64_tr_b16 v[216:217], v31 offset:40960\n\t" \
  "s_waitcnt lgkmcnt(6)\n\t" \
  "v_mfma_f32_16x16x32_bf16 v[224:227], v[196:199], v[148:151], v[224:227]\n\t" \
  "ds_read_b64_tr_b16 v[218:219], v31 offset:41472\n\t" \
  "s_waitcnt vmcnt(8)\n\t" \
  "v_mfma_f32_16x16x32_bf16 v[228:231], v[196:199], v[156:159], v[228:231]\n\t" \
  "ds_read_b64_tr_b16 v[220:221], v31 offset:45056\n\t" \
  "s_lshr_b64 s[90:91], %18, 32\n\t" \
  "s_and_b32 s90, s90, 15\n\t" \
  "s_lshl_b32 s90, s90, 14\n\t" \
  "s_add_u32 s90, s90, s92\n\t" \
  "s_addc_u32 s91, s93, 0\n\t" \
  "global_load_dwordx4 v[64:67], v12, s[90:91] offset:0 sc1\n\t" \
  "v_mfma_f32_16x16x32_bf16 v[228:231], v[192:195], v[152:155], v[228:231]\n\t" \
  "ds_read_b64_tr_b16 v[222:223], v31 offset:45568\n\t" \
  "v_mfma_f32_16x16x32_bf16 v[232:235], v[200:203], v[144:147], v[232:235]\n\t" \
  "ds_read_b64_tr_b16 v[192:193], v28 offset:49152\n\t" \
  "s_waitcnt lgkmcnt(6)\n\t" \
  "v_mfma_f32_16x16x32_bf16 v[232:235], v[204:207], v[148:151], v[232:235]\n\t" \
  "ds_read_b64_tr_b16 v[194:195], v28 offset:49664\n\t" \
  "v_mfma_f32_16x16x32_bf16 v[236:239], v[204:207], v[156:159], v[236:239]\n\t" \
  "ds_read_b64_tr_b16 v[196:197], v28 offset:53248\n\t" \
  "global_load_dwordx4 v[68:71], v12, s[90:91] offset:1024 sc1\n\t" \
  "v_mfma_f32_16x16x32_bf16 v[236:239], v[200:203], v[152:155], v[236:239]\n\t" \
  "ds_read_b64_tr_b16 v[198:199], v28 offset:53760\n\t" \
  "v_mfma_f32_16x16x32_bf16 v[240:243], v[208:211], v[144:147], v[240:243]\n\t" \
  "ds_read_b64_tr_b16 v[200:201], v29 offset:49152\n\t" \
  "s_waitcnt lgkmcnt(6)\n\t" \
  "v_mfma_f32_16x16x32_bf16 v[240:243], v[212:215], v[148:151], v[240:243]\n\t" \
  "ds_read_b128 v[40:43], v56 offset:80\n\t" \
  "ds_read_b64_tr_b16 v[202:203], v29 offset:49664\n\t" \
  "v_mfma_f32_16x16x32_bf16 v[244:247], v[212:215], v[156:159], v[244:247]\n\t" \
  "ds_read_b64_tr_b16 v[204:205], v29 offset:53248\n\t" \
  "global_load_dwordx4 v[72:75], v12, s[90:91] offset:2048 sc1\n\t" \
  "v_mfma_f32_16x16x32_bf16 v[244:247], v[208:211], v[152:155], v[244:247]\n\t" \
  "ds_read_b64_tr_b16 v[206:207], v29 offset:53760\n\t" \
  "v_mfma_f32_16x16x32_bf16 v[248:251], v[216:219], v[144:147], v[248:251]\n\t" \
  "ds_read_b64_tr_b16 v[208:209], v30 offset:49152\n\t" \
  "s_waitcnt lgkmcnt(7)\n\t" \
  "v_mfma_f32_16x16x32_bf16 v[248:251], v[220:223], v[148:151], v[248:251]\n\t" \
  "ds_read_b64_tr_b16 v[210:211], v30 offset:49664\n\t" \
  "v_mfma_f32_16x16x32_bf16 v[252:255], v[220:223], v[156:159], v[252:255]\n\t" \
  "ds_read_b64_tr_b16 v[212:213], v30 offset:53248\n\t" \
  "global_load_dwordx4 v[76:79], v12, s[90:91] offset:3072 sc1\n\t" \
  "v_mfma_f32_16x16x32_bf16 v[252:255], v[216:219], v[152:155], v[252:255]\n\t" \
  "ds_read_b64_tr_b16 v[214:215], v30 offset:53760\n\t" \
  "s_waitcnt vmcnt(10)\n\t" \
  "v_mfma_f32_16x16x32_bf16 v[224:227], v[192:195], v[160:163], v[224:227]\n\t" \
  "s_waitcnt lgkmcnt(7)\n\t" \
  "v_min_u32 v40, v40, v41\n\t" \
  "v_min3_u32 v40, v40, v42, v43\n\t" \
  "ds_read_b64_tr_b16 v[216:217], v31 offset:49152\n\t" \
  "v_mfma_f32_16x16x32_bf16 v[224:227], v[196:199], v[164:167], v[224:227]\n\t" \
  "v_readfirstlane_b32 vcc_lo, v40\n\t" \
  "ds_read_b64_tr_b16 v[218:219], v31 offset:49664\n\t" \
  "s_waitcnt vmcnt(8)\n\t" \
  "v_mfma_f32_16x16x32_bf16 v[228:231], v[196:199], v[172:175], v[228:231]\n\t" \
  "s_cmp_ge_u32 vcc_lo, 13\n\t" \
  "s_cbranch_scc0 38f\n\t" \
  "39:\n\t" \
  "ds_read_b64_tr_b16 v[220:221], v31 offset:53248\n\t" \
  "s_lshr_b64 s[90:91], %18, 36\n\t" \
  "s_and_b32 s90, s90, 15\n\t" \
  "s_lshl_b32 s90, s90, 14\n\t" \
  "s_add_u32 s90, s90, s92\n\t" \
  "s_addc_u32 s91, s93, 0\n\t" \
  "global_load_dwordx4 v[80:83], v12, s[90:91] offset:0 sc1\n\t" \
  "v_mfma_f32_16x16x32_bf16 v[228:231], v[192:195], v[168:171], v[228:231]\n\t" \
  "ds_read_b64_tr_b16 v[222:223], v31 offset:53760\n\t" \
  "s_waitcnt lgkmcnt(6)\n\t" \
  "v_mfma_f32_16x16x32_bf16 v[232:235], v[200:203], v[160:163], v[232:235]\n\t" \
  "ds_read_b64_tr_b16 v[192:193], v28 offset:57344\n\t" \
  "v_mfma_f32_16x16x32_bf16 v[232:235], v[204:207], v[164:167], v[232:235]\n\t" \
  "ds_read_b64_tr_b16 v[194:195], v28 offset:57856\n\t" \
  "v_mfma_f32_16x16x32_bf16 v[236:239], v[204:207], v[172:175], v[236:239]\n\t" \
  "ds_read_b64_tr_b16 v[196:197], v28 offset:61440\n\t" \
  "global_load_dwordx4 v[84:87], v12, s[90:91] offset:1024 sc1\n\t" \
  "v_mfma_f32_16x16x32_bf16 v[236:239], v[200:203], v[168:171], v[236:239]\n\t" \
  "ds_read_b64_tr_b16 v[198:199], v28 offset:61952\n\t" \
  "v_mfma_f32_16x16x32_bf16 v[240:243], v[208:211], v[160:163], v[240:243]\n\t" \
  "ds_read_b64_tr_b16 v[200:201], v29 offset:57344\n\t" \
  "s_waitcnt lgkmcnt(6)\n\t" \
  "v_mfma_f32_16x16x32_bf16 v[240:243], v[212:215], v[164:167], v[240:243]\n\t" \
  "ds_read_b64_tr_b16 v[202:203], v29 offset:57856\n\t" \
  "v_mfma_f32_16x16x32_bf16 v[244:247], v[212:215], v[172:175], v[244:247]\n\t" \
  "ds_read_b64_tr_b16 v[204:205], v29 offset:61440\n\t" \
  "global_load_dwordx4 v[88:91], v12, s[90:91] offset:2048 sc1\n\t" \
  "v_mfma_f32_16x16x32_bf16 v[244:247], v[208:211], v[168:171], v[244:247]\n\t" \
  "ds_read_b64_tr_b16 v[206:207], v29 offset:61952\n\t" \
  "v_mfma_f32_16x16x32_bf16 v[248:251], v[216:219], v[160:163], v[248:251]\n\t" \
  "ds_read_b64_tr_b16 v[208:209], v30 offset:57344\n\t" \
  "s_waitcnt lgkmcnt(6)\n\t" \
  "v_mfma_f32_16x16x32_bf16 v[248:251], v[220:223], v[164:167], v[248:251]\n\t" \
  "ds_read_b64_tr_b16 v[210:211], v30 offset:57856\n\t" \
  "v_mfma_f32_16x16x32_bf16 v[252:255], v[220:223], v[172:175], v[252:255]\n\t" \
  "ds_read_b64_tr_b16 v[212:213], v30 offset:61440\n\t" \
  "global_load_dwordx4 v[92:95], v12, s[90:91] offset:3072 sc1\n\t" \
  "v_mfma_f32_16x16x32_bf16 v[252:255], v[216:219], v[168:171], v[252:255]\n\t" \
  "ds_read_b64_tr_b16 v[214:215], v30 offset:61952\n\t" \
  "s_waitcnt vmcnt(10)\n\t" \
  "v_mfma_f32_16x16x32_bf16 v[224:227], v[192:195], v[176:179], v[224:227]\n\t" \
  "ds_read_b64_tr_b16 v[216:217], v31 offset:57344\n\t" \
  "s_waitcnt lgkmcnt(6)\n\t" \
  "v_mfma_f32_16x16x32_bf16 v[224:227], v[196:199], v[180:183], v[224:227]\n\t" \
  "ds_read_b64_tr_b16 v[218:219], v31 offset:57856\n\t" \
  "s_waitcnt vmcnt(8)\n\t" \
  "v_mfma_f32_16x16x32_bf16 v[228:231], v[196:199], v[188:191], v[228:231]\n\t" \
  "ds_read_b64_tr_b16 v[220:221], v31 offset:61440\n\t" \
  "s_lshr_b64 s[90:91], %18, 40\n\t" \
  "s_and_b32 s90, s90, 15\n\t" \
  "s_lshl_b32 s90, s90, 14\n\t" \
  "s_add_u32 s90, s90, s92\n\t" \
  "s_addc_u32 s91, s93, 0\n\t" \
  "global_load_dwordx4 v[96:99], v12, s[90:91] offset:0 sc1\n\t" \
  "v_mfma_f32_16x16x32_bf16 v[228:231], v[192:195], v[184:187], v[228:231]\n\t" \
  "ds_read_b64_tr_b16 v[222:223], v31 offset:61952\n\t" \
  "v_mfma_f32_16x16x32_bf16 v[232:235], v[200:203], v[176:179], v[232:235]\n\t" \
  "ds_read_b64_tr_b16 v[192:193], v32 offset:0\n\t" \
  "s_waitcnt lgkmcnt(6)\n\t" \
  "v_mfma_f32_16x16x32_bf16 v[232:235], v[204:207], v[180:183], v[232:235]\n\t" \
  "ds_read_b64_tr_b16 v[194:195], v32 offset:512\n\t" \
  "v_mfma_f32_16x16x32_bf16 v[236:239], v[204:207], v[188:191], v[236:239]\n\t" \
  "ds_read_b64_tr_b16 v[196:197], v32 offset:4096\n\t" \
  "global_load_dwordx4 v[100:103], v12, s[90:91] offset:1024 sc1\n\t" \
  "v_mfma_f32_16x16x32_bf16 v[236:239], v[200:203], v[184:187], v[236:239]\n\t" \
  "ds_read_b64_tr_b16 v[198:199], v32 offset:4608\n\t" \
  "v_mfma_f32_16x16x32_bf16 v[240:243], v[208:211], v[176:179], v[240:243]\n\t" \
  "ds_read_b64_tr_b16 v[200:201], v33 offset:0\n\t" \
  "s_waitcnt lgkmcnt(6)\n\t" \
  "v_mfma_f32_16x16x32_bf16 v[240:243], v[212:215], v[180:183], v[240:243]\n\t" \
  "ds_read_b64_tr_b16 v[202:203], v33 offset:512\n\t" \
  "v_mfma_f32_16x16x32_bf16 v[244:247], v[212:215], v[188:191], v[244:247]\n\t" \
  "ds_read_b64_tr_b16 v[204:205], v33 offset:4096\n\t" \
  "global_load_dwordx4 v[104:107], v12, s[90:91] offset:2048 sc1\n\t" \
  "v_mfma_f32_16x16x32_bf16 v[244:247], v[208:211], v[184:187], v[244:247]\n\t" \
  "ds_read_b64_tr_b16 v[206:207], v33 offset:4608\n\t" \
  "v_mfma_f32_16x16x32_bf16 v[248:251], v[216:219], v[176:179], v[248:251]\n\t" \
  "ds_read_b64_tr_b16 v[208:209], v34 offset:0\n\t" \
  "s_waitcnt lgkmcnt(6)\n\t" \
  "v_mfma_f32_16x16x32_bf16 v[248:251], v[220:223], v[180:183], v[248:251]\n\t" \
  "ds_read_b64_tr_b16 v[210:211], v34 offset:512\n\t" \
  "v_mfma_f32_16x16x32_bf16 v[252:255], v[220:223], v[188:191], v[252:255]\n\t" \
  "ds_read_b64_tr_b16 v[212:213], v34 offset:4096\n\t" \
  "global_load_dwordx4 v[108:111], v12, s[90:91] offset:3072 sc1\n\t" \
  "ds_write_b32 v56, v56 offset:108\n\t" \
  "v_mfma_f32_16x16x32_bf16 v[252:255], v[216:219], v[184:187], v[252:255]\n\t" \
  "ds_read_b64_tr_b16 v[214:215], v34 offset:4608\n\t" \
  "s_waitcnt vmcnt(10)\n\t" \
  "v_mfma_f32_16x16x32_bf16 v[224:227], v[192:195], v[64:67], v[224:227]\n\t" \
  "ds_read_b64_tr_b16 v[216:217], v35 offset:0\n\t" \
  "s_waitcnt lgkmcnt(7)\n\t" \
  "v_mfma_f32_16x16x32_bf16 v[224:227], v[196:199], v[68:71], v[224:227]\n\t" \
  "ds_read_b64_tr_b16 v[218:219], v35 offset:512\n\t" \
  "s_waitcnt vmcnt(8)\n\t" \
  "v_mfma_f32_16x16x32_bf16 v[228:231], v[196:199], v[76:79], v[228:231]\n\t" \
  "ds_read_b64_tr_b16 v[220:221], v35 offset:4096\n\t" \
  "s_lshr_b64 s[90:91], %18, 44\n\t" \
  "s_and_b32 s90, s90, 15\n\t" \
  "s_lshl_b32 s90, s90, 14\n\t" \
  "s_add_u32 s90, s90, s92\n\t" \
  "s_addc_u32 s91, s93, 0\n\t" \
  "global_load_dwordx4 v[112:115], v12, s[90:91] offset:0 sc1\n\t" \
  "v_mfma_f32_16x16x32_bf16 v[228:231], v[192:195], v[72:75], v[228:231]\n\t" \
  "ds_read_b64_tr_b16 v[222:223], v35 offset:4608\n\t" \
  "v_mfma_f32_16x16x32_bf16 v[232:235], v[200:203], v[64:67], v[232:235]\n\t" \
  "ds_read_b64_tr_b16 v[192:193], v32 offset:8192\n\t" \
  "s_waitcnt lgkmcnt(6)\n\t" \
  "v_mfma_f32_16x16x32_bf16 v[232:235], v[204:207], v[68:71], v[232:235]\n\t" \
  "ds_read_b64_tr_b16 v[194:195], v32 offset:8704\n\t" \
  "v_mfma_f32_16x16x32_bf16 v[236:239], v[204:207], v[76:79], v[236:239]\n\t" \
  "ds_read_b64_tr_b16 v[196:197], v32 offset:12288\n\t" \
  "global_load_dwordx4 v[116:119], v12, s[90:91] offset:1024 sc1\n\t" \
  "v_mfma_f32_16x16x32_bf16 v[236:239], v[200:203], v[72:75], v[236:239]\n\t" \
  "ds_read_b64_tr_b16 v[198:199], v32 offset:12800\n\t" \
  "v_mfma_f32_16x16x32_bf16 v[240:243], v[208:211], v[64:67], v[240:243]\n\t" \
  "ds_read_b64_tr_b16 v[200:201], v33 offset:8192\n\t" \
  "s_waitcnt lgkmcnt(6)\n\t" \
  "v_mfma_f32_16x16x32_bf16 v[240:243], v[212:215], v[68:71], v[240:243]\n\t" \
  "ds_read_b64_tr_b16 v[202:203], v33 offset:8704\n\t" \
  "v_mfma_f32_16x16x32_bf16 v[244:247], v[212:215], v[76:79], v[244:247]\n\t" \
  "ds_read_b64_tr_b16 v[204:205], v33 offset:12288\n\t" \
  "global_load_dwordx4 v[120:123], v12, s[90:91] offset:2048 sc1\n\t" \
  "v_mfma_f32_16x16x32_bf16 v[244:247], v[208:211], v[72:75], v[244:247]\n\t" \
  "ds_read_b64_tr_b16 v[206:207], v33 offset:12800\n\t" \
  "v_mfma_f32_16x16x32_bf16 v[248:251], v[216:219], v[64:67], v[248:251]\n\t" \
  "ds_read_b64_tr_b16 v[208:209], v34 offset:8192\n\t" \
  "s_waitcnt lgkmcnt(6)\n\t" \
  "v_mfma_f32_16x16x32_bf16 v[248:251], v[220:223], v[68:71], v[248:251]\n\t" \
  "ds_read_b64_tr_b16 v[210:211], v34 offset:8704\n\t" \
  "v_mfma_f32_16x16x32_bf16 v[252:255], v[220:223], v[76:79], v[252:255]\n\t" \
  "ds_read_b64_tr_b16 v[212:213], v34 offset:12288\n\t" \
  "global_load_dwordx4 v[124:127], v12, s[90:91] offset:3072 sc1\n\t" \
  "v_mfma_f32_16x16x32_bf16 v[252:255], v[216:219], v[72:75], v[252:255]\n\t" \
  "ds_read_b64_tr_b16 v[214:215], v34 offset:12800\n\t" \
  "s_waitcnt vmcnt(10)\n\t" \
  "v_mfma_f32_16x16x32_bf16 v[224:227], v[192:195], v[80:83], v[224:227]\n\t" \
  "ds_read_b64_tr_b16 v[216:217], v35 offset:8192\n\t" \
  "s_waitcnt lgkmcnt(6)\n\t" \
  "v_mfma_f32_16x16x32_bf16 v[224:227], v[196:199], v[84:87], v[224:227]\n\t" \
  "ds_read_b64_tr_b16 v[218:219], v35 offset:8704\n\t" \
  "s_waitcnt vmcnt(8)\n\t" \
  "v_mfma_f32_16x16x32_bf16 v[228:231], v[196:199], v[92:95], v[228:231]\n\t" \
  "ds_read_b64_tr_b16 v[220:221], v35 offset:12288\n\t" \
  "s_lshr_b64 s[90:91], %18, 48\n\t" \
  "s_and_b32 s90, s90, 15\n\t" \
  "s_lshl_b32 s90, s90, 14\n\t" \
  "s_add_u32 s90, s90, s92\n\t" \
  "s_addc_u32 s91, s93, 0\n\t" \
  "global_load_dwordx4 v[128:131], v12, s[90:91] offset:0 sc1\n\t" \
  "v_mfma_f32_16x16x32_bf16 v[228:231], v[192:195], v[88:91], v[228:231]\n\t" \
  "ds_read_b64_tr_b16 v[222:223], v35 offset:12800\n\t" \
  "v_mfma_f32_16x16x32_bf16 v[232:235], v[200:203], v[80:83], v[232:235]\n\t" \
  "ds_read_b64_tr_b16 v[192:193], v32 offset:16384\n\t" \
  "s_waitcnt lgkmcnt(6)\n\t" \
  "v_mfma_f32_16x16x32_bf16 v[232:235], v[204:207], v[84:87], v[232:235]\n\t" \
  "ds_read_b64_tr_b16 v[194:195], v32 offset:16896\n\t" \
  "v_mfma_f32_16x16x32_bf16 v[236:239], v[204:207], v[92:95], v[236:239]\n\t" \
  "ds_read_b64_tr_b16 v[196:197], v32 offset:20480\n\t" \
  "global_load_dwordx4 v[132:135], v12, s[90:91] offset:1024 sc1\n\t" \
  "v_mfma_f32_16x16x32_bf16 v[236:239], v[200:203], v[88:91], v[236:239]\n\t" \
  "ds_read_b64_tr_b16 v[198:199], v32 offset:20992\n\t" \
  "v_mfma_f32_16x16x32_bf16 v[240:243], v[208:211], v[80:83], v[240:243]\n\t" \
  "ds_read_b64_tr_b16 v[200:201], v33 offset:16384\n\t" \
  "s_waitcnt lgkmcnt(6)\n\t" \
  "v_mfma_f32_16x16x32_bf16 v[240:243], v[212:215], v[84:87], v[240:243]\n\t" \
  "ds_read_b64_tr_b16 v[202:203], v33 offset:16896\n\t" \
  "v_mfma_f32_16x16x32_bf16 v[244:247], v[212:215], v[92:95], v[244:247]\n\t" \
  "ds_read_b64_tr_b16 v[204:205], v33 offset:20480\n\t" \
  "global_load_dwordx4 v[136:139], v12, s[90:91] offset:2048 sc1\n\t" \
  "v_mfma_f32_16x16x32_bf16 v[244:247], v[208:211], v[88:91], v[244:247]\n\t" \
  "ds_read_b64_tr_b16 v[206:207], v33 offset:20992\n\t" \
  "v_mfma_f32_16x16x32_bf16 v[248:251], v[216:219], v[80:83], v[248:251]\n\t" \
  "ds_read_b64_tr_b16 v[208:209], v34 offset:16384\n\t" \
  "s_waitcnt lgkmcnt(6)\n\t" \
  "v_mfma_f32_16x16x32_bf16 v[248:251], v[220:223], v[84:87], v[248:251]\n\t" \
  "ds_read_b64_tr_b16 v[210:211], v34 offset:16896\n\t" \
  "v_mfma_f32_16x16x32_bf16 v[252:255], v[220:223], v[92:95], v[252:255]\n\t" \
  "ds_read_b64_tr_b16 v[212:213], v34 offset:20480\n\t" \
  "global_load_dwordx4 v[140:143], v12, s[90:91] offset:3072 sc1\n\t" \
  "v_mfma_f32_16x16x32_bf16 v[252:255], v[216:219], v[88:91], v[252:255]\n\t" \
  "ds_read_b64_tr_b16 v[214:215], v34 offset:20992\n\t" \
  "s_waitcnt vmcnt(10)\n\t" \
  "v_mfma_f32_16x16x32_bf16 v[224:227], v[192:195], v[96:99], v[224:227]\n\t" \
  "ds_read_b64_tr_b16 v[216:217], v35 offset:16384\n\t" \
  "s_waitcnt lgkmcnt(6)\n\t" \
  "v_mfma_f32_16x16x32_bf16 v[224:227], v[196:199], v[100:103], v[224:227]\n\t" \
  "ds_read_b64_tr_b16 v[218:219], v35 offset:16896\n\t" \
  "s_waitcnt vmcnt(8)\n\t" \
  "v_mfma_f32_16x16x32_bf16 v[228:231], v[196:199], v[108:111], v[228:231]\n\t" \
  "ds_read_b64_tr_b16 v[220:221], v35 offset:20480\n\t" \
  "s_lshr_b64 s[90:91], %18, 52\n\t" \
  "s_and_b32 s90, s90, 15\n\t" \
  "s_lshl_b32 s90, s90, 14\n\t" \
  "s_add_u32 s90, s90, s92\n\t" \
  "s_addc_u32 s91, s93, 0\n\t" \
  "global_load_dwordx4 v[144:147], v12, s[90:91] offset:0 sc1\n\t" \
  "v_mfma_f32_16x16x32_bf16 v[228:231], v[192:195], v[104:107], v[228:231]\n\t" \
  "ds_read_b64_tr_b16 v[222:223], v35 offset:20992\n\t" \
  "v_mfma_f32_16x16x32_bf16 v[232:235], v[200:203], v[96:99], v[232:235]\n\t" \
  "ds_read_b64_tr_b16 v[192:193], v32 offset:24576\n\t" \
  "s_waitcnt lgkmcnt(6)\n\t" \
  "v_mfma_f32_16x16x32_bf16 v[232:235], v[204:207], v[100:103], v[232:235]\n\t" \
  "509:\n\t" \
  "s_cbranch_execz 26b\n\t" \
  "ds_read_b64_tr_b16 v[194:195], v32 offset:25088\n\t" \
  "v_mfma_f32_16x16x32_bf16 v[236:239], v[204:207], v[108:111], v[236:239]\n\t" \
  "ds_read_b64_tr_b16 v[196:197], v32 offset:28672\n\t" \
  "global_load_dwordx4 v[148:151], v12, s[90:91] offset:1024 sc1\n\t" \
  "ds_read_b32 v16, v56 offset:96\n\t" \
  "v_mfma_f32_16x16x32_bf16 v[236:239], v[200:203], v[104:107], v[236:239]\n\t" \
  "ds_read_b64_tr_b16 v[198:199], v32 offset:29184\n\t" \
  "v_mfma_f32_16x16x32_bf16 v[240:243], v[208:211], v[96:99], v[240:243]\n\t" \
  "ds_read_b64_tr_b16 v[200:201], v33 offset:24576\n\t" \
  "s_waitcnt lgkmcnt(7)\n\t" \
  "v_mfma_f32_16x16x32_bf16 v[240:243], v[212:215], v[100:103], v[240:243]\n\t" \
  "ds_read_b64_tr_b16 v[202:203], v33 offset:25088\n\t" \
  "v_mfma_f32_16x16x32_bf16 v[244:247], v[212:215], v[108:111], v[244:247]\n\t" \
  "ds_read_b64_tr_b16 v[204:205], v33 offset:28672\n\t" \
  "global_load_dwordx4 v[152:155], v12, s[90:91] offset:2048 sc1\n\t" \
  "v_mfma_f32_16x16x32_bf16 v[244:247], v[208:211], v[104:107], v[244:247]\n\t" \
  "ds_read_b64_tr_b16 v[206:207], v33 offset:29184\n\t" \
  "v_mfma_f32_16x16x32_bf16 v[248:251], v[216:219], v[96:99], v[248:251]\n\t" \
  "ds_read_b64_tr_b16 v[208:209], v34 offset:24576\n\t" \
  "s_waitcnt lgkmcnt(6)\n\t" \
  "v_mfma_f32_16x16x32_bf16 v[248:251], v[220:223], v[100:103], v[248:251]\n\t" \
  "ds_read_b64_tr_b16 v[210:211], v34 offset:25088\n\t" \
  "v_mfma_f32_16x16x32_bf16 v[252:255], v[220:223], v[108:111], v[252:255]\n\t" \
  "ds_read_b64_tr_b16 v[212:213], v34 offset:28672\n\t" \
  "global_load_dwordx4 v[156:159], v12, s[90:91] offset:3072 sc1\n\t" \
  "v_readfirstlane_b32 vcc_lo, v16\n\t" \
  "v_mfma_f32_16x16x32_bf16 v[252:255], v[216:219], v[104:107], v[252:255]\n\t" \
  "ds_read_b64_tr_b16 v[214:215], v34 offset:29184\n\t" \
  "s_cmp_ge_u32 vcc_lo, 2\n\t" \
  "s_cbranch_scc0 49f\n\t" \
  "50:\n\t" \
  "s_waitcnt vmcnt(10)\n\t" \
  "v_mfma_f32_16x16x32_bf16 v[224:227], v[192:195], v[112:115], v[224:227]\n\t" \
  "ds_read_b64_tr_b16 v[216:217], v35 offset:24576\n\t" \
  "s_waitcnt lgkmcnt(6)\n\t" \
  "v_mfma_f32_16x16x32_bf16 v[224:227], v[196:199], v[116:119], v[224:227]\n\t" \
  "ds_read_b64_tr_b16 v[218:219], v35 offset:25088\n\t" \
  "s_waitcnt vmcnt(8)\n\t" \
  "v_mfma_f32_16x16x32_bf16 v[228:231], v[196:199], v[124:127], v[228:231]\n\t" \
  "ds_read_b64_tr_b16 v[220:221], v35 offset:28672\n\t" \
  "s_lshr_b64 s[90:91], %18, 56\n\t" \
  "s_and_b32 s90, s90, 15\n\t" \
  "s_lshl_b32 s90, s90, 14\n\t" \
  "s_add_u32 s90, s90, s92\n\t" \
  "s_addc_u32 s91, s93, 0\n\t" \
  "global_load_dwordx4 v[160:163], v12, s[90:91] offset:0 sc1\n\t" \
  "v_mfma_f32_16x16x32_bf16 v[228:231], v[192:195], v[120:123], v[228:231]\n\t" \
  "ds_read_b64_tr_b16 v[222:223], v35 offset:29184\n\t" \
  "v_mfma_f32_16x16x32_bf16 v[232:235], v[200:203], v[112:115], v[232:235]\n\t" \
  "ds_read_b64_tr_b16 v[192:193], v32 offset:32768\n\t" \
  "s_waitcnt lgkmcnt(6)\n\t" \
  "v_mfma_f32_16x16x32_bf16 v[232:235], v[204:207], v[116:119], v[232:235]\n\t" \
  "ds_read_b64_tr_b16 v[194:195], v32 offset:33280\n\t" \
  "v_mfma_f32_16x16x32_bf16 v[236:239], v[204:207], v[124:127], v[236:239]\n\t" \
  "ds_read_b64_tr_b16 v[196:197], v32 offset:36864\n\t" \
  "global_load_dwordx4 v[164:167], v12, s[90:91] offset:1024 sc1\n\t" \
  "v_mfma_f32_16x16x32_bf16 v[236:239], v[200:203], v[120:123], v[236:239]\n\t" \
  "ds_read_b64_tr_b16 v[198:199], v32 offset:37376\n\t" \
  "v_mfma_f32_16x16x32_bf16 v[240:243], v[208:211], v[112:115], v[240:243]\n\t" \
  "ds_read_b64_tr_b16 v[200:201], v33 offset:32768\n\t" \
  "s_waitcnt lgkmcnt(6)\n\t" \
  "v_mfma_f32_16x16x32_bf16 v[240:243], v[212:215], v[116:119], v[240:243]\n\t" \
  "ds_read_b128 v[40:43], v56 offset:80\n\t" \
  "ds_read_b64_tr_b16 v[202:203], v33 offset:33280\n\t" \
  "v_mfma_f32_16x16x32_bf16 v[244:247], v[212:215], v[124:127], v[244:247]\n\t" \
  "ds_read_b64_tr_b16 v[204:205], v33 offset:36864\n\t" \
  "global_load_dwordx4 v[168:171], v12, s[90:91] offset:2048 sc1\n\t" \
  "v_mfma_f32_16x16x32_bf16 v[244:247], v[208:211], v[120:123], v[244:247]\n\t" \
  "ds_read_b64_tr_b16 v[206:207], v33 offset:37376\n\t" \
  "v_mfma_f32_16x16x32_bf16 v[248:251], v[216:219], v[112:115], v[248:251]\n\t" \
  "ds_read_b64_tr_b16 v[208:209], v34 offset:32768\n\t" \
  "s_waitcnt lgkmcnt(7)\n\t" \
  "v_mfma_f32_16x16x32_bf16 v[248:251], v[220:223], v[116:119], v[248:251]\n\t" \
  "ds_read_b64_tr_b16 v[210:211], v34 offset:33280\n\t" \
  "v_mfma_f32_16x16x32_bf16 v[252:255], v[220:223], v[124:127], v[252:255]\n\t" \
  "ds_read_b64_tr_b16 v[212:213], v34 offset:36864\n\t" \
  "global_load_dwordx4 v[172:175], v12, s[90:91] offset:3072 sc1\n\t" \
  "v_mfma_f32_16x16x32_bf16 v[252:255], v[216:219], v[120:123], v[252:255]\n\t" \
  "ds_read_b64_tr_b16 v[214:215], v34 offset:37376\n\t" \
  "s_waitcnt vmcnt(10)\n\t" \
  "v_mfma_f32_16x16x32_bf16 v[224:227], v[192:195], v[128:131], v[224:227]\n\t" \
  "s_waitcnt lgkmcnt(7)\n\t" \
  "v_min_u32 v40, v40, v41\n\t" \
  "v_min3_u32 v40, v40, v42, v43\n\t" \
  "ds_read_b64_tr_b16 v[216:217], v35 offset:32768\n\t" \
  "v_mfma_f32_16x16x32_bf16 v[224:227], v[196:199], v[132:135], v[224:227]\n\t" \
  "v_readfirstlane_b32 vcc_lo, v40\n\t" \
  "ds_read_b64_tr_b16 v[218:219], v35 offset:33280\n\t" \
  "s_waitcnt vmcnt(8)\n\t" \
  "v_mfma_f32_16x16x32_bf16 v[228:231], v[196:199], v[140:143], v[228:231]\n\t" \
  "s_cmp_ge_u32 vcc_lo, 16\n\t" \
  "s_cbranch_scc0 40f\n\t" \
  "41:\n\t" \
  "ds_read_b64_tr_b16 v[220:221], v35 offset:36864\n\t" \
  "s_lshr_b64 s[90:91], %18, 60\n\t" \
  "s_and_b32 s90, s90, 15\n\t" \
  "s_lshl_b32 s90, s90, 14\n\t" \
  "s_add_u32 s90, s90, s92\n\t" \
  "s_addc_u32 s91, s93, 0\n\t" \
  "global_load_dwordx4 v[176:179], v12, s[90:91] offset:0 sc1\n\t" \
  "v_mfma_f32_16x16x32_bf16 v[228:231], v[192:195], v[136:139], v[228:231]\n\t" \
  "ds_read_b64_tr_b16 v[222:223], v35 offset:37376\n\t" \
  "s_waitcnt lgkmcnt(6)\n\t" \
  "v_mfma_f32_16x16x32_bf16 v[232:235], v[200:203], v[128:131], v[232:235]\n\t" \
  "ds_read_b64_tr_b16 v[192:193], v36 offset:0\n\t" \
  "v_mfma_f32_16x16x32_bf16 v[232:235], v[204:207], v[132:135], v[232:235]\n\t" \
  "ds_read_b64_tr_b16 v[194:195], v36 offset:512\n\t" \
  "v_mfma_f32_16x16x32_bf16 v[236:239], v[204:207], v[140:143], v[236:239]\n\t" \
  "ds_read_b64_tr_b16 v[196:197], v36 offset:4096\n\t" \
  "global_load_dwordx4 v[180:183], v12, s[90:91] offset:1024 sc1\n\t" \
  "v_mfma_f32_16x16x32_bf16 v[236:239], v[200:203], v[136:139], v[236:239]\n\t" \
  "ds_read_b64_tr_b16 v[198:199], v36 offset:4608\n\t" \
  "v_mfma_f32_16x16x32_bf16 v[240:243], v[208:211], v[128:131], v[240:243]\n\t" \
  "ds_read_b64_tr_b16 v[200:201], v37 offset:0\n\t" \
  "s_waitcnt lgkmcnt(6)\n\t" \
  "v_mfma_f32_16x16x32_bf16 v[240:243], v[212:215], v[132:135], v[240:243]\n\t" \
  "ds_read_b64_tr_b16 v[202:203], v37 offset:512\n\t" \
  "v_mfma_f32_16x16x32_bf16 v[244:247], v[212:215], v[140:143], v[244:247]\n\t" \
  "ds_read_b64_tr_b16 v[204:205], v37 offset:4096\n\t" \
  "global_load_dwordx4 v[184:187], v12, s[90:91] offset:2048 sc1\n\t" \
  "v_mfma_f32_16x16x32_bf16 v[244:247], v[208:211], v[136:139], v[244:247]\n\t" \
  "ds_read_b64_tr_b16 v[206:207], v37 offset:4608\n\t" \
  "v_mfma_f32_16x16x32_bf16 v[248:251], v[216:219], v[128:131], v[248:251]\n\t" \
  "ds_read_b64_tr_b16 v[208:209], v38 offset:0\n\t" \
  "s_waitcnt lgkmcnt(6)\n\t" \
  "v_mfma_f32_16x16x32_bf16 v[248:251], v[220:223], v[132:135], v[248:251]\n\t" \
  "ds_read_b64_tr_b16 v[210:211], v38 offset:512\n\t" \
  "v_mfma_f32_16x16x32_bf16 v[252:255], v[220:223], v[140:143], v[252:255]\n\t" \
  "ds_read_b64_tr_b16 v[212:213], v38 offset:4096\n\t" \
  "global_load_dwordx4 v[188:191], v12, s[90:91] offset:3072 sc1\n\t" \
  "v_mfma_f32_16x16x32_bf16 v[252:255], v[216:219], v[136:139], v[252:255]\n\t" \
  "ds_read_b64_tr_b16 v[214:215], v38 offset:4608\n\t" \
  "s_waitcnt vmcnt(10)\n\t" \
  "v_mfma_f32_16x16x32_bf16 v[224:227], v[192:195], v[144:147], v[224:227]\n\t" \
  "ds_read_b64_tr_b16 v[216:217], v39 offset:0\n\t" \
  "s_waitcnt lgkmcnt(6)\n\t" \
  "v_mfma_f32_16x16x32_bf16 v[224:227], v[196:199], v[148:151], v[224:227]\n\t" \
  "ds_read_b64_tr_b16 v[218:219], v39 offset:512\n\t" \
  "s_waitcnt vmcnt(8)\n\t" \
  "v_mfma_f32_16x16x32_bf16 v[228:231], v[196:199], v[156:159], v[228:231]\n\t" \
  "ds_read_b64_tr_b16 v[220:221], v39 offset:4096\n\t" \
  "v_mfma_f32_16x16x32_bf16 v[228:231], v[192:195], v[152:155], v[228:231]\n\t" \
  "ds_read_b64_tr_b16 v[222:223], v39 offset:4608\n\t" \
  "v_mfma_f32_16x16x32_bf16 v[232:235], v[200:203], v[144:147], v[232:235]\n\t" \
  "ds_read_b64_tr_b16 v[192:193], v36 offset:8192\n\t" \
  "s_waitcnt lgkmcnt(6)\n\t" \
  "v_mfma_f32_16x16x32_bf16 v[232:235], v[204:207], v[148:151], v[232:235]\n\t" \
  "ds_read_b64_tr_b16 v[194:195], v36 offset:8704\n\t" \
  "v_mfma_f32_16x16x32_bf16 v[236:239], v[204:207], v[156:159], v[236:239]\n\t" \
  "ds_read_b64_tr_b16 v[196:197], v36 offset:12288\n\t" \
  "v_mfma_f32_16x16x32_bf16 v[236:239], v[200:203], v[152:155], v[236:239]\n\t" \
  "ds_read_b64_tr_b16 v[198:199], v36 offset:12800\n\t" \
  "v_mfma_f32_16x16x32_bf16 v[240:243], v[208:211], v[144:147], v[240:243]\n\t" \
  "ds_read_b64_tr_b16 v[200:201], v37 offset:8192\n\t" \
  "s_waitcnt lgkmcnt(6)\n\t" \
  "v_mfma_f32_16x16x32_bf16 v[240:243], v[212:215], v[148:151], v[240:243]\n\t" \
  "ds_read_b128 v[40:43], v56 offset:160\n\t" \
  "ds_read_b64_tr_b16 v[202:203], v37 offset:8704\n\t" \
  "v_mfma_f32_16x16x32_bf16 v[244:247], v[212:215], v[156:159], v[244:247]\n\t" \
  "ds_read_b64_tr_b16 v[204:205], v37 offset:12288\n\t" \
  "v_mfma_f32_16x16x32_bf16 v[244:247], v[208:211], v[152:155], v[244:247]\n\t" \
  "ds_read_b64_tr_b16 v[206:207], v37 offset:12800\n\t" \
  "v_mfma_f32_16x16x32_bf16 v[248:251], v[216:219], v[144:147], v[248:251]\n\t" \
  "ds_read_b64_tr_b16 v[208:209], v38 offset:8192\n\t" \
  "s_waitcnt lgkmcnt(7)\n\t" \
  "v_mfma_f32_16x16x32_bf16 v[248:251], v[220:223], v[148:151], v[248:251]\n\t" \
  "ds_read_b64_tr_b16 v[210:211], v38 offset:8704\n\t" \
  "v_mfma_f32_16x16x32_bf16 v[252:255], v[220:223], v[156:159], v[252:255]\n\t" \
  "ds_read_b64_tr_b16 v[212:213], v38 offset:12288\n\t" \
  "v_mfma_f32_16x16x32_bf16 v[252:255], v[216:219], v[152:155], v[252:255]\n\t" \
  "ds_read_b64_tr_b16 v[214:215], v38 offset:12800\n\t" \
  "s_waitcnt vmcnt(6)\n\t" \
  "v_mfma_f32_16x16x32_bf16 v[224:227], v[192:195], v[160:163], v[224:227]\n\t" \
  "s_waitcnt lgkmcnt(7)\n\t" \
  "v_min_u32 v40, v40, v41\n\t" \
  "v_min3_u32 v40, v40, v42, v43\n\t" \
  "ds_read_b64_tr_b16 v[216:217], v39 offset:8192\n\t" \
  "v_mfma_f32_16x16x32_bf16 v[224:227], v[196:199], v[164:167], v[224:227]\n\t" \
  "v_readfirstlane_b32 vcc_lo, v40\n\t" \
  "ds_read_b64_tr_b16 v[218:219], v39 offset:8704\n\t" \
  "s_waitcnt vmcnt(4)\n\t" \
  "v_mfma_f32_16x16x32_bf16 v[228:231], v[196:199], v[172:175], v[228:231]\n\t" \
  "s_cmp_ge_u32 vcc_lo, 16\n\t" \
  "s_cbranch_scc0 42f\n\t" \
  "43:\n\t" \
  "ds_read_b64_tr_b16 v[220:221], v39 offset:12288\n\t" \
  "v_mfma_f32_16x16x32_bf16 v[228:231], v[192:195], v[168:171], v[228:231]\n\t" \
  "ds_read_b64_tr_b16 v[222:223], v39 offset:12800\n\t" \
  "s_waitcnt lgkmcnt(6)\n\t" \
  "v_mfma_f32_16x16x32_bf16 v[232:235], v[200:203], v[160:163], v[232:235]\n\t" \
  "ds_read_b64_tr_b16 v[192:193], v32 offset:45312\n\t" \
  "v_mfma_f32_16x16x32_bf16 v[232:235], v[204:207], v[164:167], v[232:235]\n\t" \
  "ds_read_b64_tr_b16 v[194:195], v32 offset:45824\n\t" \
  "v_mfma_f32_16x16x32_bf16 v[236:239], v[204:207], v[172:175], v[236:239]\n\t" \
  "ds_read_b64_tr_b16 v[196:197], v32 offset:53760\n\t" \
  "v_mfma_f32_16x16x32_bf16 v[236:239], v[200:203], v[168:171], v[236:239]\n\t" \
  "ds_read_b64_tr_b16 v[198:199], v32 offset:54272\n\t" \
  "v_mfma_f32_16x16x32_bf16 v[240:243], v[208:211], v[160:163], v[240:243]\n\t" \
  "ds_read_b64_tr_b16 v[200:201], v33 offset:45312\n\t" \
  "s_waitcnt lgkmcnt(6)\n\t" \
  "v_mfma_f32_16x16x32_bf16 v[240:243], v[212:215], v[164:167], v[240:243]\n\t" \
  "ds_read_b64_tr_b16 v[202:203], v33 offset:45824\n\t" \
  "v_mfma_f32_16x16x32_bf16 v[244:247], v[212:215], v[172:175], v[244:247]\n\t" \
  "ds_read_b64_tr_b16 v[204:205], v33 offset:53760\n\t" \
  "v_mfma_f32_16x16x32_bf16 v[244:247], v[208:211], v[168:171], v[244:247]\n\t" \
  "ds_read_b64_tr_b16 v[206:207], v33 offset:54272\n\t" \
  "v_mfma_f32_16x16x32_bf16 v[248:251], v[216:219], v[160:163], v[248:251]\n\t" \
  "ds_read_b64_tr_b16 v[208:209], v34 offset:45312\n\t" \
  "s_waitcnt lgkmcnt(6)\n\t" \
  "v_mfma_f32_16x16x32_bf16 v[248:251], v[220:223], v[164:167], v[248:251]\n\t" \
  "ds_read_b64_tr_b16 v[210:211], v34 offset:45824\n\t" \
  "v_mfma_f32_16x16x32_bf16 v[252:255], v[220:223], v[172:175], v[252:255]\n\t" \
  "ds_read_b64_tr_b16 v[212:213], v34 offset:53760\n\t" \
  "v_mfma_f32_16x16x32_bf16 v[252:255], v[216:219], v[168:171], v[252:255]\n\t" \
  "ds_read_b64_tr_b16 v[214:215], v34 offset:54272\n\t" \
  "s_waitcnt vmcnt(2)\n\t" \
  "v_mfma_f32_16x16x32_bf16 v[224:227], v[192:195], v[176:179], v[224:227]\n\t" \
  "ds_read_b64_tr_b16 v[216:217], v35 offset:45312\n\t" \
  "s_waitcnt lgkmcnt(6)\n\t" \
  "v_mfma_f32_16x16x32_bf16 v[224:227], v[196:199], v[180:183], v[224:227]\n\t" \
  "ds_read_b64_tr_b16 v[218:219], v35 offset:45824\n\t" \
  "s_waitcnt vmcnt(0)\n\t" \
  "v_mfma_f32_16x16x32_bf16 v[228:231], v[196:199], v[188:191], v[228:231]\n\t" \
  "ds_read_b64_tr_b16 v[220:221], v35 offset:53760\n\t" \
  "v_mfma_f32_16x16x32_bf16 v[228:231], v[192:195], v[184:187], v[228:231]\n\t" \
  "ds_read_b64_tr_b16 v[222:223], v35 offset:54272\n\t" \
  "v_mfma_f32_16x16x32_bf16 v[232:235], v[200:203], v[176:179], v[232:235]\n\t" \
  "s_waitcnt lgkmcnt(5)\n\t" \
  "v_mfma_f32_16x16x32_bf16 v[232:235], v[204:207], v[180:183], v[232:235]\n\t" \
  "v_mfma_f32_16x16x32_bf16 v[236:239], v[204:207], v[188:191], v[236:239]\n\t" \
  "v_cvt_pk_bf16_f32 v224, v224, v225\n\t" \
  "v_cvt_pk_bf16_f32 v225, v226, v227\n\t" \
  "v_mfma_f32_16x16x32_bf16 v[236:239], v[200:203], v[184:187], v[236:239]\n\t" \
  "ds_write_b64 v57, v[224:225] offset:0\n\t" \
  "v_mfma_f32_16x16x32_bf16 v[240:243], v[208:211], v[176:179], v[240:243]\n\t" \
  "v_cvt_pk_bf16_f32 v228, v228, v229\n\t" \
  "v_cvt_pk_bf16_f32 v229, v230, v231\n\t" \
  "s_waitcnt lgkmcnt(2)\n\t" \
  "v_mfma_f32_16x16x32_bf16 v[240:243], v[212:215], v[180:183], v[240:243]\n\t" \
  "ds_write_b64 v57, v[228:229] offset:2048\n\t" \
  "v_mfma_f32_16x16x32_bf16 v[244:247], v[212:215], v[188:191], v[244:247]\n\t" \
  "v_cvt_pk_bf16_f32 v232, v232, v233\n\t" \
  "v_cvt_pk_bf16_f32 v233, v234, v235\n\t" \
  "v_mfma_f32_16x16x32_bf16 v[244:247], v[208:211], v[184:187], v[244:247]\n\t" \
  "ds_write_b64 v58, v[232:233] offset:0\n\t" \
  "v_mfma_f32_16x16x32_bf16 v[248:251], v[216:219], v[176:179], v[248:251]\n\t" \
  "v_cvt_pk_bf16_f32 v236, v236, v237\n\t" \
  "v_cvt_pk_bf16_f32 v237, v238, v239\n\t" \
  "s_waitcnt lgkmcnt(3)\n\t" \
  "v_mfma_f32_16x16x32_bf16 v[248:251], v[220:223], v[180:183], v[248:251]\n\t" \
  "ds_write_b64 v58, v[236:237] offset:2048\n\t" \
  "v_mfma_f32_16x16x32_bf16 v[252:255], v[220:223], v[188:191], v[252:255]\n\t" \
  "v_cvt_pk_bf16_f32 v240, v240, v241\n\t" \
  "v_cvt_pk_bf16_f32 v241, v242, v243\n\t" \
  "v_mfma_f32_16x16x32_bf16 v[252:255], v[216:219], v[184:187], v[252:255]\n\t" \
  "ds_write_b64 v59, v[240:241] offset:0\n\t" \
  "v_cvt_pk_bf16_f32 v244, v244, v245\n\t" \
  "v_cvt_pk_bf16_f32 v245, v246, v247\n\t" \
  "ds_write_b64 v59, v[244:245] offset:2048\n\t" \
  "v_cvt_pk_bf16_f32 v248, v248, v249\n\t" \
  "v_cvt_pk_bf16_f32 v249, v250, v251\n\t" \
  "ds_write_b64 v60, v[248:249] offset:0\n\t" \
  "s_nop 0\n\t" \
  "v_cvt_pk_bf16_f32 v252, v252, v253\n\t" \
  "v_cvt_pk_bf16_f32 v253, v254, v255\n\t" \
  "ds_write_b64 v60, v[252:253] offset:2048\n\t" \
  "ds_read_b128 v[64:67], v61 offset:0\n\t" \
  "ds_read_b128 v[68:71], v61 offset:1024\n\t" \
  "ds_read_b128 v[72:75], v61 offset:2048\n\t" \
  "ds_read_b128 v[76:79], v61 offset:3072\n\t" \
  "s_waitcnt lgkmcnt(3)\n\t" \
  "global_store_dwordx4 v62, v[64:67], %22 offset:-4096" OUTPOL "\n\t" \
  "s_waitcnt lgkmcnt(2)\n\t" \
  "global_store_dwordx4 v63, v[68:71], %22 offset:-4096" OUTPOL "\n\t" \
  "s_waitcnt lgkmcnt(1)\n\t" \
  "global_store_dwordx4 v62, v[72:75], %22 offset:0" OUTPOL "\n\t" \
  "s_waitcnt lgkmcnt(0)\n\t" \
  "global_store_dwordx4 v63, v[76:79], %22 offset:0" OUTPOL "\n\t" \
  "s_branch 19f\n\t" \
  "23:\n\t" \
  "510:\n\t" \
  "s_cbranch_execz 26b\n\t" \
  "s_waitcnt vmcnt(0)\n\t" \
  "s_mov_b64 exec, 1\n\t" \
  "ds_add_rtn_u32 v10, v56, v10 offset:16\n\t" \
  "s_mov_b32 s74, 16\n\t" \
  "s_waitcnt lgkmcnt(0)\n\t" \
  "v_readfirstlane_b32 vcc_lo, v10\n\t" \
  "s_mov_b64 exec, 0xffff\n\t" \
  "s_nop 0\n\t" \
  "s_cmp_eq_u32 vcc_lo, 3\n\t" \
  "s_cbranch_scc0 22f\n\t" \
  "global_store_dword v11, v55, %19 sc1\n\t" \
  "22:\n\t" \
  "s_mov_b64 exec, -1\n\t" \
  "s_setpc_b64 s[68:69]\n\t" \
  "7:\n\t" \
  "s_sleep 1\n\t" \
  "ds_read_b128 v[60:63], v56 offset:64\n\t" \
  "ds_read_b64_tr_b16 v[108:109], v5 offset:0\n\t" \
  "ds_read_b64_tr_b16 v[110:111], v5 offset:16\n\t" \
  "ds_read_b128 v[112:115], v6 offset:16384\n\t" \
  "ds_read_b128 v[116:119], v6 offset:15360\n\t" \
  "ds_read_b64_tr_b16 v[120:121], v5 offset:512\n\t" \
  "ds_read_b64_tr_b16 v[122:123], v5 offset:528\n\t" \
  "ds_read_b128 v[124:127], v6 offset:14336\n\t" \
  "ds_read_b64_tr_b16 v[128:129], v5 offset:1024\n\t" \
  "ds_read_b64_tr_b16 v[130:131], v5 offset:1040\n\t" \
  "ds_read_b128 v[132:135], v6 offset:13312\n\t" \
  "ds_read_b64_tr_b16 v[136:137], v5 offset:1536\n\t" \
  "ds_read_b64_tr_b16 v[138:139], v5 offset:1552\n\t" \
  "ds_read_b128 v[140:143], v6 offset:12288\n\t" \
  "ds_read_b64_tr_b16 v[144:145], v5 offset:2048\n\t" \
  "s_waitcnt lgkmcnt(0)\n\t" \
  "v_and_b32 v60, v60, v61\n\t" \
  "v_and_b32 v62, v62, v63\n\t" \
  "v_mov_b32 v12, %13\n\t" \
  "v_and_b32 v60, v60, v62\n\t" \
  "s_nop 0\n\t" \
  "v_readfirstlane_b32 vcc_lo, v60\n\t" \
  "s_nop 1\n\t" \
  "s_cmp_eq_u32 vcc_lo, 0\n\t" \
  "s_cbranch_scc1 7b\n\t" \
  "s_branch 6b\n\t" \
  "25:\n\t" \
  "511:\n\t" \
  "s_cbranch_execz 26b\n\t" \
  "global_load_dwordx4 v[64:67], v12, s[90:91] offset:0 sc1\n\t" \
  "global_load_dwordx4 v[68:71], v12, s[90:91] offset:1024 sc1\n\t" \
  "global_load_dwordx4 v[72:75], v12, s[90:91] offset:2048 sc1\n\t" \
  "global_load_dwordx4 v[76:79], v12, s[90:91] offset:3072 sc1\n\t" \
  "s_swappc_b64 s[70:71], s[68:69]\n\t" \
  "global_load_dwordx4 v[80:83], v12, s[90:91] offset:0 sc1\n\t" \
  "global_load_dwordx4 v[84:87], v12, s[90:91] offset:1024 sc1\n\t" \
  "global_load_dwordx4 v[88:91], v12, s[90:91] offset:2048 sc1\n\t" \
  "global_load_dwordx4 v[92:95], v12, s[90:91] offset:3072 sc1\n\t" \
  "s_swappc_b64 s[70:71], s[68:69]\n\t" \
  "36:\n\t" \
  "ds_read_b128 v[40:43], v56 offset:80\n\t" \
  "s_waitcnt lgkmcnt(0)\n\t" \
  "v_min_u32 v40, v40, v41\n\t" \
  "v_min3_u32 v40, v40, v42, v43\n\t" \
  "s_nop 1\n\t" \
  "v_readfirstlane_b32 vcc_lo, v40\n\t" \
  "s_nop 1\n\t" \
  "s_cmp_ge_u32 vcc_lo, 7\n\t" \
  "s_cbranch_scc0 36b\n\t" \
  "s_branch 37b\n\t" \
  "38:\n\t" \
  "ds_read_b128 v[40:43], v56 offset:80\n\t" \
  "s_waitcnt lgkmcnt(0)\n\t" \
  "v_min_u32 v40, v40, v41\n\t" \
  "v_min3_u32 v40, v40, v42, v43\n\t" \
  "s_nop 1\n\t" \
  "v_readfirstlane_b32 vcc_lo, v40\n\t" \
  "s_nop 1\n\t" \
  "s_cmp_ge_u32 vcc_lo, 13\n\t" \
  "s_cbranch_scc0 38b\n\t" \
  "s_branch 39b\n\t" \
  "40:\n\t" \
  "ds_read_b128 v[40:43], v56 offset:80\n\t" \
  "s_waitcnt lgkmcnt(0)\n\t" \
  "v_min_u32 v40, v40, v41\n\t" \
  "v_min3_u32 v40, v40, v42, v43\n\t" \
  "s_nop 1\n\t" \
  "v_readfirstlane_b32 vcc_lo, v40\n\t" \
  "s_nop 1\n\t" \
  "s_cmp_ge_u32 vcc_lo, 16\n\t" \
  "s_cbranch_scc0 40b\n\t" \
  "s_branch 41b\n\t" \
  "42:\n\t" \
  "ds_read_b128 v[40:43], v56 offset:160\n\t" \
  "s_waitcnt lgkmcnt(0)\n\t" \
  "v_min_u32 v40, v40, v41\n\t" \
  "v_min3_u32 v40, v40, v42, v43\n\t" \
  "s_nop 1\n\t" \
  "v_readfirstlane_b32 vcc_lo, v40\n\t" \
  "s_nop 1\n\t" \
  "s_cmp_ge_u32 vcc_lo, 16\n\t" \
  "s_cbranch_scc0 42b\n\t" \
  "s_branch 43b\n\t" \
  "46:\n\t" \
  "s_cmp_eq_u32 %13, 0\n\t" \
  "s_cbranch_scc1 48f\n\t" \
  "s_sleep 1\n\t" \
  "ds_read_b32 v16, v56 offset:96\n\t" \
  "s_waitcnt lgkmcnt(0)\n\t" \
  "v_readfirstlane_b32 vcc_lo, v16\n\t" \
  "s_nop 1\n\t" \
  "s_cmp_ge_u32 vcc_lo, 1\n\t" \
  "s_cbranch_scc0 46b\n\t" \
  "s_branch 47b\n\t" \
  "48:\n\t" \
  "v_bfe_u32 v19, v12, 4, 4\n\t" \
  "v_lshlrev_b32 v19, 2, v19\n\t" \
  "s_mov_b32 s90, 0\n\t" \
  "148:\n\t" \
  "global_load_dword v16, v19, %21 sc1\n\t" \
  "s_add_u32 s90, s90, 1\n\t" \
  "s_waitcnt vmcnt(0)\n\t" \
  "v_cmp_ne_u32 vcc, v55, v16\n\t" \
  "s_mov_b32 s88, 0xf7f7f7f7\n\t" \
  "s_mov_b32 s89, s88\n\t" \
  "s_and_b64 vcc, vcc, s[88:89]\n\t" \
  "s_cbranch_scc0 248f\n\t" \
  "s_sleep 2\n\t" \
  "s_cmp_lt_u32 s90, 0x400000\n\t" \
  "s_cbranch_scc1 148b\n\t" \
  "248:\n\t" \
  "v_mov_b32 v16, 1\n\t" \
  "ds_max_u32 v56, v16 offset:96\n\t" \
  "s_branch 47b\n\t" \
  "49:\n\t" \
  "s_cmp_eq_u32 %13, 0\n\t" \
  "s_cbranch_scc1 51f\n\t" \
  "s_sleep 1\n\t" \
  "ds_read_b32 v16, v56 offset:96\n\t" \
  "s_waitcnt lgkmcnt(0)\n\t" \
  "v_readfirstlane_b32 vcc_lo, v16\n\t" \
  "s_nop 1\n\t" \
  "s_cmp_ge_u32 vcc_lo, 2\n\t" \
  "s_cbranch_scc0 49b\n\t" \
  "s_branch 50b\n\t" \
  "51:\n\t" \
  "v_bfe_u32 v19, v12, 4, 4\n\t" \
  "v_lshlrev_b32 v19, 2, v19\n\t" \
  "s_mov_b32 s90, 0\n\t" \
  "151:\n\t" \
  "global_load_dword v16, v19, %21 sc1\n\t" \
  "s_add_u32 s90, s90, 1\n\t" \
  "s_waitcnt vmcnt(0)\n\t" \
  "v_cmp_ne_u32 vcc, v55, v16\n\t" \
  "s_mov_b32 s88, 0xf7f7f7f7\n\t" \
  "s_mov_b32 s89, s88\n\t" \
  "s_and_b64 vcc, vcc, -1\n\t" \
  "s_cbranch_scc0 251f\n\t" \
  "s_sleep 2\n\t" \
  "s_cmp_lt_u32 s90, 0x400000\n\t" \
  "s_cbranch_scc1 151b\n\t" \
  "251:\n\t" \
  "v_mov_b32 v16, 2\n\t" \
  "ds_max_u32 v56, v16 offset:96\n\t" \
  "s_branch 50b\n\t" \
  "19:\n\t"
#define C_CLOB "v10","v11","v12","v13","v18","v19","v20","v21","v22","v23","v24","v25","v26","v27","v28","v29","v30","v31","v32","v33","v34","v35","v36","v37","v38","v39","v40","v41","v42","v43","v44","v45","v46","v47","v48","v49","v50","v53","v55","v56","v57","v58","v59","v60","v61","v62","v63","v64","v65","v66","v67","v68","v69","v70","v71","v72","v73","v74","v75","v76","v77","v78","v79","v80","v81","v82","v83","v84","v85","v86","v87","v88","v89","v90","v91","v92","v93","v94","v95","v96","v97","v98","v99","v100","v101","v102","v103","v104","v105","v106","v107","v108","v109","v110","v111","v112","v113","v114","v115","v116","v117","v118","v119","v120","v121","v122","v123","v124","v125","v126","v127","v128","v129","v130","v131","v132","v133","v134","v135","v136","v137","v138","v139","v140","v141","v142","v143","v144","v145","v146","v147","v148","v149","v150","v151","v152","v153","v154","v155","v156","v157","v158","v159","v160","v161","v162","v163","v164","v165","v166","v167","v168","v169","v170","v171","v172","v173","v174","v175","v176","v177","v178","v179","v180","v181","v182","v183","v184","v185","v186","v187","v188","v189","v190","v191","v192","v193","v194","v195","v196","v197","v198","v199","v200","v201","v202","v203","v204","v205","v206","v207","v208","v209","v210","v211","v212","v213","v214","v215","v216","v217","v218","v219","v220","v221","v222","v223","v224","v225","v226","v227","v228","v229","v230","v231","v232","v233","v234","v235","v236","v237","v238","v239","v240","v241","v242","v243","v244","v245","v246","v247","v248","v249","v250","v251","v252","v253","v254","v255"

static_assert(KVP<13 && KVP>TGS+1, "");

#define TS(i)
#define TSDUMP()
#define TSB(i)
#define TSR(i)
#define TSH(i)
#define TSDECL()

typedef unsigned long long u64;
typedef u64 u64x8 __attribute__((ext_vector_type(8)));
typedef u64 u64x4 __attribute__((ext_vector_type(4)));
typedef unsigned u32x8 __attribute__((ext_vector_type(8)));
typedef unsigned u32x16 __attribute__((ext_vector_type(16)));
template<class T> __device__ __forceinline__ T sg(T v){
  if constexpr(sizeof(T)==4) return (T)__builtin_amdgcn_readfirstlane((unsigned)v); else return v; }
#define GP(T,v) ((T*)(__attribute__((address_space(1))) T*)(v))
__global__ __launch_bounds__(512,2) __attribute__((aligned(4096))) void fused_kernel(const u16* x_, const u16* wqkv_, const u16* kin_, const u16* vin_, const u16* shk_, const u16* hk_,
    const u16* wo_, const u16* qin_, const u16* sD_, u16* out_, u16* ws_, u16* unused_)
{
#define ST0()
#define ST1()
#define ARGS0() u64x8 ka; u64x4 kb; asm volatile("s_load_dwordx16 %0, %2, 0\n\ts_load_dwordx8 %1, %2, 64" : "=&s"(ka), "=&s"(kb) : "s"(__builtin_amdgcn_kernarg_segment_ptr()) : "memory")
#define ARGS() asm volatile("s_waitcnt lgkmcnt(0)" : "+s"(ka), "+s"(kb) :: "memory"); ST1() \
  const u16 *x=GP(const u16,ka[0]), *wqkv=GP(const u16,ka[1]), *kin=GP(const u16,ka[2]), *vin=GP(const u16,ka[3]), *shk=GP(const u16,ka[4]), *hk=GP(const u16,ka[5]), *wo=GP(const u16,ka[6]), *qin=GP(const u16,ka[7]), *sD=GP(const u16,kb[0]); \
  u16 *out=GP(u16,kb[1]), *o=GP(u16,kb[2]), *xrT=o+(size_t)S*DM, *yT=o+(size_t)2*S*DM; unsigned* flags=(unsigned*)(o+(size_t)6*S*DM); \
  (void)x; (void)wqkv; (void)kin; (void)vin; (void)shk; (void)hk; (void)wo; (void)qin; (void)sD; (void)out; (void)xrT; (void)yT; (void)flags
  ST0()
  __shared__ __attribute__((aligned(1024))) char smem[ALL_LDS];
  const int tid=threadIdx.x;
  const int w8=__builtin_amdgcn_readfirstlane(tid>>6);
  asm volatile("s_cmp_lg_u32 %0, 7\n\ts_cbranch_scc1 1f\n\ts_lshr_b32 vcc_lo, %2, 3\n\ts_mul_i32 vcc_lo, vcc_lo, %3\n\tv_and_b32 v254, 15, %1\n\tv_lshl_add_u32 v254, v254, 7, vcc_lo\n\ts_getpc_b64 vcc\n\tglobal_load_dword v255, v254, vcc\n1:"
      :: "s"(w8), "v"(tid), "s"(blockIdx.x), "n"(CODE_ST) : "memory", "scc", "vcc", "v254", "v255");
  LDS_AS unsigned* const ct=(LDS_AS unsigned*)(LDS_AS char*)(smem+CT_OFF);
  volatile LDS_AS unsigned* const vct=ct;
  auto all4=[&](int i)->bool{ u32x4 v=*(volatile LDS_AS u32x4*)(ct+i); return __builtin_amdgcn_readfirstlane(v[0]&v[1]&v[2]&v[3])!=0; };
#define ROTF(b) ((4+4*xr+4*xc+8*(b))&15)
#define BLOCK() int bid=blockIdx.x, w=w8&3; asm volatile("" : "+s"(bid), "+s"(w)); __builtin_assume(bid>=0 && bid<256 && w>=0 && w<4); \
  const int xcd=bid&7, idx=bid>>3, ra=idx&3, rbb=idx>>2, xr=xcd>>1, xc=xcd&1, rb=xr*4+ra, hp=xc*8+rbb, s0=rb*128, tm=rb, tn=hp^TNX; (void)s0; \
  auto korder=[&]()->unsigned long long{ const int r=KR; const unsigned long long N=0xfedca987654210ull, m=(1ull<<56)-1; const int sh=4*(r>=14? r-14 : r); \
    return (((N>>sh)|(N<<(56-sh)))&m) | ((idx&4)? 0xb3ull<<56 : 0x3bull<<56); }

  if(__builtin_expect(w8>=4,0)){
  BLOCK(); constexpr int WB=4; (void)WB;
  const int hd=w>>1, par=w&1;
  __builtin_amdgcn_s_setprio(CPRIO);
  {
    ARGS0(); LANES();
    if(w==0){ if(l<44) ct[l]=0u; if(idx==0) *(u32x4*)(smem+AUX+l*16)=u32x4{0,0,0,0}; }
    unsigned a0b=fr*128+((g^(fr&7))*16);
    const int r0=8*g+tq;
    unsigned bbb=r0*128+(((2*hd)^wswz(r0))*32)+8*tp;
    unsigned kA=AUX+(fr^3)*KVP_+w*64+(g&1)*16-(g>>1)*256, kB=AUX+(20+4*g-tq)*KVP_+w*64+8*tp;
    const int cch=127-4*idx-g;
    unsigned kvo=(xcd*32+8*w+tq)*16384+(cch>>4)*2048+(cch&15)*64+((tp^((cch>>2)&3))*16);
    unsigned sho=(xcd*128+32*w+fr)*8; const unsigned ctl=CT_OFF+4*w;
    u32x4 sel;
    { const unsigned b2=(g>>1)? 0x0302u : 0x0100u, sv=(fr&1)? (b2<<16|0x0c0cu) : (0x0c0c0000u|b2);
      #pragma unroll
      for(int i=0;i<4;i++) sel[i]= (fr>>1)==(g&1)*4+i ? sv : 0x0c0c0c0cu; }
    unsigned msk=0, lx=w*1024+l*16;
    #pragma unroll
    for(int e=0;e<4;e++){ const int d=4*g+e-fr; const unsigned pv=d<9, hv=par? pv : (unsigned)(d<1 || 4*g+e>7);
      msk|=((unsigned)(d+7)>7u?1u:0u)<<e | pv<<(4+e) | hv<<(8+e) | (d<=0?1u:0u)<<(12+e); }
    msk|=(unsigned)(g? l-16 : 48+((l-1)&15))<<26;
    const unsigned hb=__builtin_amdgcn_readfirstlane(rb==0 ? 0xcfa8965fu : 0u);
    unsigned ap=AUX+w*CV_W+(tq&1)*(CV_CP-4)+8+32*g+4*tq+8*tp;
    u32x4 bp;
    #pragma unroll
    for(int b=0;b<4;b++){ const int c=15+4*b-fr+g; bp[b]=c*64+((w^((c>>2)&3))*16); }
    asm volatile("" : "+v"(a0b), "+v"(bbb), "+v"(kA), "+v"(kB), "+v"(kvo), "+v"(sho), "+v"(msk), "+v"(lx), "+v"(ap), "+v"(bp), "+v"(sel));
    ARGS();
    const u16* ow=o+((size_t)(rb*16+hp)*16+par*2+hd)*512+2048;
    asm volatile("" : "+s"(shk));
    asm volatile(C_ASM : "+{v52}"(a0b), "+{v54}"(bbb), "+{v0}"(kA), "+{v1}"(kB), "+{v2}"(kvo), "+{v51}"(sho), "+{v3}"(msk), "+{v4}"(lx), "+{v5}"(ap), "+{v[6:9]}"(bp), "+{v[14:17]}"(sel)
       : "s"(shk), "s"(ctl), "s"(w), "s"(ow), "s"(hb), "s"(0.17677669529663687f*1.44269504088896341f), "{s[92:93]}"(o+(size_t)tm*16*8192), "s"(korder()),
         "s"(flags+FL_O+rb*512+hp), "s"(AUX+w*(CV_W+64)), "s"(flags+FL_O+(tm*16+tn)*32),
         "s"(out+(size_t)(tm*128+w*32+1)*2048+1024+tn*64), "s"(w<2? 16384+w*4096 : AUX+4352+w*CV_W), "s"(xrT), "s"(flags+FL_KV+xcd*1024+idx) : "memory", "scc", "vcc", "s68", "s69", "s70", "s71", "s74", "s75", "s76", "s77", "s88", "s89", "s90", "s91",
       C_CLOB);
    TSDECL();
    TS(20); TSDUMP();
  }
  return;
  }
  {
    ARGS0(); BLOCK(); constexpr int WB=0; (void)WB; LANES(); TSDECL();
    const unsigned vx=(l>>3)*4096+(((l&7)^(l>>3))*16), vxs=vx+(s0+32*w)*2048, vw=(l>>3)*6144+((((l&7)>>1)^((l>>4)&1))*32)+(l&1)*16+hp*128+w*98304, vw1=(vw^64)+49152-1024;
    u32x8 va; u32x2 vb; unsigned vsp;
    #pragma unroll
    for(int i=0;i<6;i++) va[i]=((i&1)? vw1 : vw)+(i>>1)*2048;
    va[6]=vxs; va[7]=vxs+32768-1024; vb[0]=vxs+2048; vb[1]=vxs+2048+32768-1024;
    vsp=vx+(rb? s0-16+w : 0)*2048;
    const unsigned sw1=w*1024, sw2=w*2048;
    unsigned rot=ROTF(rbb);
    asm volatile("" : "+v"(va), "+v"(vb), "+v"(vsp));
    asm volatile("" : "+s"(rot));
    unsigned vt=(w&1)? (xr*512+idx*16+(l>>1))*2048+(l&1)*1024 : (2*idx+(l&1))*6144+((l>>1)&3)*2048+xc*1024;
    const int kt0=idx*64; int kvs=KS0+KVSEL;
    unsigned kvl=(kt0+16*w+g)*2048+xcd*256+fr*16, l16=l*16;
    const bool hl= w==0 && kt0;
    unsigned c46=sg(w<2? 128u : w==2? 393216u : 0u), c49=sg(w<2? sw1 : (unsigned)ALL_LDS-96), c64=sg(hl? 8192u : 0u), c68=sg(hl? AUX : ALL_LDS-96);
    unsigned long long c50= w<2? ~0ull : w==2? 63ull : 1ull, c66= hl? ~0ull : 1ull;
    asm volatile("" : "+v"(vt), "+v"(kvl), "+v"(l16));
    asm volatile("" : "+s"(c46), "+s"(c49), "+s"(c64), "+s"(c68), "+s"(c50), "+s"(c66), "+s"(kvs));
    ARGS();
    TSB(16);
    asm volatile(H0_ASM : "+{s[40:41]}"(x), "+{s[42:43]}"(wqkv) : "{v[0:7]}"(va), "{v[8:9]}"(vb), "{v10}"(vsp), "{s52}"(rot), "{s53}"(sw2), "{s59}"(w)
      : "memory", "scc", "s76", "s77", "s78", "s79", "s82", "s83");
    const u16* spb= w==2? wqkv : x;
    const int hl3=(3-(int)((size_t)spb>>7))&7, roth=ROTF(hl3), xs=((hl3-ROTF(0))&7)-LEAD;
    unsigned i11=sg((w==2? roth+LEAD+1 : rot+1)&15), sbo, sst;
    if(w>=2){
      vsp=vt+hl3*128;
      touch(spb, vsp+((roth+(l>>3))&15)*c46, w==2? 0x3f3f3f3f3f3f3f3full>>(56-8*LEAD) : xs<=1? 0xffffffffull : 1ull);
    }
    TSB(17);
    unsigned gof=(((bid>>4)*128+64*(w&1)+4*(w>>1)+16*((l>>5)&1)+((l>>3)&3))*DM+(bid&15)*64)*2+(l16&127);
    unsigned wv[2];
    wv[0]=(((l>>5)*16+w*4+((l>>3)&3))*DM+tn*64)*2+((l16&127)^((l&16)*2)^((w&1)*64)); wv[1]=wv[0]+32*DM*2;
    unsigned zpa=(w>>1)*9216+(w&1)*512+l16, hda=AUX+w*CV_W+(l&1)*CV_CP+(l>>1)*16, yra=AUX+tq*(CV_W+64)+(g>>1)*128+(g&1)*32+tp*8+w*1024;
    constexpr unsigned FLB=12*S*DM;
    unsigned vc=(w&1)? FLB+(g? (1024+(g-1)*8192+(g==2? tm*16+tn : bid)*32)*4 : 0) : idx*CODE_ST+fr*128u;
    unsigned kfo=FLB+(FL_KV+bid*32+(l&31))*4, ofo= w==0? FLB+(FL_O+(tm*16+tn)*32+fr)*4 : FLB;
    asm volatile("" : "+v"(kfo), "+v"(ofo), "+v"(gof), "+v"(wv[0]), "+v"(wv[1]), "+v"(hda), "+v"(yra), "+v"(zpa), "+v"(vc));
    unsigned long long kt=korder();
    asm volatile("" : "+s"(kt));
#define HDBG_O
#define HDBG_I
#define H_IN "{v[0:7]}"(va), "{v[8:9]}"(vb), "{v10}"(vsp), "{v11}"(kvl), "{s[40:41]}"(x), "{s[42:43]}"(wqkv), "{s[44:45]}"(spb), "{s46}"(c46), "{s49}"(c49), "{s[50:51]}"(c50), "{s53}"(sw2), \
      "{s58}"(kvs), "{s59}"(w), "{s[60:61]}"(kin), "{s[62:63]}"(vin), "{s64}"(c64), "{s[66:67]}"(c66), "{s68}"(c68) HDBG_I
#define H_CLOB "memory", "scc", "vcc", "s76", "s77", "s78", "s79", "s80", "s81", "s82", "s83", "s84", "s85", "v20", "v21", "v22", "v253", "v254", "v255"
    asm volatile(H1_ASM : "+{s47}"(i11), "+{s52}"(rot), "={s54}"(sbo), "={s55}"(sst) HDBG_O : H_IN : H_CLOB);
    const u16* hrow=hk+(size_t)(bid*4+w)*S;
    u32x4 h0; unsigned dvu;
    asm volatile("s_load_dwordx4 %0, %2, 0\n\ts_load_dword %1, %3, %4" : "=&s"(h0), "=&s"(dvu) : "s"(hrow), "s"(sD), "s"(bid*8+(w&2)*2) : "memory");
    unsigned long long pc; asm volatile("s_getpc_b64 %0" : "=s"(pc)); pc&=~4095ull;
    __builtin_amdgcn_s_setprio(HP1);
    asm volatile(H_ASM : "+{s47}"(i11), "+{s52}"(rot), "+{s54}"(sbo), "+{s55}"(sst) HDBG_O
      : H_IN, "{v14}"(vc), "{v16}"(kfo), "{s56}"(sg(3u<<(kvs-1) | 1u<<15)),
        "{s57}"(sg(((w&1)? (1u<<TGS | 1u<<KVP | 1u<<14 | ((1u<<(xs+8)>>8)&~3u)) : 1u<<CPS)&~(unsigned)(c50>>32))), "{s[70:71]}"(o), "{s[72:73]}"(pc) : H_CLOB);
    asm volatile("s_waitcnt lgkmcnt(0)" : "+s"(h0), "+s"(dvu) :: "memory");
    u32x4 qv[4], wr[6];
    BAR(); TSB(14);
    __builtin_amdgcn_s_setprio(HP2);
    { const unsigned fa=AUX+w*CV_W+32;
      asm volatile("s_mov_b64 exec, -2\n\ts_add_u32 m0, %3, %4\n\ts_nop 0\n\tglobal_load_lds_dwordx4 %0, %1" LDPOL "\n\ts_mov_b32 m0, %3\n\ts_nop 0\n\tglobal_load_lds_dwordx4 %0, %2" LDPOL "\n\ts_mov_b64 exec, -1"
        :: "v"(l16), "s"(hrow), "s"(hrow-2), "s"(fa), "n"(CV_CP) : "memory", "scc");
      #pragma unroll
      for(int i=1;i<4;i++){ gldsn(hrow+i*512, l16, fa, CV_CP+i*1024); gldsn(hrow-2+i*512, l16, fa, i*1024); } }
    WAITV(8);
    vct[32+w]=1u;
    TSH(6);
    const unsigned tag=__builtin_amdgcn_readfirstlane(vct[26]); const bool kvok=__builtin_amdgcn_readfirstlane(vct[5])!=0;
    u32x4 hv;
    { const unsigned d0=(h0[0]&0xffff0000u)|(pack2(bflo(h0[0])+((w&1)? bfhi(dvu) : bflo(dvu)),0.f)&0xffffu);
      hv= l==4? u32x4{0,d0,h0[1],h0[2]} : l==5? u32x4{d0,h0[1],h0[2],h0[3]} : u32x4{0,0,0,0}; }
    asm volatile("s_mov_b64 exec, 63\n\tds_write_b128 %0, %1\n\ts_mov_b64 exec, %3\n\tds_write_b128 %2, %1 offset:7680\n\ts_mov_b64 exec, -1"
      :: "v"(hda), "v"(hv), "v"(zpa), "s"(0xffffffff00000000ull) : "memory");
#define QLD() asm volatile("global_load_dwordx4 %0, %4, %5" LDPOL "\n\tglobal_load_dwordx4 %1, %4, %6" LDPOL "\n\tglobal_load_dwordx4 %2, %4, %7" LDPOL "\n\tglobal_load_dwordx4 %3, %4, %8" LDPOL \
        : "=&v"(qv[0]), "=&v"(qv[1]), "=&v"(qv[2]), "=&v"(qv[3]) : "v"(gof), "s"(qin), "s"(qin+8*DM), "s"(qin+32*DM), "s"(qin+40*DM) : "memory")
    auto kvt=[&](){
      #pragma unroll
      for(int i=0;i<4;i++) gldsc(xrT+(size_t)bid*8192+w*2048+i*512, l16, sw2, (i&1)*9216+(i>>1)*1024); };
    if(!kvok) wait_flag((const unsigned*)((const char*)o+kfo), tag);
    kvt();
    TSH(7);
    auto wos=[&](int st){ const u16* b=wo+(size_t)((kt>>(4*st))&15)*64*DM; glds(b,wv[0],sw1,WOB+st*8192); glds(b,wv[1],sw1,WOB+st*8192+4096); };
    wos(0); wos(1);
    while(!all4(36)) __builtin_amdgcn_s_sleep(1);
    #pragma unroll
    for(int st=2;st<NE;st++) wos(st);
    WAITV(2*NE); WAITL();
    vct[16+w]=1u;
    TSH(8);
    __builtin_amdgcn_s_setprio(HP4);
    if(QP==1) QLD();
    #pragma unroll
    for(int st=NE;st<7;st++){ WAITV(W1); wos(st); }
    while(!all4(0)) __builtin_amdgcn_s_sleep(1);
    #pragma unroll
    for(int st=7;st<13;st++){ if(WOC==1? all4(28) : WOC==2? vct[4]>=4u : false) WAITV(W3); else WAITV(W2); wos(st); }
    #pragma unroll
    for(int st=0;st<3;st++){
      asm volatile("global_load_dwordx4 %0, %2, %4\n\tglobal_load_dwordx4 %1, %3, %4" : "=&v"(wr[2*st]), "=&v"(wr[2*st+1]) : "v"(wv[0]), "v"(wv[1]), "s"(wo+(size_t)((kt>>(52+4*st))&15)*64*DM) : "memory");
      if(st==0){ WAITV(14); vct[20+w]=7u; }
      WAITV(W3); }
    unsigned op;
    asm volatile("global_load_dword %0, %1, %2 sc1" : "=v"(op) : "v"(ofo), "s"(o) : "memory");
    if(QP==2) QLD();
    WAITV(QP==2? 11:7); vct[20+w]=13u;
    TSH(9);
    unsigned oseen=0;
    auto olevel=[&](unsigned v){ const unsigned long long ne=__ballot(v!=tag); const unsigned lv= ne==0 ? 2u : (ne&0xf7f7f7f7f7f7f7f7ull)==0 ? 1u : 0u;
      if(lv>oseen){ if(oseen==0){ TSH(10); } oseen=lv; if(l==0) __hip_atomic_fetch_max(ct+24, lv, __ATOMIC_RELAXED, __HIP_MEMORY_SCOPE_WORKGROUP); } };
    asm volatile("s_waitcnt vmcnt(0)" : "+v"(wr[0]), "+v"(wr[1]), "+v"(wr[2]), "+v"(wr[3]), "+v"(wr[4]), "+v"(wr[5]),
                                        "+v"(qv[0]), "+v"(qv[1]), "+v"(qv[2]), "+v"(qv[3]), "+v"(op) :: "memory");
    if(w==0) olevel(op);
    __builtin_amdgcn_s_setprio(HP3);
    bool gen= !(bid==0 && w==1);
    auto trygen=[&](){
      unsigned v[4];
      #pragma unroll
      for(int i=0;i<4;i++) v[i]=__hip_atomic_load(flags+FL_KV+(i*2+(l>>5))*1024+(l&31), __ATOMIC_RELAXED, __HIP_MEMORY_SCOPE_AGENT);
      if(__all(v[0]==tag && v[1]==tag && v[2]==tag && v[3]==tag)){ if(l==0) st4c(flags, tag); gen=true; } };
    if(!gen) trygen();
    float qf[32];
    #pragma unroll
    for(int i=0;i<16;i++){ qf[2*i]=bflo(qv[i>>2][i&3]); qf[2*i+1]=bfhi(qv[i>>2][i&3]); }
    #pragma unroll
    for(int i=0;i<32;i+=8) asm volatile("" : "+v"(qf[i]), "+v"(qf[i+1]), "+v"(qf[i+2]), "+v"(qf[i+3]), "+v"(qf[i+4]), "+v"(qf[i+5]), "+v"(qf[i+6]), "+v"(qf[i+7]));
    while(!all4(8)){
      if(w==0 && oseen<2) olevel(__hip_atomic_load((const unsigned*)((const char*)o+ofo), __ATOMIC_RELAXED, __HIP_MEMORY_SCOPE_AGENT));
      __builtin_amdgcn_s_sleep(OPOLLS);
    }
    TSH(11);
    __builtin_amdgcn_s_setprio(3);
    {
      const char* yb=smem+yra;
      s16x4 ya[4], yc[4];
      #pragma unroll
      for(int i=0;i<4;i++){ ya[i]=trread(yb+(i>>1)*512+(i&1)*64); yc[i]=trread(yb+(i>>1)*512+(i&1)*64+256); }
      #pragma unroll
      for(int i=0;i<4;i++) if(NOY!=1) st16c(yT+((size_t)(w*4+i)*256+bid)*512, l16, __builtin_bit_cast(u32x4,__builtin_shufflevector(ya[i],yc[i],0,1,2,3,4,5,6,7)));
    }
    #pragma unroll
    for(int i=0;i<6;i++) *(u32x4*)(smem+(i<4? i*4096 : AUX+4352+(i&1)*CV_W)+w*1024+l16)=wr[i];
    WAITL();
    vct[20+w]=16u;
    TSH(12);
    if(NOY){ for(int sp_=0; !gen && sp_<(1<<22); sp_++) trygen(); TS(20); TSDUMP(); return; }
    unsigned po=gof+(gof&~2047u);
    const u16* p0=yT+((size_t)(bid>>4)*256+(bid&15)*16)*512;
    unsigned y0=g*1024+w*256+((fr^(g&2))*16);
    asm volatile("" : "+v"(y0), "+v"(po));
    WAITV(0);
    if(l<4) st4c(flags+FL_Y+((w*4+l)*16+(bid>>4))*32+(bid&15), tag);
    TSH(13);
    if(w==0){ int sp_=0; while(oseen<2 && vct[24]<2u && ++sp_<(1<<22)){ olevel(__hip_atomic_load((const unsigned*)((const char*)o+ofo), __ATOMIC_RELAXED, __HIP_MEMORY_SCOPE_AGENT)); __builtin_amdgcn_s_sleep(8); } }
    if(w==0){
      const unsigned* fp=flags+FL_Y+bid*32+(l&15); int spins=0;
      while(vct[27]==0u) __builtin_amdgcn_s_sleep(2);
      while(true){
        unsigned v=__hip_atomic_load(fp, __ATOMIC_RELAXED, __HIP_MEMORY_SCOPE_AGENT);
        if(__all(v==tag) || ++spins>(1<<20)) break;
        __builtin_amdgcn_s_sleep(POLLS);
      }
      { u32x4 v; do{ v=*(volatile LDS_AS u32x4*)(ct+20); }while(__builtin_amdgcn_readfirstlane(v[0]&v[1]&v[2]&v[3]&16u)==0); }
      vct[25]=1u;
    } else { while(vct[25]==0u) __builtin_amdgcn_s_sleep(2); }
    TSH(14);
    {
      u32x4 yv[4];
      #pragma unroll
      for(int i=0;i<4;i++) gldsc(p0+i*2048, y0^(i*64), w*CV_W, AUX+i*1024);
      const char* ys=smem+AUX+w*CV_W+(l&7)*512;
      const int yi=((l>>3)^((l&7)*2))*16;
      WAITV(0);
      yv[0]=*(const u32x4*)(ys+yi); yv[1]=*(const u32x4*)(ys+yi+256); yv[2]=*(const u32x4*)(ys+(yi^128)); yv[3]=*(const u32x4*)(ys+(yi^128)+256);
      TSH(15);
      #pragma unroll
      for(int r=0;r<4;r++){
        u32x4 ov;
        #pragma unroll
        for(int e=0;e<4;e++){ const unsigned y2=yv[(r>>1)*2+(e>>1)][(r&1)*2+(e&1)]; ov[e]=pack2(qf[r*8+2*e]*bflo(y2), qf[r*8+2*e+1]*bfhi(y2)); }
        stos(out+((r&1)*8+(r>>1)*32)*2048, po, ov);
      }
    }
    for(int sp_=0; !gen && sp_<(1<<22); sp_++) trygen();
    TS(20); TSDUMP();
  }

}

extern "C" void kernel_launch(void* const* d_in, const int* in_sizes, int n_in, void* d_out, int out_size, void* d_ws, size_t ws_size, hipStream_t stream){
  const u16* q=(const u16*)d_in[0]; const u16* k=(const u16*)d_in[1]; const u16* v=(const u16*)d_in[2];
  const u16* shk=(const u16*)d_in[3]; const u16* hk=(const u16*)d_in[4]; const u16* sD=(const u16*)d_in[5];
  const u16* x=(const u16*)d_in[6]; const u16* wqkv=(const u16*)d_in[7]; const u16* wo=(const u16*)d_in[8];
  fused_kernel<<<256,512,0,stream>>>(x,wqkv,k,v,shk,hk,wo,q,sD,(u16*)d_out,(u16*)d_ws,(u16*)d_ws);
}
```

```cpp
#define STAMP
#define LITE
#define PX 2
#include <hip/hip_runtime.h>
#include <hip/hip_bf16.h>
#include <stdint.h>
#define DBG_OFF (28u<<20)
#define SHOWC

typedef unsigned short u16;
typedef short s16x4 __attribute__((ext_vector_type(4)));
typedef float f32x32 __attribute__((ext_vector_type(32)));
typedef unsigned u32x4 __attribute__((ext_vector_type(4)));
typedef unsigned u32x2 __attribute__((ext_vector_type(2)));
#define LDS_AS __attribute__((address_space(3)))

__device__ __forceinline__ float bflo(unsigned x){ return __uint_as_float(x<<16); }
__device__ __forceinline__ float bfhi(unsigned x){ return __uint_as_float(x&0xffff0000u); }
typedef float f32x2 __attribute__((ext_vector_type(2)));
typedef __bf16 bf16x2_t __attribute__((ext_vector_type(2)));
__device__ __forceinline__ unsigned pack2(float lo, float hi){ return __builtin_bit_cast(unsigned, __builtin_convertvector(f32x2{lo,hi}, bf16x2_t)); }

#define GLDS(name,pol) __device__ __forceinline__ void name(const void* sb, unsigned voff, unsigned ldsb, int imm){ \
  asm volatile("s_add_u32 m0, %2, %3\n\ts_nop 0\n\tglobal_load_lds_dwordx4 %0, %1" pol :: "v"(voff), "s"(sb), "s"(ldsb), "n"(imm) : "memory", "scc"); }
#define LDPOL " nt"
GLDS(glds,"") GLDS(gldsn,LDPOL) GLDS(gldsc," sc1")
#define PACE 63
__device__ __forceinline__ void gldsp(const void* sb, unsigned voff, unsigned ldsb, int imm){
  asm volatile("s_add_u32 m0, %2, %3\n\ts_waitcnt vmcnt(%4)\n\tglobal_load_lds_dwordx4 %0, %1" :: "v"(voff), "s"(sb), "s"(ldsb), "n"(imm), "n"(PACE) : "memory", "scc"); }
__device__ __forceinline__ void touch(const void* sb, unsigned voff, unsigned long long mask){
  asm volatile("s_mov_b64 exec, %2\n\tglobal_load_dword v255, %0, %1\n\ts_mov_b64 exec, -1" :: "v"(voff), "s"(sb), "s"(mask) : "memory", "v255"); }
__device__ __forceinline__ void st16c(void* sb, unsigned vo, u32x4 v){ asm volatile("global_store_dwordx4 %0, %1, %2 sc1\n\ts_nop 1" :: "v"(vo), "v"(v), "s"(sb) : "memory"); }
#define OUTPOL " sc1 nt"
__device__ __forceinline__ void stos(void* sb, unsigned vo, u32x4 v){ asm volatile("global_store_dwordx4 %0, %1, %2" OUTPOL "\n\ts_nop 1" :: "v"(vo), "v"(v), "s"(sb) : "memory"); }
__device__ __forceinline__ void stoa(void* p, u32x4 v){ asm volatile("global_store_dwordx4 %0, %1, off" OUTPOL "\n\ts_nop 1" :: "v"(p), "v"(v) : "memory"); }
__device__ __forceinline__ void st4c(void* p, unsigned v){ asm volatile("global_store_dword %0, %1, off sc1" :: "v"(p), "v"(v) : "memory"); }
__device__ __forceinline__ void wait_flag(const unsigned* p, unsigned tag){
  int spins=0;
  while(true){
    unsigned v=__hip_atomic_load(p, __ATOMIC_RELAXED, __HIP_MEMORY_SCOPE_AGENT);
    if(__all(v==tag)) break;
    __builtin_amdgcn_s_sleep(2);
    if(++spins>(1<<22)) break;
  }
}
#define WAITV(n) asm volatile("s_waitcnt vmcnt(%0)"::"n"(n):"memory")
#define WAITL() asm volatile("s_waitcnt lgkmcnt(0)":::"memory")
#define BAR() do{ asm volatile("s_waitcnt lgkmcnt(0)":::"memory"); __builtin_amdgcn_s_barrier(); asm volatile("":::"memory"); }while(0)
#define LANES() int l; asm volatile("v_mbcnt_lo_u32_b32 %0, -1, 0\n\tv_mbcnt_hi_u32_b32 %0, -1, %0" : "=v"(l)); __builtin_assume(l>=0 && l<64); const int g=l>>4, fr=l&15, tq=(l&15)>>2, tp=l&3; (void)g; (void)fr; (void)tq; (void)tp;

__device__ __forceinline__ s16x4 trread(const char* p){ return __builtin_amdgcn_ds_read_tr16_b64_v4i16((LDS_AS s16x4*)p); }
__device__ __forceinline__ int wswz(int row){ return ((row>>1)&1) | (((row>>3)&1)<<1); }
__device__ __forceinline__ u32x4 widen(u32x2 a, u32x2 b){
  auto r0=__builtin_amdgcn_permlane16_swap(a[0],b[0],false,false);
  auto r1=__builtin_amdgcn_permlane16_swap(a[1],b[1],false,false);
  return u32x4{r0[0],r1[0],r0[1],r1[1]};
}

constexpr int S=2048, DM=1024;
constexpr int K1_XB=144*128, K1_WB=3*64*128, K1_ST=K1_XB+K1_WB;
constexpr int AUX=3*K1_ST, KVP_=1040;
constexpr int WOB=22528, CV_W=8448, CV_CP=4224;
constexpr int CT_OFF=AUX+33*KVP_;
constexpr int ALL_LDS=CT_OFF+208+96;
constexpr unsigned WS_KV=8u<<20, WS_Y=16u<<20, WS_FL=24u<<20, WS_GEN=WS_FL+(1u<<20);
__device__ __forceinline__ unsigned flo(int kind, int c){ return WS_FL+kind*65536+(c>>6)*16384+(c&63)*128; }

#define CPRIO 2
#define CODE_ST 1610
#define LEAD 3
#define HP3 3
#define HP2 3
#define HP4 3
#define QP 1
#define TNX 1
#define HWS 99
#define HP1 0
#define HUNR 15
#define NE 2
#define WOC 0
#define NS 4
#define PTCH 1
#define GATE 0
#define WRD 2
#define EP 99
#define EP2 99
#define HP5 3
#define W0 6
#define OPD 8
#define WT0 99
#define W1 2
#define W2 2
#define W3 4
#define OPOLLS 1
#define POLLS 3
#define HPRE 0
#define CPS2 99
#define KR ((rbb+2*ra)%7)
#define NOY 0
#define YLDS 1
#define OLDS 0
#define KVSEL (rbb==3? 0 : idx%KSN)
#define KS0 1
#define KSN 3
#define KST 10
#define KVP 5
#define TGS 2
#define CPS 13
#define PLS 4
#define H0_ASM \
  "s_mul_i32 s76, s52, 0x60000\n\t" \
  "s_add_u32 s76, s42, s76\n\t" \
  "s_addc_u32 s77, s43, 0\n\t" \
  "s_add_u32 s82, s53, 86016\n\t" \
  "s_add_u32 m0, s82, 18432\n\t" \
  "s_lshl_b32 s78, s52, 7\n\t" \
  "global_load_lds_dwordx4 v0, s[76:77]\n\t" \
  "global_load_lds_dwordx4 v1, s[76:77] offset:1024\n\t" \
  "s_add_u32 s78, s40, s78\n\t" \
  "s_addc_u32 s79, s41, 0\n\t" \
  "s_add_u32 m0, s82, 2048\n\t" \
  "s_nop 0\n\t" \
  "global_load_lds_dwordx4 v6, s[78:79]\n\t" \
  "global_load_lds_dwordx4 v7, s[78:79] offset:1024\n\t" \
  "s_add_u32 m0, s82, 26624\n\t" \
  "s_nop 0\n\t" \
  "global_load_lds_dwordx4 v2, s[76:77]\n\t" \
  "global_load_lds_dwordx4 v3, s[76:77] offset:1024\n\t" \
  "s_add_u32 m0, s82, 10240\n\t" \
  "s_nop 0\n\t" \
  "global_load_lds_dwordx4 v8, s[78:79]\n\t" \
  "global_load_lds_dwordx4 v9, s[78:79] offset:1024\n\t" \
  "s_add_u32 m0, s82, 34816\n\t" \
  "s_nop 0\n\t" \
  "global_load_lds_dwordx4 v4, s[76:77]\n\t" \
  "global_load_lds_dwordx4 v5, s[76:77] offset:1024\n\t" \
  "s_cmp_gt_u32 s59, 1\n\t" \
  "s_cbranch_scc1 1f\n\t" \
  "s_lshr_b32 s83, s53, 1\n\t" \
  "s_add_u32 m0, s83, 86016\n\t" \
  "s_nop 0\n\t" \
  "global_load_lds_dwordx4 v10, s[78:79]\n\t" \
  "1:\n\t"
#define H1_ASM \
  "s_mov_b32 s55, 0\n\t" \
  "s_mov_b32 s54, 0\n\t" \
  "s_add_u32 s52, s52, 1\n\t" \
  "s_and_b32 s52, s52, 15\n\t" \
  "s_cmp_eq_u32 s58, 1\n\t" \
  "s_cbranch_scc1 1f\n\t" \
  "s_mul_i32 s76, s52, 0x60000\n\t" \
  "s_add_u32 s76, s42, s76\n\t" \
  "s_addc_u32 s77, s43, 0\n\t" \
  "s_lshl_b32 s78, s52, 7\n\t" \
  "s_add_u32 s78, s40, s78\n\t" \
  "s_addc_u32 s79, s41, 0\n\t" \
  "s_mul_i32 s80, s47, s46\n\t" \
  "s_add_u32 s80, s44, s80\n\t" \
  "s_addc_u32 s81, s45, 0\n\t" \
  "s_mov_b32 s82, s53\n\t" \
  "s_add_u32 m0, s82, 18432\n\t" \
  "s_add_u32 s52, s52, 1\n\t" \
  "global_load_lds_dwordx4 v0, s[76:77]\n\t" \
  "global_load_lds_dwordx4 v1, s[76:77] offset:1024\n\t" \
  "s_add_u32 m0, s82, 2048\n\t" \
  "s_and_b32 s52, s52, 15\n\t" \
  "global_load_lds_dwordx4 v6, s[78:79]\n\t" \
  "global_load_lds_dwordx4 v7, s[78:79] offset:1024\n\t" \
  "s_add_u32 m0, s82, 26624\n\t" \
  "s_add_u32 s47, s47, 1\n\t" \
  "global_load_lds_dwordx4 v2, s[76:77]\n\t" \
  "global_load_lds_dwordx4 v3, s[76:77] offset:1024\n\t" \
  "s_add_u32 m0, s82, 10240\n\t" \
  "s_and_b32 s47, s47, 15\n\t" \
  "global_load_lds_dwordx4 v8, s[78:79]\n\t" \
  "global_load_lds_dwordx4 v9, s[78:79] offset:1024\n\t" \
  "s_add_u32 m0, s82, 34816\n\t" \
  "s_and_b32 s83, s54, s51\n\t" \
  "global_load_lds_dwordx4 v4, s[76:77]\n\t" \
  "global_load_lds_dwordx4 v5, s[76:77] offset:1024\n\t" \
  "s_add_u32 m0, s83, s49\n\t" \
  "s_mov_b64 exec, s[50:51]\n\t" \
  "global_load_lds_dwordx4 v10, s[80:81]\n\t" \
  "s_mov_b64 exec, -1\n\t" \
  "s_mov_b32 s55, 1\n\t" \
  "s_mov_b32 s54, 43008\n\t" \
  "s_waitcnt vmcnt(11)\n\t" \
  "s_branch 2f\n\t" \
  "1:\n\t" \
  "s_mul_i32 s83, s59, 4160\n\t" \
  "v_add_u32 v20, 7152, v11\n\t" \
  "v_add_u32 v21, 14304, v11\n\t" \
  "v_add_u32 v22, 21456, v11\n\t" \
  "s_add_u32 m0, s83, 130064\n\t" \
  "s_nop 0\n\t" \
  "global_load_lds_dwordx4 v11, s[60:61]" LDPOL "\n\t" \
  "global_load_lds_dwordx4 v20, s[60:61] offset:1040" LDPOL "\n\t" \
  "global_load_lds_dwordx4 v21, s[60:61] offset:2080" LDPOL "\n\t" \
  "global_load_lds_dwordx4 v22, s[60:61] offset:3120" LDPOL "\n\t" \
  "s_add_u32 m0, s83, 146704\n\t" \
  "s_nop 0\n\t" \
  "global_load_lds_dwordx4 v11, s[62:63]" LDPOL "\n\t" \
  "global_load_lds_dwordx4 v20, s[62:63] offset:1040" LDPOL "\n\t" \
  "global_load_lds_dwordx4 v21, s[62:63] offset:2080" LDPOL "\n\t" \
  "global_load_lds_dwordx4 v22, s[62:63] offset:3120" LDPOL "\n\t" \
  "v_subrev_u32 v20, s64, v11\n\t" \
  "s_mov_b32 m0, s68\n\t" \
  "s_mov_b64 exec, s[66:67]\n\t" \
  "global_load_lds_dwordx4 v20, s[60:61]" LDPOL "\n\t" \
  "s_mov_b64 exec, -1\n\t" \
  "s_waitcnt vmcnt(9)\n\t" \
  "2:\n\t" \
  "s_waitcnt lgkmcnt(0)\n\t" \
  "s_barrier\n\t"
#define H_ASM \
  "s_mul_i32 s76, s52, 0x60000\n\t" \
  "s_add_u32 s76, s42, s76\n\t" \
  "s_addc_u32 s77, s43, 0\n\t" \
  "s_lshl_b32 s78, s52, 7\n\t" \
  "s_add_u32 s78, s40, s78\n\t" \
  "s_addc_u32 s79, s41, 0\n\t" \
  "s_mul_i32 s80, s47, s46\n\t" \
  "s_add_u32 s80, s44, s80\n\t" \
  "s_addc_u32 s81, s45, 0\n\t" \
  "s_add_u32 s82, s54, s53\n\t" \
  "s_cmp_eq_u32 s58, 2\n\t" \
  "s_cbranch_scc1 5f\n\t" \
  "s_branch 3f\n\t" \
  "1:\n\t" \
  "s_waitcnt vmcnt(11)\n\t" \
  "2:\n\t" \
  "s_barrier\n\t" \
  "3:\n\t" \
  "s_add_u32 s55, s55, 1\n\t" \
  "s_add_u32 m0, s82, 18432\n\t" \
  "s_add_u32 s52, s52, 1\n\t" \
  "global_load_lds_dwordx4 v0, s[76:77]\n\t" \
  "global_load_lds_dwordx4 v1, s[76:77] offset:1024\n\t" \
  "s_add_u32 m0, s82, 2048\n\t" \
  "s_and_b32 s52, s52, 15\n\t" \
  "global_load_lds_dwordx4 v6, s[78:79]\n\t" \
  "global_load_lds_dwordx4 v7, s[78:79] offset:1024\n\t" \
  "s_add_u32 m0, s82, 26624\n\t" \
  "s_add_u32 s47, s47, 1\n\t" \
  "global_load_lds_dwordx4 v2, s[76:77]\n\t" \
  "global_load_lds_dwordx4 v3, s[76:77] offset:1024\n\t" \
  "s_add_u32 m0, s82, 10240\n\t" \
  "s_and_b32 s47, s47, 15\n\t" \
  "global_load_lds_dwordx4 v8, s[78:79]\n\t" \
  "global_load_lds_dwordx4 v9, s[78:79] offset:1024\n\t" \
  "s_add_u32 m0, s82, 34816\n\t" \
  "s_and_b32 s83, s54, s51\n\t" \
  "global_load_lds_dwordx4 v4, s[76:77]\n\t" \
  "global_load_lds_dwordx4 v5, s[76:77] offset:1024\n\t" \
  "s_bitcmp1_b32 s57, s55\n\t" \
  "s_cbranch_scc1 10f\n\t" \
  "8:\n\t" \
  "s_add_u32 m0, s83, s49\n\t" \
  "s_mov_b64 exec, s[50:51]\n\t" \
  "global_load_lds_dwordx4 v10, s[80:81]\n\t" \
  "s_mov_b64 exec, -1\n\t" \
  "4:\n\t" \
  "s_mul_i32 s76, s52, 0x60000\n\t" \
  "s_add_u32 s76, s42, s76\n\t" \
  "s_addc_u32 s77, s43, 0\n\t" \
  "s_lshl_b32 s78, s52, 7\n\t" \
  "s_add_u32 s78, s40, s78\n\t" \
  "s_addc_u32 s79, s41, 0\n\t" \
  "s_mul_i32 s80, s47, s46\n\t" \
  "s_add_u32 s80, s44, s80\n\t" \
  "s_addc_u32 s81, s45, 0\n\t" \
  "s_add_u32 s54, s54, 43008\n\t" \
  "s_cmp_eq_u32 s54, 129024\n\t" \
  "s_cselect_b32 s54, 0, s54\n\t" \
  "s_add_u32 s82, s54, s53\n\t" \
  "s_bitcmp0_b32 s56, s55\n\t" \
  "s_cbranch_scc1 1b\n\t" \
  "s_cmp_eq_u32 s55, 15\n\t" \
  "s_cbranch_scc1 19f\n\t" \
  "s_cmp_eq_u32 s55, s58\n\t" \
  "s_cbranch_scc1 3b\n\t" \
  "s_waitcnt vmcnt(11)\n\t" \
  "s_barrier\n\t" \
  "5:\n\t" \
  "s_mul_i32 s83, s59, 4160\n\t" \
  "v_add_u32 v20, 7152, v11\n\t" \
  "v_add_u32 v21, 14304, v11\n\t" \
  "v_add_u32 v22, 21456, v11\n\t" \
  "s_add_u32 m0, s83, 130064\n\t" \
  "s_nop 0\n\t" \
  "global_load_lds_dwordx4 v11, s[60:61]" LDPOL "\n\t" \
  "global_load_lds_dwordx4 v20, s[60:61] offset:1040" LDPOL "\n\t" \
  "global_load_lds_dwordx4 v21, s[60:61] offset:2080" LDPOL "\n\t" \
  "global_load_lds_dwordx4 v22, s[60:61] offset:3120" LDPOL "\n\t" \
  "s_add_u32 m0, s83, 146704\n\t" \
  "s_nop 0\n\t" \
  "global_load_lds_dwordx4 v11, s[62:63]" LDPOL "\n\t" \
  "global_load_lds_dwordx4 v20, s[62:63] offset:1040" LDPOL "\n\t" \
  "global_load_lds_dwordx4 v21, s[62:63] offset:2080" LDPOL "\n\t" \
  "global_load_lds_dwordx4 v22, s[62:63] offset:3120" LDPOL "\n\t" \
  "v_subrev_u32 v20, s64, v11\n\t" \
  "s_mov_b32 m0, s68\n\t" \
  "s_mov_b64 exec, s[66:67]\n\t" \
  "global_load_lds_dwordx4 v20, s[60:61]" LDPOL "\n\t" \
  "s_mov_b64 exec, -1\n\t" \
  "s_waitcnt vmcnt(9)\n\t" \
  "s_branch 2b\n\t" \
  "10:\n\t" \
  "s_cmp_eq_u32 s59, 2\n\t" \
  "s_cbranch_scc0 11f\n\t" \
  "s_mov_b64 exec, 0xffff\n\t" \
  "global_load_dword v255, v14, s[72:73]\n\t" \
  "s_mov_b64 exec, -1\n\t" \
  "s_branch 4b\n\t" \
  "11:\n\t" \
  "s_cmp_eq_u32 s55, 2\n\t" \
  "s_cbranch_scc0 12f\n\t" \
  "global_load_dword v254, v14, s[70:71] sc1\n\t" \
  "s_branch 4b\n\t" \
  "12:\n\t" \
  "s_cmp_eq_u32 s55, 4\n\t" \
  "s_cbranch_scc0 15f\n\t" \
  "global_load_dword v253, v13, s[70:71] sc1\n\t" \
  "s_branch 4b\n\t" \
  "15:\n\t" \
  "s_cmp_eq_u32 s55, 5\n\t" \
  "s_cbranch_scc0 13f\n\t" \
  "v_readfirstlane_b32 s84, v254\n\t" \
  "s_mov_b32 m0, 0x400000\n\t" \
  "s_waitcnt vmcnt(10)\n\t" \
  "s_add_u32 s84, s84, 1\n\t" \
  "v_mov_b32 v254, s84\n\t" \
  "16:\n\t" \
  "v_cmp_eq_u32 vcc, v253, v254\n\t" \
  "s_nop 1\n\t" \
  "s_and_b32 s84, vcc_lo, 0x3fffffff\n\t" \
  "s_bcnt1_i32_b32 s85, s84\n\t" \
  "s_cmp_ge_u32 s85, 15\n\t" \
  "s_cbranch_scc1 17f\n\t" \
  "s_sub_u32 m0, m0, 1\n\t" \
  "s_cbranch_scc1 17f\n\t" \
  "global_load_dword v253, v13, s[70:71] sc1\n\t" \
  "s_waitcnt vmcnt(0)\n\t" \
  "s_branch 16b\n\t" \
  "17:\n\t" \
  "s_and_b32 s84, s84, 0xaaaaaaaa\n\t" \
  "s_bcnt1_i32_b32 s84, s84\n\t" \
  "s_and_b32 s84, s84, 8\n\t" \
  "s_lshl_b32 s84, s84, 10\n\t" \
  "v_xor_b32 v16, s84, v16\n\t" \
  "v_mov_b32 v20, s84\n\t" \
  "v_mov_b32 v253, 163344\n\t" \
  "ds_write_b32 v253, v254 offset:104\n\t" \
  "ds_write_b32 v253, v20 offset:24\n\t" \
  "s_waitcnt lgkmcnt(0)\n\t" \
  "s_branch 8b\n\t" \
  "13:\n\t" \
  "s_cmp_eq_u32 s55, 14\n\t" \
  "s_cbranch_scc0 14f\n\t" \
  "global_load_dword v253, v16, s[70:71] sc1\n\t" \
  "s_branch 4b\n\t" \
  "14:\n\t" \
  "s_mov_b32 exec_hi, 0\n\t" \
  "global_load_dword v255, v10, s[44:45]\n\t" \
  "s_mov_b64 exec, -1\n\t" \
  "s_branch 4b\n\t" \
  "19:\n\t" \
  "s_waitcnt vmcnt(11)\n\t" \
  "s_cmp_lg_u32 s59, 3\n\t" \
  "s_cbranch_scc1 18f\n\t" \
  "v_cmp_ne_u32 vcc, v253, v254\n\t" \
  "v_mov_b32 v253, 163364\n\t" \
  "s_nop 1\n\t" \
  "s_cmp_eq_u64 vcc, 0\n\t" \
  "s_cselect_b32 s83, 1, 0\n\t" \
  "v_mov_b32 v254, s83\n\t" \
  "ds_write_b32 v253, v254\n\t" \
  "18:\n\t"
#define C_ASM \
  "s_branch 29f\n\t" \
  "21:\n\t" \
  "s_mov_b64 s[68:69], %27\n\t" \
  "s_and_b32 s74, s68, 15\n\t" \
  "s_andn2_b32 s68, s68, 15\n\t" \
  "s_load_dword vcc_lo, s[68:69], 0 glc\n\t" \
  "s_and_b32 s75, s74, 1\n\t" \
  "s_lshl_b32 s70, s74, 9\n\t" \
  "s_lshl_b32 s71, s75, 13\n\t" \
  "s_or_b32 s70, s70, s71\n\t" \
  "s_waitcnt lgkmcnt(0)\n\t" \
  "s_or_b32 s68, s68, s70\n\t" \
  "s_add_u32 vcc_lo, vcc_lo, 1\n\t" \
  "v_mov_b32 v18, vcc_lo\n\t" \
  "s_memtime s[70:71]\n\t" \
  "s_waitcnt lgkmcnt(0)\n\t" \
  "s_load_dword s76, s[68:69], 16384 glc\n\t" \
  "s_load_dword s77, s[68:69], 16512 glc\n\t" \
  "s_waitcnt lgkmcnt(0)\n\t" \
  "s_xor_b32 s68, s68, 0x2000\n\t" \
  "s_memtime s[90:91]\n\t" \
  "s_waitcnt lgkmcnt(0)\n\t" \
  "s_load_dword s76, s[68:69], 16384 glc\n\t" \
  "s_load_dword s77, s[68:69], 16512 glc\n\t" \
  "s_waitcnt lgkmcnt(0)\n\t" \
  "s_sub_u32 vcc_lo, s90, s70\n\t" \
  "s_memtime s[70:71]\n\t" \
  "s_waitcnt lgkmcnt(0)\n\t" \
  "s_sub_u32 s70, s70, s90\n\t" \
  "s_cmp_gt_u32 vcc_lo, s70\n\t" \
  "s_cselect_b32 s70, 1, 0\n\t" \
  "s_xor_b32 s70, s70, s75\n\t" \
  "s_lshl1_add_u32 s70, s74, s70\n\t" \
  "s_lshl_b32 s70, s70, 2\n\t" \
  "v_mov_b32 v19, s70\n\t" \
  "s_and_b32 s68, s68, 0xffffc000\n\t" \
  "s_mov_b64 exec, 1\n\t" \
  "global_store_dword v19, v18, s[68:69] offset:256 sc1\n\t" \
  "s_branch 26f\n\t" \
  "28:\n\t" \
  "s_cmp_lg_u64 %27, 0\n\t" \
  "s_cbranch_scc1 21b\n\t" \
  "s_mov_b64 exec, 0\n\t" \
  "s_cmp_eq_u32 %13, 0\n\t" \
  "s_cbranch_scc1 500f\n\t" \
  "s_cmp_eq_u32 %13, 1\n\t" \
  "s_cbranch_scc1 501f\n\t" \
  "s_cmp_eq_u32 %13, 2\n\t" \
  "s_cbranch_scc1 502f\n\t" \
  "s_branch 503f\n\t" \
  "26:\n\t" \
  "s_mov_b64 exec, -1\n\t" \
  "s_add_u32 s88, s88, 4\n\t" \
  "s_addc_u32 s89, s89, 0\n\t" \
  "s_setpc_b64 s[88:89]\n\t" \
  ".p2align 12\n\t" \
  "29:\n\t" \
  "s_bitcmp0_b32 %13, 0\n\t" \
  "s_cbranch_scc1 8f\n\t" \
  "global_load_dwordx2 v[10:11], v51, %11\n\t" \
  "global_load_dwordx2 v[12:13], v51, %11 offset:128\n\t" \
  "v_xor_b32 v53, 64, v52\n\t" \
  "v_xor_b32 v55, 32, v54\n\t" \
  "v_add_u32 v56, 43008, v52\n\t" \
  "v_add_u32 v58, 43008, v54\n\t" \
  "v_xor_b32 v57, 64, v56\n\t" \
  "v_xor_b32 v59, 32, v58\n\t" \
  "v_add_u32 v60, 86016, v52\n\t" \
  "v_add_u32 v62, 86016, v54\n\t" \
  "v_xor_b32 v61, 64, v60\n\t" \
  "v_xor_b32 v63, 32, v62\n\t" \
  "s_mov_b32 vcc_hi, 0x05040100\n\t" \
  "s_waitcnt vmcnt(2)\n\t" \
  "s_barrier\n\t" \
  "ds_read_b64_tr_b16 v[64:65], v62 offset:26624\n\t" \
  "ds_read_b64_tr_b16 v[66:67], v62 offset:27136\n\t" \
  "ds_read_b128 v[68:71], v60 offset:10240\n\t" \
  "ds_read_b64_tr_b16 v[72:73], v62 offset:30720\n\t" \
  "ds_read_b64_tr_b16 v[74:75], v62 offset:31232\n\t" \
  "ds_read_b128 v[76:79], v61 offset:10240\n\t" \
  "ds_read_b128 v[80:83], v61 offset:12288\n\t" \
  "ds_read_b128 v[84:87], v60 offset:12288\n\t" \
  "ds_read_b128 v[88:91], v60 offset:14336\n\t" \
  "ds_read_b128 v[92:95], v61 offset:14336\n\t" \
  "ds_read_b128 v[96:99], v61 offset:16384\n\t" \
  "ds_read_b128 v[100:103], v60 offset:16384\n\t" \
  "ds_read_b64_tr_b16 v[104:105], v63 offset:26624\n\t" \
  "ds_read_b64_tr_b16 v[106:107], v63 offset:27136\n\t" \
  "ds_read_b64_tr_b16 v[108:109], v63 offset:30720\n\t" \
  "ds_read_b64_tr_b16 v[110:111], v63 offset:31232\n\t" \
  "s_waitcnt lgkmcnt(13)\n\t" \
  "v_mfma_f32_16x16x32_bf16 v[184:187], v[64:67], v[68:71], 0\n\t" \
  "ds_read_b64_tr_b16 v[112:113], v62 offset:34816\n\t" \
  "ds_read_b64_tr_b16 v[114:115], v62 offset:35328\n\t" \
  "s_waitcnt lgkmcnt(12)\n\t" \
  "v_mfma_f32_16x16x32_bf16 v[184:187], v[72:75], v[76:79], v[184:187]\n\t" \
  "ds_read_b64_tr_b16 v[116:117], v62 offset:38912\n\t" \
  "ds_read_b64_tr_b16 v[118:119], v62 offset:39424\n\t" \
  "s_waitcnt lgkmcnt(13)\n\t" \
  "v_mfma_f32_16x16x32_bf16 v[192:195], v[72:75], v[80:83], 0\n\t" \
  "ds_read_b128 v[120:123], v60 offset:0\n\t" \
  "s_waitcnt lgkmcnt(13)\n\t" \
  "v_mfma_f32_16x16x32_bf16 v[192:195], v[64:67], v[84:87], v[192:195]\n\t" \
  "ds_read_b128 v[124:127], v61 offset:0\n\t" \
  "s_waitcnt lgkmcnt(13)\n\t" \
  "v_mfma_f32_16x16x32_bf16 v[200:203], v[64:67], v[88:91], 0\n\t" \
  "ds_read_b64_tr_b16 v[128:129], v63 offset:34816\n\t" \
  "s_waitcnt lgkmcnt(13)\n\t" \
  "v_mfma_f32_16x16x32_bf16 v[200:203], v[72:75], v[92:95], v[200:203]\n\t" \
  "ds_read_b64_tr_b16 v[130:131], v63 offset:35328\n\t" \
  "s_waitcnt lgkmcnt(13)\n\t" \
  "v_mfma_f32_16x16x32_bf16 v[208:211], v[72:75], v[96:99], 0\n\t" \
  "ds_read_b64_tr_b16 v[132:133], v63 offset:38912\n\t" \
  "s_waitcnt lgkmcnt(9)\n\t" \
  "v_mfma_f32_16x16x32_bf16 v[208:211], v[64:67], v[100:103], v[208:211]\n\t" \
  "ds_read_b64_tr_b16 v[134:135], v63 offset:39424\n\t" \
  "v_mfma_f32_16x16x32_bf16 v[188:191], v[104:107], v[68:71], 0\n\t" \
  "ds_read_b64_tr_b16 v[136:137], v62 offset:18432\n\t" \
  "v_mfma_f32_16x16x32_bf16 v[188:191], v[108:111], v[76:79], v[188:191]\n\t" \
  "ds_read_b64_tr_b16 v[138:139], v62 offset:18944\n\t" \
  "v_mfma_f32_16x16x32_bf16 v[196:199], v[108:111], v[80:83], 0\n\t" \
  "ds_read_b64_tr_b16 v[140:141], v62 offset:22528\n\t" \
  "v_mfma_f32_16x16x32_bf16 v[196:199], v[104:107], v[84:87], v[196:199]\n\t" \
  "ds_read_b64_tr_b16 v[142:143], v62 offset:23040\n\t" \
  "v_mfma_f32_16x16x32_bf16 v[204:207], v[104:107], v[88:91], 0\n\t" \
  "ds_read_b64_tr_b16 v[144:145], v63 offset:18432\n\t" \
  "v_mfma_f32_16x16x32_bf16 v[204:207], v[108:111], v[92:95], v[204:207]\n\t" \
  "ds_read_b64_tr_b16 v[146:147], v63 offset:18944\n\t" \
  "v_mfma_f32_16x16x32_bf16 v[212:215], v[108:111], v[96:99], 0\n\t" \
  "ds_read_b64_tr_b16 v[148:149], v63 offset:22528\n\t" \
  "v_mfma_f32_16x16x32_bf16 v[212:215], v[104:107], v[100:103], v[212:215]\n\t" \
  "ds_read_b64_tr_b16 v[150:151], v63 offset:23040\n\t" \
  "s_waitcnt lgkmcnt(7)\n\t" \
  "v_mfma_f32_16x16x32_bf16 v[216:219], v[68:71], v[112:115], 0\n\t" \
  "v_mfma_f32_16x16x32_bf16 v[216:219], v[76:79], v[116:119], v[216:219]\n\t" \
  "v_mfma_f32_16x16x32_bf16 v[224:227], v[80:83], v[116:119], 0\n\t" \
  "v_mfma_f32_16x16x32_bf16 v[224:227], v[84:87], v[112:115], v[224:227]\n\t" \
  "v_mfma_f32_16x16x32_bf16 v[232:235], v[88:91], v[112:115], 0\n\t" \
  "v_mfma_f32_16x16x32_bf16 v[232:235], v[92:95], v[116:119], v[232:235]\n\t" \
  "v_mfma_f32_16x16x32_bf16 v[240:243], v[96:99], v[116:119], 0\n\t" \
  "v_mfma_f32_16x16x32_bf16 v[240:243], v[100:103], v[112:115], v[240:243]\n\t" \
  "v_mfma_f32_16x16x32_bf16 v[248:251], v[120:123], v[112:115], 0\n\t" \
  "v_mfma_f32_16x16x32_bf16 v[248:251], v[124:127], v[116:119], v[248:251]\n\t" \
  "v_mfma_f32_16x16x32_bf16 v[252:255], v[120:123], v[128:131], 0\n\t" \
  "v_mfma_f32_16x16x32_bf16 v[252:255], v[124:127], v[132:135], v[252:255]\n\t" \
  "v_mfma_f32_16x16x32_bf16 v[220:223], v[76:79], v[132:135], 0\n\t" \
  "v_mfma_f32_16x16x32_bf16 v[220:223], v[68:71], v[128:131], v[220:223]\n\t" \
  "s_waitcnt lgkmcnt(0)\n\t" \
  "s_getpc_b64 s[88:89]\n\t" \
  "s_branch 28b\n\t" \
  "s_barrier\n\t" \
  "v_mfma_f32_16x16x32_bf16 v[228:231], v[84:87], v[128:131], 0\n\t" \
  "ds_read_b64_tr_b16 v[72:73], v54 offset:26624\n\t" \
  "v_mfma_f32_16x16x32_bf16 v[228:231], v[80:83], v[132:135], v[228:231]\n\t" \
  "ds_read_b64_tr_b16 v[74:75], v54 offset:27136\n\t" \
  "v_mfma_f32_16x16x32_bf16 v[236:239], v[92:95], v[132:135], 0\n\t" \
  "ds_read_b128 v[64:67], v52 offset:10240\n\t" \
  "v_mfma_f32_16x16x32_bf16 v[236:239], v[88:91], v[128:131], v[236:239]\n\t" \
  "ds_read_b64_tr_b16 v[108:109], v54 offset:30720\n\t" \
  "v_mfma_f32_16x16x32_bf16 v[244:247], v[100:103], v[128:131], 0\n\t" \
  "ds_read_b64_tr_b16 v[110:111], v54 offset:31232\n\t" \
  "v_mfma_f32_16x16x32_bf16 v[244:247], v[96:99], v[132:135], v[244:247]\n\t" \
  "ds_read_b128 v[104:107], v53 offset:10240\n\t" \
  "v_mfma_f32_16x16x32_bf16 v[152:155], v[136:139], v[68:71], 0\n\t" \
  "ds_read_b128 v[112:115], v53 offset:12288\n\t" \
  "v_mfma_f32_16x16x32_bf16 v[152:155], v[140:143], v[76:79], v[152:155]\n\t" \
  "ds_read_b128 v[116:119], v52 offset:12288\n\t" \
  "v_mfma_f32_16x16x32_bf16 v[160:163], v[140:143], v[80:83], 0\n\t" \
  "ds_read_b128 v[120:123], v52 offset:14336\n\t" \
  "v_mfma_f32_16x16x32_bf16 v[160:163], v[136:139], v[84:87], v[160:163]\n\t" \
  "ds_read_b128 v[124:127], v53 offset:14336\n\t" \
  "v_mfma_f32_16x16x32_bf16 v[168:171], v[136:139], v[88:91], 0\n\t" \
  "ds_read_b128 v[128:131], v53 offset:16384\n\t" \
  "v_mfma_f32_16x16x32_bf16 v[168:171], v[140:143], v[92:95], v[168:171]\n\t" \
  "ds_read_b128 v[132:135], v52 offset:16384\n\t" \
  "v_mfma_f32_16x16x32_bf16 v[176:179], v[140:143], v[96:99], 0\n\t" \
  "ds_read_b64_tr_b16 v[140:141], v55 offset:26624\n\t" \
  "v_mfma_f32_16x16x32_bf16 v[176:179], v[136:139], v[100:103], v[176:179]\n\t" \
  "ds_read_b64_tr_b16 v[142:143], v55 offset:27136\n\t" \
  "v_mfma_f32_16x16x32_bf16 v[156:159], v[144:147], v[68:71], 0\n\t" \
  "ds_read_b64_tr_b16 v[136:137], v55 offset:30720\n\t" \
  "v_mfma_f32_16x16x32_bf16 v[156:159], v[148:151], v[76:79], v[156:159]\n\t" \
  "ds_read_b64_tr_b16 v[138:139], v55 offset:31232\n\t" \
  "v_mfma_f32_16x16x32_bf16 v[164:167], v[148:151], v[80:83], 0\n\t" \
  "ds_read_b64_tr_b16 v[68:69], v54 offset:34816\n\t" \
  "v_mfma_f32_16x16x32_bf16 v[164:167], v[144:147], v[84:87], v[164:167]\n\t" \
  "ds_read_b64_tr_b16 v[70:71], v54 offset:35328\n\t" \
  "v_mfma_f32_16x16x32_bf16 v[172:175], v[144:147], v[88:91], 0\n\t" \
  "ds_read_b64_tr_b16 v[76:77], v54 offset:38912\n\t" \
  "v_mfma_f32_16x16x32_bf16 v[172:175], v[148:151], v[92:95], v[172:175]\n\t" \
  "ds_read_b64_tr_b16 v[78:79], v54 offset:39424\n\t" \
  "v_mfma_f32_16x16x32_bf16 v[180:183], v[148:151], v[96:99], 0\n\t" \
  "ds_read_b128 v[80:83], v52 offset:0\n\t" \
  "v_mfma_f32_16x16x32_bf16 v[180:183], v[144:147], v[100:103], v[180:183]\n\t" \
  "ds_read_b128 v[84:87], v53 offset:0\n\t" \
  "s_waitcnt lgkmcnt(7)\n\t" \
  "v_mfma_f32_16x16x32_bf16 v[184:187], v[72:75], v[64:67], v[184:187]\n\t" \
  "ds_read_b64_tr_b16 v[88:89], v55 offset:34816\n\t" \
  "v_mfma_f32_16x16x32_bf16 v[184:187], v[108:111], v[104:107], v[184:187]\n\t" \
  "ds_read_b64_tr_b16 v[90:91], v55 offset:35328\n\t" \
  "v_mfma_f32_16x16x32_bf16 v[192:195], v[108:111], v[112:115], v[192:195]\n\t" \
  "ds_read_b64_tr_b16 v[92:93], v55 offset:38912\n\t" \
  "v_mfma_f32_16x16x32_bf16 v[192:195], v[72:75], v[116:119], v[192:195]\n\t" \
  "ds_read_b64_tr_b16 v[94:95], v55 offset:39424\n\t" \
  "v_mfma_f32_16x16x32_bf16 v[200:203], v[72:75], v[120:123], v[200:203]\n\t" \
  "ds_read_b64_tr_b16 v[96:97], v54 offset:18432\n\t" \
  "v_mfma_f32_16x16x32_bf16 v[200:203], v[108:111], v[124:127], v[200:203]\n\t" \
  "ds_read_b64_tr_b16 v[98:99], v54 offset:18944\n\t" \
  "v_mfma_f32_16x16x32_bf16 v[208:211], v[108:111], v[128:131], v[208:211]\n\t" \
  "ds_read_b64_tr_b16 v[148:149], v54 offset:22528\n\t" \
  "v_mfma_f32_16x16x32_bf16 v[208:211], v[72:75], v[132:135], v[208:211]\n\t" \
  "ds_read_b64_tr_b16 v[150:151], v54 offset:23040\n\t" \
  "v_mfma_f32_16x16x32_bf16 v[188:191], v[140:143], v[64:67], v[188:191]\n\t" \
  "ds_read_b64_tr_b16 v[100:101], v55 offset:18432\n\t" \
  "s_waitcnt lgkmcnt(7)\n\t" \
  "v_mfma_f32_16x16x32_bf16 v[188:191], v[136:139], v[104:107], v[188:191]\n\t" \
  "ds_read_b64_tr_b16 v[102:103], v55 offset:18944\n\t" \
  "v_mfma_f32_16x16x32_bf16 v[196:199], v[136:139], v[112:115], v[196:199]\n\t" \
  "ds_read_b64_tr_b16 v[144:145], v55 offset:22528\n\t" \
  "v_mfma_f32_16x16x32_bf16 v[196:199], v[140:143], v[116:119], v[196:199]\n\t" \
  "ds_read_b64_tr_b16 v[146:147], v55 offset:23040\n\t" \
  "v_mfma_f32_16x16x32_bf16 v[204:207], v[140:143], v[120:123], v[204:207]\n\t" \
  "v_mfma_f32_16x16x32_bf16 v[204:207], v[136:139], v[124:127], v[204:207]\n\t" \
  "v_mfma_f32_16x16x32_bf16 v[212:215], v[136:139], v[128:131], v[212:215]\n\t" \
  "v_mfma_f32_16x16x32_bf16 v[212:215], v[140:143], v[132:135], v[212:215]\n\t" \
  "v_mfma_f32_16x16x32_bf16 v[216:219], v[64:67], v[68:71], v[216:219]\n\t" \
  "v_mfma_f32_16x16x32_bf16 v[216:219], v[104:107], v[76:79], v[216:219]\n\t" \
  "v_mfma_f32_16x16x32_bf16 v[224:227], v[112:115], v[76:79], v[224:227]\n\t" \
  "v_mfma_f32_16x16x32_bf16 v[224:227], v[116:119], v[68:71], v[224:227]\n\t" \
  "v_mfma_f32_16x16x32_bf16 v[232:235], v[120:123], v[68:71], v[232:235]\n\t" \
  "v_mfma_f32_16x16x32_bf16 v[232:235], v[124:127], v[76:79], v[232:235]\n\t" \
  "v_mfma_f32_16x16x32_bf16 v[240:243], v[128:131], v[76:79], v[240:243]\n\t" \
  "v_mfma_f32_16x16x32_bf16 v[240:243], v[132:135], v[68:71], v[240:243]\n\t" \
  "v_mfma_f32_16x16x32_bf16 v[248:251], v[80:83], v[68:71], v[248:251]\n\t" \
  "v_mfma_f32_16x16x32_bf16 v[248:251], v[84:87], v[76:79], v[248:251]\n\t" \
  "v_mfma_f32_16x16x32_bf16 v[252:255], v[80:83], v[88:91], v[252:255]\n\t" \
  "s_waitcnt lgkmcnt(0)\n\t" \
  "v_mfma_f32_16x16x32_bf16 v[252:255], v[84:87], v[92:95], v[252:255]\n\t" \
  "v_mfma_f32_16x16x32_bf16 v[220:223], v[104:107], v[92:95], v[220:223]\n\t" \
  "v_mfma_f32_16x16x32_bf16 v[220:223], v[64:67], v[88:91], v[220:223]\n\t" \
  "s_barrier\n\t" \
  "v_mfma_f32_16x16x32_bf16 v[228:231], v[116:119], v[88:91], v[228:231]\n\t" \
  "ds_read_b64_tr_b16 v[108:109], v58 offset:26624\n\t" \
  "v_mfma_f32_16x16x32_bf16 v[228:231], v[112:115], v[92:95], v[228:231]\n\t" \
  "ds_read_b64_tr_b16 v[110:111], v58 offset:27136\n\t" \
  "v_mfma_f32_16x16x32_bf16 v[236:239], v[124:127], v[92:95], v[236:239]\n\t" \
  "ds_read_b128 v[72:75], v56 offset:10240\n\t" \
  "v_mfma_f32_16x16x32_bf16 v[236:239], v[120:123], v[88:91], v[236:239]\n\t" \
  "ds_read_b64_tr_b16 v[136:137], v58 offset:30720\n\t" \
  "v_mfma_f32_16x16x32_bf16 v[244:247], v[132:135], v[88:91], v[244:247]\n\t" \
  "ds_read_b64_tr_b16 v[138:139], v58 offset:31232\n\t" \
  "v_mfma_f32_16x16x32_bf16 v[244:247], v[128:131], v[92:95], v[244:247]\n\t" \
  "ds_read_b128 v[140:143], v57 offset:10240\n\t" \
  "v_mfma_f32_16x16x32_bf16 v[152:155], v[96:99], v[64:67], v[152:155]\n\t" \
  "ds_read_b128 v[68:71], v57 offset:12288\n\t" \
  "v_mfma_f32_16x16x32_bf16 v[152:155], v[148:151], v[104:107], v[152:155]\n\t" \
  "ds_read_b128 v[76:79], v56 offset:12288\n\t" \
  "v_mfma_f32_16x16x32_bf16 v[160:163], v[148:151], v[112:115], v[160:163]\n\t" \
  "ds_read_b128 v[80:83], v56 offset:14336\n\t" \
  "v_mfma_f32_16x16x32_bf16 v[160:163], v[96:99], v[116:119], v[160:163]\n\t" \
  "ds_read_b128 v[84:87], v57 offset:14336\n\t" \
  "v_mfma_f32_16x16x32_bf16 v[168:171], v[96:99], v[120:123], v[168:171]\n\t" \
  "ds_read_b128 v[88:91], v57 offset:16384\n\t" \
  "v_mfma_f32_16x16x32_bf16 v[168:171], v[148:151], v[124:127], v[168:171]\n\t" \
  "ds_read_b128 v[92:95], v56 offset:16384\n\t" \
  "v_mfma_f32_16x16x32_bf16 v[176:179], v[148:151], v[128:131], v[176:179]\n\t" \
  "ds_read_b64_tr_b16 v[148:149], v59 offset:26624\n\t" \
  "v_mfma_f32_16x16x32_bf16 v[176:179], v[96:99], v[132:135], v[176:179]\n\t" \
  "ds_read_b64_tr_b16 v[150:151], v59 offset:27136\n\t" \
  "v_mfma_f32_16x16x32_bf16 v[156:159], v[100:103], v[64:67], v[156:159]\n\t" \
  "ds_read_b64_tr_b16 v[96:97], v59 offset:30720\n\t" \
  "v_mfma_f32_16x16x32_bf16 v[156:159], v[144:147], v[104:107], v[156:159]\n\t" \
  "ds_read_b64_tr_b16 v[98:99], v59 offset:31232\n\t" \
  "v_mfma_f32_16x16x32_bf16 v[164:167], v[144:147], v[112:115], v[164:167]\n\t" \
  "ds_read_b64_tr_b16 v[64:65], v58 offset:34816\n\t" \
  "v_mfma_f32_16x16x32_bf16 v[164:167], v[100:103], v[116:119], v[164:167]\n\t" \
  "ds_read_b64_tr_b16 v[66:67], v58 offset:35328\n\t" \
  "v_mfma_f32_16x16x32_bf16 v[172:175], v[100:103], v[120:123], v[172:175]\n\t" \
  "ds_read_b64_tr_b16 v[104:105], v58 offset:38912\n\t" \
  "v_mfma_f32_16x16x32_bf16 v[172:175], v[144:147], v[124:127], v[172:175]\n\t" \
  "ds_read_b64_tr_b16 v[106:107], v58 offset:39424\n\t" \
  "v_mfma_f32_16x16x32_bf16 v[180:183], v[144:147], v[128:131], v[180:183]\n\t" \
  "ds_read_b128 v[112:115], v56 offset:0\n\t" \
  "v_mfma_f32_16x16x32_bf16 v[180:183], v[100:103], v[132:135], v[180:183]\n\t" \
  "ds_read_b128 v[116:119], v57 offset:0\n\t" \
  "s_waitcnt lgkmcnt(7)\n\t" \
  "v_mfma_f32_16x16x32_bf16 v[184:187], v[108:111], v[72:75], v[184:187]\n\t" \
  "ds_read_b64_tr_b16 v[120:121], v59 offset:34816\n\t" \
  "v_mfma_f32_16x16x32_bf16 v[184:187], v[136:139], v[140:143], v[184:187]\n\t" \
  "ds_read_b64_tr_b16 v[122:123], v59 offset:35328\n\t" \
  "v_mfma_f32_16x16x32_bf16 v[192:195], v[136:139], v[68:71], v[192:195]\n\t" \
  "ds_read_b64_tr_b16 v[124:125], v59 offset:38912\n\t" \
  "v_mfma_f32_16x16x32_bf16 v[192:195], v[108:111], v[76:79], v[192:195]\n\t" \
  "ds_read_b64_tr_b16 v[126:127], v59 offset:39424\n\t" \
  "v_mfma_f32_16x16x32_bf16 v[200:203], v[108:111], v[80:83], v[200:203]\n\t" \
  "ds_read_b64_tr_b16 v[128:129], v58 offset:18432\n\t" \
  "v_mfma_f32_16x16x32_bf16 v[200:203], v[136:139], v[84:87], v[200:203]\n\t" \
  "ds_read_b64_tr_b16 v[130:131], v58 offset:18944\n\t" \
  "v_mfma_f32_16x16x32_bf16 v[208:211], v[136:139], v[88:91], v[208:211]\n\t" \
  "ds_read_b64_tr_b16 v[144:145], v58 offset:22528\n\t" \
  "v_mfma_f32_16x16x32_bf16 v[208:211], v[108:111], v[92:95], v[208:211]\n\t" \
  "ds_read_b64_tr_b16 v[146:147], v58 offset:23040\n\t" \
  "v_mfma_f32_16x16x32_bf16 v[188:191], v[148:151], v[72:75], v[188:191]\n\t" \
  "ds_read_b64_tr_b16 v[100:101], v59 offset:18432\n\t" \
  "s_waitcnt lgkmcnt(7)\n\t" \
  "v_mfma_f32_16x16x32_bf16 v[188:191], v[96:99], v[140:143], v[188:191]\n\t" \
  "ds_read_b64_tr_b16 v[102:103], v59 offset:18944\n\t" \
  "v_mfma_f32_16x16x32_bf16 v[196:199], v[96:99], v[68:71], v[196:199]\n\t" \
  "ds_read_b64_tr_b16 v[132:133], v59 offset:22528\n\t" \
  "v_mfma_f32_16x16x32_bf16 v[196:199], v[148:151], v[76:79], v[196:199]\n\t" \
  "ds_read_b64_tr_b16 v[134:135], v59 offset:23040\n\t" \
  "v_mfma_f32_16x16x32_bf16 v[204:207], v[148:151], v[80:83], v[204:207]\n\t" \
  "v_mfma_f32_16x16x32_bf16 v[204:207], v[96:99], v[84:87], v[204:207]\n\t" \
  "v_mfma_f32_16x16x32_bf16 v[212:215], v[96:99], v[88:91], v[212:215]\n\t" \
  "v_mfma_f32_16x16x32_bf16 v[212:215], v[148:151], v[92:95], v[212:215]\n\t" \
  "v_mfma_f32_16x16x32_bf16 v[216:219], v[72:75], v[64:67], v[216:219]\n\t" \
  "v_mfma_f32_16x16x32_bf16 v[216:219], v[140:143], v[104:107], v[216:219]\n\t" \
  "v_mfma_f32_16x16x32_bf16 v[224:227], v[68:71], v[104:107], v[224:227]\n\t" \
  "v_mfma_f32_16x16x32_bf16 v[224:227], v[76:79], v[64:67], v[224:227]\n\t" \
  "v_mfma_f32_16x16x32_bf16 v[232:235], v[80:83], v[64:67], v[232:235]\n\t" \
  "v_mfma_f32_16x16x32_bf16 v[232:235], v[84:87], v[104:107], v[232:235]\n\t" \
  "v_mfma_f32_16x16x32_bf16 v[240:243], v[88:91], v[104:107], v[240:243]\n\t" \
  "v_mfma_f32_16x16x32_bf16 v[240:243], v[92:95], v[64:67], v[240:243]\n\t" \
  "v_mfma_f32_16x16x32_bf16 v[248:251], v[112:115], v[64:67], v[248:251]\n\t" \
  "v_mfma_f32_16x16x32_bf16 v[248:251], v[116:119], v[104:107], v[248:251]\n\t" \
  "v_mfma_f32_16x16x32_bf16 v[252:255], v[112:115], v[120:123], v[252:255]\n\t" \
  "s_waitcnt lgkmcnt(0)\n\t" \
  "v_mfma_f32_16x16x32_bf16 v[252:255], v[116:119], v[124:127], v[252:255]\n\t" \
  "v_mfma_f32_16x16x32_bf16 v[220:223], v[140:143], v[124:127], v[220:223]\n\t" \
  "v_mfma_f32_16x16x32_bf16 v[220:223], v[72:75], v[120:123], v[220:223]\n\t" \
  "s_barrier\n\t" \
  "v_mfma_f32_16x16x32_bf16 v[228:231], v[76:79], v[120:123], v[228:231]\n\t" \
  "ds_read_b64_tr_b16 v[136:137], v62 offset:26624\n\t" \
  "v_bfe_u32 v30, v4, 9, 1\n\t" \
  "v_mfma_f32_16x16x32_bf16 v[228:231], v[68:71], v[124:127], v[228:231]\n\t" \
  "ds_read_b64_tr_b16 v[138:139], v62 offset:27136\n\t" \
  "v_mad_i32_i24 v30, v30, -16, v0\n\t" \
  "v_mfma_f32_16x16x32_bf16 v[236:239], v[84:87], v[124:127], v[236:239]\n\t" \
  "ds_read_b128 v[108:111], v60 offset:10240\n\t" \
  "ds_read_b128 v[26:29], v0 offset:1552\n\t" \
  "v_mfma_f32_16x16x32_bf16 v[236:239], v[80:83], v[120:123], v[236:239]\n\t" \
  "ds_read_b64_tr_b16 v[96:97], v62 offset:30720\n\t" \
  "ds_read_b128 v[30:33], v30 offset:1040\n\t" \
  "v_mfma_f32_16x16x32_bf16 v[244:247], v[92:95], v[120:123], v[244:247]\n\t" \
  "ds_read_b64_tr_b16 v[98:99], v62 offset:31232\n\t" \
  "ds_read_b128 v[34:37], v0 offset:512\n\t" \
  "v_mfma_f32_16x16x32_bf16 v[244:247], v[88:91], v[124:127], v[244:247]\n\t" \
  "ds_read_b128 v[148:151], v61 offset:10240\n\t" \
  "ds_read_b64_tr_b16 v[38:39], v1 offset:512\n\t" \
  "v_mfma_f32_16x16x32_bf16 v[152:155], v[128:131], v[72:75], v[152:155]\n\t" \
  "ds_read_b128 v[64:67], v61 offset:12288\n\t" \
  "ds_read_b64_tr_b16 v[40:41], v1 offset:0\n\t" \
  "v_mfma_f32_16x16x32_bf16 v[152:155], v[144:147], v[140:143], v[152:155]\n\t" \
  "ds_read_b128 v[104:107], v60 offset:12288\n\t" \
  "s_waitcnt vmcnt(0)\n\t" \
  "v_mfma_f32_16x16x32_bf16 v[160:163], v[144:147], v[68:71], v[160:163]\n\t" \
  "ds_read_b128 v[112:115], v60 offset:14336\n\t" \
  "v_perm_b32 v18, v10, v10, v14\n\t" \
  "v_mfma_f32_16x16x32_bf16 v[160:163], v[128:131], v[76:79], v[160:163]\n\t" \
  "ds_read_b128 v[116:119], v61 offset:14336\n\t" \
  "v_perm_b32 v19, v10, v10, v15\n\t" \
  "v_mfma_f32_16x16x32_bf16 v[168:171], v[128:131], v[80:83], v[168:171]\n\t" \
  "ds_read_b128 v[120:123], v61 offset:16384\n\t" \
  "v_perm_b32 v20, v10, v10, v16\n\t" \
  "v_mfma_f32_16x16x32_bf16 v[168:171], v[144:147], v[84:87], v[168:171]\n\t" \
  "ds_read_b128 v[124:127], v60 offset:16384\n\t" \
  "v_perm_b32 v21, v10, v10, v17\n\t" \
  "v_mfma_f32_16x16x32_bf16 v[176:179], v[144:147], v[88:91], v[176:179]\n\t" \
  "ds_read_b64_tr_b16 v[144:145], v63 offset:26624\n\t" \
  "v_perm_b32 v22, v11, v11, v14\n\t" \
  "v_mfma_f32_16x16x32_bf16 v[176:179], v[128:131], v[92:95], v[176:179]\n\t" \
  "ds_read_b64_tr_b16 v[146:147], v63 offset:27136\n\t" \
  "v_perm_b32 v23, v11, v11, v15\n\t" \
  "v_mfma_f32_16x16x32_bf16 v[156:159], v[100:103], v[72:75], v[156:159]\n\t" \
  "ds_read_b64_tr_b16 v[128:129], v63 offset:30720\n\t" \
  "v_perm_b32 v24, v11, v11, v16\n\t" \
  "v_mfma_f32_16x16x32_bf16 v[156:159], v[132:135], v[140:143], v[156:159]\n\t" \
  "ds_read_b64_tr_b16 v[130:131], v63 offset:31232\n\t" \
  "v_perm_b32 v25, v11, v11, v17\n\t" \
  "v_mfma_f32_16x16x32_bf16 v[164:167], v[132:135], v[68:71], v[164:167]\n\t" \
  "ds_read_b64_tr_b16 v[72:73], v62 offset:34816\n\t" \
  "v_mfma_f32_16x16x32_bf16 v[164:167], v[100:103], v[76:79], v[164:167]\n\t" \
  "ds_read_b64_tr_b16 v[74:75], v62 offset:35328\n\t" \
  "v_mfma_f32_16x16x32_bf16 v[172:175], v[100:103], v[80:83], v[172:175]\n\t" \
  "ds_read_b64_tr_b16 v[140:141], v62 offset:38912\n\t" \
  "s_waitcnt lgkmcnt(6)\n\t" \
  "v_mfma_f32_16x16x32_bf16 v[42:45], v[26:29], v[18:21], 0\n\t" \
  "v_mfma_f32_16x16x32_bf16 v[42:45], v[30:33], v[22:25], v[42:45]\n\t" \
  "v_mfma_f32_16x16x32_bf16 v[46:49], v[30:33], v[18:21], 0\n\t" \
  "v_mfma_f32_16x16x32_bf16 v[46:49], v[34:37], v[22:25], v[46:49]\n\t" \
  "ds_read_b128 v[26:29], v0 offset:1808\n\t" \
  "ds_read_b128 v[30:33], v0 offset:1296\n\t" \
  "v_mfma_f32_16x16x32_bf16 v[172:175], v[132:135], v[84:87], v[172:175]\n\t" \
  "ds_read_b64_tr_b16 v[142:143], v62 offset:39424\n\t" \
  "ds_read_b128 v[34:37], v0 offset:768\n\t" \
  "v_mfma_f32_16x16x32_bf16 v[180:183], v[132:135], v[88:91], v[180:183]\n\t" \
  "ds_read_b128 v[68:71], v60 offset:0\n\t" \
  "v_mfma_f32_16x16x32_bf16 v[180:183], v[100:103], v[92:95], v[180:183]\n\t" \
  "ds_read_b128 v[76:79], v61 offset:0\n\t" \
  "v_mfma_f32_16x16x32_bf16 v[184:187], v[136:139], v[108:111], v[184:187]\n\t" \
  "ds_read_b64_tr_b16 v[80:81], v63 offset:34816\n\t" \
  "v_lshlrev_b32 v50, 16, v38\n\t" \
  "v_mfma_f32_16x16x32_bf16 v[184:187], v[96:99], v[148:151], v[184:187]\n\t" \
  "ds_read_b64_tr_b16 v[82:83], v63 offset:35328\n\t" \
  "v_lshlrev_b32 v51, 16, v40\n\t" \
  "v_mfma_f32_16x16x32_bf16 v[192:195], v[96:99], v[64:67], v[192:195]\n\t" \
  "ds_read_b64_tr_b16 v[84:85], v63 offset:38912\n\t" \
  "v_mul_f32 v42, v42, v50\n\t" \
  "v_mfma_f32_16x16x32_bf16 v[192:195], v[136:139], v[104:107], v[192:195]\n\t" \
  "ds_read_b64_tr_b16 v[86:87], v63 offset:39424\n\t" \
  "v_mul_f32 v46, v46, v51\n\t" \
  "v_mfma_f32_16x16x32_bf16 v[200:203], v[136:139], v[112:115], v[200:203]\n\t" \
  "ds_read_b64_tr_b16 v[88:89], v62 offset:18432\n\t" \
  "v_cvt_pk_bf16_f32 v42, v42, v46\n\t" \
  "v_mfma_f32_16x16x32_bf16 v[200:203], v[96:99], v[116:119], v[200:203]\n\t" \
  "ds_read_b64_tr_b16 v[90:91], v62 offset:18944\n\t" \
  "v_and_b32 v50, 0xffff0000, v38\n\t" \
  "v_mfma_f32_16x16x32_bf16 v[208:211], v[96:99], v[120:123], v[208:211]\n\t" \
  "ds_read_b64_tr_b16 v[132:133], v62 offset:22528\n\t" \
  "v_and_b32 v51, 0xffff0000, v40\n\t" \
  "v_mfma_f32_16x16x32_bf16 v[208:211], v[136:139], v[124:127], v[208:211]\n\t" \
  "ds_read_b64_tr_b16 v[134:135], v62 offset:23040\n\t" \
  "v_mul_f32 v43, v43, v50\n\t" \
  "s_waitcnt lgkmcnt(7)\n\t" \
  "v_mfma_f32_16x16x32_bf16 v[188:191], v[144:147], v[108:111], v[188:191]\n\t" \
  "ds_read_b64_tr_b16 v[92:93], v63 offset:18432\n\t" \
  "v_mul_f32 v47, v47, v51\n\t" \
  "v_mfma_f32_16x16x32_bf16 v[188:191], v[128:131], v[148:151], v[188:191]\n\t" \
  "ds_read_b64_tr_b16 v[94:95], v63 offset:18944\n\t" \
  "v_cvt_pk_bf16_f32 v43, v43, v47\n\t" \
  "v_mfma_f32_16x16x32_bf16 v[196:199], v[128:131], v[64:67], v[196:199]\n\t" \
  "ds_read_b64_tr_b16 v[100:101], v63 offset:22528\n\t" \
  "v_lshlrev_b32 v50, 16, v39\n\t" \
  "v_mfma_f32_16x16x32_bf16 v[196:199], v[144:147], v[104:107], v[196:199]\n\t" \
  "ds_read_b64_tr_b16 v[102:103], v63 offset:23040\n\t" \
  "v_lshlrev_b32 v51, 16, v41\n\t" \
  "v_mfma_f32_16x16x32_bf16 v[204:207], v[144:147], v[112:115], v[204:207]\n\t" \
  "v_mul_f32 v44, v44, v50\n\t" \
  "v_mul_f32 v48, v48, v51\n\t" \
  "v_mfma_f32_16x16x32_bf16 v[204:207], v[128:131], v[116:119], v[204:207]\n\t" \
  "v_cvt_pk_bf16_f32 v44, v44, v48\n\t" \
  "v_and_b32 v50, 0xffff0000, v39\n\t" \
  "v_mfma_f32_16x16x32_bf16 v[212:215], v[128:131], v[120:123], v[212:215]\n\t" \
  "v_and_b32 v51, 0xffff0000, v41\n\t" \
  "v_mul_f32 v45, v45, v50\n\t" \
  "v_mfma_f32_16x16x32_bf16 v[212:215], v[144:147], v[124:127], v[212:215]\n\t" \
  "v_mul_f32 v49, v49, v51\n\t" \
  "ds_read_b64_tr_b16 v[38:39], v1 offset:768\n\t" \
  "v_mfma_f32_16x16x32_bf16 v[216:219], v[108:111], v[72:75], v[216:219]\n\t" \
  "ds_read_b64_tr_b16 v[40:41], v1 offset:256\n\t" \
  "v_cvt_pk_bf16_f32 v45, v45, v49\n\t" \
  "v_mfma_f32_16x16x32_bf16 v[216:219], v[148:151], v[140:143], v[216:219]\n\t" \
  "v_mfma_f32_16x16x32_bf16 v[224:227], v[64:67], v[140:143], v[224:227]\n\t" \
  "v_mfma_f32_16x16x32_bf16 v[224:227], v[104:107], v[72:75], v[224:227]\n\t" \
  "v_mfma_f32_16x16x32_bf16 v[232:235], v[112:115], v[72:75], v[232:235]\n\t" \
  "v_mfma_f32_16x16x32_bf16 v[232:235], v[116:119], v[140:143], v[232:235]\n\t" \
  "v_mfma_f32_16x16x32_bf16 v[240:243], v[120:123], v[140:143], v[240:243]\n\t" \
  "v_mfma_f32_16x16x32_bf16 v[240:243], v[124:127], v[72:75], v[240:243]\n\t" \
  "v_mfma_f32_16x16x32_bf16 v[248:251], v[68:71], v[72:75], v[248:251]\n\t" \
  "v_mfma_f32_16x16x32_bf16 v[248:251], v[76:79], v[140:143], v[248:251]\n\t" \
  "s_waitcnt lgkmcnt(0)\n\t" \
  "v_mfma_f32_16x16x32_bf16 v[252:255], v[68:71], v[80:83], v[252:255]\n\t" \
  "v_mfma_f32_16x16x32_bf16 v[252:255], v[76:79], v[84:87], v[252:255]\n\t" \
  "v_mfma_f32_16x16x32_bf16 v[220:223], v[148:151], v[84:87], v[220:223]\n\t" \
  "v_mfma_f32_16x16x32_bf16 v[220:223], v[108:111], v[80:83], v[220:223]\n\t" \
  "s_barrier\n\t" \
  "v_mfma_f32_16x16x32_bf16 v[228:231], v[104:107], v[80:83], v[228:231]\n\t" \
  "ds_read_b64_tr_b16 v[96:97], v54 offset:26624\n\t" \
  "v_mfma_f32_16x16x32_bf16 v[228:231], v[64:67], v[84:87], v[228:231]\n\t" \
  "v_mov_b32 v50, 163344\n\t" \
  "ds_read_b64_tr_b16 v[98:99], v54 offset:27136\n\t" \
  "v_mfma_f32_16x16x32_bf16 v[236:239], v[116:119], v[84:87], v[236:239]\n\t" \
  "ds_read_b32 v50, v50 offset:24\n\t" \
  "ds_read_b128 v[136:139], v52 offset:10240\n\t" \
  "v_mfma_f32_16x16x32_bf16 v[236:239], v[112:115], v[80:83], v[236:239]\n\t" \
  "ds_read_b64_tr_b16 v[128:129], v54 offset:30720\n\t" \
  "v_mfma_f32_16x16x32_bf16 v[244:247], v[124:127], v[80:83], v[244:247]\n\t" \
  "ds_read_b64_tr_b16 v[130:131], v54 offset:31232\n\t" \
  "v_mfma_f32_16x16x32_bf16 v[244:247], v[120:123], v[84:87], v[244:247]\n\t" \
  "ds_read_b128 v[144:147], v53 offset:10240\n\t" \
  "v_mfma_f32_16x16x32_bf16 v[152:155], v[88:91], v[108:111], v[152:155]\n\t" \
  "ds_read_b128 v[72:75], v53 offset:12288\n\t" \
  "v_mfma_f32_16x16x32_bf16 v[152:155], v[132:135], v[148:151], v[152:155]\n\t" \
  "ds_read_b128 v[140:143], v52 offset:12288\n\t" \
  "v_mfma_f32_16x16x32_bf16 v[160:163], v[132:135], v[64:67], v[160:163]\n\t" \
  "ds_read_b128 v[68:71], v52 offset:14336\n\t" \
  "v_mfma_f32_16x16x32_bf16 v[160:163], v[88:91], v[104:107], v[160:163]\n\t" \
  "ds_read_b128 v[76:79], v53 offset:14336\n\t" \
  "v_mfma_f32_16x16x32_bf16 v[168:171], v[88:91], v[112:115], v[168:171]\n\t" \
  "ds_read_b128 v[80:83], v53 offset:16384\n\t" \
  "v_mfma_f32_16x16x32_bf16 v[168:171], v[132:135], v[116:119], v[168:171]\n\t" \
  "ds_read_b128 v[84:87], v52 offset:16384\n\t" \
  "v_mfma_f32_16x16x32_bf16 v[176:179], v[132:135], v[120:123], v[176:179]\n\t" \
  "s_waitcnt lgkmcnt(10)\n\t" \
  "v_readfirstlane_b32 s88, v50\n\t" \
  "s_mov_b32 s89, 0\n\t" \
  "ds_read_b64_tr_b16 v[132:133], v55 offset:26624\n\t" \
  "v_mfma_f32_16x16x32_bf16 v[176:179], v[88:91], v[124:127], v[176:179]\n\t" \
  "ds_read_b64_tr_b16 v[134:135], v55 offset:27136\n\t" \
  "v_mfma_f32_16x16x32_bf16 v[156:159], v[92:95], v[108:111], v[156:159]\n\t" \
  "s_xor_b64 %24, %24, s[88:89]\n\t" \
  "ds_read_b64_tr_b16 v[88:89], v55 offset:30720\n\t" \
  "v_mfma_f32_16x16x32_bf16 v[156:159], v[100:103], v[148:151], v[156:159]\n\t" \
  "s_xor_b64 %25, %25, s[88:89]\n\t" \
  "ds_read_b64_tr_b16 v[90:91], v55 offset:31232\n\t" \
  "v_mfma_f32_16x16x32_bf16 v[164:167], v[100:103], v[64:67], v[164:167]\n\t" \
  "s_xor_b64 %14, %14, s[88:89]\n\t" \
  "ds_read_b64_tr_b16 v[108:109], v54 offset:34816\n\t" \
  "v_mfma_f32_16x16x32_bf16 v[164:167], v[92:95], v[104:107], v[164:167]\n\t" \
  "s_xor_b64 %19, %19, s[88:89]\n\t" \
  "ds_read_b64_tr_b16 v[110:111], v54 offset:35328\n\t" \
  "v_mfma_f32_16x16x32_bf16 v[172:175], v[92:95], v[112:115], v[172:175]\n\t" \
  "s_xor_b64 %21, %21, s[88:89]\n\t" \
  "ds_read_b64_tr_b16 v[148:149], v54 offset:38912\n\t" \
  "v_mfma_f32_16x16x32_bf16 v[172:175], v[100:103], v[116:119], v[172:175]\n\t" \
  "s_xor_b64 s[92:93], s[92:93], s[88:89]\n\t" \
  "ds_read_b64_tr_b16 v[150:151], v54 offset:39424\n\t" \
  "v_mfma_f32_16x16x32_bf16 v[180:183], v[100:103], v[120:123], v[180:183]\n\t" \
  "ds_read_b128 v[64:67], v52 offset:0\n\t" \
  "v_mfma_f32_16x16x32_bf16 v[180:183], v[92:95], v[124:127], v[180:183]\n\t" \
  "ds_read_b128 v[104:107], v53 offset:0\n\t" \
  "s_waitcnt lgkmcnt(7)\n\t" \
  "v_mfma_f32_16x16x32_bf16 v[184:187], v[96:99], v[136:139], v[184:187]\n\t" \
  "ds_read_b64_tr_b16 v[112:113], v55 offset:34816\n\t" \
  "v_mfma_f32_16x16x32_bf16 v[184:187], v[128:131], v[144:147], v[184:187]\n\t" \
  "ds_read_b64_tr_b16 v[114:115], v55 offset:35328\n\t" \
  "v_mfma_f32_16x16x32_bf16 v[192:195], v[128:131], v[72:75], v[192:195]\n\t" \
  "ds_read_b64_tr_b16 v[116:117], v55 offset:38912\n\t" \
  "v_mfma_f32_16x16x32_bf16 v[192:195], v[96:99], v[140:143], v[192:195]\n\t" \
  "ds_read_b64_tr_b16 v[118:119], v55 offset:39424\n\t" \
  "v_mfma_f32_16x16x32_bf16 v[200:203], v[96:99], v[68:71], v[200:203]\n\t" \
  "ds_read_b64_tr_b16 v[100:101], v54 offset:18432\n\t" \
  "v_mfma_f32_16x16x32_bf16 v[200:203], v[128:131], v[76:79], v[200:203]\n\t" \
  "ds_read_b64_tr_b16 v[102:103], v54 offset:18944\n\t" \
  "v_mfma_f32_16x16x32_bf16 v[208:211], v[128:131], v[80:83], v[208:211]\n\t" \
  "ds_read_b64_tr_b16 v[120:121], v54 offset:22528\n\t" \
  "v_mfma_f32_16x16x32_bf16 v[208:211], v[96:99], v[84:87], v[208:211]\n\t" \
  "ds_read_b64_tr_b16 v[122:123], v54 offset:23040\n\t" \
  "v_mfma_f32_16x16x32_bf16 v[188:191], v[132:135], v[136:139], v[188:191]\n\t" \
  "ds_read_b64_tr_b16 v[92:93], v55 offset:18432\n\t" \
  "s_waitcnt lgkmcnt(7)\n\t" \
  "v_mfma_f32_16x16x32_bf16 v[188:191], v[88:91], v[144:147], v[188:191]\n\t" \
  "ds_read_b64_tr_b16 v[94:95], v55 offset:18944\n\t" \
  "v_mfma_f32_16x16x32_bf16 v[196:199], v[88:91], v[72:75], v[196:199]\n\t" \
  "ds_read_b64_tr_b16 v[124:125], v55 offset:22528\n\t" \
  "v_mfma_f32_16x16x32_bf16 v[196:199], v[132:135], v[140:143], v[196:199]\n\t" \
  "ds_read_b64_tr_b16 v[126:127], v55 offset:23040\n\t" \
  "v_mfma_f32_16x16x32_bf16 v[204:207], v[132:135], v[68:71], v[204:207]\n\t" \
  "v_mfma_f32_16x16x32_bf16 v[204:207], v[88:91], v[76:79], v[204:207]\n\t" \
  "v_mfma_f32_16x16x32_bf16 v[212:215], v[88:91], v[80:83], v[212:215]\n\t" \
  "v_mfma_f32_16x16x32_bf16 v[212:215], v[132:135], v[84:87], v[212:215]\n\t" \
  "v_mfma_f32_16x16x32_bf16 v[216:219], v[136:139], v[108:111], v[216:219]\n\t" \
  "v_mfma_f32_16x16x32_bf16 v[216:219], v[144:147], v[148:151], v[216:219]\n\t" \
  "v_mfma_f32_16x16x32_bf16 v[224:227], v[72:75], v[148:151], v[224:227]\n\t" \
  "v_mfma_f32_16x16x32_bf16 v[224:227], v[140:143], v[108:111], v[224:227]\n\t" \
  "v_mfma_f32_16x16x32_bf16 v[232:235], v[68:71], v[108:111], v[232:235]\n\t" \
  "global_store_dwordx4 v2, v[42:45], %24 offset:0 sc1\n\t" \
  "v_mfma_f32_16x16x32_bf16 v[232:235], v[76:79], v[148:151], v[232:235]\n\t" \
  "s_nop 0\n\t" \
  "v_mfma_f32_16x16x32_bf16 v[42:45], v[26:29], v[18:21], 0\n\t" \
  "v_mfma_f32_16x16x32_bf16 v[42:45], v[30:33], v[22:25], v[42:45]\n\t" \
  "v_mfma_f32_16x16x32_bf16 v[46:49], v[30:33], v[18:21], 0\n\t" \
  "v_mfma_f32_16x16x32_bf16 v[46:49], v[34:37], v[22:25], v[46:49]\n\t" \
  "v_bfe_u32 v30, v4, 9, 1\n\t" \
  "v_mad_i32_i24 v30, v30, -16, v0\n\t" \
  "v_mfma_f32_16x16x32_bf16 v[240:243], v[80:83], v[148:151], v[240:243]\n\t" \
  "ds_read_b128 v[26:29], v0 offset:1584\n\t" \
  "ds_read_b128 v[30:33], v30 offset:1072\n\t" \
  "v_mfma_f32_16x16x32_bf16 v[240:243], v[84:87], v[108:111], v[240:243]\n\t" \
  "ds_read_b128 v[34:37], v0 offset:544\n\t" \
  "v_perm_b32 v18, v12, v12, v14\n\t" \
  "v_mfma_f32_16x16x32_bf16 v[248:251], v[64:67], v[108:111], v[248:251]\n\t" \
  "v_perm_b32 v19, v12, v12, v15\n\t" \
  "v_perm_b32 v20, v12, v12, v16\n\t" \
  "v_mfma_f32_16x16x32_bf16 v[248:251], v[104:107], v[148:151], v[248:251]\n\t" \
  "v_perm_b32 v21, v12, v12, v17\n\t" \
  "v_perm_b32 v22, v13, v13, v14\n\t" \
  "v_mfma_f32_16x16x32_bf16 v[252:255], v[64:67], v[112:115], v[252:255]\n\t" \
  "v_perm_b32 v23, v13, v13, v15\n\t" \
  "v_perm_b32 v24, v13, v13, v16\n\t" \
  "s_waitcnt lgkmcnt(3)\n\t" \
  "v_mfma_f32_16x16x32_bf16 v[252:255], v[104:107], v[116:119], v[252:255]\n\t" \
  "v_perm_b32 v25, v13, v13, v17\n\t" \
  "v_lshlrev_b32 v50, 16, v38\n\t" \
  "v_mfma_f32_16x16x32_bf16 v[220:223], v[144:147], v[116:119], v[220:223]\n\t" \
  "v_lshlrev_b32 v51, 16, v40\n\t" \
  "v_mul_f32 v42, v42, v50\n\t" \
  "v_mfma_f32_16x16x32_bf16 v[220:223], v[136:139], v[112:115], v[220:223]\n\t" \
  "v_mul_f32 v46, v46, v51\n\t" \
  "v_cvt_pk_bf16_f32 v42, v42, v46\n\t" \
  "s_waitcnt lgkmcnt(0)\n\t" \
  "s_barrier\n\t" \
  "v_mfma_f32_16x16x32_bf16 v[228:231], v[140:143], v[112:115], v[228:231]\n\t" \
  "ds_read_b64_tr_b16 v[128:129], v58 offset:26624\n\t" \
  "v_and_b32 v50, 0xffff0000, v38\n\t" \
  "v_mfma_f32_16x16x32_bf16 v[228:231], v[72:75], v[116:119], v[228:231]\n\t" \
  "ds_read_b64_tr_b16 v[130:131], v58 offset:27136\n\t" \
  "v_and_b32 v51, 0xffff0000, v40\n\t" \
  "v_mfma_f32_16x16x32_bf16 v[236:239], v[76:79], v[116:119], v[236:239]\n\t" \
  "ds_read_b128 v[96:99], v56 offset:10240\n\t" \
  "v_mul_f32 v43, v43, v50\n\t" \
  "v_mfma_f32_16x16x32_bf16 v[236:239], v[68:71], v[112:115], v[236:239]\n\t" \
  "ds_read_b64_tr_b16 v[88:89], v58 offset:30720\n\t" \
  "500:\n\t" \
  "s_cbranch_execz 504f\n\t" \
  "v_mul_f32 v47, v47, v51\n\t" \
  "v_mfma_f32_16x16x32_bf16 v[244:247], v[84:87], v[112:115], v[244:247]\n\t" \
  "ds_read_b64_tr_b16 v[90:91], v58 offset:31232\n\t" \
  "v_cvt_pk_bf16_f32 v43, v43, v47\n\t" \
  "v_mfma_f32_16x16x32_bf16 v[244:247], v[80:83], v[116:119], v[244:247]\n\t" \
  "ds_read_b128 v[132:135], v57 offset:10240\n\t" \
  "v_lshlrev_b32 v50, 16, v39\n\t" \
  "v_mfma_f32_16x16x32_bf16 v[152:155], v[100:103], v[136:139], v[152:155]\n\t" \
  "ds_read_b128 v[108:111], v57 offset:12288\n\t" \
  "v_lshlrev_b32 v51, 16, v41\n\t" \
  "v_mfma_f32_16x16x32_bf16 v[152:155], v[120:123], v[144:147], v[152:155]\n\t" \
  "ds_read_b128 v[148:151], v56 offset:12288\n\t" \
  "v_mul_f32 v44, v44, v50\n\t" \
  "v_mfma_f32_16x16x32_bf16 v[160:163], v[120:123], v[72:75], v[160:163]\n\t" \
  "ds_read_b128 v[64:67], v56 offset:14336\n\t" \
  "v_mul_f32 v48, v48, v51\n\t" \
  "v_mfma_f32_16x16x32_bf16 v[160:163], v[100:103], v[140:143], v[160:163]\n\t" \
  "ds_read_b128 v[104:107], v57 offset:14336\n\t" \
  "v_cvt_pk_bf16_f32 v44, v44, v48\n\t" \
  "v_mfma_f32_16x16x32_bf16 v[168:171], v[100:103], v[68:71], v[168:171]\n\t" \
  "ds_read_b128 v[112:115], v57 offset:16384\n\t" \
  "v_and_b32 v50, 0xffff0000, v39\n\t" \
  "v_mfma_f32_16x16x32_bf16 v[168:171], v[120:123], v[76:79], v[168:171]\n\t" \
  "ds_read_b128 v[116:119], v56 offset:16384\n\t" \
  "v_and_b32 v51, 0xffff0000, v41\n\t" \
  "v_mfma_f32_16x16x32_bf16 v[176:179], v[120:123], v[80:83], v[176:179]\n\t" \
  "ds_read_b64_tr_b16 v[120:121], v59 offset:26624\n\t" \
  "v_mul_f32 v45, v45, v50\n\t" \
  "v_mfma_f32_16x16x32_bf16 v[176:179], v[100:103], v[84:87], v[176:179]\n\t" \
  "ds_read_b64_tr_b16 v[122:123], v59 offset:27136\n\t" \
  "v_mul_f32 v49, v49, v51\n\t" \
  "v_mfma_f32_16x16x32_bf16 v[156:159], v[92:95], v[136:139], v[156:159]\n\t" \
  "ds_read_b64_tr_b16 v[100:101], v59 offset:30720\n\t" \
  "ds_read_b64_tr_b16 v[38:39], v1 offset:544\n\t" \
  "v_mfma_f32_16x16x32_bf16 v[156:159], v[124:127], v[144:147], v[156:159]\n\t" \
  "ds_read_b64_tr_b16 v[102:103], v59 offset:31232\n\t" \
  "ds_read_b64_tr_b16 v[40:41], v1 offset:32\n\t" \
  "v_mfma_f32_16x16x32_bf16 v[164:167], v[124:127], v[72:75], v[164:167]\n\t" \
  "ds_read_b64_tr_b16 v[136:137], v58 offset:34816\n\t" \
  "v_cvt_pk_bf16_f32 v45, v45, v49\n\t" \
  "v_mfma_f32_16x16x32_bf16 v[164:167], v[92:95], v[140:143], v[164:167]\n\t" \
  "ds_read_b64_tr_b16 v[138:139], v58 offset:35328\n\t" \
  "v_mfma_f32_16x16x32_bf16 v[172:175], v[92:95], v[68:71], v[172:175]\n\t" \
  "ds_read_b64_tr_b16 v[144:145], v58 offset:38912\n\t" \
  "v_mfma_f32_16x16x32_bf16 v[172:175], v[124:127], v[76:79], v[172:175]\n\t" \
  "ds_read_b64_tr_b16 v[146:147], v58 offset:39424\n\t" \
  "v_mfma_f32_16x16x32_bf16 v[180:183], v[124:127], v[80:83], v[180:183]\n\t" \
  "ds_read_b128 v[72:75], v56 offset:0\n\t" \
  "v_mfma_f32_16x16x32_bf16 v[180:183], v[92:95], v[84:87], v[180:183]\n\t" \
  "ds_read_b128 v[140:143], v57 offset:0\n\t" \
  "s_waitcnt lgkmcnt(8)\n\t" \
  "v_mfma_f32_16x16x32_bf16 v[184:187], v[128:131], v[96:99], v[184:187]\n\t" \
  "ds_read_b64_tr_b16 v[68:69], v59 offset:34816\n\t" \
  "v_mfma_f32_16x16x32_bf16 v[184:187], v[88:91], v[132:135], v[184:187]\n\t" \
  "ds_read_b64_tr_b16 v[70:71], v59 offset:35328\n\t" \
  "v_mfma_f32_16x16x32_bf16 v[192:195], v[88:91], v[108:111], v[192:195]\n\t" \
  "ds_read_b64_tr_b16 v[76:77], v59 offset:38912\n\t" \
  "v_mfma_f32_16x16x32_bf16 v[192:195], v[128:131], v[148:151], v[192:195]\n\t" \
  "ds_read_b64_tr_b16 v[78:79], v59 offset:39424\n\t" \
  "v_mfma_f32_16x16x32_bf16 v[200:203], v[128:131], v[64:67], v[200:203]\n\t" \
  "ds_read_b64_tr_b16 v[80:81], v58 offset:18432\n\t" \
  "v_mfma_f32_16x16x32_bf16 v[200:203], v[88:91], v[104:107], v[200:203]\n\t" \
  "ds_read_b64_tr_b16 v[82:83], v58 offset:18944\n\t" \
  "v_mfma_f32_16x16x32_bf16 v[208:211], v[88:91], v[112:115], v[208:211]\n\t" \
  "ds_read_b64_tr_b16 v[124:125], v58 offset:22528\n\t" \
  "v_mfma_f32_16x16x32_bf16 v[208:211], v[128:131], v[116:119], v[208:211]\n\t" \
  "ds_read_b64_tr_b16 v[126:127], v58 offset:23040\n\t" \
  "v_mfma_f32_16x16x32_bf16 v[188:191], v[120:123], v[96:99], v[188:191]\n\t" \
  "ds_read_b64_tr_b16 v[84:85], v59 offset:18432\n\t" \
  "s_waitcnt lgkmcnt(7)\n\t" \
  "v_mfma_f32_16x16x32_bf16 v[188:191], v[100:103], v[132:135], v[188:191]\n\t" \
  "ds_read_b64_tr_b16 v[86:87], v59 offset:18944\n\t" \
  "v_mfma_f32_16x16x32_bf16 v[196:199], v[100:103], v[108:111], v[196:199]\n\t" \
  "ds_read_b64_tr_b16 v[92:93], v59 offset:22528\n\t" \
  "v_mfma_f32_16x16x32_bf16 v[196:199], v[120:123], v[148:151], v[196:199]\n\t" \
  "ds_read_b64_tr_b16 v[94:95], v59 offset:23040\n\t" \
  "v_mfma_f32_16x16x32_bf16 v[204:207], v[120:123], v[64:67], v[204:207]\n\t" \
  "v_mfma_f32_16x16x32_bf16 v[204:207], v[100:103], v[104:107], v[204:207]\n\t" \
  "v_mfma_f32_16x16x32_bf16 v[212:215], v[100:103], v[112:115], v[212:215]\n\t" \
  "v_mfma_f32_16x16x32_bf16 v[212:215], v[120:123], v[116:119], v[212:215]\n\t" \
  "v_mfma_f32_16x16x32_bf16 v[216:219], v[96:99], v[136:139], v[216:219]\n\t" \
  "v_mfma_f32_16x16x32_bf16 v[216:219], v[132:135], v[144:147], v[216:219]\n\t" \
  "v_mfma_f32_16x16x32_bf16 v[224:227], v[108:111], v[144:147], v[224:227]\n\t" \
  "v_mfma_f32_16x16x32_bf16 v[224:227], v[148:151], v[136:139], v[224:227]\n\t" \
  "v_mfma_f32_16x16x32_bf16 v[232:235], v[64:67], v[136:139], v[232:235]\n\t" \
  "global_store_dwordx4 v2, v[42:45], %24 offset:1024 sc1\n\t" \
  "v_add_u32 v2, 0x20000, v2\n\t" \
  "v_mfma_f32_16x16x32_bf16 v[232:235], v[104:107], v[144:147], v[232:235]\n\t" \
  "v_mfma_f32_16x16x32_bf16 v[240:243], v[112:115], v[144:147], v[240:243]\n\t" \
  "v_mfma_f32_16x16x32_bf16 v[42:45], v[26:29], v[18:21], 0\n\t" \
  "v_mfma_f32_16x16x32_bf16 v[42:45], v[30:33], v[22:25], v[42:45]\n\t" \
  "v_mfma_f32_16x16x32_bf16 v[46:49], v[30:33], v[18:21], 0\n\t" \
  "v_mfma_f32_16x16x32_bf16 v[46:49], v[34:37], v[22:25], v[46:49]\n\t" \
  "ds_read_b128 v[26:29], v0 offset:1840\n\t" \
  "ds_read_b128 v[30:33], v0 offset:1328\n\t" \
  "v_mfma_f32_16x16x32_bf16 v[240:243], v[116:119], v[136:139], v[240:243]\n\t" \
  "ds_read_b128 v[34:37], v0 offset:800\n\t" \
  "v_mfma_f32_16x16x32_bf16 v[248:251], v[72:75], v[136:139], v[248:251]\n\t" \
  "v_mfma_f32_16x16x32_bf16 v[248:251], v[140:143], v[144:147], v[248:251]\n\t" \
  "v_lshlrev_b32 v50, 16, v38\n\t" \
  "v_lshlrev_b32 v51, 16, v40\n\t" \
  "v_mfma_f32_16x16x32_bf16 v[252:255], v[72:75], v[68:71], v[252:255]\n\t" \
  "v_mul_f32 v42, v42, v50\n\t" \
  "v_mul_f32 v46, v46, v51\n\t" \
  "s_waitcnt lgkmcnt(3)\n\t" \
  "v_mfma_f32_16x16x32_bf16 v[252:255], v[140:143], v[76:79], v[252:255]\n\t" \
  "v_cvt_pk_bf16_f32 v42, v42, v46\n\t" \
  "v_and_b32 v50, 0xffff0000, v38\n\t" \
  "v_mfma_f32_16x16x32_bf16 v[220:223], v[132:135], v[76:79], v[220:223]\n\t" \
  "v_and_b32 v51, 0xffff0000, v40\n\t" \
  "v_mul_f32 v43, v43, v50\n\t" \
  "v_mfma_f32_16x16x32_bf16 v[220:223], v[96:99], v[68:71], v[220:223]\n\t" \
  "v_mul_f32 v47, v47, v51\n\t" \
  "v_cvt_pk_bf16_f32 v43, v43, v47\n\t" \
  "s_waitcnt lgkmcnt(0)\n\t" \
  "s_barrier\n\t" \
  "v_mfma_f32_16x16x32_bf16 v[228:231], v[148:151], v[68:71], v[228:231]\n\t" \
  "ds_read_b64_tr_b16 v[88:89], v62 offset:26624\n\t" \
  "v_lshlrev_b32 v50, 16, v39\n\t" \
  "v_mfma_f32_16x16x32_bf16 v[228:231], v[108:111], v[76:79], v[228:231]\n\t" \
  "ds_read_b64_tr_b16 v[90:91], v62 offset:27136\n\t" \
  "v_lshlrev_b32 v51, 16, v41\n\t" \
  "v_mfma_f32_16x16x32_bf16 v[236:239], v[104:107], v[76:79], v[236:239]\n\t" \
  "ds_read_b128 v[128:131], v60 offset:10240\n\t" \
  "v_mul_f32 v44, v44, v50\n\t" \
  "v_mfma_f32_16x16x32_bf16 v[236:239], v[64:67], v[68:71], v[236:239]\n\t" \
  "ds_read_b64_tr_b16 v[100:101], v62 offset:30720\n\t" \
  "v_mul_f32 v48, v48, v51\n\t" \
  "v_mfma_f32_16x16x32_bf16 v[244:247], v[116:119], v[68:71], v[244:247]\n\t" \
  "ds_read_b64_tr_b16 v[102:103], v62 offset:31232\n\t" \
  "v_cvt_pk_bf16_f32 v44, v44, v48\n\t" \
  "v_mfma_f32_16x16x32_bf16 v[244:247], v[112:115], v[76:79], v[244:247]\n\t" \
  "ds_read_b128 v[120:123], v61 offset:10240\n\t" \
  "v_and_b32 v50, 0xffff0000, v39\n\t" \
  "v_mfma_f32_16x16x32_bf16 v[152:155], v[80:83], v[96:99], v[152:155]\n\t" \
  "ds_read_b128 v[136:139], v61 offset:12288\n\t" \
  "v_and_b32 v51, 0xffff0000, v41\n\t" \
  "v_mfma_f32_16x16x32_bf16 v[152:155], v[124:127], v[132:135], v[152:155]\n\t" \
  "ds_read_b128 v[144:147], v60 offset:12288\n\t" \
  "v_mul_f32 v45, v45, v50\n\t" \
  "v_mfma_f32_16x16x32_bf16 v[160:163], v[124:127], v[108:111], v[160:163]\n\t" \
  "ds_read_b128 v[72:75], v60 offset:14336\n\t" \
  "v_mul_f32 v49, v49, v51\n\t" \
  "v_mfma_f32_16x16x32_bf16 v[160:163], v[80:83], v[148:151], v[160:163]\n\t" \
  "ds_read_b128 v[140:143], v61 offset:14336\n\t" \
  "ds_read_b64_tr_b16 v[38:39], v1 offset:800\n\t" \
  "v_mfma_f32_16x16x32_bf16 v[168:171], v[80:83], v[64:67], v[168:171]\n\t" \
  "ds_read_b128 v[68:71], v61 offset:16384\n\t" \
  "ds_read_b64_tr_b16 v[40:41], v1 offset:288\n\t" \
  "v_mfma_f32_16x16x32_bf16 v[168:171], v[124:127], v[104:107], v[168:171]\n\t" \
  "ds_read_b128 v[76:79], v60 offset:16384\n\t" \
  "v_cvt_pk_bf16_f32 v45, v45, v49\n\t" \
  "v_mfma_f32_16x16x32_bf16 v[176:179], v[124:127], v[112:115], v[176:179]\n\t" \
  "ds_read_b64_tr_b16 v[124:125], v63 offset:26624\n\t" \
  "v_mfma_f32_16x16x32_bf16 v[176:179], v[80:83], v[116:119], v[176:179]\n\t" \
  "ds_read_b64_tr_b16 v[126:127], v63 offset:27136\n\t" \
  "v_mfma_f32_16x16x32_bf16 v[156:159], v[84:87], v[96:99], v[156:159]\n\t" \
  "ds_read_b64_tr_b16 v[80:81], v63 offset:30720\n\t" \
  "v_mfma_f32_16x16x32_bf16 v[156:159], v[92:95], v[132:135], v[156:159]\n\t" \
  "ds_read_b64_tr_b16 v[82:83], v63 offset:31232\n\t" \
  "v_mfma_f32_16x16x32_bf16 v[164:167], v[92:95], v[108:111], v[164:167]\n\t" \
  "ds_read_b64_tr_b16 v[96:97], v62 offset:34816\n\t" \
  "v_mfma_f32_16x16x32_bf16 v[164:167], v[84:87], v[148:151], v[164:167]\n\t" \
  "ds_read_b64_tr_b16 v[98:99], v62 offset:35328\n\t" \
  "v_mfma_f32_16x16x32_bf16 v[172:175], v[84:87], v[64:67], v[172:175]\n\t" \
  "ds_read_b64_tr_b16 v[132:133], v62 offset:38912\n\t" \
  "v_mfma_f32_16x16x32_bf16 v[172:175], v[92:95], v[104:107], v[172:175]\n\t" \
  "ds_read_b64_tr_b16 v[134:135], v62 offset:39424\n\t" \
  "v_mfma_f32_16x16x32_bf16 v[180:183], v[92:95], v[112:115], v[180:183]\n\t" \
  "ds_read_b128 v[108:111], v60 offset:0\n\t" \
  "v_mfma_f32_16x16x32_bf16 v[180:183], v[84:87], v[116:119], v[180:183]\n\t" \
  "ds_read_b128 v[148:151], v61 offset:0\n\t" \
  "s_waitcnt lgkmcnt(7)\n\t" \
  "v_mfma_f32_16x16x32_bf16 v[184:187], v[88:91], v[128:131], v[184:187]\n\t" \
  "ds_read_b64_tr_b16 v[64:65], v63 offset:34816\n\t" \
  "v_mfma_f32_16x16x32_bf16 v[184:187], v[100:103], v[120:123], v[184:187]\n\t" \
  "ds_read_b64_tr_b16 v[66:67], v63 offset:35328\n\t" \
  "v_mfma_f32_16x16x32_bf16 v[192:195], v[100:103], v[136:139], v[192:195]\n\t" \
  "ds_read_b64_tr_b16 v[104:105], v63 offset:38912\n\t" \
  "v_mfma_f32_16x16x32_bf16 v[192:195], v[88:91], v[144:147], v[192:195]\n\t" \
  "ds_read_b64_tr_b16 v[106:107], v63 offset:39424\n\t" \
  "v_mfma_f32_16x16x32_bf16 v[200:203], v[88:91], v[72:75], v[200:203]\n\t" \
  "ds_read_b64_tr_b16 v[92:93], v62 offset:18432\n\t" \
  "v_mfma_f32_16x16x32_bf16 v[200:203], v[100:103], v[140:143], v[200:203]\n\t" \
  "ds_read_b64_tr_b16 v[94:95], v62 offset:18944\n\t" \
  "v_mfma_f32_16x16x32_bf16 v[208:211], v[100:103], v[68:71], v[208:211]\n\t" \
  "ds_read_b64_tr_b16 v[112:113], v62 offset:22528\n\t" \
  "v_mfma_f32_16x16x32_bf16 v[208:211], v[88:91], v[76:79], v[208:211]\n\t" \
  "ds_read_b64_tr_b16 v[114:115], v62 offset:23040\n\t" \
  "v_mfma_f32_16x16x32_bf16 v[188:191], v[124:127], v[128:131], v[188:191]\n\t" \
  "ds_read_b64_tr_b16 v[84:85], v63 offset:18432\n\t" \
  "s_waitcnt lgkmcnt(7)\n\t" \
  "v_mfma_f32_16x16x32_bf16 v[188:191], v[80:83], v[120:123], v[188:191]\n\t" \
  "ds_read_b64_tr_b16 v[86:87], v63 offset:18944\n\t" \
  "v_mfma_f32_16x16x32_bf16 v[196:199], v[80:83], v[136:139], v[196:199]\n\t" \
  "ds_read_b64_tr_b16 v[116:117], v63 offset:22528\n\t" \
  "v_mfma_f32_16x16x32_bf16 v[196:199], v[124:127], v[144:147], v[196:199]\n\t" \
  "ds_read_b64_tr_b16 v[118:119], v63 offset:23040\n\t" \
  "v_mfma_f32_16x16x32_bf16 v[204:207], v[124:127], v[72:75], v[204:207]\n\t" \
  "v_mfma_f32_16x16x32_bf16 v[204:207], v[80:83], v[140:143], v[204:207]\n\t" \
  "v_mfma_f32_16x16x32_bf16 v[212:215], v[80:83], v[68:71], v[212:215]\n\t" \
  "v_mfma_f32_16x16x32_bf16 v[212:215], v[124:127], v[76:79], v[212:215]\n\t" \
  "v_mfma_f32_16x16x32_bf16 v[216:219], v[128:131], v[96:99], v[216:219]\n\t" \
  "v_mfma_f32_16x16x32_bf16 v[216:219], v[120:123], v[132:135], v[216:219]\n\t" \
  "v_mfma_f32_16x16x32_bf16 v[224:227], v[136:139], v[132:135], v[224:227]\n\t" \
  "v_mfma_f32_16x16x32_bf16 v[224:227], v[144:147], v[96:99], v[224:227]\n\t" \
  "v_mfma_f32_16x16x32_bf16 v[232:235], v[72:75], v[96:99], v[232:235]\n\t" \
  "global_store_dwordx4 v2, v[42:45], %24 offset:0 sc1\n\t" \
  "v_mfma_f32_16x16x32_bf16 v[232:235], v[140:143], v[132:135], v[232:235]\n\t" \
  "s_nop 0\n\t" \
  "v_mfma_f32_16x16x32_bf16 v[42:45], v[26:29], v[18:21], 0\n\t" \
  "v_mfma_f32_16x16x32_bf16 v[42:45], v[30:33], v[22:25], v[42:45]\n\t" \
  "v_mfma_f32_16x16x32_bf16 v[46:49], v[30:33], v[18:21], 0\n\t" \
  "v_mfma_f32_16x16x32_bf16 v[46:49], v[34:37], v[22:25], v[46:49]\n\t" \
  "v_mfma_f32_16x16x32_bf16 v[240:243], v[68:71], v[132:135], v[240:243]\n\t" \
  "v_mfma_f32_16x16x32_bf16 v[240:243], v[76:79], v[96:99], v[240:243]\n\t" \
  "v_lshlrev_b32 v50, 16, v38\n\t" \
  "v_lshlrev_b32 v51, 16, v40\n\t" \
  "v_mfma_f32_16x16x32_bf16 v[248:251], v[108:111], v[96:99], v[248:251]\n\t" \
  "s_nop 0\n\t" \
  "v_mul_f32 v42, v42, v50\n\t" \
  "s_nop 0\n\t" \
  "v_mul_f32 v46, v46, v51\n\t" \
  "v_mfma_f32_16x16x32_bf16 v[248:251], v[148:151], v[132:135], v[248:251]\n\t" \
  "v_cvt_pk_bf16_f32 v42, v42, v46\n\t" \
  "v_and_b32 v50, 0xffff0000, v38\n\t" \
  "v_mfma_f32_16x16x32_bf16 v[252:255], v[108:111], v[64:67], v[252:255]\n\t" \
  "v_and_b32 v51, 0xffff0000, v40\n\t" \
  "v_mul_f32 v43, v43, v50\n\t" \
  "s_waitcnt lgkmcnt(0)\n\t" \
  "v_mfma_f32_16x16x32_bf16 v[252:255], v[148:151], v[104:107], v[252:255]\n\t" \
  "v_mul_f32 v47, v47, v51\n\t" \
  "v_cvt_pk_bf16_f32 v43, v43, v47\n\t" \
  "v_mfma_f32_16x16x32_bf16 v[220:223], v[120:123], v[104:107], v[220:223]\n\t" \
  "v_lshlrev_b32 v50, 16, v39\n\t" \
  "v_lshlrev_b32 v51, 16, v41\n\t" \
  "v_mfma_f32_16x16x32_bf16 v[220:223], v[128:131], v[64:67], v[220:223]\n\t" \
  "v_mul_f32 v44, v44, v50\n\t" \
  "v_mul_f32 v48, v48, v51\n\t" \
  "s_barrier\n\t" \
  "v_mfma_f32_16x16x32_bf16 v[228:231], v[144:147], v[64:67], v[228:231]\n\t" \
  "ds_read_b64_tr_b16 v[100:101], v54 offset:26624\n\t" \
  "v_cvt_pk_bf16_f32 v44, v44, v48\n\t" \
  "v_mfma_f32_16x16x32_bf16 v[228:231], v[136:139], v[104:107], v[228:231]\n\t" \
  "ds_read_b64_tr_b16 v[102:103], v54 offset:27136\n\t" \
  "v_and_b32 v50, 0xffff0000, v39\n\t" \
  "v_mfma_f32_16x16x32_bf16 v[236:239], v[140:143], v[104:107], v[236:239]\n\t" \
  "ds_read_b128 v[88:91], v52 offset:10240\n\t" \
  "v_and_b32 v51, 0xffff0000, v41\n\t" \
  "v_mfma_f32_16x16x32_bf16 v[236:239], v[72:75], v[64:67], v[236:239]\n\t" \
  "ds_read_b64_tr_b16 v[80:81], v54 offset:30720\n\t" \
  "v_mul_f32 v45, v45, v50\n\t" \
  "v_mfma_f32_16x16x32_bf16 v[244:247], v[76:79], v[64:67], v[244:247]\n\t" \
  "ds_read_b64_tr_b16 v[82:83], v54 offset:31232\n\t" \
  "v_mul_f32 v49, v49, v51\n\t" \
  "v_mfma_f32_16x16x32_bf16 v[244:247], v[68:71], v[104:107], v[244:247]\n\t" \
  "ds_read_b128 v[124:127], v53 offset:10240\n\t" \
  "v_cvt_pk_bf16_f32 v45, v45, v49\n\t" \
  "v_mfma_f32_16x16x32_bf16 v[152:155], v[92:95], v[128:131], v[152:155]\n\t" \
  "ds_read_b128 v[96:99], v53 offset:12288\n\t" \
  "v_mfma_f32_16x16x32_bf16 v[152:155], v[112:115], v[120:123], v[152:155]\n\t" \
  "ds_read_b128 v[132:135], v52 offset:12288\n\t" \
  "v_mfma_f32_16x16x32_bf16 v[160:163], v[112:115], v[136:139], v[160:163]\n\t" \
  "ds_read_b128 v[108:111], v52 offset:14336\n\t" \
  "v_mfma_f32_16x16x32_bf16 v[160:163], v[92:95], v[144:147], v[160:163]\n\t" \
  "ds_read_b128 v[148:151], v53 offset:14336\n\t" \
  "v_mfma_f32_16x16x32_bf16 v[168:171], v[92:95], v[72:75], v[168:171]\n\t" \
  "ds_read_b128 v[64:67], v53 offset:16384\n\t" \
  "v_mfma_f32_16x16x32_bf16 v[168:171], v[112:115], v[140:143], v[168:171]\n\t" \
  "ds_read_b128 v[104:107], v52 offset:16384\n\t" \
  "v_mfma_f32_16x16x32_bf16 v[176:179], v[112:115], v[68:71], v[176:179]\n\t" \
  "ds_read_b64_tr_b16 v[112:113], v55 offset:26624\n\t" \
  "v_mfma_f32_16x16x32_bf16 v[176:179], v[92:95], v[76:79], v[176:179]\n\t" \
  "ds_read_b64_tr_b16 v[114:115], v55 offset:27136\n\t" \
  "v_mfma_f32_16x16x32_bf16 v[156:159], v[84:87], v[128:131], v[156:159]\n\t" \
  "ds_read_b64_tr_b16 v[92:93], v55 offset:30720\n\t" \
  "v_mfma_f32_16x16x32_bf16 v[156:159], v[116:119], v[120:123], v[156:159]\n\t" \
  "ds_read_b64_tr_b16 v[94:95], v55 offset:31232\n\t" \
  "v_mfma_f32_16x16x32_bf16 v[164:167], v[116:119], v[136:139], v[164:167]\n\t" \
  "ds_read_b64_tr_b16 v[128:129], v54 offset:34816\n\t" \
  "v_mfma_f32_16x16x32_bf16 v[164:167], v[84:87], v[144:147], v[164:167]\n\t" \
  "ds_read_b64_tr_b16 v[130:131], v54 offset:35328\n\t" \
  "v_mfma_f32_16x16x32_bf16 v[172:175], v[84:87], v[72:75], v[172:175]\n\t" \
  "ds_read_b64_tr_b16 v[120:121], v54 offset:38912\n\t" \
  "v_mfma_f32_16x16x32_bf16 v[172:175], v[116:119], v[140:143], v[172:175]\n\t" \
  "ds_read_b64_tr_b16 v[122:123], v54 offset:39424\n\t" \
  "v_mfma_f32_16x16x32_bf16 v[180:183], v[116:119], v[68:71], v[180:183]\n\t" \
  "ds_read_b128 v[136:139], v52 offset:0\n\t" \
  "v_mfma_f32_16x16x32_bf16 v[180:183], v[84:87], v[76:79], v[180:183]\n\t" \
  "ds_read_b128 v[144:147], v53 offset:0\n\t" \
  "s_waitcnt lgkmcnt(7)\n\t" \
  "v_mfma_f32_16x16x32_bf16 v[184:187], v[100:103], v[88:91], v[184:187]\n\t" \
  "ds_read_b64_tr_b16 v[72:73], v55 offset:34816\n\t" \
  "v_mfma_f32_16x16x32_bf16 v[184:187], v[80:83], v[124:127], v[184:187]\n\t" \
  "ds_read_b64_tr_b16 v[74:75], v55 offset:35328\n\t" \
  "v_mfma_f32_16x16x32_bf16 v[192:195], v[80:83], v[96:99], v[192:195]\n\t" \
  "ds_read_b64_tr_b16 v[140:141], v55 offset:38912\n\t" \
  "v_mfma_f32_16x16x32_bf16 v[192:195], v[100:103], v[132:135], v[192:195]\n\t" \
  "ds_read_b64_tr_b16 v[142:143], v55 offset:39424\n\t" \
  "v_mfma_f32_16x16x32_bf16 v[200:203], v[100:103], v[108:111], v[200:203]\n\t" \
  "ds_read_b64_tr_b16 v[68:69], v54 offset:18432\n\t" \
  "v_mfma_f32_16x16x32_bf16 v[200:203], v[80:83], v[148:151], v[200:203]\n\t" \
  "ds_read_b64_tr_b16 v[70:71], v54 offset:18944\n\t" \
  "v_mfma_f32_16x16x32_bf16 v[208:211], v[80:83], v[64:67], v[208:211]\n\t" \
  "ds_read_b64_tr_b16 v[116:117], v54 offset:22528\n\t" \
  "v_mfma_f32_16x16x32_bf16 v[208:211], v[100:103], v[104:107], v[208:211]\n\t" \
  "ds_read_b64_tr_b16 v[118:119], v54 offset:23040\n\t" \
  "v_mfma_f32_16x16x32_bf16 v[188:191], v[112:115], v[88:91], v[188:191]\n\t" \
  "ds_read_b64_tr_b16 v[76:77], v55 offset:18432\n\t" \
  "s_waitcnt lgkmcnt(7)\n\t" \
  "v_mfma_f32_16x16x32_bf16 v[188:191], v[92:95], v[124:127], v[188:191]\n\t" \
  "ds_read_b64_tr_b16 v[78:79], v55 offset:18944\n\t" \
  "v_mfma_f32_16x16x32_bf16 v[196:199], v[92:95], v[96:99], v[196:199]\n\t" \
  "ds_read_b64_tr_b16 v[84:85], v55 offset:22528\n\t" \
  "v_mfma_f32_16x16x32_bf16 v[196:199], v[112:115], v[132:135], v[196:199]\n\t" \
  "ds_read_b64_tr_b16 v[86:87], v55 offset:23040\n\t" \
  "v_mfma_f32_16x16x32_bf16 v[204:207], v[112:115], v[108:111], v[204:207]\n\t" \
  "v_mfma_f32_16x16x32_bf16 v[204:207], v[92:95], v[148:151], v[204:207]\n\t" \
  "v_mfma_f32_16x16x32_bf16 v[212:215], v[92:95], v[64:67], v[212:215]\n\t" \
  "v_mfma_f32_16x16x32_bf16 v[212:215], v[112:115], v[104:107], v[212:215]\n\t" \
  "v_mfma_f32_16x16x32_bf16 v[216:219], v[88:91], v[128:131], v[216:219]\n\t" \
  "v_mfma_f32_16x16x32_bf16 v[216:219], v[124:127], v[120:123], v[216:219]\n\t" \
  "v_mfma_f32_16x16x32_bf16 v[224:227], v[96:99], v[120:123], v[224:227]\n\t" \
  "v_mfma_f32_16x16x32_bf16 v[224:227], v[132:135], v[128:131], v[224:227]\n\t" \
  "v_mfma_f32_16x16x32_bf16 v[232:235], v[108:111], v[128:131], v[232:235]\n\t" \
  "global_store_dwordx4 v2, v[42:45], %24 offset:1024 sc1\n\t" \
  "v_mfma_f32_16x16x32_bf16 v[232:235], v[148:151], v[120:123], v[232:235]\n\t" \
  "v_mfma_f32_16x16x32_bf16 v[240:243], v[64:67], v[120:123], v[240:243]\n\t" \
  "v_mfma_f32_16x16x32_bf16 v[240:243], v[104:107], v[128:131], v[240:243]\n\t" \
  "v_mov_b32 v30, 0xff61b1e6\n\t" \
  "v_bfe_i32 v34, v3, 0, 1\n\t" \
  "v_mfma_f32_16x16x32_bf16 v[248:251], v[136:139], v[128:131], v[248:251]\n\t" \
  "v_and_b32 v34, v30, v34\n\t" \
  "v_bfe_i32 v38, v3, 4, 1\n\t" \
  "v_mfma_f32_16x16x32_bf16 v[248:251], v[144:147], v[120:123], v[248:251]\n\t" \
  "v_and_b32 v38, v30, v38\n\t" \
  "v_bfe_i32 v42, v3, 8, 1\n\t" \
  "v_mfma_f32_16x16x32_bf16 v[252:255], v[136:139], v[72:75], v[252:255]\n\t" \
  "v_bfi_b32 v42, v42, v30, %15\n\t" \
  "v_mov_b32 v48, 0x3f803f80\n\t" \
  "s_waitcnt lgkmcnt(0)\n\t" \
  "v_mfma_f32_16x16x32_bf16 v[252:255], v[144:147], v[140:143], v[252:255]\n\t" \
  "v_bfe_i32 v35, v3, 1, 1\n\t" \
  "v_and_b32 v35, v30, v35\n\t" \
  "v_mfma_f32_16x16x32_bf16 v[220:223], v[124:127], v[140:143], v[220:223]\n\t" \
  "v_bfe_i32 v39, v3, 5, 1\n\t" \
  "v_and_b32 v39, v30, v39\n\t" \
  "v_mfma_f32_16x16x32_bf16 v[220:223], v[88:91], v[72:75], v[220:223]\n\t" \
  "v_bfe_i32 v43, v3, 9, 1\n\t" \
  "v_bfi_b32 v43, v43, v30, %15\n\t" \
  "s_barrier\n\t" \
  "v_mfma_f32_16x16x32_bf16 v[228:231], v[132:135], v[72:75], v[228:231]\n\t" \
  "ds_read_b64_tr_b16 v[80:81], v58 offset:26624\n\t" \
  "v_mov_b32 v49, 0x3f803f80\n\t" \
  "v_mfma_f32_16x16x32_bf16 v[228:231], v[96:99], v[140:143], v[228:231]\n\t" \
  "ds_read_b64_tr_b16 v[82:83], v58 offset:27136\n\t" \
  "v_bfe_i32 v36, v3, 2, 1\n\t" \
  "v_mfma_f32_16x16x32_bf16 v[236:239], v[148:151], v[140:143], v[236:239]\n\t" \
  "ds_read_b128 v[100:103], v56 offset:10240\n\t" \
  "v_and_b32 v36, v30, v36\n\t" \
  "v_mfma_f32_16x16x32_bf16 v[236:239], v[108:111], v[72:75], v[236:239]\n\t" \
  "ds_read_b64_tr_b16 v[92:93], v58 offset:30720\n\t" \
  "v_bfe_i32 v40, v3, 6, 1\n\t" \
  "v_mfma_f32_16x16x32_bf16 v[244:247], v[104:107], v[72:75], v[244:247]\n\t" \
  "ds_read_b64_tr_b16 v[94:95], v58 offset:31232\n\t" \
  "v_and_b32 v40, v30, v40\n\t" \
  "v_mfma_f32_16x16x32_bf16 v[244:247], v[64:67], v[140:143], v[244:247]\n\t" \
  "ds_read_b128 v[112:115], v57 offset:10240\n\t" \
  "v_bfe_i32 v44, v3, 10, 1\n\t" \
  "v_mfma_f32_16x16x32_bf16 v[152:155], v[68:71], v[88:91], v[152:155]\n\t" \
  "ds_read_b128 v[128:131], v57 offset:12288\n\t" \
  "v_bfi_b32 v44, v44, v30, %15\n\t" \
  "v_mfma_f32_16x16x32_bf16 v[152:155], v[116:119], v[124:127], v[152:155]\n\t" \
  "ds_read_b128 v[120:123], v56 offset:12288\n\t" \
  "v_mov_b32 v50, 0x3f803f80\n\t" \
  "v_mfma_f32_16x16x32_bf16 v[160:163], v[116:119], v[96:99], v[160:163]\n\t" \
  "ds_read_b128 v[136:139], v56 offset:14336\n\t" \
  "v_bfe_i32 v37, v3, 3, 1\n\t" \
  "v_mfma_f32_16x16x32_bf16 v[160:163], v[68:71], v[132:135], v[160:163]\n\t" \
  "ds_read_b128 v[144:147], v57 offset:14336\n\t" \
  "v_and_b32 v37, v30, v37\n\t" \
  "v_mfma_f32_16x16x32_bf16 v[168:171], v[68:71], v[108:111], v[168:171]\n\t" \
  "ds_read_b128 v[72:75], v57 offset:16384\n\t" \
  "v_bfe_i32 v41, v3, 7, 1\n\t" \
  "v_mfma_f32_16x16x32_bf16 v[168:171], v[116:119], v[148:151], v[168:171]\n\t" \
  "ds_read_b128 v[140:143], v56 offset:16384\n\t" \
  "v_and_b32 v41, v30, v41\n\t" \
  "v_mfma_f32_16x16x32_bf16 v[176:179], v[116:119], v[64:67], v[176:179]\n\t" \
  "ds_read_b64_tr_b16 v[116:117], v59 offset:26624\n\t" \
  "v_bfe_i32 v45, v3, 11, 1\n\t" \
  "v_mfma_f32_16x16x32_bf16 v[176:179], v[68:71], v[104:107], v[176:179]\n\t" \
  "ds_read_b64_tr_b16 v[118:119], v59 offset:27136\n\t" \
  "v_bfi_b32 v45, v45, v30, %15\n\t" \
  "v_mfma_f32_16x16x32_bf16 v[156:159], v[76:79], v[88:91], v[156:159]\n\t" \
  "ds_read_b64_tr_b16 v[68:69], v59 offset:30720\n\t" \
  "v_mov_b32 v51, 0x3f803f80\n\t" \
  "v_mfma_f32_16x16x32_bf16 v[156:159], v[84:87], v[124:127], v[156:159]\n\t" \
  "ds_read_b64_tr_b16 v[70:71], v59 offset:31232\n\t" \
  "v_bfe_i32 v46, v3, 12, 1\n\t" \
  "v_mfma_f32_16x16x32_bf16 v[164:167], v[84:87], v[96:99], v[164:167]\n\t" \
  "ds_read_b64_tr_b16 v[88:89], v58 offset:34816\n\t" \
  "v_bfe_i32 v31, v3, 13, 1\n\t" \
  "v_mfma_f32_16x16x32_bf16 v[164:167], v[76:79], v[132:135], v[164:167]\n\t" \
  "ds_read_b64_tr_b16 v[90:91], v58 offset:35328\n\t" \
  "v_perm_b32 v46, v31, v46, vcc_hi\n\t" \
  "v_mfma_f32_16x16x32_bf16 v[172:175], v[76:79], v[108:111], v[172:175]\n\t" \
  "ds_read_b64_tr_b16 v[124:125], v58 offset:38912\n\t" \
  "v_bfe_i32 v47, v3, 14, 1\n\t" \
  "v_mfma_f32_16x16x32_bf16 v[172:175], v[84:87], v[148:151], v[172:175]\n\t" \
  "ds_read_b64_tr_b16 v[126:127], v58 offset:39424\n\t" \
  "v_bfe_i32 v31, v3, 15, 1\n\t" \
  "v_mfma_f32_16x16x32_bf16 v[180:183], v[84:87], v[64:67], v[180:183]\n\t" \
  "ds_read_b128 v[96:99], v56 offset:0\n\t" \
  "v_perm_b32 v47, v31, v47, vcc_hi\n\t" \
  "v_mfma_f32_16x16x32_bf16 v[180:183], v[76:79], v[104:107], v[180:183]\n\t" \
  "ds_read_b128 v[132:135], v57 offset:0\n\t" \
  "v_add_u32 v30, 18432, v4\n\t" \
  "s_waitcnt lgkmcnt(7)\n\t" \
  "v_mfma_f32_16x16x32_bf16 v[184:187], v[80:83], v[100:103], v[184:187]\n\t" \
  "ds_read_b64_tr_b16 v[108:109], v59 offset:34816\n\t" \
  "v_xor_b32 v31, 1024, v30\n\t" \
  "v_mfma_f32_16x16x32_bf16 v[184:187], v[92:95], v[112:115], v[184:187]\n\t" \
  "ds_read_b64_tr_b16 v[110:111], v59 offset:35328\n\t" \
  "v_mov_b32 v32, %12\n\t" \
  "v_mfma_f32_16x16x32_bf16 v[192:195], v[92:95], v[128:131], v[192:195]\n\t" \
  "ds_read_b64_tr_b16 v[148:149], v59 offset:38912\n\t" \
  "v_xor_b32 v33, 4, v32\n\t" \
  "v_mfma_f32_16x16x32_bf16 v[192:195], v[80:83], v[120:123], v[192:195]\n\t" \
  "ds_read_b64_tr_b16 v[150:151], v59 offset:39424\n\t" \
  "v_mov_b32 v0, 1\n\t" \
  "v_mfma_f32_16x16x32_bf16 v[200:203], v[80:83], v[136:139], v[200:203]\n\t" \
  "ds_read_b64_tr_b16 v[64:65], v58 offset:18432\n\t" \
  "v_and_b32 v4, 1023, v4\n\t" \
  "v_mfma_f32_16x16x32_bf16 v[200:203], v[92:95], v[144:147], v[200:203]\n\t" \
  "ds_read_b64_tr_b16 v[66:67], v58 offset:18944\n\t" \
  "v_add_u32 v1, 16384, v4\n\t" \
  "v_mfma_f32_16x16x32_bf16 v[208:211], v[92:95], v[72:75], v[208:211]\n\t" \
  "ds_read_b64_tr_b16 v[84:85], v58 offset:22528\n\t" \
  "v_mfma_f32_16x16x32_bf16 v[208:211], v[80:83], v[140:143], v[208:211]\n\t" \
  "ds_read_b64_tr_b16 v[86:87], v58 offset:23040\n\t" \
  "v_mfma_f32_16x16x32_bf16 v[188:191], v[116:119], v[100:103], v[188:191]\n\t" \
  "ds_read_b64_tr_b16 v[76:77], v59 offset:18432\n\t" \
  "s_waitcnt lgkmcnt(7)\n\t" \
  "v_mfma_f32_16x16x32_bf16 v[188:191], v[68:71], v[112:115], v[188:191]\n\t" \
  "ds_read_b64_tr_b16 v[78:79], v59 offset:18944\n\t" \
  "v_mfma_f32_16x16x32_bf16 v[196:199], v[68:71], v[128:131], v[196:199]\n\t" \
  "ds_read_b64_tr_b16 v[104:105], v59 offset:22528\n\t" \
  "v_mfma_f32_16x16x32_bf16 v[196:199], v[116:119], v[120:123], v[196:199]\n\t" \
  "ds_read_b64_tr_b16 v[106:107], v59 offset:23040\n\t" \
  "v_mfma_f32_16x16x32_bf16 v[204:207], v[116:119], v[136:139], v[204:207]\n\t" \
  "v_mfma_f32_16x16x32_bf16 v[204:207], v[68:71], v[144:147], v[204:207]\n\t" \
  "v_mfma_f32_16x16x32_bf16 v[212:215], v[68:71], v[72:75], v[212:215]\n\t" \
  "v_mfma_f32_16x16x32_bf16 v[212:215], v[116:119], v[140:143], v[212:215]\n\t" \
  "v_mfma_f32_16x16x32_bf16 v[216:219], v[100:103], v[88:91], v[216:219]\n\t" \
  "v_mfma_f32_16x16x32_bf16 v[216:219], v[112:115], v[124:127], v[216:219]\n\t" \
  "v_mfma_f32_16x16x32_bf16 v[224:227], v[128:131], v[124:127], v[224:227]\n\t" \
  "v_mfma_f32_16x16x32_bf16 v[224:227], v[120:123], v[88:91], v[224:227]\n\t" \
  "v_mfma_f32_16x16x32_bf16 v[232:235], v[136:139], v[88:91], v[232:235]\n\t" \
  "v_mfma_f32_16x16x32_bf16 v[232:235], v[144:147], v[124:127], v[232:235]\n\t" \
  "v_mfma_f32_16x16x32_bf16 v[240:243], v[72:75], v[124:127], v[240:243]\n\t" \
  "v_mfma_f32_16x16x32_bf16 v[240:243], v[140:143], v[88:91], v[240:243]\n\t" \
  "v_mfma_f32_16x16x32_bf16 v[248:251], v[96:99], v[88:91], v[248:251]\n\t" \
  "v_mfma_f32_16x16x32_bf16 v[248:251], v[132:135], v[124:127], v[248:251]\n\t" \
  "v_mfma_f32_16x16x32_bf16 v[252:255], v[96:99], v[108:111], v[252:255]\n\t" \
  "s_waitcnt lgkmcnt(0)\n\t" \
  "v_mfma_f32_16x16x32_bf16 v[252:255], v[132:135], v[148:151], v[252:255]\n\t" \
  "v_mfma_f32_16x16x32_bf16 v[220:223], v[112:115], v[148:151], v[220:223]\n\t" \
  "v_mfma_f32_16x16x32_bf16 v[220:223], v[100:103], v[108:111], v[220:223]\n\t" \
  "s_barrier\n\t" \
  "v_mfma_f32_16x16x32_bf16 v[228:231], v[120:123], v[108:111], v[228:231]\n\t" \
  "ds_read_b64_tr_b16 v[92:93], v62 offset:26624\n\t" \
  "v_mfma_f32_16x16x32_bf16 v[228:231], v[128:131], v[148:151], v[228:231]\n\t" \
  "ds_read_b64_tr_b16 v[94:95], v62 offset:27136\n\t" \
  "v_mfma_f32_16x16x32_bf16 v[236:239], v[144:147], v[148:151], v[236:239]\n\t" \
  "ds_read_b128 v[80:83], v60 offset:10240\n\t" \
  "v_mfma_f32_16x16x32_bf16 v[236:239], v[136:139], v[108:111], v[236:239]\n\t" \
  "ds_read_b64_tr_b16 v[68:69], v62 offset:30720\n\t" \
  "v_mfma_f32_16x16x32_bf16 v[244:247], v[140:143], v[108:111], v[244:247]\n\t" \
  "ds_read_b64_tr_b16 v[70:71], v62 offset:31232\n\t" \
  "v_mfma_f32_16x16x32_bf16 v[244:247], v[72:75], v[148:151], v[244:247]\n\t" \
  "ds_read_b128 v[116:119], v61 offset:10240\n\t" \
  "v_mfma_f32_16x16x32_bf16 v[152:155], v[64:67], v[100:103], v[152:155]\n\t" \
  "ds_read_b128 v[88:91], v61 offset:12288\n\t" \
  "v_mfma_f32_16x16x32_bf16 v[152:155], v[84:87], v[112:115], v[152:155]\n\t" \
  "ds_read_b128 v[124:127], v60 offset:12288\n\t" \
  "v_mfma_f32_16x16x32_bf16 v[160:163], v[84:87], v[128:131], v[160:163]\n\t" \
  "ds_read_b128 v[96:99], v60 offset:14336\n\t" \
  "v_mfma_f32_16x16x32_bf16 v[160:163], v[64:67], v[120:123], v[160:163]\n\t" \
  "ds_read_b128 v[132:135], v61 offset:14336\n\t" \
  "v_mfma_f32_16x16x32_bf16 v[168:171], v[64:67], v[136:139], v[168:171]\n\t" \
  "ds_read_b128 v[108:111], v61 offset:16384\n\t" \
  "v_mfma_f32_16x16x32_bf16 v[168:171], v[84:87], v[144:147], v[168:171]\n\t" \
  "ds_read_b128 v[148:151], v60 offset:16384\n\t" \
  "v_mfma_f32_16x16x32_bf16 v[176:179], v[84:87], v[72:75], v[176:179]\n\t" \
  "ds_read_b64_tr_b16 v[84:85], v63 offset:26624\n\t" \
  "v_mfma_f32_16x16x32_bf16 v[176:179], v[64:67], v[140:143], v[176:179]\n\t" \
  "ds_read_b64_tr_b16 v[86:87], v63 offset:27136\n\t" \
  "v_mfma_f32_16x16x32_bf16 v[156:159], v[76:79], v[100:103], v[156:159]\n\t" \
  "ds_read_b64_tr_b16 v[64:65], v63 offset:30720\n\t" \
  "v_mfma_f32_16x16x32_bf16 v[156:159], v[104:107], v[112:115], v[156:159]\n\t" \
  "ds_read_b64_tr_b16 v[66:67], v63 offset:31232\n\t" \
  "v_mfma_f32_16x16x32_bf16 v[164:167], v[104:107], v[128:131], v[164:167]\n\t" \
  "ds_read_b64_tr_b16 v[100:101], v62 offset:34816\n\t" \
  "v_mfma_f32_16x16x32_bf16 v[164:167], v[76:79], v[120:123], v[164:167]\n\t" \
  "ds_read_b64_tr_b16 v[102:103], v62 offset:35328\n\t" \
  "v_mfma_f32_16x16x32_bf16 v[172:175], v[76:79], v[136:139], v[172:175]\n\t" \
  "ds_read_b64_tr_b16 v[112:113], v62 offset:38912\n\t" \
  "v_mfma_f32_16x16x32_bf16 v[172:175], v[104:107], v[144:147], v[172:175]\n\t" \
  "ds_read_b64_tr_b16 v[114:115], v62 offset:39424\n\t" \
  "v_mfma_f32_16x16x32_bf16 v[180:183], v[104:107], v[72:75], v[180:183]\n\t" \
  "ds_read_b128 v[128:131], v60 offset:0\n\t" \
  "v_mfma_f32_16x16x32_bf16 v[180:183], v[76:79], v[140:143], v[180:183]\n\t" \
  "ds_read_b128 v[120:123], v61 offset:0\n\t" \
  "s_waitcnt lgkmcnt(7)\n\t" \
  "v_mfma_f32_16x16x32_bf16 v[184:187], v[92:95], v[80:83], v[184:187]\n\t" \
  "ds_read_b64_tr_b16 v[136:137], v63 offset:34816\n\t" \
  "v_mfma_f32_16x16x32_bf16 v[184:187], v[68:71], v[116:119], v[184:187]\n\t" \
  "ds_read_b64_tr_b16 v[138:139], v63 offset:35328\n\t" \
  "v_mfma_f32_16x16x32_bf16 v[192:195], v[68:71], v[88:91], v[192:195]\n\t" \
  "ds_read_b64_tr_b16 v[144:145], v63 offset:38912\n\t" \
  "v_mfma_f32_16x16x32_bf16 v[192:195], v[92:95], v[124:127], v[192:195]\n\t" \
  "ds_read_b64_tr_b16 v[146:147], v63 offset:39424\n\t" \
  "v_mfma_f32_16x16x32_bf16 v[200:203], v[92:95], v[96:99], v[200:203]\n\t" \
  "ds_read_b64_tr_b16 v[72:73], v62 offset:18432\n\t" \
  "v_mfma_f32_16x16x32_bf16 v[200:203], v[68:71], v[132:135], v[200:203]\n\t" \
  "ds_read_b64_tr_b16 v[74:75], v62 offset:18944\n\t" \
  "v_mfma_f32_16x16x32_bf16 v[208:211], v[68:71], v[108:111], v[208:211]\n\t" \
  "ds_read_b64_tr_b16 v[104:105], v62 offset:22528\n\t" \
  "v_mfma_f32_16x16x32_bf16 v[208:211], v[92:95], v[148:151], v[208:211]\n\t" \
  "ds_read_b64_tr_b16 v[106:107], v62 offset:23040\n\t" \
  "v_mfma_f32_16x16x32_bf16 v[188:191], v[84:87], v[80:83], v[188:191]\n\t" \
  "ds_read_b64_tr_b16 v[76:77], v63 offset:18432\n\t" \
  "s_waitcnt lgkmcnt(7)\n\t" \
  "v_mfma_f32_16x16x32_bf16 v[188:191], v[64:67], v[116:119], v[188:191]\n\t" \
  "ds_read_b64_tr_b16 v[78:79], v63 offset:18944\n\t" \
  "v_mfma_f32_16x16x32_bf16 v[196:199], v[64:67], v[88:91], v[196:199]\n\t" \
  "ds_read_b64_tr_b16 v[140:141], v63 offset:22528\n\t" \
  "v_mfma_f32_16x16x32_bf16 v[196:199], v[84:87], v[124:127], v[196:199]\n\t" \
  "ds_read_b64_tr_b16 v[142:143], v63 offset:23040\n\t" \
  "v_mfma_f32_16x16x32_bf16 v[204:207], v[84:87], v[96:99], v[204:207]\n\t" \
  "v_mfma_f32_16x16x32_bf16 v[204:207], v[64:67], v[132:135], v[204:207]\n\t" \
  "v_mfma_f32_16x16x32_bf16 v[212:215], v[64:67], v[108:111], v[212:215]\n\t" \
  "v_mfma_f32_16x16x32_bf16 v[212:215], v[84:87], v[148:151], v[212:215]\n\t" \
  "v_mfma_f32_16x16x32_bf16 v[216:219], v[80:83], v[100:103], v[216:219]\n\t" \
  "501:\n\t" \
  "s_cbranch_execz 505f\n\t" \
  "v_mfma_f32_16x16x32_bf16 v[216:219], v[116:119], v[112:115], v[216:219]\n\t" \
  "v_mfma_f32_16x16x32_bf16 v[224:227], v[88:91], v[112:115], v[224:227]\n\t" \
  "v_mfma_f32_16x16x32_bf16 v[224:227], v[124:127], v[100:103], v[224:227]\n\t" \
  "v_mfma_f32_16x16x32_bf16 v[232:235], v[96:99], v[100:103], v[232:235]\n\t" \
  "v_mfma_f32_16x16x32_bf16 v[232:235], v[132:135], v[112:115], v[232:235]\n\t" \
  "v_mfma_f32_16x16x32_bf16 v[240:243], v[108:111], v[112:115], v[240:243]\n\t" \
  "v_mfma_f32_16x16x32_bf16 v[240:243], v[148:151], v[100:103], v[240:243]\n\t" \
  "v_mfma_f32_16x16x32_bf16 v[248:251], v[128:131], v[100:103], v[248:251]\n\t" \
  "v_mfma_f32_16x16x32_bf16 v[248:251], v[120:123], v[112:115], v[248:251]\n\t" \
  "v_mfma_f32_16x16x32_bf16 v[252:255], v[128:131], v[136:139], v[252:255]\n\t" \
  "s_waitcnt lgkmcnt(0)\n\t" \
  "v_mfma_f32_16x16x32_bf16 v[252:255], v[120:123], v[144:147], v[252:255]\n\t" \
  "v_mfma_f32_16x16x32_bf16 v[220:223], v[116:119], v[144:147], v[220:223]\n\t" \
  "v_mfma_f32_16x16x32_bf16 v[220:223], v[80:83], v[136:139], v[220:223]\n\t" \
  "s_waitcnt vmcnt(0)\n\t" \
  "s_barrier\n\t" \
  "v_mfma_f32_16x16x32_bf16 v[228:231], v[124:127], v[136:139], v[228:231]\n\t" \
  "ds_read_b64_tr_b16 v[68:69], v54 offset:26624\n\t" \
  "v_mfma_f32_16x16x32_bf16 v[228:231], v[88:91], v[144:147], v[228:231]\n\t" \
  "ds_read_b64_tr_b16 v[70:71], v54 offset:27136\n\t" \
  "v_mfma_f32_16x16x32_bf16 v[236:239], v[132:135], v[144:147], v[236:239]\n\t" \
  "ds_read_b128 v[92:95], v52 offset:10240\n\t" \
  "v_mfma_f32_16x16x32_bf16 v[236:239], v[96:99], v[136:139], v[236:239]\n\t" \
  "ds_read_b64_tr_b16 v[64:65], v54 offset:30720\n\t" \
  "v_mfma_f32_16x16x32_bf16 v[244:247], v[148:151], v[136:139], v[244:247]\n\t" \
  "ds_read_b64_tr_b16 v[66:67], v54 offset:31232\n\t" \
  "v_mfma_f32_16x16x32_bf16 v[244:247], v[108:111], v[144:147], v[244:247]\n\t" \
  "ds_read_b128 v[84:87], v53 offset:10240\n\t" \
  "v_mfma_f32_16x16x32_bf16 v[152:155], v[72:75], v[80:83], v[152:155]\n\t" \
  "ds_read_b128 v[100:103], v53 offset:12288\n\t" \
  "v_mfma_f32_16x16x32_bf16 v[152:155], v[104:107], v[116:119], v[152:155]\n\t" \
  "ds_read_b128 v[112:115], v52 offset:12288\n\t" \
  "v_mfma_f32_16x16x32_bf16 v[160:163], v[104:107], v[88:91], v[160:163]\n\t" \
  "ds_read_b128 v[128:131], v52 offset:14336\n\t" \
  "v_mfma_f32_16x16x32_bf16 v[160:163], v[72:75], v[124:127], v[160:163]\n\t" \
  "ds_read_b128 v[120:123], v53 offset:14336\n\t" \
  "v_mfma_f32_16x16x32_bf16 v[168:171], v[72:75], v[96:99], v[168:171]\n\t" \
  "ds_read_b128 v[136:139], v53 offset:16384\n\t" \
  "v_mfma_f32_16x16x32_bf16 v[168:171], v[104:107], v[132:135], v[168:171]\n\t" \
  "ds_read_b128 v[144:147], v52 offset:16384\n\t" \
  "v_mfma_f32_16x16x32_bf16 v[176:179], v[104:107], v[108:111], v[176:179]\n\t" \
  "ds_read_b64_tr_b16 v[104:105], v55 offset:26624\n\t" \
  "v_mfma_f32_16x16x32_bf16 v[176:179], v[72:75], v[148:151], v[176:179]\n\t" \
  "ds_read_b64_tr_b16 v[106:107], v55 offset:27136\n\t" \
  "v_mfma_f32_16x16x32_bf16 v[156:159], v[76:79], v[80:83], v[156:159]\n\t" \
  "ds_read_b64_tr_b16 v[72:73], v55 offset:30720\n\t" \
  "v_mfma_f32_16x16x32_bf16 v[156:159], v[140:143], v[116:119], v[156:159]\n\t" \
  "ds_read_b64_tr_b16 v[74:75], v55 offset:31232\n\t" \
  "v_mfma_f32_16x16x32_bf16 v[164:167], v[140:143], v[88:91], v[164:167]\n\t" \
  "ds_read_b64_tr_b16 v[80:81], v54 offset:34816\n\t" \
  "v_mfma_f32_16x16x32_bf16 v[164:167], v[76:79], v[124:127], v[164:167]\n\t" \
  "ds_read_b64_tr_b16 v[82:83], v54 offset:35328\n\t" \
  "v_mfma_f32_16x16x32_bf16 v[172:175], v[76:79], v[96:99], v[172:175]\n\t" \
  "ds_read_b64_tr_b16 v[116:117], v54 offset:38912\n\t" \
  "v_mfma_f32_16x16x32_bf16 v[172:175], v[140:143], v[132:135], v[172:175]\n\t" \
  "ds_read_b64_tr_b16 v[118:119], v54 offset:39424\n\t" \
  "v_mfma_f32_16x16x32_bf16 v[180:183], v[140:143], v[108:111], v[180:183]\n\t" \
  "ds_read_b128 v[88:91], v52 offset:0\n\t" \
  "v_mfma_f32_16x16x32_bf16 v[180:183], v[76:79], v[148:151], v[180:183]\n\t" \
  "ds_read_b128 v[124:127], v53 offset:0\n\t" \
  "s_waitcnt lgkmcnt(7)\n\t" \
  "v_mfma_f32_16x16x32_bf16 v[184:187], v[68:71], v[92:95], v[184:187]\n\t" \
  "ds_read_b64_tr_b16 v[96:97], v55 offset:34816\n\t" \
  "v_mfma_f32_16x16x32_bf16 v[184:187], v[64:67], v[84:87], v[184:187]\n\t" \
  "ds_read_b64_tr_b16 v[98:99], v55 offset:35328\n\t" \
  "v_mfma_f32_16x16x32_bf16 v[192:195], v[64:67], v[100:103], v[192:195]\n\t" \
  "ds_read_b64_tr_b16 v[132:133], v55 offset:38912\n\t" \
  "v_mfma_f32_16x16x32_bf16 v[192:195], v[68:71], v[112:115], v[192:195]\n\t" \
  "ds_read_b64_tr_b16 v[134:135], v55 offset:39424\n\t" \
  "v_mfma_f32_16x16x32_bf16 v[200:203], v[68:71], v[128:131], v[200:203]\n\t" \
  "ds_read_b64_tr_b16 v[108:109], v54 offset:18432\n\t" \
  "v_mfma_f32_16x16x32_bf16 v[200:203], v[64:67], v[120:123], v[200:203]\n\t" \
  "ds_read_b64_tr_b16 v[110:111], v54 offset:18944\n\t" \
  "v_mfma_f32_16x16x32_bf16 v[208:211], v[64:67], v[136:139], v[208:211]\n\t" \
  "ds_read_b64_tr_b16 v[140:141], v54 offset:22528\n\t" \
  "v_mfma_f32_16x16x32_bf16 v[208:211], v[68:71], v[144:147], v[208:211]\n\t" \
  "ds_read_b64_tr_b16 v[142:143], v54 offset:23040\n\t" \
  "v_mfma_f32_16x16x32_bf16 v[188:191], v[104:107], v[92:95], v[188:191]\n\t" \
  "ds_read_b64_tr_b16 v[76:77], v55 offset:18432\n\t" \
  "s_waitcnt lgkmcnt(7)\n\t" \
  "v_mfma_f32_16x16x32_bf16 v[188:191], v[72:75], v[84:87], v[188:191]\n\t" \
  "ds_read_b64_tr_b16 v[78:79], v55 offset:18944\n\t" \
  "v_mfma_f32_16x16x32_bf16 v[196:199], v[72:75], v[100:103], v[196:199]\n\t" \
  "ds_read_b64_tr_b16 v[148:149], v55 offset:22528\n\t" \
  "v_mfma_f32_16x16x32_bf16 v[196:199], v[104:107], v[112:115], v[196:199]\n\t" \
  "ds_read_b64_tr_b16 v[150:151], v55 offset:23040\n\t" \
  "v_mfma_f32_16x16x32_bf16 v[204:207], v[104:107], v[128:131], v[204:207]\n\t" \
  "v_mfma_f32_16x16x32_bf16 v[204:207], v[72:75], v[120:123], v[204:207]\n\t" \
  "v_mfma_f32_16x16x32_bf16 v[212:215], v[72:75], v[136:139], v[212:215]\n\t" \
  "v_mfma_f32_16x16x32_bf16 v[212:215], v[104:107], v[144:147], v[212:215]\n\t" \
  "v_mfma_f32_16x16x32_bf16 v[216:219], v[92:95], v[80:83], v[216:219]\n\t" \
  "v_mfma_f32_16x16x32_bf16 v[216:219], v[84:87], v[116:119], v[216:219]\n\t" \
  "v_mfma_f32_16x16x32_bf16 v[224:227], v[100:103], v[116:119], v[224:227]\n\t" \
  "v_mfma_f32_16x16x32_bf16 v[224:227], v[112:115], v[80:83], v[224:227]\n\t" \
  "v_mfma_f32_16x16x32_bf16 v[232:235], v[128:131], v[80:83], v[232:235]\n\t" \
  "v_mfma_f32_16x16x32_bf16 v[232:235], v[120:123], v[116:119], v[232:235]\n\t" \
  "v_mfma_f32_16x16x32_bf16 v[240:243], v[136:139], v[116:119], v[240:243]\n\t" \
  "v_mfma_f32_16x16x32_bf16 v[240:243], v[144:147], v[80:83], v[240:243]\n\t" \
  "v_mfma_f32_16x16x32_bf16 v[248:251], v[88:91], v[80:83], v[248:251]\n\t" \
  "v_mfma_f32_16x16x32_bf16 v[248:251], v[124:127], v[116:119], v[248:251]\n\t" \
  "v_mfma_f32_16x16x32_bf16 v[252:255], v[88:91], v[96:99], v[252:255]\n\t" \
  "s_waitcnt lgkmcnt(0)\n\t" \
  "v_mfma_f32_16x16x32_bf16 v[252:255], v[124:127], v[132:135], v[252:255]\n\t" \
  "v_mfma_f32_16x16x32_bf16 v[220:223], v[84:87], v[132:135], v[220:223]\n\t" \
  "v_mfma_f32_16x16x32_bf16 v[220:223], v[92:95], v[96:99], v[220:223]\n\t" \
  "s_barrier\n\t" \
  "v_mfma_f32_16x16x32_bf16 v[228:231], v[112:115], v[96:99], v[228:231]\n\t" \
  "ds_read_b64_tr_b16 v[64:65], v58 offset:26624\n\t" \
  "v_mfma_f32_16x16x32_bf16 v[228:231], v[100:103], v[132:135], v[228:231]\n\t" \
  "ds_read_b64_tr_b16 v[66:67], v58 offset:27136\n\t" \
  "v_mfma_f32_16x16x32_bf16 v[236:239], v[120:123], v[132:135], v[236:239]\n\t" \
  "ds_read_b128 v[68:71], v56 offset:10240\n\t" \
  "v_mfma_f32_16x16x32_bf16 v[236:239], v[128:131], v[96:99], v[236:239]\n\t" \
  "ds_read_b64_tr_b16 v[72:73], v58 offset:30720\n\t" \
  "v_mfma_f32_16x16x32_bf16 v[244:247], v[144:147], v[96:99], v[244:247]\n\t" \
  "ds_read_b64_tr_b16 v[74:75], v58 offset:31232\n\t" \
  "v_mfma_f32_16x16x32_bf16 v[244:247], v[136:139], v[132:135], v[244:247]\n\t" \
  "ds_read_b128 v[104:107], v57 offset:10240\n\t" \
  "v_mfma_f32_16x16x32_bf16 v[152:155], v[108:111], v[92:95], v[152:155]\n\t" \
  "ds_read_b128 v[80:83], v57 offset:12288\n\t" \
  "v_mfma_f32_16x16x32_bf16 v[152:155], v[140:143], v[84:87], v[152:155]\n\t" \
  "ds_read_b128 v[116:119], v56 offset:12288\n\t" \
  "v_mfma_f32_16x16x32_bf16 v[160:163], v[140:143], v[100:103], v[160:163]\n\t" \
  "ds_read_b128 v[88:91], v56 offset:14336\n\t" \
  "v_mfma_f32_16x16x32_bf16 v[160:163], v[108:111], v[112:115], v[160:163]\n\t" \
  "ds_read_b128 v[124:127], v57 offset:14336\n\t" \
  "v_mfma_f32_16x16x32_bf16 v[168:171], v[108:111], v[128:131], v[168:171]\n\t" \
  "ds_read_b128 v[96:99], v57 offset:16384\n\t" \
  "v_mfma_f32_16x16x32_bf16 v[168:171], v[140:143], v[120:123], v[168:171]\n\t" \
  "ds_read_b128 v[132:135], v56 offset:16384\n\t" \
  "v_mfma_f32_16x16x32_bf16 v[176:179], v[140:143], v[136:139], v[176:179]\n\t" \
  "ds_read_b64_tr_b16 v[140:141], v59 offset:26624\n\t" \
  "v_mfma_f32_16x16x32_bf16 v[176:179], v[108:111], v[144:147], v[176:179]\n\t" \
  "ds_read_b64_tr_b16 v[142:143], v59 offset:27136\n\t" \
  "v_mfma_f32_16x16x32_bf16 v[156:159], v[76:79], v[92:95], v[156:159]\n\t" \
  "ds_read_b64_tr_b16 v[108:109], v59 offset:30720\n\t" \
  "v_mfma_f32_16x16x32_bf16 v[156:159], v[148:151], v[84:87], v[156:159]\n\t" \
  "ds_read_b64_tr_b16 v[110:111], v59 offset:31232\n\t" \
  "v_mfma_f32_16x16x32_bf16 v[164:167], v[148:151], v[100:103], v[164:167]\n\t" \
  "ds_read_b64_tr_b16 v[92:93], v58 offset:34816\n\t" \
  "v_mfma_f32_16x16x32_bf16 v[164:167], v[76:79], v[112:115], v[164:167]\n\t" \
  "ds_read_b64_tr_b16 v[94:95], v58 offset:35328\n\t" \
  "v_mfma_f32_16x16x32_bf16 v[172:175], v[76:79], v[128:131], v[172:175]\n\t" \
  "ds_read_b64_tr_b16 v[84:85], v58 offset:38912\n\t" \
  "v_mfma_f32_16x16x32_bf16 v[172:175], v[148:151], v[120:123], v[172:175]\n\t" \
  "ds_read_b64_tr_b16 v[86:87], v58 offset:39424\n\t" \
  "v_mfma_f32_16x16x32_bf16 v[180:183], v[148:151], v[136:139], v[180:183]\n\t" \
  "ds_read_b128 v[100:103], v56 offset:0\n\t" \
  "v_mfma_f32_16x16x32_bf16 v[180:183], v[76:79], v[144:147], v[180:183]\n\t" \
  "ds_read_b128 v[112:115], v57 offset:0\n\t" \
  "s_waitcnt lgkmcnt(7)\n\t" \
  "v_mfma_f32_16x16x32_bf16 v[184:187], v[64:67], v[68:71], v[184:187]\n\t" \
  "ds_read_b64_tr_b16 v[128:129], v59 offset:34816\n\t" \
  "v_mfma_f32_16x16x32_bf16 v[184:187], v[72:75], v[104:107], v[184:187]\n\t" \
  "ds_read_b64_tr_b16 v[130:131], v59 offset:35328\n\t" \
  "v_mfma_f32_16x16x32_bf16 v[192:195], v[72:75], v[80:83], v[192:195]\n\t" \
  "ds_read_b64_tr_b16 v[120:121], v59 offset:38912\n\t" \
  "v_mfma_f32_16x16x32_bf16 v[192:195], v[64:67], v[116:119], v[192:195]\n\t" \
  "ds_read_b64_tr_b16 v[122:123], v59 offset:39424\n\t" \
  "v_mfma_f32_16x16x32_bf16 v[200:203], v[64:67], v[88:91], v[200:203]\n\t" \
  "ds_read_b64_tr_b16 v[136:137], v58 offset:18432\n\t" \
  "v_mfma_f32_16x16x32_bf16 v[200:203], v[72:75], v[124:127], v[200:203]\n\t" \
  "ds_read_b64_tr_b16 v[138:139], v58 offset:18944\n\t" \
  "v_mfma_f32_16x16x32_bf16 v[208:211], v[72:75], v[96:99], v[208:211]\n\t" \
  "ds_read_b64_tr_b16 v[148:149], v58 offset:22528\n\t" \
  "v_mfma_f32_16x16x32_bf16 v[208:211], v[64:67], v[132:135], v[208:211]\n\t" \
  "ds_read_b64_tr_b16 v[150:151], v58 offset:23040\n\t" \
  "v_mfma_f32_16x16x32_bf16 v[188:191], v[140:143], v[68:71], v[188:191]\n\t" \
  "ds_read_b64_tr_b16 v[76:77], v59 offset:18432\n\t" \
  "s_waitcnt lgkmcnt(7)\n\t" \
  "v_mfma_f32_16x16x32_bf16 v[188:191], v[108:111], v[104:107], v[188:191]\n\t" \
  "ds_read_b64_tr_b16 v[78:79], v59 offset:18944\n\t" \
  "v_mfma_f32_16x16x32_bf16 v[196:199], v[108:111], v[80:83], v[196:199]\n\t" \
  "ds_read_b64_tr_b16 v[144:145], v59 offset:22528\n\t" \
  "v_mfma_f32_16x16x32_bf16 v[196:199], v[140:143], v[116:119], v[196:199]\n\t" \
  "ds_read_b64_tr_b16 v[146:147], v59 offset:23040\n\t" \
  "v_mfma_f32_16x16x32_bf16 v[204:207], v[140:143], v[88:91], v[204:207]\n\t" \
  "v_mfma_f32_16x16x32_bf16 v[204:207], v[108:111], v[124:127], v[204:207]\n\t" \
  "v_mfma_f32_16x16x32_bf16 v[212:215], v[108:111], v[96:99], v[212:215]\n\t" \
  "v_mfma_f32_16x16x32_bf16 v[212:215], v[140:143], v[132:135], v[212:215]\n\t" \
  "v_mfma_f32_16x16x32_bf16 v[216:219], v[68:71], v[92:95], v[216:219]\n\t" \
  "v_mfma_f32_16x16x32_bf16 v[216:219], v[104:107], v[84:87], v[216:219]\n\t" \
  "v_mfma_f32_16x16x32_bf16 v[224:227], v[80:83], v[84:87], v[224:227]\n\t" \
  "v_mfma_f32_16x16x32_bf16 v[224:227], v[116:119], v[92:95], v[224:227]\n\t" \
  "v_mfma_f32_16x16x32_bf16 v[232:235], v[88:91], v[92:95], v[232:235]\n\t" \
  "v_mfma_f32_16x16x32_bf16 v[232:235], v[124:127], v[84:87], v[232:235]\n\t" \
  "v_mfma_f32_16x16x32_bf16 v[240:243], v[96:99], v[84:87], v[240:243]\n\t" \
  "v_mfma_f32_16x16x32_bf16 v[240:243], v[132:135], v[92:95], v[240:243]\n\t" \
  "v_mfma_f32_16x16x32_bf16 v[248:251], v[100:103], v[92:95], v[248:251]\n\t" \
  "v_mfma_f32_16x16x32_bf16 v[248:251], v[112:115], v[84:87], v[248:251]\n\t" \
  "v_mfma_f32_16x16x32_bf16 v[252:255], v[100:103], v[128:131], v[252:255]\n\t" \
  "s_waitcnt lgkmcnt(0)\n\t" \
  "v_mfma_f32_16x16x32_bf16 v[252:255], v[112:115], v[120:123], v[252:255]\n\t" \
  "v_mfma_f32_16x16x32_bf16 v[220:223], v[104:107], v[120:123], v[220:223]\n\t" \
  "v_mfma_f32_16x16x32_bf16 v[220:223], v[68:71], v[128:131], v[220:223]\n\t" \
  "s_barrier\n\t" \
  "v_mfma_f32_16x16x32_bf16 v[228:231], v[116:119], v[128:131], v[228:231]\n\t" \
  "ds_read_b64_tr_b16 v[72:73], v62 offset:26624\n\t" \
  "v_mfma_f32_16x16x32_bf16 v[228:231], v[80:83], v[120:123], v[228:231]\n\t" \
  "ds_read_b64_tr_b16 v[74:75], v62 offset:27136\n\t" \
  "v_mfma_f32_16x16x32_bf16 v[236:239], v[124:127], v[120:123], v[236:239]\n\t" \
  "ds_read_b128 v[64:67], v60 offset:10240\n\t" \
  "v_mfma_f32_16x16x32_bf16 v[236:239], v[88:91], v[128:131], v[236:239]\n\t" \
  "ds_read_b64_tr_b16 v[108:109], v62 offset:30720\n\t" \
  "v_mfma_f32_16x16x32_bf16 v[244:247], v[132:135], v[128:131], v[244:247]\n\t" \
  "ds_read_b64_tr_b16 v[110:111], v62 offset:31232\n\t" \
  "v_mfma_f32_16x16x32_bf16 v[244:247], v[96:99], v[120:123], v[244:247]\n\t" \
  "ds_read_b128 v[140:143], v61 offset:10240\n\t" \
  "v_mfma_f32_16x16x32_bf16 v[152:155], v[136:139], v[68:71], v[152:155]\n\t" \
  "ds_read_b128 v[92:95], v61 offset:12288\n\t" \
  "v_mfma_f32_16x16x32_bf16 v[152:155], v[148:151], v[104:107], v[152:155]\n\t" \
  "ds_read_b128 v[84:87], v60 offset:12288\n\t" \
  "v_mfma_f32_16x16x32_bf16 v[160:163], v[148:151], v[80:83], v[160:163]\n\t" \
  "ds_read_b128 v[100:103], v60 offset:14336\n\t" \
  "v_mfma_f32_16x16x32_bf16 v[160:163], v[136:139], v[116:119], v[160:163]\n\t" \
  "ds_read_b128 v[112:115], v61 offset:14336\n\t" \
  "v_mfma_f32_16x16x32_bf16 v[168:171], v[136:139], v[88:91], v[168:171]\n\t" \
  "ds_read_b128 v[128:131], v61 offset:16384\n\t" \
  "v_mfma_f32_16x16x32_bf16 v[168:171], v[148:151], v[124:127], v[168:171]\n\t" \
  "ds_read_b128 v[120:123], v60 offset:16384\n\t" \
  "v_mfma_f32_16x16x32_bf16 v[176:179], v[148:151], v[96:99], v[176:179]\n\t" \
  "ds_read_b64_tr_b16 v[148:149], v63 offset:26624\n\t" \
  "v_mfma_f32_16x16x32_bf16 v[176:179], v[136:139], v[132:135], v[176:179]\n\t" \
  "ds_read_b64_tr_b16 v[150:151], v63 offset:27136\n\t" \
  "v_mfma_f32_16x16x32_bf16 v[156:159], v[76:79], v[68:71], v[156:159]\n\t" \
  "ds_read_b64_tr_b16 v[136:137], v63 offset:30720\n\t" \
  "v_mfma_f32_16x16x32_bf16 v[156:159], v[144:147], v[104:107], v[156:159]\n\t" \
  "ds_read_b64_tr_b16 v[138:139], v63 offset:31232\n\t" \
  "v_mfma_f32_16x16x32_bf16 v[164:167], v[144:147], v[80:83], v[164:167]\n\t" \
  "ds_read_b64_tr_b16 v[68:69], v62 offset:34816\n\t" \
  "v_mfma_f32_16x16x32_bf16 v[164:167], v[76:79], v[116:119], v[164:167]\n\t" \
  "ds_read_b64_tr_b16 v[70:71], v62 offset:35328\n\t" \
  "v_mfma_f32_16x16x32_bf16 v[172:175], v[76:79], v[88:91], v[172:175]\n\t" \
  "ds_read_b64_tr_b16 v[104:105], v62 offset:38912\n\t" \
  "v_mfma_f32_16x16x32_bf16 v[172:175], v[144:147], v[124:127], v[172:175]\n\t" \
  "ds_read_b64_tr_b16 v[106:107], v62 offset:39424\n\t" \
  "v_mfma_f32_16x16x32_bf16 v[180:183], v[144:147], v[96:99], v[180:183]\n\t" \
  "ds_read_b128 v[80:83], v60 offset:0\n\t" \
  "v_mfma_f32_16x16x32_bf16 v[180:183], v[76:79], v[132:135], v[180:183]\n\t" \
  "ds_read_b128 v[116:119], v61 offset:0\n\t" \
  "s_waitcnt lgkmcnt(7)\n\t" \
  "v_mfma_f32_16x16x32_bf16 v[184:187], v[72:75], v[64:67], v[184:187]\n\t" \
  "ds_read_b64_tr_b16 v[88:89], v63 offset:34816\n\t" \
  "v_mfma_f32_16x16x32_bf16 v[184:187], v[108:111], v[140:143], v[184:187]\n\t" \
  "ds_read_b64_tr_b16 v[90:91], v63 offset:35328\n\t" \
  "v_mfma_f32_16x16x32_bf16 v[192:195], v[108:111], v[92:95], v[192:195]\n\t" \
  "ds_read_b64_tr_b16 v[124:125], v63 offset:38912\n\t" \
  "v_mfma_f32_16x16x32_bf16 v[192:195], v[72:75], v[84:87], v[192:195]\n\t" \
  "ds_read_b64_tr_b16 v[126:127], v63 offset:39424\n\t" \
  "v_mfma_f32_16x16x32_bf16 v[200:203], v[72:75], v[100:103], v[200:203]\n\t" \
  "ds_read_b64_tr_b16 v[96:97], v62 offset:18432\n\t" \
  "v_mfma_f32_16x16x32_bf16 v[200:203], v[108:111], v[112:115], v[200:203]\n\t" \
  "ds_read_b64_tr_b16 v[98:99], v62 offset:18944\n\t" \
  "v_mfma_f32_16x16x32_bf16 v[208:211], v[108:111], v[128:131], v[208:211]\n\t" \
  "ds_read_b64_tr_b16 v[144:145], v62 offset:22528\n\t" \
  "v_mfma_f32_16x16x32_bf16 v[208:211], v[72:75], v[120:123], v[208:211]\n\t" \
  "ds_read_b64_tr_b16 v[146:147], v62 offset:23040\n\t" \
  "v_mfma_f32_16x16x32_bf16 v[188:191], v[148:151], v[64:67], v[188:191]\n\t" \
  "ds_read_b64_tr_b16 v[76:77], v63 offset:18432\n\t" \
  "s_waitcnt lgkmcnt(7)\n\t" \
  "v_mfma_f32_16x16x32_bf16 v[188:191], v[136:139], v[140:143], v[188:191]\n\t" \
  "ds_read_b64_tr_b16 v[78:79], v63 offset:18944\n\t" \
  "v_mfma_f32_16x16x32_bf16 v[196:199], v[136:139], v[92:95], v[196:199]\n\t" \
  "ds_read_b64_tr_b16 v[132:133], v63 offset:22528\n\t" \
  "v_mfma_f32_16x16x32_bf16 v[196:199], v[148:151], v[84:87], v[196:199]\n\t" \
  "ds_read_b64_tr_b16 v[134:135], v63 offset:23040\n\t" \
  "v_mfma_f32_16x16x32_bf16 v[204:207], v[148:151], v[100:103], v[204:207]\n\t" \
  "v_mfma_f32_16x16x32_bf16 v[204:207], v[136:139], v[112:115], v[204:207]\n\t" \
  "v_mfma_f32_16x16x32_bf16 v[212:215], v[136:139], v[128:131], v[212:215]\n\t" \
  "v_mfma_f32_16x16x32_bf16 v[212:215], v[148:151], v[120:123], v[212:215]\n\t" \
  "v_mfma_f32_16x16x32_bf16 v[216:219], v[64:67], v[68:71], v[216:219]\n\t" \
  "v_mfma_f32_16x16x32_bf16 v[216:219], v[140:143], v[104:107], v[216:219]\n\t" \
  "v_mfma_f32_16x16x32_bf16 v[224:227], v[92:95], v[104:107], v[224:227]\n\t" \
  "v_mfma_f32_16x16x32_bf16 v[224:227], v[84:87], v[68:71], v[224:227]\n\t" \
  "v_mfma_f32_16x16x32_bf16 v[232:235], v[100:103], v[68:71], v[232:235]\n\t" \
  "v_mfma_f32_16x16x32_bf16 v[232:235], v[112:115], v[104:107], v[232:235]\n\t" \
  "v_mfma_f32_16x16x32_bf16 v[240:243], v[128:131], v[104:107], v[240:243]\n\t" \
  "v_mfma_f32_16x16x32_bf16 v[240:243], v[120:123], v[68:71], v[240:243]\n\t" \
  "v_mfma_f32_16x16x32_bf16 v[248:251], v[80:83], v[68:71], v[248:251]\n\t" \
  "v_mfma_f32_16x16x32_bf16 v[248:251], v[116:119], v[104:107], v[248:251]\n\t" \
  "v_mfma_f32_16x16x32_bf16 v[252:255], v[80:83], v[88:91], v[252:255]\n\t" \
  "s_waitcnt lgkmcnt(0)\n\t" \
  "v_mfma_f32_16x16x32_bf16 v[252:255], v[116:119], v[124:127], v[252:255]\n\t" \
  "v_mfma_f32_16x16x32_bf16 v[220:223], v[140:143], v[124:127], v[220:223]\n\t" \
  "v_mfma_f32_16x16x32_bf16 v[220:223], v[64:67], v[88:91], v[220:223]\n\t" \
  "s_barrier\n\t" \
  "v_mfma_f32_16x16x32_bf16 v[228:231], v[84:87], v[88:91], v[228:231]\n\t" \
  "ds_read_b64_tr_b16 v[108:109], v54 offset:26624\n\t" \
  "v_mfma_f32_16x16x32_bf16 v[228:231], v[92:95], v[124:127], v[228:231]\n\t" \
  "ds_read_b64_tr_b16 v[110:111], v54 offset:27136\n\t" \
  "v_mfma_f32_16x16x32_bf16 v[236:239], v[112:115], v[124:127], v[236:239]\n\t" \
  "ds_read_b128 v[72:75], v52 offset:10240\n\t" \
  "v_mfma_f32_16x16x32_bf16 v[236:239], v[100:103], v[88:91], v[236:239]\n\t" \
  "ds_read_b64_tr_b16 v[136:137], v54 offset:30720\n\t" \
  "v_mfma_f32_16x16x32_bf16 v[244:247], v[120:123], v[88:91], v[244:247]\n\t" \
  "ds_read_b64_tr_b16 v[138:139], v54 offset:31232\n\t" \
  "v_mfma_f32_16x16x32_bf16 v[244:247], v[128:131], v[124:127], v[244:247]\n\t" \
  "ds_read_b128 v[148:151], v53 offset:10240\n\t" \
  "v_mfma_f32_16x16x32_bf16 v[152:155], v[96:99], v[64:67], v[152:155]\n\t" \
  "ds_read_b128 v[68:71], v53 offset:12288\n\t" \
  "v_mfma_f32_16x16x32_bf16 v[152:155], v[144:147], v[140:143], v[152:155]\n\t" \
  "ds_read_b128 v[104:107], v52 offset:12288\n\t" \
  "v_mfma_f32_16x16x32_bf16 v[160:163], v[144:147], v[92:95], v[160:163]\n\t" \
  "ds_read_b128 v[80:83], v52 offset:14336\n\t" \
  "v_mfma_f32_16x16x32_bf16 v[160:163], v[96:99], v[84:87], v[160:163]\n\t" \
  "ds_read_b128 v[116:119], v53 offset:14336\n\t" \
  "v_mfma_f32_16x16x32_bf16 v[168:171], v[96:99], v[100:103], v[168:171]\n\t" \
  "ds_read_b128 v[88:91], v53 offset:16384\n\t" \
  "v_mfma_f32_16x16x32_bf16 v[168:171], v[144:147], v[112:115], v[168:171]\n\t" \
  "ds_read_b128 v[124:127], v52 offset:16384\n\t" \
  "v_mfma_f32_16x16x32_bf16 v[176:179], v[144:147], v[128:131], v[176:179]\n\t" \
  "ds_read_b64_tr_b16 v[144:145], v55 offset:26624\n\t" \
  "v_mfma_f32_16x16x32_bf16 v[176:179], v[96:99], v[120:123], v[176:179]\n\t" \
  "ds_read_b64_tr_b16 v[146:147], v55 offset:27136\n\t" \
  "v_mfma_f32_16x16x32_bf16 v[156:159], v[76:79], v[64:67], v[156:159]\n\t" \
  "ds_read_b64_tr_b16 v[96:97], v55 offset:30720\n\t" \
  "v_mfma_f32_16x16x32_bf16 v[156:159], v[132:135], v[140:143], v[156:159]\n\t" \
  "ds_read_b64_tr_b16 v[98:99], v55 offset:31232\n\t" \
  "v_mfma_f32_16x16x32_bf16 v[164:167], v[132:135], v[92:95], v[164:167]\n\t" \
  "ds_read_b64_tr_b16 v[64:65], v54 offset:34816\n\t" \
  "v_mfma_f32_16x16x32_bf16 v[164:167], v[76:79], v[84:87], v[164:167]\n\t" \
  "ds_read_b64_tr_b16 v[66:67], v54 offset:35328\n\t" \
  "v_mfma_f32_16x16x32_bf16 v[172:175], v[76:79], v[100:103], v[172:175]\n\t" \
  "ds_read_b64_tr_b16 v[140:141], v54 offset:38912\n\t" \
  "v_mfma_f32_16x16x32_bf16 v[172:175], v[132:135], v[112:115], v[172:175]\n\t" \
  "ds_read_b64_tr_b16 v[142:143], v54 offset:39424\n\t" \
  "v_mfma_f32_16x16x32_bf16 v[180:183], v[132:135], v[128:131], v[180:183]\n\t" \
  "ds_read_b128 v[92:95], v52 offset:0\n\t" \
  "v_mfma_f32_16x16x32_bf16 v[180:183], v[76:79], v[120:123], v[180:183]\n\t" \
  "ds_read_b128 v[84:87], v53 offset:0\n\t" \
  "s_waitcnt lgkmcnt(7)\n\t" \
  "v_mfma_f32_16x16x32_bf16 v[184:187], v[108:111], v[72:75], v[184:187]\n\t" \
  "ds_read_b64_tr_b16 v[100:101], v55 offset:34816\n\t" \
  "v_mfma_f32_16x16x32_bf16 v[184:187], v[136:139], v[148:151], v[184:187]\n\t" \
  "ds_read_b64_tr_b16 v[102:103], v55 offset:35328\n\t" \
  "v_mfma_f32_16x16x32_bf16 v[192:195], v[136:139], v[68:71], v[192:195]\n\t" \
  "ds_read_b64_tr_b16 v[112:113], v55 offset:38912\n\t" \
  "v_mfma_f32_16x16x32_bf16 v[192:195], v[108:111], v[104:107], v[192:195]\n\t" \
  "ds_read_b64_tr_b16 v[114:115], v55 offset:39424\n\t" \
  "v_mfma_f32_16x16x32_bf16 v[200:203], v[108:111], v[80:83], v[200:203]\n\t" \
  "ds_read_b64_tr_b16 v[128:129], v54 offset:18432\n\t" \
  "v_mfma_f32_16x16x32_bf16 v[200:203], v[136:139], v[116:119], v[200:203]\n\t" \
  "ds_read_b64_tr_b16 v[130:131], v54 offset:18944\n\t" \
  "v_mfma_f32_16x16x32_bf16 v[208:211], v[136:139], v[88:91], v[208:211]\n\t" \
  "ds_read_b64_tr_b16 v[132:133], v54 offset:22528\n\t" \
  "v_mfma_f32_16x16x32_bf16 v[208:211], v[108:111], v[124:127], v[208:211]\n\t" \
  "ds_read_b64_tr_b16 v[134:135], v54 offset:23040\n\t" \
  "v_mfma_f32_16x16x32_bf16 v[188:191], v[144:147], v[72:75], v[188:191]\n\t" \
  "ds_read_b64_tr_b16 v[76:77], v55 offset:18432\n\t" \
  "s_waitcnt lgkmcnt(7)\n\t" \
  "v_mfma_f32_16x16x32_bf16 v[188:191], v[96:99], v[148:151], v[188:191]\n\t" \
  "ds_read_b64_tr_b16 v[78:79], v55 offset:18944\n\t" \
  "v_mfma_f32_16x16x32_bf16 v[196:199], v[96:99], v[68:71], v[196:199]\n\t" \
  "ds_read_b64_tr_b16 v[120:121], v55 offset:22528\n\t" \
  "v_mfma_f32_16x16x32_bf16 v[196:199], v[144:147], v[104:107], v[196:199]\n\t" \
  "ds_read_b64_tr_b16 v[122:123], v55 offset:23040\n\t" \
  "v_mfma_f32_16x16x32_bf16 v[204:207], v[144:147], v[80:83], v[204:207]\n\t" \
  "v_mfma_f32_16x16x32_bf16 v[204:207], v[96:99], v[116:119], v[204:207]\n\t" \
  "v_mfma_f32_16x16x32_bf16 v[212:215], v[96:99], v[88:91], v[212:215]\n\t" \
  "v_mfma_f32_16x16x32_bf16 v[212:215], v[144:147], v[124:127], v[212:215]\n\t" \
  "v_mfma_f32_16x16x32_bf16 v[216:219], v[72:75], v[64:67], v[216:219]\n\t" \
  "v_mfma_f32_16x16x32_bf16 v[216:219], v[148:151], v[140:143], v[216:219]\n\t" \
  "v_mfma_f32_16x16x32_bf16 v[224:227], v[68:71], v[140:143], v[224:227]\n\t" \
  "v_mfma_f32_16x16x32_bf16 v[224:227], v[104:107], v[64:67], v[224:227]\n\t" \
  "v_mfma_f32_16x16x32_bf16 v[232:235], v[80:83], v[64:67], v[232:235]\n\t" \
  "v_mfma_f32_16x16x32_bf16 v[232:235], v[116:119], v[140:143], v[232:235]\n\t" \
  "v_mfma_f32_16x16x32_bf16 v[240:243], v[88:91], v[140:143], v[240:243]\n\t" \
  "v_mfma_f32_16x16x32_bf16 v[240:243], v[124:127], v[64:67], v[240:243]\n\t" \
  "v_mfma_f32_16x16x32_bf16 v[248:251], v[92:95], v[64:67], v[248:251]\n\t" \
  "v_mfma_f32_16x16x32_bf16 v[248:251], v[84:87], v[140:143], v[248:251]\n\t" \
  "v_mfma_f32_16x16x32_bf16 v[252:255], v[92:95], v[100:103], v[252:255]\n\t" \
  "s_waitcnt lgkmcnt(0)\n\t" \
  "v_mfma_f32_16x16x32_bf16 v[252:255], v[84:87], v[112:115], v[252:255]\n\t" \
  "v_mfma_f32_16x16x32_bf16 v[220:223], v[148:151], v[112:115], v[220:223]\n\t" \
  "v_mfma_f32_16x16x32_bf16 v[220:223], v[72:75], v[100:103], v[220:223]\n\t" \
  "s_barrier\n\t" \
  "v_mfma_f32_16x16x32_bf16 v[228:231], v[104:107], v[100:103], v[228:231]\n\t" \
  "ds_read_b64_tr_b16 v[96:97], v58 offset:26624\n\t" \
  "v_mfma_f32_16x16x32_bf16 v[228:231], v[68:71], v[112:115], v[228:231]\n\t" \
  "ds_read_b64_tr_b16 v[98:99], v58 offset:27136\n\t" \
  "v_mfma_f32_16x16x32_bf16 v[236:239], v[116:119], v[112:115], v[236:239]\n\t" \
  "ds_read_b128 v[136:139], v56 offset:10240\n\t" \
  "v_mfma_f32_16x16x32_bf16 v[236:239], v[80:83], v[100:103], v[236:239]\n\t" \
  "ds_read_b64_tr_b16 v[64:65], v58 offset:30720\n\t" \
  "v_mfma_f32_16x16x32_bf16 v[244:247], v[124:127], v[100:103], v[244:247]\n\t" \
  "ds_read_b64_tr_b16 v[66:67], v58 offset:31232\n\t" \
  "v_mfma_f32_16x16x32_bf16 v[244:247], v[88:91], v[112:115], v[244:247]\n\t" \
  "ds_read_b128 v[108:111], v57 offset:10240\n\t" \
  "v_mfma_f32_16x16x32_bf16 v[152:155], v[128:131], v[72:75], v[152:155]\n\t" \
  "ds_read_b128 v[144:147], v57 offset:12288\n\t" \
  "v_mfma_f32_16x16x32_bf16 v[152:155], v[132:135], v[148:151], v[152:155]\n\t" \
  "ds_read_b128 v[140:143], v56 offset:12288\n\t" \
  "v_mfma_f32_16x16x32_bf16 v[160:163], v[132:135], v[68:71], v[160:163]\n\t" \
  "ds_read_b128 v[112:115], v56 offset:14336\n\t" \
  "v_mfma_f32_16x16x32_bf16 v[160:163], v[128:131], v[104:107], v[160:163]\n\t" \
  "ds_read_b64_tr_b16 v[92:93], v59 offset:26624\n\t" \
  "v_mfma_f32_16x16x32_bf16 v[168:171], v[128:131], v[80:83], v[168:171]\n\t" \
  "ds_read_b64_tr_b16 v[94:95], v59 offset:27136\n\t" \
  "v_mfma_f32_16x16x32_bf16 v[168:171], v[132:135], v[116:119], v[168:171]\n\t" \
  "ds_read_b64_tr_b16 v[84:85], v59 offset:30720\n\t" \
  "v_mfma_f32_16x16x32_bf16 v[176:179], v[132:135], v[88:91], v[176:179]\n\t" \
  "ds_read_b128 v[132:135], v57 offset:14336\n\t" \
  "v_mfma_f32_16x16x32_bf16 v[176:179], v[128:131], v[124:127], v[176:179]\n\t" \
  "ds_read_b128 v[128:131], v57 offset:16384\n\t" \
  "v_mfma_f32_16x16x32_bf16 v[156:159], v[76:79], v[72:75], v[156:159]\n\t" \
  "ds_read_b64_tr_b16 v[86:87], v59 offset:31232\n\t" \
  "v_mfma_f32_16x16x32_bf16 v[156:159], v[120:123], v[148:151], v[156:159]\n\t" \
  "ds_read_b128 v[148:151], v56 offset:16384\n\t" \
  "v_mfma_f32_16x16x32_bf16 v[164:167], v[120:123], v[68:71], v[164:167]\n\t" \
  "ds_read_b64_tr_b16 v[100:101], v58 offset:34816\n\t" \
  "v_mfma_f32_16x16x32_bf16 v[164:167], v[76:79], v[104:107], v[164:167]\n\t" \
  "ds_read_b64_tr_b16 v[102:103], v58 offset:35328\n\t" \
  "v_mfma_f32_16x16x32_bf16 v[172:175], v[76:79], v[80:83], v[172:175]\n\t" \
  "ds_read_b64_tr_b16 v[72:73], v58 offset:38912\n\t" \
  "v_mfma_f32_16x16x32_bf16 v[172:175], v[120:123], v[116:119], v[172:175]\n\t" \
  "ds_read_b64_tr_b16 v[74:75], v58 offset:39424\n\t" \
  "v_mfma_f32_16x16x32_bf16 v[180:183], v[120:123], v[88:91], v[180:183]\n\t" \
  "ds_read_b128 v[68:71], v56 offset:0\n\t" \
  "v_mfma_f32_16x16x32_bf16 v[180:183], v[76:79], v[124:127], v[180:183]\n\t" \
  "ds_read_b128 v[104:107], v57 offset:0\n\t" \
  "s_waitcnt lgkmcnt(7)\n\t" \
  "v_mfma_f32_16x16x32_bf16 v[184:187], v[96:99], v[136:139], v[184:187]\n\t" \
  "ds_read_b64_tr_b16 v[80:81], v59 offset:34816\n\t" \
  "v_mfma_f32_16x16x32_bf16 v[184:187], v[64:67], v[108:111], v[184:187]\n\t" \
  "ds_read_b64_tr_b16 v[82:83], v59 offset:35328\n\t" \
  "v_mfma_f32_16x16x32_bf16 v[192:195], v[64:67], v[144:147], v[192:195]\n\t" \
  "ds_read_b64_tr_b16 v[88:89], v59 offset:38912\n\t" \
  "v_mfma_f32_16x16x32_bf16 v[192:195], v[96:99], v[140:143], v[192:195]\n\t" \
  "ds_read_b64_tr_b16 v[90:91], v59 offset:39424\n\t" \
  "v_mfma_f32_16x16x32_bf16 v[200:203], v[96:99], v[112:115], v[200:203]\n\t" \
  "ds_read_b64_tr_b16 v[116:117], v58 offset:18432\n\t" \
  "v_mfma_f32_16x16x32_bf16 v[200:203], v[64:67], v[132:135], v[200:203]\n\t" \
  "ds_read_b64_tr_b16 v[118:119], v58 offset:18944\n\t" \
  "v_mfma_f32_16x16x32_bf16 v[208:211], v[64:67], v[128:131], v[208:211]\n\t" \
  "ds_read_b64_tr_b16 v[120:121], v58 offset:22528\n\t" \
  "s_waitcnt lgkmcnt(7)\n\t" \
  "v_mfma_f32_16x16x32_bf16 v[208:211], v[96:99], v[148:151], v[208:211]\n\t" \
  "ds_read_b64_tr_b16 v[122:123], v58 offset:23040\n\t" \
  "v_mfma_f32_16x16x32_bf16 v[188:191], v[92:95], v[136:139], v[188:191]\n\t" \
  "ds_read_b64_tr_b16 v[124:125], v59 offset:18432\n\t" \
  "v_mfma_f32_16x16x32_bf16 v[188:191], v[84:87], v[108:111], v[188:191]\n\t" \
  "ds_read_b64_tr_b16 v[126:127], v59 offset:18944\n\t" \
  "v_mfma_f32_16x16x32_bf16 v[196:199], v[84:87], v[144:147], v[196:199]\n\t" \
  "ds_read_b64_tr_b16 v[76:77], v59 offset:22528\n\t" \
  "v_mfma_f32_16x16x32_bf16 v[196:199], v[92:95], v[140:143], v[196:199]\n\t" \
  "ds_read_b64_tr_b16 v[78:79], v59 offset:23040\n\t" \
  "v_mfma_f32_16x16x32_bf16 v[204:207], v[92:95], v[112:115], v[204:207]\n\t" \
  "v_mfma_f32_16x16x32_bf16 v[204:207], v[84:87], v[132:135], v[204:207]\n\t" \
  "v_mfma_f32_16x16x32_bf16 v[212:215], v[84:87], v[128:131], v[212:215]\n\t" \
  "v_mfma_f32_16x16x32_bf16 v[212:215], v[92:95], v[148:151], v[212:215]\n\t" \
  "v_mfma_f32_16x16x32_bf16 v[216:219], v[136:139], v[100:103], v[216:219]\n\t" \
  "v_mfma_f32_16x16x32_bf16 v[216:219], v[108:111], v[72:75], v[216:219]\n\t" \
  "v_mfma_f32_16x16x32_bf16 v[224:227], v[144:147], v[72:75], v[224:227]\n\t" \
  "v_and_b32 v14, -16, v32\n\t" \
  "v_mfma_f32_16x16x32_bf16 v[224:227], v[140:143], v[100:103], v[224:227]\n\t" \
  "v_mfma_f32_16x16x32_bf16 v[232:235], v[112:115], v[100:103], v[232:235]\n\t" \
  "ds_read_b128 v[10:13], v14 offset:128\n\t" \
  "v_mfma_f32_16x16x32_bf16 v[232:235], v[132:135], v[72:75], v[232:235]\n\t" \
  "v_mfma_f32_16x16x32_bf16 v[240:243], v[128:131], v[72:75], v[240:243]\n\t" \
  "v_mfma_f32_16x16x32_bf16 v[240:243], v[148:151], v[100:103], v[240:243]\n\t" \
  "v_mfma_f32_16x16x32_bf16 v[248:251], v[68:71], v[100:103], v[248:251]\n\t" \
  "v_mfma_f32_16x16x32_bf16 v[248:251], v[104:107], v[72:75], v[248:251]\n\t" \
  "s_waitcnt lgkmcnt(1)\n\t" \
  "v_mfma_f32_16x16x32_bf16 v[252:255], v[68:71], v[80:83], v[252:255]\n\t" \
  "s_waitcnt lgkmcnt(0)\n\t" \
  "v_and_b32 v10, v10, v11\n\t" \
  "v_and_b32 v12, v12, v13\n\t" \
  "v_mfma_f32_16x16x32_bf16 v[252:255], v[104:107], v[88:91], v[252:255]\n\t" \
  "v_and_b32 v10, v10, v12\n\t" \
  "v_mfma_f32_16x16x32_bf16 v[220:223], v[108:111], v[88:91], v[220:223]\n\t" \
  "v_readfirstlane_b32 vcc_lo, v10\n\t" \
  "v_mfma_f32_16x16x32_bf16 v[220:223], v[136:139], v[80:83], v[220:223]\n\t" \
  "ds_write_b32 v32, v0 offset:144\n\t" \
  "s_cmp_eq_u32 vcc_lo, 0\n\t" \
  "s_cbranch_scc1 15f\n\t" \
  "14:\n\t" \
  "v_mfma_f32_16x16x32_bf16 v[228:231], v[140:143], v[80:83], v[228:231]\n\t" \
  "ds_read_b64_tr_b16 v[64:65], v62 offset:26624\n\t" \
  "v_mfma_f32_16x16x32_bf16 v[228:231], v[144:147], v[88:91], v[228:231]\n\t" \
  "ds_read_b64_tr_b16 v[66:67], v62 offset:27136\n\t" \
  "v_mfma_f32_16x16x32_bf16 v[236:239], v[132:135], v[88:91], v[236:239]\n\t" \
  "ds_read_b128 v[96:99], v60 offset:10240\n\t" \
  "v_mfma_f32_16x16x32_bf16 v[236:239], v[112:115], v[80:83], v[236:239]\n\t" \
  "ds_read_b128 v[84:87], v60 offset:12288\n\t" \
  "v_mfma_f32_16x16x32_bf16 v[244:247], v[148:151], v[80:83], v[244:247]\n\t" \
  "ds_read_b128 v[92:95], v60 offset:14336\n\t" \
  "v_mfma_f32_16x16x32_bf16 v[244:247], v[128:131], v[88:91], v[244:247]\n\t" \
  "ds_read_b128 v[100:103], v60 offset:16384\n\t" \
  "v_mfma_f32_16x16x32_bf16 v[152:155], v[116:119], v[136:139], v[152:155]\n\t" \
  "ds_read_b64_tr_b16 v[72:73], v63 offset:26624\n\t" \
  "v_mfma_f32_16x16x32_bf16 v[152:155], v[120:123], v[108:111], v[152:155]\n\t" \
  "ds_read_b64_tr_b16 v[74:75], v63 offset:27136\n\t" \
  "v_mfma_f32_16x16x32_bf16 v[160:163], v[120:123], v[144:147], v[160:163]\n\t" \
  "ds_read_b64_tr_b16 v[68:69], v62 offset:34816\n\t" \
  "v_mfma_f32_16x16x32_bf16 v[160:163], v[116:119], v[140:143], v[160:163]\n\t" \
  "ds_read_b64_tr_b16 v[70:71], v62 offset:35328\n\t" \
  "v_mfma_f32_16x16x32_bf16 v[168:171], v[116:119], v[112:115], v[168:171]\n\t" \
  "ds_read_b128 v[104:107], v60 offset:0\n\t" \
  "v_mfma_f32_16x16x32_bf16 v[168:171], v[120:123], v[132:135], v[168:171]\n\t" \
  "ds_read_b64_tr_b16 v[80:81], v63 offset:34816\n\t" \
  "v_mfma_f32_16x16x32_bf16 v[176:179], v[120:123], v[128:131], v[176:179]\n\t" \
  "ds_read_b64_tr_b16 v[82:83], v63 offset:35328\n\t" \
  "v_mfma_f32_16x16x32_bf16 v[176:179], v[116:119], v[148:151], v[176:179]\n\t" \
  "ds_read_b64_tr_b16 v[88:89], v62 offset:18432\n\t" \
  "v_mfma_f32_16x16x32_bf16 v[156:159], v[124:127], v[136:139], v[156:159]\n\t" \
  "ds_read_b64_tr_b16 v[90:91], v62 offset:18944\n\t" \
  "v_mfma_f32_16x16x32_bf16 v[156:159], v[76:79], v[108:111], v[156:159]\n\t" \
  "ds_read_b64_tr_b16 v[120:121], v62 offset:30720\n\t" \
  "v_mfma_f32_16x16x32_bf16 v[164:167], v[76:79], v[144:147], v[164:167]\n\t" \
  "ds_read_b64_tr_b16 v[122:123], v62 offset:31232\n\t" \
  "v_mfma_f32_16x16x32_bf16 v[164:167], v[124:127], v[140:143], v[164:167]\n\t" \
  "ds_read_b128 v[116:119], v61 offset:10240\n\t" \
  "v_mfma_f32_16x16x32_bf16 v[172:175], v[124:127], v[112:115], v[172:175]\n\t" \
  "ds_read_b128 v[136:139], v61 offset:12288\n\t" \
  "v_mfma_f32_16x16x32_bf16 v[172:175], v[76:79], v[132:135], v[172:175]\n\t" \
  "ds_read_b128 v[108:111], v61 offset:14336\n\t" \
  "v_mfma_f32_16x16x32_bf16 v[180:183], v[76:79], v[128:131], v[180:183]\n\t" \
  "ds_read_b64_tr_b16 v[76:77], v63 offset:18432\n\t" \
  "v_mfma_f32_16x16x32_bf16 v[180:183], v[124:127], v[148:151], v[180:183]\n\t" \
  "ds_read_b64_tr_b16 v[78:79], v63 offset:18944\n\t" \
  "s_waitcnt lgkmcnt(7)\n\t" \
  "v_mfma_f32_16x16x32_bf16 v[184:187], v[64:67], v[96:99], v[184:187]\n\t" \
  "ds_read_b128 v[144:147], v61 offset:16384\n\t" \
  "v_mfma_f32_16x16x32_bf16 v[192:195], v[64:67], v[84:87], v[192:195]\n\t" \
  "ds_read_b64_tr_b16 v[140:141], v63 offset:30720\n\t" \
  "v_mfma_f32_16x16x32_bf16 v[200:203], v[64:67], v[92:95], v[200:203]\n\t" \
  "ds_read_b64_tr_b16 v[142:143], v63 offset:31232\n\t" \
  "v_mfma_f32_16x16x32_bf16 v[208:211], v[64:67], v[100:103], v[208:211]\n\t" \
  "502:\n\t" \
  "s_cbranch_execz 506f\n\t" \
  "ds_read_b64_tr_b16 v[112:113], v62 offset:22528\n\t" \
  "v_mfma_f32_16x16x32_bf16 v[188:191], v[72:75], v[96:99], v[188:191]\n\t" \
  "ds_read_b64_tr_b16 v[114:115], v62 offset:23040\n\t" \
  "v_mfma_f32_16x16x32_bf16 v[196:199], v[72:75], v[84:87], v[196:199]\n\t" \
  "ds_read_b64_tr_b16 v[132:133], v63 offset:22528\n\t" \
  "v_mfma_f32_16x16x32_bf16 v[204:207], v[72:75], v[92:95], v[204:207]\n\t" \
  "ds_read_b64_tr_b16 v[134:135], v63 offset:23040\n\t" \
  "v_mfma_f32_16x16x32_bf16 v[212:215], v[72:75], v[100:103], v[212:215]\n\t" \
  "ds_read_b64_tr_b16 v[128:129], v62 offset:38912\n\t" \
  "v_mfma_f32_16x16x32_bf16 v[248:251], v[104:107], v[68:71], v[248:251]\n\t" \
  "ds_read_b64_tr_b16 v[130:131], v62 offset:39424\n\t" \
  "v_mfma_f32_16x16x32_bf16 v[216:219], v[96:99], v[68:71], v[216:219]\n\t" \
  "ds_read_b128 v[124:127], v61 offset:0\n\t" \
  "v_mfma_f32_16x16x32_bf16 v[224:227], v[84:87], v[68:71], v[224:227]\n\t" \
  "ds_read_b64_tr_b16 v[148:149], v63 offset:38912\n\t" \
  "v_mfma_f32_16x16x32_bf16 v[232:235], v[92:95], v[68:71], v[232:235]\n\t" \
  "ds_read_b64_tr_b16 v[150:151], v63 offset:39424\n\t" \
  "v_mfma_f32_16x16x32_bf16 v[240:243], v[100:103], v[68:71], v[240:243]\n\t" \
  "v_mfma_f32_16x16x32_bf16 v[252:255], v[104:107], v[80:83], v[252:255]\n\t" \
  "v_mfma_f32_16x16x32_bf16 v[220:223], v[96:99], v[80:83], v[220:223]\n\t" \
  "v_mfma_f32_16x16x32_bf16 v[228:231], v[84:87], v[80:83], v[228:231]\n\t" \
  "v_mfma_f32_16x16x32_bf16 v[236:239], v[92:95], v[80:83], v[236:239]\n\t" \
  "v_mfma_f32_16x16x32_bf16 v[244:247], v[100:103], v[80:83], v[244:247]\n\t" \
  "v_mfma_f32_16x16x32_bf16 v[152:155], v[88:91], v[96:99], v[152:155]\n\t" \
  "v_mfma_f32_16x16x32_bf16 v[160:163], v[88:91], v[84:87], v[160:163]\n\t" \
  "v_mfma_f32_16x16x32_bf16 v[168:171], v[88:91], v[92:95], v[168:171]\n\t" \
  "v_mfma_f32_16x16x32_bf16 v[176:179], v[88:91], v[100:103], v[176:179]\n\t" \
  "s_waitcnt lgkmcnt(0)\n\t" \
  "v_mfma_f32_16x16x32_bf16 v[156:159], v[76:79], v[96:99], v[156:159]\n\t" \
  "v_mfma_f32_16x16x32_bf16 v[164:167], v[76:79], v[84:87], v[164:167]\n\t" \
  "ds_write_b32 v32, v0\n\t" \
  "v_mfma_f32_16x16x32_bf16 v[172:175], v[76:79], v[92:95], v[172:175]\n\t" \
  "v_mfma_f32_16x16x32_bf16 v[180:183], v[76:79], v[100:103], v[180:183]\n\t" \
  "v_mfma_f32_16x16x32_bf16 v[184:187], v[120:123], v[116:119], v[184:187]\n\t" \
  "v_and_b32 v56, -16, v32\n\t" \
  "v_mfma_f32_16x16x32_bf16 v[192:195], v[120:123], v[136:139], v[192:195]\n\t" \
  "v_mfma_f32_16x16x32_bf16 v[200:203], v[120:123], v[108:111], v[200:203]\n\t" \
  "v_mfma_f32_16x16x32_bf16 v[208:211], v[120:123], v[144:147], v[208:211]\n\t" \
  "v_mfma_f32_16x16x32_bf16 v[188:191], v[140:143], v[116:119], v[188:191]\n\t" \
  "v_mfma_f32_16x16x32_bf16 v[196:199], v[140:143], v[136:139], v[196:199]\n\t" \
  "v_mfma_f32_16x16x32_bf16 v[204:207], v[140:143], v[108:111], v[204:207]\n\t" \
  "v_mfma_f32_16x16x32_bf16 v[212:215], v[140:143], v[144:147], v[212:215]\n\t" \
  "v_cvt_pk_bf16_f32 v14, v184, v185\n\t" \
  "v_cvt_pk_bf16_f32 v15, v186, v187\n\t" \
  "v_mfma_f32_16x16x32_bf16 v[152:155], v[112:115], v[116:119], v[152:155]\n\t" \
  "v_cvt_pk_bf16_f32 v18, v192, v193\n\t" \
  "v_cvt_pk_bf16_f32 v19, v194, v195\n\t" \
  "v_mfma_f32_16x16x32_bf16 v[160:163], v[112:115], v[136:139], v[160:163]\n\t" \
  "v_cvt_pk_bf16_f32 v16, v188, v189\n\t" \
  "v_cvt_pk_bf16_f32 v17, v190, v191\n\t" \
  "v_mfma_f32_16x16x32_bf16 v[168:171], v[112:115], v[108:111], v[168:171]\n\t" \
  "v_cvt_pk_bf16_f32 v20, v196, v197\n\t" \
  "v_cvt_pk_bf16_f32 v21, v198, v199\n\t" \
  "v_mfma_f32_16x16x32_bf16 v[176:179], v[112:115], v[144:147], v[176:179]\n\t" \
  "v_cvt_pk_bf16_f32 v22, v200, v201\n\t" \
  "v_cvt_pk_bf16_f32 v23, v202, v203\n\t" \
  "v_mfma_f32_16x16x32_bf16 v[156:159], v[132:135], v[116:119], v[156:159]\n\t" \
  "v_cvt_pk_bf16_f32 v24, v204, v205\n\t" \
  "v_cvt_pk_bf16_f32 v25, v206, v207\n\t" \
  "v_mfma_f32_16x16x32_bf16 v[164:167], v[132:135], v[136:139], v[164:167]\n\t" \
  "v_cvt_pk_bf16_f32 v26, v208, v209\n\t" \
  "v_cvt_pk_bf16_f32 v27, v210, v211\n\t" \
  "v_mfma_f32_16x16x32_bf16 v[172:175], v[132:135], v[108:111], v[172:175]\n\t" \
  "v_cvt_pk_bf16_f32 v28, v212, v213\n\t" \
  "v_cvt_pk_bf16_f32 v29, v214, v215\n\t" \
  "v_mfma_f32_16x16x32_bf16 v[180:183], v[132:135], v[144:147], v[180:183]\n\t" \
  "v_cvt_pk_bf16_f32 v84, v152, v153\n\t" \
  "v_cvt_pk_bf16_f32 v85, v154, v155\n\t" \
  "v_mfma_f32_16x16x32_bf16 v[248:251], v[124:127], v[128:131], v[248:251]\n\t" \
  "v_cvt_pk_bf16_f32 v86, v156, v157\n\t" \
  "v_cvt_pk_bf16_f32 v87, v158, v159\n\t" \
  "v_mfma_f32_16x16x32_bf16 v[216:219], v[116:119], v[128:131], v[216:219]\n\t" \
  "v_cvt_pk_bf16_f32 v88, v160, v161\n\t" \
  "v_cvt_pk_bf16_f32 v89, v162, v163\n\t" \
  "v_mfma_f32_16x16x32_bf16 v[224:227], v[136:139], v[128:131], v[224:227]\n\t" \
  "v_cvt_pk_bf16_f32 v90, v164, v165\n\t" \
  "v_cvt_pk_bf16_f32 v91, v166, v167\n\t" \
  "v_mfma_f32_16x16x32_bf16 v[232:235], v[108:111], v[128:131], v[232:235]\n\t" \
  "v_cvt_pk_bf16_f32 v72, v248, v249\n\t" \
  "v_cvt_pk_bf16_f32 v73, v250, v251\n\t" \
  "v_mfma_f32_16x16x32_bf16 v[240:243], v[144:147], v[128:131], v[240:243]\n\t" \
  "ds_read_b32 v52, v33 offset:48\n\t" \
  "ds_read_b128 v[10:13], v31\n\t" \
  "v_cvt_pk_bf16_f32 v92, v168, v169\n\t" \
  "v_mfma_f32_16x16x32_bf16 v[252:255], v[124:127], v[148:151], v[252:255]\n\t" \
  "v_cvt_pk_bf16_f32 v93, v170, v171\n\t" \
  "v_cvt_pk_bf16_f32 v94, v172, v173\n\t" \
  "v_mfma_f32_16x16x32_bf16 v[220:223], v[116:119], v[148:151], v[220:223]\n\t" \
  "v_cvt_pk_bf16_f32 v95, v174, v175\n\t" \
  "v_cvt_pk_bf16_f32 v96, v176, v177\n\t" \
  "v_mfma_f32_16x16x32_bf16 v[228:231], v[136:139], v[148:151], v[228:231]\n\t" \
  "v_cvt_pk_bf16_f32 v97, v178, v179\n\t" \
  "v_cvt_pk_bf16_f32 v98, v180, v181\n\t" \
  "v_mfma_f32_16x16x32_bf16 v[236:239], v[108:111], v[148:151], v[236:239]\n\t" \
  "v_cvt_pk_bf16_f32 v82, v252, v253\n\t" \
  "v_cvt_pk_bf16_f32 v83, v254, v255\n\t" \
  "v_mfma_f32_16x16x32_bf16 v[244:247], v[144:147], v[148:151], v[244:247]\n\t" \
  "ds_write_b64 v30, v[72:73]\n\t" \
  "ds_write_b64 v30, v[82:83] offset:8\n\t" \
  "ds_write_b32 v32, v0 offset:48\n\t" \
  "v_mfma_f32_16x16x32_bf16 v[200:203], v[22:25], v[92:95], v[34:37]\n\t" \
  "v_cvt_pk_bf16_f32 v99, v182, v183\n\t" \
  "s_waitcnt lgkmcnt(0)\n\t" \
  "v_readfirstlane_b32 vcc_lo, v52\n\t" \
  "s_nop 1\n\t" \
  "s_cmp_eq_u32 vcc_lo, 0\n\t" \
  "s_cbranch_scc1 5f\n\t" \
  "4:\n\t" \
  "v_mfma_f32_16x16x32_bf16 v[204:207], v[18:21], v[92:95], v[38:41]\n\t" \
  "v_cvt_pk_bf16_f32 v66, v232, v233\n\t" \
  "v_cvt_pk_bf16_f32 v67, v234, v235\n\t" \
  "v_mfma_f32_16x16x32_bf16 v[208:211], v[26:29], v[96:99], v[34:37]\n\t" \
  "v_cvt_pk_bf16_f32 v76, v236, v237\n\t" \
  "v_cvt_pk_bf16_f32 v77, v238, v239\n\t" \
  "v_mfma_f32_16x16x32_bf16 v[212:215], v[22:25], v[96:99], v[38:41]\n\t" \
  "v_cvt_pk_bf16_f32 v64, v240, v241\n\t" \
  "v_cvt_pk_bf16_f32 v65, v242, v243\n\t" \
  "v_mfma_f32_16x16x32_bf16 v[192:195], v[18:21], v[88:91], v[34:37]\n\t" \
  "v_max_f32 v200, v200, v204\n\t" \
  "v_max_f32 v201, v201, v205\n\t" \
  "v_mfma_f32_16x16x32_bf16 v[196:199], v[14:17], v[88:91], v[38:41]\n\t" \
  "v_max_f32 v202, v202, v206\n\t" \
  "v_max_f32 v203, v203, v207\n\t" \
  "v_mfma_f32_16x16x32_bf16 v[184:187], v[14:17], v[84:87], v[34:37]\n\t" \
  "v_max_f32 v208, v208, v212\n\t" \
  "v_max_f32 v209, v209, v213\n\t" \
  "v_mfma_f32_16x16x32_bf16 v[188:191], v[10:13], v[84:87], v[42:45]\n\t" \
  "v_max_f32 v210, v210, v214\n\t" \
  "v_max_f32 v211, v211, v215\n\t" \
  "v_max_f32 v192, v192, v196\n\t" \
  "v_max_f32 v193, v193, v197\n\t" \
  "v_max_f32 v194, v194, v198\n\t" \
  "v_max3_f32 v106, v208, v209, v210\n\t" \
  "v_max3_f32 v104, v200, v201, v202\n\t" \
  "v_max_f32 v195, v195, v199\n\t" \
  "v_max_f32 v184, v184, v188\n\t" \
  "v_max_f32 v185, v185, v189\n\t" \
  "v_max_f32 v186, v186, v190\n\t" \
  "v_max3_f32 v102, v192, v193, v194\n\t" \
  "v_max_f32 v187, v187, v191\n\t" \
  "v_max3_f32 v100, v184, v185, v186\n\t" \
  "v_max_f32 v106, v106, v211\n\t" \
  "v_max_f32 v104, v104, v203\n\t" \
  "v_max_f32 v102, v102, v195\n\t" \
  "v_max_f32 v100, v100, v187\n\t" \
  "v_permlane16_swap_b32 v104, v106\n\t" \
  "v_cvt_pk_bf16_f32 v74, v244, v245\n\t" \
  "v_permlane16_swap_b32 v100, v102\n\t" \
  "v_max_f32 v104, v104, v106\n\t" \
  "v_max_f32 v100, v100, v102\n\t" \
  "v_cvt_pk_bf16_f32 v75, v246, v247\n\t" \
  "v_cvt_pk_bf16_f32 v68, v224, v225\n\t" \
  "v_permlane32_swap_b32 v100, v104\n\t" \
  "v_cvt_pk_bf16_f32 v69, v226, v227\n\t" \
  "v_max_f32 v100, v100, v104\n\t" \
  "v_mul_f32 v100, 0xbe8293ee, v100\n\t" \
  "v_mov_b32 v104, v100\n\t" \
  "v_cvt_pk_bf16_f32 v78, v228, v229\n\t" \
  "v_cvt_pk_bf16_f32 v79, v230, v231\n\t" \
  "v_permlane32_swap_b32 v100, v104\n\t" \
  "v_cvt_pk_bf16_f32 v70, v216, v217\n\t" \
  "v_mov_b32 v106, v104\n\t" \
  "v_mov_b32 v102, v100\n\t" \
  "v_cvt_pk_bf16_f32 v71, v218, v219\n\t" \
  "v_permlane16_swap_b32 v104, v106\n\t" \
  "v_permlane16_swap_b32 v100, v102\n\t" \
  "v_fma_f32 v208, v208, %16, v106\n\t" \
  "v_fma_f32 v209, v209, %16, v106\n\t" \
  "v_fma_f32 v210, v210, %16, v106\n\t" \
  "v_fma_f32 v211, v211, %16, v106\n\t" \
  "v_exp_f32 v208, v208\n\t" \
  "v_exp_f32 v209, v209\n\t" \
  "v_exp_f32 v210, v210\n\t" \
  "v_exp_f32 v211, v211\n\t" \
  "v_cvt_pk_bf16_f32 v212, v208, v209\n\t" \
  "v_cvt_pk_bf16_f32 v213, v210, v211\n\t" \
  "v_bfi_b32 v214, v46, 0, v212\n\t" \
  "v_bfi_b32 v215, v47, 0, v213\n\t" \
  "v_and_b32 v212, v46, v212\n\t" \
  "v_and_b32 v213, v47, v213\n\t" \
  "v_fma_f32 v200, v200, %16, v104\n\t" \
  "v_fma_f32 v201, v201, %16, v104\n\t" \
  "v_mfma_f32_16x16x32_bf16 v[208:211], v[48:51], v[212:215], 0\n\t" \
  "v_fma_f32 v202, v202, %16, v104\n\t" \
  "v_fma_f32 v203, v203, %16, v104\n\t" \
  "v_mfma_f32_16x16x32_bf16 v[176:179], v[64:67], v[212:215], 0\n\t" \
  "v_exp_f32 v200, v200\n\t" \
  "v_mfma_f32_16x16x32_bf16 v[180:183], v[74:77], v[212:215], 0\n\t" \
  "v_exp_f32 v201, v201\n\t" \
  "v_exp_f32 v202, v202\n\t" \
  "v_exp_f32 v203, v203\n\t" \
  "v_cvt_pk_bf16_f32 v204, v200, v201\n\t" \
  "v_cvt_pk_bf16_f32 v205, v202, v203\n\t" \
  "v_bfi_b32 v206, v46, 0, v204\n\t" \
  "v_bfi_b32 v207, v47, 0, v205\n\t" \
  "v_rcp_f32 v106, v208\n\t" \
  "v_and_b32 v204, v46, v204\n\t" \
  "v_and_b32 v205, v47, v205\n\t" \
  "v_pk_mul_f32 v[176:177], v[176:177], v[106:107] op_sel_hi:[1,0]\n\t" \
  "v_pk_mul_f32 v[178:179], v[178:179], v[106:107] op_sel_hi:[1,0]\n\t" \
  "v_mfma_f32_16x16x32_bf16 v[200:203], v[48:51], v[204:207], 0\n\t" \
  "v_pk_mul_f32 v[180:181], v[180:181], v[106:107] op_sel_hi:[1,0]\n\t" \
  "v_pk_mul_f32 v[182:183], v[182:183], v[106:107] op_sel_hi:[1,0]\n\t" \
  "v_mfma_f32_16x16x32_bf16 v[168:171], v[66:69], v[204:207], 0\n\t" \
  "v_cvt_pk_bf16_f32 v240, v176, v177\n\t" \
  "v_cvt_pk_bf16_f32 v241, v178, v179\n\t" \
  "v_mfma_f32_16x16x32_bf16 v[172:175], v[76:79], v[204:207], 0\n\t" \
  "v_cvt_pk_bf16_f32 v242, v180, v181\n\t" \
  "v_cvt_pk_bf16_f32 v243, v182, v183\n\t" \
  "global_store_dwordx4 v1, v[240:243], %14 offset:0 sc1\n\t" \
  "v_fma_f32 v192, v192, %16, v102\n\t" \
  "v_fma_f32 v193, v193, %16, v102\n\t" \
  "v_fma_f32 v194, v194, %16, v102\n\t" \
  "v_fma_f32 v195, v195, %16, v102\n\t" \
  "v_exp_f32 v192, v192\n\t" \
  "v_exp_f32 v193, v193\n\t" \
  "v_exp_f32 v194, v194\n\t" \
  "v_exp_f32 v195, v195\n\t" \
  "v_cvt_pk_bf16_f32 v196, v192, v193\n\t" \
  "v_cvt_pk_bf16_f32 v197, v194, v195\n\t" \
  "v_bfi_b32 v198, v46, 0, v196\n\t" \
  "v_bfi_b32 v199, v47, 0, v197\n\t" \
  "v_rcp_f32 v104, v200\n\t" \
  "v_and_b32 v196, v46, v196\n\t" \
  "v_and_b32 v197, v47, v197\n\t" \
  "v_cvt_pk_bf16_f32 v80, v220, v221\n\t" \
  "v_cvt_pk_bf16_f32 v81, v222, v223\n\t" \
  "v_mfma_f32_16x16x32_bf16 v[192:195], v[48:51], v[196:199], 0\n\t" \
  "v_pk_mul_f32 v[168:169], v[168:169], v[104:105] op_sel_hi:[1,0]\n\t" \
  "v_pk_mul_f32 v[170:171], v[170:171], v[104:105] op_sel_hi:[1,0]\n\t" \
  "v_mfma_f32_16x16x32_bf16 v[160:163], v[68:71], v[196:199], 0\n\t" \
  "v_pk_mul_f32 v[172:173], v[172:173], v[104:105] op_sel_hi:[1,0]\n\t" \
  "v_pk_mul_f32 v[174:175], v[174:175], v[104:105] op_sel_hi:[1,0]\n\t" \
  "v_mfma_f32_16x16x32_bf16 v[164:167], v[78:81], v[196:199], 0\n\t" \
  "v_cvt_pk_bf16_f32 v232, v168, v169\n\t" \
  "v_cvt_pk_bf16_f32 v233, v170, v171\n\t" \
  "v_cvt_pk_bf16_f32 v234, v172, v173\n\t" \
  "v_cvt_pk_bf16_f32 v235, v174, v175\n\t" \
  "global_store_dwordx4 v1, v[232:235], %14 offset:-4096 sc1\n\t" \
  "v_fma_f32 v184, v184, %16, v100\n\t" \
  "v_fma_f32 v185, v185, %16, v100\n\t" \
  "v_fma_f32 v186, v186, %16, v100\n\t" \
  "v_fma_f32 v187, v187, %16, v100\n\t" \
  "v_exp_f32 v184, v184\n\t" \
  "v_exp_f32 v185, v185\n\t" \
  "v_exp_f32 v186, v186\n\t" \
  "v_exp_f32 v187, v187\n\t" \
  "v_cvt_pk_bf16_f32 v188, v184, v185\n\t" \
  "v_cvt_pk_bf16_f32 v189, v186, v187\n\t" \
  "v_bfi_b32 v190, v46, 0, v188\n\t" \
  "v_bfi_b32 v191, v47, 0, v189\n\t" \
  "v_rcp_f32 v102, v192\n\t" \
  "v_and_b32 v188, v46, v188\n\t" \
  "v_and_b32 v189, v47, v189\n\t" \
  "v_pk_mul_f32 v[160:161], v[160:161], v[102:103] op_sel_hi:[1,0]\n\t" \
  "v_pk_mul_f32 v[162:163], v[162:163], v[102:103] op_sel_hi:[1,0]\n\t" \
  "v_mfma_f32_16x16x32_bf16 v[184:187], v[48:51], v[188:191], 0\n\t" \
  "v_pk_mul_f32 v[164:165], v[164:165], v[102:103] op_sel_hi:[1,0]\n\t" \
  "v_pk_mul_f32 v[166:167], v[166:167], v[102:103] op_sel_hi:[1,0]\n\t" \
  "v_mfma_f32_16x16x32_bf16 v[152:155], v[70:73], v[188:191], 0\n\t" \
  "v_cvt_pk_bf16_f32 v224, v160, v161\n\t" \
  "v_cvt_pk_bf16_f32 v225, v162, v163\n\t" \
  "v_mfma_f32_16x16x32_bf16 v[156:159], v[80:83], v[188:191], 0\n\t" \
  "v_cvt_pk_bf16_f32 v226, v164, v165\n\t" \
  "v_cvt_pk_bf16_f32 v227, v166, v167\n\t" \
  "global_store_dwordx4 v4, v[224:227], %14 offset:0 sc1\n\t" \
  "v_rcp_f32 v100, v184\n\t" \
  "ds_read_b128 v[60:63], v56 offset:64\n\t" \
  "ds_read_b64_tr_b16 v[108:109], v5 offset:0\n\t" \
  "ds_read_b64_tr_b16 v[110:111], v5 offset:16\n\t" \
  "ds_read_b128 v[112:115], v6 offset:16384\n\t" \
  "ds_read_b128 v[116:119], v6 offset:15360\n\t" \
  "ds_read_b64_tr_b16 v[120:121], v5 offset:512\n\t" \
  "ds_read_b64_tr_b16 v[122:123], v5 offset:528\n\t" \
  "ds_read_b128 v[124:127], v6 offset:14336\n\t" \
  "ds_read_b64_tr_b16 v[128:129], v5 offset:1024\n\t" \
  "ds_read_b64_tr_b16 v[130:131], v5 offset:1040\n\t" \
  "ds_read_b128 v[132:135], v6 offset:13312\n\t" \
  "ds_read_b64_tr_b16 v[136:137], v5 offset:1536\n\t" \
  "ds_read_b64_tr_b16 v[138:139], v5 offset:1552\n\t" \
  "ds_read_b128 v[140:143], v6 offset:12288\n\t" \
  "ds_read_b64_tr_b16 v[144:145], v5 offset:2048\n\t" \
  "v_pk_mul_f32 v[152:153], v[152:153], v[100:101] op_sel_hi:[1,0]\n\t" \
  "v_pk_mul_f32 v[154:155], v[154:155], v[100:101] op_sel_hi:[1,0]\n\t" \
  "v_cvt_pk_bf16_f32 v216, v152, v153\n\t" \
  "v_pk_mul_f32 v[156:157], v[156:157], v[100:101] op_sel_hi:[1,0]\n\t" \
  "v_pk_mul_f32 v[158:159], v[158:159], v[100:101] op_sel_hi:[1,0]\n\t" \
  "v_cvt_pk_bf16_f32 v217, v154, v155\n\t" \
  "v_cvt_pk_bf16_f32 v218, v156, v157\n\t" \
  "v_cvt_pk_bf16_f32 v219, v158, v159\n\t" \
  "global_store_dwordx4 v4, v[216:219], %14 offset:-4096 sc1\n\t" \
  "ds_write_b32 v32, v0 offset:112\n\t" \
  "s_branch 9f\n\t" \
  "5:\n\t" \
  "ds_read_b32 v52, v33 offset:48\n\t" \
  "ds_read_b128 v[10:13], v31\n\t" \
  "s_waitcnt lgkmcnt(0)\n\t" \
  "v_readfirstlane_b32 vcc_lo, v52\n\t" \
  "s_nop 1\n\t" \
  "s_cmp_eq_u32 vcc_lo, 0\n\t" \
  "s_cbranch_scc1 5b\n\t" \
  "s_branch 4b\n\t" \
  "15:\n\t" \
  "ds_read_b128 v[10:13], v14 offset:128\n\t" \
  "s_waitcnt lgkmcnt(0)\n\t" \
  "v_and_b32 v10, v10, v11\n\t" \
  "v_and_b32 v12, v12, v13\n\t" \
  "v_and_b32 v10, v10, v12\n\t" \
  "s_nop 1\n\t" \
  "v_readfirstlane_b32 vcc_lo, v10\n\t" \
  "s_nop 1\n\t" \
  "s_cmp_eq_u32 vcc_lo, 0\n\t" \
  "s_cbranch_scc1 15b\n\t" \
  "s_branch 14b\n\t" \
  "8:\n\t" \
  "global_load_dwordx2 v[10:11], v51, %11\n\t" \
  "global_load_dwordx2 v[12:13], v51, %11 offset:128\n\t" \
  "v_xor_b32 v53, 64, v52\n\t" \
  "v_xor_b32 v55, 32, v54\n\t" \
  "v_add_u32 v56, 43008, v52\n\t" \
  "v_add_u32 v58, 43008, v54\n\t" \
  "v_xor_b32 v57, 64, v56\n\t" \
  "v_xor_b32 v59, 32, v58\n\t" \
  "v_add_u32 v60, 86016, v52\n\t" \
  "v_add_u32 v62, 86016, v54\n\t" \
  "v_xor_b32 v61, 64, v60\n\t" \
  "v_xor_b32 v63, 32, v62\n\t" \
  "s_mov_b32 vcc_hi, 0x05040100\n\t" \
  "s_waitcnt vmcnt(2)\n\t" \
  "s_barrier\n\t" \
  "ds_read_b64_tr_b16 v[64:65], v62 offset:34816\n\t" \
  "ds_read_b64_tr_b16 v[66:67], v62 offset:35328\n\t" \
  "ds_read_b128 v[68:71], v60 offset:2048\n\t" \
  "ds_read_b64_tr_b16 v[72:73], v62 offset:38912\n\t" \
  "ds_read_b64_tr_b16 v[74:75], v62 offset:39424\n\t" \
  "ds_read_b128 v[76:79], v61 offset:2048\n\t" \
  "ds_read_b128 v[80:83], v61 offset:4096\n\t" \
  "ds_read_b128 v[84:87], v60 offset:4096\n\t" \
  "ds_read_b128 v[88:91], v60 offset:6144\n\t" \
  "ds_read_b128 v[92:95], v61 offset:6144\n\t" \
  "ds_read_b128 v[96:99], v61 offset:8192\n\t" \
  "ds_read_b128 v[100:103], v60 offset:8192\n\t" \
  "ds_read_b64_tr_b16 v[104:105], v63 offset:34816\n\t" \
  "ds_read_b64_tr_b16 v[106:107], v63 offset:35328\n\t" \
  "ds_read_b64_tr_b16 v[108:109], v63 offset:38912\n\t" \
  "ds_read_b64_tr_b16 v[110:111], v63 offset:39424\n\t" \
  "s_waitcnt lgkmcnt(13)\n\t" \
  "v_mfma_f32_16x16x32_bf16 v[216:219], v[68:71], v[64:67], 0\n\t" \
  "ds_read_b64_tr_b16 v[112:113], v62 offset:26624\n\t" \
  "ds_read_b64_tr_b16 v[114:115], v62 offset:27136\n\t" \
  "s_waitcnt lgkmcnt(12)\n\t" \
  "v_mfma_f32_16x16x32_bf16 v[216:219], v[76:79], v[72:75], v[216:219]\n\t" \
  "ds_read_b64_tr_b16 v[116:117], v62 offset:30720\n\t" \
  "ds_read_b64_tr_b16 v[118:119], v62 offset:31232\n\t" \
  "s_waitcnt lgkmcnt(13)\n\t" \
  "v_mfma_f32_16x16x32_bf16 v[224:227], v[80:83], v[72:75], 0\n\t" \
  "ds_read_b128 v[120:123], v60 offset:0\n\t" \
  "s_waitcnt lgkmcnt(13)\n\t" \
  "v_mfma_f32_16x16x32_bf16 v[224:227], v[84:87], v[64:67], v[224:227]\n\t" \
  "ds_read_b128 v[124:127], v61 offset:0\n\t" \
  "s_waitcnt lgkmcnt(13)\n\t" \
  "v_mfma_f32_16x16x32_bf16 v[232:235], v[88:91], v[64:67], 0\n\t" \
  "ds_read_b64_tr_b16 v[128:129], v63 offset:26624\n\t" \
  "s_waitcnt lgkmcnt(13)\n\t" \
  "v_mfma_f32_16x16x32_bf16 v[232:235], v[92:95], v[72:75], v[232:235]\n\t" \
  "ds_read_b64_tr_b16 v[130:131], v63 offset:27136\n\t" \
  "s_waitcnt lgkmcnt(13)\n\t" \
  "v_mfma_f32_16x16x32_bf16 v[240:243], v[96:99], v[72:75], 0\n\t" \
  "ds_read_b64_tr_b16 v[132:133], v63 offset:30720\n\t" \
  "s_waitcnt lgkmcnt(9)\n\t" \
  "v_mfma_f32_16x16x32_bf16 v[240:243], v[100:103], v[64:67], v[240:243]\n\t" \
  "ds_read_b64_tr_b16 v[134:135], v63 offset:31232\n\t" \
  "v_mfma_f32_16x16x32_bf16 v[220:223], v[68:71], v[104:107], 0\n\t" \
  "ds_read_b64_tr_b16 v[136:137], v62 offset:18432\n\t" \
  "v_mfma_f32_16x16x32_bf16 v[220:223], v[76:79], v[108:111], v[220:223]\n\t" \
  "ds_read_b64_tr_b16 v[138:139], v62 offset:18944\n\t" \
  "v_mfma_f32_16x16x32_bf16 v[228:231], v[80:83], v[108:111], 0\n\t" \
  "ds_read_b64_tr_b16 v[140:141], v62 offset:22528\n\t" \
  "v_mfma_f32_16x16x32_bf16 v[228:231], v[84:87], v[104:107], v[228:231]\n\t" \
  "ds_read_b64_tr_b16 v[142:143], v62 offset:23040\n\t" \
  "v_mfma_f32_16x16x32_bf16 v[236:239], v[88:91], v[104:107], 0\n\t" \
  "ds_read_b64_tr_b16 v[144:145], v63 offset:18432\n\t" \
  "v_mfma_f32_16x16x32_bf16 v[236:239], v[92:95], v[108:111], v[236:239]\n\t" \
  "ds_read_b64_tr_b16 v[146:147], v63 offset:18944\n\t" \
  "v_mfma_f32_16x16x32_bf16 v[244:247], v[96:99], v[108:111], 0\n\t" \
  "ds_read_b64_tr_b16 v[148:149], v63 offset:22528\n\t" \
  "v_mfma_f32_16x16x32_bf16 v[244:247], v[100:103], v[104:107], v[244:247]\n\t" \
  "ds_read_b64_tr_b16 v[150:151], v63 offset:23040\n\t" \
  "s_waitcnt lgkmcnt(7)\n\t" \
  "v_mfma_f32_16x16x32_bf16 v[184:187], v[112:115], v[68:71], 0\n\t" \
  "v_mfma_f32_16x16x32_bf16 v[184:187], v[116:119], v[76:79], v[184:187]\n\t" \
  "v_mfma_f32_16x16x32_bf16 v[192:195], v[116:119], v[80:83], 0\n\t" \
  "v_mfma_f32_16x16x32_bf16 v[192:195], v[112:115], v[84:87], v[192:195]\n\t" \
  "v_mfma_f32_16x16x32_bf16 v[200:203], v[112:115], v[88:91], 0\n\t" \
  "v_mfma_f32_16x16x32_bf16 v[200:203], v[116:119], v[92:95], v[200:203]\n\t" \
  "v_mfma_f32_16x16x32_bf16 v[208:211], v[116:119], v[96:99], 0\n\t" \
  "v_mfma_f32_16x16x32_bf16 v[208:211], v[112:115], v[100:103], v[208:211]\n\t" \
  "v_mfma_f32_16x16x32_bf16 v[248:251], v[112:115], v[120:123], 0\n\t" \
  "v_mfma_f32_16x16x32_bf16 v[248:251], v[116:119], v[124:127], v[248:251]\n\t" \
  "v_mfma_f32_16x16x32_bf16 v[252:255], v[128:131], v[120:123], 0\n\t" \
  "v_mfma_f32_16x16x32_bf16 v[252:255], v[132:135], v[124:127], v[252:255]\n\t" \
  "v_mfma_f32_16x16x32_bf16 v[188:191], v[132:135], v[76:79], 0\n\t" \
  "v_mfma_f32_16x16x32_bf16 v[188:191], v[128:131], v[68:71], v[188:191]\n\t" \
  "s_waitcnt lgkmcnt(0)\n\t" \
  "s_getpc_b64 s[88:89]\n\t" \
  "s_branch 28b\n\t" \
  "s_barrier\n\t" \
  "v_mfma_f32_16x16x32_bf16 v[196:199], v[128:131], v[84:87], 0\n\t" \
  "ds_read_b64_tr_b16 v[72:73], v54 offset:34816\n\t" \
  "v_mfma_f32_16x16x32_bf16 v[196:199], v[132:135], v[80:83], v[196:199]\n\t" \
  "ds_read_b64_tr_b16 v[74:75], v54 offset:35328\n\t" \
  "v_mfma_f32_16x16x32_bf16 v[204:207], v[132:135], v[92:95], 0\n\t" \
  "ds_read_b128 v[64:67], v52 offset:2048\n\t" \
  "v_mfma_f32_16x16x32_bf16 v[204:207], v[128:131], v[88:91], v[204:207]\n\t" \
  "ds_read_b64_tr_b16 v[108:109], v54 offset:38912\n\t" \
  "v_mfma_f32_16x16x32_bf16 v[212:215], v[128:131], v[100:103], 0\n\t" \
  "ds_read_b64_tr_b16 v[110:111], v54 offset:39424\n\t" \
  "v_mfma_f32_16x16x32_bf16 v[212:215], v[132:135], v[96:99], v[212:215]\n\t" \
  "ds_read_b128 v[104:107], v53 offset:2048\n\t" \
  "v_mfma_f32_16x16x32_bf16 v[152:155], v[136:139], v[68:71], 0\n\t" \
  "ds_read_b128 v[112:115], v53 offset:4096\n\t" \
  "v_mfma_f32_16x16x32_bf16 v[152:155], v[140:143], v[76:79], v[152:155]\n\t" \
  "ds_read_b128 v[116:119], v52 offset:4096\n\t" \
  "v_mfma_f32_16x16x32_bf16 v[160:163], v[140:143], v[80:83], 0\n\t" \
  "ds_read_b128 v[120:123], v52 offset:6144\n\t" \
  "v_mfma_f32_16x16x32_bf16 v[160:163], v[136:139], v[84:87], v[160:163]\n\t" \
  "ds_read_b128 v[124:127], v53 offset:6144\n\t" \
  "v_mfma_f32_16x16x32_bf16 v[168:171], v[136:139], v[88:91], 0\n\t" \
  "ds_read_b128 v[128:131], v53 offset:8192\n\t" \
  "v_mfma_f32_16x16x32_bf16 v[168:171], v[140:143], v[92:95], v[168:171]\n\t" \
  "ds_read_b128 v[132:135], v52 offset:8192\n\t" \
  "v_mfma_f32_16x16x32_bf16 v[176:179], v[140:143], v[96:99], 0\n\t" \
  "ds_read_b64_tr_b16 v[140:141], v55 offset:34816\n\t" \
  "v_mfma_f32_16x16x32_bf16 v[176:179], v[136:139], v[100:103], v[176:179]\n\t" \
  "ds_read_b64_tr_b16 v[142:143], v55 offset:35328\n\t" \
  "v_mfma_f32_16x16x32_bf16 v[156:159], v[144:147], v[68:71], 0\n\t" \
  "ds_read_b64_tr_b16 v[136:137], v55 offset:38912\n\t" \
  "v_mfma_f32_16x16x32_bf16 v[156:159], v[148:151], v[76:79], v[156:159]\n\t" \
  "ds_read_b64_tr_b16 v[138:139], v55 offset:39424\n\t" \
  "v_mfma_f32_16x16x32_bf16 v[164:167], v[148:151], v[80:83], 0\n\t" \
  "ds_read_b64_tr_b16 v[68:69], v54 offset:26624\n\t" \
  "v_mfma_f32_16x16x32_bf16 v[164:167], v[144:147], v[84:87], v[164:167]\n\t" \
  "ds_read_b64_tr_b16 v[70:71], v54 offset:27136\n\t" \
  "v_mfma_f32_16x16x32_bf16 v[172:175], v[144:147], v[88:91], 0\n\t" \
  "ds_read_b64_tr_b16 v[76:77], v54 offset:30720\n\t" \
  "v_mfma_f32_16x16x32_bf16 v[172:175], v[148:151], v[92:95], v[172:175]\n\t" \
  "ds_read_b64_tr_b16 v[78:79], v54 offset:31232\n\t" \
  "v_mfma_f32_16x16x32_bf16 v[180:183], v[148:151], v[96:99], 0\n\t" \
  "ds_read_b128 v[80:83], v52 offset:0\n\t" \
  "v_mfma_f32_16x16x32_bf16 v[180:183], v[144:147], v[100:103], v[180:183]\n\t" \
  "ds_read_b128 v[84:87], v53 offset:0\n\t" \
  "s_waitcnt lgkmcnt(7)\n\t" \
  "v_mfma_f32_16x16x32_bf16 v[216:219], v[64:67], v[72:75], v[216:219]\n\t" \
  "ds_read_b64_tr_b16 v[88:89], v55 offset:26624\n\t" \
  "v_mfma_f32_16x16x32_bf16 v[216:219], v[104:107], v[108:111], v[216:219]\n\t" \
  "ds_read_b64_tr_b16 v[90:91], v55 offset:27136\n\t" \
  "v_mfma_f32_16x16x32_bf16 v[224:227], v[112:115], v[108:111], v[224:227]\n\t" \
  "ds_read_b64_tr_b16 v[92:93], v55 offset:30720\n\t" \
  "v_mfma_f32_16x16x32_bf16 v[224:227], v[116:119], v[72:75], v[224:227]\n\t" \
  "ds_read_b64_tr_b16 v[94:95], v55 offset:31232\n\t" \
  "v_mfma_f32_16x16x32_bf16 v[232:235], v[120:123], v[72:75], v[232:235]\n\t" \
  "ds_read_b64_tr_b16 v[96:97], v54 offset:18432\n\t" \
  "v_mfma_f32_16x16x32_bf16 v[232:235], v[124:127], v[108:111], v[232:235]\n\t" \
  "ds_read_b64_tr_b16 v[98:99], v54 offset:18944\n\t" \
  "v_mfma_f32_16x16x32_bf16 v[240:243], v[128:131], v[108:111], v[240:243]\n\t" \
  "ds_read_b64_tr_b16 v[148:149], v54 offset:22528\n\t" \
  "v_mfma_f32_16x16x32_bf16 v[240:243], v[132:135], v[72:75], v[240:243]\n\t" \
  "ds_read_b64_tr_b16 v[150:151], v54 offset:23040\n\t" \
  "v_mfma_f32_16x16x32_bf16 v[220:223], v[64:67], v[140:143], v[220:223]\n\t" \
  "ds_read_b64_tr_b16 v[100:101], v55 offset:18432\n\t" \
  "s_waitcnt lgkmcnt(7)\n\t" \
  "v_mfma_f32_16x16x32_bf16 v[220:223], v[104:107], v[136:139], v[220:223]\n\t" \
  "ds_read_b64_tr_b16 v[102:103], v55 offset:18944\n\t" \
  "v_mfma_f32_16x16x32_bf16 v[228:231], v[112:115], v[136:139], v[228:231]\n\t" \
  "ds_read_b64_tr_b16 v[144:145], v55 offset:22528\n\t" \
  "v_mfma_f32_16x16x32_bf16 v[228:231], v[116:119], v[140:143], v[228:231]\n\t" \
  "ds_read_b64_tr_b16 v[146:147], v55 offset:23040\n\t" \
  "v_mfma_f32_16x16x32_bf16 v[236:239], v[120:123], v[140:143], v[236:239]\n\t" \
  "v_mfma_f32_16x16x32_bf16 v[236:239], v[124:127], v[136:139], v[236:239]\n\t" \
  "v_mfma_f32_16x16x32_bf16 v[244:247], v[128:131], v[136:139], v[244:247]\n\t" \
  "v_mfma_f32_16x16x32_bf16 v[244:247], v[132:135], v[140:143], v[244:247]\n\t" \
  "v_mfma_f32_16x16x32_bf16 v[184:187], v[68:71], v[64:67], v[184:187]\n\t" \
  "v_mfma_f32_16x16x32_bf16 v[184:187], v[76:79], v[104:107], v[184:187]\n\t" \
  "v_mfma_f32_16x16x32_bf16 v[192:195], v[76:79], v[112:115], v[192:195]\n\t" \
  "v_mfma_f32_16x16x32_bf16 v[192:195], v[68:71], v[116:119], v[192:195]\n\t" \
  "v_mfma_f32_16x16x32_bf16 v[200:203], v[68:71], v[120:123], v[200:203]\n\t" \
  "v_mfma_f32_16x16x32_bf16 v[200:203], v[76:79], v[124:127], v[200:203]\n\t" \
  "v_mfma_f32_16x16x32_bf16 v[208:211], v[76:79], v[128:131], v[208:211]\n\t" \
  "v_mfma_f32_16x16x32_bf16 v[208:211], v[68:71], v[132:135], v[208:211]\n\t" \
  "v_mfma_f32_16x16x32_bf16 v[248:251], v[68:71], v[80:83], v[248:251]\n\t" \
  "v_mfma_f32_16x16x32_bf16 v[248:251], v[76:79], v[84:87], v[248:251]\n\t" \
  "v_mfma_f32_16x16x32_bf16 v[252:255], v[88:91], v[80:83], v[252:255]\n\t" \
  "s_waitcnt lgkmcnt(0)\n\t" \
  "v_mfma_f32_16x16x32_bf16 v[252:255], v[92:95], v[84:87], v[252:255]\n\t" \
  "v_mfma_f32_16x16x32_bf16 v[188:191], v[92:95], v[104:107], v[188:191]\n\t" \
  "v_mfma_f32_16x16x32_bf16 v[188:191], v[88:91], v[64:67], v[188:191]\n\t" \
  "s_barrier\n\t" \
  "v_mfma_f32_16x16x32_bf16 v[196:199], v[88:91], v[116:119], v[196:199]\n\t" \
  "ds_read_b64_tr_b16 v[108:109], v58 offset:34816\n\t" \
  "v_mfma_f32_16x16x32_bf16 v[196:199], v[92:95], v[112:115], v[196:199]\n\t" \
  "ds_read_b64_tr_b16 v[110:111], v58 offset:35328\n\t" \
  "v_mfma_f32_16x16x32_bf16 v[204:207], v[92:95], v[124:127], v[204:207]\n\t" \
  "ds_read_b128 v[72:75], v56 offset:2048\n\t" \
  "v_mfma_f32_16x16x32_bf16 v[204:207], v[88:91], v[120:123], v[204:207]\n\t" \
  "ds_read_b64_tr_b16 v[136:137], v58 offset:38912\n\t" \
  "v_mfma_f32_16x16x32_bf16 v[212:215], v[88:91], v[132:135], v[212:215]\n\t" \
  "ds_read_b64_tr_b16 v[138:139], v58 offset:39424\n\t" \
  "v_mfma_f32_16x16x32_bf16 v[212:215], v[92:95], v[128:131], v[212:215]\n\t" \
  "ds_read_b128 v[140:143], v57 offset:2048\n\t" \
  "v_mfma_f32_16x16x32_bf16 v[152:155], v[96:99], v[64:67], v[152:155]\n\t" \
  "ds_read_b128 v[68:71], v57 offset:4096\n\t" \
  "v_mfma_f32_16x16x32_bf16 v[152:155], v[148:151], v[104:107], v[152:155]\n\t" \
  "ds_read_b128 v[76:79], v56 offset:4096\n\t" \
  "v_mfma_f32_16x16x32_bf16 v[160:163], v[148:151], v[112:115], v[160:163]\n\t" \
  "ds_read_b128 v[80:83], v56 offset:6144\n\t" \
  "v_mfma_f32_16x16x32_bf16 v[160:163], v[96:99], v[116:119], v[160:163]\n\t" \
  "ds_read_b128 v[84:87], v57 offset:6144\n\t" \
  "v_mfma_f32_16x16x32_bf16 v[168:171], v[96:99], v[120:123], v[168:171]\n\t" \
  "ds_read_b128 v[88:91], v57 offset:8192\n\t" \
  "v_mfma_f32_16x16x32_bf16 v[168:171], v[148:151], v[124:127], v[168:171]\n\t" \
  "ds_read_b128 v[92:95], v56 offset:8192\n\t" \
  "v_mfma_f32_16x16x32_bf16 v[176:179], v[148:151], v[128:131], v[176:179]\n\t" \
  "ds_read_b64_tr_b16 v[148:149], v59 offset:34816\n\t" \
  "v_mfma_f32_16x16x32_bf16 v[176:179], v[96:99], v[132:135], v[176:179]\n\t" \
  "ds_read_b64_tr_b16 v[150:151], v59 offset:35328\n\t" \
  "v_mfma_f32_16x16x32_bf16 v[156:159], v[100:103], v[64:67], v[156:159]\n\t" \
  "ds_read_b64_tr_b16 v[96:97], v59 offset:38912\n\t" \
  "v_mfma_f32_16x16x32_bf16 v[156:159], v[144:147], v[104:107], v[156:159]\n\t" \
  "ds_read_b64_tr_b16 v[98:99], v59 offset:39424\n\t" \
  "v_mfma_f32_16x16x32_bf16 v[164:167], v[144:147], v[112:115], v[164:167]\n\t" \
  "ds_read_b64_tr_b16 v[64:65], v58 offset:26624\n\t" \
  "v_mfma_f32_16x16x32_bf16 v[164:167], v[100:103], v[116:119], v[164:167]\n\t" \
  "ds_read_b64_tr_b16 v[66:67], v58 offset:27136\n\t" \
  "v_mfma_f32_16x16x32_bf16 v[172:175], v[100:103], v[120:123], v[172:175]\n\t" \
  "ds_read_b64_tr_b16 v[104:105], v58 offset:30720\n\t" \
  "v_mfma_f32_16x16x32_bf16 v[172:175], v[144:147], v[124:127], v[172:175]\n\t" \
  "ds_read_b64_tr_b16 v[106:107], v58 offset:31232\n\t" \
  "v_mfma_f32_16x16x32_bf16 v[180:183], v[144:147], v[128:131], v[180:183]\n\t" \
  "ds_read_b128 v[112:115], v56 offset:0\n\t" \
  "v_mfma_f32_16x16x32_bf16 v[180:183], v[100:103], v[132:135], v[180:183]\n\t" \
  "ds_read_b128 v[116:119], v57 offset:0\n\t" \
  "s_waitcnt lgkmcnt(7)\n\t" \
  "v_mfma_f32_16x16x32_bf16 v[216:219], v[72:75], v[108:111], v[216:219]\n\t" \
  "ds_read_b64_tr_b16 v[120:121], v59 offset:26624\n\t" \
  "v_mfma_f32_16x16x32_bf16 v[216:219], v[140:143], v[136:139], v[216:219]\n\t" \
  "ds_read_b64_tr_b16 v[122:123], v59 offset:27136\n\t" \
  "v_mfma_f32_16x16x32_bf16 v[224:227], v[68:71], v[136:139], v[224:227]\n\t" \
  "ds_read_b64_tr_b16 v[124:125], v59 offset:30720\n\t" \
  "v_mfma_f32_16x16x32_bf16 v[224:227], v[76:79], v[108:111], v[224:227]\n\t" \
  "ds_read_b64_tr_b16 v[126:127], v59 offset:31232\n\t" \
  "v_mfma_f32_16x16x32_bf16 v[232:235], v[80:83], v[108:111], v[232:235]\n\t" \
  "ds_read_b64_tr_b16 v[128:129], v58 offset:18432\n\t" \
  "v_mfma_f32_16x16x32_bf16 v[232:235], v[84:87], v[136:139], v[232:235]\n\t" \
  "ds_read_b64_tr_b16 v[130:131], v58 offset:18944\n\t" \
  "v_mfma_f32_16x16x32_bf16 v[240:243], v[88:91], v[136:139], v[240:243]\n\t" \
  "ds_read_b64_tr_b16 v[144:145], v58 offset:22528\n\t" \
  "v_mfma_f32_16x16x32_bf16 v[240:243], v[92:95], v[108:111], v[240:243]\n\t" \
  "ds_read_b64_tr_b16 v[146:147], v58 offset:23040\n\t" \
  "v_mfma_f32_16x16x32_bf16 v[220:223], v[72:75], v[148:151], v[220:223]\n\t" \
  "ds_read_b64_tr_b16 v[100:101], v59 offset:18432\n\t" \
  "s_waitcnt lgkmcnt(7)\n\t" \
  "v_mfma_f32_16x16x32_bf16 v[220:223], v[140:143], v[96:99], v[220:223]\n\t" \
  "ds_read_b64_tr_b16 v[102:103], v59 offset:18944\n\t" \
  "v_mfma_f32_16x16x32_bf16 v[228:231], v[68:71], v[96:99], v[228:231]\n\t" \
  "ds_read_b64_tr_b16 v[132:133], v59 offset:22528\n\t" \
  "v_mfma_f32_16x16x32_bf16 v[228:231], v[76:79], v[148:151], v[228:231]\n\t" \
  "ds_read_b64_tr_b16 v[134:135], v59 offset:23040\n\t" \
  "v_mfma_f32_16x16x32_bf16 v[236:239], v[80:83], v[148:151], v[236:239]\n\t" \
  "v_mfma_f32_16x16x32_bf16 v[236:239], v[84:87], v[96:99], v[236:239]\n\t" \
  "v_mfma_f32_16x16x32_bf16 v[244:247], v[88:91], v[96:99], v[244:247]\n\t" \
  "v_mfma_f32_16x16x32_bf16 v[244:247], v[92:95], v[148:151], v[244:247]\n\t" \
  "503:\n\t" \
  "s_cbranch_execz 507f\n\t" \
  "v_mfma_f32_16x16x32_bf16 v[184:187], v[64:67], v[72:75], v[184:187]\n\t" \
  "v_mfma_f32_16x16x32_bf16 v[184:187], v[104:107], v[140:143], v[184:187]\n\t" \
  "v_mfma_f32_16x16x32_bf16 v[192:195], v[104:107], v[68:71], v[192:195]\n\t" \
  "v_mfma_f32_16x16x32_bf16 v[192:195], v[64:67], v[76:79], v[192:195]\n\t" \
  "v_mfma_f32_16x16x32_bf16 v[200:203], v[64:67], v[80:83], v[200:203]\n\t" \
  "v_mfma_f32_16x16x32_bf16 v[200:203], v[104:107], v[84:87], v[200:203]\n\t" \
  "v_mfma_f32_16x16x32_bf16 v[208:211], v[104:107], v[88:91], v[208:211]\n\t" \
  "v_mfma_f32_16x16x32_bf16 v[208:211], v[64:67], v[92:95], v[208:211]\n\t" \
  "v_mfma_f32_16x16x32_bf16 v[248:251], v[64:67], v[112:115], v[248:251]\n\t" \
  "v_mfma_f32_16x16x32_bf16 v[248:251], v[104:107], v[116:119], v[248:251]\n\t" \
  "v_mfma_f32_16x16x32_bf16 v[252:255], v[120:123], v[112:115], v[252:255]\n\t" \
  "s_waitcnt lgkmcnt(0)\n\t" \
  "v_mfma_f32_16x16x32_bf16 v[252:255], v[124:127], v[116:119], v[252:255]\n\t" \
  "v_mfma_f32_16x16x32_bf16 v[188:191], v[124:127], v[140:143], v[188:191]\n\t" \
  "v_mfma_f32_16x16x32_bf16 v[188:191], v[120:123], v[72:75], v[188:191]\n\t" \
  "s_barrier\n\t" \
  "v_mfma_f32_16x16x32_bf16 v[196:199], v[120:123], v[76:79], v[196:199]\n\t" \
  "ds_read_b64_tr_b16 v[136:137], v62 offset:34816\n\t" \
  "v_bfe_u32 v30, v4, 9, 1\n\t" \
  "v_mfma_f32_16x16x32_bf16 v[196:199], v[124:127], v[68:71], v[196:199]\n\t" \
  "ds_read_b64_tr_b16 v[138:139], v62 offset:35328\n\t" \
  "v_mad_i32_i24 v30, v30, -16, v0\n\t" \
  "v_mfma_f32_16x16x32_bf16 v[204:207], v[124:127], v[84:87], v[204:207]\n\t" \
  "ds_read_b128 v[108:111], v60 offset:2048\n\t" \
  "ds_read_b128 v[26:29], v0 offset:1552\n\t" \
  "v_mfma_f32_16x16x32_bf16 v[204:207], v[120:123], v[80:83], v[204:207]\n\t" \
  "ds_read_b64_tr_b16 v[96:97], v62 offset:38912\n\t" \
  "ds_read_b128 v[30:33], v30 offset:1040\n\t" \
  "v_mfma_f32_16x16x32_bf16 v[212:215], v[120:123], v[92:95], v[212:215]\n\t" \
  "ds_read_b64_tr_b16 v[98:99], v62 offset:39424\n\t" \
  "ds_read_b128 v[34:37], v0 offset:512\n\t" \
  "v_mfma_f32_16x16x32_bf16 v[212:215], v[124:127], v[88:91], v[212:215]\n\t" \
  "ds_read_b128 v[148:151], v61 offset:2048\n\t" \
  "ds_read_b64_tr_b16 v[38:39], v1 offset:512\n\t" \
  "v_mfma_f32_16x16x32_bf16 v[152:155], v[128:131], v[72:75], v[152:155]\n\t" \
  "ds_read_b128 v[64:67], v61 offset:4096\n\t" \
  "ds_read_b64_tr_b16 v[40:41], v1 offset:0\n\t" \
  "v_mfma_f32_16x16x32_bf16 v[152:155], v[144:147], v[140:143], v[152:155]\n\t" \
  "ds_read_b128 v[104:107], v60 offset:4096\n\t" \
  "s_waitcnt vmcnt(0)\n\t" \
  "v_mfma_f32_16x16x32_bf16 v[160:163], v[144:147], v[68:71], v[160:163]\n\t" \
  "ds_read_b128 v[112:115], v60 offset:6144\n\t" \
  "v_perm_b32 v18, v10, v10, v14\n\t" \
  "v_mfma_f32_16x16x32_bf16 v[160:163], v[128:131], v[76:79], v[160:163]\n\t" \
  "ds_read_b128 v[116:119], v61 offset:6144\n\t" \
  "v_perm_b32 v19, v10, v10, v15\n\t" \
  "v_mfma_f32_16x16x32_bf16 v[168:171], v[128:131], v[80:83], v[168:171]\n\t" \
  "ds_read_b128 v[120:123], v61 offset:8192\n\t" \
  "v_perm_b32 v20, v10, v10, v16\n\t" \
  "v_mfma_f32_16x16x32_bf16 v[168:171], v[144:147], v[84:87], v[168:171]\n\t" \
  "ds_read_b128 v[124:127], v60 offset:8192\n\t" \
  "v_perm_b32 v21, v10, v10, v17\n\t" \
  "v_mfma_f32_16x16x32_bf16 v[176:179], v[144:147], v[88:91], v[176:179]\n\t" \
  "ds_read_b64_tr_b16 v[144:145], v63 offset:34816\n\t" \
  "v_perm_b32 v22, v11, v11, v14\n\t" \
  "v_mfma_f32_16x16x32_bf16 v[176:179], v[128:131], v[92:95], v[176:179]\n\t" \
  "ds_read_b64_tr_b16 v[146:147], v63 offset:35328\n\t" \
  "v_perm_b32 v23, v11, v11, v15\n\t" \
  "v_mfma_f32_16x16x32_bf16 v[156:159], v[100:103], v[72:75], v[156:159]\n\t" \
  "ds_read_b64_tr_b16 v[128:129], v63 offset:38912\n\t" \
  "v_perm_b32 v24, v11, v11, v16\n\t" \
  "v_mfma_f32_16x16x32_bf16 v[156:159], v[132:135], v[140:143], v[156:159]\n\t" \
  "ds_read_b64_tr_b16 v[130:131], v63 offset:39424\n\t" \
  "v_perm_b32 v25, v11, v11, v17\n\t" \
  "v_mfma_f32_16x16x32_bf16 v[164:167], v[132:135], v[68:71], v[164:167]\n\t" \
  "ds_read_b64_tr_b16 v[72:73], v62 offset:26624\n\t" \
  "v_mfma_f32_16x16x32_bf16 v[164:167], v[100:103], v[76:79], v[164:167]\n\t" \
  "ds_read_b64_tr_b16 v[74:75], v62 offset:27136\n\t" \
  "v_mfma_f32_16x16x32_bf16 v[172:175], v[100:103], v[80:83], v[172:175]\n\t" \
  "ds_read_b64_tr_b16 v[140:141], v62 offset:30720\n\t" \
  "s_waitcnt lgkmcnt(6)\n\t" \
  "v_mfma_f32_16x16x32_bf16 v[42:45], v[26:29], v[18:21], 0\n\t" \
  "v_mfma_f32_16x16x32_bf16 v[42:45], v[30:33], v[22:25], v[42:45]\n\t" \
  "v_mfma_f32_16x16x32_bf16 v[46:49], v[30:33], v[18:21], 0\n\t" \
  "v_mfma_f32_16x16x32_bf16 v[46:49], v[34:37], v[22:25], v[46:49]\n\t" \
  "ds_read_b128 v[26:29], v0 offset:1808\n\t" \
  "ds_read_b128 v[30:33], v0 offset:1296\n\t" \
  "v_mfma_f32_16x16x32_bf16 v[172:175], v[132:135], v[84:87], v[172:175]\n\t" \
  "ds_read_b64_tr_b16 v[142:143], v62 offset:31232\n\t" \
  "ds_read_b128 v[34:37], v0 offset:768\n\t" \
  "v_mfma_f32_16x16x32_bf16 v[180:183], v[132:135], v[88:91], v[180:183]\n\t" \
  "ds_read_b128 v[68:71], v60 offset:0\n\t" \
  "v_mfma_f32_16x16x32_bf16 v[180:183], v[100:103], v[92:95], v[180:183]\n\t" \
  "ds_read_b128 v[76:79], v61 offset:0\n\t" \
  "v_mfma_f32_16x16x32_bf16 v[216:219], v[108:111], v[136:139], v[216:219]\n\t" \
  "ds_read_b64_tr_b16 v[80:81], v63 offset:26624\n\t" \
  "v_lshlrev_b32 v50, 16, v38\n\t" \
  "v_mfma_f32_16x16x32_bf16 v[216:219], v[148:151], v[96:99], v[216:219]\n\t" \
  "ds_read_b64_tr_b16 v[82:83], v63 offset:27136\n\t" \
  "v_lshlrev_b32 v51, 16, v40\n\t" \
  "v_mfma_f32_16x16x32_bf16 v[224:227], v[64:67], v[96:99], v[224:227]\n\t" \
  "ds_read_b64_tr_b16 v[84:85], v63 offset:30720\n\t" \
  "v_mul_f32 v42, v42, v50\n\t" \
  "v_mfma_f32_16x16x32_bf16 v[224:227], v[104:107], v[136:139], v[224:227]\n\t" \
  "ds_read_b64_tr_b16 v[86:87], v63 offset:31232\n\t" \
  "v_mul_f32 v46, v46, v51\n\t" \
  "v_mfma_f32_16x16x32_bf16 v[232:235], v[112:115], v[136:139], v[232:235]\n\t" \
  "ds_read_b64_tr_b16 v[88:89], v62 offset:18432\n\t" \
  "v_cvt_pk_bf16_f32 v42, v42, v46\n\t" \
  "v_mfma_f32_16x16x32_bf16 v[232:235], v[116:119], v[96:99], v[232:235]\n\t" \
  "ds_read_b64_tr_b16 v[90:91], v62 offset:18944\n\t" \
  "v_and_b32 v50, 0xffff0000, v38\n\t" \
  "v_mfma_f32_16x16x32_bf16 v[240:243], v[120:123], v[96:99], v[240:243]\n\t" \
  "ds_read_b64_tr_b16 v[132:133], v62 offset:22528\n\t" \
  "v_and_b32 v51, 0xffff0000, v40\n\t" \
  "v_mfma_f32_16x16x32_bf16 v[240:243], v[124:127], v[136:139], v[240:243]\n\t" \
  "ds_read_b64_tr_b16 v[134:135], v62 offset:23040\n\t" \
  "v_mul_f32 v43, v43, v50\n\t" \
  "s_waitcnt lgkmcnt(7)\n\t" \
  "v_mfma_f32_16x16x32_bf16 v[220:223], v[108:111], v[144:147], v[220:223]\n\t" \
  "ds_read_b64_tr_b16 v[92:93], v63 offset:18432\n\t" \
  "v_mul_f32 v47, v47, v51\n\t" \
  "v_mfma_f32_16x16x32_bf16 v[220:223], v[148:151], v[128:131], v[220:223]\n\t" \
  "ds_read_b64_tr_b16 v[94:95], v63 offset:18944\n\t" \
  "v_cvt_pk_bf16_f32 v43, v43, v47\n\t" \
  "v_mfma_f32_16x16x32_bf16 v[228:231], v[64:67], v[128:131], v[228:231]\n\t" \
  "ds_read_b64_tr_b16 v[100:101], v63 offset:22528\n\t" \
  "v_lshlrev_b32 v50, 16, v39\n\t" \
  "v_mfma_f32_16x16x32_bf16 v[228:231], v[104:107], v[144:147], v[228:231]\n\t" \
  "ds_read_b64_tr_b16 v[102:103], v63 offset:23040\n\t" \
  "v_lshlrev_b32 v51, 16, v41\n\t" \
  "v_mfma_f32_16x16x32_bf16 v[236:239], v[112:115], v[144:147], v[236:239]\n\t" \
  "v_mul_f32 v44, v44, v50\n\t" \
  "v_mul_f32 v48, v48, v51\n\t" \
  "v_mfma_f32_16x16x32_bf16 v[236:239], v[116:119], v[128:131], v[236:239]\n\t" \
  "v_cvt_pk_bf16_f32 v44, v44, v48\n\t" \
  "v_and_b32 v50, 0xffff0000, v39\n\t" \
  "v_mfma_f32_16x16x32_bf16 v[244:247], v[120:123], v[128:131], v[244:247]\n\t" \
  "v_and_b32 v51, 0xffff0000, v41\n\t" \
  "v_mul_f32 v45, v45, v50\n\t" \
  "v_mfma_f32_16x16x32_bf16 v[244:247], v[124:127], v[144:147], v[244:247]\n\t" \
  "v_mul_f32 v49, v49, v51\n\t" \
  "ds_read_b64_tr_b16 v[38:39], v1 offset:768\n\t" \
  "v_mfma_f32_16x16x32_bf16 v[184:187], v[72:75], v[108:111], v[184:187]\n\t" \
  "ds_read_b64_tr_b16 v[40:41], v1 offset:256\n\t" \
  "v_cvt_pk_bf16_f32 v45, v45, v49\n\t" \
  "v_mfma_f32_16x16x32_bf16 v[184:187], v[140:143], v[148:151], v[184:187]\n\t" \
  "v_mfma_f32_16x16x32_bf16 v[192:195], v[140:143], v[64:67], v[192:195]\n\t" \
  "v_mfma_f32_16x16x32_bf16 v[192:195], v[72:75], v[104:107], v[192:195]\n\t" \
  "v_mfma_f32_16x16x32_bf16 v[200:203], v[72:75], v[112:115], v[200:203]\n\t" \
  "v_mfma_f32_16x16x32_bf16 v[200:203], v[140:143], v[116:119], v[200:203]\n\t" \
  "v_mfma_f32_16x16x32_bf16 v[208:211], v[140:143], v[120:123], v[208:211]\n\t" \
  "v_mfma_f32_16x16x32_bf16 v[208:211], v[72:75], v[124:127], v[208:211]\n\t" \
  "v_mfma_f32_16x16x32_bf16 v[248:251], v[72:75], v[68:71], v[248:251]\n\t" \
  "v_mfma_f32_16x16x32_bf16 v[248:251], v[140:143], v[76:79], v[248:251]\n\t" \
  "s_waitcnt lgkmcnt(0)\n\t" \
  "v_mfma_f32_16x16x32_bf16 v[252:255], v[80:83], v[68:71], v[252:255]\n\t" \
  "v_mfma_f32_16x16x32_bf16 v[252:255], v[84:87], v[76:79], v[252:255]\n\t" \
  "v_mfma_f32_16x16x32_bf16 v[188:191], v[84:87], v[148:151], v[188:191]\n\t" \
  "v_mfma_f32_16x16x32_bf16 v[188:191], v[80:83], v[108:111], v[188:191]\n\t" \
  "s_barrier\n\t" \
  "v_mfma_f32_16x16x32_bf16 v[196:199], v[80:83], v[104:107], v[196:199]\n\t" \
  "ds_read_b64_tr_b16 v[96:97], v54 offset:34816\n\t" \
  "v_mfma_f32_16x16x32_bf16 v[196:199], v[84:87], v[64:67], v[196:199]\n\t" \
  "v_mov_b32 v50, 163344\n\t" \
  "ds_read_b64_tr_b16 v[98:99], v54 offset:35328\n\t" \
  "v_mfma_f32_16x16x32_bf16 v[204:207], v[84:87], v[116:119], v[204:207]\n\t" \
  "ds_read_b32 v50, v50 offset:24\n\t" \
  "ds_read_b128 v[136:139], v52 offset:2048\n\t" \
  "v_mfma_f32_16x16x32_bf16 v[204:207], v[80:83], v[112:115], v[204:207]\n\t" \
  "ds_read_b64_tr_b16 v[128:129], v54 offset:38912\n\t" \
  "v_mfma_f32_16x16x32_bf16 v[212:215], v[80:83], v[124:127], v[212:215]\n\t" \
  "ds_read_b64_tr_b16 v[130:131], v54 offset:39424\n\t" \
  "v_mfma_f32_16x16x32_bf16 v[212:215], v[84:87], v[120:123], v[212:215]\n\t" \
  "ds_read_b128 v[144:147], v53 offset:2048\n\t" \
  "v_mfma_f32_16x16x32_bf16 v[152:155], v[88:91], v[108:111], v[152:155]\n\t" \
  "ds_read_b128 v[72:75], v53 offset:4096\n\t" \
  "v_mfma_f32_16x16x32_bf16 v[152:155], v[132:135], v[148:151], v[152:155]\n\t" \
  "ds_read_b128 v[140:143], v52 offset:4096\n\t" \
  "v_mfma_f32_16x16x32_bf16 v[160:163], v[132:135], v[64:67], v[160:163]\n\t" \
  "ds_read_b128 v[68:71], v52 offset:6144\n\t" \
  "v_mfma_f32_16x16x32_bf16 v[160:163], v[88:91], v[104:107], v[160:163]\n\t" \
  "ds_read_b128 v[76:79], v53 offset:6144\n\t" \
  "v_mfma_f32_16x16x32_bf16 v[168:171], v[88:91], v[112:115], v[168:171]\n\t" \
  "ds_read_b128 v[80:83], v53 offset:8192\n\t" \
  "v_mfma_f32_16x16x32_bf16 v[168:171], v[132:135], v[116:119], v[168:171]\n\t" \
  "ds_read_b128 v[84:87], v52 offset:8192\n\t" \
  "v_mfma_f32_16x16x32_bf16 v[176:179], v[132:135], v[120:123], v[176:179]\n\t" \
  "s_waitcnt lgkmcnt(10)\n\t" \
  "v_readfirstlane_b32 s88, v50\n\t" \
  "s_mov_b32 s89, 0\n\t" \
  "ds_read_b64_tr_b16 v[132:133], v55 offset:34816\n\t" \
  "v_mfma_f32_16x16x32_bf16 v[176:179], v[88:91], v[124:127], v[176:179]\n\t" \
  "ds_read_b64_tr_b16 v[134:135], v55 offset:35328\n\t" \
  "v_mfma_f32_16x16x32_bf16 v[156:159], v[92:95], v[108:111], v[156:159]\n\t" \
  "s_xor_b64 %24, %24, s[88:89]\n\t" \
  "ds_read_b64_tr_b16 v[88:89], v55 offset:38912\n\t" \
  "v_mfma_f32_16x16x32_bf16 v[156:159], v[100:103], v[148:151], v[156:159]\n\t" \
  "s_xor_b64 %25, %25, s[88:89]\n\t" \
  "ds_read_b64_tr_b16 v[90:91], v55 offset:39424\n\t" \
  "v_mfma_f32_16x16x32_bf16 v[164:167], v[100:103], v[64:67], v[164:167]\n\t" \
  "s_xor_b64 %14, %14, s[88:89]\n\t" \
  "ds_read_b64_tr_b16 v[108:109], v54 offset:26624\n\t" \
  "v_mfma_f32_16x16x32_bf16 v[164:167], v[92:95], v[104:107], v[164:167]\n\t" \
  "s_xor_b64 %19, %19, s[88:89]\n\t" \
  "ds_read_b64_tr_b16 v[110:111], v54 offset:27136\n\t" \
  "v_mfma_f32_16x16x32_bf16 v[172:175], v[92:95], v[112:115], v[172:175]\n\t" \
  "s_xor_b64 %21, %21, s[88:89]\n\t" \
  "ds_read_b64_tr_b16 v[148:149], v54 offset:30720\n\t" \
  "v_mfma_f32_16x16x32_bf16 v[172:175], v[100:103], v[116:119], v[172:175]\n\t" \
  "s_xor_b64 s[92:93], s[92:93], s[88:89]\n\t" \
  "ds_read_b64_tr_b16 v[150:151], v54 offset:31232\n\t" \
  "v_mfma_f32_16x16x32_bf16 v[180:183], v[100:103], v[120:123], v[180:183]\n\t" \
  "ds_read_b128 v[64:67], v52 offset:0\n\t" \
  "v_mfma_f32_16x16x32_bf16 v[180:183], v[92:95], v[124:127], v[180:183]\n\t" \
  "ds_read_b128 v[104:107], v53 offset:0\n\t" \
  "s_waitcnt lgkmcnt(7)\n\t" \
  "v_mfma_f32_16x16x32_bf16 v[216:219], v[136:139], v[96:99], v[216:219]\n\t" \
  "ds_read_b64_tr_b16 v[112:113], v55 offset:26624\n\t" \
  "v_mfma_f32_16x16x32_bf16 v[216:219], v[144:147], v[128:131], v[216:219]\n\t" \
  "ds_read_b64_tr_b16 v[114:115], v55 offset:27136\n\t" \
  "v_mfma_f32_16x16x32_bf16 v[224:227], v[72:75], v[128:131], v[224:227]\n\t" \
  "ds_read_b64_tr_b16 v[116:117], v55 offset:30720\n\t" \
  "v_mfma_f32_16x16x32_bf16 v[224:227], v[140:143], v[96:99], v[224:227]\n\t" \
  "ds_read_b64_tr_b16 v[118:119], v55 offset:31232\n\t" \
  "v_mfma_f32_16x16x32_bf16 v[232:235], v[68:71], v[96:99], v[232:235]\n\t" \
  "ds_read_b64_tr_b16 v[100:101], v54 offset:18432\n\t" \
  "v_mfma_f32_16x16x32_bf16 v[232:235], v[76:79], v[128:131], v[232:235]\n\t" \
  "ds_read_b64_tr_b16 v[102:103], v54 offset:18944\n\t" \
  "v_mfma_f32_16x16x32_bf16 v[240:243], v[80:83], v[128:131], v[240:243]\n\t" \
  "ds_read_b64_tr_b16 v[120:121], v54 offset:22528\n\t" \
  "v_mfma_f32_16x16x32_bf16 v[240:243], v[84:87], v[96:99], v[240:243]\n\t" \
  "ds_read_b64_tr_b16 v[122:123], v54 offset:23040\n\t" \
  "v_mfma_f32_16x16x32_bf16 v[220:223], v[136:139], v[132:135], v[220:223]\n\t" \
  "ds_read_b64_tr_b16 v[92:93], v55 offset:18432\n\t" \
  "s_waitcnt lgkmcnt(7)\n\t" \
  "v_mfma_f32_16x16x32_bf16 v[220:223], v[144:147], v[88:91], v[220:223]\n\t" \
  "ds_read_b64_tr_b16 v[94:95], v55 offset:18944\n\t" \
  "v_mfma_f32_16x16x32_bf16 v[228:231], v[72:75], v[88:91], v[228:231]\n\t" \
  "ds_read_b64_tr_b16 v[124:125], v55 offset:22528\n\t" \
  "v_mfma_f32_16x16x32_bf16 v[228:231], v[140:143], v[132:135], v[228:231]\n\t" \
  "ds_read_b64_tr_b16 v[126:127], v55 offset:23040\n\t" \
  "v_mfma_f32_16x16x32_bf16 v[236:239], v[68:71], v[132:135], v[236:239]\n\t" \
  "v_mfma_f32_16x16x32_bf16 v[236:239], v[76:79], v[88:91], v[236:239]\n\t" \
  "v_mfma_f32_16x16x32_bf16 v[244:247], v[80:83], v[88:91], v[244:247]\n\t" \
  "v_mfma_f32_16x16x32_bf16 v[244:247], v[84:87], v[132:135], v[244:247]\n\t" \
  "v_mfma_f32_16x16x32_bf16 v[184:187], v[108:111], v[136:139], v[184:187]\n\t" \
  "v_mfma_f32_16x16x32_bf16 v[184:187], v[148:151], v[144:147], v[184:187]\n\t" \
  "v_mfma_f32_16x16x32_bf16 v[192:195], v[148:151], v[72:75], v[192:195]\n\t" \
  "v_mfma_f32_16x16x32_bf16 v[192:195], v[108:111], v[140:143], v[192:195]\n\t" \
  "v_mfma_f32_16x16x32_bf16 v[200:203], v[108:111], v[68:71], v[200:203]\n\t" \
  "global_store_dwordx4 v2, v[42:45], %24 offset:0 sc1\n\t" \
  "v_mfma_f32_16x16x32_bf16 v[200:203], v[148:151], v[76:79], v[200:203]\n\t" \
  "s_nop 0\n\t" \
  "v_mfma_f32_16x16x32_bf16 v[42:45], v[26:29], v[18:21], 0\n\t" \
  "v_mfma_f32_16x16x32_bf16 v[42:45], v[30:33], v[22:25], v[42:45]\n\t" \
  "v_mfma_f32_16x16x32_bf16 v[46:49], v[30:33], v[18:21], 0\n\t" \
  "v_mfma_f32_16x16x32_bf16 v[46:49], v[34:37], v[22:25], v[46:49]\n\t" \
  "v_bfe_u32 v30, v4, 9, 1\n\t" \
  "v_mad_i32_i24 v30, v30, -16, v0\n\t" \
  "v_mfma_f32_16x16x32_bf16 v[208:211], v[148:151], v[80:83], v[208:211]\n\t" \
  "ds_read_b128 v[26:29], v0 offset:1584\n\t" \
  "ds_read_b128 v[30:33], v30 offset:1072\n\t" \
  "v_mfma_f32_16x16x32_bf16 v[208:211], v[108:111], v[84:87], v[208:211]\n\t" \
  "ds_read_b128 v[34:37], v0 offset:544\n\t" \
  "v_perm_b32 v18, v12, v12, v14\n\t" \
  "v_mfma_f32_16x16x32_bf16 v[248:251], v[108:111], v[64:67], v[248:251]\n\t" \
  "v_perm_b32 v19, v12, v12, v15\n\t" \
  "v_perm_b32 v20, v12, v12, v16\n\t" \
  "v_mfma_f32_16x16x32_bf16 v[248:251], v[148:151], v[104:107], v[248:251]\n\t" \
  "v_perm_b32 v21, v12, v12, v17\n\t" \
  "v_perm_b32 v22, v13, v13, v14\n\t" \
  "v_mfma_f32_16x16x32_bf16 v[252:255], v[112:115], v[64:67], v[252:255]\n\t" \
  "v_perm_b32 v23, v13, v13, v15\n\t" \
  "v_perm_b32 v24, v13, v13, v16\n\t" \
  "s_waitcnt lgkmcnt(3)\n\t" \
  "v_mfma_f32_16x16x32_bf16 v[252:255], v[116:119], v[104:107], v[252:255]\n\t" \
  "v_perm_b32 v25, v13, v13, v17\n\t" \
  "v_lshlrev_b32 v50, 16, v38\n\t" \
  "v_mfma_f32_16x16x32_bf16 v[188:191], v[116:119], v[144:147], v[188:191]\n\t" \
  "v_lshlrev_b32 v51, 16, v40\n\t" \
  "v_mul_f32 v42, v42, v50\n\t" \
  "v_mfma_f32_16x16x32_bf16 v[188:191], v[112:115], v[136:139], v[188:191]\n\t" \
  "v_mul_f32 v46, v46, v51\n\t" \
  "v_cvt_pk_bf16_f32 v42, v42, v46\n\t" \
  "s_waitcnt lgkmcnt(0)\n\t" \
  "s_barrier\n\t" \
  "v_mfma_f32_16x16x32_bf16 v[196:199], v[112:115], v[140:143], v[196:199]\n\t" \
  "ds_read_b64_tr_b16 v[128:129], v58 offset:34816\n\t" \
  "v_and_b32 v50, 0xffff0000, v38\n\t" \
  "v_mfma_f32_16x16x32_bf16 v[196:199], v[116:119], v[72:75], v[196:199]\n\t" \
  "ds_read_b64_tr_b16 v[130:131], v58 offset:35328\n\t" \
  "v_and_b32 v51, 0xffff0000, v40\n\t" \
  "v_mfma_f32_16x16x32_bf16 v[204:207], v[116:119], v[76:79], v[204:207]\n\t" \
  "ds_read_b128 v[96:99], v56 offset:2048\n\t" \
  "v_mul_f32 v43, v43, v50\n\t" \
  "v_mfma_f32_16x16x32_bf16 v[204:207], v[112:115], v[68:71], v[204:207]\n\t" \
  "ds_read_b64_tr_b16 v[88:89], v58 offset:38912\n\t" \
  "v_mul_f32 v47, v47, v51\n\t" \
  "v_mfma_f32_16x16x32_bf16 v[212:215], v[112:115], v[84:87], v[212:215]\n\t" \
  "ds_read_b64_tr_b16 v[90:91], v58 offset:39424\n\t" \
  "v_cvt_pk_bf16_f32 v43, v43, v47\n\t" \
  "v_mfma_f32_16x16x32_bf16 v[212:215], v[116:119], v[80:83], v[212:215]\n\t" \
  "ds_read_b128 v[132:135], v57 offset:2048\n\t" \
  "v_lshlrev_b32 v50, 16, v39\n\t" \
  "v_mfma_f32_16x16x32_bf16 v[152:155], v[100:103], v[136:139], v[152:155]\n\t" \
  "ds_read_b128 v[108:111], v57 offset:4096\n\t" \
  "v_lshlrev_b32 v51, 16, v41\n\t" \
  "v_mfma_f32_16x16x32_bf16 v[152:155], v[120:123], v[144:147], v[152:155]\n\t" \
  "ds_read_b128 v[148:151], v56 offset:4096\n\t" \
  "v_mul_f32 v44, v44, v50\n\t" \
  "v_mfma_f32_16x16x32_bf16 v[160:163], v[120:123], v[72:75], v[160:163]\n\t" \
  "ds_read_b128 v[64:67], v56 offset:6144\n\t" \
  "v_mul_f32 v48, v48, v51\n\t" \
  "v_mfma_f32_16x16x32_bf16 v[160:163], v[100:103], v[140:143], v[160:163]\n\t" \
  "ds_read_b128 v[104:107], v57 offset:6144\n\t" \
  "v_cvt_pk_bf16_f32 v44, v44, v48\n\t" \
  "v_mfma_f32_16x16x32_bf16 v[168:171], v[100:103], v[68:71], v[168:171]\n\t" \
  "ds_read_b128 v[112:115], v57 offset:8192\n\t" \
  "v_and_b32 v50, 0xffff0000, v39\n\t" \
  "v_mfma_f32_16x16x32_bf16 v[168:171], v[120:123], v[76:79], v[168:171]\n\t" \
  "ds_read_b128 v[116:119], v56 offset:8192\n\t" \
  "v_and_b32 v51, 0xffff0000, v41\n\t" \
  "v_mfma_f32_16x16x32_bf16 v[176:179], v[120:123], v[80:83], v[176:179]\n\t" \
  "ds_read_b64_tr_b16 v[120:121], v59 offset:34816\n\t" \
  "v_mul_f32 v45, v45, v50\n\t" \
  "v_mfma_f32_16x16x32_bf16 v[176:179], v[100:103], v[84:87], v[176:179]\n\t" \
  "ds_read_b64_tr_b16 v[122:123], v59 offset:35328\n\t" \
  "v_mul_f32 v49, v49, v51\n\t" \
  "v_mfma_f32_16x16x32_bf16 v[156:159], v[92:95], v[136:139], v[156:159]\n\t" \
  "ds_read_b64_tr_b16 v[100:101], v59 offset:38912\n\t" \
  "ds_read_b64_tr_b16 v[38:39], v1 offset:544\n\t" \
  "v_mfma_f32_16x16x32_bf16 v[156:159], v[124:127], v[144:147], v[156:159]\n\t" \
  "ds_read_b64_tr_b16 v[102:103], v59 offset:39424\n\t" \
  "ds_read_b64_tr_b16 v[40:41], v1 offset:32\n\t" \
  "v_mfma_f32_16x16x32_bf16 v[164:167], v[124:127], v[72:75], v[164:167]\n\t" \
  "ds_read_b64_tr_b16 v[136:137], v58 offset:26624\n\t" \
  "v_cvt_pk_bf16_f32 v45, v45, v49\n\t" \
  "v_mfma_f32_16x16x32_bf16 v[164:167], v[92:95], v[140:143], v[164:167]\n\t" \
  "ds_read_b64_tr_b16 v[138:139], v58 offset:27136\n\t" \
  "v_mfma_f32_16x16x32_bf16 v[172:175], v[92:95], v[68:71], v[172:175]\n\t" \
  "ds_read_b64_tr_b16 v[144:145], v58 offset:30720\n\t" \
  "v_mfma_f32_16x16x32_bf16 v[172:175], v[124:127], v[76:79], v[172:175]\n\t" \
  "ds_read_b64_tr_b16 v[146:147], v58 offset:31232\n\t" \
  "v_mfma_f32_16x16x32_bf16 v[180:183], v[124:127], v[80:83], v[180:183]\n\t" \
  "ds_read_b128 v[72:75], v56 offset:0\n\t" \
  "v_mfma_f32_16x16x32_bf16 v[180:183], v[92:95], v[84:87], v[180:183]\n\t" \
  "ds_read_b128 v[140:143], v57 offset:0\n\t" \
  "s_waitcnt lgkmcnt(8)\n\t" \
  "v_mfma_f32_16x16x32_bf16 v[216:219], v[96:99], v[128:131], v[216:219]\n\t" \
  "ds_read_b64_tr_b16 v[68:69], v59 offset:26624\n\t" \
  "v_mfma_f32_16x16x32_bf16 v[216:219], v[132:135], v[88:91], v[216:219]\n\t" \
  "ds_read_b64_tr_b16 v[70:71], v59 offset:27136\n\t" \
  "v_mfma_f32_16x16x32_bf16 v[224:227], v[108:111], v[88:91], v[224:227]\n\t" \
  "ds_read_b64_tr_b16 v[76:77], v59 offset:30720\n\t" \
  "v_mfma_f32_16x16x32_bf16 v[224:227], v[148:151], v[128:131], v[224:227]\n\t" \
  "ds_read_b64_tr_b16 v[78:79], v59 offset:31232\n\t" \
  "v_mfma_f32_16x16x32_bf16 v[232:235], v[64:67], v[128:131], v[232:235]\n\t" \
  "ds_read_b64_tr_b16 v[80:81], v58 offset:18432\n\t" \
  "v_mfma_f32_16x16x32_bf16 v[232:235], v[104:107], v[88:91], v[232:235]\n\t" \
  "ds_read_b64_tr_b16 v[82:83], v58 offset:18944\n\t" \
  "v_mfma_f32_16x16x32_bf16 v[240:243], v[112:115], v[88:91], v[240:243]\n\t" \
  "ds_read_b64_tr_b16 v[124:125], v58 offset:22528\n\t" \
  "v_mfma_f32_16x16x32_bf16 v[240:243], v[116:119], v[128:131], v[240:243]\n\t" \
  "ds_read_b64_tr_b16 v[126:127], v58 offset:23040\n\t" \
  "v_mfma_f32_16x16x32_bf16 v[220:223], v[96:99], v[120:123], v[220:223]\n\t" \
  "ds_read_b64_tr_b16 v[84:85], v59 offset:18432\n\t" \
  "s_waitcnt lgkmcnt(7)\n\t" \
  "v_mfma_f32_16x16x32_bf16 v[220:223], v[132:135], v[100:103], v[220:223]\n\t" \
  "ds_read_b64_tr_b16 v[86:87], v59 offset:18944\n\t" \
  "v_mfma_f32_16x16x32_bf16 v[228:231], v[108:111], v[100:103], v[228:231]\n\t" \
  "ds_read_b64_tr_b16 v[92:93], v59 offset:22528\n\t" \
  "v_mfma_f32_16x16x32_bf16 v[228:231], v[148:151], v[120:123], v[228:231]\n\t" \
  "ds_read_b64_tr_b16 v[94:95], v59 offset:23040\n\t" \
  "v_mfma_f32_16x16x32_bf16 v[236:239], v[64:67], v[120:123], v[236:239]\n\t" \
  "v_mfma_f32_16x16x32_bf16 v[236:239], v[104:107], v[100:103], v[236:239]\n\t" \
  "v_mfma_f32_16x16x32_bf16 v[244:247], v[112:115], v[100:103], v[244:247]\n\t" \
  "v_mfma_f32_16x16x32_bf16 v[244:247], v[116:119], v[120:123], v[244:247]\n\t" \
  "v_mfma_f32_16x16x32_bf16 v[184:187], v[136:139], v[96:99], v[184:187]\n\t" \
  "v_mfma_f32_16x16x32_bf16 v[184:187], v[144:147], v[132:135], v[184:187]\n\t" \
  "v_mfma_f32_16x16x32_bf16 v[192:195], v[144:147], v[108:111], v[192:195]\n\t" \
  "v_mfma_f32_16x16x32_bf16 v[192:195], v[136:139], v[148:151], v[192:195]\n\t" \
  "v_mfma_f32_16x16x32_bf16 v[200:203], v[136:139], v[64:67], v[200:203]\n\t" \
  "global_store_dwordx4 v2, v[42:45], %24 offset:1024 sc1\n\t" \
  "v_add_u32 v2, 0x20000, v2\n\t" \
  "v_mfma_f32_16x16x32_bf16 v[200:203], v[144:147], v[104:107], v[200:203]\n\t" \
  "v_mfma_f32_16x16x32_bf16 v[208:211], v[144:147], v[112:115], v[208:211]\n\t" \
  "v_mfma_f32_16x16x32_bf16 v[42:45], v[26:29], v[18:21], 0\n\t" \
  "v_mfma_f32_16x16x32_bf16 v[42:45], v[30:33], v[22:25], v[42:45]\n\t" \
  "v_mfma_f32_16x16x32_bf16 v[46:49], v[30:33], v[18:21], 0\n\t" \
  "v_mfma_f32_16x16x32_bf16 v[46:49], v[34:37], v[22:25], v[46:49]\n\t" \
  "ds_read_b128 v[26:29], v0 offset:1840\n\t" \
  "ds_read_b128 v[30:33], v0 offset:1328\n\t" \
  "v_mfma_f32_16x16x32_bf16 v[208:211], v[136:139], v[116:119], v[208:211]\n\t" \
  "ds_read_b128 v[34:37], v0 offset:800\n\t" \
  "v_mfma_f32_16x16x32_bf16 v[248:251], v[136:139], v[72:75], v[248:251]\n\t" \
  "v_mfma_f32_16x16x32_bf16 v[248:251], v[144:147], v[140:143], v[248:251]\n\t" \
  "v_lshlrev_b32 v50, 16, v38\n\t" \
  "v_lshlrev_b32 v51, 16, v40\n\t" \
  "v_mfma_f32_16x16x32_bf16 v[252:255], v[68:71], v[72:75], v[252:255]\n\t" \
  "v_mul_f32 v42, v42, v50\n\t" \
  "v_mul_f32 v46, v46, v51\n\t" \
  "s_waitcnt lgkmcnt(3)\n\t" \
  "v_mfma_f32_16x16x32_bf16 v[252:255], v[76:79], v[140:143], v[252:255]\n\t" \
  "v_cvt_pk_bf16_f32 v42, v42, v46\n\t" \
  "v_and_b32 v50, 0xffff0000, v38\n\t" \
  "v_mfma_f32_16x16x32_bf16 v[188:191], v[76:79], v[132:135], v[188:191]\n\t" \
  "v_and_b32 v51, 0xffff0000, v40\n\t" \
  "v_mul_f32 v43, v43, v50\n\t" \
  "v_mfma_f32_16x16x32_bf16 v[188:191], v[68:71], v[96:99], v[188:191]\n\t" \
  "v_mul_f32 v47, v47, v51\n\t" \
  "v_cvt_pk_bf16_f32 v43, v43, v47\n\t" \
  "s_waitcnt lgkmcnt(0)\n\t" \
  "s_barrier\n\t" \
  "v_mfma_f32_16x16x32_bf16 v[196:199], v[68:71], v[148:151], v[196:199]\n\t" \
  "ds_read_b64_tr_b16 v[88:89], v62 offset:34816\n\t" \
  "v_lshlrev_b32 v50, 16, v39\n\t" \
  "v_mfma_f32_16x16x32_bf16 v[196:199], v[76:79], v[108:111], v[196:199]\n\t" \
  "ds_read_b64_tr_b16 v[90:91], v62 offset:35328\n\t" \
  "v_lshlrev_b32 v51, 16, v41\n\t" \
  "v_mfma_f32_16x16x32_bf16 v[204:207], v[76:79], v[104:107], v[204:207]\n\t" \
  "ds_read_b128 v[128:131], v60 offset:2048\n\t" \
  "v_mul_f32 v44, v44, v50\n\t" \
  "v_mfma_f32_16x16x32_bf16 v[204:207], v[68:71], v[64:67], v[204:207]\n\t" \
  "ds_read_b64_tr_b16 v[100:101], v62 offset:38912\n\t" \
  "v_mul_f32 v48, v48, v51\n\t" \
  "v_mfma_f32_16x16x32_bf16 v[212:215], v[68:71], v[116:119], v[212:215]\n\t" \
  "ds_read_b64_tr_b16 v[102:103], v62 offset:39424\n\t" \
  "v_cvt_pk_bf16_f32 v44, v44, v48\n\t" \
  "v_mfma_f32_16x16x32_bf16 v[212:215], v[76:79], v[112:115], v[212:215]\n\t" \
  "ds_read_b128 v[120:123], v61 offset:2048\n\t" \
  "v_and_b32 v50, 0xffff0000, v39\n\t" \
  "v_mfma_f32_16x16x32_bf16 v[152:155], v[80:83], v[96:99], v[152:155]\n\t" \
  "ds_read_b128 v[136:139], v61 offset:4096\n\t" \
  "v_and_b32 v51, 0xffff0000, v41\n\t" \
  "v_mfma_f32_16x16x32_bf16 v[152:155], v[124:127], v[132:135], v[152:155]\n\t" \
  "ds_read_b128 v[144:147], v60 offset:4096\n\t" \
  "v_mul_f32 v45, v45, v50\n\t" \
  "v_mfma_f32_16x16x32_bf16 v[160:163], v[124:127], v[108:111], v[160:163]\n\t" \
  "ds_read_b128 v[72:75], v60 offset:6144\n\t" \
  "v_mul_f32 v49, v49, v51\n\t" \
  "v_mfma_f32_16x16x32_bf16 v[160:163], v[80:83], v[148:151], v[160:163]\n\t" \
  "ds_read_b128 v[140:143], v61 offset:6144\n\t" \
  "ds_read_b64_tr_b16 v[38:39], v1 offset:800\n\t" \
  "v_mfma_f32_16x16x32_bf16 v[168:171], v[80:83], v[64:67], v[168:171]\n\t" \
  "ds_read_b128 v[68:71], v61 offset:8192\n\t" \
  "ds_read_b64_tr_b16 v[40:41], v1 offset:288\n\t" \
  "v_mfma_f32_16x16x32_bf16 v[168:171], v[124:127], v[104:107], v[168:171]\n\t" \
  "ds_read_b128 v[76:79], v60 offset:8192\n\t" \
  "v_cvt_pk_bf16_f32 v45, v45, v49\n\t" \
  "v_mfma_f32_16x16x32_bf16 v[176:179], v[124:127], v[112:115], v[176:179]\n\t" \
  "ds_read_b64_tr_b16 v[124:125], v63 offset:34816\n\t" \
  "v_mfma_f32_16x16x32_bf16 v[176:179], v[80:83], v[116:119], v[176:179]\n\t" \
  "ds_read_b64_tr_b16 v[126:127], v63 offset:35328\n\t" \
  "v_mfma_f32_16x16x32_bf16 v[156:159], v[84:87], v[96:99], v[156:159]\n\t" \
  "ds_read_b64_tr_b16 v[80:81], v63 offset:38912\n\t" \
  "v_mfma_f32_16x16x32_bf16 v[156:159], v[92:95], v[132:135], v[156:159]\n\t" \
  "ds_read_b64_tr_b16 v[82:83], v63 offset:39424\n\t" \
  "v_mfma_f32_16x16x32_bf16 v[164:167], v[92:95], v[108:111], v[164:167]\n\t" \
  "ds_read_b64_tr_b16 v[96:97], v62 offset:26624\n\t" \
  "v_mfma_f32_16x16x32_bf16 v[164:167], v[84:87], v[148:151], v[164:167]\n\t" \
  "ds_read_b64_tr_b16 v[98:99], v62 offset:27136\n\t" \
  "v_mfma_f32_16x16x32_bf16 v[172:175], v[84:87], v[64:67], v[172:175]\n\t" \
  "ds_read_b64_tr_b16 v[132:133], v62 offset:30720\n\t" \
  "v_mfma_f32_16x16x32_bf16 v[172:175], v[92:95], v[104:107], v[172:175]\n\t" \
  "ds_read_b64_tr_b16 v[134:135], v62 offset:31232\n\t" \
  "v_mfma_f32_16x16x32_bf16 v[180:183], v[92:95], v[112:115], v[180:183]\n\t" \
  "ds_read_b128 v[108:111], v60 offset:0\n\t" \
  "v_mfma_f32_16x16x32_bf16 v[180:183], v[84:87], v[116:119], v[180:183]\n\t" \
  "ds_read_b128 v[148:151], v61 offset:0\n\t" \
  "s_waitcnt lgkmcnt(7)\n\t" \
  "v_mfma_f32_16x16x32_bf16 v[216:219], v[128:131], v[88:91], v[216:219]\n\t" \
  "ds_read_b64_tr_b16 v[64:65], v63 offset:26624\n\t" \
  "v_mfma_f32_16x16x32_bf16 v[216:219], v[120:123], v[100:103], v[216:219]\n\t" \
  "ds_read_b64_tr_b16 v[66:67], v63 offset:27136\n\t" \
  "v_mfma_f32_16x16x32_bf16 v[224:227], v[136:139], v[100:103], v[224:227]\n\t" \
  "ds_read_b64_tr_b16 v[104:105], v63 offset:30720\n\t" \
  "v_mfma_f32_16x16x32_bf16 v[224:227], v[144:147], v[88:91], v[224:227]\n\t" \
  "ds_read_b64_tr_b16 v[106:107], v63 offset:31232\n\t" \
  "v_mfma_f32_16x16x32_bf16 v[232:235], v[72:75], v[88:91], v[232:235]\n\t" \
  "ds_read_b64_tr_b16 v[92:93], v62 offset:18432\n\t" \
  "v_mfma_f32_16x16x32_bf16 v[232:235], v[140:143], v[100:103], v[232:235]\n\t" \
  "ds_read_b64_tr_b16 v[94:95], v62 offset:18944\n\t" \
  "v_mfma_f32_16x16x32_bf16 v[240:243], v[68:71], v[100:103], v[240:243]\n\t" \
  "ds_read_b64_tr_b16 v[112:113], v62 offset:22528\n\t" \
  "v_mfma_f32_16x16x32_bf16 v[240:243], v[76:79], v[88:91], v[240:243]\n\t" \
  "ds_read_b64_tr_b16 v[114:115], v62 offset:23040\n\t" \
  "v_mfma_f32_16x16x32_bf16 v[220:223], v[128:131], v[124:127], v[220:223]\n\t" \
  "ds_read_b64_tr_b16 v[84:85], v63 offset:18432\n\t" \
  "s_waitcnt lgkmcnt(7)\n\t" \
  "v_mfma_f32_16x16x32_bf16 v[220:223], v[120:123], v[80:83], v[220:223]\n\t" \
  "ds_read_b64_tr_b16 v[86:87], v63 offset:18944\n\t" \
  "v_mfma_f32_16x16x32_bf16 v[228:231], v[136:139], v[80:83], v[228:231]\n\t" \
  "ds_read_b64_tr_b16 v[116:117], v63 offset:22528\n\t" \
  "v_mfma_f32_16x16x32_bf16 v[228:231], v[144:147], v[124:127], v[228:231]\n\t" \
  "ds_read_b64_tr_b16 v[118:119], v63 offset:23040\n\t" \
  "v_mfma_f32_16x16x32_bf16 v[236:239], v[72:75], v[124:127], v[236:239]\n\t" \
  "v_mfma_f32_16x16x32_bf16 v[236:239], v[140:143], v[80:83], v[236:239]\n\t" \
  "v_mfma_f32_16x16x32_bf16 v[244:247], v[68:71], v[80:83], v[244:247]\n\t" \
  "v_mfma_f32_16x16x32_bf16 v[244:247], v[76:79], v[124:127], v[244:247]\n\t" \
  "v_mfma_f32_16x16x32_bf16 v[184:187], v[96:99], v[128:131], v[184:187]\n\t" \
  "v_mfma_f32_16x16x32_bf16 v[184:187], v[132:135], v[120:123], v[184:187]\n\t" \
  "v_mfma_f32_16x16x32_bf16 v[192:195], v[132:135], v[136:139], v[192:195]\n\t" \
  "v_mfma_f32_16x16x32_bf16 v[192:195], v[96:99], v[144:147], v[192:195]\n\t" \
  "v_mfma_f32_16x16x32_bf16 v[200:203], v[96:99], v[72:75], v[200:203]\n\t" \
  "global_store_dwordx4 v2, v[42:45], %24 offset:0 sc1\n\t" \
  "v_mfma_f32_16x16x32_bf16 v[200:203], v[132:135], v[140:143], v[200:203]\n\t" \
  "s_nop 0\n\t" \
  "v_mfma_f32_16x16x32_bf16 v[42:45], v[26:29], v[18:21], 0\n\t" \
  "v_mfma_f32_16x16x32_bf16 v[42:45], v[30:33], v[22:25], v[42:45]\n\t" \
  "v_mfma_f32_16x16x32_bf16 v[46:49], v[30:33], v[18:21], 0\n\t" \
  "v_mfma_f32_16x16x32_bf16 v[46:49], v[34:37], v[22:25], v[46:49]\n\t" \
  "v_mfma_f32_16x16x32_bf16 v[208:211], v[132:135], v[68:71], v[208:211]\n\t" \
  "v_mfma_f32_16x16x32_bf16 v[208:211], v[96:99], v[76:79], v[208:211]\n\t" \
  "v_lshlrev_b32 v50, 16, v38\n\t" \
  "v_lshlrev_b32 v51, 16, v40\n\t" \
  "v_mfma_f32_16x16x32_bf16 v[248:251], v[96:99], v[108:111], v[248:251]\n\t" \
  "s_nop 0\n\t" \
  "v_mul_f32 v42, v42, v50\n\t" \
  "s_nop 0\n\t" \
  "v_mul_f32 v46, v46, v51\n\t" \
  "v_mfma_f32_16x16x32_bf16 v[248:251], v[132:135], v[148:151], v[248:251]\n\t" \
  "v_cvt_pk_bf16_f32 v42, v42, v46\n\t" \
  "v_and_b32 v50, 0xffff0000, v38\n\t" \
  "v_mfma_f32_16x16x32_bf16 v[252:255], v[64:67], v[108:111], v[252:255]\n\t" \
  "v_and_b32 v51, 0xffff0000, v40\n\t" \
  "v_mul_f32 v43, v43, v50\n\t" \
  "s_waitcnt lgkmcnt(0)\n\t" \
  "v_mfma_f32_16x16x32_bf16 v[252:255], v[104:107], v[148:151], v[252:255]\n\t" \
  "v_mul_f32 v47, v47, v51\n\t" \
  "v_cvt_pk_bf16_f32 v43, v43, v47\n\t" \
  "v_mfma_f32_16x16x32_bf16 v[188:191], v[104:107], v[120:123], v[188:191]\n\t" \
  "v_lshlrev_b32 v50, 16, v39\n\t" \
  "v_lshlrev_b32 v51, 16, v41\n\t" \
  "v_mfma_f32_16x16x32_bf16 v[188:191], v[64:67], v[128:131], v[188:191]\n\t" \
  "v_mul_f32 v44, v44, v50\n\t" \
  "v_mul_f32 v48, v48, v51\n\t" \
  "s_barrier\n\t" \
  "v_mfma_f32_16x16x32_bf16 v[196:199], v[64:67], v[144:147], v[196:199]\n\t" \
  "ds_read_b64_tr_b16 v[100:101], v54 offset:34816\n\t" \
  "v_cvt_pk_bf16_f32 v44, v44, v48\n\t" \
  "v_mfma_f32_16x16x32_bf16 v[196:199], v[104:107], v[136:139], v[196:199]\n\t" \
  "ds_read_b64_tr_b16 v[102:103], v54 offset:35328\n\t" \
  "v_and_b32 v50, 0xffff0000, v39\n\t" \
  "v_mfma_f32_16x16x32_bf16 v[204:207], v[104:107], v[140:143], v[204:207]\n\t" \
  "ds_read_b128 v[88:91], v52 offset:2048\n\t" \
  "v_and_b32 v51, 0xffff0000, v41\n\t" \
  "v_mfma_f32_16x16x32_bf16 v[204:207], v[64:67], v[72:75], v[204:207]\n\t" \
  "ds_read_b64_tr_b16 v[80:81], v54 offset:38912\n\t" \
  "v_mul_f32 v45, v45, v50\n\t" \
  "v_mfma_f32_16x16x32_bf16 v[212:215], v[64:67], v[76:79], v[212:215]\n\t" \
  "ds_read_b64_tr_b16 v[82:83], v54 offset:39424\n\t" \
  "v_mul_f32 v49, v49, v51\n\t" \
  "v_mfma_f32_16x16x32_bf16 v[212:215], v[104:107], v[68:71], v[212:215]\n\t" \
  "ds_read_b128 v[124:127], v53 offset:2048\n\t" \
  "v_cvt_pk_bf16_f32 v45, v45, v49\n\t" \
  "v_mfma_f32_16x16x32_bf16 v[152:155], v[92:95], v[128:131], v[152:155]\n\t" \
  "ds_read_b128 v[96:99], v53 offset:4096\n\t" \
  "v_mfma_f32_16x16x32_bf16 v[152:155], v[112:115], v[120:123], v[152:155]\n\t" \
  "ds_read_b128 v[132:135], v52 offset:4096\n\t" \
  "v_mfma_f32_16x16x32_bf16 v[160:163], v[112:115], v[136:139], v[160:163]\n\t" \
  "ds_read_b128 v[108:111], v52 offset:6144\n\t" \
  "504:\n\t" \
  "s_cbranch_execz 508f\n\t" \
  "v_mfma_f32_16x16x32_bf16 v[160:163], v[92:95], v[144:147], v[160:163]\n\t" \
  "ds_read_b128 v[148:151], v53 offset:6144\n\t" \
  "v_mfma_f32_16x16x32_bf16 v[168:171], v[92:95], v[72:75], v[168:171]\n\t" \
  "ds_read_b128 v[64:67], v53 offset:8192\n\t" \
  "v_mfma_f32_16x16x32_bf16 v[168:171], v[112:115], v[140:143], v[168:171]\n\t" \
  "ds_read_b128 v[104:107], v52 offset:8192\n\t" \
  "v_mfma_f32_16x16x32_bf16 v[176:179], v[112:115], v[68:71], v[176:179]\n\t" \
  "ds_read_b64_tr_b16 v[112:113], v55 offset:34816\n\t" \
  "v_mfma_f32_16x16x32_bf16 v[176:179], v[92:95], v[76:79], v[176:179]\n\t" \
  "ds_read_b64_tr_b16 v[114:115], v55 offset:35328\n\t" \
  "v_mfma_f32_16x16x32_bf16 v[156:159], v[84:87], v[128:131], v[156:159]\n\t" \
  "ds_read_b64_tr_b16 v[92:93], v55 offset:38912\n\t" \
  "v_mfma_f32_16x16x32_bf16 v[156:159], v[116:119], v[120:123], v[156:159]\n\t" \
  "ds_read_b64_tr_b16 v[94:95], v55 offset:39424\n\t" \
  "v_mfma_f32_16x16x32_bf16 v[164:167], v[116:119], v[136:139], v[164:167]\n\t" \
  "ds_read_b64_tr_b16 v[128:129], v54 offset:26624\n\t" \
  "v_mfma_f32_16x16x32_bf16 v[164:167], v[84:87], v[144:147], v[164:167]\n\t" \
  "ds_read_b64_tr_b16 v[130:131], v54 offset:27136\n\t" \
  "v_mfma_f32_16x16x32_bf16 v[172:175], v[84:87], v[72:75], v[172:175]\n\t" \
  "ds_read_b64_tr_b16 v[120:121], v54 offset:30720\n\t" \
  "v_mfma_f32_16x16x32_bf16 v[172:175], v[116:119], v[140:143], v[172:175]\n\t" \
  "ds_read_b64_tr_b16 v[122:123], v54 offset:31232\n\t" \
  "v_mfma_f32_16x16x32_bf16 v[180:183], v[116:119], v[68:71], v[180:183]\n\t" \
  "ds_read_b128 v[136:139], v52 offset:0\n\t" \
  "v_mfma_f32_16x16x32_bf16 v[180:183], v[84:87], v[76:79], v[180:183]\n\t" \
  "ds_read_b128 v[144:147], v53 offset:0\n\t" \
  "s_waitcnt lgkmcnt(7)\n\t" \
  "v_mfma_f32_16x16x32_bf16 v[216:219], v[88:91], v[100:103], v[216:219]\n\t" \
  "ds_read_b64_tr_b16 v[72:73], v55 offset:26624\n\t" \
  "v_mfma_f32_16x16x32_bf16 v[216:219], v[124:127], v[80:83], v[216:219]\n\t" \
  "ds_read_b64_tr_b16 v[74:75], v55 offset:27136\n\t" \
  "v_mfma_f32_16x16x32_bf16 v[224:227], v[96:99], v[80:83], v[224:227]\n\t" \
  "ds_read_b64_tr_b16 v[140:141], v55 offset:30720\n\t" \
  "v_mfma_f32_16x16x32_bf16 v[224:227], v[132:135], v[100:103], v[224:227]\n\t" \
  "ds_read_b64_tr_b16 v[142:143], v55 offset:31232\n\t" \
  "v_mfma_f32_16x16x32_bf16 v[232:235], v[108:111], v[100:103], v[232:235]\n\t" \
  "ds_read_b64_tr_b16 v[68:69], v54 offset:18432\n\t" \
  "v_mfma_f32_16x16x32_bf16 v[232:235], v[148:151], v[80:83], v[232:235]\n\t" \
  "ds_read_b64_tr_b16 v[70:71], v54 offset:18944\n\t" \
  "v_mfma_f32_16x16x32_bf16 v[240:243], v[64:67], v[80:83], v[240:243]\n\t" \
  "ds_read_b64_tr_b16 v[116:117], v54 offset:22528\n\t" \
  "v_mfma_f32_16x16x32_bf16 v[240:243], v[104:107], v[100:103], v[240:243]\n\t" \
  "ds_read_b64_tr_b16 v[118:119], v54 offset:23040\n\t" \
  "v_mfma_f32_16x16x32_bf16 v[220:223], v[88:91], v[112:115], v[220:223]\n\t" \
  "ds_read_b64_tr_b16 v[76:77], v55 offset:18432\n\t" \
  "s_waitcnt lgkmcnt(7)\n\t" \
  "v_mfma_f32_16x16x32_bf16 v[220:223], v[124:127], v[92:95], v[220:223]\n\t" \
  "ds_read_b64_tr_b16 v[78:79], v55 offset:18944\n\t" \
  "v_mfma_f32_16x16x32_bf16 v[228:231], v[96:99], v[92:95], v[228:231]\n\t" \
  "ds_read_b64_tr_b16 v[84:85], v55 offset:22528\n\t" \
  "v_mfma_f32_16x16x32_bf16 v[228:231], v[132:135], v[112:115], v[228:231]\n\t" \
  "ds_read_b64_tr_b16 v[86:87], v55 offset:23040\n\t" \
  "v_mfma_f32_16x16x32_bf16 v[236:239], v[108:111], v[112:115], v[236:239]\n\t" \
  "v_mfma_f32_16x16x32_bf16 v[236:239], v[148:151], v[92:95], v[236:239]\n\t" \
  "v_mfma_f32_16x16x32_bf16 v[244:247], v[64:67], v[92:95], v[244:247]\n\t" \
  "v_mfma_f32_16x16x32_bf16 v[244:247], v[104:107], v[112:115], v[244:247]\n\t" \
  "v_mfma_f32_16x16x32_bf16 v[184:187], v[128:131], v[88:91], v[184:187]\n\t" \
  "v_mfma_f32_16x16x32_bf16 v[184:187], v[120:123], v[124:127], v[184:187]\n\t" \
  "v_mfma_f32_16x16x32_bf16 v[192:195], v[120:123], v[96:99], v[192:195]\n\t" \
  "v_mfma_f32_16x16x32_bf16 v[192:195], v[128:131], v[132:135], v[192:195]\n\t" \
  "v_mfma_f32_16x16x32_bf16 v[200:203], v[128:131], v[108:111], v[200:203]\n\t" \
  "global_store_dwordx4 v2, v[42:45], %24 offset:1024 sc1\n\t" \
  "v_mfma_f32_16x16x32_bf16 v[200:203], v[120:123], v[148:151], v[200:203]\n\t" \
  "v_mfma_f32_16x16x32_bf16 v[208:211], v[120:123], v[64:67], v[208:211]\n\t" \
  "v_mfma_f32_16x16x32_bf16 v[208:211], v[128:131], v[104:107], v[208:211]\n\t" \
  "v_mov_b32 v30, 0xff61b1e6\n\t" \
  "v_bfe_i32 v34, v3, 0, 1\n\t" \
  "v_mfma_f32_16x16x32_bf16 v[248:251], v[128:131], v[136:139], v[248:251]\n\t" \
  "v_and_b32 v34, v30, v34\n\t" \
  "v_bfe_i32 v38, v3, 4, 1\n\t" \
  "v_mfma_f32_16x16x32_bf16 v[248:251], v[120:123], v[144:147], v[248:251]\n\t" \
  "v_and_b32 v38, v30, v38\n\t" \
  "v_bfe_i32 v42, v3, 8, 1\n\t" \
  "v_mfma_f32_16x16x32_bf16 v[252:255], v[72:75], v[136:139], v[252:255]\n\t" \
  "v_bfi_b32 v42, v42, v30, %15\n\t" \
  "v_mov_b32 v48, 0x3f803f80\n\t" \
  "s_waitcnt lgkmcnt(0)\n\t" \
  "v_mfma_f32_16x16x32_bf16 v[252:255], v[140:143], v[144:147], v[252:255]\n\t" \
  "v_bfe_i32 v35, v3, 1, 1\n\t" \
  "v_and_b32 v35, v30, v35\n\t" \
  "v_mfma_f32_16x16x32_bf16 v[188:191], v[140:143], v[124:127], v[188:191]\n\t" \
  "v_bfe_i32 v39, v3, 5, 1\n\t" \
  "v_and_b32 v39, v30, v39\n\t" \
  "v_mfma_f32_16x16x32_bf16 v[188:191], v[72:75], v[88:91], v[188:191]\n\t" \
  "v_bfe_i32 v43, v3, 9, 1\n\t" \
  "v_bfi_b32 v43, v43, v30, %15\n\t" \
  "s_barrier\n\t" \
  "v_mfma_f32_16x16x32_bf16 v[196:199], v[72:75], v[132:135], v[196:199]\n\t" \
  "ds_read_b64_tr_b16 v[80:81], v58 offset:34816\n\t" \
  "v_mov_b32 v49, 0x3f803f80\n\t" \
  "v_mfma_f32_16x16x32_bf16 v[196:199], v[140:143], v[96:99], v[196:199]\n\t" \
  "ds_read_b64_tr_b16 v[82:83], v58 offset:35328\n\t" \
  "v_bfe_i32 v36, v3, 2, 1\n\t" \
  "v_mfma_f32_16x16x32_bf16 v[204:207], v[140:143], v[148:151], v[204:207]\n\t" \
  "ds_read_b128 v[100:103], v56 offset:2048\n\t" \
  "v_and_b32 v36, v30, v36\n\t" \
  "v_mfma_f32_16x16x32_bf16 v[204:207], v[72:75], v[108:111], v[204:207]\n\t" \
  "ds_read_b64_tr_b16 v[92:93], v58 offset:38912\n\t" \
  "v_bfe_i32 v40, v3, 6, 1\n\t" \
  "v_mfma_f32_16x16x32_bf16 v[212:215], v[72:75], v[104:107], v[212:215]\n\t" \
  "ds_read_b64_tr_b16 v[94:95], v58 offset:39424\n\t" \
  "v_and_b32 v40, v30, v40\n\t" \
  "v_mfma_f32_16x16x32_bf16 v[212:215], v[140:143], v[64:67], v[212:215]\n\t" \
  "ds_read_b128 v[112:115], v57 offset:2048\n\t" \
  "v_bfe_i32 v44, v3, 10, 1\n\t" \
  "v_mfma_f32_16x16x32_bf16 v[152:155], v[68:71], v[88:91], v[152:155]\n\t" \
  "ds_read_b128 v[128:131], v57 offset:4096\n\t" \
  "v_bfi_b32 v44, v44, v30, %15\n\t" \
  "v_mfma_f32_16x16x32_bf16 v[152:155], v[116:119], v[124:127], v[152:155]\n\t" \
  "ds_read_b128 v[120:123], v56 offset:4096\n\t" \
  "v_mov_b32 v50, 0x3f803f80\n\t" \
  "v_mfma_f32_16x16x32_bf16 v[160:163], v[116:119], v[96:99], v[160:163]\n\t" \
  "ds_read_b128 v[136:139], v56 offset:6144\n\t" \
  "v_bfe_i32 v37, v3, 3, 1\n\t" \
  "v_mfma_f32_16x16x32_bf16 v[160:163], v[68:71], v[132:135], v[160:163]\n\t" \
  "ds_read_b128 v[144:147], v57 offset:6144\n\t" \
  "v_and_b32 v37, v30, v37\n\t" \
  "v_mfma_f32_16x16x32_bf16 v[168:171], v[68:71], v[108:111], v[168:171]\n\t" \
  "ds_read_b128 v[72:75], v57 offset:8192\n\t" \
  "v_bfe_i32 v41, v3, 7, 1\n\t" \
  "v_mfma_f32_16x16x32_bf16 v[168:171], v[116:119], v[148:151], v[168:171]\n\t" \
  "ds_read_b128 v[140:143], v56 offset:8192\n\t" \
  "v_and_b32 v41, v30, v41\n\t" \
  "v_mfma_f32_16x16x32_bf16 v[176:179], v[116:119], v[64:67], v[176:179]\n\t" \
  "ds_read_b64_tr_b16 v[116:117], v59 offset:34816\n\t" \
  "v_bfe_i32 v45, v3, 11, 1\n\t" \
  "v_mfma_f32_16x16x32_bf16 v[176:179], v[68:71], v[104:107], v[176:179]\n\t" \
  "ds_read_b64_tr_b16 v[118:119], v59 offset:35328\n\t" \
  "v_bfi_b32 v45, v45, v30, %15\n\t" \
  "v_mfma_f32_16x16x32_bf16 v[156:159], v[76:79], v[88:91], v[156:159]\n\t" \
  "ds_read_b64_tr_b16 v[68:69], v59 offset:38912\n\t" \
  "v_mov_b32 v51, 0x3f803f80\n\t" \
  "v_mfma_f32_16x16x32_bf16 v[156:159], v[84:87], v[124:127], v[156:159]\n\t" \
  "ds_read_b64_tr_b16 v[70:71], v59 offset:39424\n\t" \
  "v_bfe_i32 v46, v3, 12, 1\n\t" \
  "v_mfma_f32_16x16x32_bf16 v[164:167], v[84:87], v[96:99], v[164:167]\n\t" \
  "ds_read_b64_tr_b16 v[88:89], v58 offset:26624\n\t" \
  "v_bfe_i32 v31, v3, 13, 1\n\t" \
  "v_mfma_f32_16x16x32_bf16 v[164:167], v[76:79], v[132:135], v[164:167]\n\t" \
  "ds_read_b64_tr_b16 v[90:91], v58 offset:27136\n\t" \
  "v_perm_b32 v46, v31, v46, vcc_hi\n\t" \
  "v_mfma_f32_16x16x32_bf16 v[172:175], v[76:79], v[108:111], v[172:175]\n\t" \
  "ds_read_b64_tr_b16 v[124:125], v58 offset:30720\n\t" \
  "v_bfe_i32 v47, v3, 14, 1\n\t" \
  "v_mfma_f32_16x16x32_bf16 v[172:175], v[84:87], v[148:151], v[172:175]\n\t" \
  "ds_read_b64_tr_b16 v[126:127], v58 offset:31232\n\t" \
  "v_bfe_i32 v31, v3, 15, 1\n\t" \
  "v_mfma_f32_16x16x32_bf16 v[180:183], v[84:87], v[64:67], v[180:183]\n\t" \
  "ds_read_b128 v[96:99], v56 offset:0\n\t" \
  "v_perm_b32 v47, v31, v47, vcc_hi\n\t" \
  "v_mfma_f32_16x16x32_bf16 v[180:183], v[76:79], v[104:107], v[180:183]\n\t" \
  "ds_read_b128 v[132:135], v57 offset:0\n\t" \
  "v_add_u32 v30, 18432, v4\n\t" \
  "s_waitcnt lgkmcnt(7)\n\t" \
  "v_mfma_f32_16x16x32_bf16 v[216:219], v[100:103], v[80:83], v[216:219]\n\t" \
  "ds_read_b64_tr_b16 v[108:109], v59 offset:26624\n\t" \
  "v_xor_b32 v31, 1024, v30\n\t" \
  "v_mfma_f32_16x16x32_bf16 v[216:219], v[112:115], v[92:95], v[216:219]\n\t" \
  "ds_read_b64_tr_b16 v[110:111], v59 offset:27136\n\t" \
  "v_mov_b32 v32, %12\n\t" \
  "v_mfma_f32_16x16x32_bf16 v[224:227], v[128:131], v[92:95], v[224:227]\n\t" \
  "ds_read_b64_tr_b16 v[148:149], v59 offset:30720\n\t" \
  "v_xor_b32 v33, 4, v32\n\t" \
  "v_mfma_f32_16x16x32_bf16 v[224:227], v[120:123], v[80:83], v[224:227]\n\t" \
  "ds_read_b64_tr_b16 v[150:151], v59 offset:31232\n\t" \
  "v_mov_b32 v0, 1\n\t" \
  "v_mfma_f32_16x16x32_bf16 v[232:235], v[136:139], v[80:83], v[232:235]\n\t" \
  "ds_read_b64_tr_b16 v[64:65], v58 offset:18432\n\t" \
  "v_and_b32 v4, 1023, v4\n\t" \
  "v_mfma_f32_16x16x32_bf16 v[232:235], v[144:147], v[92:95], v[232:235]\n\t" \
  "ds_read_b64_tr_b16 v[66:67], v58 offset:18944\n\t" \
  "v_add_u32 v1, 16384, v4\n\t" \
  "v_mfma_f32_16x16x32_bf16 v[240:243], v[72:75], v[92:95], v[240:243]\n\t" \
  "ds_read_b64_tr_b16 v[84:85], v58 offset:22528\n\t" \
  "v_mfma_f32_16x16x32_bf16 v[240:243], v[140:143], v[80:83], v[240:243]\n\t" \
  "ds_read_b64_tr_b16 v[86:87], v58 offset:23040\n\t" \
  "v_mfma_f32_16x16x32_bf16 v[220:223], v[100:103], v[116:119], v[220:223]\n\t" \
  "ds_read_b64_tr_b16 v[76:77], v59 offset:18432\n\t" \
  "s_waitcnt lgkmcnt(7)\n\t" \
  "v_mfma_f32_16x16x32_bf16 v[220:223], v[112:115], v[68:71], v[220:223]\n\t" \
  "ds_read_b64_tr_b16 v[78:79], v59 offset:18944\n\t" \
  "v_mfma_f32_16x16x32_bf16 v[228:231], v[128:131], v[68:71], v[228:231]\n\t" \
  "ds_read_b64_tr_b16 v[104:105], v59 offset:22528\n\t" \
  "v_mfma_f32_16x16x32_bf16 v[228:231], v[120:123], v[116:119], v[228:231]\n\t" \
  "ds_read_b64_tr_b16 v[106:107], v59 offset:23040\n\t" \
  "v_mfma_f32_16x16x32_bf16 v[236:239], v[136:139], v[116:119], v[236:239]\n\t" \
  "v_mfma_f32_16x16x32_bf16 v[236:239], v[144:147], v[68:71], v[236:239]\n\t" \
  "v_mfma_f32_16x16x32_bf16 v[244:247], v[72:75], v[68:71], v[244:247]\n\t" \
  "v_mfma_f32_16x16x32_bf16 v[244:247], v[140:143], v[116:119], v[244:247]\n\t" \
  "v_mfma_f32_16x16x32_bf16 v[184:187], v[88:91], v[100:103], v[184:187]\n\t" \
  "v_mfma_f32_16x16x32_bf16 v[184:187], v[124:127], v[112:115], v[184:187]\n\t" \
  "v_mfma_f32_16x16x32_bf16 v[192:195], v[124:127], v[128:131], v[192:195]\n\t" \
  "v_mfma_f32_16x16x32_bf16 v[192:195], v[88:91], v[120:123], v[192:195]\n\t" \
  "v_mfma_f32_16x16x32_bf16 v[200:203], v[88:91], v[136:139], v[200:203]\n\t" \
  "v_mfma_f32_16x16x32_bf16 v[200:203], v[124:127], v[144:147], v[200:203]\n\t" \
  "v_mfma_f32_16x16x32_bf16 v[208:211], v[124:127], v[72:75], v[208:211]\n\t" \
  "v_mfma_f32_16x16x32_bf16 v[208:211], v[88:91], v[140:143], v[208:211]\n\t" \
  "v_mfma_f32_16x16x32_bf16 v[248:251], v[88:91], v[96:99], v[248:251]\n\t" \
  "v_mfma_f32_16x16x32_bf16 v[248:251], v[124:127], v[132:135], v[248:251]\n\t" \
  "v_mfma_f32_16x16x32_bf16 v[252:255], v[108:111], v[96:99], v[252:255]\n\t" \
  "s_waitcnt lgkmcnt(0)\n\t" \
  "v_mfma_f32_16x16x32_bf16 v[252:255], v[148:151], v[132:135], v[252:255]\n\t" \
  "v_mfma_f32_16x16x32_bf16 v[188:191], v[148:151], v[112:115], v[188:191]\n\t" \
  "v_mfma_f32_16x16x32_bf16 v[188:191], v[108:111], v[100:103], v[188:191]\n\t" \
  "s_barrier\n\t" \
  "v_mfma_f32_16x16x32_bf16 v[196:199], v[108:111], v[120:123], v[196:199]\n\t" \
  "ds_read_b64_tr_b16 v[92:93], v62 offset:34816\n\t" \
  "v_mfma_f32_16x16x32_bf16 v[196:199], v[148:151], v[128:131], v[196:199]\n\t" \
  "ds_read_b64_tr_b16 v[94:95], v62 offset:35328\n\t" \
  "v_mfma_f32_16x16x32_bf16 v[204:207], v[148:151], v[144:147], v[204:207]\n\t" \
  "ds_read_b128 v[80:83], v60 offset:2048\n\t" \
  "v_mfma_f32_16x16x32_bf16 v[204:207], v[108:111], v[136:139], v[204:207]\n\t" \
  "ds_read_b64_tr_b16 v[68:69], v62 offset:38912\n\t" \
  "v_mfma_f32_16x16x32_bf16 v[212:215], v[108:111], v[140:143], v[212:215]\n\t" \
  "ds_read_b64_tr_b16 v[70:71], v62 offset:39424\n\t" \
  "v_mfma_f32_16x16x32_bf16 v[212:215], v[148:151], v[72:75], v[212:215]\n\t" \
  "ds_read_b128 v[116:119], v61 offset:2048\n\t" \
  "v_mfma_f32_16x16x32_bf16 v[152:155], v[64:67], v[100:103], v[152:155]\n\t" \
  "ds_read_b128 v[88:91], v61 offset:4096\n\t" \
  "v_mfma_f32_16x16x32_bf16 v[152:155], v[84:87], v[112:115], v[152:155]\n\t" \
  "ds_read_b128 v[124:127], v60 offset:4096\n\t" \
  "v_mfma_f32_16x16x32_bf16 v[160:163], v[84:87], v[128:131], v[160:163]\n\t" \
  "ds_read_b128 v[96:99], v60 offset:6144\n\t" \
  "v_mfma_f32_16x16x32_bf16 v[160:163], v[64:67], v[120:123], v[160:163]\n\t" \
  "ds_read_b128 v[132:135], v61 offset:6144\n\t" \
  "v_mfma_f32_16x16x32_bf16 v[168:171], v[64:67], v[136:139], v[168:171]\n\t" \
  "ds_read_b128 v[108:111], v61 offset:8192\n\t" \
  "v_mfma_f32_16x16x32_bf16 v[168:171], v[84:87], v[144:147], v[168:171]\n\t" \
  "ds_read_b128 v[148:151], v60 offset:8192\n\t" \
  "v_mfma_f32_16x16x32_bf16 v[176:179], v[84:87], v[72:75], v[176:179]\n\t" \
  "ds_read_b64_tr_b16 v[84:85], v63 offset:34816\n\t" \
  "v_mfma_f32_16x16x32_bf16 v[176:179], v[64:67], v[140:143], v[176:179]\n\t" \
  "ds_read_b64_tr_b16 v[86:87], v63 offset:35328\n\t" \
  "v_mfma_f32_16x16x32_bf16 v[156:159], v[76:79], v[100:103], v[156:159]\n\t" \
  "ds_read_b64_tr_b16 v[64:65], v63 offset:38912\n\t" \
  "v_mfma_f32_16x16x32_bf16 v[156:159], v[104:107], v[112:115], v[156:159]\n\t" \
  "ds_read_b64_tr_b16 v[66:67], v63 offset:39424\n\t" \
  "v_mfma_f32_16x16x32_bf16 v[164:167], v[104:107], v[128:131], v[164:167]\n\t" \
  "ds_read_b64_tr_b16 v[100:101], v62 offset:26624\n\t" \
  "v_mfma_f32_16x16x32_bf16 v[164:167], v[76:79], v[120:123], v[164:167]\n\t" \
  "ds_read_b64_tr_b16 v[102:103], v62 offset:27136\n\t" \
  "v_mfma_f32_16x16x32_bf16 v[172:175], v[76:79], v[136:139], v[172:175]\n\t" \
  "ds_read_b64_tr_b16 v[112:113], v62 offset:30720\n\t" \
  "v_mfma_f32_16x16x32_bf16 v[172:175], v[104:107], v[144:147], v[172:175]\n\t" \
  "ds_read_b64_tr_b16 v[114:115], v62 offset:31232\n\t" \
  "v_mfma_f32_16x16x32_bf16 v[180:183], v[104:107], v[72:75], v[180:183]\n\t" \
  "ds_read_b128 v[128:131], v60 offset:0\n\t" \
  "v_mfma_f32_16x16x32_bf16 v[180:183], v[76:79], v[140:143], v[180:183]\n\t" \
  "ds_read_b128 v[120:123], v61 offset:0\n\t" \
  "s_waitcnt lgkmcnt(7)\n\t" \
  "v_mfma_f32_16x16x32_bf16 v[216:219], v[80:83], v[92:95], v[216:219]\n\t" \
  "ds_read_b64_tr_b16 v[136:137], v63 offset:26624\n\t" \
  "v_mfma_f32_16x16x32_bf16 v[216:219], v[116:119], v[68:71], v[216:219]\n\t" \
  "ds_read_b64_tr_b16 v[138:139], v63 offset:27136\n\t" \
  "v_mfma_f32_16x16x32_bf16 v[224:227], v[88:91], v[68:71], v[224:227]\n\t" \
  "ds_read_b64_tr_b16 v[144:145], v63 offset:30720\n\t" \
  "v_mfma_f32_16x16x32_bf16 v[224:227], v[124:127], v[92:95], v[224:227]\n\t" \
  "ds_read_b64_tr_b16 v[146:147], v63 offset:31232\n\t" \
  "v_mfma_f32_16x16x32_bf16 v[232:235], v[96:99], v[92:95], v[232:235]\n\t" \
  "ds_read_b64_tr_b16 v[72:73], v62 offset:18432\n\t" \
  "v_mfma_f32_16x16x32_bf16 v[232:235], v[132:135], v[68:71], v[232:235]\n\t" \
  "ds_read_b64_tr_b16 v[74:75], v62 offset:18944\n\t" \
  "v_mfma_f32_16x16x32_bf16 v[240:243], v[108:111], v[68:71], v[240:243]\n\t" \
  "ds_read_b64_tr_b16 v[104:105], v62 offset:22528\n\t" \
  "v_mfma_f32_16x16x32_bf16 v[240:243], v[148:151], v[92:95], v[240:243]\n\t" \
  "ds_read_b64_tr_b16 v[106:107], v62 offset:23040\n\t" \
  "v_mfma_f32_16x16x32_bf16 v[220:223], v[80:83], v[84:87], v[220:223]\n\t" \
  "ds_read_b64_tr_b16 v[76:77], v63 offset:18432\n\t" \
  "s_waitcnt lgkmcnt(7)\n\t" \
  "v_mfma_f32_16x16x32_bf16 v[220:223], v[116:119], v[64:67], v[220:223]\n\t" \
  "ds_read_b64_tr_b16 v[78:79], v63 offset:18944\n\t" \
  "v_mfma_f32_16x16x32_bf16 v[228:231], v[88:91], v[64:67], v[228:231]\n\t" \
  "ds_read_b64_tr_b16 v[140:141], v63 offset:22528\n\t" \
  "v_mfma_f32_16x16x32_bf16 v[228:231], v[124:127], v[84:87], v[228:231]\n\t" \
  "ds_read_b64_tr_b16 v[142:143], v63 offset:23040\n\t" \
  "v_mfma_f32_16x16x32_bf16 v[236:239], v[96:99], v[84:87], v[236:239]\n\t" \
  "v_mfma_f32_16x16x32_bf16 v[236:239], v[132:135], v[64:67], v[236:239]\n\t" \
  "v_mfma_f32_16x16x32_bf16 v[244:247], v[108:111], v[64:67], v[244:247]\n\t" \
  "v_mfma_f32_16x16x32_bf16 v[244:247], v[148:151], v[84:87], v[244:247]\n\t" \
  "v_mfma_f32_16x16x32_bf16 v[184:187], v[100:103], v[80:83], v[184:187]\n\t" \
  "v_mfma_f32_16x16x32_bf16 v[184:187], v[112:115], v[116:119], v[184:187]\n\t" \
  "v_mfma_f32_16x16x32_bf16 v[192:195], v[112:115], v[88:91], v[192:195]\n\t" \
  "v_mfma_f32_16x16x32_bf16 v[192:195], v[100:103], v[124:127], v[192:195]\n\t" \
  "v_mfma_f32_16x16x32_bf16 v[200:203], v[100:103], v[96:99], v[200:203]\n\t" \
  "v_mfma_f32_16x16x32_bf16 v[200:203], v[112:115], v[132:135], v[200:203]\n\t" \
  "v_mfma_f32_16x16x32_bf16 v[208:211], v[112:115], v[108:111], v[208:211]\n\t" \
  "v_mfma_f32_16x16x32_bf16 v[208:211], v[100:103], v[148:151], v[208:211]\n\t" \
  "v_mfma_f32_16x16x32_bf16 v[248:251], v[100:103], v[128:131], v[248:251]\n\t" \
  "v_mfma_f32_16x16x32_bf16 v[248:251], v[112:115], v[120:123], v[248:251]\n\t" \
  "v_mfma_f32_16x16x32_bf16 v[252:255], v[136:139], v[128:131], v[252:255]\n\t" \
  "s_waitcnt lgkmcnt(0)\n\t" \
  "v_mfma_f32_16x16x32_bf16 v[252:255], v[144:147], v[120:123], v[252:255]\n\t" \
  "v_mfma_f32_16x16x32_bf16 v[188:191], v[144:147], v[116:119], v[188:191]\n\t" \
  "v_mfma_f32_16x16x32_bf16 v[188:191], v[136:139], v[80:83], v[188:191]\n\t" \
  "s_waitcnt vmcnt(0)\n\t" \
  "s_barrier\n\t" \
  "v_mfma_f32_16x16x32_bf16 v[196:199], v[136:139], v[124:127], v[196:199]\n\t" \
  "ds_read_b64_tr_b16 v[68:69], v54 offset:34816\n\t" \
  "v_mfma_f32_16x16x32_bf16 v[196:199], v[144:147], v[88:91], v[196:199]\n\t" \
  "ds_read_b64_tr_b16 v[70:71], v54 offset:35328\n\t" \
  "v_mfma_f32_16x16x32_bf16 v[204:207], v[144:147], v[132:135], v[204:207]\n\t" \
  "ds_read_b128 v[92:95], v52 offset:2048\n\t" \
  "v_mfma_f32_16x16x32_bf16 v[204:207], v[136:139], v[96:99], v[204:207]\n\t" \
  "ds_read_b64_tr_b16 v[64:65], v54 offset:38912\n\t" \
  "v_mfma_f32_16x16x32_bf16 v[212:215], v[136:139], v[148:151], v[212:215]\n\t" \
  "ds_read_b64_tr_b16 v[66:67], v54 offset:39424\n\t" \
  "v_mfma_f32_16x16x32_bf16 v[212:215], v[144:147], v[108:111], v[212:215]\n\t" \
  "ds_read_b128 v[84:87], v53 offset:2048\n\t" \
  "v_mfma_f32_16x16x32_bf16 v[152:155], v[72:75], v[80:83], v[152:155]\n\t" \
  "ds_read_b128 v[100:103], v53 offset:4096\n\t" \
  "v_mfma_f32_16x16x32_bf16 v[152:155], v[104:107], v[116:119], v[152:155]\n\t" \
  "ds_read_b128 v[112:115], v52 offset:4096\n\t" \
  "v_mfma_f32_16x16x32_bf16 v[160:163], v[104:107], v[88:91], v[160:163]\n\t" \
  "ds_read_b128 v[128:131], v52 offset:6144\n\t" \
  "v_mfma_f32_16x16x32_bf16 v[160:163], v[72:75], v[124:127], v[160:163]\n\t" \
  "ds_read_b128 v[120:123], v53 offset:6144\n\t" \
  "v_mfma_f32_16x16x32_bf16 v[168:171], v[72:75], v[96:99], v[168:171]\n\t" \
  "ds_read_b128 v[136:139], v53 offset:8192\n\t" \
  "v_mfma_f32_16x16x32_bf16 v[168:171], v[104:107], v[132:135], v[168:171]\n\t" \
  "ds_read_b128 v[144:147], v52 offset:8192\n\t" \
  "v_mfma_f32_16x16x32_bf16 v[176:179], v[104:107], v[108:111], v[176:179]\n\t" \
  "ds_read_b64_tr_b16 v[104:105], v55 offset:34816\n\t" \
  "v_mfma_f32_16x16x32_bf16 v[176:179], v[72:75], v[148:151], v[176:179]\n\t" \
  "ds_read_b64_tr_b16 v[106:107], v55 offset:35328\n\t" \
  "v_mfma_f32_16x16x32_bf16 v[156:159], v[76:79], v[80:83], v[156:159]\n\t" \
  "ds_read_b64_tr_b16 v[72:73], v55 offset:38912\n\t" \
  "v_mfma_f32_16x16x32_bf16 v[156:159], v[140:143], v[116:119], v[156:159]\n\t" \
  "ds_read_b64_tr_b16 v[74:75], v55 offset:39424\n\t" \
  "v_mfma_f32_16x16x32_bf16 v[164:167], v[140:143], v[88:91], v[164:167]\n\t" \
  "ds_read_b64_tr_b16 v[80:81], v54 offset:26624\n\t" \
  "v_mfma_f32_16x16x32_bf16 v[164:167], v[76:79], v[124:127], v[164:167]\n\t" \
  "ds_read_b64_tr_b16 v[82:83], v54 offset:27136\n\t" \
  "v_mfma_f32_16x16x32_bf16 v[172:175], v[76:79], v[96:99], v[172:175]\n\t" \
  "ds_read_b64_tr_b16 v[116:117], v54 offset:30720\n\t" \
  "v_mfma_f32_16x16x32_bf16 v[172:175], v[140:143], v[132:135], v[172:175]\n\t" \
  "ds_read_b64_tr_b16 v[118:119], v54 offset:31232\n\t" \
  "v_mfma_f32_16x16x32_bf16 v[180:183], v[140:143], v[108:111], v[180:183]\n\t" \
  "ds_read_b128 v[88:91], v52 offset:0\n\t" \
  "v_mfma_f32_16x16x32_bf16 v[180:183], v[76:79], v[148:151], v[180:183]\n\t" \
  "ds_read_b128 v[124:127], v53 offset:0\n\t" \
  "s_waitcnt lgkmcnt(7)\n\t" \
  "v_mfma_f32_16x16x32_bf16 v[216:219], v[92:95], v[68:71], v[216:219]\n\t" \
  "ds_read_b64_tr_b16 v[96:97], v55 offset:26624\n\t" \
  "v_mfma_f32_16x16x32_bf16 v[216:219], v[84:87], v[64:67], v[216:219]\n\t" \
  "ds_read_b64_tr_b16 v[98:99], v55 offset:27136\n\t" \
  "v_mfma_f32_16x16x32_bf16 v[224:227], v[100:103], v[64:67], v[224:227]\n\t" \
  "ds_read_b64_tr_b16 v[132:133], v55 offset:30720\n\t" \
  "v_mfma_f32_16x16x32_bf16 v[224:227], v[112:115], v[68:71], v[224:227]\n\t" \
  "ds_read_b64_tr_b16 v[134:135], v55 offset:31232\n\t" \
  "v_mfma_f32_16x16x32_bf16 v[232:235], v[128:131], v[68:71], v[232:235]\n\t" \
  "ds_read_b64_tr_b16 v[108:109], v54 offset:18432\n\t" \
  "v_mfma_f32_16x16x32_bf16 v[232:235], v[120:123], v[64:67], v[232:235]\n\t" \
  "ds_read_b64_tr_b16 v[110:111], v54 offset:18944\n\t" \
  "v_mfma_f32_16x16x32_bf16 v[240:243], v[136:139], v[64:67], v[240:243]\n\t" \
  "ds_read_b64_tr_b16 v[140:141], v54 offset:22528\n\t" \
  "v_mfma_f32_16x16x32_bf16 v[240:243], v[144:147], v[68:71], v[240:243]\n\t" \
  "ds_read_b64_tr_b16 v[142:143], v54 offset:23040\n\t" \
  "v_mfma_f32_16x16x32_bf16 v[220:223], v[92:95], v[104:107], v[220:223]\n\t" \
  "ds_read_b64_tr_b16 v[76:77], v55 offset:18432\n\t" \
  "s_waitcnt lgkmcnt(7)\n\t" \
  "v_mfma_f32_16x16x32_bf16 v[220:223], v[84:87], v[72:75], v[220:223]\n\t" \
  "ds_read_b64_tr_b16 v[78:79], v55 offset:18944\n\t" \
  "v_mfma_f32_16x16x32_bf16 v[228:231], v[100:103], v[72:75], v[228:231]\n\t" \
  "ds_read_b64_tr_b16 v[148:149], v55 offset:22528\n\t" \
  "v_mfma_f32_16x16x32_bf16 v[228:231], v[112:115], v[104:107], v[228:231]\n\t" \
  "ds_read_b64_tr_b16 v[150:151], v55 offset:23040\n\t" \
  "v_mfma_f32_16x16x32_bf16 v[236:239], v[128:131], v[104:107], v[236:239]\n\t" \
  "v_mfma_f32_16x16x32_bf16 v[236:239], v[120:123], v[72:75], v[236:239]\n\t" \
  "v_mfma_f32_16x16x32_bf16 v[244:247], v[136:139], v[72:75], v[244:247]\n\t" \
  "v_mfma_f32_16x16x32_bf16 v[244:247], v[144:147], v[104:107], v[244:247]\n\t" \
  "v_mfma_f32_16x16x32_bf16 v[184:187], v[80:83], v[92:95], v[184:187]\n\t" \
  "ds_read_b32 v10, v32 offset:104\n\t" \
  "v_mfma_f32_16x16x32_bf16 v[184:187], v[116:119], v[84:87], v[184:187]\n\t" \
  "v_lshlrev_b32 v11, 3, v4\n\t" \
  "v_mfma_f32_16x16x32_bf16 v[192:195], v[116:119], v[100:103], v[192:195]\n\t" \
  "v_mfma_f32_16x16x32_bf16 v[192:195], v[80:83], v[112:115], v[192:195]\n\t" \
  "v_mfma_f32_16x16x32_bf16 v[200:203], v[80:83], v[128:131], v[200:203]\n\t" \
  "s_waitcnt lgkmcnt(0)\n\t" \
  "s_cmp_lg_u32 %13, 0\n\t" \
  "s_cbranch_scc1 27f\n\t" \
  "s_mov_b32 exec_hi, 0\n\t" \
  "global_store_dword v11, v10, %25 sc1\n\t" \
  "s_mov_b32 exec_hi, -1\n\t" \
  "27:\n\t" \
  "v_mfma_f32_16x16x32_bf16 v[200:203], v[116:119], v[120:123], v[200:203]\n\t" \
  "v_mfma_f32_16x16x32_bf16 v[208:211], v[116:119], v[136:139], v[208:211]\n\t" \
  "v_mfma_f32_16x16x32_bf16 v[208:211], v[80:83], v[144:147], v[208:211]\n\t" \
  "v_mfma_f32_16x16x32_bf16 v[248:251], v[80:83], v[88:91], v[248:251]\n\t" \
  "v_mfma_f32_16x16x32_bf16 v[248:251], v[116:119], v[124:127], v[248:251]\n\t" \
  "v_mfma_f32_16x16x32_bf16 v[252:255], v[96:99], v[88:91], v[252:255]\n\t" \
  "v_mfma_f32_16x16x32_bf16 v[252:255], v[132:135], v[124:127], v[252:255]\n\t" \
  "v_mfma_f32_16x16x32_bf16 v[188:191], v[132:135], v[84:87], v[188:191]\n\t" \
  "v_mfma_f32_16x16x32_bf16 v[188:191], v[96:99], v[92:95], v[188:191]\n\t" \
  "s_barrier\n\t" \
  "v_mfma_f32_16x16x32_bf16 v[196:199], v[96:99], v[112:115], v[196:199]\n\t" \
  "ds_read_b64_tr_b16 v[64:65], v58 offset:34816\n\t" \
  "v_mfma_f32_16x16x32_bf16 v[196:199], v[132:135], v[100:103], v[196:199]\n\t" \
  "ds_read_b64_tr_b16 v[66:67], v58 offset:35328\n\t" \
  "v_mfma_f32_16x16x32_bf16 v[204:207], v[132:135], v[120:123], v[204:207]\n\t" \
  "ds_read_b128 v[68:71], v56 offset:2048\n\t" \
  "v_mfma_f32_16x16x32_bf16 v[204:207], v[96:99], v[128:131], v[204:207]\n\t" \
  "ds_read_b64_tr_b16 v[72:73], v58 offset:38912\n\t" \
  "v_mfma_f32_16x16x32_bf16 v[212:215], v[96:99], v[144:147], v[212:215]\n\t" \
  "ds_read_b64_tr_b16 v[74:75], v58 offset:39424\n\t" \
  "v_mfma_f32_16x16x32_bf16 v[212:215], v[132:135], v[136:139], v[212:215]\n\t" \
  "ds_read_b128 v[104:107], v57 offset:2048\n\t" \
  "v_mfma_f32_16x16x32_bf16 v[152:155], v[108:111], v[92:95], v[152:155]\n\t" \
  "ds_read_b128 v[80:83], v57 offset:4096\n\t" \
  "v_mfma_f32_16x16x32_bf16 v[152:155], v[140:143], v[84:87], v[152:155]\n\t" \
  "ds_read_b128 v[116:119], v56 offset:4096\n\t" \
  "v_mfma_f32_16x16x32_bf16 v[160:163], v[140:143], v[100:103], v[160:163]\n\t" \
  "ds_read_b128 v[88:91], v56 offset:6144\n\t" \
  "v_mfma_f32_16x16x32_bf16 v[160:163], v[108:111], v[112:115], v[160:163]\n\t" \
  "ds_read_b128 v[124:127], v57 offset:6144\n\t" \
  "v_mfma_f32_16x16x32_bf16 v[168:171], v[108:111], v[128:131], v[168:171]\n\t" \
  "ds_read_b128 v[96:99], v57 offset:8192\n\t" \
  "v_mfma_f32_16x16x32_bf16 v[168:171], v[140:143], v[120:123], v[168:171]\n\t" \
  "ds_read_b128 v[132:135], v56 offset:8192\n\t" \
  "v_mfma_f32_16x16x32_bf16 v[176:179], v[140:143], v[136:139], v[176:179]\n\t" \
  "ds_read_b64_tr_b16 v[140:141], v59 offset:34816\n\t" \
  "v_mfma_f32_16x16x32_bf16 v[176:179], v[108:111], v[144:147], v[176:179]\n\t" \
  "ds_read_b64_tr_b16 v[142:143], v59 offset:35328\n\t" \
  "v_mfma_f32_16x16x32_bf16 v[156:159], v[76:79], v[92:95], v[156:159]\n\t" \
  "ds_read_b64_tr_b16 v[108:109], v59 offset:38912\n\t" \
  "v_mfma_f32_16x16x32_bf16 v[156:159], v[148:151], v[84:87], v[156:159]\n\t" \
  "ds_read_b64_tr_b16 v[110:111], v59 offset:39424\n\t" \
  "v_mfma_f32_16x16x32_bf16 v[164:167], v[148:151], v[100:103], v[164:167]\n\t" \
  "ds_read_b64_tr_b16 v[92:93], v58 offset:26624\n\t" \
  "v_mfma_f32_16x16x32_bf16 v[164:167], v[76:79], v[112:115], v[164:167]\n\t" \
  "ds_read_b64_tr_b16 v[94:95], v58 offset:27136\n\t" \
  "v_mfma_f32_16x16x32_bf16 v[172:175], v[76:79], v[128:131], v[172:175]\n\t" \
  "ds_read_b64_tr_b16 v[84:85], v58 offset:30720\n\t" \
  "v_mfma_f32_16x16x32_bf16 v[172:175], v[148:151], v[120:123], v[172:175]\n\t" \
  "ds_read_b64_tr_b16 v[86:87], v58 offset:31232\n\t" \
  "v_mfma_f32_16x16x32_bf16 v[180:183], v[148:151], v[136:139], v[180:183]\n\t" \
  "ds_read_b128 v[100:103], v56 offset:0\n\t" \
  "v_mfma_f32_16x16x32_bf16 v[180:183], v[76:79], v[144:147], v[180:183]\n\t" \
  "ds_read_b128 v[112:115], v57 offset:0\n\t" \
  "s_waitcnt lgkmcnt(7)\n\t" \
  "v_mfma_f32_16x16x32_bf16 v[216:219], v[68:71], v[64:67], v[216:219]\n\t" \
  "ds_read_b64_tr_b16 v[128:129], v59 offset:26624\n\t" \
  "v_mfma_f32_16x16x32_bf16 v[216:219], v[104:107], v[72:75], v[216:219]\n\t" \
  "ds_read_b64_tr_b16 v[130:131], v59 offset:27136\n\t" \
  "v_mfma_f32_16x16x32_bf16 v[224:227], v[80:83], v[72:75], v[224:227]\n\t" \
  "ds_read_b64_tr_b16 v[120:121], v59 offset:30720\n\t" \
  "v_mfma_f32_16x16x32_bf16 v[224:227], v[116:119], v[64:67], v[224:227]\n\t" \
  "ds_read_b64_tr_b16 v[122:123], v59 offset:31232\n\t" \
  "v_mfma_f32_16x16x32_bf16 v[232:235], v[88:91], v[64:67], v[232:235]\n\t" \
  "ds_read_b64_tr_b16 v[136:137], v58 offset:18432\n\t" \
  "v_mfma_f32_16x16x32_bf16 v[232:235], v[124:127], v[72:75], v[232:235]\n\t" \
  "ds_read_b64_tr_b16 v[138:139], v58 offset:18944\n\t" \
  "v_mfma_f32_16x16x32_bf16 v[240:243], v[96:99], v[72:75], v[240:243]\n\t" \
  "ds_read_b64_tr_b16 v[148:149], v58 offset:22528\n\t" \
  "v_mfma_f32_16x16x32_bf16 v[240:243], v[132:135], v[64:67], v[240:243]\n\t" \
  "ds_read_b64_tr_b16 v[150:151], v58 offset:23040\n\t" \
  "v_mfma_f32_16x16x32_bf16 v[220:223], v[68:71], v[140:143], v[220:223]\n\t" \
  "ds_read_b64_tr_b16 v[76:77], v59 offset:18432\n\t" \
  "s_waitcnt lgkmcnt(7)\n\t" \
  "v_mfma_f32_16x16x32_bf16 v[220:223], v[104:107], v[108:111], v[220:223]\n\t" \
  "ds_read_b64_tr_b16 v[78:79], v59 offset:18944\n\t" \
  "v_mfma_f32_16x16x32_bf16 v[228:231], v[80:83], v[108:111], v[228:231]\n\t" \
  "ds_read_b64_tr_b16 v[144:145], v59 offset:22528\n\t" \
  "v_mfma_f32_16x16x32_bf16 v[228:231], v[116:119], v[140:143], v[228:231]\n\t" \
  "ds_read_b64_tr_b16 v[146:147], v59 offset:23040\n\t" \
  "v_mfma_f32_16x16x32_bf16 v[236:239], v[88:91], v[140:143], v[236:239]\n\t" \
  "v_mfma_f32_16x16x32_bf16 v[236:239], v[124:127], v[108:111], v[236:239]\n\t" \
  "v_mfma_f32_16x16x32_bf16 v[244:247], v[96:99], v[108:111], v[244:247]\n\t" \
  "v_mfma_f32_16x16x32_bf16 v[244:247], v[132:135], v[140:143], v[244:247]\n\t" \
  "v_mfma_f32_16x16x32_bf16 v[184:187], v[92:95], v[68:71], v[184:187]\n\t" \
  "v_mfma_f32_16x16x32_bf16 v[184:187], v[84:87], v[104:107], v[184:187]\n\t" \
  "v_mfma_f32_16x16x32_bf16 v[192:195], v[84:87], v[80:83], v[192:195]\n\t" \
  "v_mfma_f32_16x16x32_bf16 v[192:195], v[92:95], v[116:119], v[192:195]\n\t" \
  "v_mfma_f32_16x16x32_bf16 v[200:203], v[92:95], v[88:91], v[200:203]\n\t" \
  "v_mfma_f32_16x16x32_bf16 v[200:203], v[84:87], v[124:127], v[200:203]\n\t" \
  "v_mfma_f32_16x16x32_bf16 v[208:211], v[84:87], v[96:99], v[208:211]\n\t" \
  "v_mfma_f32_16x16x32_bf16 v[208:211], v[92:95], v[132:135], v[208:211]\n\t" \
  "v_mfma_f32_16x16x32_bf16 v[248:251], v[92:95], v[100:103], v[248:251]\n\t" \
  "v_mfma_f32_16x16x32_bf16 v[248:251], v[84:87], v[112:115], v[248:251]\n\t" \
  "v_mfma_f32_16x16x32_bf16 v[252:255], v[128:131], v[100:103], v[252:255]\n\t" \
  "s_waitcnt lgkmcnt(0)\n\t" \
  "v_mfma_f32_16x16x32_bf16 v[252:255], v[120:123], v[112:115], v[252:255]\n\t" \
  "v_mfma_f32_16x16x32_bf16 v[188:191], v[120:123], v[104:107], v[188:191]\n\t" \
  "v_mfma_f32_16x16x32_bf16 v[188:191], v[128:131], v[68:71], v[188:191]\n\t" \
  "s_barrier\n\t" \
  "v_mfma_f32_16x16x32_bf16 v[196:199], v[128:131], v[116:119], v[196:199]\n\t" \
  "ds_read_b64_tr_b16 v[72:73], v62 offset:34816\n\t" \
  "v_mfma_f32_16x16x32_bf16 v[196:199], v[120:123], v[80:83], v[196:199]\n\t" \
  "ds_read_b64_tr_b16 v[74:75], v62 offset:35328\n\t" \
  "v_mfma_f32_16x16x32_bf16 v[204:207], v[120:123], v[124:127], v[204:207]\n\t" \
  "ds_read_b128 v[64:67], v60 offset:2048\n\t" \
  "v_mfma_f32_16x16x32_bf16 v[204:207], v[128:131], v[88:91], v[204:207]\n\t" \
  "ds_read_b64_tr_b16 v[108:109], v62 offset:38912\n\t" \
  "v_mfma_f32_16x16x32_bf16 v[212:215], v[128:131], v[132:135], v[212:215]\n\t" \
  "ds_read_b64_tr_b16 v[110:111], v62 offset:39424\n\t" \
  "v_mfma_f32_16x16x32_bf16 v[212:215], v[120:123], v[96:99], v[212:215]\n\t" \
  "ds_read_b128 v[140:143], v61 offset:2048\n\t" \
  "v_mfma_f32_16x16x32_bf16 v[152:155], v[136:139], v[68:71], v[152:155]\n\t" \
  "ds_read_b128 v[92:95], v61 offset:4096\n\t" \
  "v_mfma_f32_16x16x32_bf16 v[152:155], v[148:151], v[104:107], v[152:155]\n\t" \
  "ds_read_b128 v[84:87], v60 offset:4096\n\t" \
  "v_mfma_f32_16x16x32_bf16 v[160:163], v[148:151], v[80:83], v[160:163]\n\t" \
  "ds_read_b128 v[100:103], v60 offset:6144\n\t" \
  "v_mfma_f32_16x16x32_bf16 v[160:163], v[136:139], v[116:119], v[160:163]\n\t" \
  "ds_read_b128 v[112:115], v61 offset:6144\n\t" \
  "v_mfma_f32_16x16x32_bf16 v[168:171], v[136:139], v[88:91], v[168:171]\n\t" \
  "ds_read_b128 v[128:131], v61 offset:8192\n\t" \
  "v_mfma_f32_16x16x32_bf16 v[168:171], v[148:151], v[124:127], v[168:171]\n\t" \
  "ds_read_b128 v[120:123], v60 offset:8192\n\t" \
  "v_mfma_f32_16x16x32_bf16 v[176:179], v[148:151], v[96:99], v[176:179]\n\t" \
  "ds_read_b64_tr_b16 v[148:149], v63 offset:34816\n\t" \
  "v_mfma_f32_16x16x32_bf16 v[176:179], v[136:139], v[132:135], v[176:179]\n\t" \
  "ds_read_b64_tr_b16 v[150:151], v63 offset:35328\n\t" \
  "v_mfma_f32_16x16x32_bf16 v[156:159], v[76:79], v[68:71], v[156:159]\n\t" \
  "ds_read_b64_tr_b16 v[136:137], v63 offset:38912\n\t" \
  "v_mfma_f32_16x16x32_bf16 v[156:159], v[144:147], v[104:107], v[156:159]\n\t" \
  "ds_read_b64_tr_b16 v[138:139], v63 offset:39424\n\t" \
  "v_mfma_f32_16x16x32_bf16 v[164:167], v[144:147], v[80:83], v[164:167]\n\t" \
  "ds_read_b64_tr_b16 v[68:69], v62 offset:26624\n\t" \
  "v_mfma_f32_16x16x32_bf16 v[164:167], v[76:79], v[116:119], v[164:167]\n\t" \
  "ds_read_b64_tr_b16 v[70:71], v62 offset:27136\n\t" \
  "v_mfma_f32_16x16x32_bf16 v[172:175], v[76:79], v[88:91], v[172:175]\n\t" \
  "ds_read_b64_tr_b16 v[104:105], v62 offset:30720\n\t" \
  "v_mfma_f32_16x16x32_bf16 v[172:175], v[144:147], v[124:127], v[172:175]\n\t" \
  "ds_read_b64_tr_b16 v[106:107], v62 offset:31232\n\t" \
  "v_mfma_f32_16x16x32_bf16 v[180:183], v[144:147], v[96:99], v[180:183]\n\t" \
  "ds_read_b128 v[80:83], v60 offset:0\n\t" \
  "v_mfma_f32_16x16x32_bf16 v[180:183], v[76:79], v[132:135], v[180:183]\n\t" \
  "ds_read_b128 v[116:119], v61 offset:0\n\t" \
  "s_waitcnt lgkmcnt(7)\n\t" \
  "v_mfma_f32_16x16x32_bf16 v[216:219], v[64:67], v[72:75], v[216:219]\n\t" \
  "ds_read_b64_tr_b16 v[88:89], v63 offset:26624\n\t" \
  "v_mfma_f32_16x16x32_bf16 v[216:219], v[140:143], v[108:111], v[216:219]\n\t" \
  "505:\n\t" \
  "s_cbranch_execz 509f\n\t" \
  "ds_read_b64_tr_b16 v[90:91], v63 offset:27136\n\t" \
  "v_mfma_f32_16x16x32_bf16 v[224:227], v[92:95], v[108:111], v[224:227]\n\t" \
  "ds_read_b64_tr_b16 v[124:125], v63 offset:30720\n\t" \
  "v_mfma_f32_16x16x32_bf16 v[224:227], v[84:87], v[72:75], v[224:227]\n\t" \
  "ds_read_b64_tr_b16 v[126:127], v63 offset:31232\n\t" \
  "v_mfma_f32_16x16x32_bf16 v[232:235], v[100:103], v[72:75], v[232:235]\n\t" \
  "ds_read_b64_tr_b16 v[96:97], v62 offset:18432\n\t" \
  "v_mfma_f32_16x16x32_bf16 v[232:235], v[112:115], v[108:111], v[232:235]\n\t" \
  "ds_read_b64_tr_b16 v[98:99], v62 offset:18944\n\t" \
  "v_mfma_f32_16x16x32_bf16 v[240:243], v[128:131], v[108:111], v[240:243]\n\t" \
  "ds_read_b64_tr_b16 v[144:145], v62 offset:22528\n\t" \
  "v_mfma_f32_16x16x32_bf16 v[240:243], v[120:123], v[72:75], v[240:243]\n\t" \
  "ds_read_b64_tr_b16 v[146:147], v62 offset:23040\n\t" \
  "v_mfma_f32_16x16x32_bf16 v[220:223], v[64:67], v[148:151], v[220:223]\n\t" \
  "ds_read_b64_tr_b16 v[76:77], v63 offset:18432\n\t" \
  "s_waitcnt lgkmcnt(7)\n\t" \
  "v_mfma_f32_16x16x32_bf16 v[220:223], v[140:143], v[136:139], v[220:223]\n\t" \
  "ds_read_b64_tr_b16 v[78:79], v63 offset:18944\n\t" \
  "v_mfma_f32_16x16x32_bf16 v[228:231], v[92:95], v[136:139], v[228:231]\n\t" \
  "ds_read_b64_tr_b16 v[132:133], v63 offset:22528\n\t" \
  "v_mfma_f32_16x16x32_bf16 v[228:231], v[84:87], v[148:151], v[228:231]\n\t" \
  "ds_read_b64_tr_b16 v[134:135], v63 offset:23040\n\t" \
  "v_mfma_f32_16x16x32_bf16 v[236:239], v[100:103], v[148:151], v[236:239]\n\t" \
  "v_mfma_f32_16x16x32_bf16 v[236:239], v[112:115], v[136:139], v[236:239]\n\t" \
  "v_mfma_f32_16x16x32_bf16 v[244:247], v[128:131], v[136:139], v[244:247]\n\t" \
  "v_mfma_f32_16x16x32_bf16 v[244:247], v[120:123], v[148:151], v[244:247]\n\t" \
  "v_mfma_f32_16x16x32_bf16 v[184:187], v[68:71], v[64:67], v[184:187]\n\t" \
  "v_mfma_f32_16x16x32_bf16 v[184:187], v[104:107], v[140:143], v[184:187]\n\t" \
  "v_mfma_f32_16x16x32_bf16 v[192:195], v[104:107], v[92:95], v[192:195]\n\t" \
  "v_mfma_f32_16x16x32_bf16 v[192:195], v[68:71], v[84:87], v[192:195]\n\t" \
  "v_mfma_f32_16x16x32_bf16 v[200:203], v[68:71], v[100:103], v[200:203]\n\t" \
  "v_mfma_f32_16x16x32_bf16 v[200:203], v[104:107], v[112:115], v[200:203]\n\t" \
  "v_mfma_f32_16x16x32_bf16 v[208:211], v[104:107], v[128:131], v[208:211]\n\t" \
  "v_mfma_f32_16x16x32_bf16 v[208:211], v[68:71], v[120:123], v[208:211]\n\t" \
  "v_mfma_f32_16x16x32_bf16 v[248:251], v[68:71], v[80:83], v[248:251]\n\t" \
  "v_mfma_f32_16x16x32_bf16 v[248:251], v[104:107], v[116:119], v[248:251]\n\t" \
  "v_mfma_f32_16x16x32_bf16 v[252:255], v[88:91], v[80:83], v[252:255]\n\t" \
  "s_waitcnt lgkmcnt(0)\n\t" \
  "v_mfma_f32_16x16x32_bf16 v[252:255], v[124:127], v[116:119], v[252:255]\n\t" \
  "v_mfma_f32_16x16x32_bf16 v[188:191], v[124:127], v[140:143], v[188:191]\n\t" \
  "v_mfma_f32_16x16x32_bf16 v[188:191], v[88:91], v[64:67], v[188:191]\n\t" \
  "s_barrier\n\t" \
  "v_mfma_f32_16x16x32_bf16 v[196:199], v[88:91], v[84:87], v[196:199]\n\t" \
  "ds_read_b64_tr_b16 v[108:109], v54 offset:34816\n\t" \
  "v_mfma_f32_16x16x32_bf16 v[196:199], v[124:127], v[92:95], v[196:199]\n\t" \
  "ds_read_b64_tr_b16 v[110:111], v54 offset:35328\n\t" \
  "v_mfma_f32_16x16x32_bf16 v[204:207], v[124:127], v[112:115], v[204:207]\n\t" \
  "ds_read_b128 v[72:75], v52 offset:2048\n\t" \
  "v_mfma_f32_16x16x32_bf16 v[204:207], v[88:91], v[100:103], v[204:207]\n\t" \
  "ds_read_b64_tr_b16 v[136:137], v54 offset:38912\n\t" \
  "v_mfma_f32_16x16x32_bf16 v[212:215], v[88:91], v[120:123], v[212:215]\n\t" \
  "ds_read_b64_tr_b16 v[138:139], v54 offset:39424\n\t" \
  "v_mfma_f32_16x16x32_bf16 v[212:215], v[124:127], v[128:131], v[212:215]\n\t" \
  "ds_read_b128 v[148:151], v53 offset:2048\n\t" \
  "v_mfma_f32_16x16x32_bf16 v[152:155], v[96:99], v[64:67], v[152:155]\n\t" \
  "ds_read_b128 v[68:71], v53 offset:4096\n\t" \
  "v_mfma_f32_16x16x32_bf16 v[152:155], v[144:147], v[140:143], v[152:155]\n\t" \
  "ds_read_b128 v[104:107], v52 offset:4096\n\t" \
  "v_mfma_f32_16x16x32_bf16 v[160:163], v[144:147], v[92:95], v[160:163]\n\t" \
  "ds_read_b128 v[80:83], v52 offset:6144\n\t" \
  "v_mfma_f32_16x16x32_bf16 v[160:163], v[96:99], v[84:87], v[160:163]\n\t" \
  "ds_read_b128 v[116:119], v53 offset:6144\n\t" \
  "v_mfma_f32_16x16x32_bf16 v[168:171], v[96:99], v[100:103], v[168:171]\n\t" \
  "ds_read_b128 v[88:91], v53 offset:8192\n\t" \
  "v_mfma_f32_16x16x32_bf16 v[168:171], v[144:147], v[112:115], v[168:171]\n\t" \
  "ds_read_b128 v[124:127], v52 offset:8192\n\t" \
  "v_mfma_f32_16x16x32_bf16 v[176:179], v[144:147], v[128:131], v[176:179]\n\t" \
  "ds_read_b64_tr_b16 v[144:145], v55 offset:34816\n\t" \
  "v_mfma_f32_16x16x32_bf16 v[176:179], v[96:99], v[120:123], v[176:179]\n\t" \
  "ds_read_b64_tr_b16 v[146:147], v55 offset:35328\n\t" \
  "v_mfma_f32_16x16x32_bf16 v[156:159], v[76:79], v[64:67], v[156:159]\n\t" \
  "ds_read_b64_tr_b16 v[96:97], v55 offset:38912\n\t" \
  "v_mfma_f32_16x16x32_bf16 v[156:159], v[132:135], v[140:143], v[156:159]\n\t" \
  "ds_read_b64_tr_b16 v[98:99], v55 offset:39424\n\t" \
  "v_mfma_f32_16x16x32_bf16 v[164:167], v[132:135], v[92:95], v[164:167]\n\t" \
  "ds_read_b64_tr_b16 v[64:65], v54 offset:26624\n\t" \
  "v_mfma_f32_16x16x32_bf16 v[164:167], v[76:79], v[84:87], v[164:167]\n\t" \
  "ds_read_b64_tr_b16 v[66:67], v54 offset:27136\n\t" \
  "v_mfma_f32_16x16x32_bf16 v[172:175], v[76:79], v[100:103], v[172:175]\n\t" \
  "ds_read_b64_tr_b16 v[140:141], v54 offset:30720\n\t" \
  "v_mfma_f32_16x16x32_bf16 v[172:175], v[132:135], v[112:115], v[172:175]\n\t" \
  "ds_read_b64_tr_b16 v[142:143], v54 offset:31232\n\t" \
  "v_mfma_f32_16x16x32_bf16 v[180:183], v[132:135], v[128:131], v[180:183]\n\t" \
  "ds_read_b128 v[92:95], v52 offset:0\n\t" \
  "v_mfma_f32_16x16x32_bf16 v[180:183], v[76:79], v[120:123], v[180:183]\n\t" \
  "ds_read_b128 v[84:87], v53 offset:0\n\t" \
  "s_waitcnt lgkmcnt(7)\n\t" \
  "v_mfma_f32_16x16x32_bf16 v[216:219], v[72:75], v[108:111], v[216:219]\n\t" \
  "ds_read_b64_tr_b16 v[100:101], v55 offset:26624\n\t" \
  "v_mfma_f32_16x16x32_bf16 v[216:219], v[148:151], v[136:139], v[216:219]\n\t" \
  "ds_read_b64_tr_b16 v[102:103], v55 offset:27136\n\t" \
  "v_mfma_f32_16x16x32_bf16 v[224:227], v[68:71], v[136:139], v[224:227]\n\t" \
  "ds_read_b64_tr_b16 v[112:113], v55 offset:30720\n\t" \
  "v_mfma_f32_16x16x32_bf16 v[224:227], v[104:107], v[108:111], v[224:227]\n\t" \
  "ds_read_b64_tr_b16 v[114:115], v55 offset:31232\n\t" \
  "v_mfma_f32_16x16x32_bf16 v[232:235], v[80:83], v[108:111], v[232:235]\n\t" \
  "ds_read_b64_tr_b16 v[128:129], v54 offset:18432\n\t" \
  "v_mfma_f32_16x16x32_bf16 v[232:235], v[116:119], v[136:139], v[232:235]\n\t" \
  "ds_read_b64_tr_b16 v[130:131], v54 offset:18944\n\t" \
  "v_mfma_f32_16x16x32_bf16 v[240:243], v[88:91], v[136:139], v[240:243]\n\t" \
  "ds_read_b64_tr_b16 v[132:133], v54 offset:22528\n\t" \
  "v_mfma_f32_16x16x32_bf16 v[240:243], v[124:127], v[108:111], v[240:243]\n\t" \
  "ds_read_b64_tr_b16 v[134:135], v54 offset:23040\n\t" \
  "v_mfma_f32_16x16x32_bf16 v[220:223], v[72:75], v[144:147], v[220:223]\n\t" \
  "ds_read_b64_tr_b16 v[76:77], v55 offset:18432\n\t" \
  "s_waitcnt lgkmcnt(7)\n\t" \
  "v_mfma_f32_16x16x32_bf16 v[220:223], v[148:151], v[96:99], v[220:223]\n\t" \
  "ds_read_b64_tr_b16 v[78:79], v55 offset:18944\n\t" \
  "v_mfma_f32_16x16x32_bf16 v[228:231], v[68:71], v[96:99], v[228:231]\n\t" \
  "ds_read_b64_tr_b16 v[120:121], v55 offset:22528\n\t" \
  "v_mfma_f32_16x16x32_bf16 v[228:231], v[104:107], v[144:147], v[228:231]\n\t" \
  "ds_read_b64_tr_b16 v[122:123], v55 offset:23040\n\t" \
  "v_mfma_f32_16x16x32_bf16 v[236:239], v[80:83], v[144:147], v[236:239]\n\t" \
  "v_mfma_f32_16x16x32_bf16 v[236:239], v[116:119], v[96:99], v[236:239]\n\t" \
  "v_mfma_f32_16x16x32_bf16 v[244:247], v[88:91], v[96:99], v[244:247]\n\t" \
  "v_mfma_f32_16x16x32_bf16 v[244:247], v[124:127], v[144:147], v[244:247]\n\t" \
  "v_mfma_f32_16x16x32_bf16 v[184:187], v[64:67], v[72:75], v[184:187]\n\t" \
  "v_mfma_f32_16x16x32_bf16 v[184:187], v[140:143], v[148:151], v[184:187]\n\t" \
  "v_mfma_f32_16x16x32_bf16 v[192:195], v[140:143], v[68:71], v[192:195]\n\t" \
  "v_mfma_f32_16x16x32_bf16 v[192:195], v[64:67], v[104:107], v[192:195]\n\t" \
  "v_mfma_f32_16x16x32_bf16 v[200:203], v[64:67], v[80:83], v[200:203]\n\t" \
  "v_mfma_f32_16x16x32_bf16 v[200:203], v[140:143], v[116:119], v[200:203]\n\t" \
  "v_mfma_f32_16x16x32_bf16 v[208:211], v[140:143], v[88:91], v[208:211]\n\t" \
  "v_mfma_f32_16x16x32_bf16 v[208:211], v[64:67], v[124:127], v[208:211]\n\t" \
  "v_mfma_f32_16x16x32_bf16 v[248:251], v[64:67], v[92:95], v[248:251]\n\t" \
  "v_mfma_f32_16x16x32_bf16 v[248:251], v[140:143], v[84:87], v[248:251]\n\t" \
  "v_mfma_f32_16x16x32_bf16 v[252:255], v[100:103], v[92:95], v[252:255]\n\t" \
  "s_waitcnt lgkmcnt(0)\n\t" \
  "v_mfma_f32_16x16x32_bf16 v[252:255], v[112:115], v[84:87], v[252:255]\n\t" \
  "v_mfma_f32_16x16x32_bf16 v[188:191], v[112:115], v[148:151], v[188:191]\n\t" \
  "v_mfma_f32_16x16x32_bf16 v[188:191], v[100:103], v[72:75], v[188:191]\n\t" \
  "s_barrier\n\t" \
  "v_mfma_f32_16x16x32_bf16 v[196:199], v[100:103], v[104:107], v[196:199]\n\t" \
  "ds_read_b64_tr_b16 v[96:97], v58 offset:34816\n\t" \
  "v_mfma_f32_16x16x32_bf16 v[196:199], v[112:115], v[68:71], v[196:199]\n\t" \
  "ds_read_b64_tr_b16 v[98:99], v58 offset:35328\n\t" \
  "v_mfma_f32_16x16x32_bf16 v[204:207], v[112:115], v[116:119], v[204:207]\n\t" \
  "ds_read_b128 v[136:139], v56 offset:2048\n\t" \
  "v_mfma_f32_16x16x32_bf16 v[204:207], v[100:103], v[80:83], v[204:207]\n\t" \
  "ds_read_b64_tr_b16 v[64:65], v58 offset:38912\n\t" \
  "v_mfma_f32_16x16x32_bf16 v[212:215], v[100:103], v[124:127], v[212:215]\n\t" \
  "ds_read_b64_tr_b16 v[66:67], v58 offset:39424\n\t" \
  "v_mfma_f32_16x16x32_bf16 v[212:215], v[112:115], v[88:91], v[212:215]\n\t" \
  "ds_read_b128 v[108:111], v57 offset:2048\n\t" \
  "v_mfma_f32_16x16x32_bf16 v[152:155], v[128:131], v[72:75], v[152:155]\n\t" \
  "ds_read_b128 v[144:147], v57 offset:4096\n\t" \
  "v_mfma_f32_16x16x32_bf16 v[152:155], v[132:135], v[148:151], v[152:155]\n\t" \
  "ds_read_b128 v[140:143], v56 offset:4096\n\t" \
  "v_mfma_f32_16x16x32_bf16 v[160:163], v[132:135], v[68:71], v[160:163]\n\t" \
  "ds_read_b128 v[112:115], v56 offset:6144\n\t" \
  "v_mfma_f32_16x16x32_bf16 v[160:163], v[128:131], v[104:107], v[160:163]\n\t" \
  "ds_read_b64_tr_b16 v[92:93], v59 offset:34816\n\t" \
  "v_mfma_f32_16x16x32_bf16 v[168:171], v[128:131], v[80:83], v[168:171]\n\t" \
  "ds_read_b64_tr_b16 v[94:95], v59 offset:35328\n\t" \
  "v_mfma_f32_16x16x32_bf16 v[168:171], v[132:135], v[116:119], v[168:171]\n\t" \
  "ds_read_b64_tr_b16 v[84:85], v59 offset:38912\n\t" \
  "v_mfma_f32_16x16x32_bf16 v[176:179], v[132:135], v[88:91], v[176:179]\n\t" \
  "ds_read_b128 v[132:135], v57 offset:6144\n\t" \
  "v_mfma_f32_16x16x32_bf16 v[176:179], v[128:131], v[124:127], v[176:179]\n\t" \
  "ds_read_b128 v[128:131], v57 offset:8192\n\t" \
  "v_mfma_f32_16x16x32_bf16 v[156:159], v[76:79], v[72:75], v[156:159]\n\t" \
  "ds_read_b64_tr_b16 v[86:87], v59 offset:39424\n\t" \
  "v_mfma_f32_16x16x32_bf16 v[156:159], v[120:123], v[148:151], v[156:159]\n\t" \
  "ds_read_b128 v[148:151], v56 offset:8192\n\t" \
  "v_mfma_f32_16x16x32_bf16 v[164:167], v[120:123], v[68:71], v[164:167]\n\t" \
  "ds_read_b64_tr_b16 v[100:101], v58 offset:26624\n\t" \
  "v_mfma_f32_16x16x32_bf16 v[164:167], v[76:79], v[104:107], v[164:167]\n\t" \
  "ds_read_b64_tr_b16 v[102:103], v58 offset:27136\n\t" \
  "v_mfma_f32_16x16x32_bf16 v[172:175], v[76:79], v[80:83], v[172:175]\n\t" \
  "ds_read_b64_tr_b16 v[72:73], v58 offset:30720\n\t" \
  "v_mfma_f32_16x16x32_bf16 v[172:175], v[120:123], v[116:119], v[172:175]\n\t" \
  "ds_read_b64_tr_b16 v[74:75], v58 offset:31232\n\t" \
  "v_mfma_f32_16x16x32_bf16 v[180:183], v[120:123], v[88:91], v[180:183]\n\t" \
  "ds_read_b128 v[68:71], v56 offset:0\n\t" \
  "v_mfma_f32_16x16x32_bf16 v[180:183], v[76:79], v[124:127], v[180:183]\n\t" \
  "ds_read_b128 v[104:107], v57 offset:0\n\t" \
  "s_waitcnt lgkmcnt(7)\n\t" \
  "v_mfma_f32_16x16x32_bf16 v[216:219], v[136:139], v[96:99], v[216:219]\n\t" \
  "ds_read_b64_tr_b16 v[80:81], v59 offset:26624\n\t" \
  "v_mfma_f32_16x16x32_bf16 v[216:219], v[108:111], v[64:67], v[216:219]\n\t" \
  "ds_read_b64_tr_b16 v[82:83], v59 offset:27136\n\t" \
  "v_mfma_f32_16x16x32_bf16 v[224:227], v[144:147], v[64:67], v[224:227]\n\t" \
  "ds_read_b64_tr_b16 v[88:89], v59 offset:30720\n\t" \
  "v_mfma_f32_16x16x32_bf16 v[224:227], v[140:143], v[96:99], v[224:227]\n\t" \
  "ds_read_b64_tr_b16 v[90:91], v59 offset:31232\n\t" \
  "v_mfma_f32_16x16x32_bf16 v[232:235], v[112:115], v[96:99], v[232:235]\n\t" \
  "ds_read_b64_tr_b16 v[116:117], v58 offset:18432\n\t" \
  "v_mfma_f32_16x16x32_bf16 v[232:235], v[132:135], v[64:67], v[232:235]\n\t" \
  "ds_read_b64_tr_b16 v[118:119], v58 offset:18944\n\t" \
  "v_mfma_f32_16x16x32_bf16 v[240:243], v[128:131], v[64:67], v[240:243]\n\t" \
  "ds_read_b64_tr_b16 v[120:121], v58 offset:22528\n\t" \
  "s_waitcnt lgkmcnt(7)\n\t" \
  "v_mfma_f32_16x16x32_bf16 v[240:243], v[148:151], v[96:99], v[240:243]\n\t" \
  "ds_read_b64_tr_b16 v[122:123], v58 offset:23040\n\t" \
  "v_mfma_f32_16x16x32_bf16 v[220:223], v[136:139], v[92:95], v[220:223]\n\t" \
  "ds_read_b64_tr_b16 v[124:125], v59 offset:18432\n\t" \
  "v_mfma_f32_16x16x32_bf16 v[220:223], v[108:111], v[84:87], v[220:223]\n\t" \
  "ds_read_b64_tr_b16 v[126:127], v59 offset:18944\n\t" \
  "v_mfma_f32_16x16x32_bf16 v[228:231], v[144:147], v[84:87], v[228:231]\n\t" \
  "ds_read_b64_tr_b16 v[76:77], v59 offset:22528\n\t" \
  "v_mfma_f32_16x16x32_bf16 v[228:231], v[140:143], v[92:95], v[228:231]\n\t" \
  "ds_read_b64_tr_b16 v[78:79], v59 offset:23040\n\t" \
  "v_mfma_f32_16x16x32_bf16 v[236:239], v[112:115], v[92:95], v[236:239]\n\t" \
  "v_mfma_f32_16x16x32_bf16 v[236:239], v[132:135], v[84:87], v[236:239]\n\t" \
  "v_mfma_f32_16x16x32_bf16 v[244:247], v[128:131], v[84:87], v[244:247]\n\t" \
  "v_mfma_f32_16x16x32_bf16 v[244:247], v[148:151], v[92:95], v[244:247]\n\t" \
  "v_mfma_f32_16x16x32_bf16 v[184:187], v[100:103], v[136:139], v[184:187]\n\t" \
  "v_mfma_f32_16x16x32_bf16 v[184:187], v[72:75], v[108:111], v[184:187]\n\t" \
  "v_mfma_f32_16x16x32_bf16 v[192:195], v[72:75], v[144:147], v[192:195]\n\t" \
  "v_and_b32 v14, -16, v32\n\t" \
  "v_mfma_f32_16x16x32_bf16 v[192:195], v[100:103], v[140:143], v[192:195]\n\t" \
  "v_mfma_f32_16x16x32_bf16 v[200:203], v[100:103], v[112:115], v[200:203]\n\t" \
  "ds_read_b128 v[10:13], v14 offset:128\n\t" \
  "v_mfma_f32_16x16x32_bf16 v[200:203], v[72:75], v[132:135], v[200:203]\n\t" \
  "v_mfma_f32_16x16x32_bf16 v[208:211], v[72:75], v[128:131], v[208:211]\n\t" \
  "v_mfma_f32_16x16x32_bf16 v[208:211], v[100:103], v[148:151], v[208:211]\n\t" \
  "v_mfma_f32_16x16x32_bf16 v[248:251], v[100:103], v[68:71], v[248:251]\n\t" \
  "v_mfma_f32_16x16x32_bf16 v[248:251], v[72:75], v[104:107], v[248:251]\n\t" \
  "s_waitcnt lgkmcnt(1)\n\t" \
  "v_mfma_f32_16x16x32_bf16 v[252:255], v[80:83], v[68:71], v[252:255]\n\t" \
  "s_waitcnt lgkmcnt(0)\n\t" \
  "v_and_b32 v10, v10, v11\n\t" \
  "v_and_b32 v12, v12, v13\n\t" \
  "v_mfma_f32_16x16x32_bf16 v[252:255], v[88:91], v[104:107], v[252:255]\n\t" \
  "v_and_b32 v10, v10, v12\n\t" \
  "v_mfma_f32_16x16x32_bf16 v[188:191], v[88:91], v[108:111], v[188:191]\n\t" \
  "v_readfirstlane_b32 vcc_lo, v10\n\t" \
  "v_mfma_f32_16x16x32_bf16 v[188:191], v[80:83], v[136:139], v[188:191]\n\t" \
  "ds_write_b32 v32, v0 offset:144\n\t" \
  "s_cmp_eq_u32 vcc_lo, 0\n\t" \
  "s_cbranch_scc1 15f\n\t" \
  "14:\n\t" \
  "v_mfma_f32_16x16x32_bf16 v[196:199], v[80:83], v[140:143], v[196:199]\n\t" \
  "ds_read_b64_tr_b16 v[64:65], v62 offset:26624\n\t" \
  "v_mfma_f32_16x16x32_bf16 v[196:199], v[88:91], v[144:147], v[196:199]\n\t" \
  "ds_read_b64_tr_b16 v[66:67], v62 offset:27136\n\t" \
  "v_mfma_f32_16x16x32_bf16 v[204:207], v[88:91], v[132:135], v[204:207]\n\t" \
  "ds_read_b128 v[96:99], v60 offset:0\n\t" \
  "v_mfma_f32_16x16x32_bf16 v[204:207], v[80:83], v[112:115], v[204:207]\n\t" \
  "ds_read_b128 v[84:87], v60 offset:2048\n\t" \
  "v_mfma_f32_16x16x32_bf16 v[212:215], v[80:83], v[148:151], v[212:215]\n\t" \
  "ds_read_b128 v[92:95], v60 offset:4096\n\t" \
  "v_mfma_f32_16x16x32_bf16 v[212:215], v[88:91], v[128:131], v[212:215]\n\t" \
  "ds_read_b128 v[100:103], v60 offset:6144\n\t" \
  "v_mfma_f32_16x16x32_bf16 v[152:155], v[116:119], v[136:139], v[152:155]\n\t" \
  "ds_read_b128 v[72:75], v60 offset:8192\n\t" \
  "v_mfma_f32_16x16x32_bf16 v[152:155], v[120:123], v[108:111], v[152:155]\n\t" \
  "ds_read_b64_tr_b16 v[68:69], v63 offset:26624\n\t" \
  "v_mfma_f32_16x16x32_bf16 v[160:163], v[120:123], v[144:147], v[160:163]\n\t" \
  "ds_read_b64_tr_b16 v[70:71], v63 offset:27136\n\t" \
  "v_mfma_f32_16x16x32_bf16 v[160:163], v[116:119], v[140:143], v[160:163]\n\t" \
  "ds_read_b64_tr_b16 v[104:105], v62 offset:34816\n\t" \
  "v_mfma_f32_16x16x32_bf16 v[168:171], v[116:119], v[112:115], v[168:171]\n\t" \
  "ds_read_b64_tr_b16 v[106:107], v62 offset:35328\n\t" \
  "v_mfma_f32_16x16x32_bf16 v[168:171], v[120:123], v[132:135], v[168:171]\n\t" \
  "ds_read_b64_tr_b16 v[80:81], v63 offset:34816\n\t" \
  "v_mfma_f32_16x16x32_bf16 v[176:179], v[120:123], v[128:131], v[176:179]\n\t" \
  "ds_read_b64_tr_b16 v[82:83], v63 offset:35328\n\t" \
  "v_mfma_f32_16x16x32_bf16 v[176:179], v[116:119], v[148:151], v[176:179]\n\t" \
  "ds_read_b64_tr_b16 v[88:89], v62 offset:18432\n\t" \
  "v_mfma_f32_16x16x32_bf16 v[156:159], v[124:127], v[136:139], v[156:159]\n\t" \
  "ds_read_b64_tr_b16 v[90:91], v62 offset:18944\n\t" \
  "v_mfma_f32_16x16x32_bf16 v[156:159], v[76:79], v[108:111], v[156:159]\n\t" \
  "ds_read_b64_tr_b16 v[120:121], v62 offset:30720\n\t" \
  "v_mfma_f32_16x16x32_bf16 v[164:167], v[76:79], v[144:147], v[164:167]\n\t" \
  "ds_read_b64_tr_b16 v[122:123], v62 offset:31232\n\t" \
  "v_mfma_f32_16x16x32_bf16 v[164:167], v[124:127], v[140:143], v[164:167]\n\t" \
  "ds_read_b128 v[116:119], v61 offset:0\n\t" \
  "v_mfma_f32_16x16x32_bf16 v[172:175], v[124:127], v[112:115], v[172:175]\n\t" \
  "ds_read_b128 v[136:139], v61 offset:2048\n\t" \
  "v_mfma_f32_16x16x32_bf16 v[172:175], v[76:79], v[132:135], v[172:175]\n\t" \
  "ds_read_b128 v[108:111], v61 offset:4096\n\t" \
  "v_mfma_f32_16x16x32_bf16 v[180:183], v[76:79], v[128:131], v[180:183]\n\t" \
  "ds_read_b64_tr_b16 v[76:77], v63 offset:18432\n\t" \
  "v_mfma_f32_16x16x32_bf16 v[180:183], v[124:127], v[148:151], v[180:183]\n\t" \
  "ds_read_b64_tr_b16 v[78:79], v63 offset:18944\n\t" \
  "s_waitcnt lgkmcnt(7)\n\t" \
  "v_mfma_f32_16x16x32_bf16 v[248:251], v[64:67], v[96:99], v[248:251]\n\t" \
  "ds_read_b128 v[144:147], v61 offset:6144\n\t" \
  "v_mfma_f32_16x16x32_bf16 v[184:187], v[64:67], v[84:87], v[184:187]\n\t" \
  "ds_read_b128 v[140:143], v61 offset:8192\n\t" \
  "v_mfma_f32_16x16x32_bf16 v[192:195], v[64:67], v[92:95], v[192:195]\n\t" \
  "ds_read_b64_tr_b16 v[112:113], v63 offset:30720\n\t" \
  "v_mfma_f32_16x16x32_bf16 v[200:203], v[64:67], v[100:103], v[200:203]\n\t" \
  "ds_read_b64_tr_b16 v[114:115], v63 offset:31232\n\t" \
  "v_mfma_f32_16x16x32_bf16 v[208:211], v[64:67], v[72:75], v[208:211]\n\t" \
  "ds_read_b64_tr_b16 v[132:133], v62 offset:22528\n\t" \
  "v_mfma_f32_16x16x32_bf16 v[252:255], v[68:71], v[96:99], v[252:255]\n\t" \
  "ds_read_b64_tr_b16 v[134:135], v62 offset:23040\n\t" \
  "v_mfma_f32_16x16x32_bf16 v[188:191], v[68:71], v[84:87], v[188:191]\n\t" \
  "ds_read_b64_tr_b16 v[128:129], v63 offset:22528\n\t" \
  "v_mfma_f32_16x16x32_bf16 v[196:199], v[68:71], v[92:95], v[196:199]\n\t" \
  "ds_read_b64_tr_b16 v[130:131], v63 offset:23040\n\t" \
  "v_mfma_f32_16x16x32_bf16 v[204:207], v[68:71], v[100:103], v[204:207]\n\t" \
  "ds_read_b64_tr_b16 v[124:125], v62 offset:38912\n\t" \
  "v_mfma_f32_16x16x32_bf16 v[212:215], v[68:71], v[72:75], v[212:215]\n\t" \
  "ds_read_b64_tr_b16 v[126:127], v62 offset:39424\n\t" \
  "v_mfma_f32_16x16x32_bf16 v[216:219], v[84:87], v[104:107], v[216:219]\n\t" \
  "ds_read_b64_tr_b16 v[148:149], v63 offset:38912\n\t" \
  "v_mfma_f32_16x16x32_bf16 v[224:227], v[92:95], v[104:107], v[224:227]\n\t" \
  "ds_read_b64_tr_b16 v[150:151], v63 offset:39424\n\t" \
  "v_mfma_f32_16x16x32_bf16 v[232:235], v[100:103], v[104:107], v[232:235]\n\t" \
  "v_mfma_f32_16x16x32_bf16 v[240:243], v[72:75], v[104:107], v[240:243]\n\t" \
  "v_mfma_f32_16x16x32_bf16 v[220:223], v[84:87], v[80:83], v[220:223]\n\t" \
  "v_mfma_f32_16x16x32_bf16 v[228:231], v[92:95], v[80:83], v[228:231]\n\t" \
  "v_mfma_f32_16x16x32_bf16 v[236:239], v[100:103], v[80:83], v[236:239]\n\t" \
  "v_mfma_f32_16x16x32_bf16 v[244:247], v[72:75], v[80:83], v[244:247]\n\t" \
  "v_mfma_f32_16x16x32_bf16 v[152:155], v[88:91], v[84:87], v[152:155]\n\t" \
  "v_mfma_f32_16x16x32_bf16 v[160:163], v[88:91], v[92:95], v[160:163]\n\t" \
  "v_mfma_f32_16x16x32_bf16 v[168:171], v[88:91], v[100:103], v[168:171]\n\t" \
  "v_mfma_f32_16x16x32_bf16 v[176:179], v[88:91], v[72:75], v[176:179]\n\t" \
  "s_waitcnt lgkmcnt(0)\n\t" \
  "v_mfma_f32_16x16x32_bf16 v[156:159], v[76:79], v[84:87], v[156:159]\n\t" \
  "v_mfma_f32_16x16x32_bf16 v[164:167], v[76:79], v[92:95], v[164:167]\n\t" \
  "ds_write_b32 v32, v0\n\t" \
  "v_mfma_f32_16x16x32_bf16 v[172:175], v[76:79], v[100:103], v[172:175]\n\t" \
  "v_mfma_f32_16x16x32_bf16 v[180:183], v[76:79], v[72:75], v[180:183]\n\t" \
  "v_mfma_f32_16x16x32_bf16 v[248:251], v[120:123], v[116:119], v[248:251]\n\t" \
  "v_and_b32 v56, -16, v32\n\t" \
  "v_mfma_f32_16x16x32_bf16 v[184:187], v[120:123], v[136:139], v[184:187]\n\t" \
  "v_mfma_f32_16x16x32_bf16 v[192:195], v[120:123], v[108:111], v[192:195]\n\t" \
  "v_mfma_f32_16x16x32_bf16 v[200:203], v[120:123], v[144:147], v[200:203]\n\t" \
  "v_mfma_f32_16x16x32_bf16 v[208:211], v[120:123], v[140:143], v[208:211]\n\t" \
  "v_mfma_f32_16x16x32_bf16 v[252:255], v[112:115], v[116:119], v[252:255]\n\t" \
  "v_mfma_f32_16x16x32_bf16 v[188:191], v[112:115], v[136:139], v[188:191]\n\t" \
  "v_mfma_f32_16x16x32_bf16 v[196:199], v[112:115], v[108:111], v[196:199]\n\t" \
  "v_cvt_pk_bf16_f32 v10, v248, v249\n\t" \
  "v_cvt_pk_bf16_f32 v11, v250, v251\n\t" \
  "v_mfma_f32_16x16x32_bf16 v[204:207], v[112:115], v[144:147], v[204:207]\n\t" \
  "v_cvt_pk_bf16_f32 v14, v184, v185\n\t" \
  "v_cvt_pk_bf16_f32 v15, v186, v187\n\t" \
  "v_mfma_f32_16x16x32_bf16 v[212:215], v[112:115], v[140:143], v[212:215]\n\t" \
  "v_cvt_pk_bf16_f32 v12, v252, v253\n\t" \
  "v_cvt_pk_bf16_f32 v13, v254, v255\n\t" \
  "v_mfma_f32_16x16x32_bf16 v[152:155], v[132:135], v[136:139], v[152:155]\n\t" \
  "ds_write_b128 v30, v[10:13]\n\t" \
  "ds_write_b32 v32, v0 offset:48\n\t" \
  "v_mfma_f32_16x16x32_bf16 v[160:163], v[132:135], v[108:111], v[160:163]\n\t" \
  "v_cvt_pk_bf16_f32 v16, v188, v189\n\t" \
  "v_cvt_pk_bf16_f32 v17, v190, v191\n\t" \
  "v_mfma_f32_16x16x32_bf16 v[168:171], v[132:135], v[144:147], v[168:171]\n\t" \
  "v_cvt_pk_bf16_f32 v18, v192, v193\n\t" \
  "v_cvt_pk_bf16_f32 v19, v194, v195\n\t" \
  "v_mfma_f32_16x16x32_bf16 v[176:179], v[132:135], v[140:143], v[176:179]\n\t" \
  "v_cvt_pk_bf16_f32 v20, v196, v197\n\t" \
  "v_cvt_pk_bf16_f32 v21, v198, v199\n\t" \
  "v_mfma_f32_16x16x32_bf16 v[156:159], v[128:131], v[136:139], v[156:159]\n\t" \
  "v_cvt_pk_bf16_f32 v22, v200, v201\n\t" \
  "v_cvt_pk_bf16_f32 v23, v202, v203\n\t" \
  "v_mfma_f32_16x16x32_bf16 v[164:167], v[128:131], v[108:111], v[164:167]\n\t" \
  "v_cvt_pk_bf16_f32 v24, v204, v205\n\t" \
  "v_cvt_pk_bf16_f32 v25, v206, v207\n\t" \
  "v_mfma_f32_16x16x32_bf16 v[172:175], v[128:131], v[144:147], v[172:175]\n\t" \
  "v_cvt_pk_bf16_f32 v26, v208, v209\n\t" \
  "v_cvt_pk_bf16_f32 v27, v210, v211\n\t" \
  "v_mfma_f32_16x16x32_bf16 v[180:183], v[128:131], v[140:143], v[180:183]\n\t" \
  "v_cvt_pk_bf16_f32 v28, v212, v213\n\t" \
  "v_cvt_pk_bf16_f32 v29, v214, v215\n\t" \
  "v_mfma_f32_16x16x32_bf16 v[216:219], v[136:139], v[124:127], v[216:219]\n\t" \
  "v_cvt_pk_bf16_f32 v84, v152, v153\n\t" \
  "v_cvt_pk_bf16_f32 v85, v154, v155\n\t" \
  "v_mfma_f32_16x16x32_bf16 v[224:227], v[108:111], v[124:127], v[224:227]\n\t" \
  "v_cvt_pk_bf16_f32 v86, v156, v157\n\t" \
  "v_cvt_pk_bf16_f32 v87, v158, v159\n\t" \
  "v_mfma_f32_16x16x32_bf16 v[232:235], v[144:147], v[124:127], v[232:235]\n\t" \
  "v_cvt_pk_bf16_f32 v88, v160, v161\n\t" \
  "v_cvt_pk_bf16_f32 v89, v162, v163\n\t" \
  "v_mfma_f32_16x16x32_bf16 v[240:243], v[140:143], v[124:127], v[240:243]\n\t" \
  "v_cvt_pk_bf16_f32 v90, v164, v165\n\t" \
  "v_cvt_pk_bf16_f32 v91, v166, v167\n\t" \
  "v_mfma_f32_16x16x32_bf16 v[220:223], v[136:139], v[148:151], v[220:223]\n\t" \
  "v_cvt_pk_bf16_f32 v92, v168, v169\n\t" \
  "v_cvt_pk_bf16_f32 v93, v170, v171\n\t" \
  "v_mfma_f32_16x16x32_bf16 v[228:231], v[108:111], v[148:151], v[228:231]\n\t" \
  "v_cvt_pk_bf16_f32 v94, v172, v173\n\t" \
  "v_cvt_pk_bf16_f32 v95, v174, v175\n\t" \
  "v_mfma_f32_16x16x32_bf16 v[236:239], v[144:147], v[148:151], v[236:239]\n\t" \
  "v_cvt_pk_bf16_f32 v96, v176, v177\n\t" \
  "v_cvt_pk_bf16_f32 v97, v178, v179\n\t" \
  "v_mfma_f32_16x16x32_bf16 v[244:247], v[140:143], v[148:151], v[244:247]\n\t" \
  "v_cvt_pk_bf16_f32 v98, v180, v181\n\t" \
  "v_cvt_pk_bf16_f32 v99, v182, v183\n\t" \
  "v_mfma_f32_16x16x32_bf16 v[200:203], v[22:25], v[92:95], v[34:37]\n\t" \
  "v_cvt_pk_bf16_f32 v66, v232, v233\n\t" \
  "v_cvt_pk_bf16_f32 v67, v234, v235\n\t" \
  "v_mfma_f32_16x16x32_bf16 v[208:211], v[26:29], v[96:99], v[34:37]\n\t" \
  "v_cvt_pk_bf16_f32 v76, v236, v237\n\t" \
  "v_cvt_pk_bf16_f32 v77, v238, v239\n\t" \
  "v_mfma_f32_16x16x32_bf16 v[212:215], v[22:25], v[96:99], v[38:41]\n\t" \
  "v_cvt_pk_bf16_f32 v64, v240, v241\n\t" \
  "v_cvt_pk_bf16_f32 v65, v242, v243\n\t" \
  "v_mfma_f32_16x16x32_bf16 v[204:207], v[18:21], v[92:95], v[38:41]\n\t" \
  "v_cvt_pk_bf16_f32 v74, v244, v245\n\t" \
  "v_cvt_pk_bf16_f32 v75, v246, v247\n\t" \
  "v_mfma_f32_16x16x32_bf16 v[192:195], v[18:21], v[88:91], v[34:37]\n\t" \
  "v_cvt_pk_bf16_f32 v68, v224, v225\n\t" \
  "v_cvt_pk_bf16_f32 v69, v226, v227\n\t" \
  "v_mfma_f32_16x16x32_bf16 v[196:199], v[14:17], v[88:91], v[38:41]\n\t" \
  "v_max_f32 v208, v208, v212\n\t" \
  "v_max_f32 v209, v209, v213\n\t" \
  "v_mfma_f32_16x16x32_bf16 v[184:187], v[14:17], v[84:87], v[34:37]\n\t" \
  "v_max_f32 v210, v210, v214\n\t" \
  "v_max_f32 v200, v200, v204\n\t" \
  "v_mfma_f32_16x16x32_bf16 v[188:191], v[10:13], v[84:87], v[42:45]\n\t" \
  "v_max_f32 v201, v201, v205\n\t" \
  "v_max_f32 v202, v202, v206\n\t" \
  "v_max_f32 v192, v192, v196\n\t" \
  "v_max_f32 v193, v193, v197\n\t" \
  "v_max_f32 v194, v194, v198\n\t" \
  "v_max_f32 v211, v211, v215\n\t" \
  "ds_read_b32 v52, v33 offset:48\n\t" \
  "ds_read_b64 v[72:73], v31\n\t" \
  "ds_read_b64 v[82:83], v31 offset:8\n\t" \
  "v_max3_f32 v106, v208, v209, v210\n\t" \
  "v_max_f32 v184, v184, v188\n\t" \
  "v_max_f32 v185, v185, v189\n\t" \
  "v_max_f32 v186, v186, v190\n\t" \
  "v_max_f32 v203, v203, v207\n\t" \
  "v_max3_f32 v104, v200, v201, v202\n\t" \
  "v_max_f32 v195, v195, v199\n\t" \
  "v_max3_f32 v102, v192, v193, v194\n\t" \
  "v_max_f32 v187, v187, v191\n\t" \
  "v_max3_f32 v100, v184, v185, v186\n\t" \
  "v_max_f32 v106, v106, v211\n\t" \
  "v_max_f32 v104, v104, v203\n\t" \
  "v_max_f32 v102, v102, v195\n\t" \
  "v_max_f32 v100, v100, v187\n\t" \
  "v_permlane16_swap_b32 v104, v106\n\t" \
  "v_cvt_pk_bf16_f32 v78, v228, v229\n\t" \
  "v_permlane16_swap_b32 v100, v102\n\t" \
  "v_max_f32 v104, v104, v106\n\t" \
  "v_max_f32 v100, v100, v102\n\t" \
  "v_cvt_pk_bf16_f32 v79, v230, v231\n\t" \
  "v_cvt_pk_bf16_f32 v70, v216, v217\n\t" \
  "v_permlane32_swap_b32 v100, v104\n\t" \
  "v_cvt_pk_bf16_f32 v71, v218, v219\n\t" \
  "v_max_f32 v100, v100, v104\n\t" \
  "v_mul_f32 v100, 0xbe8293ee, v100\n\t" \
  "v_mov_b32 v104, v100\n\t" \
  "v_cvt_pk_bf16_f32 v80, v220, v221\n\t" \
  "v_cvt_pk_bf16_f32 v81, v222, v223\n\t" \
  "v_permlane32_swap_b32 v100, v104\n\t" \
  "s_waitcnt lgkmcnt(0)\n\t" \
  "v_readfirstlane_b32 vcc_lo, v52\n\t" \
  "s_nop 1\n\t" \
  "s_cmp_eq_u32 vcc_lo, 0\n\t" \
  "s_cbranch_scc1 5f\n\t" \
  "4:\n\t" \
  "v_mov_b32 v106, v104\n\t" \
  "v_mov_b32 v102, v100\n\t" \
  "s_nop 0\n\t" \
  "v_permlane16_swap_b32 v104, v106\n\t" \
  "v_permlane16_swap_b32 v100, v102\n\t" \
  "v_fma_f32 v208, v208, %16, v106\n\t" \
  "v_fma_f32 v209, v209, %16, v106\n\t" \
  "v_fma_f32 v210, v210, %16, v106\n\t" \
  "v_fma_f32 v211, v211, %16, v106\n\t" \
  "v_exp_f32 v208, v208\n\t" \
  "v_exp_f32 v209, v209\n\t" \
  "v_exp_f32 v210, v210\n\t" \
  "v_exp_f32 v211, v211\n\t" \
  "v_cvt_pk_bf16_f32 v212, v208, v209\n\t" \
  "v_cvt_pk_bf16_f32 v213, v210, v211\n\t" \
  "v_bfi_b32 v214, v46, 0, v212\n\t" \
  "v_bfi_b32 v215, v47, 0, v213\n\t" \
  "v_and_b32 v212, v46, v212\n\t" \
  "v_and_b32 v213, v47, v213\n\t" \
  "v_fma_f32 v200, v200, %16, v104\n\t" \
  "v_fma_f32 v201, v201, %16, v104\n\t" \
  "v_mfma_f32_16x16x32_bf16 v[208:211], v[48:51], v[212:215], 0\n\t" \
  "v_fma_f32 v202, v202, %16, v104\n\t" \
  "v_fma_f32 v203, v203, %16, v104\n\t" \
  "v_mfma_f32_16x16x32_bf16 v[176:179], v[64:67], v[212:215], 0\n\t" \
  "v_exp_f32 v200, v200\n\t" \
  "v_mfma_f32_16x16x32_bf16 v[180:183], v[74:77], v[212:215], 0\n\t" \
  "v_exp_f32 v201, v201\n\t" \
  "v_exp_f32 v202, v202\n\t" \
  "v_exp_f32 v203, v203\n\t" \
  "v_cvt_pk_bf16_f32 v204, v200, v201\n\t" \
  "v_cvt_pk_bf16_f32 v205, v202, v203\n\t" \
  "v_bfi_b32 v206, v46, 0, v204\n\t" \
  "v_bfi_b32 v207, v47, 0, v205\n\t" \
  "v_rcp_f32 v106, v208\n\t" \
  "v_and_b32 v204, v46, v204\n\t" \
  "v_and_b32 v205, v47, v205\n\t" \
  "v_pk_mul_f32 v[176:177], v[176:177], v[106:107] op_sel_hi:[1,0]\n\t" \
  "v_pk_mul_f32 v[178:179], v[178:179], v[106:107] op_sel_hi:[1,0]\n\t" \
  "v_mfma_f32_16x16x32_bf16 v[200:203], v[48:51], v[204:207], 0\n\t" \
  "v_pk_mul_f32 v[180:181], v[180:181], v[106:107] op_sel_hi:[1,0]\n\t" \
  "v_pk_mul_f32 v[182:183], v[182:183], v[106:107] op_sel_hi:[1,0]\n\t" \
  "v_mfma_f32_16x16x32_bf16 v[168:171], v[66:69], v[204:207], 0\n\t" \
  "v_cvt_pk_bf16_f32 v240, v176, v177\n\t" \
  "v_cvt_pk_bf16_f32 v241, v178, v179\n\t" \
  "v_mfma_f32_16x16x32_bf16 v[172:175], v[76:79], v[204:207], 0\n\t" \
  "v_cvt_pk_bf16_f32 v242, v180, v181\n\t" \
  "v_cvt_pk_bf16_f32 v243, v182, v183\n\t" \
  "global_store_dwordx4 v1, v[240:243], %14 offset:0 sc1\n\t" \
  "v_fma_f32 v192, v192, %16, v102\n\t" \
  "v_fma_f32 v193, v193, %16, v102\n\t" \
  "v_fma_f32 v194, v194, %16, v102\n\t" \
  "v_fma_f32 v195, v195, %16, v102\n\t" \
  "v_exp_f32 v192, v192\n\t" \
  "v_exp_f32 v193, v193\n\t" \
  "v_exp_f32 v194, v194\n\t" \
  "v_exp_f32 v195, v195\n\t" \
  "v_cvt_pk_bf16_f32 v196, v192, v193\n\t" \
  "v_cvt_pk_bf16_f32 v197, v194, v195\n\t" \
  "v_bfi_b32 v198, v46, 0, v196\n\t" \
  "v_bfi_b32 v199, v47, 0, v197\n\t" \
  "v_rcp_f32 v104, v200\n\t" \
  "v_and_b32 v196, v46, v196\n\t" \
  "v_and_b32 v197, v47, v197\n\t" \
  "v_pk_mul_f32 v[168:169], v[168:169], v[104:105] op_sel_hi:[1,0]\n\t" \
  "v_pk_mul_f32 v[170:171], v[170:171], v[104:105] op_sel_hi:[1,0]\n\t" \
  "v_mfma_f32_16x16x32_bf16 v[192:195], v[48:51], v[196:199], 0\n\t" \
  "v_pk_mul_f32 v[172:173], v[172:173], v[104:105] op_sel_hi:[1,0]\n\t" \
  "v_pk_mul_f32 v[174:175], v[174:175], v[104:105] op_sel_hi:[1,0]\n\t" \
  "v_mfma_f32_16x16x32_bf16 v[160:163], v[68:71], v[196:199], 0\n\t" \
  "v_cvt_pk_bf16_f32 v232, v168, v169\n\t" \
  "v_cvt_pk_bf16_f32 v233, v170, v171\n\t" \
  "v_mfma_f32_16x16x32_bf16 v[164:167], v[78:81], v[196:199], 0\n\t" \
  "v_cvt_pk_bf16_f32 v234, v172, v173\n\t" \
  "v_cvt_pk_bf16_f32 v235, v174, v175\n\t" \
  "global_store_dwordx4 v1, v[232:235], %14 offset:-4096 sc1\n\t" \
  "v_fma_f32 v184, v184, %16, v100\n\t" \
  "v_fma_f32 v185, v185, %16, v100\n\t" \
  "v_fma_f32 v186, v186, %16, v100\n\t" \
  "v_fma_f32 v187, v187, %16, v100\n\t" \
  "v_exp_f32 v184, v184\n\t" \
  "v_exp_f32 v185, v185\n\t" \
  "v_exp_f32 v186, v186\n\t" \
  "v_exp_f32 v187, v187\n\t" \
  "v_cvt_pk_bf16_f32 v188, v184, v185\n\t" \
  "v_cvt_pk_bf16_f32 v189, v186, v187\n\t" \
  "v_bfi_b32 v190, v46, 0, v188\n\t" \
  "v_bfi_b32 v191, v47, 0, v189\n\t" \
  "v_rcp_f32 v102, v192\n\t" \
  "v_and_b32 v188, v46, v188\n\t" \
  "v_and_b32 v189, v47, v189\n\t" \
  "v_pk_mul_f32 v[160:161], v[160:161], v[102:103] op_sel_hi:[1,0]\n\t" \
  "v_pk_mul_f32 v[162:163], v[162:163], v[102:103] op_sel_hi:[1,0]\n\t" \
  "v_mfma_f32_16x16x32_bf16 v[184:187], v[48:51], v[188:191], 0\n\t" \
  "v_pk_mul_f32 v[164:165], v[164:165], v[102:103] op_sel_hi:[1,0]\n\t" \
  "v_pk_mul_f32 v[166:167], v[166:167], v[102:103] op_sel_hi:[1,0]\n\t" \
  "v_mfma_f32_16x16x32_bf16 v[152:155], v[70:73], v[188:191], 0\n\t" \
  "v_cvt_pk_bf16_f32 v224, v160, v161\n\t" \
  "v_cvt_pk_bf16_f32 v225, v162, v163\n\t" \
  "v_mfma_f32_16x16x32_bf16 v[156:159], v[80:83], v[188:191], 0\n\t" \
  "v_cvt_pk_bf16_f32 v226, v164, v165\n\t" \
  "v_cvt_pk_bf16_f32 v227, v166, v167\n\t" \
  "global_store_dwordx4 v4, v[224:227], %14 offset:0 sc1\n\t" \
  "v_rcp_f32 v100, v184\n\t" \
  "ds_read_b128 v[60:63], v56 offset:64\n\t" \
  "ds_read_b64_tr_b16 v[108:109], v5 offset:0\n\t" \
  "ds_read_b64_tr_b16 v[110:111], v5 offset:16\n\t" \
  "ds_read_b128 v[112:115], v6 offset:16384\n\t" \
  "ds_read_b128 v[116:119], v6 offset:15360\n\t" \
  "506:\n\t" \
  "s_cbranch_execz 510f\n\t" \
  "ds_read_b64_tr_b16 v[120:121], v5 offset:512\n\t" \
  "ds_read_b64_tr_b16 v[122:123], v5 offset:528\n\t" \
  "ds_read_b128 v[124:127], v6 offset:14336\n\t" \
  "ds_read_b64_tr_b16 v[128:129], v5 offset:1024\n\t" \
  "ds_read_b64_tr_b16 v[130:131], v5 offset:1040\n\t" \
  "ds_read_b128 v[132:135], v6 offset:13312\n\t" \
  "ds_read_b64_tr_b16 v[136:137], v5 offset:1536\n\t" \
  "ds_read_b64_tr_b16 v[138:139], v5 offset:1552\n\t" \
  "ds_read_b128 v[140:143], v6 offset:12288\n\t" \
  "ds_read_b64_tr_b16 v[144:145], v5 offset:2048\n\t" \
  "v_pk_mul_f32 v[152:153], v[152:153], v[100:101] op_sel_hi:[1,0]\n\t" \
  "v_pk_mul_f32 v[154:155], v[154:155], v[100:101] op_sel_hi:[1,0]\n\t" \
  "v_cvt_pk_bf16_f32 v216, v152, v153\n\t" \
  "v_pk_mul_f32 v[156:157], v[156:157], v[100:101] op_sel_hi:[1,0]\n\t" \
  "v_pk_mul_f32 v[158:159], v[158:159], v[100:101] op_sel_hi:[1,0]\n\t" \
  "v_cvt_pk_bf16_f32 v217, v154, v155\n\t" \
  "v_cvt_pk_bf16_f32 v218, v156, v157\n\t" \
  "v_cvt_pk_bf16_f32 v219, v158, v159\n\t" \
  "global_store_dwordx4 v4, v[216:219], %14 offset:-4096 sc1\n\t" \
  "ds_write_b32 v32, v0 offset:112\n\t" \
  "s_branch 9f\n\t" \
  "5:\n\t" \
  "ds_read_b32 v52, v33 offset:48\n\t" \
  "ds_read_b64 v[72:73], v31\n\t" \
  "ds_read_b64 v[82:83], v31 offset:8\n\t" \
  "s_waitcnt lgkmcnt(0)\n\t" \
  "v_readfirstlane_b32 vcc_lo, v52\n\t" \
  "s_nop 1\n\t" \
  "s_cmp_eq_u32 vcc_lo, 0\n\t" \
  "s_cbranch_scc1 5b\n\t" \
  "s_branch 4b\n\t" \
  "15:\n\t" \
  "ds_read_b128 v[10:13], v14 offset:128\n\t" \
  "s_waitcnt lgkmcnt(0)\n\t" \
  "v_and_b32 v10, v10, v11\n\t" \
  "v_and_b32 v12, v12, v13\n\t" \
  "v_and_b32 v10, v10, v12\n\t" \
  "s_nop 1\n\t" \
  "v_readfirstlane_b32 vcc_lo, v10\n\t" \
  "s_nop 1\n\t" \
  "s_cmp_eq_u32 vcc_lo, 0\n\t" \
  "s_cbranch_scc1 15b\n\t" \
  "s_branch 14b\n\t" \
  "9:\n\t" \
  "507:\n\t" \
  "s_cbranch_execz 511f\n\t" \
  "s_waitcnt lgkmcnt(14)\n\t" \
  "v_and_b32 v60, v60, v61\n\t" \
  "v_and_b32 v62, v62, v63\n\t" \
  "v_mov_b32 v12, %13\n\t" \
  "v_and_b32 v60, v60, v62\n\t" \
  "s_nop 0\n\t" \
  "v_readfirstlane_b32 vcc_lo, v60\n\t" \
  "s_nop 1\n\t" \
  "s_cmp_eq_u32 vcc_lo, 0\n\t" \
  "s_cbranch_scc1 7f\n\t" \
  "6:\n\t" \
  "s_waitcnt lgkmcnt(11)\n\t" \
  "v_mfma_f32_16x16x32_bf16 v[192:195], v[108:111], v[112:115], 0\n\t" \
  "ds_read_b64_tr_b16 v[146:147], v5 offset:2064\n\t" \
  "ds_read_b128 v[148:151], v6 offset:11264\n\t" \
  "s_waitcnt lgkmcnt(12)\n\t" \
  "v_mfma_f32_16x16x32_bf16 v[196:199], v[108:111], v[116:119], 0\n\t" \
  "ds_read_b64_tr_b16 v[64:65], v5 offset:2560\n\t" \
  "ds_read_b64_tr_b16 v[66:67], v5 offset:2576\n\t" \
  "s_waitcnt lgkmcnt(12)\n\t" \
  "v_mfma_f32_16x16x32_bf16 v[196:199], v[120:123], v[112:115], v[196:199]\n\t" \
  "ds_read_b128 v[68:71], v6 offset:10240\n\t" \
  "ds_read_b64_tr_b16 v[72:73], v5 offset:3072\n\t" \
  "s_waitcnt lgkmcnt(13)\n\t" \
  "v_mfma_f32_16x16x32_bf16 v[200:203], v[108:111], v[124:127], 0\n\t" \
  "ds_read_b64_tr_b16 v[74:75], v5 offset:3088\n\t" \
  "ds_read_b128 v[76:79], v6 offset:9216\n\t" \
  "v_mfma_f32_16x16x32_bf16 v[200:203], v[120:123], v[116:119], v[200:203]\n\t" \
  "ds_read_b64_tr_b16 v[80:81], v5 offset:3584\n\t" \
  "ds_read_b64_tr_b16 v[82:83], v5 offset:3600\n\t" \
  "s_waitcnt lgkmcnt(15)\n\t" \
  "v_mfma_f32_16x16x32_bf16 v[200:203], v[128:131], v[112:115], v[200:203]\n\t" \
  "ds_read_b128 v[84:87], v6 offset:0\n\t" \
  "ds_read_b128 v[88:91], v6 offset:1024\n\t" \
  "s_waitcnt lgkmcnt(15)\n\t" \
  "v_mfma_f32_16x16x32_bf16 v[204:207], v[108:111], v[132:135], 0\n\t" \
  "ds_read_b128 v[92:95], v6 offset:2048\n\t" \
  "ds_read_b128 v[96:99], v6 offset:3072\n\t" \
  "v_mfma_f32_16x16x32_bf16 v[204:207], v[120:123], v[124:127], v[204:207]\n\t" \
  "ds_read_b128 v[100:103], v6 offset:4096\n\t" \
  "ds_read_b128 v[104:107], v6 offset:5120\n\t" \
  "v_mfma_f32_16x16x32_bf16 v[204:207], v[128:131], v[116:119], v[204:207]\n\t" \
  "ds_read_b128 v[152:155], v6 offset:6144\n\t" \
  "ds_read_b128 v[156:159], v6 offset:7168\n\t" \
  "s_waitcnt lgkmcnt(14)\n\t" \
  "v_mfma_f32_16x16x32_bf16 v[204:207], v[136:139], v[112:115], v[204:207]\n\t" \
  "v_lshl_add_u32 v12, v12, 12, v4\n\t" \
  "s_lshl_b32 s88, %13, 5\n\t" \
  "v_mfma_f32_16x16x32_bf16 v[208:211], v[108:111], v[140:143], 0\n\t" \
  "v_mfma_f32_16x16x32_bf16 v[208:211], v[120:123], v[132:135], v[208:211]\n\t" \
  "v_mfma_f32_16x16x32_bf16 v[208:211], v[128:131], v[124:127], v[208:211]\n\t" \
  "s_and_b32 s88, s88, 64\n\t" \
  "v_xor_b32 v13, s88, v54\n\t" \
  "v_mfma_f32_16x16x32_bf16 v[208:211], v[136:139], v[116:119], v[208:211]\n\t" \
  "v_mfma_f32_16x16x32_bf16 v[208:211], v[144:147], v[112:115], v[208:211]\n\t" \
  "v_mfma_f32_16x16x32_bf16 v[212:215], v[108:111], v[148:151], 0\n\t" \
  "ds_read_b32 v55, v56 offset:104\n\t" \
  "v_mfma_f32_16x16x32_bf16 v[212:215], v[120:123], v[140:143], v[212:215]\n\t" \
  "v_mfma_f32_16x16x32_bf16 v[212:215], v[128:131], v[132:135], v[212:215]\n\t" \
  "v_bfe_u32 v11, v12, 4, 4\n\t" \
  "s_mov_b32 s74, 0\n\t" \
  "v_mfma_f32_16x16x32_bf16 v[212:215], v[136:139], v[124:127], v[212:215]\n\t" \
  "v_lshlrev_b32 v11, 7, v11\n\t" \
  "v_mfma_f32_16x16x32_bf16 v[212:215], v[144:147], v[116:119], v[212:215]\n\t" \
  "v_mov_b32 v10, 1\n\t" \
  "v_mfma_f32_16x16x32_bf16 v[212:215], v[64:67], v[112:115], v[212:215]\n\t" \
  "s_getpc_b64 s[70:71]\n\t" \
  "24:\n\t" \
  "s_add_u32 s70, s70, 23f-24b\n\t" \
  "s_addc_u32 s71, s71, 0\n\t" \
  "s_waitcnt lgkmcnt(1)\n\t" \
  "v_mfma_f32_16x16x32_bf16 v[216:219], v[108:111], v[68:71], 0\n\t" \
  "v_mfma_f32_16x16x32_bf16 v[216:219], v[120:123], v[148:151], v[216:219]\n\t" \
  "v_and_b32 v1, 0x180, v4\n\t" \
  "v_mfma_f32_16x16x32_bf16 v[216:219], v[128:131], v[140:143], v[216:219]\n\t" \
  "v_and_b32 v2, 0x70, v4\n\t" \
  "v_mfma_f32_16x16x32_bf16 v[216:219], v[136:139], v[132:135], v[216:219]\n\t" \
  "v_lshlrev_b32 v2, 3, v2\n\t" \
  "v_mfma_f32_16x16x32_bf16 v[216:219], v[144:147], v[124:127], v[216:219]\n\t" \
  "v_lshl_add_u32 v1, v1, 5, v2\n\t" \
  "v_mfma_f32_16x16x32_bf16 v[216:219], v[64:67], v[116:119], v[216:219]\n\t" \
  "v_and_b32 v2, 0x2000, v1\n\t" \
  "v_mfma_f32_16x16x32_bf16 v[216:219], v[72:75], v[112:115], v[216:219]\n\t" \
  "v_add_u32 v1, v1, v2\n\t" \
  "v_mfma_f32_16x16x32_bf16 v[220:223], v[108:111], v[76:79], 0\n\t" \
  "v_mfma_f32_16x16x32_bf16 v[220:223], v[120:123], v[68:71], v[220:223]\n\t" \
  "v_mfma_f32_16x16x32_bf16 v[220:223], v[128:131], v[148:151], v[220:223]\n\t" \
  "v_mfma_f32_16x16x32_bf16 v[220:223], v[136:139], v[140:143], v[220:223]\n\t" \
  "v_mfma_f32_16x16x32_bf16 v[220:223], v[144:147], v[132:135], v[220:223]\n\t" \
  "ds_read_b64_tr_b16 v[160:161], v5 offset:128\n\t" \
  "v_mfma_f32_16x16x32_bf16 v[220:223], v[64:67], v[124:127], v[220:223]\n\t" \
  "ds_read_b64_tr_b16 v[162:163], v5 offset:144\n\t" \
  "v_mfma_f32_16x16x32_bf16 v[220:223], v[72:75], v[116:119], v[220:223]\n\t" \
  "ds_read_b128 v[164:167], v7 offset:16384\n\t" \
  "v_mfma_f32_16x16x32_bf16 v[220:223], v[80:83], v[112:115], v[220:223]\n\t" \
  "ds_read_b128 v[168:171], v7 offset:15360\n\t" \
  "v_mfma_f32_16x16x32_bf16 v[252:255], v[108:111], v[84:87], 0\n\t" \
  "ds_read_b64_tr_b16 v[172:173], v5 offset:640\n\t" \
  "v_mfma_f32_16x16x32_bf16 v[252:255], v[120:123], v[88:91], v[252:255]\n\t" \
  "ds_read_b64_tr_b16 v[174:175], v5 offset:656\n\t" \
  "v_mfma_f32_16x16x32_bf16 v[252:255], v[128:131], v[92:95], v[252:255]\n\t" \
  "ds_read_b128 v[176:179], v7 offset:14336\n\t" \
  "v_mfma_f32_16x16x32_bf16 v[252:255], v[136:139], v[96:99], v[252:255]\n\t" \
  "ds_read_b64_tr_b16 v[180:181], v5 offset:1152\n\t" \
  "v_mfma_f32_16x16x32_bf16 v[252:255], v[144:147], v[100:103], v[252:255]\n\t" \
  "ds_read_b64_tr_b16 v[182:183], v5 offset:1168\n\t" \
  "v_mfma_f32_16x16x32_bf16 v[252:255], v[64:67], v[104:107], v[252:255]\n\t" \
  "ds_read_b128 v[184:187], v7 offset:13312\n\t" \
  "v_mfma_f32_16x16x32_bf16 v[252:255], v[72:75], v[152:155], v[252:255]\n\t" \
  "ds_read_b64_tr_b16 v[188:189], v5 offset:1664\n\t" \
  "v_mfma_f32_16x16x32_bf16 v[252:255], v[80:83], v[156:159], v[252:255]\n\t" \
  "ds_read_b64_tr_b16 v[190:191], v5 offset:1680\n\t" \
  "v_mfma_f32_16x16x32_bf16 v[248:251], v[108:111], v[88:91], 0\n\t" \
  "ds_read_b128 v[76:79], v7 offset:12288\n\t" \
  "v_mfma_f32_16x16x32_bf16 v[248:251], v[120:123], v[92:95], v[248:251]\n\t" \
  "v_mfma_f32_16x16x32_bf16 v[248:251], v[128:131], v[96:99], v[248:251]\n\t" \
  "ds_read_b64_tr_b16 v[68:69], v5 offset:2176\n\t" \
  "v_mfma_f32_16x16x32_bf16 v[248:251], v[136:139], v[100:103], v[248:251]\n\t" \
  "ds_read_b64_tr_b16 v[70:71], v5 offset:2192\n\t" \
  "v_mfma_f32_16x16x32_bf16 v[248:251], v[144:147], v[104:107], v[248:251]\n\t" \
  "ds_read_b128 v[148:151], v7 offset:11264\n\t" \
  "v_mfma_f32_16x16x32_bf16 v[248:251], v[64:67], v[152:155], v[248:251]\n\t" \
  "v_mfma_f32_16x16x32_bf16 v[248:251], v[72:75], v[156:159], v[248:251]\n\t" \
  "v_mfma_f32_16x16x32_bf16 v[244:247], v[108:111], v[92:95], 0\n\t" \
  "v_mfma_f32_16x16x32_bf16 v[244:247], v[120:123], v[96:99], v[244:247]\n\t" \
  "ds_read_b64_tr_b16 v[140:141], v5 offset:2688\n\t" \
  "v_mfma_f32_16x16x32_bf16 v[244:247], v[128:131], v[100:103], v[244:247]\n\t" \
  "ds_read_b64_tr_b16 v[142:143], v5 offset:2704\n\t" \
  "v_mfma_f32_16x16x32_bf16 v[244:247], v[136:139], v[104:107], v[244:247]\n\t" \
  "ds_read_b128 v[132:135], v7 offset:10240\n\t" \
  "v_mfma_f32_16x16x32_bf16 v[244:247], v[144:147], v[152:155], v[244:247]\n\t" \
  "v_mfma_f32_16x16x32_bf16 v[244:247], v[64:67], v[156:159], v[244:247]\n\t" \
  "v_mfma_f32_16x16x32_bf16 v[240:243], v[108:111], v[96:99], 0\n\t" \
  "v_mfma_f32_16x16x32_bf16 v[240:243], v[120:123], v[100:103], v[240:243]\n\t" \
  "v_mfma_f32_16x16x32_bf16 v[240:243], v[128:131], v[104:107], v[240:243]\n\t" \
  "ds_read_b64_tr_b16 v[124:125], v5 offset:3200\n\t" \
  "v_mfma_f32_16x16x32_bf16 v[240:243], v[136:139], v[152:155], v[240:243]\n\t" \
  "ds_read_b64_tr_b16 v[126:127], v5 offset:3216\n\t" \
  "v_mfma_f32_16x16x32_bf16 v[240:243], v[144:147], v[156:159], v[240:243]\n\t" \
  "ds_read_b128 v[116:119], v7 offset:9216\n\t" \
  "v_mfma_f32_16x16x32_bf16 v[236:239], v[108:111], v[100:103], 0\n\t" \
  "v_mfma_f32_16x16x32_bf16 v[236:239], v[120:123], v[104:107], v[236:239]\n\t" \
  "s_getreg_b32 vcc_lo, hwreg(HW_REG_IB_STS, 0, 4)\n\t" \
  "v_mfma_f32_16x16x32_bf16 v[236:239], v[128:131], v[152:155], v[236:239]\n\t" \
  "s_or_b32 vcc_lo, vcc_lo, s74\n\t" \
  "s_cbranch_scc1 30f\n\t" \
  "s_swappc_b64 s[68:69], s[70:71]\n\t" \
  "30:\n\t" \
  "v_mfma_f32_16x16x32_bf16 v[236:239], v[136:139], v[156:159], v[236:239]\n\t" \
  "v_mfma_f32_16x16x32_bf16 v[232:235], v[108:111], v[104:107], 0\n\t" \
  "v_mfma_f32_16x16x32_bf16 v[232:235], v[120:123], v[152:155], v[232:235]\n\t" \
  "ds_read_b64_tr_b16 v[64:65], v5 offset:3712\n\t" \
  "v_mfma_f32_16x16x32_bf16 v[232:235], v[128:131], v[156:159], v[232:235]\n\t" \
  "ds_read_b64_tr_b16 v[66:67], v5 offset:3728\n\t" \
  "v_mfma_f32_16x16x32_bf16 v[228:231], v[108:111], v[152:155], 0\n\t" \
  "ds_read_b128 v[72:75], v7 offset:0\n\t" \
  "v_mfma_f32_16x16x32_bf16 v[228:231], v[120:123], v[156:159], v[228:231]\n\t" \
  "ds_read_b128 v[80:83], v7 offset:1024\n\t" \
  "v_mfma_f32_16x16x32_bf16 v[224:227], v[108:111], v[156:159], 0\n\t" \
  "ds_read_b128 v[84:87], v7 offset:2048\n\t" \
  "s_waitcnt lgkmcnt(5)\n\t" \
  "v_mfma_f32_16x16x32_bf16 v[192:195], v[160:163], v[164:167], v[192:195]\n\t" \
  "ds_read_b128 v[88:91], v7 offset:3072\n\t" \
  "v_mfma_f32_16x16x32_bf16 v[196:199], v[160:163], v[168:171], v[196:199]\n\t" \
  "ds_read_b128 v[92:95], v7 offset:4096\n\t" \
  "v_mfma_f32_16x16x32_bf16 v[196:199], v[172:175], v[164:167], v[196:199]\n\t" \
  "ds_read_b128 v[108:111], v7 offset:5120\n\t" \
  "v_mfma_f32_16x16x32_bf16 v[200:203], v[160:163], v[176:179], v[200:203]\n\t" \
  "ds_read_b128 v[112:115], v7 offset:6144\n\t" \
  "v_mfma_f32_16x16x32_bf16 v[200:203], v[172:175], v[168:171], v[200:203]\n\t" \
  "ds_read_b128 v[120:123], v7 offset:7168\n\t" \
  "v_mfma_f32_16x16x32_bf16 v[200:203], v[180:183], v[164:167], v[200:203]\n\t" \
  "v_mfma_f32_16x16x32_bf16 v[204:207], v[160:163], v[184:187], v[204:207]\n\t" \
  "v_mfma_f32_16x16x32_bf16 v[204:207], v[172:175], v[176:179], v[204:207]\n\t" \
  "v_mfma_f32_16x16x32_bf16 v[204:207], v[180:183], v[168:171], v[204:207]\n\t" \
  "v_mfma_f32_16x16x32_bf16 v[204:207], v[188:191], v[164:167], v[204:207]\n\t" \
  "v_mfma_f32_16x16x32_bf16 v[208:211], v[160:163], v[76:79], v[208:211]\n\t" \
  "v_mfma_f32_16x16x32_bf16 v[208:211], v[172:175], v[184:187], v[208:211]\n\t" \
  "s_getreg_b32 vcc_lo, hwreg(HW_REG_IB_STS, 0, 4)\n\t" \
  "v_mfma_f32_16x16x32_bf16 v[208:211], v[180:183], v[176:179], v[208:211]\n\t" \
  "s_or_b32 vcc_lo, vcc_lo, s74\n\t" \
  "s_cbranch_scc1 31f\n\t" \
  "s_swappc_b64 s[68:69], s[70:71]\n\t" \
  "31:\n\t" \
  "v_mfma_f32_16x16x32_bf16 v[208:211], v[188:191], v[168:171], v[208:211]\n\t" \
  "v_mfma_f32_16x16x32_bf16 v[208:211], v[68:71], v[164:167], v[208:211]\n\t" \
  "v_mfma_f32_16x16x32_bf16 v[212:215], v[160:163], v[148:151], v[212:215]\n\t" \
  "v_mfma_f32_16x16x32_bf16 v[212:215], v[172:175], v[76:79], v[212:215]\n\t" \
  "v_mfma_f32_16x16x32_bf16 v[212:215], v[180:183], v[184:187], v[212:215]\n\t" \
  "v_mfma_f32_16x16x32_bf16 v[212:215], v[188:191], v[176:179], v[212:215]\n\t" \
  "v_mfma_f32_16x16x32_bf16 v[212:215], v[68:71], v[168:171], v[212:215]\n\t" \
  "v_mfma_f32_16x16x32_bf16 v[212:215], v[140:143], v[164:167], v[212:215]\n\t" \
  "v_mfma_f32_16x16x32_bf16 v[216:219], v[160:163], v[132:135], v[216:219]\n\t" \
  "v_mfma_f32_16x16x32_bf16 v[216:219], v[172:175], v[148:151], v[216:219]\n\t" \
  "v_mfma_f32_16x16x32_bf16 v[216:219], v[180:183], v[76:79], v[216:219]\n\t" \
  "v_mfma_f32_16x16x32_bf16 v[216:219], v[188:191], v[184:187], v[216:219]\n\t" \
  "v_mfma_f32_16x16x32_bf16 v[216:219], v[68:71], v[176:179], v[216:219]\n\t" \
  "v_mfma_f32_16x16x32_bf16 v[216:219], v[140:143], v[168:171], v[216:219]\n\t" \
  "v_mfma_f32_16x16x32_bf16 v[216:219], v[124:127], v[164:167], v[216:219]\n\t" \
  "v_mfma_f32_16x16x32_bf16 v[220:223], v[160:163], v[116:119], v[220:223]\n\t" \
  "v_mfma_f32_16x16x32_bf16 v[220:223], v[172:175], v[132:135], v[220:223]\n\t" \
  "v_mfma_f32_16x16x32_bf16 v[220:223], v[180:183], v[148:151], v[220:223]\n\t" \
  "v_mfma_f32_16x16x32_bf16 v[220:223], v[188:191], v[76:79], v[220:223]\n\t" \
  "v_mfma_f32_16x16x32_bf16 v[220:223], v[68:71], v[184:187], v[220:223]\n\t" \
  "s_cmp_lg_u32 s74, 0\n\t" \
  "s_cbranch_scc1 32f\n\t" \
  "s_swappc_b64 s[68:69], s[70:71]\n\t" \
  "32:\n\t" \
  "v_mfma_f32_16x16x32_bf16 v[220:223], v[140:143], v[176:179], v[220:223]\n\t" \
  "ds_read_b64_tr_b16 v[76:77], v5 offset:256\n\t" \
  "v_mfma_f32_16x16x32_bf16 v[220:223], v[124:127], v[168:171], v[220:223]\n\t" \
  "ds_read_b64_tr_b16 v[78:79], v5 offset:272\n\t" \
  "s_waitcnt lgkmcnt(2)\n\t" \
  "v_mfma_f32_16x16x32_bf16 v[220:223], v[64:67], v[164:167], v[220:223]\n\t" \
  "s_getpc_b64 s[70:71]\n\t" \
  "24:\n\t" \
  "s_add_u32 s70, s70, 25f-24b\n\t" \
  "s_addc_u32 s71, s71, 0\n\t" \
  "v_mfma_f32_16x16x32_bf16 v[252:255], v[160:163], v[72:75], v[252:255]\n\t" \
  "ds_read_b128 v[128:131], v8 offset:16384\n\t" \
  "v_mfma_f32_16x16x32_bf16 v[252:255], v[172:175], v[80:83], v[252:255]\n\t" \
  "ds_read_b128 v[136:139], v8 offset:15360\n\t" \
  "v_mfma_f32_16x16x32_bf16 v[252:255], v[180:183], v[84:87], v[252:255]\n\t" \
  "v_and_b32 v53, 0x2000, v12\n\t" \
  "ds_read_b64_tr_b16 v[144:145], v5 offset:768\n\t" \
  "v_mfma_f32_16x16x32_bf16 v[252:255], v[188:191], v[88:91], v[252:255]\n\t" \
  "ds_read_b64_tr_b16 v[146:147], v5 offset:784\n\t" \
  "v_mfma_f32_16x16x32_bf16 v[252:255], v[68:71], v[92:95], v[252:255]\n\t" \
  "v_add_u32 v53, v53, v12\n\t" \
  "ds_read_b128 v[96:99], v8 offset:14336\n\t" \
  "v_mfma_f32_16x16x32_bf16 v[252:255], v[140:143], v[108:111], v[252:255]\n\t" \
  "ds_read_b64_tr_b16 v[72:73], v5 offset:1280\n\t" \
  "v_mfma_f32_16x16x32_bf16 v[252:255], v[124:127], v[112:115], v[252:255]\n\t" \
  "v_lshrrev_b32 v14, 1, v4\n\t" \
  "ds_read_b64_tr_b16 v[74:75], v5 offset:1296\n\t" \
  "v_mfma_f32_16x16x32_bf16 v[252:255], v[64:67], v[120:123], v[252:255]\n\t" \
  "ds_read_b128 v[100:103], v8 offset:13312\n\t" \
  "v_mfma_f32_16x16x32_bf16 v[248:251], v[160:163], v[80:83], v[248:251]\n\t" \
  "v_lshrrev_b32 v15, 24, v3\n\t" \
  "ds_read_b64_tr_b16 v[104:105], v5 offset:1792\n\t" \
  "v_mfma_f32_16x16x32_bf16 v[248:251], v[172:175], v[84:87], v[248:251]\n\t" \
  "ds_read_b64_tr_b16 v[106:107], v5 offset:1808\n\t" \
  "v_mfma_f32_16x16x32_bf16 v[248:251], v[180:183], v[88:91], v[248:251]\n\t" \
  "v_add_u32 v14, %20, v14\n\t" \
  "ds_read_b128 v[152:155], v8 offset:12288\n\t" \
  "v_mfma_f32_16x16x32_bf16 v[248:251], v[188:191], v[92:95], v[248:251]\n\t" \
  "ds_read_b64_tr_b16 v[64:65], v5 offset:2304\n\t" \
  "v_mfma_f32_16x16x32_bf16 v[248:251], v[68:71], v[108:111], v[248:251]\n\t" \
  "v_lshrrev_b32 v18, 10, v12\n\t" \
  "ds_read_b64_tr_b16 v[66:67], v5 offset:2320\n\t" \
  "v_mfma_f32_16x16x32_bf16 v[248:251], v[140:143], v[112:115], v[248:251]\n\t" \
  "ds_read_b128 v[80:83], v8 offset:11264\n\t" \
  "v_mfma_f32_16x16x32_bf16 v[248:251], v[124:127], v[120:123], v[248:251]\n\t" \
  "v_and_b32 v18, 12, v18\n\t" \
  "v_mfma_f32_16x16x32_bf16 v[244:247], v[160:163], v[84:87], v[244:247]\n\t" \
  "v_mfma_f32_16x16x32_bf16 v[244:247], v[172:175], v[88:91], v[244:247]\n\t" \
  "v_mov_b32 v17, 16\n\t" \
  "ds_read_b64_tr_b16 v[84:85], v5 offset:2816\n\t" \
  "v_mfma_f32_16x16x32_bf16 v[244:247], v[180:183], v[92:95], v[244:247]\n\t" \
  "ds_read_b64_tr_b16 v[86:87], v5 offset:2832\n\t" \
  "v_mfma_f32_16x16x32_bf16 v[244:247], v[188:191], v[108:111], v[244:247]\n\t" \
  "v_add_u32 v18, v56, v18\n\t" \
  "ds_read_b128 v[156:159], v8 offset:10240\n\t" \
  "v_mfma_f32_16x16x32_bf16 v[244:247], v[68:71], v[112:115], v[244:247]\n\t" \
  "v_mfma_f32_16x16x32_bf16 v[244:247], v[140:143], v[120:123], v[244:247]\n\t" \
  "s_mov_b32 s76, 0\n\t" \
  "v_mfma_f32_16x16x32_bf16 v[240:243], v[160:163], v[88:91], v[240:243]\n\t" \
  "v_mfma_f32_16x16x32_bf16 v[240:243], v[172:175], v[92:95], v[240:243]\n\t" \
  "s_brev_b32 s77, 1\n\t" \
  "v_mfma_f32_16x16x32_bf16 v[240:243], v[180:183], v[108:111], v[240:243]\n\t" \
  "ds_read_b64_tr_b16 v[88:89], v5 offset:3328\n\t" \
  "v_mfma_f32_16x16x32_bf16 v[240:243], v[188:191], v[112:115], v[240:243]\n\t" \
  "s_mov_b32 s75, 0\n\t" \
  "ds_read_b64_tr_b16 v[90:91], v5 offset:3344\n\t" \
  "v_mfma_f32_16x16x32_bf16 v[240:243], v[68:71], v[120:123], v[240:243]\n\t" \
  "ds_read_b128 v[164:167], v8 offset:9216\n\t" \
  "v_mfma_f32_16x16x32_bf16 v[236:239], v[160:163], v[92:95], v[236:239]\n\t" \
  "v_add_u32 v28, 0, v13\n\t" \
  "v_mfma_f32_16x16x32_bf16 v[236:239], v[172:175], v[108:111], v[236:239]\n\t" \
  "v_mfma_f32_16x16x32_bf16 v[236:239], v[180:183], v[112:115], v[236:239]\n\t" \
  "v_xor_b32 v29, 32, v28\n\t" \
  "v_mfma_f32_16x16x32_bf16 v[236:239], v[188:191], v[120:123], v[236:239]\n\t" \
  "v_mfma_f32_16x16x32_bf16 v[232:235], v[160:163], v[108:111], v[232:235]\n\t" \
  "v_xor_b32 v30, 64, v28\n\t" \
  "v_mfma_f32_16x16x32_bf16 v[232:235], v[172:175], v[112:115], v[232:235]\n\t" \
  "ds_read_b64_tr_b16 v[68:69], v5 offset:3840\n\t" \
  "v_mfma_f32_16x16x32_bf16 v[232:235], v[180:183], v[120:123], v[232:235]\n\t" \
  "v_xor_b32 v31, 96, v28\n\t" \
  "ds_read_b64_tr_b16 v[70:71], v5 offset:3856\n\t" \
  "v_mfma_f32_16x16x32_bf16 v[228:231], v[160:163], v[112:115], v[228:231]\n\t" \
  "ds_read_b128 v[92:95], v8 offset:0\n\t" \
  "v_mfma_f32_16x16x32_bf16 v[228:231], v[172:175], v[120:123], v[228:231]\n\t" \
  "v_add_u32 v32, 65536, v13\n\t" \
  "ds_read_b128 v[168:171], v8 offset:1024\n\t" \
  "v_mfma_f32_16x16x32_bf16 v[224:227], v[160:163], v[120:123], v[224:227]\n\t" \
  "ds_read_b128 v[172:175], v8 offset:2048\n\t" \
  "s_waitcnt lgkmcnt(5)\n\t" \
  "v_mfma_f32_16x16x32_bf16 v[192:195], v[76:79], v[128:131], v[192:195]\n\t" \
  "v_xor_b32 v33, 32, v32\n\t" \
  "ds_read_b128 v[160:163], v8 offset:3072\n\t" \
  "v_mfma_f32_16x16x32_bf16 v[196:199], v[76:79], v[136:139], v[196:199]\n\t" \
  "ds_read_b128 v[176:179], v8 offset:4096\n\t" \
  "v_mfma_f32_16x16x32_bf16 v[196:199], v[144:147], v[128:131], v[196:199]\n\t" \
  "v_xor_b32 v34, 64, v32\n\t" \
  "ds_read_b128 v[180:183], v8 offset:5120\n\t" \
  "v_mfma_f32_16x16x32_bf16 v[200:203], v[76:79], v[96:99], v[200:203]\n\t" \
  "ds_read_b128 v[184:187], v8 offset:6144\n\t" \
  "v_mfma_f32_16x16x32_bf16 v[200:203], v[144:147], v[136:139], v[200:203]\n\t" \
  "v_xor_b32 v35, 96, v32\n\t" \
  "ds_read_b128 v[188:191], v8 offset:7168\n\t" \
  "v_mfma_f32_16x16x32_bf16 v[200:203], v[72:75], v[128:131], v[200:203]\n\t" \
  "v_mfma_f32_16x16x32_bf16 v[204:207], v[76:79], v[100:103], v[204:207]\n\t" \
  "v_add_u32 v36, 131072, v13\n\t" \
  "v_mfma_f32_16x16x32_bf16 v[204:207], v[144:147], v[96:99], v[204:207]\n\t" \
  "v_mfma_f32_16x16x32_bf16 v[204:207], v[72:75], v[136:139], v[204:207]\n\t" \
  "v_xor_b32 v37, 32, v36\n\t" \
  "v_mfma_f32_16x16x32_bf16 v[204:207], v[104:107], v[128:131], v[204:207]\n\t" \
  "v_mfma_f32_16x16x32_bf16 v[208:211], v[76:79], v[152:155], v[208:211]\n\t" \
  "v_xor_b32 v38, 64, v36\n\t" \
  "v_mfma_f32_16x16x32_bf16 v[208:211], v[144:147], v[100:103], v[208:211]\n\t" \
  "v_mfma_f32_16x16x32_bf16 v[208:211], v[72:75], v[96:99], v[208:211]\n\t" \
  "v_xor_b32 v39, 96, v36\n\t" \
  "v_mfma_f32_16x16x32_bf16 v[208:211], v[104:107], v[136:139], v[208:211]\n\t" \
  "v_mfma_f32_16x16x32_bf16 v[208:211], v[64:67], v[128:131], v[208:211]\n\t" \
  "v_lshrrev_b32 v57, 5, v4\n\t" \
  "v_mfma_f32_16x16x32_bf16 v[212:215], v[76:79], v[80:83], v[212:215]\n\t" \
  "v_mfma_f32_16x16x32_bf16 v[212:215], v[144:147], v[152:155], v[212:215]\n\t" \
  "v_and_b32 v57, 0x18, v57\n\t" \
  "v_mfma_f32_16x16x32_bf16 v[212:215], v[72:75], v[100:103], v[212:215]\n\t" \
  "v_mfma_f32_16x16x32_bf16 v[212:215], v[104:107], v[96:99], v[212:215]\n\t" \
  "v_and_b32 v62, 0x70, v4\n\t" \
  "v_mfma_f32_16x16x32_bf16 v[212:215], v[64:67], v[136:139], v[212:215]\n\t" \
  "v_mfma_f32_16x16x32_bf16 v[212:215], v[84:87], v[128:131], v[212:215]\n\t" \
  "v_xor_b32 v57, v57, v62\n\t" \
  "v_mfma_f32_16x16x32_bf16 v[216:219], v[76:79], v[156:159], v[216:219]\n\t" \
  "v_mfma_f32_16x16x32_bf16 v[216:219], v[144:147], v[80:83], v[216:219]\n\t" \
  "v_and_b32 v58, 0xf0, v4\n\t" \
  "v_mfma_f32_16x16x32_bf16 v[216:219], v[72:75], v[152:155], v[216:219]\n\t" \
  "v_mfma_f32_16x16x32_bf16 v[216:219], v[104:107], v[100:103], v[216:219]\n\t" \
  "v_lshl_add_u32 v57, v58, 3, v57\n\t" \
  "v_mfma_f32_16x16x32_bf16 v[216:219], v[64:67], v[96:99], v[216:219]\n\t" \
  "v_mfma_f32_16x16x32_bf16 v[216:219], v[84:87], v[136:139], v[216:219]\n\t" \
  "v_add_u32 v57, %23, v57\n\t" \
  "v_mfma_f32_16x16x32_bf16 v[216:219], v[88:91], v[128:131], v[216:219]\n\t" \
  "v_mfma_f32_16x16x32_bf16 v[220:223], v[76:79], v[164:167], v[220:223]\n\t" \
  "v_xor_b32 v58, 32, v57\n\t" \
  "v_mfma_f32_16x16x32_bf16 v[220:223], v[144:147], v[156:159], v[220:223]\n\t" \
  "v_mfma_f32_16x16x32_bf16 v[220:223], v[72:75], v[80:83], v[220:223]\n\t" \
  "v_xor_b32 v59, 64, v57\n\t" \
  "v_mfma_f32_16x16x32_bf16 v[220:223], v[104:107], v[152:155], v[220:223]\n\t" \
  "v_mfma_f32_16x16x32_bf16 v[220:223], v[64:67], v[100:103], v[220:223]\n\t" \
  "v_xor_b32 v60, 96, v57\n\t" \
  "ds_read_b64_tr_b16 v[148:149], v5 offset:384\n\t" \
  "v_mfma_f32_16x16x32_bf16 v[220:223], v[84:87], v[96:99], v[220:223]\n\t" \
  "ds_read_b64_tr_b16 v[150:151], v5 offset:400\n\t" \
  "v_mfma_f32_16x16x32_bf16 v[220:223], v[88:91], v[136:139], v[220:223]\n\t" \
  "v_lshrrev_b32 v61, 3, v4\n\t" \
  "ds_read_b128 v[80:83], v9 offset:16384\n\t" \
  "s_waitcnt lgkmcnt(3)\n\t" \
  "v_mfma_f32_16x16x32_bf16 v[220:223], v[68:71], v[128:131], v[220:223]\n\t" \
  "ds_read_b128 v[140:143], v9 offset:15360\n\t" \
  "v_mfma_f32_16x16x32_bf16 v[252:255], v[76:79], v[92:95], v[252:255]\n\t" \
  "v_and_b32 v61, 0x70, v61\n\t" \
  "ds_read_b64_tr_b16 v[132:133], v5 offset:896\n\t" \
  "v_mfma_f32_16x16x32_bf16 v[252:255], v[144:147], v[168:171], v[252:255]\n\t" \
  "ds_read_b64_tr_b16 v[134:135], v5 offset:912\n\t" \
  "v_mfma_f32_16x16x32_bf16 v[252:255], v[72:75], v[172:175], v[252:255]\n\t" \
  "v_xor_b32 v61, v61, v4\n\t" \
  "ds_read_b128 v[124:127], v9 offset:14336\n\t" \
  "v_mfma_f32_16x16x32_bf16 v[252:255], v[104:107], v[160:163], v[252:255]\n\t" \
  "ds_read_b64_tr_b16 v[116:117], v5 offset:1408\n\t" \
  "v_mfma_f32_16x16x32_bf16 v[252:255], v[64:67], v[176:179], v[252:255]\n\t" \
  "v_add_u32 v61, %23, v61\n\t" \
  "ds_read_b64_tr_b16 v[118:119], v5 offset:1424\n\t" \
  "v_mfma_f32_16x16x32_bf16 v[252:255], v[84:87], v[180:183], v[252:255]\n\t" \
  "ds_read_b128 v[92:95], v9 offset:13312\n\t" \
  "v_mfma_f32_16x16x32_bf16 v[252:255], v[88:91], v[184:187], v[252:255]\n\t" \
  "v_and_b32 v63, 0x380, v4\n\t" \
  "ds_read_b64_tr_b16 v[108:109], v5 offset:1920\n\t" \
  "v_mfma_f32_16x16x32_bf16 v[252:255], v[68:71], v[188:191], v[252:255]\n\t" \
  "ds_read_b64_tr_b16 v[110:111], v5 offset:1936\n\t" \
  "v_mfma_f32_16x16x32_bf16 v[248:251], v[76:79], v[168:171], v[248:251]\n\t" \
  "v_lshl_add_u32 v62, v63, 6, v62\n\t" \
  "ds_read_b128 v[112:115], v9 offset:12288\n\t" \
  "v_mfma_f32_16x16x32_bf16 v[248:251], v[144:147], v[172:175], v[248:251]\n\t" \
  "v_mfma_f32_16x16x32_bf16 v[248:251], v[72:75], v[160:163], v[248:251]\n\t" \
  "v_add_u32 v63, 0x10000, v62\n\t" \
  "ds_read_b64_tr_b16 v[120:121], v5 offset:2432\n\t" \
  "v_mfma_f32_16x16x32_bf16 v[248:251], v[104:107], v[176:179], v[248:251]\n\t" \
  "ds_read_b64_tr_b16 v[122:123], v5 offset:2448\n\t" \
  "v_mfma_f32_16x16x32_bf16 v[248:251], v[64:67], v[180:183], v[248:251]\n\t" \
  "ds_read_b128 v[68:71], v9 offset:11264\n\t" \
  "v_mfma_f32_16x16x32_bf16 v[248:251], v[84:87], v[184:187], v[248:251]\n\t" \
  "v_mfma_f32_16x16x32_bf16 v[248:251], v[88:91], v[188:191], v[248:251]\n\t" \
  "v_mfma_f32_16x16x32_bf16 v[244:247], v[76:79], v[172:175], v[244:247]\n\t" \
  "v_mfma_f32_16x16x32_bf16 v[244:247], v[144:147], v[160:163], v[244:247]\n\t" \
  "ds_read_b64_tr_b16 v[128:129], v5 offset:2944\n\t" \
  "v_mfma_f32_16x16x32_bf16 v[244:247], v[72:75], v[176:179], v[244:247]\n\t" \
  "ds_read_b64_tr_b16 v[130:131], v5 offset:2960\n\t" \
  "v_mfma_f32_16x16x32_bf16 v[244:247], v[104:107], v[180:183], v[244:247]\n\t" \
  "ds_read_b128 v[88:91], v9 offset:10240\n\t" \
  "v_mfma_f32_16x16x32_bf16 v[244:247], v[64:67], v[184:187], v[244:247]\n\t" \
  "v_mfma_f32_16x16x32_bf16 v[244:247], v[84:87], v[188:191], v[244:247]\n\t" \
  "v_mfma_f32_16x16x32_bf16 v[240:243], v[76:79], v[160:163], v[240:243]\n\t" \
  "v_mfma_f32_16x16x32_bf16 v[240:243], v[144:147], v[176:179], v[240:243]\n\t" \
  "v_mfma_f32_16x16x32_bf16 v[240:243], v[72:75], v[180:183], v[240:243]\n\t" \
  "ds_read_b64_tr_b16 v[136:137], v5 offset:3456\n\t" \
  "v_mfma_f32_16x16x32_bf16 v[240:243], v[104:107], v[184:187], v[240:243]\n\t" \
  "ds_read_b64_tr_b16 v[138:139], v5 offset:3472\n\t" \
  "v_mfma_f32_16x16x32_bf16 v[240:243], v[64:67], v[188:191], v[240:243]\n\t" \
  "ds_read_b128 v[84:87], v9 offset:9216\n\t" \
  "v_mfma_f32_16x16x32_bf16 v[236:239], v[76:79], v[176:179], v[236:239]\n\t" \
  "v_mfma_f32_16x16x32_bf16 v[236:239], v[144:147], v[180:183], v[236:239]\n\t" \
  "v_mfma_f32_16x16x32_bf16 v[236:239], v[72:75], v[184:187], v[236:239]\n\t" \
  "v_mfma_f32_16x16x32_bf16 v[236:239], v[104:107], v[188:191], v[236:239]\n\t" \
  "v_mfma_f32_16x16x32_bf16 v[232:235], v[76:79], v[180:183], v[232:235]\n\t" \
  "v_mfma_f32_16x16x32_bf16 v[232:235], v[144:147], v[184:187], v[232:235]\n\t" \
  "ds_read_b64_tr_b16 v[96:97], v5 offset:3968\n\t" \
  "v_mfma_f32_16x16x32_bf16 v[232:235], v[72:75], v[188:191], v[232:235]\n\t" \
  "ds_read_b64_tr_b16 v[98:99], v5 offset:3984\n\t" \
  "ds_write_b32 v18, v17 offset:176\n\t" \
  "v_mfma_f32_16x16x32_bf16 v[228:231], v[76:79], v[184:187], v[228:231]\n\t" \
  "ds_read_b128 v[100:103], v9 offset:7168\n\t" \
  "v_mfma_f32_16x16x32_bf16 v[228:231], v[144:147], v[188:191], v[228:231]\n\t" \
  "ds_read_b128 v[104:107], v9 offset:6144\n\t" \
  "v_mfma_f32_16x16x32_bf16 v[224:227], v[76:79], v[188:191], v[224:227]\n\t" \
  "ds_read_b128 v[144:147], v9 offset:5120\n\t" \
  "s_waitcnt lgkmcnt(6)\n\t" \
  "v_mfma_f32_16x16x32_bf16 v[192:195], v[148:151], v[80:83], v[192:195]\n\t" \
  "v_mfma_f32_16x16x32_bf16 v[196:199], v[148:151], v[140:143], v[196:199]\n\t" \
  "v_mfma_f32_16x16x32_bf16 v[196:199], v[132:135], v[80:83], v[196:199]\n\t" \
  "ds_read_b128 v[152:155], v9 offset:4096\n\t" \
  "v_mfma_f32_16x16x32_bf16 v[200:203], v[148:151], v[124:127], v[200:203]\n\t" \
  "v_mfma_f32_16x16x32_bf16 v[200:203], v[132:135], v[140:143], v[200:203]\n\t" \
  "v_mfma_f32_16x16x32_bf16 v[200:203], v[116:119], v[80:83], v[200:203]\n\t" \
  "v_mfma_f32_16x16x32_bf16 v[204:207], v[148:151], v[92:95], v[204:207]\n\t" \
  "ds_read_b128 v[156:159], v9 offset:3072\n\t" \
  "v_cndmask_b32 v20, v195, 0, s[76:77]\n\t" \
  "v_mfma_f32_16x16x32_bf16 v[204:207], v[132:135], v[124:127], v[204:207]\n\t" \
  "ds_bpermute_b32 v20, v15, v20\n\t" \
  "v_add_f32 v225, v225, v192\n\t" \
  "v_mfma_f32_16x16x32_bf16 v[204:207], v[116:119], v[140:143], v[204:207]\n\t" \
  "v_add_f32 v226, v226, v193\n\t" \
  "v_add_f32 v227, v227, v194\n\t" \
  "v_mfma_f32_16x16x32_bf16 v[204:207], v[108:111], v[80:83], v[204:207]\n\t" \
  "v_cndmask_b32 v21, v199, v195, s[76:77]\n\t" \
  "ds_bpermute_b32 v21, v15, v21\n\t" \
  "v_mfma_f32_16x16x32_bf16 v[208:211], v[148:151], v[112:115], v[208:211]\n\t" \
  "v_add_f32 v229, v229, v196\n\t" \
  "v_add_f32 v230, v230, v197\n\t" \
  "v_mfma_f32_16x16x32_bf16 v[208:211], v[132:135], v[92:95], v[208:211]\n\t" \
  "ds_read_b128 v[164:167], v9 offset:2048\n\t" \
  "v_add_f32 v231, v231, v198\n\t" \
  "v_mfma_f32_16x16x32_bf16 v[208:211], v[116:119], v[124:127], v[208:211]\n\t" \
  "v_cndmask_b32 v22, v203, v199, s[76:77]\n\t" \
  "ds_bpermute_b32 v22, v15, v22\n\t" \
  "v_mfma_f32_16x16x32_bf16 v[208:211], v[108:111], v[140:143], v[208:211]\n\t" \
  "v_add_f32 v233, v233, v200\n\t" \
  "v_add_f32 v234, v234, v201\n\t" \
  "v_mfma_f32_16x16x32_bf16 v[208:211], v[120:123], v[80:83], v[208:211]\n\t" \
  "v_add_f32 v235, v235, v202\n\t" \
  "s_waitcnt lgkmcnt(3)\n\t" \
  "v_add_f32 v224, v224, v20\n\t" \
  "v_mfma_f32_16x16x32_bf16 v[212:215], v[148:151], v[68:71], v[212:215]\n\t" \
  "v_cndmask_b32 v23, v207, v203, s[76:77]\n\t" \
  "ds_bpermute_b32 v23, v15, v23\n\t" \
  "v_mfma_f32_16x16x32_bf16 v[212:215], v[132:135], v[112:115], v[212:215]\n\t" \
  "v_add_f32 v237, v237, v204\n\t" \
  "v_add_f32 v238, v238, v205\n\t" \
  "v_mfma_f32_16x16x32_bf16 v[212:215], v[116:119], v[92:95], v[212:215]\n\t" \
  "ds_read_b128 v[168:171], v9 offset:1024\n\t" \
  "v_add_f32 v239, v239, v206\n\t" \
  "v_mfma_f32_16x16x32_bf16 v[212:215], v[108:111], v[124:127], v[212:215]\n\t" \
  "s_waitcnt lgkmcnt(4)\n\t" \
  "v_add_f32 v228, v228, v21\n\t" \
  "v_mfma_f32_16x16x32_bf16 v[212:215], v[120:123], v[140:143], v[212:215]\n\t" \
  "s_waitcnt lgkmcnt(2)\n\t" \
  "v_add_f32 v232, v232, v22\n\t" \
  "v_mfma_f32_16x16x32_bf16 v[212:215], v[128:131], v[80:83], v[212:215]\n\t" \
  "v_cndmask_b32 v24, v211, v207, s[76:77]\n\t" \
  "ds_bpermute_b32 v24, v15, v24\n\t" \
  "v_mfma_f32_16x16x32_bf16 v[216:219], v[148:151], v[88:91], v[216:219]\n\t" \
  "v_add_f32 v241, v241, v208\n\t" \
  "v_add_f32 v242, v242, v209\n\t" \
  "v_mfma_f32_16x16x32_bf16 v[216:219], v[132:135], v[68:71], v[216:219]\n\t" \
  "v_add_f32 v243, v243, v210\n\t" \
  "v_mfma_f32_16x16x32_bf16 v[216:219], v[116:119], v[112:115], v[216:219]\n\t" \
  "s_waitcnt lgkmcnt(2)\n\t" \
  "v_add_f32 v236, v236, v23\n\t" \
  "v_mfma_f32_16x16x32_bf16 v[216:219], v[108:111], v[92:95], v[216:219]\n\t" \
  "ds_read_b128 v[172:175], v9 offset:0\n\t" \
  "ds_write_b32 v18, v17 offset:192\n\t" \
  "v_mfma_f32_16x16x32_bf16 v[216:219], v[120:123], v[124:127], v[216:219]\n\t" \
  "v_mfma_f32_16x16x32_bf16 v[216:219], v[128:131], v[140:143], v[216:219]\n\t" \
  "ds_read_b32 v16, v56 offset:96\n\t" \
  "v_cndmask_b32 v25, v215, v211, s[76:77]\n\t" \
  "v_mfma_f32_16x16x32_bf16 v[216:219], v[136:139], v[80:83], v[216:219]\n\t" \
  "ds_bpermute_b32 v25, v15, v25\n\t" \
  "v_add_f32 v245, v245, v212\n\t" \
  "v_mfma_f32_16x16x32_bf16 v[220:223], v[148:151], v[84:87], v[220:223]\n\t" \
  "v_add_f32 v246, v246, v213\n\t" \
  "v_add_f32 v247, v247, v214\n\t" \
  "v_mfma_f32_16x16x32_bf16 v[220:223], v[132:135], v[88:91], v[220:223]\n\t" \
  "s_waitcnt lgkmcnt(4)\n\t" \
  "v_add_f32 v240, v240, v24\n\t" \
  "v_mfma_f32_16x16x32_bf16 v[220:223], v[116:119], v[68:71], v[220:223]\n\t" \
  "v_mfma_f32_16x16x32_bf16 v[220:223], v[108:111], v[112:115], v[220:223]\n\t" \
  "v_mfma_f32_16x16x32_bf16 v[220:223], v[120:123], v[92:95], v[220:223]\n\t" \
  "v_mfma_f32_16x16x32_bf16 v[220:223], v[128:131], v[124:127], v[220:223]\n\t" \
  "s_waitcnt lgkmcnt(1)\n\t" \
  "v_readfirstlane_b32 vcc_lo, v16\n\t" \
  "v_mfma_f32_16x16x32_bf16 v[220:223], v[136:139], v[140:143], v[220:223]\n\t" \
  "s_cmp_eq_u32 vcc_lo, 0\n\t" \
  "s_cbranch_scc1 33f\n\t" \
  "s_lshr_b64 s[90:91], %18, s75\n\t" \
  "s_and_b32 s90, s90, 15\n\t" \
  "s_lshl_b32 s90, s90, 15\n\t" \
  "s_add_u32 s90, s90, s92\n\t" \
  "s_addc_u32 s91, s93, 0\n\t" \
  "s_add_u32 s75, s75, 4\n\t" \
  "s_swappc_b64 s[68:69], s[70:71]\n\t" \
  "33:\n\t" \
  "v_mfma_f32_16x16x32_bf16 v[220:223], v[96:99], v[80:83], v[220:223]\n\t" \
  "v_cndmask_b32 v26, v219, v215, s[76:77]\n\t" \
  "ds_bpermute_b32 v26, v15, v26\n\t" \
  "v_mfma_f32_16x16x32_bf16 v[224:227], v[148:151], v[100:103], v[224:227]\n\t" \
  "v_add_f32 v249, v249, v216\n\t" \
  "v_add_f32 v250, v250, v217\n\t" \
  "v_mfma_f32_16x16x32_bf16 v[228:231], v[148:151], v[104:107], v[228:231]\n\t" \
  "ds_read_b128 v[40:43], v56 offset:80\n\t" \
  "v_add_f32 v251, v251, v218\n\t" \
  "v_mfma_f32_16x16x32_bf16 v[228:231], v[132:135], v[100:103], v[228:231]\n\t" \
  "s_waitcnt lgkmcnt(2)\n\t" \
  "v_add_f32 v244, v244, v25\n\t" \
  "v_mfma_f32_16x16x32_bf16 v[232:235], v[148:151], v[144:147], v[232:235]\n\t" \
  "v_mfma_f32_16x16x32_bf16 v[232:235], v[132:135], v[104:107], v[232:235]\n\t" \
  "v_mfma_f32_16x16x32_bf16 v[232:235], v[116:119], v[100:103], v[232:235]\n\t" \
  "s_waitcnt lgkmcnt(1)\n\t" \
  "v_add_f32 v248, v248, v26\n\t" \
  "v_cndmask_b32 v27, v223, v219, s[76:77]\n\t" \
  "v_mfma_f32_16x16x32_bf16 v[236:239], v[148:151], v[152:155], v[236:239]\n\t" \
  "ds_bpermute_b32 v27, v15, v27\n\t" \
  "v_add_f32 v253, v253, v220\n\t" \
  "v_mfma_f32_16x16x32_bf16 v[236:239], v[132:135], v[144:147], v[236:239]\n\t" \
  "v_add_f32 v254, v254, v221\n\t" \
  "v_add_f32 v255, v255, v222\n\t" \
  "508:\n\t" \
  "s_cbranch_execz 26b\n\t" \
  "v_mfma_f32_16x16x32_bf16 v[236:239], v[116:119], v[104:107], v[236:239]\n\t" \
  "s_waitcnt lgkmcnt(1)\n\t" \
  "v_min_u32 v40, v40, v41\n\t" \
  "v_min3_u32 v40, v40, v42, v43\n\t" \
  "v_mfma_f32_16x16x32_bf16 v[236:239], v[108:111], v[100:103], v[236:239]\n\t" \
  "v_readfirstlane_b32 vcc_lo, v40\n\t" \
  "v_cvt_pk_bf16_f32 v45, v226, v227\n\t" \
  "v_mfma_f32_16x16x32_bf16 v[240:243], v[148:151], v[156:159], v[240:243]\n\t" \
  "ds_read_b32 v16, v56 offset:96\n\t" \
  "s_cmp_ge_u32 vcc_lo, 7\n\t" \
  "s_cbranch_scc0 36f\n\t" \
  "37:\n\t" \
  "v_mfma_f32_16x16x32_bf16 v[240:243], v[132:135], v[152:155], v[240:243]\n\t" \
  "v_cvt_pk_bf16_f32 v44, v224, v225\n\t" \
  "ds_write_b64 v14, v[44:45] offset:0\n\t" \
  "v_mfma_f32_16x16x32_bf16 v[240:243], v[116:119], v[144:147], v[240:243]\n\t" \
  "v_cvt_pk_bf16_f32 v47, v230, v231\n\t" \
  "v_cvt_pk_bf16_f32 v46, v228, v229\n\t" \
  "v_mfma_f32_16x16x32_bf16 v[240:243], v[108:111], v[104:107], v[240:243]\n\t" \
  "v_mov_b32 v19, 1\n\t" \
  "ds_write_b64 v14, v[46:47] offset:512\n\t" \
  "v_mfma_f32_16x16x32_bf16 v[240:243], v[120:123], v[100:103], v[240:243]\n\t" \
  "ds_write_b32 v18, v19 offset:32\n\t" \
  "v_cvt_pk_bf16_f32 v49, v234, v235\n\t" \
  "v_mfma_f32_16x16x32_bf16 v[244:247], v[148:151], v[164:167], v[244:247]\n\t" \
  "v_cvt_pk_bf16_f32 v48, v232, v233\n\t" \
  "ds_write_b64 v14, v[48:49] offset:1024\n\t" \
  "v_mfma_f32_16x16x32_bf16 v[244:247], v[132:135], v[156:159], v[244:247]\n\t" \
  "s_waitcnt lgkmcnt(5)\n\t" \
  "v_add_f32 v252, v252, v27\n\t" \
  "v_cvt_pk_bf16_f32 v51, v238, v239\n\t" \
  "v_mfma_f32_16x16x32_bf16 v[244:247], v[116:119], v[152:155], v[244:247]\n\t" \
  "s_waitcnt lgkmcnt(4)\n\t" \
  "v_readfirstlane_b32 vcc_lo, v16\n\t" \
  "v_mfma_f32_16x16x32_bf16 v[244:247], v[108:111], v[144:147], v[244:247]\n\t" \
  "s_cmp_eq_u32 vcc_lo, 0\n\t" \
  "s_cbranch_scc1 34f\n\t" \
  "s_lshr_b64 s[90:91], %18, s75\n\t" \
  "s_and_b32 s90, s90, 15\n\t" \
  "s_lshl_b32 s90, s90, 15\n\t" \
  "s_add_u32 s90, s90, s92\n\t" \
  "s_addc_u32 s91, s93, 0\n\t" \
  "s_add_u32 s75, s75, 4\n\t" \
  "s_swappc_b64 s[68:69], s[70:71]\n\t" \
  "34:\n\t" \
  "v_mfma_f32_16x16x32_bf16 v[244:247], v[120:123], v[104:107], v[244:247]\n\t" \
  "v_cvt_pk_bf16_f32 v50, v236, v237\n\t" \
  "v_mov_b32 v19, 3\n\t" \
  "v_mfma_f32_16x16x32_bf16 v[244:247], v[128:131], v[100:103], v[244:247]\n\t" \
  "ds_write_b64 v14, v[50:51] offset:1536\n\t" \
  "ds_write_b32 v18, v19 offset:32\n\t" \
  "v_mfma_f32_16x16x32_bf16 v[248:251], v[148:151], v[168:171], v[248:251]\n\t" \
  "v_cvt_pk_bf16_f32 v45, v242, v243\n\t" \
  "v_cvt_pk_bf16_f32 v44, v240, v241\n\t" \
  "v_mfma_f32_16x16x32_bf16 v[248:251], v[132:135], v[164:167], v[248:251]\n\t" \
  "ds_read_b32 v16, v56 offset:96\n\t" \
  "ds_write_b64 v14, v[44:45] offset:2048\n\t" \
  "v_mfma_f32_16x16x32_bf16 v[248:251], v[116:119], v[156:159], v[248:251]\n\t" \
  "ds_read_b64_tr_b16 v[192:193], v28 offset:22528\n\t" \
  "v_mfma_f32_16x16x32_bf16 v[248:251], v[108:111], v[152:155], v[248:251]\n\t" \
  "ds_read_b64_tr_b16 v[194:195], v28 offset:23040\n\t" \
  "v_mfma_f32_16x16x32_bf16 v[248:251], v[120:123], v[144:147], v[248:251]\n\t" \
  "ds_read_b64_tr_b16 v[196:197], v28 offset:26624\n\t" \
  "v_mfma_f32_16x16x32_bf16 v[248:251], v[128:131], v[104:107], v[248:251]\n\t" \
  "v_cvt_pk_bf16_f32 v47, v246, v247\n\t" \
  "v_cvt_pk_bf16_f32 v46, v244, v245\n\t" \
  "v_mfma_f32_16x16x32_bf16 v[248:251], v[136:139], v[100:103], v[248:251]\n\t" \
  "v_mov_b32 v19, 7\n\t" \
  "ds_write_b64 v14, v[46:47] offset:2560\n\t" \
  "v_mfma_f32_16x16x32_bf16 v[252:255], v[148:151], v[172:175], v[252:255]\n\t" \
  "ds_write_b32 v18, v19 offset:32\n\t" \
  "ds_read_b64_tr_b16 v[198:199], v28 offset:27136\n\t" \
  "v_mfma_f32_16x16x32_bf16 v[252:255], v[132:135], v[168:171], v[252:255]\n\t" \
  "s_waitcnt lgkmcnt(7)\n\t" \
  "v_readfirstlane_b32 vcc_lo, v16\n\t" \
  "v_mfma_f32_16x16x32_bf16 v[252:255], v[116:119], v[164:167], v[252:255]\n\t" \
  "s_cmp_eq_u32 vcc_lo, 0\n\t" \
  "s_cbranch_scc1 35f\n\t" \
  "s_cmp_ge_u32 s75, 8\n\t" \
  "s_cbranch_scc1 35f\n\t" \
  "s_lshr_b64 s[90:91], %18, s75\n\t" \
  "s_and_b32 s90, s90, 15\n\t" \
  "s_lshl_b32 s90, s90, 15\n\t" \
  "s_add_u32 s90, s90, s92\n\t" \
  "s_addc_u32 s91, s93, 0\n\t" \
  "s_add_u32 s75, s75, 4\n\t" \
  "s_swappc_b64 s[68:69], s[70:71]\n\t" \
  "35:\n\t" \
  "v_mfma_f32_16x16x32_bf16 v[252:255], v[108:111], v[156:159], v[252:255]\n\t" \
  "ds_read_b64_tr_b16 v[200:201], v29 offset:22528\n\t" \
  "v_mfma_f32_16x16x32_bf16 v[252:255], v[120:123], v[152:155], v[252:255]\n\t" \
  "ds_read_b64_tr_b16 v[202:203], v29 offset:23040\n\t" \
  "v_mfma_f32_16x16x32_bf16 v[252:255], v[128:131], v[144:147], v[252:255]\n\t" \
  "v_cvt_pk_bf16_f32 v49, v250, v251\n\t" \
  "v_cvt_pk_bf16_f32 v48, v248, v249\n\t" \
  "v_mfma_f32_16x16x32_bf16 v[252:255], v[136:139], v[104:107], v[252:255]\n\t" \
  "ds_write_b64 v14, v[48:49] offset:3072\n\t" \
  "ds_read_b64_tr_b16 v[204:205], v29 offset:26624\n\t" \
  "v_mfma_f32_16x16x32_bf16 v[252:255], v[96:99], v[100:103], v[252:255]\n\t" \
  "ds_read_b64_tr_b16 v[206:207], v29 offset:27136\n\t" \
  "ds_read_b32 v16, v56 offset:96\n\t" \
  "ds_read_b64_tr_b16 v[208:209], v30 offset:22528\n\t" \
  "ds_read_b64_tr_b16 v[210:211], v30 offset:23040\n\t" \
  "ds_read_b64_tr_b16 v[212:213], v30 offset:26624\n\t" \
  "ds_read_b64_tr_b16 v[214:215], v30 offset:27136\n\t" \
  "ds_read_b64_tr_b16 v[216:217], v31 offset:22528\n\t" \
  "ds_read_b64_tr_b16 v[218:219], v31 offset:23040\n\t" \
  "ds_read_b64_tr_b16 v[220:221], v31 offset:26624\n\t" \
  "ds_read_b64_tr_b16 v[222:223], v31 offset:27136\n\t" \
  "v_cvt_pk_bf16_f32 v51, v254, v255\n\t" \
  "v_cvt_pk_bf16_f32 v50, v252, v253\n\t" \
  "v_mov_b32 v19, 15\n\t" \
  "ds_write_b64 v14, v[50:51] offset:3584\n\t" \
  "ds_write_b32 v18, v19 offset:32\n\t" \
  "s_cmp_lg_u32 s75, 0\n\t" \
  "s_cbranch_scc1 44f\n\t" \
  "s_waitcnt lgkmcnt(10)\n\t" \
  "v_readfirstlane_b32 vcc_lo, v16\n\t" \
  "s_nop 1\n\t" \
  "s_cmp_ge_u32 vcc_lo, 1\n\t" \
  "s_cbranch_scc0 46f\n\t" \
  "47:\n\t" \
  "44:\n\t" \
  "s_cmp_ge_u32 s75, 8\n\t" \
  "s_cbranch_scc1 45f\n\t" \
  "s_lshr_b64 s[90:91], %18, s75\n\t" \
  "s_and_b32 s90, s90, 15\n\t" \
  "s_lshl_b32 s90, s90, 15\n\t" \
  "s_add_u32 s90, s90, s92\n\t" \
  "s_addc_u32 s91, s93, 0\n\t" \
  "s_add_u32 s75, s75, 4\n\t" \
  "s_swappc_b64 s[68:69], s[70:71]\n\t" \
  "s_branch 44b\n\t" \
  "45:\n\t" \
  "s_waitcnt lgkmcnt(15)\n\t" \
  "s_waitcnt vmcnt(6)\n\t" \
  "v_mfma_f32_16x16x32_bf16 v[224:227], v[192:195], v[64:67], 0\n\t" \
  "v_mfma_f32_16x16x32_bf16 v[224:227], v[196:199], v[68:71], v[224:227]\n\t" \
  "s_lshr_b64 s[90:91], %18, 8\n\t" \
  "s_and_b32 s90, s90, 15\n\t" \
  "s_lshl_b32 s90, s90, 15\n\t" \
  "s_add_u32 s90, s90, s92\n\t" \
  "s_addc_u32 s91, s93, 0\n\t" \
  "global_load_dwordx4 v[96:99], v53, s[90:91] offset:0 sc1\n\t" \
  "s_waitcnt vmcnt(5)\n\t" \
  "v_mfma_f32_16x16x32_bf16 v[228:231], v[196:199], v[76:79], 0\n\t" \
  "v_mfma_f32_16x16x32_bf16 v[228:231], v[192:195], v[72:75], v[228:231]\n\t" \
  "global_load_dwordx4 v[100:103], v53, s[90:91] offset:1024 sc1\n\t" \
  "s_waitcnt lgkmcnt(14)\n\t" \
  "v_mfma_f32_16x16x32_bf16 v[232:235], v[200:203], v[64:67], 0\n\t" \
  "ds_read_b64_tr_b16 v[192:193], v28 offset:30720\n\t" \
  "s_waitcnt lgkmcnt(12)\n\t" \
  "v_mfma_f32_16x16x32_bf16 v[232:235], v[204:207], v[68:71], v[232:235]\n\t" \
  "ds_read_b64_tr_b16 v[194:195], v28 offset:31232\n\t" \
  "global_load_dwordx4 v[104:107], v53, s[90:91] offset:2048 sc1\n\t" \
  "v_mfma_f32_16x16x32_bf16 v[236:239], v[204:207], v[76:79], 0\n\t" \
  "ds_read_b64_tr_b16 v[196:197], v28 offset:34816\n\t" \
  "v_mfma_f32_16x16x32_bf16 v[236:239], v[200:203], v[72:75], v[236:239]\n\t" \
  "ds_read_b64_tr_b16 v[198:199], v28 offset:35328\n\t" \
  "global_load_dwordx4 v[108:111], v53, s[90:91] offset:3072 sc1\n\t" \
  "s_waitcnt lgkmcnt(6)\n\t" \
  "v_mfma_f32_16x16x32_bf16 v[240:243], v[208:211], v[64:67], 0\n\t" \
  "ds_read_b64_tr_b16 v[200:201], v29 offset:30720\n\t" \
  "v_mfma_f32_16x16x32_bf16 v[240:243], v[212:215], v[68:71], v[240:243]\n\t" \
  "ds_read_b64_tr_b16 v[202:203], v29 offset:31232\n\t" \
  "s_lshr_b64 s[90:91], %18, 12\n\t" \
  "s_and_b32 s90, s90, 15\n\t" \
  "s_lshl_b32 s90, s90, 15\n\t" \
  "s_add_u32 s90, s90, s92\n\t" \
  "s_addc_u32 s91, s93, 0\n\t" \
  "global_load_dwordx4 v[112:115], v53, s[90:91] offset:0 sc1\n\t" \
  "v_mfma_f32_16x16x32_bf16 v[244:247], v[212:215], v[76:79], 0\n\t" \
  "ds_read_b64_tr_b16 v[204:205], v29 offset:34816\n\t" \
  "v_mfma_f32_16x16x32_bf16 v[244:247], v[208:211], v[72:75], v[244:247]\n\t" \
  "ds_read_b64_tr_b16 v[206:207], v29 offset:35328\n\t" \
  "global_load_dwordx4 v[116:119], v53, s[90:91] offset:1024 sc1\n\t" \
  "v_mfma_f32_16x16x32_bf16 v[248:251], v[216:219], v[64:67], 0\n\t" \
  "ds_read_b64_tr_b16 v[208:209], v30 offset:30720\n\t" \
  "v_mfma_f32_16x16x32_bf16 v[248:251], v[220:223], v[68:71], v[248:251]\n\t" \
  "ds_read_b64_tr_b16 v[210:211], v30 offset:31232\n\t" \
  "global_load_dwordx4 v[120:123], v53, s[90:91] offset:2048 sc1\n\t" \
  "v_mfma_f32_16x16x32_bf16 v[252:255], v[220:223], v[76:79], 0\n\t" \
  "ds_read_b64_tr_b16 v[212:213], v30 offset:34816\n\t" \
  "v_mfma_f32_16x16x32_bf16 v[252:255], v[216:219], v[72:75], v[252:255]\n\t" \
  "ds_read_b64_tr_b16 v[214:215], v30 offset:35328\n\t" \
  "global_load_dwordx4 v[124:127], v53, s[90:91] offset:3072 sc1\n\t" \
  "s_waitcnt lgkmcnt(6)\n\t" \
  "s_waitcnt vmcnt(10)\n\t" \
  "v_mfma_f32_16x16x32_bf16 v[224:227], v[192:195], v[80:83], v[224:227]\n\t" \
  "ds_read_b64_tr_b16 v[216:217], v31 offset:30720\n\t" \
  "v_mfma_f32_16x16x32_bf16 v[224:227], v[196:199], v[84:87], v[224:227]\n\t" \
  "ds_read_b64_tr_b16 v[218:219], v31 offset:31232\n\t" \
  "s_waitcnt vmcnt(8)\n\t" \
  "v_mfma_f32_16x16x32_bf16 v[228:231], v[196:199], v[92:95], v[228:231]\n\t" \
  "ds_read_b64_tr_b16 v[220:221], v31 offset:34816\n\t" \
  "s_lshr_b64 s[90:91], %18, 16\n\t" \
  "s_and_b32 s90, s90, 15\n\t" \
  "s_lshl_b32 s90, s90, 15\n\t" \
  "s_add_u32 s90, s90, s92\n\t" \
  "s_addc_u32 s91, s93, 0\n\t" \
  "global_load_dwordx4 v[128:131], v53, s[90:91] offset:0 sc1\n\t" \
  "v_mfma_f32_16x16x32_bf16 v[228:231], v[192:195], v[88:91], v[228:231]\n\t" \
  "ds_read_b64_tr_b16 v[222:223], v31 offset:35328\n\t" \
  "v_mfma_f32_16x16x32_bf16 v[232:235], v[200:203], v[80:83], v[232:235]\n\t" \
  "ds_read_b64_tr_b16 v[192:193], v28 offset:38912\n\t" \
  "s_waitcnt lgkmcnt(6)\n\t" \
  "v_mfma_f32_16x16x32_bf16 v[232:235], v[204:207], v[84:87], v[232:235]\n\t" \
  "ds_read_b64_tr_b16 v[194:195], v28 offset:39424\n\t" \
  "ds_write_b32 v18, v17 offset:160\n\t" \
  "v_mfma_f32_16x16x32_bf16 v[236:239], v[204:207], v[92:95], v[236:239]\n\t" \
  "ds_read_b64_tr_b16 v[196:197], v28 offset:43008\n\t" \
  "global_load_dwordx4 v[132:135], v53, s[90:91] offset:1024 sc1\n\t" \
  "v_mfma_f32_16x16x32_bf16 v[236:239], v[200:203], v[88:91], v[236:239]\n\t" \
  "ds_read_b64_tr_b16 v[198:199], v28 offset:43520\n\t" \
  "v_mfma_f32_16x16x32_bf16 v[240:243], v[208:211], v[80:83], v[240:243]\n\t" \
  "ds_read_b64_tr_b16 v[200:201], v29 offset:38912\n\t" \
  "s_waitcnt lgkmcnt(7)\n\t" \
  "v_mfma_f32_16x16x32_bf16 v[240:243], v[212:215], v[84:87], v[240:243]\n\t" \
  "ds_read_b64_tr_b16 v[202:203], v29 offset:39424\n\t" \
  "v_mfma_f32_16x16x32_bf16 v[244:247], v[212:215], v[92:95], v[244:247]\n\t" \
  "ds_read_b64_tr_b16 v[204:205], v29 offset:43008\n\t" \
  "global_load_dwordx4 v[136:139], v53, s[90:91] offset:2048 sc1\n\t" \
  "v_mfma_f32_16x16x32_bf16 v[244:247], v[208:211], v[88:91], v[244:247]\n\t" \
  "ds_read_b64_tr_b16 v[206:207], v29 offset:43520\n\t" \
  "v_mfma_f32_16x16x32_bf16 v[248:251], v[216:219], v[80:83], v[248:251]\n\t" \
  "ds_read_b64_tr_b16 v[208:209], v30 offset:38912\n\t" \
  "s_waitcnt lgkmcnt(6)\n\t" \
  "v_mfma_f32_16x16x32_bf16 v[248:251], v[220:223], v[84:87], v[248:251]\n\t" \
  "ds_read_b64_tr_b16 v[210:211], v30 offset:39424\n\t" \
  "v_mfma_f32_16x16x32_bf16 v[252:255], v[220:223], v[92:95], v[252:255]\n\t" \
  "ds_read_b64_tr_b16 v[212:213], v30 offset:43008\n\t" \
  "global_load_dwordx4 v[140:143], v53, s[90:91] offset:3072 sc1\n\t" \
  "v_mfma_f32_16x16x32_bf16 v[252:255], v[216:219], v[88:91], v[252:255]\n\t" \
  "ds_read_b64_tr_b16 v[214:215], v30 offset:43520\n\t" \
  "s_waitcnt vmcnt(10)\n\t" \
  "v_mfma_f32_16x16x32_bf16 v[224:227], v[192:195], v[96:99], v[224:227]\n\t" \
  "ds_read_b64_tr_b16 v[216:217], v31 offset:38912\n\t" \
  "s_waitcnt lgkmcnt(6)\n\t" \
  "v_mfma_f32_16x16x32_bf16 v[224:227], v[196:199], v[100:103], v[224:227]\n\t" \
  "ds_read_b64_tr_b16 v[218:219], v31 offset:39424\n\t" \
  "s_waitcnt vmcnt(8)\n\t" \
  "v_mfma_f32_16x16x32_bf16 v[228:231], v[196:199], v[108:111], v[228:231]\n\t" \
  "ds_read_b64_tr_b16 v[220:221], v31 offset:43008\n\t" \
  "s_lshr_b64 s[90:91], %18, 20\n\t" \
  "s_and_b32 s90, s90, 15\n\t" \
  "s_lshl_b32 s90, s90, 15\n\t" \
  "s_add_u32 s90, s90, s92\n\t" \
  "s_addc_u32 s91, s93, 0\n\t" \
  "global_load_dwordx4 v[144:147], v53, s[90:91] offset:0 sc1\n\t" \
  "v_mfma_f32_16x16x32_bf16 v[228:231], v[192:195], v[104:107], v[228:231]\n\t" \
  "ds_read_b64_tr_b16 v[222:223], v31 offset:43520\n\t" \
  "v_mfma_f32_16x16x32_bf16 v[232:235], v[200:203], v[96:99], v[232:235]\n\t" \
  "ds_read_b64_tr_b16 v[192:193], v28 offset:47104\n\t" \
  "s_waitcnt lgkmcnt(6)\n\t" \
  "v_mfma_f32_16x16x32_bf16 v[232:235], v[204:207], v[100:103], v[232:235]\n\t" \
  "ds_read_b64_tr_b16 v[194:195], v28 offset:47616\n\t" \
  "v_mfma_f32_16x16x32_bf16 v[236:239], v[204:207], v[108:111], v[236:239]\n\t" \
  "ds_read_b64_tr_b16 v[196:197], v28 offset:51200\n\t" \
  "global_load_dwordx4 v[148:151], v53, s[90:91] offset:1024 sc1\n\t" \
  "v_mfma_f32_16x16x32_bf16 v[236:239], v[200:203], v[104:107], v[236:239]\n\t" \
  "ds_read_b64_tr_b16 v[198:199], v28 offset:51712\n\t" \
  "v_mfma_f32_16x16x32_bf16 v[240:243], v[208:211], v[96:99], v[240:243]\n\t" \
  "ds_read_b64_tr_b16 v[200:201], v29 offset:47104\n\t" \
  "s_waitcnt lgkmcnt(6)\n\t" \
  "v_mfma_f32_16x16x32_bf16 v[240:243], v[212:215], v[100:103], v[240:243]\n\t" \
  "ds_read_b64_tr_b16 v[202:203], v29 offset:47616\n\t" \
  "v_mfma_f32_16x16x32_bf16 v[244:247], v[212:215], v[108:111], v[244:247]\n\t" \
  "ds_read_b64_tr_b16 v[204:205], v29 offset:51200\n\t" \
  "global_load_dwordx4 v[152:155], v53, s[90:91] offset:2048 sc1\n\t" \
  "v_mfma_f32_16x16x32_bf16 v[244:247], v[208:211], v[104:107], v[244:247]\n\t" \
  "ds_read_b64_tr_b16 v[206:207], v29 offset:51712\n\t" \
  "v_mfma_f32_16x16x32_bf16 v[248:251], v[216:219], v[96:99], v[248:251]\n\t" \
  "ds_read_b64_tr_b16 v[208:209], v30 offset:47104\n\t" \
  "s_waitcnt lgkmcnt(6)\n\t" \
  "v_mfma_f32_16x16x32_bf16 v[248:251], v[220:223], v[100:103], v[248:251]\n\t" \
  "ds_read_b64_tr_b16 v[210:211], v30 offset:47616\n\t" \
  "v_mfma_f32_16x16x32_bf16 v[252:255], v[220:223], v[108:111], v[252:255]\n\t" \
  "ds_read_b64_tr_b16 v[212:213], v30 offset:51200\n\t" \
  "global_load_dwordx4 v[156:159], v53, s[90:91] offset:3072 sc1\n\t" \
  "v_mfma_f32_16x16x32_bf16 v[252:255], v[216:219], v[104:107], v[252:255]\n\t" \
  "ds_read_b64_tr_b16 v[214:215], v30 offset:51712\n\t" \
  "s_waitcnt vmcnt(10)\n\t" \
  "v_mfma_f32_16x16x32_bf16 v[224:227], v[192:195], v[112:115], v[224:227]\n\t" \
  "ds_read_b64_tr_b16 v[216:217], v31 offset:47104\n\t" \
  "s_waitcnt lgkmcnt(6)\n\t" \
  "v_mfma_f32_16x16x32_bf16 v[224:227], v[196:199], v[116:119], v[224:227]\n\t" \
  "ds_read_b64_tr_b16 v[218:219], v31 offset:47616\n\t" \
  "s_waitcnt vmcnt(8)\n\t" \
  "v_mfma_f32_16x16x32_bf16 v[228:231], v[196:199], v[124:127], v[228:231]\n\t" \
  "ds_read_b64_tr_b16 v[220:221], v31 offset:51200\n\t" \
  "s_lshr_b64 s[90:91], %18, 24\n\t" \
  "s_and_b32 s90, s90, 15\n\t" \
  "s_lshl_b32 s90, s90, 15\n\t" \
  "s_add_u32 s90, s90, s92\n\t" \
  "s_addc_u32 s91, s93, 0\n\t" \
  "global_load_dwordx4 v[160:163], v53, s[90:91] offset:0 sc1\n\t" \
  "v_mfma_f32_16x16x32_bf16 v[228:231], v[192:195], v[120:123], v[228:231]\n\t" \
  "ds_read_b64_tr_b16 v[222:223], v31 offset:51712\n\t" \
  "v_mfma_f32_16x16x32_bf16 v[232:235], v[200:203], v[112:115], v[232:235]\n\t" \
  "ds_read_b64_tr_b16 v[192:193], v28 offset:55296\n\t" \
  "s_waitcnt lgkmcnt(6)\n\t" \
  "v_mfma_f32_16x16x32_bf16 v[232:235], v[204:207], v[116:119], v[232:235]\n\t" \
  "ds_read_b64_tr_b16 v[194:195], v28 offset:55808\n\t" \
  "v_mfma_f32_16x16x32_bf16 v[236:239], v[204:207], v[124:127], v[236:239]\n\t" \
  "ds_read_b64_tr_b16 v[196:197], v28 offset:59392\n\t" \
  "global_load_dwordx4 v[164:167], v53, s[90:91] offset:1024 sc1\n\t" \
  "ds_read_b32 v16, v56 offset:96\n\t" \
  "v_mfma_f32_16x16x32_bf16 v[236:239], v[200:203], v[120:123], v[236:239]\n\t" \
  "ds_read_b64_tr_b16 v[198:199], v28 offset:59904\n\t" \
  "v_mfma_f32_16x16x32_bf16 v[240:243], v[208:211], v[112:115], v[240:243]\n\t" \
  "ds_read_b64_tr_b16 v[200:201], v29 offset:55296\n\t" \
  "s_waitcnt lgkmcnt(7)\n\t" \
  "v_mfma_f32_16x16x32_bf16 v[240:243], v[212:215], v[116:119], v[240:243]\n\t" \
  "ds_read_b64_tr_b16 v[202:203], v29 offset:55808\n\t" \
  "v_mfma_f32_16x16x32_bf16 v[244:247], v[212:215], v[124:127], v[244:247]\n\t" \
  "ds_read_b64_tr_b16 v[204:205], v29 offset:59392\n\t" \
  "global_load_dwordx4 v[168:171], v53, s[90:91] offset:2048 sc1\n\t" \
  "v_mfma_f32_16x16x32_bf16 v[244:247], v[208:211], v[120:123], v[244:247]\n\t" \
  "ds_read_b64_tr_b16 v[206:207], v29 offset:59904\n\t" \
  "v_mfma_f32_16x16x32_bf16 v[248:251], v[216:219], v[112:115], v[248:251]\n\t" \
  "ds_read_b64_tr_b16 v[208:209], v30 offset:55296\n\t" \
  "s_waitcnt lgkmcnt(6)\n\t" \
  "v_mfma_f32_16x16x32_bf16 v[248:251], v[220:223], v[116:119], v[248:251]\n\t" \
  "ds_read_b64_tr_b16 v[210:211], v30 offset:55808\n\t" \
  "v_mfma_f32_16x16x32_bf16 v[252:255], v[220:223], v[124:127], v[252:255]\n\t" \
  "ds_read_b64_tr_b16 v[212:213], v30 offset:59392\n\t" \
  "global_load_dwordx4 v[172:175], v53, s[90:91] offset:3072 sc1\n\t" \
  "v_readfirstlane_b32 vcc_lo, v16\n\t" \
  "v_mfma_f32_16x16x32_bf16 v[252:255], v[216:219], v[120:123], v[252:255]\n\t" \
  "ds_read_b64_tr_b16 v[214:215], v30 offset:59904\n\t" \
  "s_cmp_ge_u32 vcc_lo, 2\n\t" \
  "s_cbranch_scc0 49f\n\t" \
  "50:\n\t" \
  "s_waitcnt vmcnt(10)\n\t" \
  "v_mfma_f32_16x16x32_bf16 v[224:227], v[192:195], v[128:131], v[224:227]\n\t" \
  "ds_read_b64_tr_b16 v[216:217], v31 offset:55296\n\t" \
  "s_waitcnt lgkmcnt(6)\n\t" \
  "v_mfma_f32_16x16x32_bf16 v[224:227], v[196:199], v[132:135], v[224:227]\n\t" \
  "ds_read_b64_tr_b16 v[218:219], v31 offset:55808\n\t" \
  "s_waitcnt vmcnt(8)\n\t" \
  "v_mfma_f32_16x16x32_bf16 v[228:231], v[196:199], v[140:143], v[228:231]\n\t" \
  "ds_read_b64_tr_b16 v[220:221], v31 offset:59392\n\t" \
  "s_lshr_b64 s[90:91], %18, 28\n\t" \
  "s_and_b32 s90, s90, 15\n\t" \
  "s_lshl_b32 s90, s90, 15\n\t" \
  "s_add_u32 s90, s90, s92\n\t" \
  "s_addc_u32 s91, s93, 0\n\t" \
  "global_load_dwordx4 v[176:179], v53, s[90:91] offset:0 sc1\n\t" \
  "v_mfma_f32_16x16x32_bf16 v[228:231], v[192:195], v[136:139], v[228:231]\n\t" \
  "ds_read_b64_tr_b16 v[222:223], v31 offset:59904\n\t" \
  "v_mfma_f32_16x16x32_bf16 v[232:235], v[200:203], v[128:131], v[232:235]\n\t" \
  "ds_read_b64_tr_b16 v[192:193], v28 offset:63488\n\t" \
  "s_waitcnt lgkmcnt(6)\n\t" \
  "v_mfma_f32_16x16x32_bf16 v[232:235], v[204:207], v[132:135], v[232:235]\n\t" \
  "ds_read_b64_tr_b16 v[194:195], v28 offset:64000\n\t" \
  "v_mfma_f32_16x16x32_bf16 v[236:239], v[204:207], v[140:143], v[236:239]\n\t" \
  "ds_read_b64_tr_b16 v[196:197], v32 offset:2048\n\t" \
  "global_load_dwordx4 v[180:183], v53, s[90:91] offset:1024 sc1\n\t" \
  "v_mfma_f32_16x16x32_bf16 v[236:239], v[200:203], v[136:139], v[236:239]\n\t" \
  "ds_read_b64_tr_b16 v[198:199], v32 offset:2560\n\t" \
  "v_mfma_f32_16x16x32_bf16 v[240:243], v[208:211], v[128:131], v[240:243]\n\t" \
  "ds_read_b64_tr_b16 v[200:201], v29 offset:63488\n\t" \
  "s_waitcnt lgkmcnt(6)\n\t" \
  "v_mfma_f32_16x16x32_bf16 v[240:243], v[212:215], v[132:135], v[240:243]\n\t" \
  "ds_read_b64_tr_b16 v[202:203], v29 offset:64000\n\t" \
  "v_mfma_f32_16x16x32_bf16 v[244:247], v[212:215], v[140:143], v[244:247]\n\t" \
  "ds_read_b64_tr_b16 v[204:205], v33 offset:2048\n\t" \
  "global_load_dwordx4 v[184:187], v53, s[90:91] offset:2048 sc1\n\t" \
  "v_mfma_f32_16x16x32_bf16 v[244:247], v[208:211], v[136:139], v[244:247]\n\t" \
  "ds_read_b64_tr_b16 v[206:207], v33 offset:2560\n\t" \
  "v_mfma_f32_16x16x32_bf16 v[248:251], v[216:219], v[128:131], v[248:251]\n\t" \
  "ds_read_b64_tr_b16 v[208:209], v30 offset:63488\n\t" \
  "s_waitcnt lgkmcnt(6)\n\t" \
  "v_mfma_f32_16x16x32_bf16 v[248:251], v[220:223], v[132:135], v[248:251]\n\t" \
  "ds_read_b64_tr_b16 v[210:211], v30 offset:64000\n\t" \
  "v_mfma_f32_16x16x32_bf16 v[252:255], v[220:223], v[140:143], v[252:255]\n\t" \
  "ds_read_b64_tr_b16 v[212:213], v34 offset:2048\n\t" \
  "global_load_dwordx4 v[188:191], v53, s[90:91] offset:3072 sc1\n\t" \
  "v_mfma_f32_16x16x32_bf16 v[252:255], v[216:219], v[136:139], v[252:255]\n\t" \
  "ds_read_b64_tr_b16 v[214:215], v34 offset:2560\n\t" \
  "s_waitcnt vmcnt(10)\n\t" \
  "v_mfma_f32_16x16x32_bf16 v[224:227], v[192:195], v[144:147], v[224:227]\n\t" \
  "ds_read_b64_tr_b16 v[216:217], v31 offset:63488\n\t" \
  "s_waitcnt lgkmcnt(6)\n\t" \
  "v_mfma_f32_16x16x32_bf16 v[224:227], v[196:199], v[148:151], v[224:227]\n\t" \
  "ds_read_b64_tr_b16 v[218:219], v31 offset:64000\n\t" \
  "s_waitcnt vmcnt(8)\n\t" \
  "v_mfma_f32_16x16x32_bf16 v[228:231], v[196:199], v[156:159], v[228:231]\n\t" \
  "ds_read_b64_tr_b16 v[220:221], v35 offset:2048\n\t" \
  "s_lshr_b64 s[90:91], %18, 32\n\t" \
  "s_and_b32 s90, s90, 15\n\t" \
  "s_lshl_b32 s90, s90, 15\n\t" \
  "s_add_u32 s90, s90, s92\n\t" \
  "s_addc_u32 s91, s93, 0\n\t" \
  "global_load_dwordx4 v[64:67], v53, s[90:91] offset:0 sc1\n\t" \
  "v_mfma_f32_16x16x32_bf16 v[228:231], v[192:195], v[152:155], v[228:231]\n\t" \
  "ds_read_b64_tr_b16 v[222:223], v35 offset:2560\n\t" \
  "v_mfma_f32_16x16x32_bf16 v[232:235], v[200:203], v[144:147], v[232:235]\n\t" \
  "ds_read_b64_tr_b16 v[192:193], v32 offset:6144\n\t" \
  "s_waitcnt lgkmcnt(6)\n\t" \
  "v_mfma_f32_16x16x32_bf16 v[232:235], v[204:207], v[148:151], v[232:235]\n\t" \
  "ds_read_b64_tr_b16 v[194:195], v32 offset:6656\n\t" \
  "v_mfma_f32_16x16x32_bf16 v[236:239], v[204:207], v[156:159], v[236:239]\n\t" \
  "ds_read_b64_tr_b16 v[196:197], v32 offset:10240\n\t" \
  "global_load_dwordx4 v[68:71], v53, s[90:91] offset:1024 sc1\n\t" \
  "v_mfma_f32_16x16x32_bf16 v[236:239], v[200:203], v[152:155], v[236:239]\n\t" \
  "ds_read_b64_tr_b16 v[198:199], v32 offset:10752\n\t" \
  "v_mfma_f32_16x16x32_bf16 v[240:243], v[208:211], v[144:147], v[240:243]\n\t" \
  "ds_read_b64_tr_b16 v[200:201], v33 offset:6144\n\t" \
  "s_waitcnt lgkmcnt(6)\n\t" \
  "v_mfma_f32_16x16x32_bf16 v[240:243], v[212:215], v[148:151], v[240:243]\n\t" \
  "ds_read_b128 v[40:43], v56 offset:80\n\t" \
  "ds_read_b64_tr_b16 v[202:203], v33 offset:6656\n\t" \
  "v_mfma_f32_16x16x32_bf16 v[244:247], v[212:215], v[156:159], v[244:247]\n\t" \
  "ds_read_b64_tr_b16 v[204:205], v33 offset:10240\n\t" \
  "global_load_dwordx4 v[72:75], v53, s[90:91] offset:2048 sc1\n\t" \
  "v_mfma_f32_16x16x32_bf16 v[244:247], v[208:211], v[152:155], v[244:247]\n\t" \
  "ds_read_b64_tr_b16 v[206:207], v33 offset:10752\n\t" \
  "v_mfma_f32_16x16x32_bf16 v[248:251], v[216:219], v[144:147], v[248:251]\n\t" \
  "ds_read_b64_tr_b16 v[208:209], v34 offset:6144\n\t" \
  "s_waitcnt lgkmcnt(7)\n\t" \
  "v_mfma_f32_16x16x32_bf16 v[248:251], v[220:223], v[148:151], v[248:251]\n\t" \
  "ds_read_b64_tr_b16 v[210:211], v34 offset:6656\n\t" \
  "v_mfma_f32_16x16x32_bf16 v[252:255], v[220:223], v[156:159], v[252:255]\n\t" \
  "ds_read_b64_tr_b16 v[212:213], v34 offset:10240\n\t" \
  "global_load_dwordx4 v[76:79], v53, s[90:91] offset:3072 sc1\n\t" \
  "v_mfma_f32_16x16x32_bf16 v[252:255], v[216:219], v[152:155], v[252:255]\n\t" \
  "ds_read_b64_tr_b16 v[214:215], v34 offset:10752\n\t" \
  "s_waitcnt vmcnt(10)\n\t" \
  "v_mfma_f32_16x16x32_bf16 v[224:227], v[192:195], v[160:163], v[224:227]\n\t" \
  "s_waitcnt lgkmcnt(7)\n\t" \
  "v_min_u32 v40, v40, v41\n\t" \
  "v_min3_u32 v40, v40, v42, v43\n\t" \
  "ds_read_b64_tr_b16 v[216:217], v35 offset:6144\n\t" \
  "v_mfma_f32_16x16x32_bf16 v[224:227], v[196:199], v[164:167], v[224:227]\n\t" \
  "v_readfirstlane_b32 vcc_lo, v40\n\t" \
  "ds_read_b64_tr_b16 v[218:219], v35 offset:6656\n\t" \
  "s_waitcnt vmcnt(8)\n\t" \
  "v_mfma_f32_16x16x32_bf16 v[228:231], v[196:199], v[172:175], v[228:231]\n\t" \
  "s_cmp_ge_u32 vcc_lo, 13\n\t" \
  "s_cbranch_scc0 38f\n\t" \
  "39:\n\t" \
  "ds_read_b64_tr_b16 v[220:221], v35 offset:10240\n\t" \
  "s_lshr_b64 s[90:91], %18, 36\n\t" \
  "s_and_b32 s90, s90, 15\n\t" \
  "s_lshl_b32 s90, s90, 15\n\t" \
  "s_add_u32 s90, s90, s92\n\t" \
  "s_addc_u32 s91, s93, 0\n\t" \
  "global_load_dwordx4 v[80:83], v53, s[90:91] offset:0 sc1\n\t" \
  "v_mfma_f32_16x16x32_bf16 v[228:231], v[192:195], v[168:171], v[228:231]\n\t" \
  "ds_read_b64_tr_b16 v[222:223], v35 offset:10752\n\t" \
  "s_waitcnt lgkmcnt(6)\n\t" \
  "v_mfma_f32_16x16x32_bf16 v[232:235], v[200:203], v[160:163], v[232:235]\n\t" \
  "ds_read_b64_tr_b16 v[192:193], v32 offset:14336\n\t" \
  "v_mfma_f32_16x16x32_bf16 v[232:235], v[204:207], v[164:167], v[232:235]\n\t" \
  "ds_read_b64_tr_b16 v[194:195], v32 offset:14848\n\t" \
  "v_mfma_f32_16x16x32_bf16 v[236:239], v[204:207], v[172:175], v[236:239]\n\t" \
  "ds_read_b64_tr_b16 v[196:197], v32 offset:18432\n\t" \
  "global_load_dwordx4 v[84:87], v53, s[90:91] offset:1024 sc1\n\t" \
  "v_mfma_f32_16x16x32_bf16 v[236:239], v[200:203], v[168:171], v[236:239]\n\t" \
  "ds_read_b64_tr_b16 v[198:199], v32 offset:18944\n\t" \
  "v_mfma_f32_16x16x32_bf16 v[240:243], v[208:211], v[160:163], v[240:243]\n\t" \
  "ds_read_b64_tr_b16 v[200:201], v33 offset:14336\n\t" \
  "s_waitcnt lgkmcnt(6)\n\t" \
  "v_mfma_f32_16x16x32_bf16 v[240:243], v[212:215], v[164:167], v[240:243]\n\t" \
  "ds_read_b64_tr_b16 v[202:203], v33 offset:14848\n\t" \
  "v_mfma_f32_16x16x32_bf16 v[244:247], v[212:215], v[172:175], v[244:247]\n\t" \
  "ds_read_b64_tr_b16 v[204:205], v33 offset:18432\n\t" \
  "global_load_dwordx4 v[88:91], v53, s[90:91] offset:2048 sc1\n\t" \
  "v_mfma_f32_16x16x32_bf16 v[244:247], v[208:211], v[168:171], v[244:247]\n\t" \
  "ds_read_b64_tr_b16 v[206:207], v33 offset:18944\n\t" \
  "v_mfma_f32_16x16x32_bf16 v[248:251], v[216:219], v[160:163], v[248:251]\n\t" \
  "ds_read_b64_tr_b16 v[208:209], v34 offset:14336\n\t" \
  "s_waitcnt lgkmcnt(6)\n\t" \
  "v_mfma_f32_16x16x32_bf16 v[248:251], v[220:223], v[164:167], v[248:251]\n\t" \
  "ds_read_b64_tr_b16 v[210:211], v34 offset:14848\n\t" \
  "v_mfma_f32_16x16x32_bf16 v[252:255], v[220:223], v[172:175], v[252:255]\n\t" \
  "ds_read_b64_tr_b16 v[212:213], v34 offset:18432\n\t" \
  "global_load_dwordx4 v[92:95], v53, s[90:91] offset:3072 sc1\n\t" \
  "v_mfma_f32_16x16x32_bf16 v[252:255], v[216:219], v[168:171], v[252:255]\n\t" \
  "ds_read_b64_tr_b16 v[214:215], v34 offset:18944\n\t" \
  "s_waitcnt vmcnt(10)\n\t" \
  "v_mfma_f32_16x16x32_bf16 v[224:227], v[192:195], v[176:179], v[224:227]\n\t" \
  "ds_read_b64_tr_b16 v[216:217], v35 offset:14336\n\t" \
  "s_waitcnt lgkmcnt(6)\n\t" \
  "v_mfma_f32_16x16x32_bf16 v[224:227], v[196:199], v[180:183], v[224:227]\n\t" \
  "ds_read_b64_tr_b16 v[218:219], v35 offset:14848\n\t" \
  "s_waitcnt vmcnt(8)\n\t" \
  "v_mfma_f32_16x16x32_bf16 v[228:231], v[196:199], v[188:191], v[228:231]\n\t" \
  "ds_read_b64_tr_b16 v[220:221], v35 offset:18432\n\t" \
  "s_lshr_b64 s[90:91], %18, 40\n\t" \
  "s_and_b32 s90, s90, 15\n\t" \
  "s_lshl_b32 s90, s90, 15\n\t" \
  "s_add_u32 s90, s90, s92\n\t" \
  "s_addc_u32 s91, s93, 0\n\t" \
  "global_load_dwordx4 v[96:99], v53, s[90:91] offset:0 sc1\n\t" \
  "v_mfma_f32_16x16x32_bf16 v[228:231], v[192:195], v[184:187], v[228:231]\n\t" \
  "ds_read_b64_tr_b16 v[222:223], v35 offset:18944\n\t" \
  "v_mfma_f32_16x16x32_bf16 v[232:235], v[200:203], v[176:179], v[232:235]\n\t" \
  "ds_read_b64_tr_b16 v[192:193], v32 offset:22528\n\t" \
  "s_waitcnt lgkmcnt(6)\n\t" \
  "v_mfma_f32_16x16x32_bf16 v[232:235], v[204:207], v[180:183], v[232:235]\n\t" \
  "ds_read_b64_tr_b16 v[194:195], v32 offset:23040\n\t" \
  "v_mfma_f32_16x16x32_bf16 v[236:239], v[204:207], v[188:191], v[236:239]\n\t" \
  "ds_read_b64_tr_b16 v[196:197], v32 offset:26624\n\t" \
  "global_load_dwordx4 v[100:103], v53, s[90:91] offset:1024 sc1\n\t" \
  "v_mfma_f32_16x16x32_bf16 v[236:239], v[200:203], v[184:187], v[236:239]\n\t" \
  "ds_read_b64_tr_b16 v[198:199], v32 offset:27136\n\t" \
  "v_mfma_f32_16x16x32_bf16 v[240:243], v[208:211], v[176:179], v[240:243]\n\t" \
  "ds_read_b64_tr_b16 v[200:201], v33 offset:22528\n\t" \
  "s_waitcnt lgkmcnt(6)\n\t" \
  "v_mfma_f32_16x16x32_bf16 v[240:243], v[212:215], v[180:183], v[240:243]\n\t" \
  "ds_read_b64_tr_b16 v[202:203], v33 offset:23040\n\t" \
  "v_mfma_f32_16x16x32_bf16 v[244:247], v[212:215], v[188:191], v[244:247]\n\t" \
  "ds_read_b64_tr_b16 v[204:205], v33 offset:26624\n\t" \
  "global_load_dwordx4 v[104:107], v53, s[90:91] offset:2048 sc1\n\t" \
  "v_mfma_f32_16x16x32_bf16 v[244:247], v[208:211], v[184:187], v[244:247]\n\t" \
  "ds_read_b64_tr_b16 v[206:207], v33 offset:27136\n\t" \
  "v_mfma_f32_16x16x32_bf16 v[248:251], v[216:219], v[176:179], v[248:251]\n\t" \
  "ds_read_b64_tr_b16 v[208:209], v34 offset:22528\n\t" \
  "s_waitcnt lgkmcnt(6)\n\t" \
  "v_mfma_f32_16x16x32_bf16 v[248:251], v[220:223], v[180:183], v[248:251]\n\t" \
  "ds_read_b64_tr_b16 v[210:211], v34 offset:23040\n\t" \
  "v_mfma_f32_16x16x32_bf16 v[252:255], v[220:223], v[188:191], v[252:255]\n\t" \
  "ds_read_b64_tr_b16 v[212:213], v34 offset:26624\n\t" \
  "global_load_dwordx4 v[108:111], v53, s[90:91] offset:3072 sc1\n\t" \
  "ds_write_b32 v56, v56 offset:108\n\t" \
  "v_mfma_f32_16x16x32_bf16 v[252:255], v[216:219], v[184:187], v[252:255]\n\t" \
  "ds_read_b64_tr_b16 v[214:215], v34 offset:27136\n\t" \
  "s_waitcnt vmcnt(10)\n\t" \
  "v_mfma_f32_16x16x32_bf16 v[224:227], v[192:195], v[64:67], v[224:227]\n\t" \
  "ds_read_b64_tr_b16 v[216:217], v35 offset:22528\n\t" \
  "s_waitcnt lgkmcnt(7)\n\t" \
  "v_mfma_f32_16x16x32_bf16 v[224:227], v[196:199], v[68:71], v[224:227]\n\t" \
  "ds_read_b64_tr_b16 v[218:219], v35 offset:23040\n\t" \
  "s_waitcnt vmcnt(8)\n\t" \
  "v_mfma_f32_16x16x32_bf16 v[228:231], v[196:199], v[76:79], v[228:231]\n\t" \
  "ds_read_b64_tr_b16 v[220:221], v35 offset:26624\n\t" \
  "s_lshr_b64 s[90:91], %18, 44\n\t" \
  "s_and_b32 s90, s90, 15\n\t" \
  "s_lshl_b32 s90, s90, 15\n\t" \
  "s_add_u32 s90, s90, s92\n\t" \
  "s_addc_u32 s91, s93, 0\n\t" \
  "global_load_dwordx4 v[112:115], v53, s[90:91] offset:0 sc1\n\t" \
  "v_mfma_f32_16x16x32_bf16 v[228:231], v[192:195], v[72:75], v[228:231]\n\t" \
  "ds_read_b64_tr_b16 v[222:223], v35 offset:27136\n\t" \
  "v_mfma_f32_16x16x32_bf16 v[232:235], v[200:203], v[64:67], v[232:235]\n\t" \
  "ds_read_b64_tr_b16 v[192:193], v32 offset:30720\n\t" \
  "s_waitcnt lgkmcnt(6)\n\t" \
  "v_mfma_f32_16x16x32_bf16 v[232:235], v[204:207], v[68:71], v[232:235]\n\t" \
  "ds_read_b64_tr_b16 v[194:195], v32 offset:31232\n\t" \
  "v_mfma_f32_16x16x32_bf16 v[236:239], v[204:207], v[76:79], v[236:239]\n\t" \
  "ds_read_b64_tr_b16 v[196:197], v32 offset:34816\n\t" \
  "global_load_dwordx4 v[116:119], v53, s[90:91] offset:1024 sc1\n\t" \
  "v_mfma_f32_16x16x32_bf16 v[236:239], v[200:203], v[72:75], v[236:239]\n\t" \
  "ds_read_b64_tr_b16 v[198:199], v32 offset:35328\n\t" \
  "v_mfma_f32_16x16x32_bf16 v[240:243], v[208:211], v[64:67], v[240:243]\n\t" \
  "ds_read_b64_tr_b16 v[200:201], v33 offset:30720\n\t" \
  "s_waitcnt lgkmcnt(6)\n\t" \
  "v_mfma_f32_16x16x32_bf16 v[240:243], v[212:215], v[68:71], v[240:243]\n\t" \
  "ds_read_b64_tr_b16 v[202:203], v33 offset:31232\n\t" \
  "v_mfma_f32_16x16x32_bf16 v[244:247], v[212:215], v[76:79], v[244:247]\n\t" \
  "ds_read_b64_tr_b16 v[204:205], v33 offset:34816\n\t" \
  "global_load_dwordx4 v[120:123], v53, s[90:91] offset:2048 sc1\n\t" \
  "v_mfma_f32_16x16x32_bf16 v[244:247], v[208:211], v[72:75], v[244:247]\n\t" \
  "ds_read_b64_tr_b16 v[206:207], v33 offset:35328\n\t" \
  "v_mfma_f32_16x16x32_bf16 v[248:251], v[216:219], v[64:67], v[248:251]\n\t" \
  "ds_read_b64_tr_b16 v[208:209], v34 offset:30720\n\t" \
  "s_waitcnt lgkmcnt(6)\n\t" \
  "v_mfma_f32_16x16x32_bf16 v[248:251], v[220:223], v[68:71], v[248:251]\n\t" \
  "ds_read_b64_tr_b16 v[210:211], v34 offset:31232\n\t" \
  "v_mfma_f32_16x16x32_bf16 v[252:255], v[220:223], v[76:79], v[252:255]\n\t" \
  "ds_read_b64_tr_b16 v[212:213], v34 offset:34816\n\t" \
  "global_load_dwordx4 v[124:127], v53, s[90:91] offset:3072 sc1\n\t" \
  "v_mfma_f32_16x16x32_bf16 v[252:255], v[216:219], v[72:75], v[252:255]\n\t" \
  "ds_read_b64_tr_b16 v[214:215], v34 offset:35328\n\t" \
  "s_waitcnt vmcnt(10)\n\t" \
  "v_mfma_f32_16x16x32_bf16 v[224:227], v[192:195], v[80:83], v[224:227]\n\t" \
  "ds_read_b64_tr_b16 v[216:217], v35 offset:30720\n\t" \
  "s_waitcnt lgkmcnt(6)\n\t" \
  "v_mfma_f32_16x16x32_bf16 v[224:227], v[196:199], v[84:87], v[224:227]\n\t" \
  "ds_read_b64_tr_b16 v[218:219], v35 offset:31232\n\t" \
  "s_waitcnt vmcnt(8)\n\t" \
  "v_mfma_f32_16x16x32_bf16 v[228:231], v[196:199], v[92:95], v[228:231]\n\t" \
  "ds_read_b64_tr_b16 v[220:221], v35 offset:34816\n\t" \
  "s_lshr_b64 s[90:91], %18, 48\n\t" \
  "s_and_b32 s90, s90, 15\n\t" \
  "s_lshl_b32 s90, s90, 15\n\t" \
  "s_add_u32 s90, s90, s92\n\t" \
  "s_addc_u32 s91, s93, 0\n\t" \
  "global_load_dwordx4 v[128:131], v53, s[90:91] offset:0 sc1\n\t" \
  "v_mfma_f32_16x16x32_bf16 v[228:231], v[192:195], v[88:91], v[228:231]\n\t" \
  "ds_read_b64_tr_b16 v[222:223], v35 offset:35328\n\t" \
  "v_mfma_f32_16x16x32_bf16 v[232:235], v[200:203], v[80:83], v[232:235]\n\t" \
  "ds_read_b64_tr_b16 v[192:193], v32 offset:38912\n\t" \
  "s_waitcnt lgkmcnt(6)\n\t" \
  "v_mfma_f32_16x16x32_bf16 v[232:235], v[204:207], v[84:87], v[232:235]\n\t" \
  "ds_read_b64_tr_b16 v[194:195], v32 offset:39424\n\t" \
  "v_mfma_f32_16x16x32_bf16 v[236:239], v[204:207], v[92:95], v[236:239]\n\t" \
  "ds_read_b64_tr_b16 v[196:197], v32 offset:43008\n\t" \
  "global_load_dwordx4 v[132:135], v53, s[90:91] offset:1024 sc1\n\t" \
  "v_mfma_f32_16x16x32_bf16 v[236:239], v[200:203], v[88:91], v[236:239]\n\t" \
  "ds_read_b64_tr_b16 v[198:199], v32 offset:43520\n\t" \
  "v_mfma_f32_16x16x32_bf16 v[240:243], v[208:211], v[80:83], v[240:243]\n\t" \
  "ds_read_b64_tr_b16 v[200:201], v33 offset:38912\n\t" \
  "s_waitcnt lgkmcnt(6)\n\t" \
  "509:\n\t" \
  "s_cbranch_execz 26b\n\t" \
  "v_mfma_f32_16x16x32_bf16 v[240:243], v[212:215], v[84:87], v[240:243]\n\t" \
  "ds_read_b64_tr_b16 v[202:203], v33 offset:39424\n\t" \
  "v_mfma_f32_16x16x32_bf16 v[244:247], v[212:215], v[92:95], v[244:247]\n\t" \
  "ds_read_b64_tr_b16 v[204:205], v33 offset:43008\n\t" \
  "global_load_dwordx4 v[136:139], v53, s[90:91] offset:2048 sc1\n\t" \
  "v_mfma_f32_16x16x32_bf16 v[244:247], v[208:211], v[88:91], v[244:247]\n\t" \
  "ds_read_b64_tr_b16 v[206:207], v33 offset:43520\n\t" \
  "v_mfma_f32_16x16x32_bf16 v[248:251], v[216:219], v[80:83], v[248:251]\n\t" \
  "ds_read_b64_tr_b16 v[208:209], v34 offset:38912\n\t" \
  "s_waitcnt lgkmcnt(6)\n\t" \
  "v_mfma_f32_16x16x32_bf16 v[248:251], v[220:223], v[84:87], v[248:251]\n\t" \
  "ds_read_b64_tr_b16 v[210:211], v34 offset:39424\n\t" \
  "v_mfma_f32_16x16x32_bf16 v[252:255], v[220:223], v[92:95], v[252:255]\n\t" \
  "ds_read_b64_tr_b16 v[212:213], v34 offset:43008\n\t" \
  "global_load_dwordx4 v[140:143], v53, s[90:91] offset:3072 sc1\n\t" \
  "v_mfma_f32_16x16x32_bf16 v[252:255], v[216:219], v[88:91], v[252:255]\n\t" \
  "ds_read_b64_tr_b16 v[214:215], v34 offset:43520\n\t" \
  "s_waitcnt vmcnt(10)\n\t" \
  "v_mfma_f32_16x16x32_bf16 v[224:227], v[192:195], v[96:99], v[224:227]\n\t" \
  "ds_read_b64_tr_b16 v[216:217], v35 offset:38912\n\t" \
  "s_waitcnt lgkmcnt(6)\n\t" \
  "v_mfma_f32_16x16x32_bf16 v[224:227], v[196:199], v[100:103], v[224:227]\n\t" \
  "ds_read_b64_tr_b16 v[218:219], v35 offset:39424\n\t" \
  "s_waitcnt vmcnt(8)\n\t" \
  "v_mfma_f32_16x16x32_bf16 v[228:231], v[196:199], v[108:111], v[228:231]\n\t" \
  "ds_read_b64_tr_b16 v[220:221], v35 offset:43008\n\t" \
  "s_lshr_b64 s[90:91], %18, 52\n\t" \
  "s_and_b32 s90, s90, 15\n\t" \
  "s_lshl_b32 s90, s90, 15\n\t" \
  "s_add_u32 s90, s90, s92\n\t" \
  "s_addc_u32 s91, s93, 0\n\t" \
  "global_load_dwordx4 v[144:147], v53, s[90:91] offset:0 sc1\n\t" \
  "v_mfma_f32_16x16x32_bf16 v[228:231], v[192:195], v[104:107], v[228:231]\n\t" \
  "ds_read_b64_tr_b16 v[222:223], v35 offset:43520\n\t" \
  "v_mfma_f32_16x16x32_bf16 v[232:235], v[200:203], v[96:99], v[232:235]\n\t" \
  "ds_read_b64_tr_b16 v[192:193], v32 offset:47104\n\t" \
  "s_waitcnt lgkmcnt(6)\n\t" \
  "v_mfma_f32_16x16x32_bf16 v[232:235], v[204:207], v[100:103], v[232:235]\n\t" \
  "ds_read_b64_tr_b16 v[194:195], v32 offset:47616\n\t" \
  "v_mfma_f32_16x16x32_bf16 v[236:239], v[204:207], v[108:111], v[236:239]\n\t" \
  "ds_read_b64_tr_b16 v[196:197], v32 offset:51200\n\t" \
  "global_load_dwordx4 v[148:151], v53, s[90:91] offset:1024 sc1\n\t" \
  "v_mfma_f32_16x16x32_bf16 v[236:239], v[200:203], v[104:107], v[236:239]\n\t" \
  "ds_read_b64_tr_b16 v[198:199], v32 offset:51712\n\t" \
  "v_mfma_f32_16x16x32_bf16 v[240:243], v[208:211], v[96:99], v[240:243]\n\t" \
  "ds_read_b64_tr_b16 v[200:201], v33 offset:47104\n\t" \
  "s_waitcnt lgkmcnt(6)\n\t" \
  "v_mfma_f32_16x16x32_bf16 v[240:243], v[212:215], v[100:103], v[240:243]\n\t" \
  "ds_read_b64_tr_b16 v[202:203], v33 offset:47616\n\t" \
  "v_mfma_f32_16x16x32_bf16 v[244:247], v[212:215], v[108:111], v[244:247]\n\t" \
  "ds_read_b64_tr_b16 v[204:205], v33 offset:51200\n\t" \
  "global_load_dwordx4 v[152:155], v53, s[90:91] offset:2048 sc1\n\t" \
  "v_mfma_f32_16x16x32_bf16 v[244:247], v[208:211], v[104:107], v[244:247]\n\t" \
  "ds_read_b64_tr_b16 v[206:207], v33 offset:51712\n\t" \
  "v_mfma_f32_16x16x32_bf16 v[248:251], v[216:219], v[96:99], v[248:251]\n\t" \
  "ds_read_b64_tr_b16 v[208:209], v34 offset:47104\n\t" \
  "s_waitcnt lgkmcnt(6)\n\t" \
  "v_mfma_f32_16x16x32_bf16 v[248:251], v[220:223], v[100:103], v[248:251]\n\t" \
  "ds_read_b64_tr_b16 v[210:211], v34 offset:47616\n\t" \
  "v_mfma_f32_16x16x32_bf16 v[252:255], v[220:223], v[108:111], v[252:255]\n\t" \
  "ds_read_b64_tr_b16 v[212:213], v34 offset:51200\n\t" \
  "global_load_dwordx4 v[156:159], v53, s[90:91] offset:3072 sc1\n\t" \
  "v_mfma_f32_16x16x32_bf16 v[252:255], v[216:219], v[104:107], v[252:255]\n\t" \
  "ds_read_b64_tr_b16 v[214:215], v34 offset:51712\n\t" \
  "s_waitcnt vmcnt(10)\n\t" \
  "v_mfma_f32_16x16x32_bf16 v[224:227], v[192:195], v[112:115], v[224:227]\n\t" \
  "ds_read_b64_tr_b16 v[216:217], v35 offset:47104\n\t" \
  "s_waitcnt lgkmcnt(6)\n\t" \
  "v_mfma_f32_16x16x32_bf16 v[224:227], v[196:199], v[116:119], v[224:227]\n\t" \
  "ds_read_b64_tr_b16 v[218:219], v35 offset:47616\n\t" \
  "s_waitcnt vmcnt(8)\n\t" \
  "v_mfma_f32_16x16x32_bf16 v[228:231], v[196:199], v[124:127], v[228:231]\n\t" \
  "ds_read_b64_tr_b16 v[220:221], v35 offset:51200\n\t" \
  "s_lshr_b64 s[90:91], %18, 56\n\t" \
  "s_and_b32 s90, s90, 15\n\t" \
  "s_lshl_b32 s90, s90, 15\n\t" \
  "s_add_u32 s90, s90, s92\n\t" \
  "s_addc_u32 s91, s93, 0\n\t" \
  "global_load_dwordx4 v[160:163], v53, s[90:91] offset:0 sc1\n\t" \
  "v_mfma_f32_16x16x32_bf16 v[228:231], v[192:195], v[120:123], v[228:231]\n\t" \
  "ds_read_b64_tr_b16 v[222:223], v35 offset:51712\n\t" \
  "v_mfma_f32_16x16x32_bf16 v[232:235], v[200:203], v[112:115], v[232:235]\n\t" \
  "ds_read_b64_tr_b16 v[192:193], v32 offset:55296\n\t" \
  "s_waitcnt lgkmcnt(6)\n\t" \
  "v_mfma_f32_16x16x32_bf16 v[232:235], v[204:207], v[116:119], v[232:235]\n\t" \
  "ds_read_b64_tr_b16 v[194:195], v32 offset:55808\n\t" \
  "v_mfma_f32_16x16x32_bf16 v[236:239], v[204:207], v[124:127], v[236:239]\n\t" \
  "ds_read_b64_tr_b16 v[196:197], v32 offset:59392\n\t" \
  "global_load_dwordx4 v[164:167], v53, s[90:91] offset:1024 sc1\n\t" \
  "v_mfma_f32_16x16x32_bf16 v[236:239], v[200:203], v[120:123], v[236:239]\n\t" \
  "ds_read_b64_tr_b16 v[198:199], v32 offset:59904\n\t" \
  "v_mfma_f32_16x16x32_bf16 v[240:243], v[208:211], v[112:115], v[240:243]\n\t" \
  "ds_read_b64_tr_b16 v[200:201], v33 offset:55296\n\t" \
  "s_waitcnt lgkmcnt(6)\n\t" \
  "v_mfma_f32_16x16x32_bf16 v[240:243], v[212:215], v[116:119], v[240:243]\n\t" \
  "ds_read_b128 v[40:43], v56 offset:80\n\t" \
  "ds_read_b64_tr_b16 v[202:203], v33 offset:55808\n\t" \
  "v_mfma_f32_16x16x32_bf16 v[244:247], v[212:215], v[124:127], v[244:247]\n\t" \
  "ds_read_b64_tr_b16 v[204:205], v33 offset:59392\n\t" \
  "global_load_dwordx4 v[168:171], v53, s[90:91] offset:2048 sc1\n\t" \
  "v_mfma_f32_16x16x32_bf16 v[244:247], v[208:211], v[120:123], v[244:247]\n\t" \
  "ds_read_b64_tr_b16 v[206:207], v33 offset:59904\n\t" \
  "v_mfma_f32_16x16x32_bf16 v[248:251], v[216:219], v[112:115], v[248:251]\n\t" \
  "ds_read_b64_tr_b16 v[208:209], v34 offset:55296\n\t" \
  "s_waitcnt lgkmcnt(7)\n\t" \
  "v_mfma_f32_16x16x32_bf16 v[248:251], v[220:223], v[116:119], v[248:251]\n\t" \
  "ds_read_b64_tr_b16 v[210:211], v34 offset:55808\n\t" \
  "v_mfma_f32_16x16x32_bf16 v[252:255], v[220:223], v[124:127], v[252:255]\n\t" \
  "ds_read_b64_tr_b16 v[212:213], v34 offset:59392\n\t" \
  "global_load_dwordx4 v[172:175], v53, s[90:91] offset:3072 sc1\n\t" \
  "v_mfma_f32_16x16x32_bf16 v[252:255], v[216:219], v[120:123], v[252:255]\n\t" \
  "ds_read_b64_tr_b16 v[214:215], v34 offset:59904\n\t" \
  "s_waitcnt vmcnt(10)\n\t" \
  "v_mfma_f32_16x16x32_bf16 v[224:227], v[192:195], v[128:131], v[224:227]\n\t" \
  "s_waitcnt lgkmcnt(7)\n\t" \
  "v_min_u32 v40, v40, v41\n\t" \
  "v_min3_u32 v40, v40, v42, v43\n\t" \
  "ds_read_b64_tr_b16 v[216:217], v35 offset:55296\n\t" \
  "v_mfma_f32_16x16x32_bf16 v[224:227], v[196:199], v[132:135], v[224:227]\n\t" \
  "v_readfirstlane_b32 vcc_lo, v40\n\t" \
  "ds_read_b64_tr_b16 v[218:219], v35 offset:55808\n\t" \
  "s_waitcnt vmcnt(8)\n\t" \
  "v_mfma_f32_16x16x32_bf16 v[228:231], v[196:199], v[140:143], v[228:231]\n\t" \
  "s_cmp_ge_u32 vcc_lo, 16\n\t" \
  "s_cbranch_scc0 40f\n\t" \
  "41:\n\t" \
  "ds_read_b64_tr_b16 v[220:221], v35 offset:59392\n\t" \
  "s_lshr_b64 s[90:91], %18, 60\n\t" \
  "s_and_b32 s90, s90, 15\n\t" \
  "s_lshl_b32 s90, s90, 15\n\t" \
  "s_add_u32 s90, s90, s92\n\t" \
  "s_addc_u32 s91, s93, 0\n\t" \
  "global_load_dwordx4 v[176:179], v53, s[90:91] offset:0 sc1\n\t" \
  "v_mfma_f32_16x16x32_bf16 v[228:231], v[192:195], v[136:139], v[228:231]\n\t" \
  "ds_read_b64_tr_b16 v[222:223], v35 offset:59904\n\t" \
  "s_waitcnt lgkmcnt(6)\n\t" \
  "v_mfma_f32_16x16x32_bf16 v[232:235], v[200:203], v[128:131], v[232:235]\n\t" \
  "ds_read_b64_tr_b16 v[192:193], v36 offset:2304\n\t" \
  "v_mfma_f32_16x16x32_bf16 v[232:235], v[204:207], v[132:135], v[232:235]\n\t" \
  "ds_read_b64_tr_b16 v[194:195], v36 offset:2816\n\t" \
  "v_mfma_f32_16x16x32_bf16 v[236:239], v[204:207], v[140:143], v[236:239]\n\t" \
  "ds_read_b64_tr_b16 v[196:197], v36 offset:10752\n\t" \
  "global_load_dwordx4 v[180:183], v53, s[90:91] offset:1024 sc1\n\t" \
  "v_mfma_f32_16x16x32_bf16 v[236:239], v[200:203], v[136:139], v[236:239]\n\t" \
  "ds_read_b64_tr_b16 v[198:199], v36 offset:11264\n\t" \
  "v_mfma_f32_16x16x32_bf16 v[240:243], v[208:211], v[128:131], v[240:243]\n\t" \
  "ds_read_b64_tr_b16 v[200:201], v37 offset:2304\n\t" \
  "s_waitcnt lgkmcnt(6)\n\t" \
  "v_mfma_f32_16x16x32_bf16 v[240:243], v[212:215], v[132:135], v[240:243]\n\t" \
  "ds_read_b64_tr_b16 v[202:203], v37 offset:2816\n\t" \
  "v_mfma_f32_16x16x32_bf16 v[244:247], v[212:215], v[140:143], v[244:247]\n\t" \
  "ds_read_b64_tr_b16 v[204:205], v37 offset:10752\n\t" \
  "global_load_dwordx4 v[184:187], v53, s[90:91] offset:2048 sc1\n\t" \
  "v_mfma_f32_16x16x32_bf16 v[244:247], v[208:211], v[136:139], v[244:247]\n\t" \
  "ds_read_b64_tr_b16 v[206:207], v37 offset:11264\n\t" \
  "v_mfma_f32_16x16x32_bf16 v[248:251], v[216:219], v[128:131], v[248:251]\n\t" \
  "ds_read_b64_tr_b16 v[208:209], v38 offset:2304\n\t" \
  "s_waitcnt lgkmcnt(6)\n\t" \
  "v_mfma_f32_16x16x32_bf16 v[248:251], v[220:223], v[132:135], v[248:251]\n\t" \
  "ds_read_b64_tr_b16 v[210:211], v38 offset:2816\n\t" \
  "v_mfma_f32_16x16x32_bf16 v[252:255], v[220:223], v[140:143], v[252:255]\n\t" \
  "ds_read_b64_tr_b16 v[212:213], v38 offset:10752\n\t" \
  "global_load_dwordx4 v[188:191], v53, s[90:91] offset:3072 sc1\n\t" \
  "v_mfma_f32_16x16x32_bf16 v[252:255], v[216:219], v[136:139], v[252:255]\n\t" \
  "ds_read_b64_tr_b16 v[214:215], v38 offset:11264\n\t" \
  "s_waitcnt vmcnt(10)\n\t" \
  "v_mfma_f32_16x16x32_bf16 v[224:227], v[192:195], v[144:147], v[224:227]\n\t" \
  "ds_read_b64_tr_b16 v[216:217], v39 offset:2304\n\t" \
  "s_waitcnt lgkmcnt(6)\n\t" \
  "v_mfma_f32_16x16x32_bf16 v[224:227], v[196:199], v[148:151], v[224:227]\n\t" \
  "ds_read_b64_tr_b16 v[218:219], v39 offset:2816\n\t" \
  "s_waitcnt vmcnt(8)\n\t" \
  "v_mfma_f32_16x16x32_bf16 v[228:231], v[196:199], v[156:159], v[228:231]\n\t" \
  "ds_read_b64_tr_b16 v[220:221], v39 offset:10752\n\t" \
  "v_mfma_f32_16x16x32_bf16 v[228:231], v[192:195], v[152:155], v[228:231]\n\t" \
  "ds_read_b64_tr_b16 v[222:223], v39 offset:11264\n\t" \
  "v_mfma_f32_16x16x32_bf16 v[232:235], v[200:203], v[144:147], v[232:235]\n\t" \
  "ds_read_b64_tr_b16 v[192:193], v36 offset:19200\n\t" \
  "s_waitcnt lgkmcnt(6)\n\t" \
  "v_mfma_f32_16x16x32_bf16 v[232:235], v[204:207], v[148:151], v[232:235]\n\t" \
  "ds_read_b64_tr_b16 v[194:195], v36 offset:19712\n\t" \
  "v_mfma_f32_16x16x32_bf16 v[236:239], v[204:207], v[156:159], v[236:239]\n\t" \
  "ds_read_b64_tr_b16 v[196:197], v36 offset:27648\n\t" \
  "v_mfma_f32_16x16x32_bf16 v[236:239], v[200:203], v[152:155], v[236:239]\n\t" \
  "ds_read_b64_tr_b16 v[198:199], v36 offset:28160\n\t" \
  "v_mfma_f32_16x16x32_bf16 v[240:243], v[208:211], v[144:147], v[240:243]\n\t" \
  "ds_read_b64_tr_b16 v[200:201], v37 offset:19200\n\t" \
  "s_waitcnt lgkmcnt(6)\n\t" \
  "v_mfma_f32_16x16x32_bf16 v[240:243], v[212:215], v[148:151], v[240:243]\n\t" \
  "ds_read_b128 v[40:43], v56 offset:160\n\t" \
  "ds_read_b64_tr_b16 v[202:203], v37 offset:19712\n\t" \
  "v_mfma_f32_16x16x32_bf16 v[244:247], v[212:215], v[156:159], v[244:247]\n\t" \
  "ds_read_b64_tr_b16 v[204:205], v37 offset:27648\n\t" \
  "v_mfma_f32_16x16x32_bf16 v[244:247], v[208:211], v[152:155], v[244:247]\n\t" \
  "ds_read_b64_tr_b16 v[206:207], v37 offset:28160\n\t" \
  "v_mfma_f32_16x16x32_bf16 v[248:251], v[216:219], v[144:147], v[248:251]\n\t" \
  "ds_read_b64_tr_b16 v[208:209], v38 offset:19200\n\t" \
  "s_waitcnt lgkmcnt(7)\n\t" \
  "v_mfma_f32_16x16x32_bf16 v[248:251], v[220:223], v[148:151], v[248:251]\n\t" \
  "ds_read_b64_tr_b16 v[210:211], v38 offset:19712\n\t" \
  "v_mfma_f32_16x16x32_bf16 v[252:255], v[220:223], v[156:159], v[252:255]\n\t" \
  "ds_read_b64_tr_b16 v[212:213], v38 offset:27648\n\t" \
  "v_mfma_f32_16x16x32_bf16 v[252:255], v[216:219], v[152:155], v[252:255]\n\t" \
  "ds_read_b64_tr_b16 v[214:215], v38 offset:28160\n\t" \
  "s_waitcnt vmcnt(6)\n\t" \
  "v_mfma_f32_16x16x32_bf16 v[224:227], v[192:195], v[160:163], v[224:227]\n\t" \
  "s_waitcnt lgkmcnt(7)\n\t" \
  "v_min_u32 v40, v40, v41\n\t" \
  "v_min3_u32 v40, v40, v42, v43\n\t" \
  "ds_read_b64_tr_b16 v[216:217], v39 offset:19200\n\t" \
  "v_mfma_f32_16x16x32_bf16 v[224:227], v[196:199], v[164:167], v[224:227]\n\t" \
  "v_readfirstlane_b32 vcc_lo, v40\n\t" \
  "ds_read_b64_tr_b16 v[218:219], v39 offset:19712\n\t" \
  "s_waitcnt vmcnt(4)\n\t" \
  "v_mfma_f32_16x16x32_bf16 v[228:231], v[196:199], v[172:175], v[228:231]\n\t" \
  "s_cmp_ge_u32 vcc_lo, 16\n\t" \
  "s_cbranch_scc0 42f\n\t" \
  "43:\n\t" \
  "ds_read_b64_tr_b16 v[220:221], v39 offset:27648\n\t" \
  "v_mfma_f32_16x16x32_bf16 v[228:231], v[192:195], v[168:171], v[228:231]\n\t" \
  "ds_read_b64_tr_b16 v[222:223], v39 offset:28160\n\t" \
  "s_waitcnt lgkmcnt(6)\n\t" \
  "v_mfma_f32_16x16x32_bf16 v[232:235], v[200:203], v[160:163], v[232:235]\n\t" \
  "ds_read_b64_tr_b16 v[192:193], v28 offset:0\n\t" \
  "v_mfma_f32_16x16x32_bf16 v[232:235], v[204:207], v[164:167], v[232:235]\n\t" \
  "ds_read_b64_tr_b16 v[194:195], v28 offset:512\n\t" \
  "v_mfma_f32_16x16x32_bf16 v[236:239], v[204:207], v[172:175], v[236:239]\n\t" \
  "ds_read_b64_tr_b16 v[196:197], v28 offset:4096\n\t" \
  "v_mfma_f32_16x16x32_bf16 v[236:239], v[200:203], v[168:171], v[236:239]\n\t" \
  "ds_read_b64_tr_b16 v[198:199], v28 offset:4608\n\t" \
  "v_mfma_f32_16x16x32_bf16 v[240:243], v[208:211], v[160:163], v[240:243]\n\t" \
  "ds_read_b64_tr_b16 v[200:201], v29 offset:0\n\t" \
  "s_waitcnt lgkmcnt(6)\n\t" \
  "v_mfma_f32_16x16x32_bf16 v[240:243], v[212:215], v[164:167], v[240:243]\n\t" \
  "ds_read_b64_tr_b16 v[202:203], v29 offset:512\n\t" \
  "v_mfma_f32_16x16x32_bf16 v[244:247], v[212:215], v[172:175], v[244:247]\n\t" \
  "ds_read_b64_tr_b16 v[204:205], v29 offset:4096\n\t" \
  "v_mfma_f32_16x16x32_bf16 v[244:247], v[208:211], v[168:171], v[244:247]\n\t" \
  "ds_read_b64_tr_b16 v[206:207], v29 offset:4608\n\t" \
  "v_mfma_f32_16x16x32_bf16 v[248:251], v[216:219], v[160:163], v[248:251]\n\t" \
  "ds_read_b64_tr_b16 v[208:209], v30 offset:0\n\t" \
  "s_waitcnt lgkmcnt(6)\n\t" \
  "v_mfma_f32_16x16x32_bf16 v[248:251], v[220:223], v[164:167], v[248:251]\n\t" \
  "ds_read_b64_tr_b16 v[210:211], v30 offset:512\n\t" \
  "v_mfma_f32_16x16x32_bf16 v[252:255], v[220:223], v[172:175], v[252:255]\n\t" \
  "ds_read_b64_tr_b16 v[212:213], v30 offset:4096\n\t" \
  "v_mfma_f32_16x16x32_bf16 v[252:255], v[216:219], v[168:171], v[252:255]\n\t" \
  "ds_read_b64_tr_b16 v[214:215], v30 offset:4608\n\t" \
  "s_waitcnt vmcnt(2)\n\t" \
  "v_mfma_f32_16x16x32_bf16 v[224:227], v[192:195], v[176:179], v[224:227]\n\t" \
  "ds_read_b64_tr_b16 v[216:217], v31 offset:0\n\t" \
  "s_waitcnt lgkmcnt(6)\n\t" \
  "v_mfma_f32_16x16x32_bf16 v[224:227], v[196:199], v[180:183], v[224:227]\n\t" \
  "ds_read_b64_tr_b16 v[218:219], v31 offset:512\n\t" \
  "s_waitcnt vmcnt(0)\n\t" \
  "v_mfma_f32_16x16x32_bf16 v[228:231], v[196:199], v[188:191], v[228:231]\n\t" \
  "ds_read_b64_tr_b16 v[220:221], v31 offset:4096\n\t" \
  "v_mfma_f32_16x16x32_bf16 v[228:231], v[192:195], v[184:187], v[228:231]\n\t" \
  "ds_read_b64_tr_b16 v[222:223], v31 offset:4608\n\t" \
  "v_mfma_f32_16x16x32_bf16 v[232:235], v[200:203], v[176:179], v[232:235]\n\t" \
  "s_waitcnt lgkmcnt(5)\n\t" \
  "v_mfma_f32_16x16x32_bf16 v[232:235], v[204:207], v[180:183], v[232:235]\n\t" \
  "v_mfma_f32_16x16x32_bf16 v[236:239], v[204:207], v[188:191], v[236:239]\n\t" \
  "v_cvt_pk_bf16_f32 v224, v224, v225\n\t" \
  "v_cvt_pk_bf16_f32 v225, v226, v227\n\t" \
  "v_mfma_f32_16x16x32_bf16 v[236:239], v[200:203], v[184:187], v[236:239]\n\t" \
  "ds_write_b64 v57, v[224:225] offset:0\n\t" \
  "v_mfma_f32_16x16x32_bf16 v[240:243], v[208:211], v[176:179], v[240:243]\n\t" \
  "v_cvt_pk_bf16_f32 v228, v228, v229\n\t" \
  "v_cvt_pk_bf16_f32 v229, v230, v231\n\t" \
  "s_waitcnt lgkmcnt(2)\n\t" \
  "v_mfma_f32_16x16x32_bf16 v[240:243], v[212:215], v[180:183], v[240:243]\n\t" \
  "ds_write_b64 v57, v[228:229] offset:2048\n\t" \
  "v_mfma_f32_16x16x32_bf16 v[244:247], v[212:215], v[188:191], v[244:247]\n\t" \
  "v_cvt_pk_bf16_f32 v232, v232, v233\n\t" \
  "v_cvt_pk_bf16_f32 v233, v234, v235\n\t" \
  "v_mfma_f32_16x16x32_bf16 v[244:247], v[208:211], v[184:187], v[244:247]\n\t" \
  "ds_write_b64 v58, v[232:233] offset:0\n\t" \
  "v_mfma_f32_16x16x32_bf16 v[248:251], v[216:219], v[176:179], v[248:251]\n\t" \
  "v_cvt_pk_bf16_f32 v236, v236, v237\n\t" \
  "v_cvt_pk_bf16_f32 v237, v238, v239\n\t" \
  "s_waitcnt lgkmcnt(3)\n\t" \
  "v_mfma_f32_16x16x32_bf16 v[248:251], v[220:223], v[180:183], v[248:251]\n\t" \
  "ds_write_b64 v58, v[236:237] offset:2048\n\t" \
  "v_mfma_f32_16x16x32_bf16 v[252:255], v[220:223], v[188:191], v[252:255]\n\t" \
  "v_cvt_pk_bf16_f32 v240, v240, v241\n\t" \
  "v_cvt_pk_bf16_f32 v241, v242, v243\n\t" \
  "v_mfma_f32_16x16x32_bf16 v[252:255], v[216:219], v[184:187], v[252:255]\n\t" \
  "ds_write_b64 v59, v[240:241] offset:0\n\t" \
  "v_cvt_pk_bf16_f32 v244, v244, v245\n\t" \
  "v_cvt_pk_bf16_f32 v245, v246, v247\n\t" \
  "ds_write_b64 v59, v[244:245] offset:2048\n\t" \
  "v_cvt_pk_bf16_f32 v248, v248, v249\n\t" \
  "v_cvt_pk_bf16_f32 v249, v250, v251\n\t" \
  "ds_write_b64 v60, v[248:249] offset:0\n\t" \
  "s_nop 0\n\t" \
  "v_cvt_pk_bf16_f32 v252, v252, v253\n\t" \
  "v_cvt_pk_bf16_f32 v253, v254, v255\n\t" \
  "ds_write_b64 v60, v[252:253] offset:2048\n\t" \
  "ds_read_b128 v[64:67], v61 offset:0\n\t" \
  "ds_read_b128 v[68:71], v61 offset:1024\n\t" \
  "ds_read_b128 v[72:75], v61 offset:2048\n\t" \
  "ds_read_b128 v[76:79], v61 offset:3072\n\t" \
  "s_waitcnt lgkmcnt(3)\n\t" \
  "global_store_dwordx4 v62, v[64:67], %22 offset:-4096" OUTPOL "\n\t" \
  "s_waitcnt lgkmcnt(2)\n\t" \
  "global_store_dwordx4 v63, v[68:71], %22 offset:-4096" OUTPOL "\n\t" \
  "s_waitcnt lgkmcnt(1)\n\t" \
  "global_store_dwordx4 v62, v[72:75], %22 offset:0" OUTPOL "\n\t" \
  "s_waitcnt lgkmcnt(0)\n\t" \
  "global_store_dwordx4 v63, v[76:79], %22 offset:0" OUTPOL "\n\t" \
  "s_branch 19f\n\t" \
  "23:\n\t" \
  "510:\n\t" \
  "s_cbranch_execz 26b\n\t" \
  "s_waitcnt vmcnt(0)\n\t" \
  "s_mov_b64 exec, 1\n\t" \
  "ds_add_rtn_u32 v10, v56, v10 offset:16\n\t" \
  "s_mov_b32 s74, 16\n\t" \
  "s_waitcnt lgkmcnt(0)\n\t" \
  "v_readfirstlane_b32 vcc_lo, v10\n\t" \
  "s_mov_b64 exec, 0xffff\n\t" \
  "s_nop 0\n\t" \
  "s_cmp_eq_u32 vcc_lo, 3\n\t" \
  "s_cbranch_scc0 22f\n\t" \
  "global_store_dword v11, v55, %19 sc1\n\t" \
  "22:\n\t" \
  "s_mov_b32 exec_lo, -1\n\t" \
  "global_load_dword v2, v1, %14 offset:-4096 sc1\n\t" \
  "s_mov_b64 exec, -1\n\t" \
  "s_setpc_b64 s[68:69]\n\t" \
  "7:\n\t" \
  "s_sleep 1\n\t" \
  "ds_read_b128 v[60:63], v56 offset:64\n\t" \
  "ds_read_b64_tr_b16 v[108:109], v5 offset:0\n\t" \
  "ds_read_b64_tr_b16 v[110:111], v5 offset:16\n\t" \
  "ds_read_b128 v[112:115], v6 offset:16384\n\t" \
  "ds_read_b128 v[116:119], v6 offset:15360\n\t" \
  "ds_read_b64_tr_b16 v[120:121], v5 offset:512\n\t" \
  "ds_read_b64_tr_b16 v[122:123], v5 offset:528\n\t" \
  "ds_read_b128 v[124:127], v6 offset:14336\n\t" \
  "ds_read_b64_tr_b16 v[128:129], v5 offset:1024\n\t" \
  "ds_read_b64_tr_b16 v[130:131], v5 offset:1040\n\t" \
  "ds_read_b128 v[132:135], v6 offset:13312\n\t" \
  "ds_read_b64_tr_b16 v[136:137], v5 offset:1536\n\t" \
  "ds_read_b64_tr_b16 v[138:139], v5 offset:1552\n\t" \
  "ds_read_b128 v[140:143], v6 offset:12288\n\t" \
  "ds_read_b64_tr_b16 v[144:145], v5 offset:2048\n\t" \
  "s_waitcnt lgkmcnt(0)\n\t" \
  "v_and_b32 v60, v60, v61\n\t" \
  "v_and_b32 v62, v62, v63\n\t" \
  "v_mov_b32 v12, %13\n\t" \
  "v_and_b32 v60, v60, v62\n\t" \
  "s_nop 0\n\t" \
  "v_readfirstlane_b32 vcc_lo, v60\n\t" \
  "s_nop 1\n\t" \
  "s_cmp_eq_u32 vcc_lo, 0\n\t" \
  "s_cbranch_scc1 7b\n\t" \
  "s_branch 6b\n\t" \
  "25:\n\t" \
  "511:\n\t" \
  "s_cbranch_execz 26b\n\t" \
  "global_load_dwordx4 v[64:67], v53, s[90:91] offset:0 sc1\n\t" \
  "global_load_dwordx4 v[68:71], v53, s[90:91] offset:1024 sc1\n\t" \
  "global_load_dwordx4 v[72:75], v53, s[90:91] offset:2048 sc1\n\t" \
  "global_load_dwordx4 v[76:79], v53, s[90:91] offset:3072 sc1\n\t" \
  "s_swappc_b64 s[70:71], s[68:69]\n\t" \
  "global_load_dwordx4 v[80:83], v53, s[90:91] offset:0 sc1\n\t" \
  "global_load_dwordx4 v[84:87], v53, s[90:91] offset:1024 sc1\n\t" \
  "global_load_dwordx4 v[88:91], v53, s[90:91] offset:2048 sc1\n\t" \
  "global_load_dwordx4 v[92:95], v53, s[90:91] offset:3072 sc1\n\t" \
  "s_swappc_b64 s[70:71], s[68:69]\n\t" \
  "36:\n\t" \
  "ds_read_b128 v[40:43], v56 offset:80\n\t" \
  "s_waitcnt lgkmcnt(0)\n\t" \
  "v_min_u32 v40, v40, v41\n\t" \
  "v_min3_u32 v40, v40, v42, v43\n\t" \
  "s_nop 1\n\t" \
  "v_readfirstlane_b32 vcc_lo, v40\n\t" \
  "s_nop 1\n\t" \
  "s_cmp_ge_u32 vcc_lo, 7\n\t" \
  "s_cbranch_scc0 36b\n\t" \
  "s_branch 37b\n\t" \
  "38:\n\t" \
  "ds_read_b128 v[40:43], v56 offset:80\n\t" \
  "s_waitcnt lgkmcnt(0)\n\t" \
  "v_min_u32 v40, v40, v41\n\t" \
  "v_min3_u32 v40, v40, v42, v43\n\t" \
  "s_nop 1\n\t" \
  "v_readfirstlane_b32 vcc_lo, v40\n\t" \
  "s_nop 1\n\t" \
  "s_cmp_ge_u32 vcc_lo, 13\n\t" \
  "s_cbranch_scc0 38b\n\t" \
  "s_branch 39b\n\t" \
  "40:\n\t" \
  "ds_read_b128 v[40:43], v56 offset:80\n\t" \
  "s_waitcnt lgkmcnt(0)\n\t" \
  "v_min_u32 v40, v40, v41\n\t" \
  "v_min3_u32 v40, v40, v42, v43\n\t" \
  "s_nop 1\n\t" \
  "v_readfirstlane_b32 vcc_lo, v40\n\t" \
  "s_nop 1\n\t" \
  "s_cmp_ge_u32 vcc_lo, 16\n\t" \
  "s_cbranch_scc0 40b\n\t" \
  "s_branch 41b\n\t" \
  "42:\n\t" \
  "ds_read_b128 v[40:43], v56 offset:160\n\t" \
  "s_waitcnt lgkmcnt(0)\n\t" \
  "v_min_u32 v40, v40, v41\n\t" \
  "v_min3_u32 v40, v40, v42, v43\n\t" \
  "s_nop 1\n\t" \
  "v_readfirstlane_b32 vcc_lo, v40\n\t" \
  "s_nop 1\n\t" \
  "s_cmp_ge_u32 vcc_lo, 16\n\t" \
  "s_cbranch_scc0 42b\n\t" \
  "s_branch 43b\n\t" \
  "46:\n\t" \
  "s_cmp_eq_u32 %13, 0\n\t" \
  "s_cbranch_scc1 48f\n\t" \
  "s_sleep 1\n\t" \
  "ds_read_b32 v16, v56 offset:96\n\t" \
  "s_waitcnt lgkmcnt(0)\n\t" \
  "v_readfirstlane_b32 vcc_lo, v16\n\t" \
  "s_nop 1\n\t" \
  "s_cmp_ge_u32 vcc_lo, 1\n\t" \
  "s_cbranch_scc0 46b\n\t" \
  "s_branch 47b\n\t" \
  "48:\n\t" \
  "v_bfe_u32 v19, v12, 4, 4\n\t" \
  "v_lshlrev_b32 v19, 2, v19\n\t" \
  "s_mov_b32 s90, 0\n\t" \
  "148:\n\t" \
  "global_load_dword v16, v19, %21 sc1\n\t" \
  "s_add_u32 s90, s90, 1\n\t" \
  "s_waitcnt vmcnt(0)\n\t" \
  "v_cmp_ne_u32 vcc, v55, v16\n\t" \
  "s_mov_b32 s88, %26\n\t" \
  "s_mov_b32 s89, s88\n\t" \
  "s_and_b64 vcc, vcc, s[88:89]\n\t" \
  "s_cbranch_scc0 248f\n\t" \
  "s_sleep 2\n\t" \
  "s_cmp_lt_u32 s90, 0x400000\n\t" \
  "s_cbranch_scc1 148b\n\t" \
  "248:\n\t" \
  "v_mov_b32 v16, 1\n\t" \
  "ds_max_u32 v56, v16 offset:96\n\t" \
  "s_branch 47b\n\t" \
  "49:\n\t" \
  "s_cmp_eq_u32 %13, 0\n\t" \
  "s_cbranch_scc1 51f\n\t" \
  "s_sleep 1\n\t" \
  "ds_read_b32 v16, v56 offset:96\n\t" \
  "s_waitcnt lgkmcnt(0)\n\t" \
  "v_readfirstlane_b32 vcc_lo, v16\n\t" \
  "s_nop 1\n\t" \
  "s_cmp_ge_u32 vcc_lo, 2\n\t" \
  "s_cbranch_scc0 49b\n\t" \
  "s_branch 50b\n\t" \
  "51:\n\t" \
  "v_bfe_u32 v19, v12, 4, 4\n\t" \
  "v_lshlrev_b32 v19, 2, v19\n\t" \
  "s_mov_b32 s90, 0\n\t" \
  "151:\n\t" \
  "global_load_dword v16, v19, %21 sc1\n\t" \
  "s_add_u32 s90, s90, 1\n\t" \
  "s_waitcnt vmcnt(0)\n\t" \
  "v_cmp_ne_u32 vcc, v55, v16\n\t" \
  "s_mov_b32 s88, %26\n\t" \
  "s_mov_b32 s89, s88\n\t" \
  "s_and_b64 vcc, vcc, -1\n\t" \
  "s_cbranch_scc0 251f\n\t" \
  "s_sleep 2\n\t" \
  "s_cmp_lt_u32 s90, 0x400000\n\t" \
  "s_cbranch_scc1 151b\n\t" \
  "251:\n\t" \
  "v_mov_b32 v16, 2\n\t" \
  "ds_max_u32 v56, v16 offset:96\n\t" \
  "s_branch 50b\n\t" \
  "19:\n\t"
#define C_CLOB "v10","v11","v12","v13","v18","v19","v20","v21","v22","v23","v24","v25","v26","v27","v28","v29","v30","v31","v32","v33","v34","v35","v36","v37","v38","v39","v40","v41","v42","v43","v44","v45","v46","v47","v48","v49","v50","v53","v55","v56","v57","v58","v59","v60","v61","v62","v63","v64","v65","v66","v67","v68","v69","v70","v71","v72","v73","v74","v75","v76","v77","v78","v79","v80","v81","v82","v83","v84","v85","v86","v87","v88","v89","v90","v91","v92","v93","v94","v95","v96","v97","v98","v99","v100","v101","v102","v103","v104","v105","v106","v107","v108","v109","v110","v111","v112","v113","v114","v115","v116","v117","v118","v119","v120","v121","v122","v123","v124","v125","v126","v127","v128","v129","v130","v131","v132","v133","v134","v135","v136","v137","v138","v139","v140","v141","v142","v143","v144","v145","v146","v147","v148","v149","v150","v151","v152","v153","v154","v155","v156","v157","v158","v159","v160","v161","v162","v163","v164","v165","v166","v167","v168","v169","v170","v171","v172","v173","v174","v175","v176","v177","v178","v179","v180","v181","v182","v183","v184","v185","v186","v187","v188","v189","v190","v191","v192","v193","v194","v195","v196","v197","v198","v199","v200","v201","v202","v203","v204","v205","v206","v207","v208","v209","v210","v211","v212","v213","v214","v215","v216","v217","v218","v219","v220","v221","v222","v223","v224","v225","v226","v227","v228","v229","v230","v231","v232","v233","v234","v235","v236","v237","v238","v239","v240","v241","v242","v243","v244","v245","v246","v247","v248","v249","v250","v251","v252","v253","v254","v255"

static_assert(KVP<13 && KVP>TGS+1, "");

#define NST 48
#define TS(i)
#define STW(i,v) asm volatile("s_nop 0\n\tv_writelane_b32 %0, %1, %2" : "+v"(stv) : "s"(v), "n"(i))
#define TSDUMP() do{ unsigned r0_; asm volatile("s_nop 4\n\tv_readlane_b32 %0, %1, 62\n\ts_nop 4" : "=&s"(r0_) : "v"(stv)); \
  if((bid&7)==PX) while((unsigned)__builtin_amdgcn_s_memrealtime()-r0_<4000u) __builtin_amdgcn_s_sleep(1); }while(0)
#define TSB(i) do{ if(w==3) TS(i); }while(0)
#define TSR(i)
#define TSH(i) do{ if(w!=3) TS(i); }while(0)
#define TSDECL() unsigned stv=0

#define TSF(i)
typedef unsigned long long u64;
typedef u64 u64x8 __attribute__((ext_vector_type(8)));
typedef u64 u64x4 __attribute__((ext_vector_type(4)));
typedef unsigned u32x8 __attribute__((ext_vector_type(8)));
typedef unsigned u32x16 __attribute__((ext_vector_type(16)));
template<class T> __device__ __forceinline__ T sg(T v){
  if constexpr(sizeof(T)==4) return (T)__builtin_amdgcn_readfirstlane((unsigned)v); else return v; }
#define GP(T,v) ((T*)(__attribute__((address_space(1))) T*)(v))
__global__ __launch_bounds__(512,2) __attribute__((aligned(4096))) void fused_kernel(const u16* x_, const u16* wqkv_, const u16* kin_, const u16* vin_, const u16* shk_, const u16* hk_,
    const u16* wo_, const u16* qin_, const u16* sD_, u16* out_, u16* ws_, u16* unused_)
{
#define ST0() unsigned long long T0, R0; asm volatile("s_memtime %0\n\ts_memrealtime %1" : "=&s"(T0), "=&s"(R0) :: "memory");
#define ST1() asm volatile("" : "+s"(T0), "+s"(R0));
#define ARGS0() u64x8 ka; u64x4 kb; asm volatile("s_load_dwordx16 %0, %2, 0\n\ts_load_dwordx8 %1, %2, 64" : "=&s"(ka), "=&s"(kb) : "s"(__builtin_amdgcn_kernarg_segment_ptr()) : "memory")
#define ARGS() asm volatile("s_waitcnt lgkmcnt(0)" : "+s"(ka), "+s"(kb) :: "memory"); ST1() \
  const u16 *x=GP(const u16,ka[0]), *wqkv=GP(const u16,ka[1]), *kin=GP(const u16,ka[2]), *vin=GP(const u16,ka[3]), *shk=GP(const u16,ka[4]), *hk=GP(const u16,ka[5]), *wo=GP(const u16,ka[6]), *qin=GP(const u16,ka[7]), *sD=GP(const u16,kb[0]); \
  u16 *out=GP(u16,kb[1]), *o=GP(u16,kb[2]); char* ws=(char*)o; unsigned cpar=xcd>>2; \
  auto loc=[&](unsigned off)->unsigned{ return off|(((cpar^(off>>21))&1)<<13); }; \
  (void)x; (void)wqkv; (void)kin; (void)vin; (void)shk; (void)hk; (void)wo; (void)qin; (void)sD; (void)out; (void)loc
  ST0()
  __shared__ __attribute__((aligned(1024))) char smem[ALL_LDS];
  const int tid=threadIdx.x;
  const int w8=__builtin_amdgcn_readfirstlane(tid>>6);
  asm volatile("s_sub_u32 vcc_lo, %0, 4\n\ts_cbranch_scc1 1f\n\ts_cmp_lg_u32 %0, 7\n\ts_cbranch_scc1 2f\n\t"
      "s_lshr_b32 vcc_lo, %2, 3\n\ts_mul_i32 vcc_lo, vcc_lo, %5\n\tv_and_b32 v254, 15, %1\n\tv_lshl_add_u32 v254, v254, 7, vcc_lo\n\ts_getpc_b64 vcc\n\tglobal_load_dword v255, v254, vcc\n\ts_branch 1f\n"
      "2:\n\ts_and_b32 vcc_hi, %2, 7\n\ts_cbranch_scc1 1f\n\ts_cmp_gt_u32 %2, 32\n\ts_cbranch_scc1 1f\n\ts_mul_i32 vcc_hi, %2, 3\n\ts_lshr_b32 vcc_hi, vcc_hi, 3\n\ts_add_u32 vcc_lo, vcc_lo, vcc_hi\n\t"
      "v_and_b32 v253, 3, %1\n\tv_and_b32 v254, 4, %1\n\tv_lshlrev_b32 v253, 7, v253\n\tv_lshl_add_u32 v253, v254, 11, v253\n\tv_lshl_add_u32 v253, vcc_lo, 9, v253\n\tv_add_u32 v253, %4, v253\n\t"
      "s_load_dwordx2 vcc, %3, 80\n\ts_mov_b64 exec, 255\n\ts_waitcnt lgkmcnt(0)\n\tglobal_store_dword v253, v253, vcc sc1\n\ts_mov_b64 exec, -1\n1:"
      :: "s"(w8), "v"(tid), "s"(blockIdx.x), "s"(__builtin_amdgcn_kernarg_segment_ptr()), "n"(WS_GEN+16384), "n"(CODE_ST) : "memory", "scc", "vcc", "v253", "v254", "v255");
  LDS_AS unsigned* const ct=(LDS_AS unsigned*)(LDS_AS char*)(smem+CT_OFF);
  volatile LDS_AS unsigned* const vct=ct;
  auto all4=[&](int i)->unsigned{ u32x4 v=*(volatile LDS_AS u32x4*)(ct+i); return __builtin_amdgcn_readfirstlane(v[0]&v[1]&v[2]&v[3]); };
#define ROTF(b) ((4+4*xr+4*xc+8*(b))&15)
#define BLOCK() int bid=blockIdx.x, w=w8&3; asm volatile("" : "+s"(bid), "+s"(w)); __builtin_assume(bid>=0 && bid<256 && w>=0 && w<4); DERIVE()
#define DERIVE() const int xcd=bid&7, idx=bid>>3, ra=idx&3, rbb=idx>>2, xr=xcd>>1, xc=xcd&1, rb=xr*4+ra, hp=xc*8+rbb, s0=rb*128, tm=rb, tn=hp^TNX, cb=(xcd>>2)*128+idx*4+(xcd&3), gi=cb^16; (void)s0; (void)gi; \
  auto korder=[&]()->unsigned long long{ const unsigned N=0x7654210u, sh=4*(KR), q=(((N>>sh)|(N<<(28-sh)))&0xfffffffu)|0x30000000u; \
    return ((unsigned long long)q<<32|q)^(xc? 0x88888888ull : 0x8888888800000000ull); }; \
  const unsigned l1m=0xf7u<<(8*xc); (void)l1m; (void)korder

  if(__builtin_expect(w8>=4,0)){
  BLOCK(); constexpr int WB=4; (void)WB;
  const int hd=w>>1, par=w&1;
  __builtin_amdgcn_s_setprio(CPRIO);
  {
    ARGS0(); LANES();
    if(w==0){ if(l<52) ct[l]=0u; if(idx==0) *(u32x4*)(smem+AUX+l*16)=u32x4{0,0,0,0}; }
    unsigned a0b=fr*128+((g^(fr&7))*16);
    const int r0=8*g+tq;
    unsigned bbb=r0*128+(((2*hd)^wswz(r0))*32)+8*tp;
    unsigned kA=AUX+(fr^3)*KVP_+w*64+(g&1)*16-(g>>1)*256, kB=AUX+(20+4*g-tq)*KVP_+w*64+8*tp;
    const int cch=127-4*idx-g;
    unsigned kvo=(8*w+tq)*32768+(cch>>6)*16384+((cch>>4)&3)*2048+(cch&15)*64+((tp^((cch>>2)&3))*16);
    unsigned sho=(xcd*128+32*w+fr)*8; const unsigned ctl=CT_OFF+4*w;
    u32x4 sel;
    { const unsigned b2=(g>>1)? 0x0302u : 0x0100u, sv=(fr&1)? (b2<<16|0x0c0cu) : (0x0c0c0000u|b2);
      #pragma unroll
      for(int i=0;i<4;i++) sel[i]= (fr>>1)==(g&1)*4+i ? sv : 0x0c0c0c0cu; }
    unsigned msk=0, lx=w*1024+l*16;
    #pragma unroll
    for(int e=0;e<4;e++){ const int d=4*g+e-fr; const unsigned pv=d<9, hv=par? pv : (unsigned)(d<1 || 4*g+e>7);
      msk|=((unsigned)(d+7)>7u?1u:0u)<<e | pv<<(4+e) | hv<<(8+e) | (d<=0?1u:0u)<<(12+e); }
    msk|=(unsigned)(g? l-16 : 48+((l-1)&15))<<26;
    const unsigned hb=__builtin_amdgcn_readfirstlane(rb==0 ? 0xcfa8965fu : 0u);
    unsigned ap=AUX+w*CV_W+(tq&1)*(CV_CP-4)+8+32*g+4*tq+8*tp;
    u32x4 bp;
    #pragma unroll
    for(int b=0;b<4;b++){ const int c=15+4*b-fr+g; bp[b]=c*64+((w^((c>>2)&3))*16); }
    asm volatile("" : "+v"(a0b), "+v"(bbb), "+v"(kA), "+v"(kB), "+v"(kvo), "+v"(sho), "+v"(msk), "+v"(lx), "+v"(ap), "+v"(bp), "+v"(sel));
    ARGS();
    const char* ow=ws+loc((rb*16+hp)*32768)+(par*2+hd)*1024+4096;
    asm volatile("" : "+s"(ws));
    asm volatile("" : "+s"(shk));
    asm volatile(C_ASM : "+{v52}"(a0b), "+{v54}"(bbb), "+{v0}"(kA), "+{v1}"(kB), "+{v2}"(kvo), "+{v51}"(sho), "+{v3}"(msk), "+{v4}"(lx), "+{v5}"(ap), "+{v[6:9]}"(bp), "+{v[14:17]}"(sel)
       : "s"(shk), "s"(ctl), "s"(w), "s"(ow), "s"(hb), "s"(0.17677669529663687f*1.44269504088896341f), "{s[92:93]}"(ws+loc(tm*16*32768)), "s"(korder()),
         "s"(ws+loc(flo(1,rb*16))+hp*4), "s"(AUX+w*(CV_W+64)), "s"(ws+loc(flo(1,tm*16+tn))),
         "s"(out+(size_t)(tm*128+w*32+1)*2048+1024+tn*64), "s"(8192+w*4096), "s"(ws+loc(WS_KV+xcd*32*32768)), "s"(ws+loc(flo(0,xcd*32))+idx*4), "s"(l1m), "s"(xcd==0 && idx<5 && w<3? (u64)(ws+WS_GEN+idx*3+w) : 0ull) : "memory", "scc", "vcc", "s68", "s69", "s70", "s71", "s74", "s75", "s76", "s77", "s88", "s89", "s90", "s91",
       C_CLOB);
    TSDECL();
    STW(62,(unsigned)R0);
    TS(20); TSDUMP();
  }
  return;
  }
  {
    ARGS0(); BLOCK(); constexpr int WB=0; (void)WB; LANES(); TSDECL();
    const unsigned vx=(l>>3)*4096+(((l&7)^(l>>3))*16), vxs=vx+(s0+32*w)*2048, vw=(l>>3)*6144+((((l&7)>>1)^((l>>4)&1))*32)+(l&1)*16+hp*128+w*98304, vw1=(vw^64)+49152-1024;
    u32x8 va; u32x2 vb; unsigned vsp;
    #pragma unroll
    for(int i=0;i<6;i++) va[i]=((i&1)? vw1 : vw)+(i>>1)*2048;
    va[6]=vxs; va[7]=vxs+32768-1024; vb[0]=vxs+2048; vb[1]=vxs+2048+32768-1024;
    vsp=vx+(rb? s0-16+w : 0)*2048;
    const unsigned sw1=w*1024, sw2=w*2048;
    unsigned rot=ROTF(rbb);
    asm volatile("" : "+v"(va), "+v"(vb), "+v"(vsp));
    asm volatile("" : "+s"(rot));
    unsigned vt=(w&1)? (xr*512+idx*16+(l>>1))*2048+(l&1)*1024 : (2*idx+(l&1))*6144+((l>>1)&3)*2048+xc*1024;
    const int kt0=idx*64; int kvs=KS0+KVSEL;
    unsigned kvl=(kt0+16*w+g)*2048+xcd*256+fr*16, l16=l*16;
    const bool hl= w==0 && kt0;
    unsigned c46=sg(w<2? 128u : w==2? 393216u : 0u), c49=sg(w<2? sw1 : (unsigned)ALL_LDS-96), c64=sg(hl? 8192u : 0u), c68=sg(hl? AUX : ALL_LDS-96);
    unsigned long long c50= w<2? ~0ull : w==2? 63ull : 1ull, c66= hl? ~0ull : 1ull;
    asm volatile("" : "+v"(vt), "+v"(kvl), "+v"(l16));
    asm volatile("" : "+s"(c46), "+s"(c49), "+s"(c64), "+s"(c68), "+s"(c50), "+s"(c66), "+s"(kvs));
    ARGS();
    STW(62,(unsigned)R0);
    TSB(16);
    asm volatile(H0_ASM : "+{s[40:41]}"(x), "+{s[42:43]}"(wqkv) : "{v[0:7]}"(va), "{v[8:9]}"(vb), "{v10}"(vsp), "{s52}"(rot), "{s53}"(sw2), "{s59}"(w)
      : "memory", "scc", "s76", "s77", "s78", "s79", "s82", "s83");
    const u16* spb= w==2? wqkv : x;
    const int hl3=(3-(int)((size_t)spb>>7))&7, roth=ROTF(hl3), xs=((hl3-ROTF(0))&7)-LEAD;
    unsigned i11=sg((w==2? roth+LEAD+1 : rot+1)&15), sbo, sst;
    if(w>=2){
      vsp=vt+hl3*128;
      touch(spb, vsp+((roth+(l>>3))&15)*c46, w==2? 0x3f3f3f3f3f3f3f3full>>(56-8*LEAD) : xs<=1? 0xffffffffull : 1ull);
    }
    TSB(17);
    unsigned gof=(((gi&15)*128+64*(w&1)+4*(w>>1)+16*((l>>5)&1)+((l>>3)&3))*DM+(gi>>4)*64)*2+(l16&127);
    unsigned wv[2];
    wv[0]=(((l>>5)*16+w*4+((l>>3)&3))*DM+tn*64)*2+((l16&127)^((l&16)*2)^((w&1)*64)); wv[1]=wv[0]+32*DM*2;
    unsigned zpa=(w>>1)*9216+(w&1)*512+l16, hda=AUX+w*CV_W+(l&1)*CV_CP+(l>>1)*16, yra=AUX+tq*(CV_W+64)+(g>>1)*128+(g&1)*32+tp*8+w*1024;
    const unsigned f0=loc(flo(0,cb)), f1=loc(flo(1,tm*16+tn)), f2=loc(flo(2,gi));
    unsigned vc=(w&1)? (g==0? WS_GEN : (g==1? f0 : g==2? f1 : f2)^((l&1)<<13)) : idx*CODE_ST+fr*128u;
    unsigned kfo=f0+(l&31)*4, ofo= w==0? f1+fr*4 : WS_GEN;
    asm volatile("" : "+v"(kfo), "+v"(ofo), "+v"(gof), "+v"(wv[0]), "+v"(wv[1]), "+v"(hda), "+v"(yra), "+v"(zpa), "+v"(vc));
    unsigned long long kt=korder();
    asm volatile("" : "+s"(kt));
#define HDBG_O
#define HDBG_I
#define H_IN "{v[0:7]}"(va), "{v[8:9]}"(vb), "{v10}"(vsp), "{v11}"(kvl), "{s[40:41]}"(x), "{s[42:43]}"(wqkv), "{s[44:45]}"(spb), "{s46}"(c46), "{s49}"(c49), "{s[50:51]}"(c50), "{s53}"(sw2), \
      "{s58}"(kvs), "{s59}"(w), "{s[60:61]}"(kin), "{s[62:63]}"(vin), "{s64}"(c64), "{s[66:67]}"(c66), "{s68}"(c68) HDBG_I
#define H_CLOB "memory", "scc", "vcc", "s76", "s77", "s78", "s79", "s80", "s81", "s82", "s83", "s84", "s85", "v20", "v21", "v22", "v253", "v254", "v255"
    asm volatile(H1_ASM : "+{s47}"(i11), "+{s52}"(rot), "={s54}"(sbo), "={s55}"(sst) HDBG_O : H_IN : H_CLOB);
    const u16* hrow=hk+(size_t)(cb*4+w)*S;
    u32x4 h0; unsigned dvu;
    asm volatile("s_load_dwordx4 %0, %2, 0\n\ts_load_dword %1, %3, %4" : "=&s"(h0), "=&s"(dvu) : "s"(hrow), "s"(sD), "s"(cb*8+(w&2)*2) : "memory");
    unsigned long long pc; asm volatile("s_getpc_b64 %0" : "=s"(pc)); pc&=~4095ull;
    asm volatile("" : "+s"(wo), "+s"(qin), "+s"(out), "+s"(ws));
    __builtin_amdgcn_s_setprio(HP1);
    unsigned kf2=kfo;
    asm volatile(H_ASM : "+{v16}"(kf2), "+{s47}"(i11), "+{s52}"(rot), "+{s54}"(sbo), "+{s55}"(sst) HDBG_O
      : H_IN, "{v14}"(vc), "{v13}"(WS_GEN+256+(l&31)*4), "{s56}"(sg(3u<<(kvs-1) | 1u<<15)),
        "{s57}"(sg(((w&1)? (1u<<TGS | 3u<<PLS | 1u<<14 | (xs>1? 8u : 0u)) : 1u<<CPS)&~(unsigned)(c50>>32))), "{s[70:71]}"(ws), "{s[72:73]}"(pc) : H_CLOB);
    asm volatile("s_waitcnt lgkmcnt(0)" : "+s"(h0), "+s"(dvu) :: "memory");
    asm volatile("" : "+s"(bid), "+s"(w)); __builtin_assume(bid>=0 && bid<256 && w>=0 && w<4);
    { DERIVE(); const unsigned sw1=w*1024, sw2=w*2048;
    u32x4 qv[4];
    BAR(); TSB(14);
    __builtin_amdgcn_s_setprio(HP2);
    { const unsigned fa=AUX+w*CV_W+32;
      asm volatile("s_mov_b64 exec, -2\n\ts_add_u32 m0, %3, %4\n\ts_nop 0\n\tglobal_load_lds_dwordx4 %0, %1" LDPOL "\n\ts_mov_b32 m0, %3\n\ts_nop 0\n\tglobal_load_lds_dwordx4 %0, %2" LDPOL "\n\ts_mov_b64 exec, -1"
        :: "v"(l16), "s"(hrow), "s"(hrow-2), "s"(fa), "n"(CV_CP) : "memory", "scc");
      #pragma unroll
      for(int i=1;i<4;i++){ gldsn(hrow+i*512, l16, fa, CV_CP+i*1024); gldsn(hrow-2+i*512, l16, fa, i*1024); } }
    WAITV(8);
    vct[32+w]=1u;
    TSH(6);
    const unsigned tag=__builtin_amdgcn_readfirstlane(vct[26]); const bool kvok=__builtin_amdgcn_readfirstlane(vct[5])!=0;
    { const unsigned pl=__builtin_amdgcn_readfirstlane(vct[6]); cpar=(xcd>>2)^(pl>>13); kfo^=pl; if(w==0) ofo^=pl; }
    u32x4 hv;
    { const unsigned d0=(h0[0]&0xffff0000u)|(pack2(bflo(h0[0])+((w&1)? bfhi(dvu) : bflo(dvu)),0.f)&0xffffu);
      hv= l==4? u32x4{0,d0,h0[1],h0[2]} : l==5? u32x4{d0,h0[1],h0[2],h0[3]} : u32x4{0,0,0,0}; }
    asm volatile("s_mov_b64 exec, 63\n\tds_write_b128 %0, %1\n\ts_mov_b64 exec, %3\n\tds_write_b128 %2, %1 offset:7680\n\ts_mov_b64 exec, -1"
      :: "v"(hda), "v"(hv), "v"(zpa), "s"(0xffffffff00000000ull) : "memory");
#define QLD() asm volatile("global_load_dwordx4 %0, %4, %5" LDPOL "\n\tglobal_load_dwordx4 %1, %4, %6" LDPOL "\n\tglobal_load_dwordx4 %2, %4, %7" LDPOL "\n\tglobal_load_dwordx4 %3, %4, %8" LDPOL \
        : "=&v"(qv[0]), "=&v"(qv[1]), "=&v"(qv[2]), "=&v"(qv[3]) : "v"(gof), "s"(qin), "s"(qin+8*DM), "s"(qin+32*DM), "s"(qin+40*DM) : "memory")
    auto kvt=[&](){
      #pragma unroll
      for(int i=0;i<4;i++) gldsc(ws+loc(WS_KV+cb*32768)+(w>>1)*16384+(w&1)*4096+i*1024, l16, sw2, (i&1)*9216+(i>>1)*1024); };
    if(!kvok) wait_flag((const unsigned*)(ws+kfo), tag);
    kvt();
    TSH(7);
    auto wos=[&](int st){ const u16* b=wo+(size_t)((kt>>(4*st))&15)*64*DM; glds(b,wv[0],sw1,WOB+st*8192); glds(b,wv[1],sw1,WOB+st*8192+4096); };
    wos(0); wos(1);
    while(!all4(36)) __builtin_amdgcn_s_sleep(1);
    #pragma unroll
    for(int st=2;st<NE;st++) wos(st);
    WAITV(2*NE); WAITL();
    vct[16+w]=1u;
    TSH(8);
    __builtin_amdgcn_s_setprio(HP4);
    if(GATE==1) while(!all4(28)) __builtin_amdgcn_s_sleep(1);
    if(GATE==2) while(vct[4]<4u) __builtin_amdgcn_s_sleep(1);
    TSF(24);
    QLD();
    #pragma unroll
    for(int st=NE;st<13;st++){ if(GATE==0 && st==7) while(!all4(0)) __builtin_amdgcn_s_sleep(1);
      WAITV(W1); TSF(24+st);
      if(st==EP+W1/2+1 || st==EP2+W1/2+1)
        asm volatile("s_cmp_lg_u32 %0, 0\n\ts_cbranch_scc1 1f\n\tv_cmp_ne_u32 vcc, %1, v253\n\tv_mov_b32 v253, %3\n\ts_nop 1\n\ts_and_b32 vcc_lo, vcc_lo, %2\n\ts_cbranch_scc1 1f\n\tv_mov_b32 v254, 1\n\tds_max_u32 v253, v254\n1:"
          :: "s"(w), "s"(tag), "s"(l1m), "n"(CT_OFF+96) : "memory", "scc", "vcc", "v253", "v254");
      if(st==EP || st==EP2) asm volatile("global_load_dword v253, %0, %1 sc1" :: "v"(ofo), "s"(ws) : "memory", "v253");
      wos(st); if(st==7+W1/2) vct[20+w]=7u; }
    unsigned op;
    asm volatile("global_load_dword %0, %1, %2 sc1" : "=v"(op) : "v"(ofo), "s"(ws) : "memory");
    WAITV(1); vct[20+w]=13u;
    TSH(9);
    asm volatile("s_waitcnt vmcnt(0)" : "+v"(qv[0]), "+v"(qv[1]), "+v"(qv[2]), "+v"(qv[3]), "+v"(op) :: "memory");
    unsigned oseen=0; bool ptch=false;
    auto olevel=[&](unsigned v){
      const unsigned ne=(unsigned)__ballot(v!=tag), lv= ne==0 ? 2u : (ne&l1m)==0 ? 1u : 0u;
      if(lv>oseen){ if(oseen==0){ TSH(10); } oseen=lv; if(l==0) __hip_atomic_fetch_max(ct+24, lv, __ATOMIC_RELAXED, __HIP_MEMORY_SCOPE_WORKGROUP); }
      if(PTCH && !ptch && !((ne>>(hp^8))&1)){ ptch=true; const char* b=ws+loc((tm*16+(hp^8))*32768);
        asm volatile("global_load_dword v255, %0, %1 sc1\n\tglobal_load_dword v255, %0, %2 sc1" :: "v"(l16*8), "s"(b), "s"(b+16384) : "memory", "v255"); } };
    auto opoll=[&](){ olevel(__hip_atomic_load((const unsigned*)(ws+ofo), __ATOMIC_RELAXED, __HIP_MEMORY_SCOPE_AGENT)); };
    if(w==0) olevel(op);
    float qf[32];
    auto qun=[&](){
    #pragma unroll
    for(int i=0;i<16;i++){ qf[2*i]=bflo(qv[i>>2][i&3]); qf[2*i+1]=bfhi(qv[i>>2][i&3]); }
    #pragma unroll
    for(int i=0;i<32;i+=8) asm volatile("" : "+v"(qf[i]), "+v"(qf[i+1]), "+v"(qf[i+2]), "+v"(qf[i+3]), "+v"(qf[i+4]), "+v"(qf[i+5]), "+v"(qf[i+6]), "+v"(qf[i+7]));
    };
    qun();
    auto wol=[&](int i){ glds(wo+(size_t)((kt>>(52+4*(i>>1)))&15)*64*DM, wv[i&1], sw1, i<4? AUX+4352+i*CV_W : (i&1)*4096); };
    while(!all4(44)){ if(w==0 && oseen<1) opoll(); else __builtin_amdgcn_s_sleep(1); }
    wol(0); wol(1); wol(2); wol(3);
    while(!all4(48)){ if(w==0 && oseen<1) opoll(); else __builtin_amdgcn_s_sleep(1); }
    wol(4); wol(5);
    __builtin_amdgcn_s_setprio(HP3);
    bool gen= !(bid==0 && w==1);
    auto trygen=[&](){
      unsigned v[4];
      #pragma unroll
      for(int i=0;i<4;i++) v[i]=__hip_atomic_load((const unsigned*)(ws+(loc(flo(0,i*64))^((i>>1)<<13))+(l>>5)*4096+(l&31)*4), __ATOMIC_RELAXED, __HIP_MEMORY_SCOPE_AGENT);
      if(__all(v[0]==tag && v[1]==tag && v[2]==tag && v[3]==tag)){ if(l==0) st4c(ws+WS_GEN, tag); gen=true; } };
    if(!gen) trygen();
    while(!((all4(8)>>w)&1)){
      if(w==0 && oseen<2) opoll();
      __builtin_amdgcn_s_sleep(OPOLLS);
    }
    TSH(11);
    __builtin_amdgcn_s_setprio(HP5);
    {
      const char* yb=smem+yra;
      s16x4 ya[4], yc[4];
      #pragma unroll
      for(int i=0;i<4;i++){ ya[i]=trread(yb+(i>>1)*512+(i&1)*64); yc[i]=trread(yb+(i>>1)*512+(i&1)*64+256); }
      #pragma unroll
      for(int i=0;i<4;i++) if(NOY!=1) st16c(ws+loc(WS_Y+(cb&~15)*32768)+(w*4+i)*32768+((cb>>3)&1)*16384+(cb&7)*1024, l16, __builtin_bit_cast(u32x4,__builtin_shufflevector(ya[i],yc[i],0,1,2,3,4,5,6,7)));
    }
    TSH(12);
    if(NOY){ WAITV(0); vct[20+w]=16u; for(int sp_=0; !gen && sp_<(1<<22); sp_++) trygen(); TS(20); TSDUMP(); return; }
    unsigned po=gof+(gof&~2047u);
    const char* p0=ws+loc(WS_Y+gi*32768);
    unsigned y0=g*1024+w*256+((fr^(g&2))*16);
    asm volatile("" : "+v"(y0), "+v"(po));
    WAITV(0);
    vct[20+w]=16u;
    if(l<4) st4c(ws+loc(flo(2,cb&~15))+(w*4+l)*128+(cb&15)*4, tag);
    TSH(13);
    if(w==0){ int sp_=0; while(oseen<2 && vct[24]<2u && ++sp_<(1<<22)){ opoll(); __builtin_amdgcn_s_sleep(8); } }
    if(w==0){
      const unsigned* fp=(const unsigned*)(ws+loc(flo(2,gi)))+(l&15); int spins=0;
      while(vct[27]==0u) __builtin_amdgcn_s_sleep(2);
      while(true){
        unsigned v=__hip_atomic_load(fp, __ATOMIC_RELAXED, __HIP_MEMORY_SCOPE_AGENT);
        if(__all(v==tag) || ++spins>(1<<20)) break;
        __builtin_amdgcn_s_sleep(POLLS);
      }
      { u32x4 v; do{ v=*(volatile LDS_AS u32x4*)(ct+20); }while(__builtin_amdgcn_readfirstlane(v[0]&v[1]&v[2]&v[3]&16u)==0); }
      vct[25]=1u;
    } else { while(vct[25]==0u) __builtin_amdgcn_s_sleep(2); }
    TSH(14);
    {
      u32x4 yv[4];
      #pragma unroll
      for(int i=0;i<4;i++) gldsc(p0+(i>>1)*16384+(i&1)*4096, y0^(i*64), w*CV_W, AUX+i*1024);
      const char* ys=smem+AUX+w*CV_W+(l&7)*512;
      const int yi=((l>>3)^((l&7)*2))*16;
      WAITV(0);
      yv[0]=*(const u32x4*)(ys+yi); yv[1]=*(const u32x4*)(ys+yi+256); yv[2]=*(const u32x4*)(ys+(yi^128)); yv[3]=*(const u32x4*)(ys+(yi^128)+256);
      TSH(15);
      #pragma unroll
      for(int r=0;r<4;r++){
        u32x4 ov;
        #pragma unroll
        for(int e=0;e<4;e++){ const unsigned y2=yv[(r>>1)*2+(e>>1)][(r&1)*2+(e&1)]; ov[e]=pack2(qf[r*8+2*e]*bflo(y2), qf[r*8+2*e+1]*bfhi(y2)); }
        stos(out+((r&1)*8+(r>>1)*32)*2048, po, ov);
      }
    }
    for(int sp_=0; !gen && sp_<(1<<22); sp_++) trygen();
    TS(20); TSDUMP();
    }
  }

}

extern "C" void kernel_launch(void* const* d_in, const int* in_sizes, int n_in, void* d_out, int out_size, void* d_ws, size_t ws_size, hipStream_t stream){
  const u16* q=(const u16*)d_in[0]; const u16* k=(const u16*)d_in[1]; const u16* v=(const u16*)d_in[2];
  const u16* shk=(const u16*)d_in[3]; const u16* hk=(const u16*)d_in[4]; const u16* sD=(const u16*)d_in[5];
  const u16* x=(const u16*)d_in[6]; const u16* wqkv=(const u16*)d_in[7]; const u16* wo=(const u16*)d_in[8];
  u16* ws=(u16*)(((size_t)d_ws+16383)&~(size_t)16383);
  fused_kernel<<<256,512,0,stream>>>(x,wqkv,k,v,shk,hk,wo,q,sD,(u16*)d_out,ws,ws);
}
```

```cpp
#include <hip/hip_runtime.h>
#include <hip/hip_bf16.h>
#include <stdint.h>
#define DBG_OFF (28u<<20)
#define SHOWC

typedef unsigned short u16;
typedef short s16x4 __attribute__((ext_vector_type(4)));
typedef float f32x32 __attribute__((ext_vector_type(32)));
typedef unsigned u32x4 __attribute__((ext_vector_type(4)));
typedef unsigned u32x2 __attribute__((ext_vector_type(2)));
#define LDS_AS __attribute__((address_space(3)))

__device__ __forceinline__ float bflo(unsigned x){ return __uint_as_float(x<<16); }
__device__ __forceinline__ float bfhi(unsigned x){ return __uint_as_float(x&0xffff0000u); }
typedef float f32x2 __attribute__((ext_vector_type(2)));
typedef __bf16 bf16x2_t __attribute__((ext_vector_type(2)));
__device__ __forceinline__ unsigned pack2(float lo, float hi){ return __builtin_bit_cast(unsigned, __builtin_convertvector(f32x2{lo,hi}, bf16x2_t)); }

#define GLDS(name,pol) __device__ __forceinline__ void name(const void* sb, unsigned voff, unsigned ldsb, int imm){ \
  asm volatile("s_add_u32 m0, %2, %3\n\ts_nop 0\n\tglobal_load_lds_dwordx4 %0, %1" pol :: "v"(voff), "s"(sb), "s"(ldsb), "n"(imm) : "memory", "scc"); }
#define LDPOL " nt"
GLDS(glds,"") GLDS(gldsn,LDPOL) GLDS(gldsc," sc1")
#define PACE 63
__device__ __forceinline__ void gldsp(const void* sb, unsigned voff, unsigned ldsb, int imm){
  asm volatile("s_add_u32 m0, %2, %3\n\ts_waitcnt vmcnt(%4)\n\tglobal_load_lds_dwordx4 %0, %1" :: "v"(voff), "s"(sb), "s"(ldsb), "n"(imm), "n"(PACE) : "memory", "scc"); }
__device__ __forceinline__ void touch(const void* sb, unsigned voff, unsigned long long mask){
  asm volatile("s_mov_b64 exec, %2\n\tglobal_load_dword v255, %0, %1\n\ts_mov_b64 exec, -1" :: "v"(voff), "s"(sb), "s"(mask) : "memory", "v255"); }
__device__ __forceinline__ void st16c(void* sb, unsigned vo, u32x4 v){ asm volatile("global_store_dwordx4 %0, %1, %2 sc1\n\ts_nop 1" :: "v"(vo), "v"(v), "s"(sb) : "memory"); }
#define OUTPOL " sc1 nt"
__device__ __forceinline__ void stos(void* sb, unsigned vo, u32x4 v){ asm volatile("global_store_dwordx4 %0, %1, %2" OUTPOL "\n\ts_nop 1" :: "v"(vo), "v"(v), "s"(sb) : "memory"); }
__device__ __forceinline__ void stoa(void* p, u32x4 v){ asm volatile("global_store_dwordx4 %0, %1, off" OUTPOL "\n\ts_nop 1" :: "v"(p), "v"(v) : "memory"); }
__device__ __forceinline__ void st4c(void* p, unsigned v){ asm volatile("global_store_dword %0, %1, off sc1" :: "v"(p), "v"(v) : "memory"); }
__device__ __forceinline__ void wait_flag(const unsigned* p, unsigned tag){
  int spins=0;
  while(true){
    unsigned v=__hip_atomic_load(p, __ATOMIC_RELAXED, __HIP_MEMORY_SCOPE_AGENT);
    if(__all(v==tag)) break;
    __builtin_amdgcn_s_sleep(2);
    if(++spins>(1<<22)) break;
  }
}
#define WAITV(n) asm volatile("s_waitcnt vmcnt(%0)"::"n"(n):"memory")
#define WAITL() asm volatile("s_waitcnt lgkmcnt(0)":::"memory")
#define BAR() do{ asm volatile("s_waitcnt lgkmcnt(0)":::"memory"); __builtin_amdgcn_s_barrier(); asm volatile("":::"memory"); }while(0)
#define LANES() int l; asm volatile("v_mbcnt_lo_u32_b32 %0, -1, 0\n\tv_mbcnt_hi_u32_b32 %0, -1, %0" : "=v"(l)); __builtin_assume(l>=0 && l<64); const int g=l>>4, fr=l&15, tq=(l&15)>>2, tp=l&3; (void)g; (void)fr; (void)tq; (void)tp;

__device__ __forceinline__ s16x4 trread(const char* p){ return __builtin_amdgcn_ds_read_tr16_b64_v4i16((LDS_AS s16x4*)p); }
__device__ __forceinline__ int wswz(int row){ return ((row>>1)&1) | (((row>>3)&1)<<1); }
__device__ __forceinline__ u32x4 widen(u32x2 a, u32x2 b){
  auto r0=__builtin_amdgcn_permlane16_swap(a[0],b[0],false,false);
  auto r1=__builtin_amdgcn_permlane16_swap(a[1],b[1],false,false);
  return u32x4{r0[0],r1[0],r0[1],r1[1]};
}

constexpr int S=2048, DM=1024;
constexpr int K1_XB=144*128, K1_WB=3*64*128, K1_ST=K1_XB+K1_WB;
constexpr int AUX=3*K1_ST, KVP_=1040;
constexpr int WOB=22528, CV_W=8448, CV_CP=4224;
constexpr int CT_OFF=AUX+33*KVP_;
constexpr int ALL_LDS=CT_OFF+208+96;
constexpr unsigned WS_KV=8u<<20, WS_Y=16u<<20, WS_FL=24u<<20, WS_GEN=WS_FL+(1u<<20);
__device__ __forceinline__ unsigned flo(int kind, int c){ return WS_FL+kind*65536+(c>>6)*16384+(c&63)*128; }

#define CPRIO 2
#define CODE_ST 1610
#define LEAD 3
#define HP3 3
#define HP2 3
#define HP4 3
#define QP 1
#define TNX 1
#define HWS 99
#define HP1 0
#define HUNR 15
#define NE 2
#define WOC 0
#define NS 4
#define PTCH 1
#define GATE 0
#define WRD 2
#define EP 99
#define EP2 99
#define HP5 3
#define W0 6
#define OPD 8
#define WT0 99
#define W1 2
#define W2 2
#define W3 4
#define OPOLLS 1
#define POLLS 3
#define HPRE 0
#define CPS2 99
#define KR ((rbb+2*ra)%7)
#define NOY 0
#define YLDS 1
#define OLDS 0
#define KVSEL (rbb==3? 0 : idx%KSN)
#define KS0 1
#define KSN 3
#define KST 10
#define KVP 5
#define TGS 2
#define CPS 13
#define PLS 4
#define H0_ASM \
  "s_mul_i32 s76, s52, 0x60000\n\t" \
  "s_add_u32 s76, s42, s76\n\t" \
  "s_addc_u32 s77, s43, 0\n\t" \
  "s_add_u32 s82, s53, 86016\n\t" \
  "s_add_u32 m0, s82, 18432\n\t" \
  "s_lshl_b32 s78, s52, 7\n\t" \
  "global_load_lds_dwordx4 v0, s[76:77]\n\t" \
  "global_load_lds_dwordx4 v1, s[76:77] offset:1024\n\t" \
  "s_add_u32 s78, s40, s78\n\t" \
  "s_addc_u32 s79, s41, 0\n\t" \
  "s_add_u32 m0, s82, 2048\n\t" \
  "s_nop 0\n\t" \
  "global_load_lds_dwordx4 v6, s[78:79]\n\t" \
  "global_load_lds_dwordx4 v7, s[78:79] offset:1024\n\t" \
  "s_add_u32 m0, s82, 26624\n\t" \
  "s_nop 0\n\t" \
  "global_load_lds_dwordx4 v2, s[76:77]\n\t" \
  "global_load_lds_dwordx4 v3, s[76:77] offset:1024\n\t" \
  "s_add_u32 m0, s82, 10240\n\t" \
  "s_nop 0\n\t" \
  "global_load_lds_dwordx4 v8, s[78:79]\n\t" \
  "global_load_lds_dwordx4 v9, s[78:79] offset:1024\n\t" \
  "s_add_u32 m0, s82, 34816\n\t" \
  "s_nop 0\n\t" \
  "global_load_lds_dwordx4 v4, s[76:77]\n\t" \
  "global_load_lds_dwordx4 v5, s[76:77] offset:1024\n\t" \
  "s_cmp_gt_u32 s59, 1\n\t" \
  "s_cbranch_scc1 1f\n\t" \
  "s_lshr_b32 s83, s53, 1\n\t" \
  "s_add_u32 m0, s83, 86016\n\t" \
  "s_nop 0\n\t" \
  "global_load_lds_dwordx4 v10, s[78:79]\n\t" \
  "1:\n\t"
#define H1_ASM \
  "s_mov_b32 s55, 0\n\t" \
  "s_mov_b32 s54, 0\n\t" \
  "s_add_u32 s52, s52, 1\n\t" \
  "s_and_b32 s52, s52, 15\n\t" \
  "s_cmp_eq_u32 s58, 1\n\t" \
  "s_cbranch_scc1 1f\n\t" \
  "s_mul_i32 s76, s52, 0x60000\n\t" \
  "s_add_u32 s76, s42, s76\n\t" \
  "s_addc_u32 s77, s43, 0\n\t" \
  "s_lshl_b32 s78, s52, 7\n\t" \
  "s_add_u32 s78, s40, s78\n\t" \
  "s_addc_u32 s79, s41, 0\n\t" \
  "s_mul_i32 s80, s47, s46\n\t" \
  "s_add_u32 s80, s44, s80\n\t" \
  "s_addc_u32 s81, s45, 0\n\t" \
  "s_mov_b32 s82, s53\n\t" \
  "s_add_u32 m0, s82, 18432\n\t" \
  "s_add_u32 s52, s52, 1\n\t" \
  "global_load_lds_dwordx4 v0, s[76:77]\n\t" \
  "global_load_lds_dwordx4 v1, s[76:77] offset:1024\n\t" \
  "s_add_u32 m0, s82, 2048\n\t" \
  "s_and_b32 s52, s52, 15\n\t" \
  "global_load_lds_dwordx4 v6, s[78:79]\n\t" \
  "global_load_lds_dwordx4 v7, s[78:79] offset:1024\n\t" \
  "s_add_u32 m0, s82, 26624\n\t" \
  "s_add_u32 s47, s47, 1\n\t" \
  "global_load_lds_dwordx4 v2, s[76:77]\n\t" \
  "global_load_lds_dwordx4 v3, s[76:77] offset:1024\n\t" \
  "s_add_u32 m0, s82, 10240\n\t" \
  "s_and_b32 s47, s47, 15\n\t" \
  "global_load_lds_dwordx4 v8, s[78:79]\n\t" \
  "global_load_lds_dwordx4 v9, s[78:79] offset:1024\n\t" \
  "s_add_u32 m0, s82, 34816\n\t" \
  "s_and_b32 s83, s54, s51\n\t" \
  "global_load_lds_dwordx4 v4, s[76:77]\n\t" \
  "global_load_lds_dwordx4 v5, s[76:77] offset:1024\n\t" \
  "s_add_u32 m0, s83, s49\n\t" \
  "s_mov_b64 exec, s[50:51]\n\t" \
  "global_load_lds_dwordx4 v10, s[80:81]\n\t" \
  "s_mov_b64 exec, -1\n\t" \
  "s_mov_b32 s55, 1\n\t" \
  "s_mov_b32 s54, 43008\n\t" \
  "s_waitcnt vmcnt(11)\n\t" \
  "s_branch 2f\n\t" \
  "1:\n\t" \
  "s_mul_i32 s83, s59, 4160\n\t" \
  "v_add_u32 v20, 7152, v11\n\t" \
  "v_add_u32 v21, 14304, v11\n\t" \
  "v_add_u32 v22, 21456, v11\n\t" \
  "s_add_u32 m0, s83, 130064\n\t" \
  "s_nop 0\n\t" \
  "global_load_lds_dwordx4 v11, s[60:61]" LDPOL "\n\t" \
  "global_load_lds_dwordx4 v20, s[60:61] offset:1040" LDPOL "\n\t" \
  "global_load_lds_dwordx4 v21, s[60:61] offset:2080" LDPOL "\n\t" \
  "global_load_lds_dwordx4 v22, s[60:61] offset:3120" LDPOL "\n\t" \
  "s_add_u32 m0, s83, 146704\n\t" \
  "s_nop 0\n\t" \
  "global_load_lds_dwordx4 v11, s[62:63]" LDPOL "\n\t" \
  "global_load_lds_dwordx4 v20, s[62:63] offset:1040" LDPOL "\n\t" \
  "global_load_lds_dwordx4 v21, s[62:63] offset:2080" LDPOL "\n\t" \
  "global_load_lds_dwordx4 v22, s[62:63] offset:3120" LDPOL "\n\t" \
  "v_subrev_u32 v20, s64, v11\n\t" \
  "s_mov_b32 m0, s68\n\t" \
  "s_mov_b64 exec, s[66:67]\n\t" \
  "global_load_lds_dwordx4 v20, s[60:61]" LDPOL "\n\t" \
  "s_mov_b64 exec, -1\n\t" \
  "s_waitcnt vmcnt(9)\n\t" \
  "2:\n\t" \
  "s_waitcnt lgkmcnt(0)\n\t" \
  "s_barrier\n\t"
#define H_ASM \
  "s_mul_i32 s76, s52, 0x60000\n\t" \
  "s_add_u32 s76, s42, s76\n\t" \
  "s_addc_u32 s77, s43, 0\n\t" \
  "s_lshl_b32 s78, s52, 7\n\t" \
  "s_add_u32 s78, s40, s78\n\t" \
  "s_addc_u32 s79, s41, 0\n\t" \
  "s_mul_i32 s80, s47, s46\n\t" \
  "s_add_u32 s80, s44, s80\n\t" \
  "s_addc_u32 s81, s45, 0\n\t" \
  "s_add_u32 s82, s54, s53\n\t" \
  "s_cmp_eq_u32 s58, 2\n\t" \
  "s_cbranch_scc1 5f\n\t" \
  "s_branch 3f\n\t" \
  "1:\n\t" \
  "s_waitcnt vmcnt(11)\n\t" \
  "2:\n\t" \
  "s_barrier\n\t" \
  "3:\n\t" \
  "s_add_u32 s55, s55, 1\n\t" \
  "s_add_u32 m0, s82, 18432\n\t" \
  "s_add_u32 s52, s52, 1\n\t" \
  "global_load_lds_dwordx4 v0, s[76:77]\n\t" \
  "global_load_lds_dwordx4 v1, s[76:77] offset:1024\n\t" \
  "s_add_u32 m0, s82, 2048\n\t" \
  "s_and_b32 s52, s52, 15\n\t" \
  "global_load_lds_dwordx4 v6, s[78:79]\n\t" \
  "global_load_lds_dwordx4 v7, s[78:79] offset:1024\n\t" \
  "s_add_u32 m0, s82, 26624\n\t" \
  "s_add_u32 s47, s47, 1\n\t" \
  "global_load_lds_dwordx4 v2, s[76:77]\n\t" \
  "global_load_lds_dwordx4 v3, s[76:77] offset:1024\n\t" \
  "s_add_u32 m0, s82, 10240\n\t" \
  "s_and_b32 s47, s47, 15\n\t" \
  "global_load_lds_dwordx4 v8, s[78:79]\n\t" \
  "global_load_lds_dwordx4 v9, s[78:79] offset:1024\n\t" \
  "s_add_u32 m0, s82, 34816\n\t" \
  "s_and_b32 s83, s54, s51\n\t" \
  "global_load_lds_dwordx4 v4, s[76:77]\n\t" \
  "global_load_lds_dwordx4 v5, s[76:77] offset:1024\n\t" \
  "s_bitcmp1_b32 s57, s55\n\t" \
  "s_cbranch_scc1 10f\n\t" \
  "8:\n\t" \
  "s_add_u32 m0, s83, s49\n\t" \
  "s_mov_b64 exec, s[50:51]\n\t" \
  "global_load_lds_dwordx4 v10, s[80:81]\n\t" \
  "s_mov_b64 exec, -1\n\t" \
  "4:\n\t" \
  "s_mul_i32 s76, s52, 0x60000\n\t" \
  "s_add_u32 s76, s42, s76\n\t" \
  "s_addc_u32 s77, s43, 0\n\t" \
  "s_lshl_b32 s78, s52, 7\n\t" \
  "s_add_u32 s78, s40, s78\n\t" \
  "s_addc_u32 s79, s41, 0\n\t" \
  "s_mul_i32 s80, s47, s46\n\t" \
  "s_add_u32 s80, s44, s80\n\t" \
  "s_addc_u32 s81, s45, 0\n\t" \
  "s_add_u32 s54, s54, 43008\n\t" \
  "s_cmp_eq_u32 s54, 129024\n\t" \
  "s_cselect_b32 s54, 0, s54\n\t" \
  "s_add_u32 s82, s54, s53\n\t" \
  "s_bitcmp0_b32 s56, s55\n\t" \
  "s_cbranch_scc1 1b\n\t" \
  "s_cmp_eq_u32 s55, 15\n\t" \
  "s_cbranch_scc1 19f\n\t" \
  "s_cmp_eq_u32 s55, s58\n\t" \
  "s_cbranch_scc1 3b\n\t" \
  "s_waitcnt vmcnt(11)\n\t" \
  "s_barrier\n\t" \
  "5:\n\t" \
  "s_mul_i32 s83, s59, 4160\n\t" \
  "v_add_u32 v20, 7152, v11\n\t" \
  "v_add_u32 v21, 14304, v11\n\t" \
  "v_add_u32 v22, 21456, v11\n\t" \
  "s_add_u32 m0, s83, 130064\n\t" \
  "s_nop 0\n\t" \
  "global_load_lds_dwordx4 v11, s[60:61]" LDPOL "\n\t" \
  "global_load_lds_dwordx4 v20, s[60:61] offset:1040" LDPOL "\n\t" \
  "global_load_lds_dwordx4 v21, s[60:61] offset:2080" LDPOL "\n\t" \
  "global_load_lds_dwordx4 v22, s[60:61] offset:3120" LDPOL "\n\t" \
  "s_add_u32 m0, s83, 146704\n\t" \
  "s_nop 0\n\t" \
  "global_load_lds_dwordx4 v11, s[62:63]" LDPOL "\n\t" \
  "global_load_lds_dwordx4 v20, s[62:63] offset:1040" LDPOL "\n\t" \
  "global_load_lds_dwordx4 v21, s[62:63] offset:2080" LDPOL "\n\t" \
  "global_load_lds_dwordx4 v22, s[62:63] offset:3120" LDPOL "\n\t" \
  "v_subrev_u32 v20, s64, v11\n\t" \
  "s_mov_b32 m0, s68\n\t" \
  "s_mov_b64 exec, s[66:67]\n\t" \
  "global_load_lds_dwordx4 v20, s[60:61]" LDPOL "\n\t" \
  "s_mov_b64 exec, -1\n\t" \
  "s_waitcnt vmcnt(9)\n\t" \
  "s_branch 2b\n\t" \
  "10:\n\t" \
  "s_cmp_eq_u32 s59, 2\n\t" \
  "s_cbranch_scc0 11f\n\t" \
  "s_mov_b64 exec, 0xffff\n\t" \
  "global_load_dword v255, v14, s[72:73]\n\t" \
  "s_mov_b64 exec, -1\n\t" \
  "s_branch 4b\n\t" \
  "11:\n\t" \
  "s_cmp_eq_u32 s55, 2\n\t" \
  "s_cbranch_scc0 12f\n\t" \
  "global_load_dword v254, v14, s[70:71] sc1\n\t" \
  "s_branch 4b\n\t" \
  "12:\n\t" \
  "s_cmp_eq_u32 s55, 4\n\t" \
  "s_cbranch_scc0 15f\n\t" \
  "global_load_dword v253, v13, s[70:71] sc1\n\t" \
  "s_branch 4b\n\t" \
  "15:\n\t" \
  "s_cmp_eq_u32 s55, 5\n\t" \
  "s_cbranch_scc0 13f\n\t" \
  "v_readfirstlane_b32 s84, v254\n\t" \
  "s_mov_b32 m0, 0x400000\n\t" \
  "s_waitcnt vmcnt(10)\n\t" \
  "s_add_u32 s84, s84, 1\n\t" \
  "v_mov_b32 v254, s84\n\t" \
  "16:\n\t" \
  "v_cmp_eq_u32 vcc, v253, v254\n\t" \
  "s_nop 1\n\t" \
  "s_and_b32 s84, vcc_lo, 0x3fffffff\n\t" \
  "s_bcnt1_i32_b32 s85, s84\n\t" \
  "s_cmp_ge_u32 s85, 15\n\t" \
  "s_cbranch_scc1 17f\n\t" \
  "s_sub_u32 m0, m0, 1\n\t" \
  "s_cbranch_scc1 17f\n\t" \
  "global_load_dword v253, v13, s[70:71] sc1\n\t" \
  "s_waitcnt vmcnt(0)\n\t" \
  "s_branch 16b\n\t" \
  "17:\n\t" \
  "s_and_b32 s84, s84, 0xaaaaaaaa\n\t" \
  "s_bcnt1_i32_b32 s84, s84\n\t" \
  "s_and_b32 s84, s84, 8\n\t" \
  "s_lshl_b32 s84, s84, 10\n\t" \
  "v_xor_b32 v16, s84, v16\n\t" \
  "v_mov_b32 v20, s84\n\t" \
  "v_mov_b32 v253, 163344\n\t" \
  "ds_write_b32 v253, v254 offset:104\n\t" \
  "ds_write_b32 v253, v20 offset:24\n\t" \
  "s_waitcnt lgkmcnt(0)\n\t" \
  "s_branch 8b\n\t" \
  "13:\n\t" \
  "s_cmp_eq_u32 s55, 14\n\t" \
  "s_cbranch_scc0 14f\n\t" \
  "global_load_dword v253, v16, s[70:71] sc1\n\t" \
  "s_branch 4b\n\t" \
  "14:\n\t" \
  "s_mov_b32 exec_hi, 0\n\t" \
  "global_load_dword v255, v10, s[44:45]\n\t" \
  "s_mov_b64 exec, -1\n\t" \
  "s_branch 4b\n\t" \
  "19:\n\t" \
  "s_waitcnt vmcnt(11)\n\t" \
  "s_cmp_lg_u32 s59, 3\n\t" \
  "s_cbranch_scc1 18f\n\t" \
  "v_cmp_ne_u32 vcc, v253, v254\n\t" \
  "v_mov_b32 v253, 163364\n\t" \
  "s_nop 1\n\t" \
  "s_cmp_eq_u64 vcc, 0\n\t" \
  "s_cselect_b32 s83, 1, 0\n\t" \
  "v_mov_b32 v254, s83\n\t" \
  "ds_write_b32 v253, v254\n\t" \
  "18:\n\t"
#define C_ASM \
  "s_branch 29f\n\t" \
  "21:\n\t" \
  "s_mov_b64 s[68:69], %27\n\t" \
  "s_and_b32 s74, s68, 15\n\t" \
  "s_andn2_b32 s68, s68, 15\n\t" \
  "s_load_dword vcc_lo, s[68:69], 0 glc\n\t" \
  "s_and_b32 s75, s74, 1\n\t" \
  "s_lshl_b32 s70, s74, 9\n\t" \
  "s_lshl_b32 s71, s75, 13\n\t" \
  "s_or_b32 s70, s70, s71\n\t" \
  "s_waitcnt lgkmcnt(0)\n\t" \
  "s_or_b32 s68, s68, s70\n\t" \
  "s_add_u32 vcc_lo, vcc_lo, 1\n\t" \
  "v_mov_b32 v18, vcc_lo\n\t" \
  "s_memtime s[70:71]\n\t" \
  "s_waitcnt lgkmcnt(0)\n\t" \
  "s_load_dword s76, s[68:69], 16384 glc\n\t" \
  "s_load_dword s77, s[68:69], 16512 glc\n\t" \
  "s_waitcnt lgkmcnt(0)\n\t" \
  "s_xor_b32 s68, s68, 0x2000\n\t" \
  "s_memtime s[90:91]\n\t" \
  "s_waitcnt lgkmcnt(0)\n\t" \
  "s_load_dword s76, s[68:69], 16384 glc\n\t" \
  "s_load_dword s77, s[68:69], 16512 glc\n\t" \
  "s_waitcnt lgkmcnt(0)\n\t" \
  "s_sub_u32 vcc_lo, s90, s70\n\t" \
  "s_memtime s[70:71]\n\t" \
  "s_waitcnt lgkmcnt(0)\n\t" \
  "s_sub_u32 s70, s70, s90\n\t" \
  "s_cmp_gt_u32 vcc_lo, s70\n\t" \
  "s_cselect_b32 s70, 1, 0\n\t" \
  "s_xor_b32 s70, s70, s75\n\t" \
  "s_lshl1_add_u32 s70, s74, s70\n\t" \
  "s_lshl_b32 s70, s70, 2\n\t" \
  "v_mov_b32 v19, s70\n\t" \
  "s_and_b32 s68, s68, 0xffffc000\n\t" \
  "s_mov_b64 exec, 1\n\t" \
  "global_store_dword v19, v18, s[68:69] offset:256 sc1\n\t" \
  "s_branch 26f\n\t" \
  "28:\n\t" \
  "s_cmp_lg_u64 %27, 0\n\t" \
  "s_cbranch_scc1 21b\n\t" \
  "s_mov_b64 exec, 0\n\t" \
  "s_cmp_eq_u32 %13, 0\n\t" \
  "s_cbranch_scc1 500f\n\t" \
  "s_cmp_eq_u32 %13, 1\n\t" \
  "s_cbranch_scc1 501f\n\t" \
  "s_cmp_eq_u32 %13, 2\n\t" \
  "s_cbranch_scc1 502f\n\t" \
  "s_branch 503f\n\t" \
  "26:\n\t" \
  "s_mov_b64 exec, -1\n\t" \
  "s_add_u32 s88, s88, 4\n\t" \
  "s_addc_u32 s89, s89, 0\n\t" \
  "s_setpc_b64 s[88:89]\n\t" \
  ".p2align 12\n\t" \
  "29:\n\t" \
  "s_bitcmp0_b32 %13, 0\n\t" \
  "s_cbranch_scc1 8f\n\t" \
  "global_load_dwordx2 v[10:11], v51, %11\n\t" \
  "global_load_dwordx2 v[12:13], v51, %11 offset:128\n\t" \
  "v_xor_b32 v53, 64, v52\n\t" \
  "v_xor_b32 v55, 32, v54\n\t" \
  "v_add_u32 v56, 43008, v52\n\t" \
  "v_add_u32 v58, 43008, v54\n\t" \
  "v_xor_b32 v57, 64, v56\n\t" \
  "v_xor_b32 v59, 32, v58\n\t" \
  "v_add_u32 v60, 86016, v52\n\t" \
  "v_add_u32 v62, 86016, v54\n\t" \
  "v_xor_b32 v61, 64, v60\n\t" \
  "v_xor_b32 v63, 32, v62\n\t" \
  "s_mov_b32 vcc_hi, 0x05040100\n\t" \
  "s_waitcnt vmcnt(2)\n\t" \
  "s_barrier\n\t" \
  "ds_read_b64_tr_b16 v[64:65], v62 offset:26624\n\t" \
  "ds_read_b64_tr_b16 v[66:67], v62 offset:27136\n\t" \
  "ds_read_b128 v[68:71], v60 offset:10240\n\t" \
  "ds_read_b64_tr_b16 v[72:73], v62 offset:30720\n\t" \
  "ds_read_b64_tr_b16 v[74:75], v62 offset:31232\n\t" \
  "ds_read_b128 v[76:79], v61 offset:10240\n\t" \
  "ds_read_b128 v[80:83], v61 offset:12288\n\t" \
  "ds_read_b128 v[84:87], v60 offset:12288\n\t" \
  "ds_read_b128 v[88:91], v60 offset:14336\n\t" \
  "ds_read_b128 v[92:95], v61 offset:14336\n\t" \
  "ds_read_b128 v[96:99], v61 offset:16384\n\t" \
  "ds_read_b128 v[100:103], v60 offset:16384\n\t" \
  "ds_read_b64_tr_b16 v[104:105], v63 offset:26624\n\t" \
  "ds_read_b64_tr_b16 v[106:107], v63 offset:27136\n\t" \
  "ds_read_b64_tr_b16 v[108:109], v63 offset:30720\n\t" \
  "ds_read_b64_tr_b16 v[110:111], v63 offset:31232\n\t" \
  "s_waitcnt lgkmcnt(13)\n\t" \
  "v_mfma_f32_16x16x32_bf16 v[184:187], v[64:67], v[68:71], 0\n\t" \
  "ds_read_b64_tr_b16 v[112:113], v62 offset:34816\n\t" \
  "ds_read_b64_tr_b16 v[114:115], v62 offset:35328\n\t" \
  "s_waitcnt lgkmcnt(12)\n\t" \
  "v_mfma_f32_16x16x32_bf16 v[184:187], v[72:75], v[76:79], v[184:187]\n\t" \
  "ds_read_b64_tr_b16 v[116:117], v62 offset:38912\n\t" \
  "ds_read_b64_tr_b16 v[118:119], v62 offset:39424\n\t" \
  "s_waitcnt lgkmcnt(13)\n\t" \
  "v_mfma_f32_16x16x32_bf16 v[192:195], v[72:75], v[80:83], 0\n\t" \
  "ds_read_b128 v[120:123], v60 offset:0\n\t" \
  "s_waitcnt lgkmcnt(13)\n\t" \
  "v_mfma_f32_16x16x32_bf16 v[192:195], v[64:67], v[84:87], v[192:195]\n\t" \
  "ds_read_b128 v[124:127], v61 offset:0\n\t" \
  "s_waitcnt lgkmcnt(13)\n\t" \
  "v_mfma_f32_16x16x32_bf16 v[200:203], v[64:67], v[88:91], 0\n\t" \
  "ds_read_b64_tr_b16 v[128:129], v63 offset:34816\n\t" \
  "s_waitcnt lgkmcnt(13)\n\t" \
  "v_mfma_f32_16x16x32_bf16 v[200:203], v[72:75], v[92:95], v[200:203]\n\t" \
  "ds_read_b64_tr_b16 v[130:131], v63 offset:35328\n\t" \
  "s_waitcnt lgkmcnt(13)\n\t" \
  "v_mfma_f32_16x16x32_bf16 v[208:211], v[72:75], v[96:99], 0\n\t" \
  "ds_read_b64_tr_b16 v[132:133], v63 offset:38912\n\t" \
  "s_waitcnt lgkmcnt(9)\n\t" \
  "v_mfma_f32_16x16x32_bf16 v[208:211], v[64:67], v[100:103], v[208:211]\n\t" \
  "ds_read_b64_tr_b16 v[134:135], v63 offset:39424\n\t" \
  "v_mfma_f32_16x16x32_bf16 v[188:191], v[104:107], v[68:71], 0\n\t" \
  "ds_read_b64_tr_b16 v[136:137], v62 offset:18432\n\t" \
  "v_mfma_f32_16x16x32_bf16 v[188:191], v[108:111], v[76:79], v[188:191]\n\t" \
  "ds_read_b64_tr_b16 v[138:139], v62 offset:18944\n\t" \
  "v_mfma_f32_16x16x32_bf16 v[196:199], v[108:111], v[80:83], 0\n\t" \
  "ds_read_b64_tr_b16 v[140:141], v62 offset:22528\n\t" \
  "v_mfma_f32_16x16x32_bf16 v[196:199], v[104:107], v[84:87], v[196:199]\n\t" \
  "ds_read_b64_tr_b16 v[142:143], v62 offset:23040\n\t" \
  "v_mfma_f32_16x16x32_bf16 v[204:207], v[104:107], v[88:91], 0\n\t" \
  "ds_read_b64_tr_b16 v[144:145], v63 offset:18432\n\t" \
  "v_mfma_f32_16x16x32_bf16 v[204:207], v[108:111], v[92:95], v[204:207]\n\t" \
  "ds_read_b64_tr_b16 v[146:147], v63 offset:18944\n\t" \
  "v_mfma_f32_16x16x32_bf16 v[212:215], v[108:111], v[96:99], 0\n\t" \
  "ds_read_b64_tr_b16 v[148:149], v63 offset:22528\n\t" \
  "v_mfma_f32_16x16x32_bf16 v[212:215], v[104:107], v[100:103], v[212:215]\n\t" \
  "ds_read_b64_tr_b16 v[150:151], v63 offset:23040\n\t" \
  "s_waitcnt lgkmcnt(7)\n\t" \
  "v_mfma_f32_16x16x32_bf16 v[216:219], v[68:71], v[112:115], 0\n\t" \
  "v_mfma_f32_16x16x32_bf16 v[216:219], v[76:79], v[116:119], v[216:219]\n\t" \
  "v_mfma_f32_16x16x32_bf16 v[224:227], v[80:83], v[116:119], 0\n\t" \
  "v_mfma_f32_16x16x32_bf16 v[224:227], v[84:87], v[112:115], v[224:227]\n\t" \
  "v_mfma_f32_16x16x32_bf16 v[232:235], v[88:91], v[112:115], 0\n\t" \
  "v_mfma_f32_16x16x32_bf16 v[232:235], v[92:95], v[116:119], v[232:235]\n\t" \
  "v_mfma_f32_16x16x32_bf16 v[240:243], v[96:99], v[116:119], 0\n\t" \
  "v_mfma_f32_16x16x32_bf16 v[240:243], v[100:103], v[112:115], v[240:243]\n\t" \
  "v_mfma_f32_16x16x32_bf16 v[248:251], v[120:123], v[112:115], 0\n\t" \
  "v_mfma_f32_16x16x32_bf16 v[248:251], v[124:127], v[116:119], v[248:251]\n\t" \
  "v_mfma_f32_16x16x32_bf16 v[252:255], v[120:123], v[128:131], 0\n\t" \
  "v_mfma_f32_16x16x32_bf16 v[252:255], v[124:127], v[132:135], v[252:255]\n\t" \
  "v_mfma_f32_16x16x32_bf16 v[220:223], v[76:79], v[132:135], 0\n\t" \
  "v_mfma_f32_16x16x32_bf16 v[220:223], v[68:71], v[128:131], v[220:223]\n\t" \
  "s_waitcnt lgkmcnt(0)\n\t" \
  "s_getpc_b64 s[88:89]\n\t" \
  "s_branch 28b\n\t" \
  "s_barrier\n\t" \
  "v_mfma_f32_16x16x32_bf16 v[228:231], v[84:87], v[128:131], 0\n\t" \
  "ds_read_b64_tr_b16 v[72:73], v54 offset:26624\n\t" \
  "v_mfma_f32_16x16x32_bf16 v[228:231], v[80:83], v[132:135], v[228:231]\n\t" \
  "ds_read_b64_tr_b16 v[74:75], v54 offset:27136\n\t" \
  "v_mfma_f32_16x16x32_bf16 v[236:239], v[92:95], v[132:135], 0\n\t" \
  "ds_read_b128 v[64:67], v52 offset:10240\n\t" \
  "v_mfma_f32_16x16x32_bf16 v[236:239], v[88:91], v[128:131], v[236:239]\n\t" \
  "ds_read_b64_tr_b16 v[108:109], v54 offset:30720\n\t" \
  "v_mfma_f32_16x16x32_bf16 v[244:247], v[100:103], v[128:131], 0\n\t" \
  "ds_read_b64_tr_b16 v[110:111], v54 offset:31232\n\t" \
  "v_mfma_f32_16x16x32_bf16 v[244:247], v[96:99], v[132:135], v[244:247]\n\t" \
  "ds_read_b128 v[104:107], v53 offset:10240\n\t" \
  "v_mfma_f32_16x16x32_bf16 v[152:155], v[136:139], v[68:71], 0\n\t" \
  "ds_read_b128 v[112:115], v53 offset:12288\n\t" \
  "v_mfma_f32_16x16x32_bf16 v[152:155], v[140:143], v[76:79], v[152:155]\n\t" \
  "ds_read_b128 v[116:119], v52 offset:12288\n\t" \
  "v_mfma_f32_16x16x32_bf16 v[160:163], v[140:143], v[80:83], 0\n\t" \
  "ds_read_b128 v[120:123], v52 offset:14336\n\t" \
  "v_mfma_f32_16x16x32_bf16 v[160:163], v[136:139], v[84:87], v[160:163]\n\t" \
  "ds_read_b128 v[124:127], v53 offset:14336\n\t" \
  "v_mfma_f32_16x16x32_bf16 v[168:171], v[136:139], v[88:91], 0\n\t" \
  "ds_read_b128 v[128:131], v53 offset:16384\n\t" \
  "v_mfma_f32_16x16x32_bf16 v[168:171], v[140:143], v[92:95], v[168:171]\n\t" \
  "ds_read_b128 v[132:135], v52 offset:16384\n\t" \
  "v_mfma_f32_16x16x32_bf16 v[176:179], v[140:143], v[96:99], 0\n\t" \
  "ds_read_b64_tr_b16 v[140:141], v55 offset:26624\n\t" \
  "v_mfma_f32_16x16x32_bf16 v[176:179], v[136:139], v[100:103], v[176:179]\n\t" \
  "ds_read_b64_tr_b16 v[142:143], v55 offset:27136\n\t" \
  "v_mfma_f32_16x16x32_bf16 v[156:159], v[144:147], v[68:71], 0\n\t" \
  "ds_read_b64_tr_b16 v[136:137], v55 offset:30720\n\t" \
  "v_mfma_f32_16x16x32_bf16 v[156:159], v[148:151], v[76:79], v[156:159]\n\t" \
  "ds_read_b64_tr_b16 v[138:139], v55 offset:31232\n\t" \
  "v_mfma_f32_16x16x32_bf16 v[164:167], v[148:151], v[80:83], 0\n\t" \
  "ds_read_b64_tr_b16 v[68:69], v54 offset:34816\n\t" \
  "v_mfma_f32_16x16x32_bf16 v[164:167], v[144:147], v[84:87], v[164:167]\n\t" \
  "ds_read_b64_tr_b16 v[70:71], v54 offset:35328\n\t" \
  "v_mfma_f32_16x16x32_bf16 v[172:175], v[144:147], v[88:91], 0\n\t" \
  "ds_read_b64_tr_b16 v[76:77], v54 offset:38912\n\t" \
  "v_mfma_f32_16x16x32_bf16 v[172:175], v[148:151], v[92:95], v[172:175]\n\t" \
  "ds_read_b64_tr_b16 v[78:79], v54 offset:39424\n\t" \
  "v_mfma_f32_16x16x32_bf16 v[180:183], v[148:151], v[96:99], 0\n\t" \
  "ds_read_b128 v[80:83], v52 offset:0\n\t" \
  "v_mfma_f32_16x16x32_bf16 v[180:183], v[144:147], v[100:103], v[180:183]\n\t" \
  "ds_read_b128 v[84:87], v53 offset:0\n\t" \
  "s_waitcnt lgkmcnt(7)\n\t" \
  "v_mfma_f32_16x16x32_bf16 v[184:187], v[72:75], v[64:67], v[184:187]\n\t" \
  "ds_read_b64_tr_b16 v[88:89], v55 offset:34816\n\t" \
  "v_mfma_f32_16x16x32_bf16 v[184:187], v[108:111], v[104:107], v[184:187]\n\t" \
  "ds_read_b64_tr_b16 v[90:91], v55 offset:35328\n\t" \
  "v_mfma_f32_16x16x32_bf16 v[192:195], v[108:111], v[112:115], v[192:195]\n\t" \
  "ds_read_b64_tr_b16 v[92:93], v55 offset:38912\n\t" \
  "v_mfma_f32_16x16x32_bf16 v[192:195], v[72:75], v[116:119], v[192:195]\n\t" \
  "ds_read_b64_tr_b16 v[94:95], v55 offset:39424\n\t" \
  "v_mfma_f32_16x16x32_bf16 v[200:203], v[72:75], v[120:123], v[200:203]\n\t" \
  "ds_read_b64_tr_b16 v[96:97], v54 offset:18432\n\t" \
  "v_mfma_f32_16x16x32_bf16 v[200:203], v[108:111], v[124:127], v[200:203]\n\t" \
  "ds_read_b64_tr_b16 v[98:99], v54 offset:18944\n\t" \
  "v_mfma_f32_16x16x32_bf16 v[208:211], v[108:111], v[128:131], v[208:211]\n\t" \
  "ds_read_b64_tr_b16 v[148:149], v54 offset:22528\n\t" \
  "v_mfma_f32_16x16x32_bf16 v[208:211], v[72:75], v[132:135], v[208:211]\n\t" \
  "ds_read_b64_tr_b16 v[150:151], v54 offset:23040\n\t" \
  "v_mfma_f32_16x16x32_bf16 v[188:191], v[140:143], v[64:67], v[188:191]\n\t" \
  "ds_read_b64_tr_b16 v[100:101], v55 offset:18432\n\t" \
  "s_waitcnt lgkmcnt(7)\n\t" \
  "v_mfma_f32_16x16x32_bf16 v[188:191], v[136:139], v[104:107], v[188:191]\n\t" \
  "ds_read_b64_tr_b16 v[102:103], v55 offset:18944\n\t" \
  "v_mfma_f32_16x16x32_bf16 v[196:199], v[136:139], v[112:115], v[196:199]\n\t" \
  "ds_read_b64_tr_b16 v[144:145], v55 offset:22528\n\t" \
  "v_mfma_f32_16x16x32_bf16 v[196:199], v[140:143], v[116:119], v[196:199]\n\t" \
  "ds_read_b64_tr_b16 v[146:147], v55 offset:23040\n\t" \
  "v_mfma_f32_16x16x32_bf16 v[204:207], v[140:143], v[120:123], v[204:207]\n\t" \
  "v_mfma_f32_16x16x32_bf16 v[204:207], v[136:139], v[124:127], v[204:207]\n\t" \
  "v_mfma_f32_16x16x32_bf16 v[212:215], v[136:139], v[128:131], v[212:215]\n\t" \
  "v_mfma_f32_16x16x32_bf16 v[212:215], v[140:143], v[132:135], v[212:215]\n\t" \
  "v_mfma_f32_16x16x32_bf16 v[216:219], v[64:67], v[68:71], v[216:219]\n\t" \
  "v_mfma_f32_16x16x32_bf16 v[216:219], v[104:107], v[76:79], v[216:219]\n\t" \
  "v_mfma_f32_16x16x32_bf16 v[224:227], v[112:115], v[76:79], v[224:227]\n\t" \
  "v_mfma_f32_16x16x32_bf16 v[224:227], v[116:119], v[68:71], v[224:227]\n\t" \
  "v_mfma_f32_16x16x32_bf16 v[232:235], v[120:123], v[68:71], v[232:235]\n\t" \
  "v_mfma_f32_16x16x32_bf16 v[232:235], v[124:127], v[76:79], v[232:235]\n\t" \
  "v_mfma_f32_16x16x32_bf16 v[240:243], v[128:131], v[76:79], v[240:243]\n\t" \
  "v_mfma_f32_16x16x32_bf16 v[240:243], v[132:135], v[68:71], v[240:243]\n\t" \
  "v_mfma_f32_16x16x32_bf16 v[248:251], v[80:83], v[68:71], v[248:251]\n\t" \
  "v_mfma_f32_16x16x32_bf16 v[248:251], v[84:87], v[76:79], v[248:251]\n\t" \
  "v_mfma_f32_16x16x32_bf16 v[252:255], v[80:83], v[88:91], v[252:255]\n\t" \
  "s_waitcnt lgkmcnt(0)\n\t" \
  "v_mfma_f32_16x16x32_bf16 v[252:255], v[84:87], v[92:95], v[252:255]\n\t" \
  "v_mfma_f32_16x16x32_bf16 v[220:223], v[104:107], v[92:95], v[220:223]\n\t" \
  "v_mfma_f32_16x16x32_bf16 v[220:223], v[64:67], v[88:91], v[220:223]\n\t" \
  "s_barrier\n\t" \
  "v_mfma_f32_16x16x32_bf16 v[228:231], v[116:119], v[88:91], v[228:231]\n\t" \
  "ds_read_b64_tr_b16 v[108:109], v58 offset:26624\n\t" \
  "v_mfma_f32_16x16x32_bf16 v[228:231], v[112:115], v[92:95], v[228:231]\n\t" \
  "ds_read_b64_tr_b16 v[110:111], v58 offset:27136\n\t" \
  "v_mfma_f32_16x16x32_bf16 v[236:239], v[124:127], v[92:95], v[236:239]\n\t" \
  "ds_read_b128 v[72:75], v56 offset:10240\n\t" \
  "v_mfma_f32_16x16x32_bf16 v[236:239], v[120:123], v[88:91], v[236:239]\n\t" \
  "ds_read_b64_tr_b16 v[136:137], v58 offset:30720\n\t" \
  "v_mfma_f32_16x16x32_bf16 v[244:247], v[132:135], v[88:91], v[244:247]\n\t" \
  "ds_read_b64_tr_b16 v[138:139], v58 offset:31232\n\t" \
  "v_mfma_f32_16x16x32_bf16 v[244:247], v[128:131], v[92:95], v[244:247]\n\t" \
  "ds_read_b128 v[140:143], v57 offset:10240\n\t" \
  "v_mfma_f32_16x16x32_bf16 v[152:155], v[96:99], v[64:67], v[152:155]\n\t" \
  "ds_read_b128 v[68:71], v57 offset:12288\n\t" \
  "v_mfma_f32_16x16x32_bf16 v[152:155], v[148:151], v[104:107], v[152:155]\n\t" \
  "ds_read_b128 v[76:79], v56 offset:12288\n\t" \
  "v_mfma_f32_16x16x32_bf16 v[160:163], v[148:151], v[112:115], v[160:163]\n\t" \
  "ds_read_b128 v[80:83], v56 offset:14336\n\t" \
  "v_mfma_f32_16x16x32_bf16 v[160:163], v[96:99], v[116:119], v[160:163]\n\t" \
  "ds_read_b128 v[84:87], v57 offset:14336\n\t" \
  "v_mfma_f32_16x16x32_bf16 v[168:171], v[96:99], v[120:123], v[168:171]\n\t" \
  "ds_read_b128 v[88:91], v57 offset:16384\n\t" \
  "v_mfma_f32_16x16x32_bf16 v[168:171], v[148:151], v[124:127], v[168:171]\n\t" \
  "ds_read_b128 v[92:95], v56 offset:16384\n\t" \
  "v_mfma_f32_16x16x32_bf16 v[176:179], v[148:151], v[128:131], v[176:179]\n\t" \
  "ds_read_b64_tr_b16 v[148:149], v59 offset:26624\n\t" \
  "v_mfma_f32_16x16x32_bf16 v[176:179], v[96:99], v[132:135], v[176:179]\n\t" \
  "ds_read_b64_tr_b16 v[150:151], v59 offset:27136\n\t" \
  "v_mfma_f32_16x16x32_bf16 v[156:159], v[100:103], v[64:67], v[156:159]\n\t" \
  "ds_read_b64_tr_b16 v[96:97], v59 offset:30720\n\t" \
  "v_mfma_f32_16x16x32_bf16 v[156:159], v[144:147], v[104:107], v[156:159]\n\t" \
  "ds_read_b64_tr_b16 v[98:99], v59 offset:31232\n\t" \
  "v_mfma_f32_16x16x32_bf16 v[164:167], v[144:147], v[112:115], v[164:167]\n\t" \
  "ds_read_b64_tr_b16 v[64:65], v58 offset:34816\n\t" \
  "v_mfma_f32_16x16x32_bf16 v[164:167], v[100:103], v[116:119], v[164:167]\n\t" \
  "ds_read_b64_tr_b16 v[66:67], v58 offset:35328\n\t" \
  "v_mfma_f32_16x16x32_bf16 v[172:175], v[100:103], v[120:123], v[172:175]\n\t" \
  "ds_read_b64_tr_b16 v[104:105], v58 offset:38912\n\t" \
  "v_mfma_f32_16x16x32_bf16 v[172:175], v[144:147], v[124:127], v[172:175]\n\t" \
  "ds_read_b64_tr_b16 v[106:107], v58 offset:39424\n\t" \
  "v_mfma_f32_16x16x32_bf16 v[180:183], v[144:147], v[128:131], v[180:183]\n\t" \
  "ds_read_b128 v[112:115], v56 offset:0\n\t" \
  "v_mfma_f32_16x16x32_bf16 v[180:183], v[100:103], v[132:135], v[180:183]\n\t" \
  "ds_read_b128 v[116:119], v57 offset:0\n\t" \
  "s_waitcnt lgkmcnt(7)\n\t" \
  "v_mfma_f32_16x16x32_bf16 v[184:187], v[108:111], v[72:75], v[184:187]\n\t" \
  "ds_read_b64_tr_b16 v[120:121], v59 offset:34816\n\t" \
  "v_mfma_f32_16x16x32_bf16 v[184:187], v[136:139], v[140:143], v[184:187]\n\t" \
  "ds_read_b64_tr_b16 v[122:123], v59 offset:35328\n\t" \
  "v_mfma_f32_16x16x32_bf16 v[192:195], v[136:139], v[68:71], v[192:195]\n\t" \
  "ds_read_b64_tr_b16 v[124:125], v59 offset:38912\n\t" \
  "v_mfma_f32_16x16x32_bf16 v[192:195], v[108:111], v[76:79], v[192:195]\n\t" \
  "ds_read_b64_tr_b16 v[126:127], v59 offset:39424\n\t" \
  "v_mfma_f32_16x16x32_bf16 v[200:203], v[108:111], v[80:83], v[200:203]\n\t" \
  "ds_read_b64_tr_b16 v[128:129], v58 offset:18432\n\t" \
  "v_mfma_f32_16x16x32_bf16 v[200:203], v[136:139], v[84:87], v[200:203]\n\t" \
  "ds_read_b64_tr_b16 v[130:131], v58 offset:18944\n\t" \
  "v_mfma_f32_16x16x32_bf16 v[208:211], v[136:139], v[88:91], v[208:211]\n\t" \
  "ds_read_b64_tr_b16 v[144:145], v58 offset:22528\n\t" \
  "v_mfma_f32_16x16x32_bf16 v[208:211], v[108:111], v[92:95], v[208:211]\n\t" \
  "ds_read_b64_tr_b16 v[146:147], v58 offset:23040\n\t" \
  "v_mfma_f32_16x16x32_bf16 v[188:191], v[148:151], v[72:75], v[188:191]\n\t" \
  "ds_read_b64_tr_b16 v[100:101], v59 offset:18432\n\t" \
  "s_waitcnt lgkmcnt(7)\n\t" \
  "v_mfma_f32_16x16x32_bf16 v[188:191], v[96:99], v[140:143], v[188:191]\n\t" \
  "ds_read_b64_tr_b16 v[102:103], v59 offset:18944\n\t" \
  "v_mfma_f32_16x16x32_bf16 v[196:199], v[96:99], v[68:71], v[196:199]\n\t" \
  "ds_read_b64_tr_b16 v[132:133], v59 offset:22528\n\t" \
  "v_mfma_f32_16x16x32_bf16 v[196:199], v[148:151], v[76:79], v[196:199]\n\t" \
  "ds_read_b64_tr_b16 v[134:135], v59 offset:23040\n\t" \
  "v_mfma_f32_16x16x32_bf16 v[204:207], v[148:151], v[80:83], v[204:207]\n\t" \
  "v_mfma_f32_16x16x32_bf16 v[204:207], v[96:99], v[84:87], v[204:207]\n\t" \
  "v_mfma_f32_16x16x32_bf16 v[212:215], v[96:99], v[88:91], v[212:215]\n\t" \
  "v_mfma_f32_16x16x32_bf16 v[212:215], v[148:151], v[92:95], v[212:215]\n\t" \
  "v_mfma_f32_16x16x32_bf16 v[216:219], v[72:75], v[64:67], v[216:219]\n\t" \
  "v_mfma_f32_16x16x32_bf16 v[216:219], v[140:143], v[104:107], v[216:219]\n\t" \
  "v_mfma_f32_16x16x32_bf16 v[224:227], v[68:71], v[104:107], v[224:227]\n\t" \
  "v_mfma_f32_16x16x32_bf16 v[224:227], v[76:79], v[64:67], v[224:227]\n\t" \
  "v_mfma_f32_16x16x32_bf16 v[232:235], v[80:83], v[64:67], v[232:235]\n\t" \
  "v_mfma_f32_16x16x32_bf16 v[232:235], v[84:87], v[104:107], v[232:235]\n\t" \
  "v_mfma_f32_16x16x32_bf16 v[240:243], v[88:91], v[104:107], v[240:243]\n\t" \
  "v_mfma_f32_16x16x32_bf16 v[240:243], v[92:95], v[64:67], v[240:243]\n\t" \
  "v_mfma_f32_16x16x32_bf16 v[248:251], v[112:115], v[64:67], v[248:251]\n\t" \
  "v_mfma_f32_16x16x32_bf16 v[248:251], v[116:119], v[104:107], v[248:251]\n\t" \
  "v_mfma_f32_16x16x32_bf16 v[252:255], v[112:115], v[120:123], v[252:255]\n\t" \
  "s_waitcnt lgkmcnt(0)\n\t" \
  "v_mfma_f32_16x16x32_bf16 v[252:255], v[116:119], v[124:127], v[252:255]\n\t" \
  "v_mfma_f32_16x16x32_bf16 v[220:223], v[140:143], v[124:127], v[220:223]\n\t" \
  "v_mfma_f32_16x16x32_bf16 v[220:223], v[72:75], v[120:123], v[220:223]\n\t" \
  "s_barrier\n\t" \
  "v_mfma_f32_16x16x32_bf16 v[228:231], v[76:79], v[120:123], v[228:231]\n\t" \
  "ds_read_b64_tr_b16 v[136:137], v62 offset:26624\n\t" \
  "v_bfe_u32 v30, v4, 9, 1\n\t" \
  "v_mfma_f32_16x16x32_bf16 v[228:231], v[68:71], v[124:127], v[228:231]\n\t" \
  "ds_read_b64_tr_b16 v[138:139], v62 offset:27136\n\t" \
  "v_mad_i32_i24 v30, v30, -16, v0\n\t" \
  "v_mfma_f32_16x16x32_bf16 v[236:239], v[84:87], v[124:127], v[236:239]\n\t" \
  "ds_read_b128 v[108:111], v60 offset:10240\n\t" \
  "ds_read_b128 v[26:29], v0 offset:1552\n\t" \
  "v_mfma_f32_16x16x32_bf16 v[236:239], v[80:83], v[120:123], v[236:239]\n\t" \
  "ds_read_b64_tr_b16 v[96:97], v62 offset:30720\n\t" \
  "ds_read_b128 v[30:33], v30 offset:1040\n\t" \
  "v_mfma_f32_16x16x32_bf16 v[244:247], v[92:95], v[120:123], v[244:247]\n\t" \
  "ds_read_b64_tr_b16 v[98:99], v62 offset:31232\n\t" \
  "ds_read_b128 v[34:37], v0 offset:512\n\t" \
  "v_mfma_f32_16x16x32_bf16 v[244:247], v[88:91], v[124:127], v[244:247]\n\t" \
  "ds_read_b128 v[148:151], v61 offset:10240\n\t" \
  "ds_read_b64_tr_b16 v[38:39], v1 offset:512\n\t" \
  "v_mfma_f32_16x16x32_bf16 v[152:155], v[128:131], v[72:75], v[152:155]\n\t" \
  "ds_read_b128 v[64:67], v61 offset:12288\n\t" \
  "ds_read_b64_tr_b16 v[40:41], v1 offset:0\n\t" \
  "v_mfma_f32_16x16x32_bf16 v[152:155], v[144:147], v[140:143], v[152:155]\n\t" \
  "ds_read_b128 v[104:107], v60 offset:12288\n\t" \
  "s_waitcnt vmcnt(0)\n\t" \
  "v_mfma_f32_16x16x32_bf16 v[160:163], v[144:147], v[68:71], v[160:163]\n\t" \
  "ds_read_b128 v[112:115], v60 offset:14336\n\t" \
  "v_perm_b32 v18, v10, v10, v14\n\t" \
  "v_mfma_f32_16x16x32_bf16 v[160:163], v[128:131], v[76:79], v[160:163]\n\t" \
  "ds_read_b128 v[116:119], v61 offset:14336\n\t" \
  "v_perm_b32 v19, v10, v10, v15\n\t" \
  "v_mfma_f32_16x16x32_bf16 v[168:171], v[128:131], v[80:83], v[168:171]\n\t" \
  "ds_read_b128 v[120:123], v61 offset:16384\n\t" \
  "v_perm_b32 v20, v10, v10, v16\n\t" \
  "v_mfma_f32_16x16x32_bf16 v[168:171], v[144:147], v[84:87], v[168:171]\n\t" \
  "ds_read_b128 v[124:127], v60 offset:16384\n\t" \
  "v_perm_b32 v21, v10, v10, v17\n\t" \
  "v_mfma_f32_16x16x32_bf16 v[176:179], v[144:147], v[88:91], v[176:179]\n\t" \
  "ds_read_b64_tr_b16 v[144:145], v63 offset:26624\n\t" \
  "v_perm_b32 v22, v11, v11, v14\n\t" \
  "v_mfma_f32_16x16x32_bf16 v[176:179], v[128:131], v[92:95], v[176:179]\n\t" \
  "ds_read_b64_tr_b16 v[146:147], v63 offset:27136\n\t" \
  "v_perm_b32 v23, v11, v11, v15\n\t" \
  "v_mfma_f32_16x16x32_bf16 v[156:159], v[100:103], v[72:75], v[156:159]\n\t" \
  "ds_read_b64_tr_b16 v[128:129], v63 offset:30720\n\t" \
  "v_perm_b32 v24, v11, v11, v16\n\t" \
  "v_mfma_f32_16x16x32_bf16 v[156:159], v[132:135], v[140:143], v[156:159]\n\t" \
  "ds_read_b64_tr_b16 v[130:131], v63 offset:31232\n\t" \
  "v_perm_b32 v25, v11, v11, v17\n\t" \
  "v_mfma_f32_16x16x32_bf16 v[164:167], v[132:135], v[68:71], v[164:167]\n\t" \
  "ds_read_b64_tr_b16 v[72:73], v62 offset:34816\n\t" \
  "v_mfma_f32_16x16x32_bf16 v[164:167], v[100:103], v[76:79], v[164:167]\n\t" \
  "ds_read_b64_tr_b16 v[74:75], v62 offset:35328\n\t" \
  "v_mfma_f32_16x16x32_bf16 v[172:175], v[100:103], v[80:83], v[172:175]\n\t" \
  "ds_read_b64_tr_b16 v[140:141], v62 offset:38912\n\t" \
  "s_waitcnt lgkmcnt(6)\n\t" \
  "v_mfma_f32_16x16x32_bf16 v[42:45], v[26:29], v[18:21], 0\n\t" \
  "v_mfma_f32_16x16x32_bf16 v[42:45], v[30:33], v[22:25], v[42:45]\n\t" \
  "v_mfma_f32_16x16x32_bf16 v[46:49], v[30:33], v[18:21], 0\n\t" \
  "v_mfma_f32_16x16x32_bf16 v[46:49], v[34:37], v[22:25], v[46:49]\n\t" \
  "ds_read_b128 v[26:29], v0 offset:1808\n\t" \
  "ds_read_b128 v[30:33], v0 offset:1296\n\t" \
  "v_mfma_f32_16x16x32_bf16 v[172:175], v[132:135], v[84:87], v[172:175]\n\t" \
  "ds_read_b64_tr_b16 v[142:143], v62 offset:39424\n\t" \
  "ds_read_b128 v[34:37], v0 offset:768\n\t" \
  "v_mfma_f32_16x16x32_bf16 v[180:183], v[132:135], v[88:91], v[180:183]\n\t" \
  "ds_read_b128 v[68:71], v60 offset:0\n\t" \
  "v_mfma_f32_16x16x32_bf16 v[180:183], v[100:103], v[92:95], v[180:183]\n\t" \
  "ds_read_b128 v[76:79], v61 offset:0\n\t" \
  "v_mfma_f32_16x16x32_bf16 v[184:187], v[136:139], v[108:111], v[184:187]\n\t" \
  "ds_read_b64_tr_b16 v[80:81], v63 offset:34816\n\t" \
  "v_lshlrev_b32 v50, 16, v38\n\t" \
  "v_mfma_f32_16x16x32_bf16 v[184:187], v[96:99], v[148:151], v[184:187]\n\t" \
  "ds_read_b64_tr_b16 v[82:83], v63 offset:35328\n\t" \
  "v_lshlrev_b32 v51, 16, v40\n\t" \
  "v_mfma_f32_16x16x32_bf16 v[192:195], v[96:99], v[64:67], v[192:195]\n\t" \
  "ds_read_b64_tr_b16 v[84:85], v63 offset:38912\n\t" \
  "v_mul_f32 v42, v42, v50\n\t" \
  "v_mfma_f32_16x16x32_bf16 v[192:195], v[136:139], v[104:107], v[192:195]\n\t" \
  "ds_read_b64_tr_b16 v[86:87], v63 offset:39424\n\t" \
  "v_mul_f32 v46, v46, v51\n\t" \
  "v_mfma_f32_16x16x32_bf16 v[200:203], v[136:139], v[112:115], v[200:203]\n\t" \
  "ds_read_b64_tr_b16 v[88:89], v62 offset:18432\n\t" \
  "v_cvt_pk_bf16_f32 v42, v42, v46\n\t" \
  "v_mfma_f32_16x16x32_bf16 v[200:203], v[96:99], v[116:119], v[200:203]\n\t" \
  "ds_read_b64_tr_b16 v[90:91], v62 offset:18944\n\t" \
  "v_and_b32 v50, 0xffff0000, v38\n\t" \
  "v_mfma_f32_16x16x32_bf16 v[208:211], v[96:99], v[120:123], v[208:211]\n\t" \
  "ds_read_b64_tr_b16 v[132:133], v62 offset:22528\n\t" \
  "v_and_b32 v51, 0xffff0000, v40\n\t" \
  "v_mfma_f32_16x16x32_bf16 v[208:211], v[136:139], v[124:127], v[208:211]\n\t" \
  "ds_read_b64_tr_b16 v[134:135], v62 offset:23040\n\t" \
  "v_mul_f32 v43, v43, v50\n\t" \
  "s_waitcnt lgkmcnt(7)\n\t" \
  "v_mfma_f32_16x16x32_bf16 v[188:191], v[144:147], v[108:111], v[188:191]\n\t" \
  "ds_read_b64_tr_b16 v[92:93], v63 offset:18432\n\t" \
  "v_mul_f32 v47, v47, v51\n\t" \
  "v_mfma_f32_16x16x32_bf16 v[188:191], v[128:131], v[148:151], v[188:191]\n\t" \
  "ds_read_b64_tr_b16 v[94:95], v63 offset:18944\n\t" \
  "v_cvt_pk_bf16_f32 v43, v43, v47\n\t" \
  "v_mfma_f32_16x16x32_bf16 v[196:199], v[128:131], v[64:67], v[196:199]\n\t" \
  "ds_read_b64_tr_b16 v[100:101], v63 offset:22528\n\t" \
  "v_lshlrev_b32 v50, 16, v39\n\t" \
  "v_mfma_f32_16x16x32_bf16 v[196:199], v[144:147], v[104:107], v[196:199]\n\t" \
  "ds_read_b64_tr_b16 v[102:103], v63 offset:23040\n\t" \
  "v_lshlrev_b32 v51, 16, v41\n\t" \
  "v_mfma_f32_16x16x32_bf16 v[204:207], v[144:147], v[112:115], v[204:207]\n\t" \
  "v_mul_f32 v44, v44, v50\n\t" \
  "v_mul_f32 v48, v48, v51\n\t" \
  "v_mfma_f32_16x16x32_bf16 v[204:207], v[128:131], v[116:119], v[204:207]\n\t" \
  "v_cvt_pk_bf16_f32 v44, v44, v48\n\t" \
  "v_and_b32 v50, 0xffff0000, v39\n\t" \
  "v_mfma_f32_16x16x32_bf16 v[212:215], v[128:131], v[120:123], v[212:215]\n\t" \
  "v_and_b32 v51, 0xffff0000, v41\n\t" \
  "v_mul_f32 v45, v45, v50\n\t" \
  "v_mfma_f32_16x16x32_bf16 v[212:215], v[144:147], v[124:127], v[212:215]\n\t" \
  "v_mul_f32 v49, v49, v51\n\t" \
  "ds_read_b64_tr_b16 v[38:39], v1 offset:768\n\t" \
  "v_mfma_f32_16x16x32_bf16 v[216:219], v[108:111], v[72:75], v[216:219]\n\t" \
  "ds_read_b64_tr_b16 v[40:41], v1 offset:256\n\t" \
  "v_cvt_pk_bf16_f32 v45, v45, v49\n\t" \
  "v_mfma_f32_16x16x32_bf16 v[216:219], v[148:151], v[140:143], v[216:219]\n\t" \
  "v_mfma_f32_16x16x32_bf16 v[224:227], v[64:67], v[140:143], v[224:227]\n\t" \
  "v_mfma_f32_16x16x32_bf16 v[224:227], v[104:107], v[72:75], v[224:227]\n\t" \
  "v_mfma_f32_16x16x32_bf16 v[232:235], v[112:115], v[72:75], v[232:235]\n\t" \
  "v_mfma_f32_16x16x32_bf16 v[232:235], v[116:119], v[140:143], v[232:235]\n\t" \
  "v_mfma_f32_16x16x32_bf16 v[240:243], v[120:123], v[140:143], v[240:243]\n\t" \
  "v_mfma_f32_16x16x32_bf16 v[240:243], v[124:127], v[72:75], v[240:243]\n\t" \
  "v_mfma_f32_16x16x32_bf16 v[248:251], v[68:71], v[72:75], v[248:251]\n\t" \
  "v_mfma_f32_16x16x32_bf16 v[248:251], v[76:79], v[140:143], v[248:251]\n\t" \
  "s_waitcnt lgkmcnt(0)\n\t" \
  "v_mfma_f32_16x16x32_bf16 v[252:255], v[68:71], v[80:83], v[252:255]\n\t" \
  "v_mfma_f32_16x16x32_bf16 v[252:255], v[76:79], v[84:87], v[252:255]\n\t" \
  "v_mfma_f32_16x16x32_bf16 v[220:223], v[148:151], v[84:87], v[220:223]\n\t" \
  "v_mfma_f32_16x16x32_bf16 v[220:223], v[108:111], v[80:83], v[220:223]\n\t" \
  "s_barrier\n\t" \
  "v_mfma_f32_16x16x32_bf16 v[228:231], v[104:107], v[80:83], v[228:231]\n\t" \
  "ds_read_b64_tr_b16 v[96:97], v54 offset:26624\n\t" \
  "v_mfma_f32_16x16x32_bf16 v[228:231], v[64:67], v[84:87], v[228:231]\n\t" \
  "v_mov_b32 v50, 163344\n\t" \
  "ds_read_b64_tr_b16 v[98:99], v54 offset:27136\n\t" \
  "v_mfma_f32_16x16x32_bf16 v[236:239], v[116:119], v[84:87], v[236:239]\n\t" \
  "ds_read_b32 v50, v50 offset:24\n\t" \
  "ds_read_b128 v[136:139], v52 offset:10240\n\t" \
  "v_mfma_f32_16x16x32_bf16 v[236:239], v[112:115], v[80:83], v[236:239]\n\t" \
  "ds_read_b64_tr_b16 v[128:129], v54 offset:30720\n\t" \
  "v_mfma_f32_16x16x32_bf16 v[244:247], v[124:127], v[80:83], v[244:247]\n\t" \
  "ds_read_b64_tr_b16 v[130:131], v54 offset:31232\n\t" \
  "v_mfma_f32_16x16x32_bf16 v[244:247], v[120:123], v[84:87], v[244:247]\n\t" \
  "ds_read_b128 v[144:147], v53 offset:10240\n\t" \
  "v_mfma_f32_16x16x32_bf16 v[152:155], v[88:91], v[108:111], v[152:155]\n\t" \
  "ds_read_b128 v[72:75], v53 offset:12288\n\t" \
  "v_mfma_f32_16x16x32_bf16 v[152:155], v[132:135], v[148:151], v[152:155]\n\t" \
  "ds_read_b128 v[140:143], v52 offset:12288\n\t" \
  "v_mfma_f32_16x16x32_bf16 v[160:163], v[132:135], v[64:67], v[160:163]\n\t" \
  "ds_read_b128 v[68:71], v52 offset:14336\n\t" \
  "v_mfma_f32_16x16x32_bf16 v[160:163], v[88:91], v[104:107], v[160:163]\n\t" \
  "ds_read_b128 v[76:79], v53 offset:14336\n\t" \
  "v_mfma_f32_16x16x32_bf16 v[168:171], v[88:91], v[112:115], v[168:171]\n\t" \
  "ds_read_b128 v[80:83], v53 offset:16384\n\t" \
  "v_mfma_f32_16x16x32_bf16 v[168:171], v[132:135], v[116:119], v[168:171]\n\t" \
  "ds_read_b128 v[84:87], v52 offset:16384\n\t" \
  "v_mfma_f32_16x16x32_bf16 v[176:179], v[132:135], v[120:123], v[176:179]\n\t" \
  "s_waitcnt lgkmcnt(10)\n\t" \
  "v_readfirstlane_b32 s88, v50\n\t" \
  "s_mov_b32 s89, 0\n\t" \
  "ds_read_b64_tr_b16 v[132:133], v55 offset:26624\n\t" \
  "v_mfma_f32_16x16x32_bf16 v[176:179], v[88:91], v[124:127], v[176:179]\n\t" \
  "ds_read_b64_tr_b16 v[134:135], v55 offset:27136\n\t" \
  "v_mfma_f32_16x16x32_bf16 v[156:159], v[92:95], v[108:111], v[156:159]\n\t" \
  "s_xor_b64 %24, %24, s[88:89]\n\t" \
  "ds_read_b64_tr_b16 v[88:89], v55 offset:30720\n\t" \
  "v_mfma_f32_16x16x32_bf16 v[156:159], v[100:103], v[148:151], v[156:159]\n\t" \
  "s_xor_b64 %25, %25, s[88:89]\n\t" \
  "ds_read_b64_tr_b16 v[90:91], v55 offset:31232\n\t" \
  "v_mfma_f32_16x16x32_bf16 v[164:167], v[100:103], v[64:67], v[164:167]\n\t" \
  "s_xor_b64 %14, %14, s[88:89]\n\t" \
  "ds_read_b64_tr_b16 v[108:109], v54 offset:34816\n\t" \
  "v_mfma_f32_16x16x32_bf16 v[164:167], v[92:95], v[104:107], v[164:167]\n\t" \
  "s_xor_b64 %19, %19, s[88:89]\n\t" \
  "ds_read_b64_tr_b16 v[110:111], v54 offset:35328\n\t" \
  "v_mfma_f32_16x16x32_bf16 v[172:175], v[92:95], v[112:115], v[172:175]\n\t" \
  "s_xor_b64 %21, %21, s[88:89]\n\t" \
  "ds_read_b64_tr_b16 v[148:149], v54 offset:38912\n\t" \
  "v_mfma_f32_16x16x32_bf16 v[172:175], v[100:103], v[116:119], v[172:175]\n\t" \
  "s_xor_b64 s[92:93], s[92:93], s[88:89]\n\t" \
  "ds_read_b64_tr_b16 v[150:151], v54 offset:39424\n\t" \
  "v_mfma_f32_16x16x32_bf16 v[180:183], v[100:103], v[120:123], v[180:183]\n\t" \
  "ds_read_b128 v[64:67], v52 offset:0\n\t" \
  "v_mfma_f32_16x16x32_bf16 v[180:183], v[92:95], v[124:127], v[180:183]\n\t" \
  "ds_read_b128 v[104:107], v53 offset:0\n\t" \
  "s_waitcnt lgkmcnt(7)\n\t" \
  "v_mfma_f32_16x16x32_bf16 v[184:187], v[96:99], v[136:139], v[184:187]\n\t" \
  "ds_read_b64_tr_b16 v[112:113], v55 offset:34816\n\t" \
  "v_mfma_f32_16x16x32_bf16 v[184:187], v[128:131], v[144:147], v[184:187]\n\t" \
  "ds_read_b64_tr_b16 v[114:115], v55 offset:35328\n\t" \
  "v_mfma_f32_16x16x32_bf16 v[192:195], v[128:131], v[72:75], v[192:195]\n\t" \
  "ds_read_b64_tr_b16 v[116:117], v55 offset:38912\n\t" \
  "v_mfma_f32_16x16x32_bf16 v[192:195], v[96:99], v[140:143], v[192:195]\n\t" \
  "ds_read_b64_tr_b16 v[118:119], v55 offset:39424\n\t" \
  "v_mfma_f32_16x16x32_bf16 v[200:203], v[96:99], v[68:71], v[200:203]\n\t" \
  "ds_read_b64_tr_b16 v[100:101], v54 offset:18432\n\t" \
  "v_mfma_f32_16x16x32_bf16 v[200:203], v[128:131], v[76:79], v[200:203]\n\t" \
  "ds_read_b64_tr_b16 v[102:103], v54 offset:18944\n\t" \
  "v_mfma_f32_16x16x32_bf16 v[208:211], v[128:131], v[80:83], v[208:211]\n\t" \
  "ds_read_b64_tr_b16 v[120:121], v54 offset:22528\n\t" \
  "v_mfma_f32_16x16x32_bf16 v[208:211], v[96:99], v[84:87], v[208:211]\n\t" \
  "ds_read_b64_tr_b16 v[122:123], v54 offset:23040\n\t" \
  "v_mfma_f32_16x16x32_bf16 v[188:191], v[132:135], v[136:139], v[188:191]\n\t" \
  "ds_read_b64_tr_b16 v[92:93], v55 offset:18432\n\t" \
  "s_waitcnt lgkmcnt(7)\n\t" \
  "v_mfma_f32_16x16x32_bf16 v[188:191], v[88:91], v[144:147], v[188:191]\n\t" \
  "ds_read_b64_tr_b16 v[94:95], v55 offset:18944\n\t" \
  "v_mfma_f32_16x16x32_bf16 v[196:199], v[88:91], v[72:75], v[196:199]\n\t" \
  "ds_read_b64_tr_b16 v[124:125], v55 offset:22528\n\t" \
  "v_mfma_f32_16x16x32_bf16 v[196:199], v[132:135], v[140:143], v[196:199]\n\t" \
  "ds_read_b64_tr_b16 v[126:127], v55 offset:23040\n\t" \
  "v_mfma_f32_16x16x32_bf16 v[204:207], v[132:135], v[68:71], v[204:207]\n\t" \
  "v_mfma_f32_16x16x32_bf16 v[204:207], v[88:91], v[76:79], v[204:207]\n\t" \
  "v_mfma_f32_16x16x32_bf16 v[212:215], v[88:91], v[80:83], v[212:215]\n\t" \
  "v_mfma_f32_16x16x32_bf16 v[212:215], v[132:135], v[84:87], v[212:215]\n\t" \
  "v_mfma_f32_16x16x32_bf16 v[216:219], v[136:139], v[108:111], v[216:219]\n\t" \
  "v_mfma_f32_16x16x32_bf16 v[216:219], v[144:147], v[148:151], v[216:219]\n\t" \
  "v_mfma_f32_16x16x32_bf16 v[224:227], v[72:75], v[148:151], v[224:227]\n\t" \
  "v_mfma_f32_16x16x32_bf16 v[224:227], v[140:143], v[108:111], v[224:227]\n\t" \
  "v_mfma_f32_16x16x32_bf16 v[232:235], v[68:71], v[108:111], v[232:235]\n\t" \
  "global_store_dwordx4 v2, v[42:45], %24 offset:0 sc1\n\t" \
  "v_mfma_f32_16x16x32_bf16 v[232:235], v[76:79], v[148:151], v[232:235]\n\t" \
  "s_nop 0\n\t" \
  "v_mfma_f32_16x16x32_bf16 v[42:45], v[26:29], v[18:21], 0\n\t" \
  "v_mfma_f32_16x16x32_bf16 v[42:45], v[30:33], v[22:25], v[42:45]\n\t" \
  "v_mfma_f32_16x16x32_bf16 v[46:49], v[30:33], v[18:21], 0\n\t" \
  "v_mfma_f32_16x16x32_bf16 v[46:49], v[34:37], v[22:25], v[46:49]\n\t" \
  "v_bfe_u32 v30, v4, 9, 1\n\t" \
  "v_mad_i32_i24 v30, v30, -16, v0\n\t" \
  "v_mfma_f32_16x16x32_bf16 v[240:243], v[80:83], v[148:151], v[240:243]\n\t" \
  "ds_read_b128 v[26:29], v0 offset:1584\n\t" \
  "ds_read_b128 v[30:33], v30 offset:1072\n\t" \
  "v_mfma_f32_16x16x32_bf16 v[240:243], v[84:87], v[108:111], v[240:243]\n\t" \
  "ds_read_b128 v[34:37], v0 offset:544\n\t" \
  "v_perm_b32 v18, v12, v12, v14\n\t" \
  "v_mfma_f32_16x16x32_bf16 v[248:251], v[64:67], v[108:111], v[248:251]\n\t" \
  "v_perm_b32 v19, v12, v12, v15\n\t" \
  "v_perm_b32 v20, v12, v12, v16\n\t" \
  "v_mfma_f32_16x16x32_bf16 v[248:251], v[104:107], v[148:151], v[248:251]\n\t" \
  "v_perm_b32 v21, v12, v12, v17\n\t" \
  "v_perm_b32 v22, v13, v13, v14\n\t" \
  "v_mfma_f32_16x16x32_bf16 v[252:255], v[64:67], v[112:115], v[252:255]\n\t" \
  "v_perm_b32 v23, v13, v13, v15\n\t" \
  "v_perm_b32 v24, v13, v13, v16\n\t" \
  "s_waitcnt lgkmcnt(3)\n\t" \
  "v_mfma_f32_16x16x32_bf16 v[252:255], v[104:107], v[116:119], v[252:255]\n\t" \
  "v_perm_b32 v25, v13, v13, v17\n\t" \
  "v_lshlrev_b32 v50, 16, v38\n\t" \
  "v_mfma_f32_16x16x32_bf16 v[220:223], v[144:147], v[116:119], v[220:223]\n\t" \
  "v_lshlrev_b32 v51, 16, v40\n\t" \
  "v_mul_f32 v42, v42, v50\n\t" \
  "v_mfma_f32_16x16x32_bf16 v[220:223], v[136:139], v[112:115], v[220:223]\n\t" \
  "v_mul_f32 v46, v46, v51\n\t" \
  "v_cvt_pk_bf16_f32 v42, v42, v46\n\t" \
  "s_waitcnt lgkmcnt(0)\n\t" \
  "s_barrier\n\t" \
  "v_mfma_f32_16x16x32_bf16 v[228:231], v[140:143], v[112:115], v[228:231]\n\t" \
  "ds_read_b64_tr_b16 v[128:129], v58 offset:26624\n\t" \
  "v_and_b32 v50, 0xffff0000, v38\n\t" \
  "v_mfma_f32_16x16x32_bf16 v[228:231], v[72:75], v[116:119], v[228:231]\n\t" \
  "ds_read_b64_tr_b16 v[130:131], v58 offset:27136\n\t" \
  "v_and_b32 v51, 0xffff0000, v40\n\t" \
  "v_mfma_f32_16x16x32_bf16 v[236:239], v[76:79], v[116:119], v[236:239]\n\t" \
  "ds_read_b128 v[96:99], v56 offset:10240\n\t" \
  "v_mul_f32 v43, v43, v50\n\t" \
  "v_mfma_f32_16x16x32_bf16 v[236:239], v[68:71], v[112:115], v[236:239]\n\t" \
  "ds_read_b64_tr_b16 v[88:89], v58 offset:30720\n\t" \
  "500:\n\t" \
  "s_cbranch_execz 504f\n\t" \
  "v_mul_f32 v47, v47, v51\n\t" \
  "v_mfma_f32_16x16x32_bf16 v[244:247], v[84:87], v[112:115], v[244:247]\n\t" \
  "ds_read_b64_tr_b16 v[90:91], v58 offset:31232\n\t" \
  "v_cvt_pk_bf16_f32 v43, v43, v47\n\t" \
  "v_mfma_f32_16x16x32_bf16 v[244:247], v[80:83], v[116:119], v[244:247]\n\t" \
  "ds_read_b128 v[132:135], v57 offset:10240\n\t" \
  "v_lshlrev_b32 v50, 16, v39\n\t" \
  "v_mfma_f32_16x16x32_bf16 v[152:155], v[100:103], v[136:139], v[152:155]\n\t" \
  "ds_read_b128 v[108:111], v57 offset:12288\n\t" \
  "v_lshlrev_b32 v51, 16, v41\n\t" \
  "v_mfma_f32_16x16x32_bf16 v[152:155], v[120:123], v[144:147], v[152:155]\n\t" \
  "ds_read_b128 v[148:151], v56 offset:12288\n\t" \
  "v_mul_f32 v44, v44, v50\n\t" \
  "v_mfma_f32_16x16x32_bf16 v[160:163], v[120:123], v[72:75], v[160:163]\n\t" \
  "ds_read_b128 v[64:67], v56 offset:14336\n\t" \
  "v_mul_f32 v48, v48, v51\n\t" \
  "v_mfma_f32_16x16x32_bf16 v[160:163], v[100:103], v[140:143], v[160:163]\n\t" \
  "ds_read_b128 v[104:107], v57 offset:14336\n\t" \
  "v_cvt_pk_bf16_f32 v44, v44, v48\n\t" \
  "v_mfma_f32_16x16x32_bf16 v[168:171], v[100:103], v[68:71], v[168:171]\n\t" \
  "ds_read_b128 v[112:115], v57 offset:16384\n\t" \
  "v_and_b32 v50, 0xffff0000, v39\n\t" \
  "v_mfma_f32_16x16x32_bf16 v[168:171], v[120:123], v[76:79], v[168:171]\n\t" \
  "ds_read_b128 v[116:119], v56 offset:16384\n\t" \
  "v_and_b32 v51, 0xffff0000, v41\n\t" \
  "v_mfma_f32_16x16x32_bf16 v[176:179], v[120:123], v[80:83], v[176:179]\n\t" \
  "ds_read_b64_tr_b16 v[120:121], v59 offset:26624\n\t" \
  "v_mul_f32 v45, v45, v50\n\t" \
  "v_mfma_f32_16x16x32_bf16 v[176:179], v[100:103], v[84:87], v[176:179]\n\t" \
  "ds_read_b64_tr_b16 v[122:123], v59 offset:27136\n\t" \
  "v_mul_f32 v49, v49, v51\n\t" \
  "v_mfma_f32_16x16x32_bf16 v[156:159], v[92:95], v[136:139], v[156:159]\n\t" \
  "ds_read_b64_tr_b16 v[100:101], v59 offset:30720\n\t" \
  "ds_read_b64_tr_b16 v[38:39], v1 offset:544\n\t" \
  "v_mfma_f32_16x16x32_bf16 v[156:159], v[124:127], v[144:147], v[156:159]\n\t" \
  "ds_read_b64_tr_b16 v[102:103], v59 offset:31232\n\t" \
  "ds_read_b64_tr_b16 v[40:41], v1 offset:32\n\t" \
  "v_mfma_f32_16x16x32_bf16 v[164:167], v[124:127], v[72:75], v[164:167]\n\t" \
  "ds_read_b64_tr_b16 v[136:137], v58 offset:34816\n\t" \
  "v_cvt_pk_bf16_f32 v45, v45, v49\n\t" \
  "v_mfma_f32_16x16x32_bf16 v[164:167], v[92:95], v[140:143], v[164:167]\n\t" \
  "ds_read_b64_tr_b16 v[138:139], v58 offset:35328\n\t" \
  "v_mfma_f32_16x16x32_bf16 v[172:175], v[92:95], v[68:71], v[172:175]\n\t" \
  "ds_read_b64_tr_b16 v[144:145], v58 offset:38912\n\t" \
  "v_mfma_f32_16x16x32_bf16 v[172:175], v[124:127], v[76:79], v[172:175]\n\t" \
  "ds_read_b64_tr_b16 v[146:147], v58 offset:39424\n\t" \
  "v_mfma_f32_16x16x32_bf16 v[180:183], v[124:127], v[80:83], v[180:183]\n\t" \
  "ds_read_b128 v[72:75], v56 offset:0\n\t" \
  "v_mfma_f32_16x16x32_bf16 v[180:183], v[92:95], v[84:87], v[180:183]\n\t" \
  "ds_read_b128 v[140:143], v57 offset:0\n\t" \
  "s_waitcnt lgkmcnt(8)\n\t" \
  "v_mfma_f32_16x16x32_bf16 v[184:187], v[128:131], v[96:99], v[184:187]\n\t" \
  "ds_read_b64_tr_b16 v[68:69], v59 offset:34816\n\t" \
  "v_mfma_f32_16x16x32_bf16 v[184:187], v[88:91], v[132:135], v[184:187]\n\t" \
  "ds_read_b64_tr_b16 v[70:71], v59 offset:35328\n\t" \
  "v_mfma_f32_16x16x32_bf16 v[192:195], v[88:91], v[108:111], v[192:195]\n\t" \
  "ds_read_b64_tr_b16 v[76:77], v59 offset:38912\n\t" \
  "v_mfma_f32_16x16x32_bf16 v[192:195], v[128:131], v[148:151], v[192:195]\n\t" \
  "ds_read_b64_tr_b16 v[78:79], v59 offset:39424\n\t" \
  "v_mfma_f32_16x16x32_bf16 v[200:203], v[128:131], v[64:67], v[200:203]\n\t" \
  "ds_read_b64_tr_b16 v[80:81], v58 offset:18432\n\t" \
  "v_mfma_f32_16x16x32_bf16 v[200:203], v[88:91], v[104:107], v[200:203]\n\t" \
  "ds_read_b64_tr_b16 v[82:83], v58 offset:18944\n\t" \
  "v_mfma_f32_16x16x32_bf16 v[208:211], v[88:91], v[112:115], v[208:211]\n\t" \
  "ds_read_b64_tr_b16 v[124:125], v58 offset:22528\n\t" \
  "v_mfma_f32_16x16x32_bf16 v[208:211], v[128:131], v[116:119], v[208:211]\n\t" \
  "ds_read_b64_tr_b16 v[126:127], v58 offset:23040\n\t" \
  "v_mfma_f32_16x16x32_bf16 v[188:191], v[120:123], v[96:99], v[188:191]\n\t" \
  "ds_read_b64_tr_b16 v[84:85], v59 offset:18432\n\t" \
  "s_waitcnt lgkmcnt(7)\n\t" \
  "v_mfma_f32_16x16x32_bf16 v[188:191], v[100:103], v[132:135], v[188:191]\n\t" \
  "ds_read_b64_tr_b16 v[86:87], v59 offset:18944\n\t" \
  "v_mfma_f32_16x16x32_bf16 v[196:199], v[100:103], v[108:111], v[196:199]\n\t" \
  "ds_read_b64_tr_b16 v[92:93], v59 offset:22528\n\t" \
  "v_mfma_f32_16x16x32_bf16 v[196:199], v[120:123], v[148:151], v[196:199]\n\t" \
  "ds_read_b64_tr_b16 v[94:95], v59 offset:23040\n\t" \
  "v_mfma_f32_16x16x32_bf16 v[204:207], v[120:123], v[64:67], v[204:207]\n\t" \
  "v_mfma_f32_16x16x32_bf16 v[204:207], v[100:103], v[104:107], v[204:207]\n\t" \
  "v_mfma_f32_16x16x32_bf16 v[212:215], v[100:103], v[112:115], v[212:215]\n\t" \
  "v_mfma_f32_16x16x32_bf16 v[212:215], v[120:123], v[116:119], v[212:215]\n\t" \
  "v_mfma_f32_16x16x32_bf16 v[216:219], v[96:99], v[136:139], v[216:219]\n\t" \
  "v_mfma_f32_16x16x32_bf16 v[216:219], v[132:135], v[144:147], v[216:219]\n\t" \
  "v_mfma_f32_16x16x32_bf16 v[224:227], v[108:111], v[144:147], v[224:227]\n\t" \
  "v_mfma_f32_16x16x32_bf16 v[224:227], v[148:151], v[136:139], v[224:227]\n\t" \
  "v_mfma_f32_16x16x32_bf16 v[232:235], v[64:67], v[136:139], v[232:235]\n\t" \
  "global_store_dwordx4 v2, v[42:45], %24 offset:1024 sc1\n\t" \
  "v_add_u32 v2, 0x20000, v2\n\t" \
  "v_mfma_f32_16x16x32_bf16 v[232:235], v[104:107], v[144:147], v[232:235]\n\t" \
  "v_mfma_f32_16x16x32_bf16 v[240:243], v[112:115], v[144:147], v[240:243]\n\t" \
  "v_mfma_f32_16x16x32_bf16 v[42:45], v[26:29], v[18:21], 0\n\t" \
  "v_mfma_f32_16x16x32_bf16 v[42:45], v[30:33], v[22:25], v[42:45]\n\t" \
  "v_mfma_f32_16x16x32_bf16 v[46:49], v[30:33], v[18:21], 0\n\t" \
  "v_mfma_f32_16x16x32_bf16 v[46:49], v[34:37], v[22:25], v[46:49]\n\t" \
  "ds_read_b128 v[26:29], v0 offset:1840\n\t" \
  "ds_read_b128 v[30:33], v0 offset:1328\n\t" \
  "v_mfma_f32_16x16x32_bf16 v[240:243], v[116:119], v[136:139], v[240:243]\n\t" \
  "ds_read_b128 v[34:37], v0 offset:800\n\t" \
  "v_mfma_f32_16x16x32_bf16 v[248:251], v[72:75], v[136:139], v[248:251]\n\t" \
  "v_mfma_f32_16x16x32_bf16 v[248:251], v[140:143], v[144:147], v[248:251]\n\t" \
  "v_lshlrev_b32 v50, 16, v38\n\t" \
  "v_lshlrev_b32 v51, 16, v40\n\t" \
  "v_mfma_f32_16x16x32_bf16 v[252:255], v[72:75], v[68:71], v[252:255]\n\t" \
  "v_mul_f32 v42, v42, v50\n\t" \
  "v_mul_f32 v46, v46, v51\n\t" \
  "s_waitcnt lgkmcnt(3)\n\t" \
  "v_mfma_f32_16x16x32_bf16 v[252:255], v[140:143], v[76:79], v[252:255]\n\t" \
  "v_cvt_pk_bf16_f32 v42, v42, v46\n\t" \
  "v_and_b32 v50, 0xffff0000, v38\n\t" \
  "v_mfma_f32_16x16x32_bf16 v[220:223], v[132:135], v[76:79], v[220:223]\n\t" \
  "v_and_b32 v51, 0xffff0000, v40\n\t" \
  "v_mul_f32 v43, v43, v50\n\t" \
  "v_mfma_f32_16x16x32_bf16 v[220:223], v[96:99], v[68:71], v[220:223]\n\t" \
  "v_mul_f32 v47, v47, v51\n\t" \
  "v_cvt_pk_bf16_f32 v43, v43, v47\n\t" \
  "s_waitcnt lgkmcnt(0)\n\t" \
  "s_barrier\n\t" \
  "v_mfma_f32_16x16x32_bf16 v[228:231], v[148:151], v[68:71], v[228:231]\n\t" \
  "ds_read_b64_tr_b16 v[88:89], v62 offset:26624\n\t" \
  "v_lshlrev_b32 v50, 16, v39\n\t" \
  "v_mfma_f32_16x16x32_bf16 v[228:231], v[108:111], v[76:79], v[228:231]\n\t" \
  "ds_read_b64_tr_b16 v[90:91], v62 offset:27136\n\t" \
  "v_lshlrev_b32 v51, 16, v41\n\t" \
  "v_mfma_f32_16x16x32_bf16 v[236:239], v[104:107], v[76:79], v[236:239]\n\t" \
  "ds_read_b128 v[128:131], v60 offset:10240\n\t" \
  "v_mul_f32 v44, v44, v50\n\t" \
  "v_mfma_f32_16x16x32_bf16 v[236:239], v[64:67], v[68:71], v[236:239]\n\t" \
  "ds_read_b64_tr_b16 v[100:101], v62 offset:30720\n\t" \
  "v_mul_f32 v48, v48, v51\n\t" \
  "v_mfma_f32_16x16x32_bf16 v[244:247], v[116:119], v[68:71], v[244:247]\n\t" \
  "ds_read_b64_tr_b16 v[102:103], v62 offset:31232\n\t" \
  "v_cvt_pk_bf16_f32 v44, v44, v48\n\t" \
  "v_mfma_f32_16x16x32_bf16 v[244:247], v[112:115], v[76:79], v[244:247]\n\t" \
  "ds_read_b128 v[120:123], v61 offset:10240\n\t" \
  "v_and_b32 v50, 0xffff0000, v39\n\t" \
  "v_mfma_f32_16x16x32_bf16 v[152:155], v[80:83], v[96:99], v[152:155]\n\t" \
  "ds_read_b128 v[136:139], v61 offset:12288\n\t" \
  "v_and_b32 v51, 0xffff0000, v41\n\t" \
  "v_mfma_f32_16x16x32_bf16 v[152:155], v[124:127], v[132:135], v[152:155]\n\t" \
  "ds_read_b128 v[144:147], v60 offset:12288\n\t" \
  "v_mul_f32 v45, v45, v50\n\t" \
  "v_mfma_f32_16x16x32_bf16 v[160:163], v[124:127], v[108:111], v[160:163]\n\t" \
  "ds_read_b128 v[72:75], v60 offset:14336\n\t" \
  "v_mul_f32 v49, v49, v51\n\t" \
  "v_mfma_f32_16x16x32_bf16 v[160:163], v[80:83], v[148:151], v[160:163]\n\t" \
  "ds_read_b128 v[140:143], v61 offset:14336\n\t" \
  "ds_read_b64_tr_b16 v[38:39], v1 offset:800\n\t" \
  "v_mfma_f32_16x16x32_bf16 v[168:171], v[80:83], v[64:67], v[168:171]\n\t" \
  "ds_read_b128 v[68:71], v61 offset:16384\n\t" \
  "ds_read_b64_tr_b16 v[40:41], v1 offset:288\n\t" \
  "v_mfma_f32_16x16x32_bf16 v[168:171], v[124:127], v[104:107], v[168:171]\n\t" \
  "ds_read_b128 v[76:79], v60 offset:16384\n\t" \
  "v_cvt_pk_bf16_f32 v45, v45, v49\n\t" \
  "v_mfma_f32_16x16x32_bf16 v[176:179], v[124:127], v[112:115], v[176:179]\n\t" \
  "ds_read_b64_tr_b16 v[124:125], v63 offset:26624\n\t" \
  "v_mfma_f32_16x16x32_bf16 v[176:179], v[80:83], v[116:119], v[176:179]\n\t" \
  "ds_read_b64_tr_b16 v[126:127], v63 offset:27136\n\t" \
  "v_mfma_f32_16x16x32_bf16 v[156:159], v[84:87], v[96:99], v[156:159]\n\t" \
  "ds_read_b64_tr_b16 v[80:81], v63 offset:30720\n\t" \
  "v_mfma_f32_16x16x32_bf16 v[156:159], v[92:95], v[132:135], v[156:159]\n\t" \
  "ds_read_b64_tr_b16 v[82:83], v63 offset:31232\n\t" \
  "v_mfma_f32_16x16x32_bf16 v[164:167], v[92:95], v[108:111], v[164:167]\n\t" \
  "ds_read_b64_tr_b16 v[96:97], v62 offset:34816\n\t" \
  "v_mfma_f32_16x16x32_bf16 v[164:167], v[84:87], v[148:151], v[164:167]\n\t" \
  "ds_read_b64_tr_b16 v[98:99], v62 offset:35328\n\t" \
  "v_mfma_f32_16x16x32_bf16 v[172:175], v[84:87], v[64:67], v[172:175]\n\t" \
  "ds_read_b64_tr_b16 v[132:133], v62 offset:38912\n\t" \
  "v_mfma_f32_16x16x32_bf16 v[172:175], v[92:95], v[104:107], v[172:175]\n\t" \
  "ds_read_b64_tr_b16 v[134:135], v62 offset:39424\n\t" \
  "v_mfma_f32_16x16x32_bf16 v[180:183], v[92:95], v[112:115], v[180:183]\n\t" \
  "ds_read_b128 v[108:111], v60 offset:0\n\t" \
  "v_mfma_f32_16x16x32_bf16 v[180:183], v[84:87], v[116:119], v[180:183]\n\t" \
  "ds_read_b128 v[148:151], v61 offset:0\n\t" \
  "s_waitcnt lgkmcnt(7)\n\t" \
  "v_mfma_f32_16x16x32_bf16 v[184:187], v[88:91], v[128:131], v[184:187]\n\t" \
  "ds_read_b64_tr_b16 v[64:65], v63 offset:34816\n\t" \
  "v_mfma_f32_16x16x32_bf16 v[184:187], v[100:103], v[120:123], v[184:187]\n\t" \
  "ds_read_b64_tr_b16 v[66:67], v63 offset:35328\n\t" \
  "v_mfma_f32_16x16x32_bf16 v[192:195], v[100:103], v[136:139], v[192:195]\n\t" \
  "ds_read_b64_tr_b16 v[104:105], v63 offset:38912\n\t" \
  "v_mfma_f32_16x16x32_bf16 v[192:195], v[88:91], v[144:147], v[192:195]\n\t" \
  "ds_read_b64_tr_b16 v[106:107], v63 offset:39424\n\t" \
  "v_mfma_f32_16x16x32_bf16 v[200:203], v[88:91], v[72:75], v[200:203]\n\t" \
  "ds_read_b64_tr_b16 v[92:93], v62 offset:18432\n\t" \
  "v_mfma_f32_16x16x32_bf16 v[200:203], v[100:103], v[140:143], v[200:203]\n\t" \
  "ds_read_b64_tr_b16 v[94:95], v62 offset:18944\n\t" \
  "v_mfma_f32_16x16x32_bf16 v[208:211], v[100:103], v[68:71], v[208:211]\n\t" \
  "ds_read_b64_tr_b16 v[112:113], v62 offset:22528\n\t" \
  "v_mfma_f32_16x16x32_bf16 v[208:211], v[88:91], v[76:79], v[208:211]\n\t" \
  "ds_read_b64_tr_b16 v[114:115], v62 offset:23040\n\t" \
  "v_mfma_f32_16x16x32_bf16 v[188:191], v[124:127], v[128:131], v[188:191]\n\t" \
  "ds_read_b64_tr_b16 v[84:85], v63 offset:18432\n\t" \
  "s_waitcnt lgkmcnt(7)\n\t" \
  "v_mfma_f32_16x16x32_bf16 v[188:191], v[80:83], v[120:123], v[188:191]\n\t" \
  "ds_read_b64_tr_b16 v[86:87], v63 offset:18944\n\t" \
  "v_mfma_f32_16x16x32_bf16 v[196:199], v[80:83], v[136:139], v[196:199]\n\t" \
  "ds_read_b64_tr_b16 v[116:117], v63 offset:22528\n\t" \
  "v_mfma_f32_16x16x32_bf16 v[196:199], v[124:127], v[144:147], v[196:199]\n\t" \
  "ds_read_b64_tr_b16 v[118:119], v63 offset:23040\n\t" \
  "v_mfma_f32_16x16x32_bf16 v[204:207], v[124:127], v[72:75], v[204:207]\n\t" \
  "v_mfma_f32_16x16x32_bf16 v[204:207], v[80:83], v[140:143], v[204:207]\n\t" \
  "v_mfma_f32_16x16x32_bf16 v[212:215], v[80:83], v[68:71], v[212:215]\n\t" \
  "v_mfma_f32_16x16x32_bf16 v[212:215], v[124:127], v[76:79], v[212:215]\n\t" \
  "v_mfma_f32_16x16x32_bf16 v[216:219], v[128:131], v[96:99], v[216:219]\n\t" \
  "v_mfma_f32_16x16x32_bf16 v[216:219], v[120:123], v[132:135], v[216:219]\n\t" \
  "v_mfma_f32_16x16x32_bf16 v[224:227], v[136:139], v[132:135], v[224:227]\n\t" \
  "v_mfma_f32_16x16x32_bf16 v[224:227], v[144:147], v[96:99], v[224:227]\n\t" \
  "v_mfma_f32_16x16x32_bf16 v[232:235], v[72:75], v[96:99], v[232:235]\n\t" \
  "global_store_dwordx4 v2, v[42:45], %24 offset:0 sc1\n\t" \
  "v_mfma_f32_16x16x32_bf16 v[232:235], v[140:143], v[132:135], v[232:235]\n\t" \
  "s_nop 0\n\t" \
  "v_mfma_f32_16x16x32_bf16 v[42:45], v[26:29], v[18:21], 0\n\t" \
  "v_mfma_f32_16x16x32_bf16 v[42:45], v[30:33], v[22:25], v[42:45]\n\t" \
  "v_mfma_f32_16x16x32_bf16 v[46:49], v[30:33], v[18:21], 0\n\t" \
  "v_mfma_f32_16x16x32_bf16 v[46:49], v[34:37], v[22:25], v[46:49]\n\t" \
  "v_mfma_f32_16x16x32_bf16 v[240:243], v[68:71], v[132:135], v[240:243]\n\t" \
  "v_mfma_f32_16x16x32_bf16 v[240:243], v[76:79], v[96:99], v[240:243]\n\t" \
  "v_lshlrev_b32 v50, 16, v38\n\t" \
  "v_lshlrev_b32 v51, 16, v40\n\t" \
  "v_mfma_f32_16x16x32_bf16 v[248:251], v[108:111], v[96:99], v[248:251]\n\t" \
  "s_nop 0\n\t" \
  "v_mul_f32 v42, v42, v50\n\t" \
  "s_nop 0\n\t" \
  "v_mul_f32 v46, v46, v51\n\t" \
  "v_mfma_f32_16x16x32_bf16 v[248:251], v[148:151], v[132:135], v[248:251]\n\t" \
  "v_cvt_pk_bf16_f32 v42, v42, v46\n\t" \
  "v_and_b32 v50, 0xffff0000, v38\n\t" \
  "v_mfma_f32_16x16x32_bf16 v[252:255], v[108:111], v[64:67], v[252:255]\n\t" \
  "v_and_b32 v51, 0xffff0000, v40\n\t" \
  "v_mul_f32 v43, v43, v50\n\t" \
  "s_waitcnt lgkmcnt(0)\n\t" \
  "v_mfma_f32_16x16x32_bf16 v[252:255], v[148:151], v[104:107], v[252:255]\n\t" \
  "v_mul_f32 v47, v47, v51\n\t" \
  "v_cvt_pk_bf16_f32 v43, v43, v47\n\t" \
  "v_mfma_f32_16x16x32_bf16 v[220:223], v[120:123], v[104:107], v[220:223]\n\t" \
  "v_lshlrev_b32 v50, 16, v39\n\t" \
  "v_lshlrev_b32 v51, 16, v41\n\t" \
  "v_mfma_f32_16x16x32_bf16 v[220:223], v[128:131], v[64:67], v[220:223]\n\t" \
  "v_mul_f32 v44, v44, v50\n\t" \
  "v_mul_f32 v48, v48, v51\n\t" \
  "s_barrier\n\t" \
  "v_mfma_f32_16x16x32_bf16 v[228:231], v[144:147], v[64:67], v[228:231]\n\t" \
  "ds_read_b64_tr_b16 v[100:101], v54 offset:26624\n\t" \
  "v_cvt_pk_bf16_f32 v44, v44, v48\n\t" \
  "v_mfma_f32_16x16x32_bf16 v[228:231], v[136:139], v[104:107], v[228:231]\n\t" \
  "ds_read_b64_tr_b16 v[102:103], v54 offset:27136\n\t" \
  "v_and_b32 v50, 0xffff0000, v39\n\t" \
  "v_mfma_f32_16x16x32_bf16 v[236:239], v[140:143], v[104:107], v[236:239]\n\t" \
  "ds_read_b128 v[88:91], v52 offset:10240\n\t" \
  "v_and_b32 v51, 0xffff0000, v41\n\t" \
  "v_mfma_f32_16x16x32_bf16 v[236:239], v[72:75], v[64:67], v[236:239]\n\t" \
  "ds_read_b64_tr_b16 v[80:81], v54 offset:30720\n\t" \
  "v_mul_f32 v45, v45, v50\n\t" \
  "v_mfma_f32_16x16x32_bf16 v[244:247], v[76:79], v[64:67], v[244:247]\n\t" \
  "ds_read_b64_tr_b16 v[82:83], v54 offset:31232\n\t" \
  "v_mul_f32 v49, v49, v51\n\t" \
  "v_mfma_f32_16x16x32_bf16 v[244:247], v[68:71], v[104:107], v[244:247]\n\t" \
  "ds_read_b128 v[124:127], v53 offset:10240\n\t" \
  "v_cvt_pk_bf16_f32 v45, v45, v49\n\t" \
  "v_mfma_f32_16x16x32_bf16 v[152:155], v[92:95], v[128:131], v[152:155]\n\t" \
  "ds_read_b128 v[96:99], v53 offset:12288\n\t" \
  "v_mfma_f32_16x16x32_bf16 v[152:155], v[112:115], v[120:123], v[152:155]\n\t" \
  "ds_read_b128 v[132:135], v52 offset:12288\n\t" \
  "v_mfma_f32_16x16x32_bf16 v[160:163], v[112:115], v[136:139], v[160:163]\n\t" \
  "ds_read_b128 v[108:111], v52 offset:14336\n\t" \
  "v_mfma_f32_16x16x32_bf16 v[160:163], v[92:95], v[144:147], v[160:163]\n\t" \
  "ds_read_b128 v[148:151], v53 offset:14336\n\t" \
  "v_mfma_f32_16x16x32_bf16 v[168:171], v[92:95], v[72:75], v[168:171]\n\t" \
  "ds_read_b128 v[64:67], v53 offset:16384\n\t" \
  "v_mfma_f32_16x16x32_bf16 v[168:171], v[112:115], v[140:143], v[168:171]\n\t" \
  "ds_read_b128 v[104:107], v52 offset:16384\n\t" \
  "v_mfma_f32_16x16x32_bf16 v[176:179], v[112:115], v[68:71], v[176:179]\n\t" \
  "ds_read_b64_tr_b16 v[112:113], v55 offset:26624\n\t" \
  "v_mfma_f32_16x16x32_bf16 v[176:179], v[92:95], v[76:79], v[176:179]\n\t" \
  "ds_read_b64_tr_b16 v[114:115], v55 offset:27136\n\t" \
  "v_mfma_f32_16x16x32_bf16 v[156:159], v[84:87], v[128:131], v[156:159]\n\t" \
  "ds_read_b64_tr_b16 v[92:93], v55 offset:30720\n\t" \
  "v_mfma_f32_16x16x32_bf16 v[156:159], v[116:119], v[120:123], v[156:159]\n\t" \
  "ds_read_b64_tr_b16 v[94:95], v55 offset:31232\n\t" \
  "v_mfma_f32_16x16x32_bf16 v[164:167], v[116:119], v[136:139], v[164:167]\n\t" \
  "ds_read_b64_tr_b16 v[128:129], v54 offset:34816\n\t" \
  "v_mfma_f32_16x16x32_bf16 v[164:167], v[84:87], v[144:147], v[164:167]\n\t" \
  "ds_read_b64_tr_b16 v[130:131], v54 offset:35328\n\t" \
  "v_mfma_f32_16x16x32_bf16 v[172:175], v[84:87], v[72:75], v[172:175]\n\t" \
  "ds_read_b64_tr_b16 v[120:121], v54 offset:38912\n\t" \
  "v_mfma_f32_16x16x32_bf16 v[172:175], v[116:119], v[140:143], v[172:175]\n\t" \
  "ds_read_b64_tr_b16 v[122:123], v54 offset:39424\n\t" \
  "v_mfma_f32_16x16x32_bf16 v[180:183], v[116:119], v[68:71], v[180:183]\n\t" \
  "ds_read_b128 v[136:139], v52 offset:0\n\t" \
  "v_mfma_f32_16x16x32_bf16 v[180:183], v[84:87], v[76:79], v[180:183]\n\t" \
  "ds_read_b128 v[144:147], v53 offset:0\n\t" \
  "s_waitcnt lgkmcnt(7)\n\t" \
  "v_mfma_f32_16x16x32_bf16 v[184:187], v[100:103], v[88:91], v[184:187]\n\t" \
  "ds_read_b64_tr_b16 v[72:73], v55 offset:34816\n\t" \
  "v_mfma_f32_16x16x32_bf16 v[184:187], v[80:83], v[124:127], v[184:187]\n\t" \
  "ds_read_b64_tr_b16 v[74:75], v55 offset:35328\n\t" \
  "v_mfma_f32_16x16x32_bf16 v[192:195], v[80:83], v[96:99], v[192:195]\n\t" \
  "ds_read_b64_tr_b16 v[140:141], v55 offset:38912\n\t" \
  "v_mfma_f32_16x16x32_bf16 v[192:195], v[100:103], v[132:135], v[192:195]\n\t" \
  "ds_read_b64_tr_b16 v[142:143], v55 offset:39424\n\t" \
  "v_mfma_f32_16x16x32_bf16 v[200:203], v[100:103], v[108:111], v[200:203]\n\t" \
  "ds_read_b64_tr_b16 v[68:69], v54 offset:18432\n\t" \
  "v_mfma_f32_16x16x32_bf16 v[200:203], v[80:83], v[148:151], v[200:203]\n\t" \
  "ds_read_b64_tr_b16 v[70:71], v54 offset:18944\n\t" \
  "v_mfma_f32_16x16x32_bf16 v[208:211], v[80:83], v[64:67], v[208:211]\n\t" \
  "ds_read_b64_tr_b16 v[116:117], v54 offset:22528\n\t" \
  "v_mfma_f32_16x16x32_bf16 v[208:211], v[100:103], v[104:107], v[208:211]\n\t" \
  "ds_read_b64_tr_b16 v[118:119], v54 offset:23040\n\t" \
  "v_mfma_f32_16x16x32_bf16 v[188:191], v[112:115], v[88:91], v[188:191]\n\t" \
  "ds_read_b64_tr_b16 v[76:77], v55 offset:18432\n\t" \
  "s_waitcnt lgkmcnt(7)\n\t" \
  "v_mfma_f32_16x16x32_bf16 v[188:191], v[92:95], v[124:127], v[188:191]\n\t" \
  "ds_read_b64_tr_b16 v[78:79], v55 offset:18944\n\t" \
  "v_mfma_f32_16x16x32_bf16 v[196:199], v[92:95], v[96:99], v[196:199]\n\t" \
  "ds_read_b64_tr_b16 v[84:85], v55 offset:22528\n\t" \
  "v_mfma_f32_16x16x32_bf16 v[196:199], v[112:115], v[132:135], v[196:199]\n\t" \
  "ds_read_b64_tr_b16 v[86:87], v55 offset:23040\n\t" \
  "v_mfma_f32_16x16x32_bf16 v[204:207], v[112:115], v[108:111], v[204:207]\n\t" \
  "v_mfma_f32_16x16x32_bf16 v[204:207], v[92:95], v[148:151], v[204:207]\n\t" \
  "v_mfma_f32_16x16x32_bf16 v[212:215], v[92:95], v[64:67], v[212:215]\n\t" \
  "v_mfma_f32_16x16x32_bf16 v[212:215], v[112:115], v[104:107], v[212:215]\n\t" \
  "v_mfma_f32_16x16x32_bf16 v[216:219], v[88:91], v[128:131], v[216:219]\n\t" \
  "v_mfma_f32_16x16x32_bf16 v[216:219], v[124:127], v[120:123], v[216:219]\n\t" \
  "v_mfma_f32_16x16x32_bf16 v[224:227], v[96:99], v[120:123], v[224:227]\n\t" \
  "v_mfma_f32_16x16x32_bf16 v[224:227], v[132:135], v[128:131], v[224:227]\n\t" \
  "v_mfma_f32_16x16x32_bf16 v[232:235], v[108:111], v[128:131], v[232:235]\n\t" \
  "global_store_dwordx4 v2, v[42:45], %24 offset:1024 sc1\n\t" \
  "v_mfma_f32_16x16x32_bf16 v[232:235], v[148:151], v[120:123], v[232:235]\n\t" \
  "v_mfma_f32_16x16x32_bf16 v[240:243], v[64:67], v[120:123], v[240:243]\n\t" \
  "v_mfma_f32_16x16x32_bf16 v[240:243], v[104:107], v[128:131], v[240:243]\n\t" \
  "v_mov_b32 v30, 0xff61b1e6\n\t" \
  "v_bfe_i32 v34, v3, 0, 1\n\t" \
  "v_mfma_f32_16x16x32_bf16 v[248:251], v[136:139], v[128:131], v[248:251]\n\t" \
  "v_and_b32 v34, v30, v34\n\t" \
  "v_bfe_i32 v38, v3, 4, 1\n\t" \
  "v_mfma_f32_16x16x32_bf16 v[248:251], v[144:147], v[120:123], v[248:251]\n\t" \
  "v_and_b32 v38, v30, v38\n\t" \
  "v_bfe_i32 v42, v3, 8, 1\n\t" \
  "v_mfma_f32_16x16x32_bf16 v[252:255], v[136:139], v[72:75], v[252:255]\n\t" \
  "v_bfi_b32 v42, v42, v30, %15\n\t" \
  "v_mov_b32 v48, 0x3f803f80\n\t" \
  "s_waitcnt lgkmcnt(0)\n\t" \
  "v_mfma_f32_16x16x32_bf16 v[252:255], v[144:147], v[140:143], v[252:255]\n\t" \
  "v_bfe_i32 v35, v3, 1, 1\n\t" \
  "v_and_b32 v35, v30, v35\n\t" \
  "v_mfma_f32_16x16x32_bf16 v[220:223], v[124:127], v[140:143], v[220:223]\n\t" \
  "v_bfe_i32 v39, v3, 5, 1\n\t" \
  "v_and_b32 v39, v30, v39\n\t" \
  "v_mfma_f32_16x16x32_bf16 v[220:223], v[88:91], v[72:75], v[220:223]\n\t" \
  "v_bfe_i32 v43, v3, 9, 1\n\t" \
  "v_bfi_b32 v43, v43, v30, %15\n\t" \
  "s_barrier\n\t" \
  "v_mfma_f32_16x16x32_bf16 v[228:231], v[132:135], v[72:75], v[228:231]\n\t" \
  "ds_read_b64_tr_b16 v[80:81], v58 offset:26624\n\t" \
  "v_mov_b32 v49, 0x3f803f80\n\t" \
  "v_mfma_f32_16x16x32_bf16 v[228:231], v[96:99], v[140:143], v[228:231]\n\t" \
  "ds_read_b64_tr_b16 v[82:83], v58 offset:27136\n\t" \
  "v_bfe_i32 v36, v3, 2, 1\n\t" \
  "v_mfma_f32_16x16x32_bf16 v[236:239], v[148:151], v[140:143], v[236:239]\n\t" \
  "ds_read_b128 v[100:103], v56 offset:10240\n\t" \
  "v_and_b32 v36, v30, v36\n\t" \
  "v_mfma_f32_16x16x32_bf16 v[236:239], v[108:111], v[72:75], v[236:239]\n\t" \
  "ds_read_b64_tr_b16 v[92:93], v58 offset:30720\n\t" \
  "v_bfe_i32 v40, v3, 6, 1\n\t" \
  "v_mfma_f32_16x16x32_bf16 v[244:247], v[104:107], v[72:75], v[244:247]\n\t" \
  "ds_read_b64_tr_b16 v[94:95], v58 offset:31232\n\t" \
  "v_and_b32 v40, v30, v40\n\t" \
  "v_mfma_f32_16x16x32_bf16 v[244:247], v[64:67], v[140:143], v[244:247]\n\t" \
  "ds_read_b128 v[112:115], v57 offset:10240\n\t" \
  "v_bfe_i32 v44, v3, 10, 1\n\t" \
  "v_mfma_f32_16x16x32_bf16 v[152:155], v[68:71], v[88:91], v[152:155]\n\t" \
  "ds_read_b128 v[128:131], v57 offset:12288\n\t" \
  "v_bfi_b32 v44, v44, v30, %15\n\t" \
  "v_mfma_f32_16x16x32_bf16 v[152:155], v[116:119], v[124:127], v[152:155]\n\t" \
  "ds_read_b128 v[120:123], v56 offset:12288\n\t" \
  "v_mov_b32 v50, 0x3f803f80\n\t" \
  "v_mfma_f32_16x16x32_bf16 v[160:163], v[116:119], v[96:99], v[160:163]\n\t" \
  "ds_read_b128 v[136:139], v56 offset:14336\n\t" \
  "v_bfe_i32 v37, v3, 3, 1\n\t" \
  "v_mfma_f32_16x16x32_bf16 v[160:163], v[68:71], v[132:135], v[160:163]\n\t" \
  "ds_read_b128 v[144:147], v57 offset:14336\n\t" \
  "v_and_b32 v37, v30, v37\n\t" \
  "v_mfma_f32_16x16x32_bf16 v[168:171], v[68:71], v[108:111], v[168:171]\n\t" \
  "ds_read_b128 v[72:75], v57 offset:16384\n\t" \
  "v_bfe_i32 v41, v3, 7, 1\n\t" \
  "v_mfma_f32_16x16x32_bf16 v[168:171], v[116:119], v[148:151], v[168:171]\n\t" \
  "ds_read_b128 v[140:143], v56 offset:16384\n\t" \
  "v_and_b32 v41, v30, v41\n\t" \
  "v_mfma_f32_16x16x32_bf16 v[176:179], v[116:119], v[64:67], v[176:179]\n\t" \
  "ds_read_b64_tr_b16 v[116:117], v59 offset:26624\n\t" \
  "v_bfe_i32 v45, v3, 11, 1\n\t" \
  "v_mfma_f32_16x16x32_bf16 v[176:179], v[68:71], v[104:107], v[176:179]\n\t" \
  "ds_read_b64_tr_b16 v[118:119], v59 offset:27136\n\t" \
  "v_bfi_b32 v45, v45, v30, %15\n\t" \
  "v_mfma_f32_16x16x32_bf16 v[156:159], v[76:79], v[88:91], v[156:159]\n\t" \
  "ds_read_b64_tr_b16 v[68:69], v59 offset:30720\n\t" \
  "v_mov_b32 v51, 0x3f803f80\n\t" \
  "v_mfma_f32_16x16x32_bf16 v[156:159], v[84:87], v[124:127], v[156:159]\n\t" \
  "ds_read_b64_tr_b16 v[70:71], v59 offset:31232\n\t" \
  "v_bfe_i32 v46, v3, 12, 1\n\t" \
  "v_mfma_f32_16x16x32_bf16 v[164:167], v[84:87], v[96:99], v[164:167]\n\t" \
  "ds_read_b64_tr_b16 v[88:89], v58 offset:34816\n\t" \
  "v_bfe_i32 v31, v3, 13, 1\n\t" \
  "v_mfma_f32_16x16x32_bf16 v[164:167], v[76:79], v[132:135], v[164:167]\n\t" \
  "ds_read_b64_tr_b16 v[90:91], v58 offset:35328\n\t" \
  "v_perm_b32 v46, v31, v46, vcc_hi\n\t" \
  "v_mfma_f32_16x16x32_bf16 v[172:175], v[76:79], v[108:111], v[172:175]\n\t" \
  "ds_read_b64_tr_b16 v[124:125], v58 offset:38912\n\t" \
  "v_bfe_i32 v47, v3, 14, 1\n\t" \
  "v_mfma_f32_16x16x32_bf16 v[172:175], v[84:87], v[148:151], v[172:175]\n\t" \
  "ds_read_b64_tr_b16 v[126:127], v58 offset:39424\n\t" \
  "v_bfe_i32 v31, v3, 15, 1\n\t" \
  "v_mfma_f32_16x16x32_bf16 v[180:183], v[84:87], v[64:67], v[180:183]\n\t" \
  "ds_read_b128 v[96:99], v56 offset:0\n\t" \
  "v_perm_b32 v47, v31, v47, vcc_hi\n\t" \
  "v_mfma_f32_16x16x32_bf16 v[180:183], v[76:79], v[104:107], v[180:183]\n\t" \
  "ds_read_b128 v[132:135], v57 offset:0\n\t" \
  "v_add_u32 v30, 18432, v4\n\t" \
  "s_waitcnt lgkmcnt(7)\n\t" \
  "v_mfma_f32_16x16x32_bf16 v[184:187], v[80:83], v[100:103], v[184:187]\n\t" \
  "ds_read_b64_tr_b16 v[108:109], v59 offset:34816\n\t" \
  "v_xor_b32 v31, 1024, v30\n\t" \
  "v_mfma_f32_16x16x32_bf16 v[184:187], v[92:95], v[112:115], v[184:187]\n\t" \
  "ds_read_b64_tr_b16 v[110:111], v59 offset:35328\n\t" \
  "v_mov_b32 v32, %12\n\t" \
  "v_mfma_f32_16x16x32_bf16 v[192:195], v[92:95], v[128:131], v[192:195]\n\t" \
  "ds_read_b64_tr_b16 v[148:149], v59 offset:38912\n\t" \
  "v_xor_b32 v33, 4, v32\n\t" \
  "v_mfma_f32_16x16x32_bf16 v[192:195], v[80:83], v[120:123], v[192:195]\n\t" \
  "ds_read_b64_tr_b16 v[150:151], v59 offset:39424\n\t" \
  "v_mov_b32 v0, 1\n\t" \
  "v_mfma_f32_16x16x32_bf16 v[200:203], v[80:83], v[136:139], v[200:203]\n\t" \
  "ds_read_b64_tr_b16 v[64:65], v58 offset:18432\n\t" \
  "v_and_b32 v4, 1023, v4\n\t" \
  "v_mfma_f32_16x16x32_bf16 v[200:203], v[92:95], v[144:147], v[200:203]\n\t" \
  "ds_read_b64_tr_b16 v[66:67], v58 offset:18944\n\t" \
  "v_add_u32 v1, 16384, v4\n\t" \
  "v_mfma_f32_16x16x32_bf16 v[208:211], v[92:95], v[72:75], v[208:211]\n\t" \
  "ds_read_b64_tr_b16 v[84:85], v58 offset:22528\n\t" \
  "v_mfma_f32_16x16x32_bf16 v[208:211], v[80:83], v[140:143], v[208:211]\n\t" \
  "ds_read_b64_tr_b16 v[86:87], v58 offset:23040\n\t" \
  "v_mfma_f32_16x16x32_bf16 v[188:191], v[116:119], v[100:103], v[188:191]\n\t" \
  "ds_read_b64_tr_b16 v[76:77], v59 offset:18432\n\t" \
  "s_waitcnt lgkmcnt(7)\n\t" \
  "v_mfma_f32_16x16x32_bf16 v[188:191], v[68:71], v[112:115], v[188:191]\n\t" \
  "ds_read_b64_tr_b16 v[78:79], v59 offset:18944\n\t" \
  "v_mfma_f32_16x16x32_bf16 v[196:199], v[68:71], v[128:131], v[196:199]\n\t" \
  "ds_read_b64_tr_b16 v[104:105], v59 offset:22528\n\t" \
  "v_mfma_f32_16x16x32_bf16 v[196:199], v[116:119], v[120:123], v[196:199]\n\t" \
  "ds_read_b64_tr_b16 v[106:107], v59 offset:23040\n\t" \
  "v_mfma_f32_16x16x32_bf16 v[204:207], v[116:119], v[136:139], v[204:207]\n\t" \
  "v_mfma_f32_16x16x32_bf16 v[204:207], v[68:71], v[144:147], v[204:207]\n\t" \
  "v_mfma_f32_16x16x32_bf16 v[212:215], v[68:71], v[72:75], v[212:215]\n\t" \
  "v_mfma_f32_16x16x32_bf16 v[212:215], v[116:119], v[140:143], v[212:215]\n\t" \
  "v_mfma_f32_16x16x32_bf16 v[216:219], v[100:103], v[88:91], v[216:219]\n\t" \
  "v_mfma_f32_16x16x32_bf16 v[216:219], v[112:115], v[124:127], v[216:219]\n\t" \
  "v_mfma_f32_16x16x32_bf16 v[224:227], v[128:131], v[124:127], v[224:227]\n\t" \
  "v_mfma_f32_16x16x32_bf16 v[224:227], v[120:123], v[88:91], v[224:227]\n\t" \
  "v_mfma_f32_16x16x32_bf16 v[232:235], v[136:139], v[88:91], v[232:235]\n\t" \
  "v_mfma_f32_16x16x32_bf16 v[232:235], v[144:147], v[124:127], v[232:235]\n\t" \
  "v_mfma_f32_16x16x32_bf16 v[240:243], v[72:75], v[124:127], v[240:243]\n\t" \
  "v_mfma_f32_16x16x32_bf16 v[240:243], v[140:143], v[88:91], v[240:243]\n\t" \
  "v_mfma_f32_16x16x32_bf16 v[248:251], v[96:99], v[88:91], v[248:251]\n\t" \
  "v_mfma_f32_16x16x32_bf16 v[248:251], v[132:135], v[124:127], v[248:251]\n\t" \
  "v_mfma_f32_16x16x32_bf16 v[252:255], v[96:99], v[108:111], v[252:255]\n\t" \
  "s_waitcnt lgkmcnt(0)\n\t" \
  "v_mfma_f32_16x16x32_bf16 v[252:255], v[132:135], v[148:151], v[252:255]\n\t" \
  "v_mfma_f32_16x16x32_bf16 v[220:223], v[112:115], v[148:151], v[220:223]\n\t" \
  "v_mfma_f32_16x16x32_bf16 v[220:223], v[100:103], v[108:111], v[220:223]\n\t" \
  "s_barrier\n\t" \
  "v_mfma_f32_16x16x32_bf16 v[228:231], v[120:123], v[108:111], v[228:231]\n\t" \
  "ds_read_b64_tr_b16 v[92:93], v62 offset:26624\n\t" \
  "v_mfma_f32_16x16x32_bf16 v[228:231], v[128:131], v[148:151], v[228:231]\n\t" \
  "ds_read_b64_tr_b16 v[94:95], v62 offset:27136\n\t" \
  "v_mfma_f32_16x16x32_bf16 v[236:239], v[144:147], v[148:151], v[236:239]\n\t" \
  "ds_read_b128 v[80:83], v60 offset:10240\n\t" \
  "v_mfma_f32_16x16x32_bf16 v[236:239], v[136:139], v[108:111], v[236:239]\n\t" \
  "ds_read_b64_tr_b16 v[68:69], v62 offset:30720\n\t" \
  "v_mfma_f32_16x16x32_bf16 v[244:247], v[140:143], v[108:111], v[244:247]\n\t" \
  "ds_read_b64_tr_b16 v[70:71], v62 offset:31232\n\t" \
  "v_mfma_f32_16x16x32_bf16 v[244:247], v[72:75], v[148:151], v[244:247]\n\t" \
  "ds_read_b128 v[116:119], v61 offset:10240\n\t" \
  "v_mfma_f32_16x16x32_bf16 v[152:155], v[64:67], v[100:103], v[152:155]\n\t" \
  "ds_read_b128 v[88:91], v61 offset:12288\n\t" \
  "v_mfma_f32_16x16x32_bf16 v[152:155], v[84:87], v[112:115], v[152:155]\n\t" \
  "ds_read_b128 v[124:127], v60 offset:12288\n\t" \
  "v_mfma_f32_16x16x32_bf16 v[160:163], v[84:87], v[128:131], v[160:163]\n\t" \
  "ds_read_b128 v[96:99], v60 offset:14336\n\t" \
  "v_mfma_f32_16x16x32_bf16 v[160:163], v[64:67], v[120:123], v[160:163]\n\t" \
  "ds_read_b128 v[132:135], v61 offset:14336\n\t" \
  "v_mfma_f32_16x16x32_bf16 v[168:171], v[64:67], v[136:139], v[168:171]\n\t" \
  "ds_read_b128 v[108:111], v61 offset:16384\n\t" \
  "v_mfma_f32_16x16x32_bf16 v[168:171], v[84:87], v[144:147], v[168:171]\n\t" \
  "ds_read_b128 v[148:151], v60 offset:16384\n\t" \
  "v_mfma_f32_16x16x32_bf16 v[176:179], v[84:87], v[72:75], v[176:179]\n\t" \
  "ds_read_b64_tr_b16 v[84:85], v63 offset:26624\n\t" \
  "v_mfma_f32_16x16x32_bf16 v[176:179], v[64:67], v[140:143], v[176:179]\n\t" \
  "ds_read_b64_tr_b16 v[86:87], v63 offset:27136\n\t" \
  "v_mfma_f32_16x16x32_bf16 v[156:159], v[76:79], v[100:103], v[156:159]\n\t" \
  "ds_read_b64_tr_b16 v[64:65], v63 offset:30720\n\t" \
  "v_mfma_f32_16x16x32_bf16 v[156:159], v[104:107], v[112:115], v[156:159]\n\t" \
  "ds_read_b64_tr_b16 v[66:67], v63 offset:31232\n\t" \
  "v_mfma_f32_16x16x32_bf16 v[164:167], v[104:107], v[128:131], v[164:167]\n\t" \
  "ds_read_b64_tr_b16 v[100:101], v62 offset:34816\n\t" \
  "v_mfma_f32_16x16x32_bf16 v[164:167], v[76:79], v[120:123], v[164:167]\n\t" \
  "ds_read_b64_tr_b16 v[102:103], v62 offset:35328\n\t" \
  "v_mfma_f32_16x16x32_bf16 v[172:175], v[76:79], v[136:139], v[172:175]\n\t" \
  "ds_read_b64_tr_b16 v[112:113], v62 offset:38912\n\t" \
  "v_mfma_f32_16x16x32_bf16 v[172:175], v[104:107], v[144:147], v[172:175]\n\t" \
  "ds_read_b64_tr_b16 v[114:115], v62 offset:39424\n\t" \
  "v_mfma_f32_16x16x32_bf16 v[180:183], v[104:107], v[72:75], v[180:183]\n\t" \
  "ds_read_b128 v[128:131], v60 offset:0\n\t" \
  "v_mfma_f32_16x16x32_bf16 v[180:183], v[76:79], v[140:143], v[180:183]\n\t" \
  "ds_read_b128 v[120:123], v61 offset:0\n\t" \
  "s_waitcnt lgkmcnt(7)\n\t" \
  "v_mfma_f32_16x16x32_bf16 v[184:187], v[92:95], v[80:83], v[184:187]\n\t" \
  "ds_read_b64_tr_b16 v[136:137], v63 offset:34816\n\t" \
  "v_mfma_f32_16x16x32_bf16 v[184:187], v[68:71], v[116:119], v[184:187]\n\t" \
  "ds_read_b64_tr_b16 v[138:139], v63 offset:35328\n\t" \
  "v_mfma_f32_16x16x32_bf16 v[192:195], v[68:71], v[88:91], v[192:195]\n\t" \
  "ds_read_b64_tr_b16 v[144:145], v63 offset:38912\n\t" \
  "v_mfma_f32_16x16x32_bf16 v[192:195], v[92:95], v[124:127], v[192:195]\n\t" \
  "ds_read_b64_tr_b16 v[146:147], v63 offset:39424\n\t" \
  "v_mfma_f32_16x16x32_bf16 v[200:203], v[92:95], v[96:99], v[200:203]\n\t" \
  "ds_read_b64_tr_b16 v[72:73], v62 offset:18432\n\t" \
  "v_mfma_f32_16x16x32_bf16 v[200:203], v[68:71], v[132:135], v[200:203]\n\t" \
  "ds_read_b64_tr_b16 v[74:75], v62 offset:18944\n\t" \
  "v_mfma_f32_16x16x32_bf16 v[208:211], v[68:71], v[108:111], v[208:211]\n\t" \
  "ds_read_b64_tr_b16 v[104:105], v62 offset:22528\n\t" \
  "v_mfma_f32_16x16x32_bf16 v[208:211], v[92:95], v[148:151], v[208:211]\n\t" \
  "ds_read_b64_tr_b16 v[106:107], v62 offset:23040\n\t" \
  "v_mfma_f32_16x16x32_bf16 v[188:191], v[84:87], v[80:83], v[188:191]\n\t" \
  "ds_read_b64_tr_b16 v[76:77], v63 offset:18432\n\t" \
  "s_waitcnt lgkmcnt(7)\n\t" \
  "v_mfma_f32_16x16x32_bf16 v[188:191], v[64:67], v[116:119], v[188:191]\n\t" \
  "ds_read_b64_tr_b16 v[78:79], v63 offset:18944\n\t" \
  "v_mfma_f32_16x16x32_bf16 v[196:199], v[64:67], v[88:91], v[196:199]\n\t" \
  "ds_read_b64_tr_b16 v[140:141], v63 offset:22528\n\t" \
  "v_mfma_f32_16x16x32_bf16 v[196:199], v[84:87], v[124:127], v[196:199]\n\t" \
  "ds_read_b64_tr_b16 v[142:143], v63 offset:23040\n\t" \
  "v_mfma_f32_16x16x32_bf16 v[204:207], v[84:87], v[96:99], v[204:207]\n\t" \
  "v_mfma_f32_16x16x32_bf16 v[204:207], v[64:67], v[132:135], v[204:207]\n\t" \
  "v_mfma_f32_16x16x32_bf16 v[212:215], v[64:67], v[108:111], v[212:215]\n\t" \
  "v_mfma_f32_16x16x32_bf16 v[212:215], v[84:87], v[148:151], v[212:215]\n\t" \
  "v_mfma_f32_16x16x32_bf16 v[216:219], v[80:83], v[100:103], v[216:219]\n\t" \
  "501:\n\t" \
  "s_cbranch_execz 505f\n\t" \
  "v_mfma_f32_16x16x32_bf16 v[216:219], v[116:119], v[112:115], v[216:219]\n\t" \
  "v_mfma_f32_16x16x32_bf16 v[224:227], v[88:91], v[112:115], v[224:227]\n\t" \
  "v_mfma_f32_16x16x32_bf16 v[224:227], v[124:127], v[100:103], v[224:227]\n\t" \
  "v_mfma_f32_16x16x32_bf16 v[232:235], v[96:99], v[100:103], v[232:235]\n\t" \
  "v_mfma_f32_16x16x32_bf16 v[232:235], v[132:135], v[112:115], v[232:235]\n\t" \
  "v_mfma_f32_16x16x32_bf16 v[240:243], v[108:111], v[112:115], v[240:243]\n\t" \
  "v_mfma_f32_16x16x32_bf16 v[240:243], v[148:151], v[100:103], v[240:243]\n\t" \
  "v_mfma_f32_16x16x32_bf16 v[248:251], v[128:131], v[100:103], v[248:251]\n\t" \
  "v_mfma_f32_16x16x32_bf16 v[248:251], v[120:123], v[112:115], v[248:251]\n\t" \
  "v_mfma_f32_16x16x32_bf16 v[252:255], v[128:131], v[136:139], v[252:255]\n\t" \
  "s_waitcnt lgkmcnt(0)\n\t" \
  "v_mfma_f32_16x16x32_bf16 v[252:255], v[120:123], v[144:147], v[252:255]\n\t" \
  "v_mfma_f32_16x16x32_bf16 v[220:223], v[116:119], v[144:147], v[220:223]\n\t" \
  "v_mfma_f32_16x16x32_bf16 v[220:223], v[80:83], v[136:139], v[220:223]\n\t" \
  "s_waitcnt vmcnt(0)\n\t" \
  "s_barrier\n\t" \
  "v_mfma_f32_16x16x32_bf16 v[228:231], v[124:127], v[136:139], v[228:231]\n\t" \
  "ds_read_b64_tr_b16 v[68:69], v54 offset:26624\n\t" \
  "v_mfma_f32_16x16x32_bf16 v[228:231], v[88:91], v[144:147], v[228:231]\n\t" \
  "ds_read_b64_tr_b16 v[70:71], v54 offset:27136\n\t" \
  "v_mfma_f32_16x16x32_bf16 v[236:239], v[132:135], v[144:147], v[236:239]\n\t" \
  "ds_read_b128 v[92:95], v52 offset:10240\n\t" \
  "v_mfma_f32_16x16x32_bf16 v[236:239], v[96:99], v[136:139], v[236:239]\n\t" \
  "ds_read_b64_tr_b16 v[64:65], v54 offset:30720\n\t" \
  "v_mfma_f32_16x16x32_bf16 v[244:247], v[148:151], v[136:139], v[244:247]\n\t" \
  "ds_read_b64_tr_b16 v[66:67], v54 offset:31232\n\t" \
  "v_mfma_f32_16x16x32_bf16 v[244:247], v[108:111], v[144:147], v[244:247]\n\t" \
  "ds_read_b128 v[84:87], v53 offset:10240\n\t" \
  "v_mfma_f32_16x16x32_bf16 v[152:155], v[72:75], v[80:83], v[152:155]\n\t" \
  "ds_read_b128 v[100:103], v53 offset:12288\n\t" \
  "v_mfma_f32_16x16x32_bf16 v[152:155], v[104:107], v[116:119], v[152:155]\n\t" \
  "ds_read_b128 v[112:115], v52 offset:12288\n\t" \
  "v_mfma_f32_16x16x32_bf16 v[160:163], v[104:107], v[88:91], v[160:163]\n\t" \
  "ds_read_b128 v[128:131], v52 offset:14336\n\t" \
  "v_mfma_f32_16x16x32_bf16 v[160:163], v[72:75], v[124:127], v[160:163]\n\t" \
  "ds_read_b128 v[120:123], v53 offset:14336\n\t" \
  "v_mfma_f32_16x16x32_bf16 v[168:171], v[72:75], v[96:99], v[168:171]\n\t" \
  "ds_read_b128 v[136:139], v53 offset:16384\n\t" \
  "v_mfma_f32_16x16x32_bf16 v[168:171], v[104:107], v[132:135], v[168:171]\n\t" \
  "ds_read_b128 v[144:147], v52 offset:16384\n\t" \
  "v_mfma_f32_16x16x32_bf16 v[176:179], v[104:107], v[108:111], v[176:179]\n\t" \
  "ds_read_b64_tr_b16 v[104:105], v55 offset:26624\n\t" \
  "v_mfma_f32_16x16x32_bf16 v[176:179], v[72:75], v[148:151], v[176:179]\n\t" \
  "ds_read_b64_tr_b16 v[106:107], v55 offset:27136\n\t" \
  "v_mfma_f32_16x16x32_bf16 v[156:159], v[76:79], v[80:83], v[156:159]\n\t" \
  "ds_read_b64_tr_b16 v[72:73], v55 offset:30720\n\t" \
  "v_mfma_f32_16x16x32_bf16 v[156:159], v[140:143], v[116:119], v[156:159]\n\t" \
  "ds_read_b64_tr_b16 v[74:75], v55 offset:31232\n\t" \
  "v_mfma_f32_16x16x32_bf16 v[164:167], v[140:143], v[88:91], v[164:167]\n\t" \
  "ds_read_b64_tr_b16 v[80:81], v54 offset:34816\n\t" \
  "v_mfma_f32_16x16x32_bf16 v[164:167], v[76:79], v[124:127], v[164:167]\n\t" \
  "ds_read_b64_tr_b16 v[82:83], v54 offset:35328\n\t" \
  "v_mfma_f32_16x16x32_bf16 v[172:175], v[76:79], v[96:99], v[172:175]\n\t" \
  "ds_read_b64_tr_b16 v[116:117], v54 offset:38912\n\t" \
  "v_mfma_f32_16x16x32_bf16 v[172:175], v[140:143], v[132:135], v[172:175]\n\t" \
  "ds_read_b64_tr_b16 v[118:119], v54 offset:39424\n\t" \
  "v_mfma_f32_16x16x32_bf16 v[180:183], v[140:143], v[108:111], v[180:183]\n\t" \
  "ds_read_b128 v[88:91], v52 offset:0\n\t" \
  "v_mfma_f32_16x16x32_bf16 v[180:183], v[76:79], v[148:151], v[180:183]\n\t" \
  "ds_read_b128 v[124:127], v53 offset:0\n\t" \
  "s_waitcnt lgkmcnt(7)\n\t" \
  "v_mfma_f32_16x16x32_bf16 v[184:187], v[68:71], v[92:95], v[184:187]\n\t" \
  "ds_read_b64_tr_b16 v[96:97], v55 offset:34816\n\t" \
  "v_mfma_f32_16x16x32_bf16 v[184:187], v[64:67], v[84:87], v[184:187]\n\t" \
  "ds_read_b64_tr_b16 v[98:99], v55 offset:35328\n\t" \
  "v_mfma_f32_16x16x32_bf16 v[192:195], v[64:67], v[100:103], v[192:195]\n\t" \
  "ds_read_b64_tr_b16 v[132:133], v55 offset:38912\n\t" \
  "v_mfma_f32_16x16x32_bf16 v[192:195], v[68:71], v[112:115], v[192:195]\n\t" \
  "ds_read_b64_tr_b16 v[134:135], v55 offset:39424\n\t" \
  "v_mfma_f32_16x16x32_bf16 v[200:203], v[68:71], v[128:131], v[200:203]\n\t" \
  "ds_read_b64_tr_b16 v[108:109], v54 offset:18432\n\t" \
  "v_mfma_f32_16x16x32_bf16 v[200:203], v[64:67], v[120:123], v[200:203]\n\t" \
  "ds_read_b64_tr_b16 v[110:111], v54 offset:18944\n\t" \
  "v_mfma_f32_16x16x32_bf16 v[208:211], v[64:67], v[136:139], v[208:211]\n\t" \
  "ds_read_b64_tr_b16 v[140:141], v54 offset:22528\n\t" \
  "v_mfma_f32_16x16x32_bf16 v[208:211], v[68:71], v[144:147], v[208:211]\n\t" \
  "ds_read_b64_tr_b16 v[142:143], v54 offset:23040\n\t" \
  "v_mfma_f32_16x16x32_bf16 v[188:191], v[104:107], v[92:95], v[188:191]\n\t" \
  "ds_read_b64_tr_b16 v[76:77], v55 offset:18432\n\t" \
  "s_waitcnt lgkmcnt(7)\n\t" \
  "v_mfma_f32_16x16x32_bf16 v[188:191], v[72:75], v[84:87], v[188:191]\n\t" \
  "ds_read_b64_tr_b16 v[78:79], v55 offset:18944\n\t" \
  "v_mfma_f32_16x16x32_bf16 v[196:199], v[72:75], v[100:103], v[196:199]\n\t" \
  "ds_read_b64_tr_b16 v[148:149], v55 offset:22528\n\t" \
  "v_mfma_f32_16x16x32_bf16 v[196:199], v[104:107], v[112:115], v[196:199]\n\t" \
  "ds_read_b64_tr_b16 v[150:151], v55 offset:23040\n\t" \
  "v_mfma_f32_16x16x32_bf16 v[204:207], v[104:107], v[128:131], v[204:207]\n\t" \
  "v_mfma_f32_16x16x32_bf16 v[204:207], v[72:75], v[120:123], v[204:207]\n\t" \
  "v_mfma_f32_16x16x32_bf16 v[212:215], v[72:75], v[136:139], v[212:215]\n\t" \
  "v_mfma_f32_16x16x32_bf16 v[212:215], v[104:107], v[144:147], v[212:215]\n\t" \
  "v_mfma_f32_16x16x32_bf16 v[216:219], v[92:95], v[80:83], v[216:219]\n\t" \
  "v_mfma_f32_16x16x32_bf16 v[216:219], v[84:87], v[116:119], v[216:219]\n\t" \
  "v_mfma_f32_16x16x32_bf16 v[224:227], v[100:103], v[116:119], v[224:227]\n\t" \
  "v_mfma_f32_16x16x32_bf16 v[224:227], v[112:115], v[80:83], v[224:227]\n\t" \
  "v_mfma_f32_16x16x32_bf16 v[232:235], v[128:131], v[80:83], v[232:235]\n\t" \
  "v_mfma_f32_16x16x32_bf16 v[232:235], v[120:123], v[116:119], v[232:235]\n\t" \
  "v_mfma_f32_16x16x32_bf16 v[240:243], v[136:139], v[116:119], v[240:243]\n\t" \
  "v_mfma_f32_16x16x32_bf16 v[240:243], v[144:147], v[80:83], v[240:243]\n\t" \
  "v_mfma_f32_16x16x32_bf16 v[248:251], v[88:91], v[80:83], v[248:251]\n\t" \
  "v_mfma_f32_16x16x32_bf16 v[248:251], v[124:127], v[116:119], v[248:251]\n\t" \
  "v_mfma_f32_16x16x32_bf16 v[252:255], v[88:91], v[96:99], v[252:255]\n\t" \
  "s_waitcnt lgkmcnt(0)\n\t" \
  "v_mfma_f32_16x16x32_bf16 v[252:255], v[124:127], v[132:135], v[252:255]\n\t" \
  "v_mfma_f32_16x16x32_bf16 v[220:223], v[84:87], v[132:135], v[220:223]\n\t" \
  "v_mfma_f32_16x16x32_bf16 v[220:223], v[92:95], v[96:99], v[220:223]\n\t" \
  "s_barrier\n\t" \
  "v_mfma_f32_16x16x32_bf16 v[228:231], v[112:115], v[96:99], v[228:231]\n\t" \
  "ds_read_b64_tr_b16 v[64:65], v58 offset:26624\n\t" \
  "v_mfma_f32_16x16x32_bf16 v[228:231], v[100:103], v[132:135], v[228:231]\n\t" \
  "ds_read_b64_tr_b16 v[66:67], v58 offset:27136\n\t" \
  "v_mfma_f32_16x16x32_bf16 v[236:239], v[120:123], v[132:135], v[236:239]\n\t" \
  "ds_read_b128 v[68:71], v56 offset:10240\n\t" \
  "v_mfma_f32_16x16x32_bf16 v[236:239], v[128:131], v[96:99], v[236:239]\n\t" \
  "ds_read_b64_tr_b16 v[72:73], v58 offset:30720\n\t" \
  "v_mfma_f32_16x16x32_bf16 v[244:247], v[144:147], v[96:99], v[244:247]\n\t" \
  "ds_read_b64_tr_b16 v[74:75], v58 offset:31232\n\t" \
  "v_mfma_f32_16x16x32_bf16 v[244:247], v[136:139], v[132:135], v[244:247]\n\t" \
  "ds_read_b128 v[104:107], v57 offset:10240\n\t" \
  "v_mfma_f32_16x16x32_bf16 v[152:155], v[108:111], v[92:95], v[152:155]\n\t" \
  "ds_read_b128 v[80:83], v57 offset:12288\n\t" \
  "v_mfma_f32_16x16x32_bf16 v[152:155], v[140:143], v[84:87], v[152:155]\n\t" \
  "ds_read_b128 v[116:119], v56 offset:12288\n\t" \
  "v_mfma_f32_16x16x32_bf16 v[160:163], v[140:143], v[100:103], v[160:163]\n\t" \
  "ds_read_b128 v[88:91], v56 offset:14336\n\t" \
  "v_mfma_f32_16x16x32_bf16 v[160:163], v[108:111], v[112:115], v[160:163]\n\t" \
  "ds_read_b128 v[124:127], v57 offset:14336\n\t" \
  "v_mfma_f32_16x16x32_bf16 v[168:171], v[108:111], v[128:131], v[168:171]\n\t" \
  "ds_read_b128 v[96:99], v57 offset:16384\n\t" \
  "v_mfma_f32_16x16x32_bf16 v[168:171], v[140:143], v[120:123], v[168:171]\n\t" \
  "ds_read_b128 v[132:135], v56 offset:16384\n\t" \
  "v_mfma_f32_16x16x32_bf16 v[176:179], v[140:143], v[136:139], v[176:179]\n\t" \
  "ds_read_b64_tr_b16 v[140:141], v59 offset:26624\n\t" \
  "v_mfma_f32_16x16x32_bf16 v[176:179], v[108:111], v[144:147], v[176:179]\n\t" \
  "ds_read_b64_tr_b16 v[142:143], v59 offset:27136\n\t" \
  "v_mfma_f32_16x16x32_bf16 v[156:159], v[76:79], v[92:95], v[156:159]\n\t" \
  "ds_read_b64_tr_b16 v[108:109], v59 offset:30720\n\t" \
  "v_mfma_f32_16x16x32_bf16 v[156:159], v[148:151], v[84:87], v[156:159]\n\t" \
  "ds_read_b64_tr_b16 v[110:111], v59 offset:31232\n\t" \
  "v_mfma_f32_16x16x32_bf16 v[164:167], v[148:151], v[100:103], v[164:167]\n\t" \
  "ds_read_b64_tr_b16 v[92:93], v58 offset:34816\n\t" \
  "v_mfma_f32_16x16x32_bf16 v[164:167], v[76:79], v[112:115], v[164:167]\n\t" \
  "ds_read_b64_tr_b16 v[94:95], v58 offset:35328\n\t" \
  "v_mfma_f32_16x16x32_bf16 v[172:175], v[76:79], v[128:131], v[172:175]\n\t" \
  "ds_read_b64_tr_b16 v[84:85], v58 offset:38912\n\t" \
  "v_mfma_f32_16x16x32_bf16 v[172:175], v[148:151], v[120:123], v[172:175]\n\t" \
  "ds_read_b64_tr_b16 v[86:87], v58 offset:39424\n\t" \
  "v_mfma_f32_16x16x32_bf16 v[180:183], v[148:151], v[136:139], v[180:183]\n\t" \
  "ds_read_b128 v[100:103], v56 offset:0\n\t" \
  "v_mfma_f32_16x16x32_bf16 v[180:183], v[76:79], v[144:147], v[180:183]\n\t" \
  "ds_read_b128 v[112:115], v57 offset:0\n\t" \
  "s_waitcnt lgkmcnt(7)\n\t" \
  "v_mfma_f32_16x16x32_bf16 v[184:187], v[64:67], v[68:71], v[184:187]\n\t" \
  "ds_read_b64_tr_b16 v[128:129], v59 offset:34816\n\t" \
  "v_mfma_f32_16x16x32_bf16 v[184:187], v[72:75], v[104:107], v[184:187]\n\t" \
  "ds_read_b64_tr_b16 v[130:131], v59 offset:35328\n\t" \
  "v_mfma_f32_16x16x32_bf16 v[192:195], v[72:75], v[80:83], v[192:195]\n\t" \
  "ds_read_b64_tr_b16 v[120:121], v59 offset:38912\n\t" \
  "v_mfma_f32_16x16x32_bf16 v[192:195], v[64:67], v[116:119], v[192:195]\n\t" \
  "ds_read_b64_tr_b16 v[122:123], v59 offset:39424\n\t" \
  "v_mfma_f32_16x16x32_bf16 v[200:203], v[64:67], v[88:91], v[200:203]\n\t" \
  "ds_read_b64_tr_b16 v[136:137], v58 offset:18432\n\t" \
  "v_mfma_f32_16x16x32_bf16 v[200:203], v[72:75], v[124:127], v[200:203]\n\t" \
  "ds_read_b64_tr_b16 v[138:139], v58 offset:18944\n\t" \
  "v_mfma_f32_16x16x32_bf16 v[208:211], v[72:75], v[96:99], v[208:211]\n\t" \
  "ds_read_b64_tr_b16 v[148:149], v58 offset:22528\n\t" \
  "v_mfma_f32_16x16x32_bf16 v[208:211], v[64:67], v[132:135], v[208:211]\n\t" \
  "ds_read_b64_tr_b16 v[150:151], v58 offset:23040\n\t" \
  "v_mfma_f32_16x16x32_bf16 v[188:191], v[140:143], v[68:71], v[188:191]\n\t" \
  "ds_read_b64_tr_b16 v[76:77], v59 offset:18432\n\t" \
  "s_waitcnt lgkmcnt(7)\n\t" \
  "v_mfma_f32_16x16x32_bf16 v[188:191], v[108:111], v[104:107], v[188:191]\n\t" \
  "ds_read_b64_tr_b16 v[78:79], v59 offset:18944\n\t" \
  "v_mfma_f32_16x16x32_bf16 v[196:199], v[108:111], v[80:83], v[196:199]\n\t" \
  "ds_read_b64_tr_b16 v[144:145], v59 offset:22528\n\t" \
  "v_mfma_f32_16x16x32_bf16 v[196:199], v[140:143], v[116:119], v[196:199]\n\t" \
  "ds_read_b64_tr_b16 v[146:147], v59 offset:23040\n\t" \
  "v_mfma_f32_16x16x32_bf16 v[204:207], v[140:143], v[88:91], v[204:207]\n\t" \
  "v_mfma_f32_16x16x32_bf16 v[204:207], v[108:111], v[124:127], v[204:207]\n\t" \
  "v_mfma_f32_16x16x32_bf16 v[212:215], v[108:111], v[96:99], v[212:215]\n\t" \
  "v_mfma_f32_16x16x32_bf16 v[212:215], v[140:143], v[132:135], v[212:215]\n\t" \
  "v_mfma_f32_16x16x32_bf16 v[216:219], v[68:71], v[92:95], v[216:219]\n\t" \
  "v_mfma_f32_16x16x32_bf16 v[216:219], v[104:107], v[84:87], v[216:219]\n\t" \
  "v_mfma_f32_16x16x32_bf16 v[224:227], v[80:83], v[84:87], v[224:227]\n\t" \
  "v_mfma_f32_16x16x32_bf16 v[224:227], v[116:119], v[92:95], v[224:227]\n\t" \
  "v_mfma_f32_16x16x32_bf16 v[232:235], v[88:91], v[92:95], v[232:235]\n\t" \
  "v_mfma_f32_16x16x32_bf16 v[232:235], v[124:127], v[84:87], v[232:235]\n\t" \
  "v_mfma_f32_16x16x32_bf16 v[240:243], v[96:99], v[84:87], v[240:243]\n\t" \
  "v_mfma_f32_16x16x32_bf16 v[240:243], v[132:135], v[92:95], v[240:243]\n\t" \
  "v_mfma_f32_16x16x32_bf16 v[248:251], v[100:103], v[92:95], v[248:251]\n\t" \
  "v_mfma_f32_16x16x32_bf16 v[248:251], v[112:115], v[84:87], v[248:251]\n\t" \
  "v_mfma_f32_16x16x32_bf16 v[252:255], v[100:103], v[128:131], v[252:255]\n\t" \
  "s_waitcnt lgkmcnt(0)\n\t" \
  "v_mfma_f32_16x16x32_bf16 v[252:255], v[112:115], v[120:123], v[252:255]\n\t" \
  "v_mfma_f32_16x16x32_bf16 v[220:223], v[104:107], v[120:123], v[220:223]\n\t" \
  "v_mfma_f32_16x16x32_bf16 v[220:223], v[68:71], v[128:131], v[220:223]\n\t" \
  "s_barrier\n\t" \
  "v_mfma_f32_16x16x32_bf16 v[228:231], v[116:119], v[128:131], v[228:231]\n\t" \
  "ds_read_b64_tr_b16 v[72:73], v62 offset:26624\n\t" \
  "v_mfma_f32_16x16x32_bf16 v[228:231], v[80:83], v[120:123], v[228:231]\n\t" \
  "ds_read_b64_tr_b16 v[74:75], v62 offset:27136\n\t" \
  "v_mfma_f32_16x16x32_bf16 v[236:239], v[124:127], v[120:123], v[236:239]\n\t" \
  "ds_read_b128 v[64:67], v60 offset:10240\n\t" \
  "v_mfma_f32_16x16x32_bf16 v[236:239], v[88:91], v[128:131], v[236:239]\n\t" \
  "ds_read_b64_tr_b16 v[108:109], v62 offset:30720\n\t" \
  "v_mfma_f32_16x16x32_bf16 v[244:247], v[132:135], v[128:131], v[244:247]\n\t" \
  "ds_read_b64_tr_b16 v[110:111], v62 offset:31232\n\t" \
  "v_mfma_f32_16x16x32_bf16 v[244:247], v[96:99], v[120:123], v[244:247]\n\t" \
  "ds_read_b128 v[140:143], v61 offset:10240\n\t" \
  "v_mfma_f32_16x16x32_bf16 v[152:155], v[136:139], v[68:71], v[152:155]\n\t" \
  "ds_read_b128 v[92:95], v61 offset:12288\n\t" \
  "v_mfma_f32_16x16x32_bf16 v[152:155], v[148:151], v[104:107], v[152:155]\n\t" \
  "ds_read_b128 v[84:87], v60 offset:12288\n\t" \
  "v_mfma_f32_16x16x32_bf16 v[160:163], v[148:151], v[80:83], v[160:163]\n\t" \
  "ds_read_b128 v[100:103], v60 offset:14336\n\t" \
  "v_mfma_f32_16x16x32_bf16 v[160:163], v[136:139], v[116:119], v[160:163]\n\t" \
  "ds_read_b128 v[112:115], v61 offset:14336\n\t" \
  "v_mfma_f32_16x16x32_bf16 v[168:171], v[136:139], v[88:91], v[168:171]\n\t" \
  "ds_read_b128 v[128:131], v61 offset:16384\n\t" \
  "v_mfma_f32_16x16x32_bf16 v[168:171], v[148:151], v[124:127], v[168:171]\n\t" \
  "ds_read_b128 v[120:123], v60 offset:16384\n\t" \
  "v_mfma_f32_16x16x32_bf16 v[176:179], v[148:151], v[96:99], v[176:179]\n\t" \
  "ds_read_b64_tr_b16 v[148:149], v63 offset:26624\n\t" \
  "v_mfma_f32_16x16x32_bf16 v[176:179], v[136:139], v[132:135], v[176:179]\n\t" \
  "ds_read_b64_tr_b16 v[150:151], v63 offset:27136\n\t" \
  "v_mfma_f32_16x16x32_bf16 v[156:159], v[76:79], v[68:71], v[156:159]\n\t" \
  "ds_read_b64_tr_b16 v[136:137], v63 offset:30720\n\t" \
  "v_mfma_f32_16x16x32_bf16 v[156:159], v[144:147], v[104:107], v[156:159]\n\t" \
  "ds_read_b64_tr_b16 v[138:139], v63 offset:31232\n\t" \
  "v_mfma_f32_16x16x32_bf16 v[164:167], v[144:147], v[80:83], v[164:167]\n\t" \
  "ds_read_b64_tr_b16 v[68:69], v62 offset:34816\n\t" \
  "v_mfma_f32_16x16x32_bf16 v[164:167], v[76:79], v[116:119], v[164:167]\n\t" \
  "ds_read_b64_tr_b16 v[70:71], v62 offset:35328\n\t" \
  "v_mfma_f32_16x16x32_bf16 v[172:175], v[76:79], v[88:91], v[172:175]\n\t" \
  "ds_read_b64_tr_b16 v[104:105], v62 offset:38912\n\t" \
  "v_mfma_f32_16x16x32_bf16 v[172:175], v[144:147], v[124:127], v[172:175]\n\t" \
  "ds_read_b64_tr_b16 v[106:107], v62 offset:39424\n\t" \
  "v_mfma_f32_16x16x32_bf16 v[180:183], v[144:147], v[96:99], v[180:183]\n\t" \
  "ds_read_b128 v[80:83], v60 offset:0\n\t" \
  "v_mfma_f32_16x16x32_bf16 v[180:183], v[76:79], v[132:135], v[180:183]\n\t" \
  "ds_read_b128 v[116:119], v61 offset:0\n\t" \
  "s_waitcnt lgkmcnt(7)\n\t" \
  "v_mfma_f32_16x16x32_bf16 v[184:187], v[72:75], v[64:67], v[184:187]\n\t" \
  "ds_read_b64_tr_b16 v[88:89], v63 offset:34816\n\t" \
  "v_mfma_f32_16x16x32_bf16 v[184:187], v[108:111], v[140:143], v[184:187]\n\t" \
  "ds_read_b64_tr_b16 v[90:91], v63 offset:35328\n\t" \
  "v_mfma_f32_16x16x32_bf16 v[192:195], v[108:111], v[92:95], v[192:195]\n\t" \
  "ds_read_b64_tr_b16 v[124:125], v63 offset:38912\n\t" \
  "v_mfma_f32_16x16x32_bf16 v[192:195], v[72:75], v[84:87], v[192:195]\n\t" \
  "ds_read_b64_tr_b16 v[126:127], v63 offset:39424\n\t" \
  "v_mfma_f32_16x16x32_bf16 v[200:203], v[72:75], v[100:103], v[200:203]\n\t" \
  "ds_read_b64_tr_b16 v[96:97], v62 offset:18432\n\t" \
  "v_mfma_f32_16x16x32_bf16 v[200:203], v[108:111], v[112:115], v[200:203]\n\t" \
  "ds_read_b64_tr_b16 v[98:99], v62 offset:18944\n\t" \
  "v_mfma_f32_16x16x32_bf16 v[208:211], v[108:111], v[128:131], v[208:211]\n\t" \
  "ds_read_b64_tr_b16 v[144:145], v62 offset:22528\n\t" \
  "v_mfma_f32_16x16x32_bf16 v[208:211], v[72:75], v[120:123], v[208:211]\n\t" \
  "ds_read_b64_tr_b16 v[146:147], v62 offset:23040\n\t" \
  "v_mfma_f32_16x16x32_bf16 v[188:191], v[148:151], v[64:67], v[188:191]\n\t" \
  "ds_read_b64_tr_b16 v[76:77], v63 offset:18432\n\t" \
  "s_waitcnt lgkmcnt(7)\n\t" \
  "v_mfma_f32_16x16x32_bf16 v[188:191], v[136:139], v[140:143], v[188:191]\n\t" \
  "ds_read_b64_tr_b16 v[78:79], v63 offset:18944\n\t" \
  "v_mfma_f32_16x16x32_bf16 v[196:199], v[136:139], v[92:95], v[196:199]\n\t" \
  "ds_read_b64_tr_b16 v[132:133], v63 offset:22528\n\t" \
  "v_mfma_f32_16x16x32_bf16 v[196:199], v[148:151], v[84:87], v[196:199]\n\t" \
  "ds_read_b64_tr_b16 v[134:135], v63 offset:23040\n\t" \
  "v_mfma_f32_16x16x32_bf16 v[204:207], v[148:151], v[100:103], v[204:207]\n\t" \
  "v_mfma_f32_16x16x32_bf16 v[204:207], v[136:139], v[112:115], v[204:207]\n\t" \
  "v_mfma_f32_16x16x32_bf16 v[212:215], v[136:139], v[128:131], v[212:215]\n\t" \
  "v_mfma_f32_16x16x32_bf16 v[212:215], v[148:151], v[120:123], v[212:215]\n\t" \
  "v_mfma_f32_16x16x32_bf16 v[216:219], v[64:67], v[68:71], v[216:219]\n\t" \
  "v_mfma_f32_16x16x32_bf16 v[216:219], v[140:143], v[104:107], v[216:219]\n\t" \
  "v_mfma_f32_16x16x32_bf16 v[224:227], v[92:95], v[104:107], v[224:227]\n\t" \
  "v_mfma_f32_16x16x32_bf16 v[224:227], v[84:87], v[68:71], v[224:227]\n\t" \
  "v_mfma_f32_16x16x32_bf16 v[232:235], v[100:103], v[68:71], v[232:235]\n\t" \
  "v_mfma_f32_16x16x32_bf16 v[232:235], v[112:115], v[104:107], v[232:235]\n\t" \
  "v_mfma_f32_16x16x32_bf16 v[240:243], v[128:131], v[104:107], v[240:243]\n\t" \
  "v_mfma_f32_16x16x32_bf16 v[240:243], v[120:123], v[68:71], v[240:243]\n\t" \
  "v_mfma_f32_16x16x32_bf16 v[248:251], v[80:83], v[68:71], v[248:251]\n\t" \
  "v_mfma_f32_16x16x32_bf16 v[248:251], v[116:119], v[104:107], v[248:251]\n\t" \
  "v_mfma_f32_16x16x32_bf16 v[252:255], v[80:83], v[88:91], v[252:255]\n\t" \
  "s_waitcnt lgkmcnt(0)\n\t" \
  "v_mfma_f32_16x16x32_bf16 v[252:255], v[116:119], v[124:127], v[252:255]\n\t" \
  "v_mfma_f32_16x16x32_bf16 v[220:223], v[140:143], v[124:127], v[220:223]\n\t" \
  "v_mfma_f32_16x16x32_bf16 v[220:223], v[64:67], v[88:91], v[220:223]\n\t" \
  "s_barrier\n\t" \
  "v_mfma_f32_16x16x32_bf16 v[228:231], v[84:87], v[88:91], v[228:231]\n\t" \
  "ds_read_b64_tr_b16 v[108:109], v54 offset:26624\n\t" \
  "v_mfma_f32_16x16x32_bf16 v[228:231], v[92:95], v[124:127], v[228:231]\n\t" \
  "ds_read_b64_tr_b16 v[110:111], v54 offset:27136\n\t" \
  "v_mfma_f32_16x16x32_bf16 v[236:239], v[112:115], v[124:127], v[236:239]\n\t" \
  "ds_read_b128 v[72:75], v52 offset:10240\n\t" \
  "v_mfma_f32_16x16x32_bf16 v[236:239], v[100:103], v[88:91], v[236:239]\n\t" \
  "ds_read_b64_tr_b16 v[136:137], v54 offset:30720\n\t" \
  "v_mfma_f32_16x16x32_bf16 v[244:247], v[120:123], v[88:91], v[244:247]\n\t" \
  "ds_read_b64_tr_b16 v[138:139], v54 offset:31232\n\t" \
  "v_mfma_f32_16x16x32_bf16 v[244:247], v[128:131], v[124:127], v[244:247]\n\t" \
  "ds_read_b128 v[148:151], v53 offset:10240\n\t" \
  "v_mfma_f32_16x16x32_bf16 v[152:155], v[96:99], v[64:67], v[152:155]\n\t" \
  "ds_read_b128 v[68:71], v53 offset:12288\n\t" \
  "v_mfma_f32_16x16x32_bf16 v[152:155], v[144:147], v[140:143], v[152:155]\n\t" \
  "ds_read_b128 v[104:107], v52 offset:12288\n\t" \
  "v_mfma_f32_16x16x32_bf16 v[160:163], v[144:147], v[92:95], v[160:163]\n\t" \
  "ds_read_b128 v[80:83], v52 offset:14336\n\t" \
  "v_mfma_f32_16x16x32_bf16 v[160:163], v[96:99], v[84:87], v[160:163]\n\t" \
  "ds_read_b128 v[116:119], v53 offset:14336\n\t" \
  "v_mfma_f32_16x16x32_bf16 v[168:171], v[96:99], v[100:103], v[168:171]\n\t" \
  "ds_read_b128 v[88:91], v53 offset:16384\n\t" \
  "v_mfma_f32_16x16x32_bf16 v[168:171], v[144:147], v[112:115], v[168:171]\n\t" \
  "ds_read_b128 v[124:127], v52 offset:16384\n\t" \
  "v_mfma_f32_16x16x32_bf16 v[176:179], v[144:147], v[128:131], v[176:179]\n\t" \
  "ds_read_b64_tr_b16 v[144:145], v55 offset:26624\n\t" \
  "v_mfma_f32_16x16x32_bf16 v[176:179], v[96:99], v[120:123], v[176:179]\n\t" \
  "ds_read_b64_tr_b16 v[146:147], v55 offset:27136\n\t" \
  "v_mfma_f32_16x16x32_bf16 v[156:159], v[76:79], v[64:67], v[156:159]\n\t" \
  "ds_read_b64_tr_b16 v[96:97], v55 offset:30720\n\t" \
  "v_mfma_f32_16x16x32_bf16 v[156:159], v[132:135], v[140:143], v[156:159]\n\t" \
  "ds_read_b64_tr_b16 v[98:99], v55 offset:31232\n\t" \
  "v_mfma_f32_16x16x32_bf16 v[164:167], v[132:135], v[92:95], v[164:167]\n\t" \
  "ds_read_b64_tr_b16 v[64:65], v54 offset:34816\n\t" \
  "v_mfma_f32_16x16x32_bf16 v[164:167], v[76:79], v[84:87], v[164:167]\n\t" \
  "ds_read_b64_tr_b16 v[66:67], v54 offset:35328\n\t" \
  "v_mfma_f32_16x16x32_bf16 v[172:175], v[76:79], v[100:103], v[172:175]\n\t" \
  "ds_read_b64_tr_b16 v[140:141], v54 offset:38912\n\t" \
  "v_mfma_f32_16x16x32_bf16 v[172:175], v[132:135], v[112:115], v[172:175]\n\t" \
  "ds_read_b64_tr_b16 v[142:143], v54 offset:39424\n\t" \
  "v_mfma_f32_16x16x32_bf16 v[180:183], v[132:135], v[128:131], v[180:183]\n\t" \
  "ds_read_b128 v[92:95], v52 offset:0\n\t" \
  "v_mfma_f32_16x16x32_bf16 v[180:183], v[76:79], v[120:123], v[180:183]\n\t" \
  "ds_read_b128 v[84:87], v53 offset:0\n\t" \
  "s_waitcnt lgkmcnt(7)\n\t" \
  "v_mfma_f32_16x16x32_bf16 v[184:187], v[108:111], v[72:75], v[184:187]\n\t" \
  "ds_read_b64_tr_b16 v[100:101], v55 offset:34816\n\t" \
  "v_mfma_f32_16x16x32_bf16 v[184:187], v[136:139], v[148:151], v[184:187]\n\t" \
  "ds_read_b64_tr_b16 v[102:103], v55 offset:35328\n\t" \
  "v_mfma_f32_16x16x32_bf16 v[192:195], v[136:139], v[68:71], v[192:195]\n\t" \
  "ds_read_b64_tr_b16 v[112:113], v55 offset:38912\n\t" \
  "v_mfma_f32_16x16x32_bf16 v[192:195], v[108:111], v[104:107], v[192:195]\n\t" \
  "ds_read_b64_tr_b16 v[114:115], v55 offset:39424\n\t" \
  "v_mfma_f32_16x16x32_bf16 v[200:203], v[108:111], v[80:83], v[200:203]\n\t" \
  "ds_read_b64_tr_b16 v[128:129], v54 offset:18432\n\t" \
  "v_mfma_f32_16x16x32_bf16 v[200:203], v[136:139], v[116:119], v[200:203]\n\t" \
  "ds_read_b64_tr_b16 v[130:131], v54 offset:18944\n\t" \
  "v_mfma_f32_16x16x32_bf16 v[208:211], v[136:139], v[88:91], v[208:211]\n\t" \
  "ds_read_b64_tr_b16 v[132:133], v54 offset:22528\n\t" \
  "v_mfma_f32_16x16x32_bf16 v[208:211], v[108:111], v[124:127], v[208:211]\n\t" \
  "ds_read_b64_tr_b16 v[134:135], v54 offset:23040\n\t" \
  "v_mfma_f32_16x16x32_bf16 v[188:191], v[144:147], v[72:75], v[188:191]\n\t" \
  "ds_read_b64_tr_b16 v[76:77], v55 offset:18432\n\t" \
  "s_waitcnt lgkmcnt(7)\n\t" \
  "v_mfma_f32_16x16x32_bf16 v[188:191], v[96:99], v[148:151], v[188:191]\n\t" \
  "ds_read_b64_tr_b16 v[78:79], v55 offset:18944\n\t" \
  "v_mfma_f32_16x16x32_bf16 v[196:199], v[96:99], v[68:71], v[196:199]\n\t" \
  "ds_read_b64_tr_b16 v[120:121], v55 offset:22528\n\t" \
  "v_mfma_f32_16x16x32_bf16 v[196:199], v[144:147], v[104:107], v[196:199]\n\t" \
  "ds_read_b64_tr_b16 v[122:123], v55 offset:23040\n\t" \
  "v_mfma_f32_16x16x32_bf16 v[204:207], v[144:147], v[80:83], v[204:207]\n\t" \
  "v_mfma_f32_16x16x32_bf16 v[204:207], v[96:99], v[116:119], v[204:207]\n\t" \
  "v_mfma_f32_16x16x32_bf16 v[212:215], v[96:99], v[88:91], v[212:215]\n\t" \
  "v_mfma_f32_16x16x32_bf16 v[212:215], v[144:147], v[124:127], v[212:215]\n\t" \
  "v_mfma_f32_16x16x32_bf16 v[216:219], v[72:75], v[64:67], v[216:219]\n\t" \
  "v_mfma_f32_16x16x32_bf16 v[216:219], v[148:151], v[140:143], v[216:219]\n\t" \
  "v_mfma_f32_16x16x32_bf16 v[224:227], v[68:71], v[140:143], v[224:227]\n\t" \
  "v_mfma_f32_16x16x32_bf16 v[224:227], v[104:107], v[64:67], v[224:227]\n\t" \
  "v_mfma_f32_16x16x32_bf16 v[232:235], v[80:83], v[64:67], v[232:235]\n\t" \
  "v_mfma_f32_16x16x32_bf16 v[232:235], v[116:119], v[140:143], v[232:235]\n\t" \
  "v_mfma_f32_16x16x32_bf16 v[240:243], v[88:91], v[140:143], v[240:243]\n\t" \
  "v_mfma_f32_16x16x32_bf16 v[240:243], v[124:127], v[64:67], v[240:243]\n\t" \
  "v_mfma_f32_16x16x32_bf16 v[248:251], v[92:95], v[64:67], v[248:251]\n\t" \
  "v_mfma_f32_16x16x32_bf16 v[248:251], v[84:87], v[140:143], v[248:251]\n\t" \
  "v_mfma_f32_16x16x32_bf16 v[252:255], v[92:95], v[100:103], v[252:255]\n\t" \
  "s_waitcnt lgkmcnt(0)\n\t" \
  "v_mfma_f32_16x16x32_bf16 v[252:255], v[84:87], v[112:115], v[252:255]\n\t" \
  "v_mfma_f32_16x16x32_bf16 v[220:223], v[148:151], v[112:115], v[220:223]\n\t" \
  "v_mfma_f32_16x16x32_bf16 v[220:223], v[72:75], v[100:103], v[220:223]\n\t" \
  "s_barrier\n\t" \
  "v_mfma_f32_16x16x32_bf16 v[228:231], v[104:107], v[100:103], v[228:231]\n\t" \
  "ds_read_b64_tr_b16 v[96:97], v58 offset:26624\n\t" \
  "v_mfma_f32_16x16x32_bf16 v[228:231], v[68:71], v[112:115], v[228:231]\n\t" \
  "ds_read_b64_tr_b16 v[98:99], v58 offset:27136\n\t" \
  "v_mfma_f32_16x16x32_bf16 v[236:239], v[116:119], v[112:115], v[236:239]\n\t" \
  "ds_read_b128 v[136:139], v56 offset:10240\n\t" \
  "v_mfma_f32_16x16x32_bf16 v[236:239], v[80:83], v[100:103], v[236:239]\n\t" \
  "ds_read_b64_tr_b16 v[64:65], v58 offset:30720\n\t" \
  "v_mfma_f32_16x16x32_bf16 v[244:247], v[124:127], v[100:103], v[244:247]\n\t" \
  "ds_read_b64_tr_b16 v[66:67], v58 offset:31232\n\t" \
  "v_mfma_f32_16x16x32_bf16 v[244:247], v[88:91], v[112:115], v[244:247]\n\t" \
  "ds_read_b128 v[108:111], v57 offset:10240\n\t" \
  "v_mfma_f32_16x16x32_bf16 v[152:155], v[128:131], v[72:75], v[152:155]\n\t" \
  "ds_read_b128 v[144:147], v57 offset:12288\n\t" \
  "v_mfma_f32_16x16x32_bf16 v[152:155], v[132:135], v[148:151], v[152:155]\n\t" \
  "ds_read_b128 v[140:143], v56 offset:12288\n\t" \
  "v_mfma_f32_16x16x32_bf16 v[160:163], v[132:135], v[68:71], v[160:163]\n\t" \
  "ds_read_b128 v[112:115], v56 offset:14336\n\t" \
  "v_mfma_f32_16x16x32_bf16 v[160:163], v[128:131], v[104:107], v[160:163]\n\t" \
  "ds_read_b64_tr_b16 v[92:93], v59 offset:26624\n\t" \
  "v_mfma_f32_16x16x32_bf16 v[168:171], v[128:131], v[80:83], v[168:171]\n\t" \
  "ds_read_b64_tr_b16 v[94:95], v59 offset:27136\n\t" \
  "v_mfma_f32_16x16x32_bf16 v[168:171], v[132:135], v[116:119], v[168:171]\n\t" \
  "ds_read_b64_tr_b16 v[84:85], v59 offset:30720\n\t" \
  "v_mfma_f32_16x16x32_bf16 v[176:179], v[132:135], v[88:91], v[176:179]\n\t" \
  "ds_read_b128 v[132:135], v57 offset:14336\n\t" \
  "v_mfma_f32_16x16x32_bf16 v[176:179], v[128:131], v[124:127], v[176:179]\n\t" \
  "ds_read_b128 v[128:131], v57 offset:16384\n\t" \
  "v_mfma_f32_16x16x32_bf16 v[156:159], v[76:79], v[72:75], v[156:159]\n\t" \
  "ds_read_b64_tr_b16 v[86:87], v59 offset:31232\n\t" \
  "v_mfma_f32_16x16x32_bf16 v[156:159], v[120:123], v[148:151], v[156:159]\n\t" \
  "ds_read_b128 v[148:151], v56 offset:16384\n\t" \
  "v_mfma_f32_16x16x32_bf16 v[164:167], v[120:123], v[68:71], v[164:167]\n\t" \
  "ds_read_b64_tr_b16 v[100:101], v58 offset:34816\n\t" \
  "v_mfma_f32_16x16x32_bf16 v[164:167], v[76:79], v[104:107], v[164:167]\n\t" \
  "ds_read_b64_tr_b16 v[102:103], v58 offset:35328\n\t" \
  "v_mfma_f32_16x16x32_bf16 v[172:175], v[76:79], v[80:83], v[172:175]\n\t" \
  "ds_read_b64_tr_b16 v[72:73], v58 offset:38912\n\t" \
  "v_mfma_f32_16x16x32_bf16 v[172:175], v[120:123], v[116:119], v[172:175]\n\t" \
  "ds_read_b64_tr_b16 v[74:75], v58 offset:39424\n\t" \
  "v_mfma_f32_16x16x32_bf16 v[180:183], v[120:123], v[88:91], v[180:183]\n\t" \
  "ds_read_b128 v[68:71], v56 offset:0\n\t" \
  "v_mfma_f32_16x16x32_bf16 v[180:183], v[76:79], v[124:127], v[180:183]\n\t" \
  "ds_read_b128 v[104:107], v57 offset:0\n\t" \
  "s_waitcnt lgkmcnt(7)\n\t" \
  "v_mfma_f32_16x16x32_bf16 v[184:187], v[96:99], v[136:139], v[184:187]\n\t" \
  "ds_read_b64_tr_b16 v[80:81], v59 offset:34816\n\t" \
  "v_mfma_f32_16x16x32_bf16 v[184:187], v[64:67], v[108:111], v[184:187]\n\t" \
  "ds_read_b64_tr_b16 v[82:83], v59 offset:35328\n\t" \
  "v_mfma_f32_16x16x32_bf16 v[192:195], v[64:67], v[144:147], v[192:195]\n\t" \
  "ds_read_b64_tr_b16 v[88:89], v59 offset:38912\n\t" \
  "v_mfma_f32_16x16x32_bf16 v[192:195], v[96:99], v[140:143], v[192:195]\n\t" \
  "ds_read_b64_tr_b16 v[90:91], v59 offset:39424\n\t" \
  "v_mfma_f32_16x16x32_bf16 v[200:203], v[96:99], v[112:115], v[200:203]\n\t" \
  "ds_read_b64_tr_b16 v[116:117], v58 offset:18432\n\t" \
  "v_mfma_f32_16x16x32_bf16 v[200:203], v[64:67], v[132:135], v[200:203]\n\t" \
  "ds_read_b64_tr_b16 v[118:119], v58 offset:18944\n\t" \
  "v_mfma_f32_16x16x32_bf16 v[208:211], v[64:67], v[128:131], v[208:211]\n\t" \
  "ds_read_b64_tr_b16 v[120:121], v58 offset:22528\n\t" \
  "s_waitcnt lgkmcnt(7)\n\t" \
  "v_mfma_f32_16x16x32_bf16 v[208:211], v[96:99], v[148:151], v[208:211]\n\t" \
  "ds_read_b64_tr_b16 v[122:123], v58 offset:23040\n\t" \
  "v_mfma_f32_16x16x32_bf16 v[188:191], v[92:95], v[136:139], v[188:191]\n\t" \
  "ds_read_b64_tr_b16 v[124:125], v59 offset:18432\n\t" \
  "v_mfma_f32_16x16x32_bf16 v[188:191], v[84:87], v[108:111], v[188:191]\n\t" \
  "ds_read_b64_tr_b16 v[126:127], v59 offset:18944\n\t" \
  "v_mfma_f32_16x16x32_bf16 v[196:199], v[84:87], v[144:147], v[196:199]\n\t" \
  "ds_read_b64_tr_b16 v[76:77], v59 offset:22528\n\t" \
  "v_mfma_f32_16x16x32_bf16 v[196:199], v[92:95], v[140:143], v[196:199]\n\t" \
  "ds_read_b64_tr_b16 v[78:79], v59 offset:23040\n\t" \
  "v_mfma_f32_16x16x32_bf16 v[204:207], v[92:95], v[112:115], v[204:207]\n\t" \
  "v_mfma_f32_16x16x32_bf16 v[204:207], v[84:87], v[132:135], v[204:207]\n\t" \
  "v_mfma_f32_16x16x32_bf16 v[212:215], v[84:87], v[128:131], v[212:215]\n\t" \
  "v_mfma_f32_16x16x32_bf16 v[212:215], v[92:95], v[148:151], v[212:215]\n\t" \
  "v_mfma_f32_16x16x32_bf16 v[216:219], v[136:139], v[100:103], v[216:219]\n\t" \
  "v_mfma_f32_16x16x32_bf16 v[216:219], v[108:111], v[72:75], v[216:219]\n\t" \
  "v_mfma_f32_16x16x32_bf16 v[224:227], v[144:147], v[72:75], v[224:227]\n\t" \
  "v_and_b32 v14, -16, v32\n\t" \
  "v_mfma_f32_16x16x32_bf16 v[224:227], v[140:143], v[100:103], v[224:227]\n\t" \
  "v_mfma_f32_16x16x32_bf16 v[232:235], v[112:115], v[100:103], v[232:235]\n\t" \
  "ds_read_b128 v[10:13], v14 offset:128\n\t" \
  "v_mfma_f32_16x16x32_bf16 v[232:235], v[132:135], v[72:75], v[232:235]\n\t" \
  "v_mfma_f32_16x16x32_bf16 v[240:243], v[128:131], v[72:75], v[240:243]\n\t" \
  "v_mfma_f32_16x16x32_bf16 v[240:243], v[148:151], v[100:103], v[240:243]\n\t" \
  "v_mfma_f32_16x16x32_bf16 v[248:251], v[68:71], v[100:103], v[248:251]\n\t" \
  "v_mfma_f32_16x16x32_bf16 v[248:251], v[104:107], v[72:75], v[248:251]\n\t" \
  "s_waitcnt lgkmcnt(1)\n\t" \
  "v_mfma_f32_16x16x32_bf16 v[252:255], v[68:71], v[80:83], v[252:255]\n\t" \
  "s_waitcnt lgkmcnt(0)\n\t" \
  "v_and_b32 v10, v10, v11\n\t" \
  "v_and_b32 v12, v12, v13\n\t" \
  "v_mfma_f32_16x16x32_bf16 v[252:255], v[104:107], v[88:91], v[252:255]\n\t" \
  "v_and_b32 v10, v10, v12\n\t" \
  "v_mfma_f32_16x16x32_bf16 v[220:223], v[108:111], v[88:91], v[220:223]\n\t" \
  "v_readfirstlane_b32 vcc_lo, v10\n\t" \
  "v_mfma_f32_16x16x32_bf16 v[220:223], v[136:139], v[80:83], v[220:223]\n\t" \
  "ds_write_b32 v32, v0 offset:144\n\t" \
  "s_cmp_eq_u32 vcc_lo, 0\n\t" \
  "s_cbranch_scc1 15f\n\t" \
  "14:\n\t" \
  "v_mfma_f32_16x16x32_bf16 v[228:231], v[140:143], v[80:83], v[228:231]\n\t" \
  "ds_read_b64_tr_b16 v[64:65], v62 offset:26624\n\t" \
  "v_mfma_f32_16x16x32_bf16 v[228:231], v[144:147], v[88:91], v[228:231]\n\t" \
  "ds_read_b64_tr_b16 v[66:67], v62 offset:27136\n\t" \
  "v_mfma_f32_16x16x32_bf16 v[236:239], v[132:135], v[88:91], v[236:239]\n\t" \
  "ds_read_b128 v[96:99], v60 offset:10240\n\t" \
  "v_mfma_f32_16x16x32_bf16 v[236:239], v[112:115], v[80:83], v[236:239]\n\t" \
  "ds_read_b128 v[84:87], v60 offset:12288\n\t" \
  "v_mfma_f32_16x16x32_bf16 v[244:247], v[148:151], v[80:83], v[244:247]\n\t" \
  "ds_read_b128 v[92:95], v60 offset:14336\n\t" \
  "v_mfma_f32_16x16x32_bf16 v[244:247], v[128:131], v[88:91], v[244:247]\n\t" \
  "ds_read_b128 v[100:103], v60 offset:16384\n\t" \
  "v_mfma_f32_16x16x32_bf16 v[152:155], v[116:119], v[136:139], v[152:155]\n\t" \
  "ds_read_b64_tr_b16 v[72:73], v63 offset:26624\n\t" \
  "v_mfma_f32_16x16x32_bf16 v[152:155], v[120:123], v[108:111], v[152:155]\n\t" \
  "ds_read_b64_tr_b16 v[74:75], v63 offset:27136\n\t" \
  "v_mfma_f32_16x16x32_bf16 v[160:163], v[120:123], v[144:147], v[160:163]\n\t" \
  "ds_read_b64_tr_b16 v[68:69], v62 offset:34816\n\t" \
  "v_mfma_f32_16x16x32_bf16 v[160:163], v[116:119], v[140:143], v[160:163]\n\t" \
  "ds_read_b64_tr_b16 v[70:71], v62 offset:35328\n\t" \
  "v_mfma_f32_16x16x32_bf16 v[168:171], v[116:119], v[112:115], v[168:171]\n\t" \
  "ds_read_b128 v[104:107], v60 offset:0\n\t" \
  "v_mfma_f32_16x16x32_bf16 v[168:171], v[120:123], v[132:135], v[168:171]\n\t" \
  "ds_read_b64_tr_b16 v[80:81], v63 offset:34816\n\t" \
  "v_mfma_f32_16x16x32_bf16 v[176:179], v[120:123], v[128:131], v[176:179]\n\t" \
  "ds_read_b64_tr_b16 v[82:83], v63 offset:35328\n\t" \
  "v_mfma_f32_16x16x32_bf16 v[176:179], v[116:119], v[148:151], v[176:179]\n\t" \
  "ds_read_b64_tr_b16 v[88:89], v62 offset:18432\n\t" \
  "v_mfma_f32_16x16x32_bf16 v[156:159], v[124:127], v[136:139], v[156:159]\n\t" \
  "ds_read_b64_tr_b16 v[90:91], v62 offset:18944\n\t" \
  "v_mfma_f32_16x16x32_bf16 v[156:159], v[76:79], v[108:111], v[156:159]\n\t" \
  "ds_read_b64_tr_b16 v[120:121], v62 offset:30720\n\t" \
  "v_mfma_f32_16x16x32_bf16 v[164:167], v[76:79], v[144:147], v[164:167]\n\t" \
  "ds_read_b64_tr_b16 v[122:123], v62 offset:31232\n\t" \
  "v_mfma_f32_16x16x32_bf16 v[164:167], v[124:127], v[140:143], v[164:167]\n\t" \
  "ds_read_b128 v[116:119], v61 offset:10240\n\t" \
  "v_mfma_f32_16x16x32_bf16 v[172:175], v[124:127], v[112:115], v[172:175]\n\t" \
  "ds_read_b128 v[136:139], v61 offset:12288\n\t" \
  "v_mfma_f32_16x16x32_bf16 v[172:175], v[76:79], v[132:135], v[172:175]\n\t" \
  "ds_read_b128 v[108:111], v61 offset:14336\n\t" \
  "v_mfma_f32_16x16x32_bf16 v[180:183], v[76:79], v[128:131], v[180:183]\n\t" \
  "ds_read_b64_tr_b16 v[76:77], v63 offset:18432\n\t" \
  "v_mfma_f32_16x16x32_bf16 v[180:183], v[124:127], v[148:151], v[180:183]\n\t" \
  "ds_read_b64_tr_b16 v[78:79], v63 offset:18944\n\t" \
  "s_waitcnt lgkmcnt(7)\n\t" \
  "v_mfma_f32_16x16x32_bf16 v[184:187], v[64:67], v[96:99], v[184:187]\n\t" \
  "ds_read_b128 v[144:147], v61 offset:16384\n\t" \
  "v_mfma_f32_16x16x32_bf16 v[192:195], v[64:67], v[84:87], v[192:195]\n\t" \
  "ds_read_b64_tr_b16 v[140:141], v63 offset:30720\n\t" \
  "v_mfma_f32_16x16x32_bf16 v[200:203], v[64:67], v[92:95], v[200:203]\n\t" \
  "ds_read_b64_tr_b16 v[142:143], v63 offset:31232\n\t" \
  "v_mfma_f32_16x16x32_bf16 v[208:211], v[64:67], v[100:103], v[208:211]\n\t" \
  "502:\n\t" \
  "s_cbranch_execz 506f\n\t" \
  "ds_read_b64_tr_b16 v[112:113], v62 offset:22528\n\t" \
  "v_mfma_f32_16x16x32_bf16 v[188:191], v[72:75], v[96:99], v[188:191]\n\t" \
  "ds_read_b64_tr_b16 v[114:115], v62 offset:23040\n\t" \
  "v_mfma_f32_16x16x32_bf16 v[196:199], v[72:75], v[84:87], v[196:199]\n\t" \
  "ds_read_b64_tr_b16 v[132:133], v63 offset:22528\n\t" \
  "v_mfma_f32_16x16x32_bf16 v[204:207], v[72:75], v[92:95], v[204:207]\n\t" \
  "ds_read_b64_tr_b16 v[134:135], v63 offset:23040\n\t" \
  "v_mfma_f32_16x16x32_bf16 v[212:215], v[72:75], v[100:103], v[212:215]\n\t" \
  "ds_read_b64_tr_b16 v[128:129], v62 offset:38912\n\t" \
  "v_mfma_f32_16x16x32_bf16 v[248:251], v[104:107], v[68:71], v[248:251]\n\t" \
  "ds_read_b64_tr_b16 v[130:131], v62 offset:39424\n\t" \
  "v_mfma_f32_16x16x32_bf16 v[216:219], v[96:99], v[68:71], v[216:219]\n\t" \
  "ds_read_b128 v[124:127], v61 offset:0\n\t" \
  "v_mfma_f32_16x16x32_bf16 v[224:227], v[84:87], v[68:71], v[224:227]\n\t" \
  "ds_read_b64_tr_b16 v[148:149], v63 offset:38912\n\t" \
  "v_mfma_f32_16x16x32_bf16 v[232:235], v[92:95], v[68:71], v[232:235]\n\t" \
  "ds_read_b64_tr_b16 v[150:151], v63 offset:39424\n\t" \
  "v_mfma_f32_16x16x32_bf16 v[240:243], v[100:103], v[68:71], v[240:243]\n\t" \
  "v_mfma_f32_16x16x32_bf16 v[252:255], v[104:107], v[80:83], v[252:255]\n\t" \
  "v_mfma_f32_16x16x32_bf16 v[220:223], v[96:99], v[80:83], v[220:223]\n\t" \
  "v_mfma_f32_16x16x32_bf16 v[228:231], v[84:87], v[80:83], v[228:231]\n\t" \
  "v_mfma_f32_16x16x32_bf16 v[236:239], v[92:95], v[80:83], v[236:239]\n\t" \
  "v_mfma_f32_16x16x32_bf16 v[244:247], v[100:103], v[80:83], v[244:247]\n\t" \
  "v_mfma_f32_16x16x32_bf16 v[152:155], v[88:91], v[96:99], v[152:155]\n\t" \
  "v_mfma_f32_16x16x32_bf16 v[160:163], v[88:91], v[84:87], v[160:163]\n\t" \
  "v_mfma_f32_16x16x32_bf16 v[168:171], v[88:91], v[92:95], v[168:171]\n\t" \
  "v_mfma_f32_16x16x32_bf16 v[176:179], v[88:91], v[100:103], v[176:179]\n\t" \
  "s_waitcnt lgkmcnt(0)\n\t" \
  "v_mfma_f32_16x16x32_bf16 v[156:159], v[76:79], v[96:99], v[156:159]\n\t" \
  "v_mfma_f32_16x16x32_bf16 v[164:167], v[76:79], v[84:87], v[164:167]\n\t" \
  "ds_write_b32 v32, v0\n\t" \
  "v_mfma_f32_16x16x32_bf16 v[172:175], v[76:79], v[92:95], v[172:175]\n\t" \
  "v_mfma_f32_16x16x32_bf16 v[180:183], v[76:79], v[100:103], v[180:183]\n\t" \
  "v_mfma_f32_16x16x32_bf16 v[184:187], v[120:123], v[116:119], v[184:187]\n\t" \
  "v_and_b32 v56, -16, v32\n\t" \
  "v_mfma_f32_16x16x32_bf16 v[192:195], v[120:123], v[136:139], v[192:195]\n\t" \
  "v_mfma_f32_16x16x32_bf16 v[200:203], v[120:123], v[108:111], v[200:203]\n\t" \
  "v_mfma_f32_16x16x32_bf16 v[208:211], v[120:123], v[144:147], v[208:211]\n\t" \
  "v_mfma_f32_16x16x32_bf16 v[188:191], v[140:143], v[116:119], v[188:191]\n\t" \
  "v_mfma_f32_16x16x32_bf16 v[196:199], v[140:143], v[136:139], v[196:199]\n\t" \
  "v_mfma_f32_16x16x32_bf16 v[204:207], v[140:143], v[108:111], v[204:207]\n\t" \
  "v_mfma_f32_16x16x32_bf16 v[212:215], v[140:143], v[144:147], v[212:215]\n\t" \
  "v_cvt_pk_bf16_f32 v14, v184, v185\n\t" \
  "v_cvt_pk_bf16_f32 v15, v186, v187\n\t" \
  "v_mfma_f32_16x16x32_bf16 v[152:155], v[112:115], v[116:119], v[152:155]\n\t" \
  "v_cvt_pk_bf16_f32 v18, v192, v193\n\t" \
  "v_cvt_pk_bf16_f32 v19, v194, v195\n\t" \
  "v_mfma_f32_16x16x32_bf16 v[160:163], v[112:115], v[136:139], v[160:163]\n\t" \
  "v_cvt_pk_bf16_f32 v16, v188, v189\n\t" \
  "v_cvt_pk_bf16_f32 v17, v190, v191\n\t" \
  "v_mfma_f32_16x16x32_bf16 v[168:171], v[112:115], v[108:111], v[168:171]\n\t" \
  "v_cvt_pk_bf16_f32 v20, v196, v197\n\t" \
  "v_cvt_pk_bf16_f32 v21, v198, v199\n\t" \
  "v_mfma_f32_16x16x32_bf16 v[176:179], v[112:115], v[144:147], v[176:179]\n\t" \
  "v_cvt_pk_bf16_f32 v22, v200, v201\n\t" \
  "v_cvt_pk_bf16_f32 v23, v202, v203\n\t" \
  "v_mfma_f32_16x16x32_bf16 v[156:159], v[132:135], v[116:119], v[156:159]\n\t" \
  "v_cvt_pk_bf16_f32 v24, v204, v205\n\t" \
  "v_cvt_pk_bf16_f32 v25, v206, v207\n\t" \
  "v_mfma_f32_16x16x32_bf16 v[164:167], v[132:135], v[136:139], v[164:167]\n\t" \
  "v_cvt_pk_bf16_f32 v26, v208, v209\n\t" \
  "v_cvt_pk_bf16_f32 v27, v210, v211\n\t" \
  "v_mfma_f32_16x16x32_bf16 v[172:175], v[132:135], v[108:111], v[172:175]\n\t" \
  "v_cvt_pk_bf16_f32 v28, v212, v213\n\t" \
  "v_cvt_pk_bf16_f32 v29, v214, v215\n\t" \
  "v_mfma_f32_16x16x32_bf16 v[180:183], v[132:135], v[144:147], v[180:183]\n\t" \
  "v_cvt_pk_bf16_f32 v84, v152, v153\n\t" \
  "v_cvt_pk_bf16_f32 v85, v154, v155\n\t" \
  "v_mfma_f32_16x16x32_bf16 v[248:251], v[124:127], v[128:131], v[248:251]\n\t" \
  "v_cvt_pk_bf16_f32 v86, v156, v157\n\t" \
  "v_cvt_pk_bf16_f32 v87, v158, v159\n\t" \
  "v_mfma_f32_16x16x32_bf16 v[216:219], v[116:119], v[128:131], v[216:219]\n\t" \
  "v_cvt_pk_bf16_f32 v88, v160, v161\n\t" \
  "v_cvt_pk_bf16_f32 v89, v162, v163\n\t" \
  "v_mfma_f32_16x16x32_bf16 v[224:227], v[136:139], v[128:131], v[224:227]\n\t" \
  "v_cvt_pk_bf16_f32 v90, v164, v165\n\t" \
  "v_cvt_pk_bf16_f32 v91, v166, v167\n\t" \
  "v_mfma_f32_16x16x32_bf16 v[232:235], v[108:111], v[128:131], v[232:235]\n\t" \
  "v_cvt_pk_bf16_f32 v72, v248, v249\n\t" \
  "v_cvt_pk_bf16_f32 v73, v250, v251\n\t" \
  "v_mfma_f32_16x16x32_bf16 v[240:243], v[144:147], v[128:131], v[240:243]\n\t" \
  "ds_read_b32 v52, v33 offset:48\n\t" \
  "ds_read_b128 v[10:13], v31\n\t" \
  "v_cvt_pk_bf16_f32 v92, v168, v169\n\t" \
  "v_mfma_f32_16x16x32_bf16 v[252:255], v[124:127], v[148:151], v[252:255]\n\t" \
  "v_cvt_pk_bf16_f32 v93, v170, v171\n\t" \
  "v_cvt_pk_bf16_f32 v94, v172, v173\n\t" \
  "v_mfma_f32_16x16x32_bf16 v[220:223], v[116:119], v[148:151], v[220:223]\n\t" \
  "v_cvt_pk_bf16_f32 v95, v174, v175\n\t" \
  "v_cvt_pk_bf16_f32 v96, v176, v177\n\t" \
  "v_mfma_f32_16x16x32_bf16 v[228:231], v[136:139], v[148:151], v[228:231]\n\t" \
  "v_cvt_pk_bf16_f32 v97, v178, v179\n\t" \
  "v_cvt_pk_bf16_f32 v98, v180, v181\n\t" \
  "v_mfma_f32_16x16x32_bf16 v[236:239], v[108:111], v[148:151], v[236:239]\n\t" \
  "v_cvt_pk_bf16_f32 v82, v252, v253\n\t" \
  "v_cvt_pk_bf16_f32 v83, v254, v255\n\t" \
  "v_mfma_f32_16x16x32_bf16 v[244:247], v[144:147], v[148:151], v[244:247]\n\t" \
  "ds_write_b64 v30, v[72:73]\n\t" \
  "ds_write_b64 v30, v[82:83] offset:8\n\t" \
  "ds_write_b32 v32, v0 offset:48\n\t" \
  "v_mfma_f32_16x16x32_bf16 v[200:203], v[22:25], v[92:95], v[34:37]\n\t" \
  "v_cvt_pk_bf16_f32 v99, v182, v183\n\t" \
  "s_waitcnt lgkmcnt(0)\n\t" \
  "v_readfirstlane_b32 vcc_lo, v52\n\t" \
  "s_nop 1\n\t" \
  "s_cmp_eq_u32 vcc_lo, 0\n\t" \
  "s_cbranch_scc1 5f\n\t" \
  "4:\n\t" \
  "v_mfma_f32_16x16x32_bf16 v[204:207], v[18:21], v[92:95], v[38:41]\n\t" \
  "v_cvt_pk_bf16_f32 v66, v232, v233\n\t" \
  "v_cvt_pk_bf16_f32 v67, v234, v235\n\t" \
  "v_mfma_f32_16x16x32_bf16 v[208:211], v[26:29], v[96:99], v[34:37]\n\t" \
  "v_cvt_pk_bf16_f32 v76, v236, v237\n\t" \
  "v_cvt_pk_bf16_f32 v77, v238, v239\n\t" \
  "v_mfma_f32_16x16x32_bf16 v[212:215], v[22:25], v[96:99], v[38:41]\n\t" \
  "v_cvt_pk_bf16_f32 v64, v240, v241\n\t" \
  "v_cvt_pk_bf16_f32 v65, v242, v243\n\t" \
  "v_mfma_f32_16x16x32_bf16 v[192:195], v[18:21], v[88:91], v[34:37]\n\t" \
  "v_max_f32 v200, v200, v204\n\t" \
  "v_max_f32 v201, v201, v205\n\t" \
  "v_mfma_f32_16x16x32_bf16 v[196:199], v[14:17], v[88:91], v[38:41]\n\t" \
  "v_max_f32 v202, v202, v206\n\t" \
  "v_max_f32 v203, v203, v207\n\t" \
  "v_mfma_f32_16x16x32_bf16 v[184:187], v[14:17], v[84:87], v[34:37]\n\t" \
  "v_max_f32 v208, v208, v212\n\t" \
  "v_max_f32 v209, v209, v213\n\t" \
  "v_mfma_f32_16x16x32_bf16 v[188:191], v[10:13], v[84:87], v[42:45]\n\t" \
  "v_max_f32 v210, v210, v214\n\t" \
  "v_max_f32 v211, v211, v215\n\t" \
  "v_max_f32 v192, v192, v196\n\t" \
  "v_max_f32 v193, v193, v197\n\t" \
  "v_max_f32 v194, v194, v198\n\t" \
  "v_max3_f32 v106, v208, v209, v210\n\t" \
  "v_max3_f32 v104, v200, v201, v202\n\t" \
  "v_max_f32 v195, v195, v199\n\t" \
  "v_max_f32 v184, v184, v188\n\t" \
  "v_max_f32 v185, v185, v189\n\t" \
  "v_max_f32 v186, v186, v190\n\t" \
  "v_max3_f32 v102, v192, v193, v194\n\t" \
  "v_max_f32 v187, v187, v191\n\t" \
  "v_max3_f32 v100, v184, v185, v186\n\t" \
  "v_max_f32 v106, v106, v211\n\t" \
  "v_max_f32 v104, v104, v203\n\t" \
  "v_max_f32 v102, v102, v195\n\t" \
  "v_max_f32 v100, v100, v187\n\t" \
  "v_permlane16_swap_b32 v104, v106\n\t" \
  "v_cvt_pk_bf16_f32 v74, v244, v245\n\t" \
  "v_permlane16_swap_b32 v100, v102\n\t" \
  "v_max_f32 v104, v104, v106\n\t" \
  "v_max_f32 v100, v100, v102\n\t" \
  "v_cvt_pk_bf16_f32 v75, v246, v247\n\t" \
  "v_cvt_pk_bf16_f32 v68, v224, v225\n\t" \
  "v_permlane32_swap_b32 v100, v104\n\t" \
  "v_cvt_pk_bf16_f32 v69, v226, v227\n\t" \
  "v_max_f32 v100, v100, v104\n\t" \
  "v_mul_f32 v100, 0xbe8293ee, v100\n\t" \
  "v_mov_b32 v104, v100\n\t" \
  "v_cvt_pk_bf16_f32 v78, v228, v229\n\t" \
  "v_cvt_pk_bf16_f32 v79, v230, v231\n\t" \
  "v_permlane32_swap_b32 v100, v104\n\t" \
  "v_cvt_pk_bf16_f32 v70, v216, v217\n\t" \
  "v_mov_b32 v106, v104\n\t" \
  "v_mov_b32 v102, v100\n\t" \
  "v_cvt_pk_bf16_f32 v71, v218, v219\n\t" \
  "v_permlane16_swap_b32 v104, v106\n\t" \
  "v_permlane16_swap_b32 v100, v102\n\t" \
  "v_fma_f32 v208, v208, %16, v106\n\t" \
  "v_fma_f32 v209, v209, %16, v106\n\t" \
  "v_fma_f32 v210, v210, %16, v106\n\t" \
  "v_fma_f32 v211, v211, %16, v106\n\t" \
  "v_exp_f32 v208, v208\n\t" \
  "v_exp_f32 v209, v209\n\t" \
  "v_exp_f32 v210, v210\n\t" \
  "v_exp_f32 v211, v211\n\t" \
  "v_cvt_pk_bf16_f32 v212, v208, v209\n\t" \
  "v_cvt_pk_bf16_f32 v213, v210, v211\n\t" \
  "v_bfi_b32 v214, v46, 0, v212\n\t" \
  "v_bfi_b32 v215, v47, 0, v213\n\t" \
  "v_and_b32 v212, v46, v212\n\t" \
  "v_and_b32 v213, v47, v213\n\t" \
  "v_fma_f32 v200, v200, %16, v104\n\t" \
  "v_fma_f32 v201, v201, %16, v104\n\t" \
  "v_mfma_f32_16x16x32_bf16 v[208:211], v[48:51], v[212:215], 0\n\t" \
  "v_fma_f32 v202, v202, %16, v104\n\t" \
  "v_fma_f32 v203, v203, %16, v104\n\t" \
  "v_mfma_f32_16x16x32_bf16 v[176:179], v[64:67], v[212:215], 0\n\t" \
  "v_exp_f32 v200, v200\n\t" \
  "v_mfma_f32_16x16x32_bf16 v[180:183], v[74:77], v[212:215], 0\n\t" \
  "v_exp_f32 v201, v201\n\t" \
  "v_exp_f32 v202, v202\n\t" \
  "v_exp_f32 v203, v203\n\t" \
  "v_cvt_pk_bf16_f32 v204, v200, v201\n\t" \
  "v_cvt_pk_bf16_f32 v205, v202, v203\n\t" \
  "v_bfi_b32 v206, v46, 0, v204\n\t" \
  "v_bfi_b32 v207, v47, 0, v205\n\t" \
  "v_rcp_f32 v106, v208\n\t" \
  "v_and_b32 v204, v46, v204\n\t" \
  "v_and_b32 v205, v47, v205\n\t" \
  "v_pk_mul_f32 v[176:177], v[176:177], v[106:107] op_sel_hi:[1,0]\n\t" \
  "v_pk_mul_f32 v[178:179], v[178:179], v[106:107] op_sel_hi:[1,0]\n\t" \
  "v_mfma_f32_16x16x32_bf16 v[200:203], v[48:51], v[204:207], 0\n\t" \
  "v_pk_mul_f32 v[180:181], v[180:181], v[106:107] op_sel_hi:[1,0]\n\t" \
  "v_pk_mul_f32 v[182:183], v[182:183], v[106:107] op_sel_hi:[1,0]\n\t" \
  "v_mfma_f32_16x16x32_bf16 v[168:171], v[66:69], v[204:207], 0\n\t" \
  "v_cvt_pk_bf16_f32 v240, v176, v177\n\t" \
  "v_cvt_pk_bf16_f32 v241, v178, v179\n\t" \
  "v_mfma_f32_16x16x32_bf16 v[172:175], v[76:79], v[204:207], 0\n\t" \
  "v_cvt_pk_bf16_f32 v242, v180, v181\n\t" \
  "v_cvt_pk_bf16_f32 v243, v182, v183\n\t" \
  "global_store_dwordx4 v1, v[240:243], %14 offset:0 sc1\n\t" \
  "v_fma_f32 v192, v192, %16, v102\n\t" \
  "v_fma_f32 v193, v193, %16, v102\n\t" \
  "v_fma_f32 v194, v194, %16, v102\n\t" \
  "v_fma_f32 v195, v195, %16, v102\n\t" \
  "v_exp_f32 v192, v192\n\t" \
  "v_exp_f32 v193, v193\n\t" \
  "v_exp_f32 v194, v194\n\t" \
  "v_exp_f32 v195, v195\n\t" \
  "v_cvt_pk_bf16_f32 v196, v192, v193\n\t" \
  "v_cvt_pk_bf16_f32 v197, v194, v195\n\t" \
  "v_bfi_b32 v198, v46, 0, v196\n\t" \
  "v_bfi_b32 v199, v47, 0, v197\n\t" \
  "v_rcp_f32 v104, v200\n\t" \
  "v_and_b32 v196, v46, v196\n\t" \
  "v_and_b32 v197, v47, v197\n\t" \
  "v_cvt_pk_bf16_f32 v80, v220, v221\n\t" \
  "v_cvt_pk_bf16_f32 v81, v222, v223\n\t" \
  "v_mfma_f32_16x16x32_bf16 v[192:195], v[48:51], v[196:199], 0\n\t" \
  "v_pk_mul_f32 v[168:169], v[168:169], v[104:105] op_sel_hi:[1,0]\n\t" \
  "v_pk_mul_f32 v[170:171], v[170:171], v[104:105] op_sel_hi:[1,0]\n\t" \
  "v_mfma_f32_16x16x32_bf16 v[160:163], v[68:71], v[196:199], 0\n\t" \
  "v_pk_mul_f32 v[172:173], v[172:173], v[104:105] op_sel_hi:[1,0]\n\t" \
  "v_pk_mul_f32 v[174:175], v[174:175], v[104:105] op_sel_hi:[1,0]\n\t" \
  "v_mfma_f32_16x16x32_bf16 v[164:167], v[78:81], v[196:199], 0\n\t" \
  "v_cvt_pk_bf16_f32 v232, v168, v169\n\t" \
  "v_cvt_pk_bf16_f32 v233, v170, v171\n\t" \
  "v_cvt_pk_bf16_f32 v234, v172, v173\n\t" \
  "v_cvt_pk_bf16_f32 v235, v174, v175\n\t" \
  "global_store_dwordx4 v1, v[232:235], %14 offset:-4096 sc1\n\t" \
  "v_fma_f32 v184, v184, %16, v100\n\t" \
  "v_fma_f32 v185, v185, %16, v100\n\t" \
  "v_fma_f32 v186, v186, %16, v100\n\t" \
  "v_fma_f32 v187, v187, %16, v100\n\t" \
  "v_exp_f32 v184, v184\n\t" \
  "v_exp_f32 v185, v185\n\t" \
  "v_exp_f32 v186, v186\n\t" \
  "v_exp_f32 v187, v187\n\t" \
  "v_cvt_pk_bf16_f32 v188, v184, v185\n\t" \
  "v_cvt_pk_bf16_f32 v189, v186, v187\n\t" \
  "v_bfi_b32 v190, v46, 0, v188\n\t" \
  "v_bfi_b32 v191, v47, 0, v189\n\t" \
  "v_rcp_f32 v102, v192\n\t" \
  "v_and_b32 v188, v46, v188\n\t" \
  "v_and_b32 v189, v47, v189\n\t" \
  "v_pk_mul_f32 v[160:161], v[160:161], v[102:103] op_sel_hi:[1,0]\n\t" \
  "v_pk_mul_f32 v[162:163], v[162:163], v[102:103] op_sel_hi:[1,0]\n\t" \
  "v_mfma_f32_16x16x32_bf16 v[184:187], v[48:51], v[188:191], 0\n\t" \
  "v_pk_mul_f32 v[164:165], v[164:165], v[102:103] op_sel_hi:[1,0]\n\t" \
  "v_pk_mul_f32 v[166:167], v[166:167], v[102:103] op_sel_hi:[1,0]\n\t" \
  "v_mfma_f32_16x16x32_bf16 v[152:155], v[70:73], v[188:191], 0\n\t" \
  "v_cvt_pk_bf16_f32 v224, v160, v161\n\t" \
  "v_cvt_pk_bf16_f32 v225, v162, v163\n\t" \
  "v_mfma_f32_16x16x32_bf16 v[156:159], v[80:83], v[188:191], 0\n\t" \
  "v_cvt_pk_bf16_f32 v226, v164, v165\n\t" \
  "v_cvt_pk_bf16_f32 v227, v166, v167\n\t" \
  "global_store_dwordx4 v4, v[224:227], %14 offset:0 sc1\n\t" \
  "v_rcp_f32 v100, v184\n\t" \
  "ds_read_b128 v[60:63], v56 offset:64\n\t" \
  "ds_read_b64_tr_b16 v[108:109], v5 offset:0\n\t" \
  "ds_read_b64_tr_b16 v[110:111], v5 offset:16\n\t" \
  "ds_read_b128 v[112:115], v6 offset:16384\n\t" \
  "ds_read_b128 v[116:119], v6 offset:15360\n\t" \
  "ds_read_b64_tr_b16 v[120:121], v5 offset:512\n\t" \
  "ds_read_b64_tr_b16 v[122:123], v5 offset:528\n\t" \
  "ds_read_b128 v[124:127], v6 offset:14336\n\t" \
  "ds_read_b64_tr_b16 v[128:129], v5 offset:1024\n\t" \
  "ds_read_b64_tr_b16 v[130:131], v5 offset:1040\n\t" \
  "ds_read_b128 v[132:135], v6 offset:13312\n\t" \
  "ds_read_b64_tr_b16 v[136:137], v5 offset:1536\n\t" \
  "ds_read_b64_tr_b16 v[138:139], v5 offset:1552\n\t" \
  "ds_read_b128 v[140:143], v6 offset:12288\n\t" \
  "ds_read_b64_tr_b16 v[144:145], v5 offset:2048\n\t" \
  "v_pk_mul_f32 v[152:153], v[152:153], v[100:101] op_sel_hi:[1,0]\n\t" \
  "v_pk_mul_f32 v[154:155], v[154:155], v[100:101] op_sel_hi:[1,0]\n\t" \
  "v_cvt_pk_bf16_f32 v216, v152, v153\n\t" \
  "v_pk_mul_f32 v[156:157], v[156:157], v[100:101] op_sel_hi:[1,0]\n\t" \
  "v_pk_mul_f32 v[158:159], v[158:159], v[100:101] op_sel_hi:[1,0]\n\t" \
  "v_cvt_pk_bf16_f32 v217, v154, v155\n\t" \
  "v_cvt_pk_bf16_f32 v218, v156, v157\n\t" \
  "v_cvt_pk_bf16_f32 v219, v158, v159\n\t" \
  "global_store_dwordx4 v4, v[216:219], %14 offset:-4096 sc1\n\t" \
  "ds_write_b32 v32, v0 offset:112\n\t" \
  "s_branch 9f\n\t" \
  "5:\n\t" \
  "ds_read_b32 v52, v33 offset:48\n\t" \
  "ds_read_b128 v[10:13], v31\n\t" \
  "s_waitcnt lgkmcnt(0)\n\t" \
  "v_readfirstlane_b32 vcc_lo, v52\n\t" \
  "s_nop 1\n\t" \
  "s_cmp_eq_u32 vcc_lo, 0\n\t" \
  "s_cbranch_scc1 5b\n\t" \
  "s_branch 4b\n\t" \
  "15:\n\t" \
  "ds_read_b128 v[10:13], v14 offset:128\n\t" \
  "s_waitcnt lgkmcnt(0)\n\t" \
  "v_and_b32 v10, v10, v11\n\t" \
  "v_and_b32 v12, v12, v13\n\t" \
  "v_and_b32 v10, v10, v12\n\t" \
  "s_nop 1\n\t" \
  "v_readfirstlane_b32 vcc_lo, v10\n\t" \
  "s_nop 1\n\t" \
  "s_cmp_eq_u32 vcc_lo, 0\n\t" \
  "s_cbranch_scc1 15b\n\t" \
  "s_branch 14b\n\t" \
  "8:\n\t" \
  "global_load_dwordx2 v[10:11], v51, %11\n\t" \
  "global_load_dwordx2 v[12:13], v51, %11 offset:128\n\t" \
  "v_xor_b32 v53, 64, v52\n\t" \
  "v_xor_b32 v55, 32, v54\n\t" \
  "v_add_u32 v56, 43008, v52\n\t" \
  "v_add_u32 v58, 43008, v54\n\t" \
  "v_xor_b32 v57, 64, v56\n\t" \
  "v_xor_b32 v59, 32, v58\n\t" \
  "v_add_u32 v60, 86016, v52\n\t" \
  "v_add_u32 v62, 86016, v54\n\t" \
  "v_xor_b32 v61, 64, v60\n\t" \
  "v_xor_b32 v63, 32, v62\n\t" \
  "s_mov_b32 vcc_hi, 0x05040100\n\t" \
  "s_waitcnt vmcnt(2)\n\t" \
  "s_barrier\n\t" \
  "ds_read_b64_tr_b16 v[64:65], v62 offset:34816\n\t" \
  "ds_read_b64_tr_b16 v[66:67], v62 offset:35328\n\t" \
  "ds_read_b128 v[68:71], v60 offset:2048\n\t" \
  "ds_read_b64_tr_b16 v[72:73], v62 offset:38912\n\t" \
  "ds_read_b64_tr_b16 v[74:75], v62 offset:39424\n\t" \
  "ds_read_b128 v[76:79], v61 offset:2048\n\t" \
  "ds_read_b128 v[80:83], v61 offset:4096\n\t" \
  "ds_read_b128 v[84:87], v60 offset:4096\n\t" \
  "ds_read_b128 v[88:91], v60 offset:6144\n\t" \
  "ds_read_b128 v[92:95], v61 offset:6144\n\t" \
  "ds_read_b128 v[96:99], v61 offset:8192\n\t" \
  "ds_read_b128 v[100:103], v60 offset:8192\n\t" \
  "ds_read_b64_tr_b16 v[104:105], v63 offset:34816\n\t" \
  "ds_read_b64_tr_b16 v[106:107], v63 offset:35328\n\t" \
  "ds_read_b64_tr_b16 v[108:109], v63 offset:38912\n\t" \
  "ds_read_b64_tr_b16 v[110:111], v63 offset:39424\n\t" \
  "s_waitcnt lgkmcnt(13)\n\t" \
  "v_mfma_f32_16x16x32_bf16 v[216:219], v[68:71], v[64:67], 0\n\t" \
  "ds_read_b64_tr_b16 v[112:113], v62 offset:26624\n\t" \
  "ds_read_b64_tr_b16 v[114:115], v62 offset:27136\n\t" \
  "s_waitcnt lgkmcnt(12)\n\t" \
  "v_mfma_f32_16x16x32_bf16 v[216:219], v[76:79], v[72:75], v[216:219]\n\t" \
  "ds_read_b64_tr_b16 v[116:117], v62 offset:30720\n\t" \
  "ds_read_b64_tr_b16 v[118:119], v62 offset:31232\n\t" \
  "s_waitcnt lgkmcnt(13)\n\t" \
  "v_mfma_f32_16x16x32_bf16 v[224:227], v[80:83], v[72:75], 0\n\t" \
  "ds_read_b128 v[120:123], v60 offset:0\n\t" \
  "s_waitcnt lgkmcnt(13)\n\t" \
  "v_mfma_f32_16x16x32_bf16 v[224:227], v[84:87], v[64:67], v[224:227]\n\t" \
  "ds_read_b128 v[124:127], v61 offset:0\n\t" \
  "s_waitcnt lgkmcnt(13)\n\t" \
  "v_mfma_f32_16x16x32_bf16 v[232:235], v[88:91], v[64:67], 0\n\t" \
  "ds_read_b64_tr_b16 v[128:129], v63 offset:26624\n\t" \
  "s_waitcnt lgkmcnt(13)\n\t" \
  "v_mfma_f32_16x16x32_bf16 v[232:235], v[92:95], v[72:75], v[232:235]\n\t" \
  "ds_read_b64_tr_b16 v[130:131], v63 offset:27136\n\t" \
  "s_waitcnt lgkmcnt(13)\n\t" \
  "v_mfma_f32_16x16x32_bf16 v[240:243], v[96:99], v[72:75], 0\n\t" \
  "ds_read_b64_tr_b16 v[132:133], v63 offset:30720\n\t" \
  "s_waitcnt lgkmcnt(9)\n\t" \
  "v_mfma_f32_16x16x32_bf16 v[240:243], v[100:103], v[64:67], v[240:243]\n\t" \
  "ds_read_b64_tr_b16 v[134:135], v63 offset:31232\n\t" \
  "v_mfma_f32_16x16x32_bf16 v[220:223], v[68:71], v[104:107], 0\n\t" \
  "ds_read_b64_tr_b16 v[136:137], v62 offset:18432\n\t" \
  "v_mfma_f32_16x16x32_bf16 v[220:223], v[76:79], v[108:111], v[220:223]\n\t" \
  "ds_read_b64_tr_b16 v[138:139], v62 offset:18944\n\t" \
  "v_mfma_f32_16x16x32_bf16 v[228:231], v[80:83], v[108:111], 0\n\t" \
  "ds_read_b64_tr_b16 v[140:141], v62 offset:22528\n\t" \
  "v_mfma_f32_16x16x32_bf16 v[228:231], v[84:87], v[104:107], v[228:231]\n\t" \
  "ds_read_b64_tr_b16 v[142:143], v62 offset:23040\n\t" \
  "v_mfma_f32_16x16x32_bf16 v[236:239], v[88:91], v[104:107], 0\n\t" \
  "ds_read_b64_tr_b16 v[144:145], v63 offset:18432\n\t" \
  "v_mfma_f32_16x16x32_bf16 v[236:239], v[92:95], v[108:111], v[236:239]\n\t" \
  "ds_read_b64_tr_b16 v[146:147], v63 offset:18944\n\t" \
  "v_mfma_f32_16x16x32_bf16 v[244:247], v[96:99], v[108:111], 0\n\t" \
  "ds_read_b64_tr_b16 v[148:149], v63 offset:22528\n\t" \
  "v_mfma_f32_16x16x32_bf16 v[244:247], v[100:103], v[104:107], v[244:247]\n\t" \
  "ds_read_b64_tr_b16 v[150:151], v63 offset:23040\n\t" \
  "s_waitcnt lgkmcnt(7)\n\t" \
  "v_mfma_f32_16x16x32_bf16 v[184:187], v[112:115], v[68:71], 0\n\t" \
  "v_mfma_f32_16x16x32_bf16 v[184:187], v[116:119], v[76:79], v[184:187]\n\t" \
  "v_mfma_f32_16x16x32_bf16 v[192:195], v[116:119], v[80:83], 0\n\t" \
  "v_mfma_f32_16x16x32_bf16 v[192:195], v[112:115], v[84:87], v[192:195]\n\t" \
  "v_mfma_f32_16x16x32_bf16 v[200:203], v[112:115], v[88:91], 0\n\t" \
  "v_mfma_f32_16x16x32_bf16 v[200:203], v[116:119], v[92:95], v[200:203]\n\t" \
  "v_mfma_f32_16x16x32_bf16 v[208:211], v[116:119], v[96:99], 0\n\t" \
  "v_mfma_f32_16x16x32_bf16 v[208:211], v[112:115], v[100:103], v[208:211]\n\t" \
  "v_mfma_f32_16x16x32_bf16 v[248:251], v[112:115], v[120:123], 0\n\t" \
  "v_mfma_f32_16x16x32_bf16 v[248:251], v[116:119], v[124:127], v[248:251]\n\t" \
  "v_mfma_f32_16x16x32_bf16 v[252:255], v[128:131], v[120:123], 0\n\t" \
  "v_mfma_f32_16x16x32_bf16 v[252:255], v[132:135], v[124:127], v[252:255]\n\t" \
  "v_mfma_f32_16x16x32_bf16 v[188:191], v[132:135], v[76:79], 0\n\t" \
  "v_mfma_f32_16x16x32_bf16 v[188:191], v[128:131], v[68:71], v[188:191]\n\t" \
  "s_waitcnt lgkmcnt(0)\n\t" \
  "s_getpc_b64 s[88:89]\n\t" \
  "s_branch 28b\n\t" \
  "s_barrier\n\t" \
  "v_mfma_f32_16x16x32_bf16 v[196:199], v[128:131], v[84:87], 0\n\t" \
  "ds_read_b64_tr_b16 v[72:73], v54 offset:34816\n\t" \
  "v_mfma_f32_16x16x32_bf16 v[196:199], v[132:135], v[80:83], v[196:199]\n\t" \
  "ds_read_b64_tr_b16 v[74:75], v54 offset:35328\n\t" \
  "v_mfma_f32_16x16x32_bf16 v[204:207], v[132:135], v[92:95], 0\n\t" \
  "ds_read_b128 v[64:67], v52 offset:2048\n\t" \
  "v_mfma_f32_16x16x32_bf16 v[204:207], v[128:131], v[88:91], v[204:207]\n\t" \
  "ds_read_b64_tr_b16 v[108:109], v54 offset:38912\n\t" \
  "v_mfma_f32_16x16x32_bf16 v[212:215], v[128:131], v[100:103], 0\n\t" \
  "ds_read_b64_tr_b16 v[110:111], v54 offset:39424\n\t" \
  "v_mfma_f32_16x16x32_bf16 v[212:215], v[132:135], v[96:99], v[212:215]\n\t" \
  "ds_read_b128 v[104:107], v53 offset:2048\n\t" \
  "v_mfma_f32_16x16x32_bf16 v[152:155], v[136:139], v[68:71], 0\n\t" \
  "ds_read_b128 v[112:115], v53 offset:4096\n\t" \
  "v_mfma_f32_16x16x32_bf16 v[152:155], v[140:143], v[76:79], v[152:155]\n\t" \
  "ds_read_b128 v[116:119], v52 offset:4096\n\t" \
  "v_mfma_f32_16x16x32_bf16 v[160:163], v[140:143], v[80:83], 0\n\t" \
  "ds_read_b128 v[120:123], v52 offset:6144\n\t" \
  "v_mfma_f32_16x16x32_bf16 v[160:163], v[136:139], v[84:87], v[160:163]\n\t" \
  "ds_read_b128 v[124:127], v53 offset:6144\n\t" \
  "v_mfma_f32_16x16x32_bf16 v[168:171], v[136:139], v[88:91], 0\n\t" \
  "ds_read_b128 v[128:131], v53 offset:8192\n\t" \
  "v_mfma_f32_16x16x32_bf16 v[168:171], v[140:143], v[92:95], v[168:171]\n\t" \
  "ds_read_b128 v[132:135], v52 offset:8192\n\t" \
  "v_mfma_f32_16x16x32_bf16 v[176:179], v[140:143], v[96:99], 0\n\t" \
  "ds_read_b64_tr_b16 v[140:141], v55 offset:34816\n\t" \
  "v_mfma_f32_16x16x32_bf16 v[176:179], v[136:139], v[100:103], v[176:179]\n\t" \
  "ds_read_b64_tr_b16 v[142:143], v55 offset:35328\n\t" \
  "v_mfma_f32_16x16x32_bf16 v[156:159], v[144:147], v[68:71], 0\n\t" \
  "ds_read_b64_tr_b16 v[136:137], v55 offset:38912\n\t" \
  "v_mfma_f32_16x16x32_bf16 v[156:159], v[148:151], v[76:79], v[156:159]\n\t" \
  "ds_read_b64_tr_b16 v[138:139], v55 offset:39424\n\t" \
  "v_mfma_f32_16x16x32_bf16 v[164:167], v[148:151], v[80:83], 0\n\t" \
  "ds_read_b64_tr_b16 v[68:69], v54 offset:26624\n\t" \
  "v_mfma_f32_16x16x32_bf16 v[164:167], v[144:147], v[84:87], v[164:167]\n\t" \
  "ds_read_b64_tr_b16 v[70:71], v54 offset:27136\n\t" \
  "v_mfma_f32_16x16x32_bf16 v[172:175], v[144:147], v[88:91], 0\n\t" \
  "ds_read_b64_tr_b16 v[76:77], v54 offset:30720\n\t" \
  "v_mfma_f32_16x16x32_bf16 v[172:175], v[148:151], v[92:95], v[172:175]\n\t" \
  "ds_read_b64_tr_b16 v[78:79], v54 offset:31232\n\t" \
  "v_mfma_f32_16x16x32_bf16 v[180:183], v[148:151], v[96:99], 0\n\t" \
  "ds_read_b128 v[80:83], v52 offset:0\n\t" \
  "v_mfma_f32_16x16x32_bf16 v[180:183], v[144:147], v[100:103], v[180:183]\n\t" \
  "ds_read_b128 v[84:87], v53 offset:0\n\t" \
  "s_waitcnt lgkmcnt(7)\n\t" \
  "v_mfma_f32_16x16x32_bf16 v[216:219], v[64:67], v[72:75], v[216:219]\n\t" \
  "ds_read_b64_tr_b16 v[88:89], v55 offset:26624\n\t" \
  "v_mfma_f32_16x16x32_bf16 v[216:219], v[104:107], v[108:111], v[216:219]\n\t" \
  "ds_read_b64_tr_b16 v[90:91], v55 offset:27136\n\t" \
  "v_mfma_f32_16x16x32_bf16 v[224:227], v[112:115], v[108:111], v[224:227]\n\t" \
  "ds_read_b64_tr_b16 v[92:93], v55 offset:30720\n\t" \
  "v_mfma_f32_16x16x32_bf16 v[224:227], v[116:119], v[72:75], v[224:227]\n\t" \
  "ds_read_b64_tr_b16 v[94:95], v55 offset:31232\n\t" \
  "v_mfma_f32_16x16x32_bf16 v[232:235], v[120:123], v[72:75], v[232:235]\n\t" \
  "ds_read_b64_tr_b16 v[96:97], v54 offset:18432\n\t" \
  "v_mfma_f32_16x16x32_bf16 v[232:235], v[124:127], v[108:111], v[232:235]\n\t" \
  "ds_read_b64_tr_b16 v[98:99], v54 offset:18944\n\t" \
  "v_mfma_f32_16x16x32_bf16 v[240:243], v[128:131], v[108:111], v[240:243]\n\t" \
  "ds_read_b64_tr_b16 v[148:149], v54 offset:22528\n\t" \
  "v_mfma_f32_16x16x32_bf16 v[240:243], v[132:135], v[72:75], v[240:243]\n\t" \
  "ds_read_b64_tr_b16 v[150:151], v54 offset:23040\n\t" \
  "v_mfma_f32_16x16x32_bf16 v[220:223], v[64:67], v[140:143], v[220:223]\n\t" \
  "ds_read_b64_tr_b16 v[100:101], v55 offset:18432\n\t" \
  "s_waitcnt lgkmcnt(7)\n\t" \
  "v_mfma_f32_16x16x32_bf16 v[220:223], v[104:107], v[136:139], v[220:223]\n\t" \
  "ds_read_b64_tr_b16 v[102:103], v55 offset:18944\n\t" \
  "v_mfma_f32_16x16x32_bf16 v[228:231], v[112:115], v[136:139], v[228:231]\n\t" \
  "ds_read_b64_tr_b16 v[144:145], v55 offset:22528\n\t" \
  "v_mfma_f32_16x16x32_bf16 v[228:231], v[116:119], v[140:143], v[228:231]\n\t" \
  "ds_read_b64_tr_b16 v[146:147], v55 offset:23040\n\t" \
  "v_mfma_f32_16x16x32_bf16 v[236:239], v[120:123], v[140:143], v[236:239]\n\t" \
  "v_mfma_f32_16x16x32_bf16 v[236:239], v[124:127], v[136:139], v[236:239]\n\t" \
  "v_mfma_f32_16x16x32_bf16 v[244:247], v[128:131], v[136:139], v[244:247]\n\t" \
  "v_mfma_f32_16x16x32_bf16 v[244:247], v[132:135], v[140:143], v[244:247]\n\t" \
  "v_mfma_f32_16x16x32_bf16 v[184:187], v[68:71], v[64:67], v[184:187]\n\t" \
  "v_mfma_f32_16x16x32_bf16 v[184:187], v[76:79], v[104:107], v[184:187]\n\t" \
  "v_mfma_f32_16x16x32_bf16 v[192:195], v[76:79], v[112:115], v[192:195]\n\t" \
  "v_mfma_f32_16x16x32_bf16 v[192:195], v[68:71], v[116:119], v[192:195]\n\t" \
  "v_mfma_f32_16x16x32_bf16 v[200:203], v[68:71], v[120:123], v[200:203]\n\t" \
  "v_mfma_f32_16x16x32_bf16 v[200:203], v[76:79], v[124:127], v[200:203]\n\t" \
  "v_mfma_f32_16x16x32_bf16 v[208:211], v[76:79], v[128:131], v[208:211]\n\t" \
  "v_mfma_f32_16x16x32_bf16 v[208:211], v[68:71], v[132:135], v[208:211]\n\t" \
  "v_mfma_f32_16x16x32_bf16 v[248:251], v[68:71], v[80:83], v[248:251]\n\t" \
  "v_mfma_f32_16x16x32_bf16 v[248:251], v[76:79], v[84:87], v[248:251]\n\t" \
  "v_mfma_f32_16x16x32_bf16 v[252:255], v[88:91], v[80:83], v[252:255]\n\t" \
  "s_waitcnt lgkmcnt(0)\n\t" \
  "v_mfma_f32_16x16x32_bf16 v[252:255], v[92:95], v[84:87], v[252:255]\n\t" \
  "v_mfma_f32_16x16x32_bf16 v[188:191], v[92:95], v[104:107], v[188:191]\n\t" \
  "v_mfma_f32_16x16x32_bf16 v[188:191], v[88:91], v[64:67], v[188:191]\n\t" \
  "s_barrier\n\t" \
  "v_mfma_f32_16x16x32_bf16 v[196:199], v[88:91], v[116:119], v[196:199]\n\t" \
  "ds_read_b64_tr_b16 v[108:109], v58 offset:34816\n\t" \
  "v_mfma_f32_16x16x32_bf16 v[196:199], v[92:95], v[112:115], v[196:199]\n\t" \
  "ds_read_b64_tr_b16 v[110:111], v58 offset:35328\n\t" \
  "v_mfma_f32_16x16x32_bf16 v[204:207], v[92:95], v[124:127], v[204:207]\n\t" \
  "ds_read_b128 v[72:75], v56 offset:2048\n\t" \
  "v_mfma_f32_16x16x32_bf16 v[204:207], v[88:91], v[120:123], v[204:207]\n\t" \
  "ds_read_b64_tr_b16 v[136:137], v58 offset:38912\n\t" \
  "v_mfma_f32_16x16x32_bf16 v[212:215], v[88:91], v[132:135], v[212:215]\n\t" \
  "ds_read_b64_tr_b16 v[138:139], v58 offset:39424\n\t" \
  "v_mfma_f32_16x16x32_bf16 v[212:215], v[92:95], v[128:131], v[212:215]\n\t" \
  "ds_read_b128 v[140:143], v57 offset:2048\n\t" \
  "v_mfma_f32_16x16x32_bf16 v[152:155], v[96:99], v[64:67], v[152:155]\n\t" \
  "ds_read_b128 v[68:71], v57 offset:4096\n\t" \
  "v_mfma_f32_16x16x32_bf16 v[152:155], v[148:151], v[104:107], v[152:155]\n\t" \
  "ds_read_b128 v[76:79], v56 offset:4096\n\t" \
  "v_mfma_f32_16x16x32_bf16 v[160:163], v[148:151], v[112:115], v[160:163]\n\t" \
  "ds_read_b128 v[80:83], v56 offset:6144\n\t" \
  "v_mfma_f32_16x16x32_bf16 v[160:163], v[96:99], v[116:119], v[160:163]\n\t" \
  "ds_read_b128 v[84:87], v57 offset:6144\n\t" \
  "v_mfma_f32_16x16x32_bf16 v[168:171], v[96:99], v[120:123], v[168:171]\n\t" \
  "ds_read_b128 v[88:91], v57 offset:8192\n\t" \
  "v_mfma_f32_16x16x32_bf16 v[168:171], v[148:151], v[124:127], v[168:171]\n\t" \
  "ds_read_b128 v[92:95], v56 offset:8192\n\t" \
  "v_mfma_f32_16x16x32_bf16 v[176:179], v[148:151], v[128:131], v[176:179]\n\t" \
  "ds_read_b64_tr_b16 v[148:149], v59 offset:34816\n\t" \
  "v_mfma_f32_16x16x32_bf16 v[176:179], v[96:99], v[132:135], v[176:179]\n\t" \
  "ds_read_b64_tr_b16 v[150:151], v59 offset:35328\n\t" \
  "v_mfma_f32_16x16x32_bf16 v[156:159], v[100:103], v[64:67], v[156:159]\n\t" \
  "ds_read_b64_tr_b16 v[96:97], v59 offset:38912\n\t" \
  "v_mfma_f32_16x16x32_bf16 v[156:159], v[144:147], v[104:107], v[156:159]\n\t" \
  "ds_read_b64_tr_b16 v[98:99], v59 offset:39424\n\t" \
  "v_mfma_f32_16x16x32_bf16 v[164:167], v[144:147], v[112:115], v[164:167]\n\t" \
  "ds_read_b64_tr_b16 v[64:65], v58 offset:26624\n\t" \
  "v_mfma_f32_16x16x32_bf16 v[164:167], v[100:103], v[116:119], v[164:167]\n\t" \
  "ds_read_b64_tr_b16 v[66:67], v58 offset:27136\n\t" \
  "v_mfma_f32_16x16x32_bf16 v[172:175], v[100:103], v[120:123], v[172:175]\n\t" \
  "ds_read_b64_tr_b16 v[104:105], v58 offset:30720\n\t" \
  "v_mfma_f32_16x16x32_bf16 v[172:175], v[144:147], v[124:127], v[172:175]\n\t" \
  "ds_read_b64_tr_b16 v[106:107], v58 offset:31232\n\t" \
  "v_mfma_f32_16x16x32_bf16 v[180:183], v[144:147], v[128:131], v[180:183]\n\t" \
  "ds_read_b128 v[112:115], v56 offset:0\n\t" \
  "v_mfma_f32_16x16x32_bf16 v[180:183], v[100:103], v[132:135], v[180:183]\n\t" \
  "ds_read_b128 v[116:119], v57 offset:0\n\t" \
  "s_waitcnt lgkmcnt(7)\n\t" \
  "v_mfma_f32_16x16x32_bf16 v[216:219], v[72:75], v[108:111], v[216:219]\n\t" \
  "ds_read_b64_tr_b16 v[120:121], v59 offset:26624\n\t" \
  "v_mfma_f32_16x16x32_bf16 v[216:219], v[140:143], v[136:139], v[216:219]\n\t" \
  "ds_read_b64_tr_b16 v[122:123], v59 offset:27136\n\t" \
  "v_mfma_f32_16x16x32_bf16 v[224:227], v[68:71], v[136:139], v[224:227]\n\t" \
  "ds_read_b64_tr_b16 v[124:125], v59 offset:30720\n\t" \
  "v_mfma_f32_16x16x32_bf16 v[224:227], v[76:79], v[108:111], v[224:227]\n\t" \
  "ds_read_b64_tr_b16 v[126:127], v59 offset:31232\n\t" \
  "v_mfma_f32_16x16x32_bf16 v[232:235], v[80:83], v[108:111], v[232:235]\n\t" \
  "ds_read_b64_tr_b16 v[128:129], v58 offset:18432\n\t" \
  "v_mfma_f32_16x16x32_bf16 v[232:235], v[84:87], v[136:139], v[232:235]\n\t" \
  "ds_read_b64_tr_b16 v[130:131], v58 offset:18944\n\t" \
  "v_mfma_f32_16x16x32_bf16 v[240:243], v[88:91], v[136:139], v[240:243]\n\t" \
  "ds_read_b64_tr_b16 v[144:145], v58 offset:22528\n\t" \
  "v_mfma_f32_16x16x32_bf16 v[240:243], v[92:95], v[108:111], v[240:243]\n\t" \
  "ds_read_b64_tr_b16 v[146:147], v58 offset:23040\n\t" \
  "v_mfma_f32_16x16x32_bf16 v[220:223], v[72:75], v[148:151], v[220:223]\n\t" \
  "ds_read_b64_tr_b16 v[100:101], v59 offset:18432\n\t" \
  "s_waitcnt lgkmcnt(7)\n\t" \
  "v_mfma_f32_16x16x32_bf16 v[220:223], v[140:143], v[96:99], v[220:223]\n\t" \
  "ds_read_b64_tr_b16 v[102:103], v59 offset:18944\n\t" \
  "v_mfma_f32_16x16x32_bf16 v[228:231], v[68:71], v[96:99], v[228:231]\n\t" \
  "ds_read_b64_tr_b16 v[132:133], v59 offset:22528\n\t" \
  "v_mfma_f32_16x16x32_bf16 v[228:231], v[76:79], v[148:151], v[228:231]\n\t" \
  "ds_read_b64_tr_b16 v[134:135], v59 offset:23040\n\t" \
  "v_mfma_f32_16x16x32_bf16 v[236:239], v[80:83], v[148:151], v[236:239]\n\t" \
  "v_mfma_f32_16x16x32_bf16 v[236:239], v[84:87], v[96:99], v[236:239]\n\t" \
  "v_mfma_f32_16x16x32_bf16 v[244:247], v[88:91], v[96:99], v[244:247]\n\t" \
  "v_mfma_f32_16x16x32_bf16 v[244:247], v[92:95], v[148:151], v[244:247]\n\t" \
  "503:\n\t" \
  "s_cbranch_execz 507f\n\t" \
  "v_mfma_f32_16x16x32_bf16 v[184:187], v[64:67], v[72:75], v[184:187]\n\t" \
  "v_mfma_f32_16x16x32_bf16 v[184:187], v[104:107], v[140:143], v[184:187]\n\t" \
  "v_mfma_f32_16x16x32_bf16 v[192:195], v[104:107], v[68:71], v[192:195]\n\t" \
  "v_mfma_f32_16x16x32_bf16 v[192:195], v[64:67], v[76:79], v[192:195]\n\t" \
  "v_mfma_f32_16x16x32_bf16 v[200:203], v[64:67], v[80:83], v[200:203]\n\t" \
  "v_mfma_f32_16x16x32_bf16 v[200:203], v[104:107], v[84:87], v[200:203]\n\t" \
  "v_mfma_f32_16x16x32_bf16 v[208:211], v[104:107], v[88:91], v[208:211]\n\t" \
  "v_mfma_f32_16x16x32_bf16 v[208:211], v[64:67], v[92:95], v[208:211]\n\t" \
  "v_mfma_f32_16x16x32_bf16 v[248:251], v[64:67], v[112:115], v[248:251]\n\t" \
  "v_mfma_f32_16x16x32_bf16 v[248:251], v[104:107], v[116:119], v[248:251]\n\t" \
  "v_mfma_f32_16x16x32_bf16 v[252:255], v[120:123], v[112:115], v[252:255]\n\t" \
  "s_waitcnt lgkmcnt(0)\n\t" \
  "v_mfma_f32_16x16x32_bf16 v[252:255], v[124:127], v[116:119], v[252:255]\n\t" \
  "v_mfma_f32_16x16x32_bf16 v[188:191], v[124:127], v[140:143], v[188:191]\n\t" \
  "v_mfma_f32_16x16x32_bf16 v[188:191], v[120:123], v[72:75], v[188:191]\n\t" \
  "s_barrier\n\t" \
  "v_mfma_f32_16x16x32_bf16 v[196:199], v[120:123], v[76:79], v[196:199]\n\t" \
  "ds_read_b64_tr_b16 v[136:137], v62 offset:34816\n\t" \
  "v_bfe_u32 v30, v4, 9, 1\n\t" \
  "v_mfma_f32_16x16x32_bf16 v[196:199], v[124:127], v[68:71], v[196:199]\n\t" \
  "ds_read_b64_tr_b16 v[138:139], v62 offset:35328\n\t" \
  "v_mad_i32_i24 v30, v30, -16, v0\n\t" \
  "v_mfma_f32_16x16x32_bf16 v[204:207], v[124:127], v[84:87], v[204:207]\n\t" \
  "ds_read_b128 v[108:111], v60 offset:2048\n\t" \
  "ds_read_b128 v[26:29], v0 offset:1552\n\t" \
  "v_mfma_f32_16x16x32_bf16 v[204:207], v[120:123], v[80:83], v[204:207]\n\t" \
  "ds_read_b64_tr_b16 v[96:97], v62 offset:38912\n\t" \
  "ds_read_b128 v[30:33], v30 offset:1040\n\t" \
  "v_mfma_f32_16x16x32_bf16 v[212:215], v[120:123], v[92:95], v[212:215]\n\t" \
  "ds_read_b64_tr_b16 v[98:99], v62 offset:39424\n\t" \
  "ds_read_b128 v[34:37], v0 offset:512\n\t" \
  "v_mfma_f32_16x16x32_bf16 v[212:215], v[124:127], v[88:91], v[212:215]\n\t" \
  "ds_read_b128 v[148:151], v61 offset:2048\n\t" \
  "ds_read_b64_tr_b16 v[38:39], v1 offset:512\n\t" \
  "v_mfma_f32_16x16x32_bf16 v[152:155], v[128:131], v[72:75], v[152:155]\n\t" \
  "ds_read_b128 v[64:67], v61 offset:4096\n\t" \
  "ds_read_b64_tr_b16 v[40:41], v1 offset:0\n\t" \
  "v_mfma_f32_16x16x32_bf16 v[152:155], v[144:147], v[140:143], v[152:155]\n\t" \
  "ds_read_b128 v[104:107], v60 offset:4096\n\t" \
  "s_waitcnt vmcnt(0)\n\t" \
  "v_mfma_f32_16x16x32_bf16 v[160:163], v[144:147], v[68:71], v[160:163]\n\t" \
  "ds_read_b128 v[112:115], v60 offset:6144\n\t" \
  "v_perm_b32 v18, v10, v10, v14\n\t" \
  "v_mfma_f32_16x16x32_bf16 v[160:163], v[128:131], v[76:79], v[160:163]\n\t" \
  "ds_read_b128 v[116:119], v61 offset:6144\n\t" \
  "v_perm_b32 v19, v10, v10, v15\n\t" \
  "v_mfma_f32_16x16x32_bf16 v[168:171], v[128:131], v[80:83], v[168:171]\n\t" \
  "ds_read_b128 v[120:123], v61 offset:8192\n\t" \
  "v_perm_b32 v20, v10, v10, v16\n\t" \
  "v_mfma_f32_16x16x32_bf16 v[168:171], v[144:147], v[84:87], v[168:171]\n\t" \
  "ds_read_b128 v[124:127], v60 offset:8192\n\t" \
  "v_perm_b32 v21, v10, v10, v17\n\t" \
  "v_mfma_f32_16x16x32_bf16 v[176:179], v[144:147], v[88:91], v[176:179]\n\t" \
  "ds_read_b64_tr_b16 v[144:145], v63 offset:34816\n\t" \
  "v_perm_b32 v22, v11, v11, v14\n\t" \
  "v_mfma_f32_16x16x32_bf16 v[176:179], v[128:131], v[92:95], v[176:179]\n\t" \
  "ds_read_b64_tr_b16 v[146:147], v63 offset:35328\n\t" \
  "v_perm_b32 v23, v11, v11, v15\n\t" \
  "v_mfma_f32_16x16x32_bf16 v[156:159], v[100:103], v[72:75], v[156:159]\n\t" \
  "ds_read_b64_tr_b16 v[128:129], v63 offset:38912\n\t" \
  "v_perm_b32 v24, v11, v11, v16\n\t" \
  "v_mfma_f32_16x16x32_bf16 v[156:159], v[132:135], v[140:143], v[156:159]\n\t" \
  "ds_read_b64_tr_b16 v[130:131], v63 offset:39424\n\t" \
  "v_perm_b32 v25, v11, v11, v17\n\t" \
  "v_mfma_f32_16x16x32_bf16 v[164:167], v[132:135], v[68:71], v[164:167]\n\t" \
  "ds_read_b64_tr_b16 v[72:73], v62 offset:26624\n\t" \
  "v_mfma_f32_16x16x32_bf16 v[164:167], v[100:103], v[76:79], v[164:167]\n\t" \
  "ds_read_b64_tr_b16 v[74:75], v62 offset:27136\n\t" \
  "v_mfma_f32_16x16x32_bf16 v[172:175], v[100:103], v[80:83], v[172:175]\n\t" \
  "ds_read_b64_tr_b16 v[140:141], v62 offset:30720\n\t" \
  "s_waitcnt lgkmcnt(6)\n\t" \
  "v_mfma_f32_16x16x32_bf16 v[42:45], v[26:29], v[18:21], 0\n\t" \
  "v_mfma_f32_16x16x32_bf16 v[42:45], v[30:33], v[22:25], v[42:45]\n\t" \
  "v_mfma_f32_16x16x32_bf16 v[46:49], v[30:33], v[18:21], 0\n\t" \
  "v_mfma_f32_16x16x32_bf16 v[46:49], v[34:37], v[22:25], v[46:49]\n\t" \
  "ds_read_b128 v[26:29], v0 offset:1808\n\t" \
  "ds_read_b128 v[30:33], v0 offset:1296\n\t" \
  "v_mfma_f32_16x16x32_bf16 v[172:175], v[132:135], v[84:87], v[172:175]\n\t" \
  "ds_read_b64_tr_b16 v[142:143], v62 offset:31232\n\t" \
  "ds_read_b128 v[34:37], v0 offset:768\n\t" \
  "v_mfma_f32_16x16x32_bf16 v[180:183], v[132:135], v[88:91], v[180:183]\n\t" \
  "ds_read_b128 v[68:71], v60 offset:0\n\t" \
  "v_mfma_f32_16x16x32_bf16 v[180:183], v[100:103], v[92:95], v[180:183]\n\t" \
  "ds_read_b128 v[76:79], v61 offset:0\n\t" \
  "v_mfma_f32_16x16x32_bf16 v[216:219], v[108:111], v[136:139], v[216:219]\n\t" \
  "ds_read_b64_tr_b16 v[80:81], v63 offset:26624\n\t" \
  "v_lshlrev_b32 v50, 16, v38\n\t" \
  "v_mfma_f32_16x16x32_bf16 v[216:219], v[148:151], v[96:99], v[216:219]\n\t" \
  "ds_read_b64_tr_b16 v[82:83], v63 offset:27136\n\t" \
  "v_lshlrev_b32 v51, 16, v40\n\t" \
  "v_mfma_f32_16x16x32_bf16 v[224:227], v[64:67], v[96:99], v[224:227]\n\t" \
  "ds_read_b64_tr_b16 v[84:85], v63 offset:30720\n\t" \
  "v_mul_f32 v42, v42, v50\n\t" \
  "v_mfma_f32_16x16x32_bf16 v[224:227], v[104:107], v[136:139], v[224:227]\n\t" \
  "ds_read_b64_tr_b16 v[86:87], v63 offset:31232\n\t" \
  "v_mul_f32 v46, v46, v51\n\t" \
  "v_mfma_f32_16x16x32_bf16 v[232:235], v[112:115], v[136:139], v[232:235]\n\t" \
  "ds_read_b64_tr_b16 v[88:89], v62 offset:18432\n\t" \
  "v_cvt_pk_bf16_f32 v42, v42, v46\n\t" \
  "v_mfma_f32_16x16x32_bf16 v[232:235], v[116:119], v[96:99], v[232:235]\n\t" \
  "ds_read_b64_tr_b16 v[90:91], v62 offset:18944\n\t" \
  "v_and_b32 v50, 0xffff0000, v38\n\t" \
  "v_mfma_f32_16x16x32_bf16 v[240:243], v[120:123], v[96:99], v[240:243]\n\t" \
  "ds_read_b64_tr_b16 v[132:133], v62 offset:22528\n\t" \
  "v_and_b32 v51, 0xffff0000, v40\n\t" \
  "v_mfma_f32_16x16x32_bf16 v[240:243], v[124:127], v[136:139], v[240:243]\n\t" \
  "ds_read_b64_tr_b16 v[134:135], v62 offset:23040\n\t" \
  "v_mul_f32 v43, v43, v50\n\t" \
  "s_waitcnt lgkmcnt(7)\n\t" \
  "v_mfma_f32_16x16x32_bf16 v[220:223], v[108:111], v[144:147], v[220:223]\n\t" \
  "ds_read_b64_tr_b16 v[92:93], v63 offset:18432\n\t" \
  "v_mul_f32 v47, v47, v51\n\t" \
  "v_mfma_f32_16x16x32_bf16 v[220:223], v[148:151], v[128:131], v[220:223]\n\t" \
  "ds_read_b64_tr_b16 v[94:95], v63 offset:18944\n\t" \
  "v_cvt_pk_bf16_f32 v43, v43, v47\n\t" \
  "v_mfma_f32_16x16x32_bf16 v[228:231], v[64:67], v[128:131], v[228:231]\n\t" \
  "ds_read_b64_tr_b16 v[100:101], v63 offset:22528\n\t" \
  "v_lshlrev_b32 v50, 16, v39\n\t" \
  "v_mfma_f32_16x16x32_bf16 v[228:231], v[104:107], v[144:147], v[228:231]\n\t" \
  "ds_read_b64_tr_b16 v[102:103], v63 offset:23040\n\t" \
  "v_lshlrev_b32 v51, 16, v41\n\t" \
  "v_mfma_f32_16x16x32_bf16 v[236:239], v[112:115], v[144:147], v[236:239]\n\t" \
  "v_mul_f32 v44, v44, v50\n\t" \
  "v_mul_f32 v48, v48, v51\n\t" \
  "v_mfma_f32_16x16x32_bf16 v[236:239], v[116:119], v[128:131], v[236:239]\n\t" \
  "v_cvt_pk_bf16_f32 v44, v44, v48\n\t" \
  "v_and_b32 v50, 0xffff0000, v39\n\t" \
  "v_mfma_f32_16x16x32_bf16 v[244:247], v[120:123], v[128:131], v[244:247]\n\t" \
  "v_and_b32 v51, 0xffff0000, v41\n\t" \
  "v_mul_f32 v45, v45, v50\n\t" \
  "v_mfma_f32_16x16x32_bf16 v[244:247], v[124:127], v[144:147], v[244:247]\n\t" \
  "v_mul_f32 v49, v49, v51\n\t" \
  "ds_read_b64_tr_b16 v[38:39], v1 offset:768\n\t" \
  "v_mfma_f32_16x16x32_bf16 v[184:187], v[72:75], v[108:111], v[184:187]\n\t" \
  "ds_read_b64_tr_b16 v[40:41], v1 offset:256\n\t" \
  "v_cvt_pk_bf16_f32 v45, v45, v49\n\t" \
  "v_mfma_f32_16x16x32_bf16 v[184:187], v[140:143], v[148:151], v[184:187]\n\t" \
  "v_mfma_f32_16x16x32_bf16 v[192:195], v[140:143], v[64:67], v[192:195]\n\t" \
  "v_mfma_f32_16x16x32_bf16 v[192:195], v[72:75], v[104:107], v[192:195]\n\t" \
  "v_mfma_f32_16x16x32_bf16 v[200:203], v[72:75], v[112:115], v[200:203]\n\t" \
  "v_mfma_f32_16x16x32_bf16 v[200:203], v[140:143], v[116:119], v[200:203]\n\t" \
  "v_mfma_f32_16x16x32_bf16 v[208:211], v[140:143], v[120:123], v[208:211]\n\t" \
  "v_mfma_f32_16x16x32_bf16 v[208:211], v[72:75], v[124:127], v[208:211]\n\t" \
  "v_mfma_f32_16x16x32_bf16 v[248:251], v[72:75], v[68:71], v[248:251]\n\t" \
  "v_mfma_f32_16x16x32_bf16 v[248:251], v[140:143], v[76:79], v[248:251]\n\t" \
  "s_waitcnt lgkmcnt(0)\n\t" \
  "v_mfma_f32_16x16x32_bf16 v[252:255], v[80:83], v[68:71], v[252:255]\n\t" \
  "v_mfma_f32_16x16x32_bf16 v[252:255], v[84:87], v[76:79], v[252:255]\n\t" \
  "v_mfma_f32_16x16x32_bf16 v[188:191], v[84:87], v[148:151], v[188:191]\n\t" \
  "v_mfma_f32_16x16x32_bf16 v[188:191], v[80:83], v[108:111], v[188:191]\n\t" \
  "s_barrier\n\t" \
  "v_mfma_f32_16x16x32_bf16 v[196:199], v[80:83], v[104:107], v[196:199]\n\t" \
  "ds_read_b64_tr_b16 v[96:97], v54 offset:34816\n\t" \
  "v_mfma_f32_16x16x32_bf16 v[196:199], v[84:87], v[64:67], v[196:199]\n\t" \
  "v_mov_b32 v50, 163344\n\t" \
  "ds_read_b64_tr_b16 v[98:99], v54 offset:35328\n\t" \
  "v_mfma_f32_16x16x32_bf16 v[204:207], v[84:87], v[116:119], v[204:207]\n\t" \
  "ds_read_b32 v50, v50 offset:24\n\t" \
  "ds_read_b128 v[136:139], v52 offset:2048\n\t" \
  "v_mfma_f32_16x16x32_bf16 v[204:207], v[80:83], v[112:115], v[204:207]\n\t" \
  "ds_read_b64_tr_b16 v[128:129], v54 offset:38912\n\t" \
  "v_mfma_f32_16x16x32_bf16 v[212:215], v[80:83], v[124:127], v[212:215]\n\t" \
  "ds_read_b64_tr_b16 v[130:131], v54 offset:39424\n\t" \
  "v_mfma_f32_16x16x32_bf16 v[212:215], v[84:87], v[120:123], v[212:215]\n\t" \
  "ds_read_b128 v[144:147], v53 offset:2048\n\t" \
  "v_mfma_f32_16x16x32_bf16 v[152:155], v[88:91], v[108:111], v[152:155]\n\t" \
  "ds_read_b128 v[72:75], v53 offset:4096\n\t" \
  "v_mfma_f32_16x16x32_bf16 v[152:155], v[132:135], v[148:151], v[152:155]\n\t" \
  "ds_read_b128 v[140:143], v52 offset:4096\n\t" \
  "v_mfma_f32_16x16x32_bf16 v[160:163], v[132:135], v[64:67], v[160:163]\n\t" \
  "ds_read_b128 v[68:71], v52 offset:6144\n\t" \
  "v_mfma_f32_16x16x32_bf16 v[160:163], v[88:91], v[104:107], v[160:163]\n\t" \
  "ds_read_b128 v[76:79], v53 offset:6144\n\t" \
  "v_mfma_f32_16x16x32_bf16 v[168:171], v[88:91], v[112:115], v[168:171]\n\t" \
  "ds_read_b128 v[80:83], v53 offset:8192\n\t" \
  "v_mfma_f32_16x16x32_bf16 v[168:171], v[132:135], v[116:119], v[168:171]\n\t" \
  "ds_read_b128 v[84:87], v52 offset:8192\n\t" \
  "v_mfma_f32_16x16x32_bf16 v[176:179], v[132:135], v[120:123], v[176:179]\n\t" \
  "s_waitcnt lgkmcnt(10)\n\t" \
  "v_readfirstlane_b32 s88, v50\n\t" \
  "s_mov_b32 s89, 0\n\t" \
  "ds_read_b64_tr_b16 v[132:133], v55 offset:34816\n\t" \
  "v_mfma_f32_16x16x32_bf16 v[176:179], v[88:91], v[124:127], v[176:179]\n\t" \
  "ds_read_b64_tr_b16 v[134:135], v55 offset:35328\n\t" \
  "v_mfma_f32_16x16x32_bf16 v[156:159], v[92:95], v[108:111], v[156:159]\n\t" \
  "s_xor_b64 %24, %24, s[88:89]\n\t" \
  "ds_read_b64_tr_b16 v[88:89], v55 offset:38912\n\t" \
  "v_mfma_f32_16x16x32_bf16 v[156:159], v[100:103], v[148:151], v[156:159]\n\t" \
  "s_xor_b64 %25, %25, s[88:89]\n\t" \
  "ds_read_b64_tr_b16 v[90:91], v55 offset:39424\n\t" \
  "v_mfma_f32_16x16x32_bf16 v[164:167], v[100:103], v[64:67], v[164:167]\n\t" \
  "s_xor_b64 %14, %14, s[88:89]\n\t" \
  "ds_read_b64_tr_b16 v[108:109], v54 offset:26624\n\t" \
  "v_mfma_f32_16x16x32_bf16 v[164:167], v[92:95], v[104:107], v[164:167]\n\t" \
  "s_xor_b64 %19, %19, s[88:89]\n\t" \
  "ds_read_b64_tr_b16 v[110:111], v54 offset:27136\n\t" \
  "v_mfma_f32_16x16x32_bf16 v[172:175], v[92:95], v[112:115], v[172:175]\n\t" \
  "s_xor_b64 %21, %21, s[88:89]\n\t" \
  "ds_read_b64_tr_b16 v[148:149], v54 offset:30720\n\t" \
  "v_mfma_f32_16x16x32_bf16 v[172:175], v[100:103], v[116:119], v[172:175]\n\t" \
  "s_xor_b64 s[92:93], s[92:93], s[88:89]\n\t" \
  "ds_read_b64_tr_b16 v[150:151], v54 offset:31232\n\t" \
  "v_mfma_f32_16x16x32_bf16 v[180:183], v[100:103], v[120:123], v[180:183]\n\t" \
  "ds_read_b128 v[64:67], v52 offset:0\n\t" \
  "v_mfma_f32_16x16x32_bf16 v[180:183], v[92:95], v[124:127], v[180:183]\n\t" \
  "ds_read_b128 v[104:107], v53 offset:0\n\t" \
  "s_waitcnt lgkmcnt(7)\n\t" \
  "v_mfma_f32_16x16x32_bf16 v[216:219], v[136:139], v[96:99], v[216:219]\n\t" \
  "ds_read_b64_tr_b16 v[112:113], v55 offset:26624\n\t" \
  "v_mfma_f32_16x16x32_bf16 v[216:219], v[144:147], v[128:131], v[216:219]\n\t" \
  "ds_read_b64_tr_b16 v[114:115], v55 offset:27136\n\t" \
  "v_mfma_f32_16x16x32_bf16 v[224:227], v[72:75], v[128:131], v[224:227]\n\t" \
  "ds_read_b64_tr_b16 v[116:117], v55 offset:30720\n\t" \
  "v_mfma_f32_16x16x32_bf16 v[224:227], v[140:143], v[96:99], v[224:227]\n\t" \
  "ds_read_b64_tr_b16 v[118:119], v55 offset:31232\n\t" \
  "v_mfma_f32_16x16x32_bf16 v[232:235], v[68:71], v[96:99], v[232:235]\n\t" \
  "ds_read_b64_tr_b16 v[100:101], v54 offset:18432\n\t" \
  "v_mfma_f32_16x16x32_bf16 v[232:235], v[76:79], v[128:131], v[232:235]\n\t" \
  "ds_read_b64_tr_b16 v[102:103], v54 offset:18944\n\t" \
  "v_mfma_f32_16x16x32_bf16 v[240:243], v[80:83], v[128:131], v[240:243]\n\t" \
  "ds_read_b64_tr_b16 v[120:121], v54 offset:22528\n\t" \
  "v_mfma_f32_16x16x32_bf16 v[240:243], v[84:87], v[96:99], v[240:243]\n\t" \
  "ds_read_b64_tr_b16 v[122:123], v54 offset:23040\n\t" \
  "v_mfma_f32_16x16x32_bf16 v[220:223], v[136:139], v[132:135], v[220:223]\n\t" \
  "ds_read_b64_tr_b16 v[92:93], v55 offset:18432\n\t" \
  "s_waitcnt lgkmcnt(7)\n\t" \
  "v_mfma_f32_16x16x32_bf16 v[220:223], v[144:147], v[88:91], v[220:223]\n\t" \
  "ds_read_b64_tr_b16 v[94:95], v55 offset:18944\n\t" \
  "v_mfma_f32_16x16x32_bf16 v[228:231], v[72:75], v[88:91], v[228:231]\n\t" \
  "ds_read_b64_tr_b16 v[124:125], v55 offset:22528\n\t" \
  "v_mfma_f32_16x16x32_bf16 v[228:231], v[140:143], v[132:135], v[228:231]\n\t" \
  "ds_read_b64_tr_b16 v[126:127], v55 offset:23040\n\t" \
  "v_mfma_f32_16x16x32_bf16 v[236:239], v[68:71], v[132:135], v[236:239]\n\t" \
  "v_mfma_f32_16x16x32_bf16 v[236:239], v[76:79], v[88:91], v[236:239]\n\t" \
  "v_mfma_f32_16x16x32_bf16 v[244:247], v[80:83], v[88:91], v[244:247]\n\t" \
  "v_mfma_f32_16x16x32_bf16 v[244:247], v[84:87], v[132:135], v[244:247]\n\t" \
  "v_mfma_f32_16x16x32_bf16 v[184:187], v[108:111], v[136:139], v[184:187]\n\t" \
  "v_mfma_f32_16x16x32_bf16 v[184:187], v[148:151], v[144:147], v[184:187]\n\t" \
  "v_mfma_f32_16x16x32_bf16 v[192:195], v[148:151], v[72:75], v[192:195]\n\t" \
  "v_mfma_f32_16x16x32_bf16 v[192:195], v[108:111], v[140:143], v[192:195]\n\t" \
  "v_mfma_f32_16x16x32_bf16 v[200:203], v[108:111], v[68:71], v[200:203]\n\t" \
  "global_store_dwordx4 v2, v[42:45], %24 offset:0 sc1\n\t" \
  "v_mfma_f32_16x16x32_bf16 v[200:203], v[148:151], v[76:79], v[200:203]\n\t" \
  "s_nop 0\n\t" \
  "v_mfma_f32_16x16x32_bf16 v[42:45], v[26:29], v[18:21], 0\n\t" \
  "v_mfma_f32_16x16x32_bf16 v[42:45], v[30:33], v[22:25], v[42:45]\n\t" \
  "v_mfma_f32_16x16x32_bf16 v[46:49], v[30:33], v[18:21], 0\n\t" \
  "v_mfma_f32_16x16x32_bf16 v[46:49], v[34:37], v[22:25], v[46:49]\n\t" \
  "v_bfe_u32 v30, v4, 9, 1\n\t" \
  "v_mad_i32_i24 v30, v30, -16, v0\n\t" \
  "v_mfma_f32_16x16x32_bf16 v[208:211], v[148:151], v[80:83], v[208:211]\n\t" \
  "ds_read_b128 v[26:29], v0 offset:1584\n\t" \
  "ds_read_b128 v[30:33], v30 offset:1072\n\t" \
  "v_mfma_f32_16x16x32_bf16 v[208:211], v[108:111], v[84:87], v[208:211]\n\t" \
  "ds_read_b128 v[34:37], v0 offset:544\n\t" \
  "v_perm_b32 v18, v12, v12, v14\n\t" \
  "v_mfma_f32_16x16x32_bf16 v[248:251], v[108:111], v[64:67], v[248:251]\n\t" \
  "v_perm_b32 v19, v12, v12, v15\n\t" \
  "v_perm_b32 v20, v12, v12, v16\n\t" \
  "v_mfma_f32_16x16x32_bf16 v[248:251], v[148:151], v[104:107], v[248:251]\n\t" \
  "v_perm_b32 v21, v12, v12, v17\n\t" \
  "v_perm_b32 v22, v13, v13, v14\n\t" \
  "v_mfma_f32_16x16x32_bf16 v[252:255], v[112:115], v[64:67], v[252:255]\n\t" \
  "v_perm_b32 v23, v13, v13, v15\n\t" \
  "v_perm_b32 v24, v13, v13, v16\n\t" \
  "s_waitcnt lgkmcnt(3)\n\t" \
  "v_mfma_f32_16x16x32_bf16 v[252:255], v[116:119], v[104:107], v[252:255]\n\t" \
  "v_perm_b32 v25, v13, v13, v17\n\t" \
  "v_lshlrev_b32 v50, 16, v38\n\t" \
  "v_mfma_f32_16x16x32_bf16 v[188:191], v[116:119], v[144:147], v[188:191]\n\t" \
  "v_lshlrev_b32 v51, 16, v40\n\t" \
  "v_mul_f32 v42, v42, v50\n\t" \
  "v_mfma_f32_16x16x32_bf16 v[188:191], v[112:115], v[136:139], v[188:191]\n\t" \
  "v_mul_f32 v46, v46, v51\n\t" \
  "v_cvt_pk_bf16_f32 v42, v42, v46\n\t" \
  "s_waitcnt lgkmcnt(0)\n\t" \
  "s_barrier\n\t" \
  "v_mfma_f32_16x16x32_bf16 v[196:199], v[112:115], v[140:143], v[196:199]\n\t" \
  "ds_read_b64_tr_b16 v[128:129], v58 offset:34816\n\t" \
  "v_and_b32 v50, 0xffff0000, v38\n\t" \
  "v_mfma_f32_16x16x32_bf16 v[196:199], v[116:119], v[72:75], v[196:199]\n\t" \
  "ds_read_b64_tr_b16 v[130:131], v58 offset:35328\n\t" \
  "v_and_b32 v51, 0xffff0000, v40\n\t" \
  "v_mfma_f32_16x16x32_bf16 v[204:207], v[116:119], v[76:79], v[204:207]\n\t" \
  "ds_read_b128 v[96:99], v56 offset:2048\n\t" \
  "v_mul_f32 v43, v43, v50\n\t" \
  "v_mfma_f32_16x16x32_bf16 v[204:207], v[112:115], v[68:71], v[204:207]\n\t" \
  "ds_read_b64_tr_b16 v[88:89], v58 offset:38912\n\t" \
  "v_mul_f32 v47, v47, v51\n\t" \
  "v_mfma_f32_16x16x32_bf16 v[212:215], v[112:115], v[84:87], v[212:215]\n\t" \
  "ds_read_b64_tr_b16 v[90:91], v58 offset:39424\n\t" \
  "v_cvt_pk_bf16_f32 v43, v43, v47\n\t" \
  "v_mfma_f32_16x16x32_bf16 v[212:215], v[116:119], v[80:83], v[212:215]\n\t" \
  "ds_read_b128 v[132:135], v57 offset:2048\n\t" \
  "v_lshlrev_b32 v50, 16, v39\n\t" \
  "v_mfma_f32_16x16x32_bf16 v[152:155], v[100:103], v[136:139], v[152:155]\n\t" \
  "ds_read_b128 v[108:111], v57 offset:4096\n\t" \
  "v_lshlrev_b32 v51, 16, v41\n\t" \
  "v_mfma_f32_16x16x32_bf16 v[152:155], v[120:123], v[144:147], v[152:155]\n\t" \
  "ds_read_b128 v[148:151], v56 offset:4096\n\t" \
  "v_mul_f32 v44, v44, v50\n\t" \
  "v_mfma_f32_16x16x32_bf16 v[160:163], v[120:123], v[72:75], v[160:163]\n\t" \
  "ds_read_b128 v[64:67], v56 offset:6144\n\t" \
  "v_mul_f32 v48, v48, v51\n\t" \
  "v_mfma_f32_16x16x32_bf16 v[160:163], v[100:103], v[140:143], v[160:163]\n\t" \
  "ds_read_b128 v[104:107], v57 offset:6144\n\t" \
  "v_cvt_pk_bf16_f32 v44, v44, v48\n\t" \
  "v_mfma_f32_16x16x32_bf16 v[168:171], v[100:103], v[68:71], v[168:171]\n\t" \
  "ds_read_b128 v[112:115], v57 offset:8192\n\t" \
  "v_and_b32 v50, 0xffff0000, v39\n\t" \
  "v_mfma_f32_16x16x32_bf16 v[168:171], v[120:123], v[76:79], v[168:171]\n\t" \
  "ds_read_b128 v[116:119], v56 offset:8192\n\t" \
  "v_and_b32 v51, 0xffff0000, v41\n\t" \
  "v_mfma_f32_16x16x32_bf16 v[176:179], v[120:123], v[80:83], v[176:179]\n\t" \
  "ds_read_b64_tr_b16 v[120:121], v59 offset:34816\n\t" \
  "v_mul_f32 v45, v45, v50\n\t" \
  "v_mfma_f32_16x16x32_bf16 v[176:179], v[100:103], v[84:87], v[176:179]\n\t" \
  "ds_read_b64_tr_b16 v[122:123], v59 offset:35328\n\t" \
  "v_mul_f32 v49, v49, v51\n\t" \
  "v_mfma_f32_16x16x32_bf16 v[156:159], v[92:95], v[136:139], v[156:159]\n\t" \
  "ds_read_b64_tr_b16 v[100:101], v59 offset:38912\n\t" \
  "ds_read_b64_tr_b16 v[38:39], v1 offset:544\n\t" \
  "v_mfma_f32_16x16x32_bf16 v[156:159], v[124:127], v[144:147], v[156:159]\n\t" \
  "ds_read_b64_tr_b16 v[102:103], v59 offset:39424\n\t" \
  "ds_read_b64_tr_b16 v[40:41], v1 offset:32\n\t" \
  "v_mfma_f32_16x16x32_bf16 v[164:167], v[124:127], v[72:75], v[164:167]\n\t" \
  "ds_read_b64_tr_b16 v[136:137], v58 offset:26624\n\t" \
  "v_cvt_pk_bf16_f32 v45, v45, v49\n\t" \
  "v_mfma_f32_16x16x32_bf16 v[164:167], v[92:95], v[140:143], v[164:167]\n\t" \
  "ds_read_b64_tr_b16 v[138:139], v58 offset:27136\n\t" \
  "v_mfma_f32_16x16x32_bf16 v[172:175], v[92:95], v[68:71], v[172:175]\n\t" \
  "ds_read_b64_tr_b16 v[144:145], v58 offset:30720\n\t" \
  "v_mfma_f32_16x16x32_bf16 v[172:175], v[124:127], v[76:79], v[172:175]\n\t" \
  "ds_read_b64_tr_b16 v[146:147], v58 offset:31232\n\t" \
  "v_mfma_f32_16x16x32_bf16 v[180:183], v[124:127], v[80:83], v[180:183]\n\t" \
  "ds_read_b128 v[72:75], v56 offset:0\n\t" \
  "v_mfma_f32_16x16x32_bf16 v[180:183], v[92:95], v[84:87], v[180:183]\n\t" \
  "ds_read_b128 v[140:143], v57 offset:0\n\t" \
  "s_waitcnt lgkmcnt(8)\n\t" \
  "v_mfma_f32_16x16x32_bf16 v[216:219], v[96:99], v[128:131], v[216:219]\n\t" \
  "ds_read_b64_tr_b16 v[68:69], v59 offset:26624\n\t" \
  "v_mfma_f32_16x16x32_bf16 v[216:219], v[132:135], v[88:91], v[216:219]\n\t" \
  "ds_read_b64_tr_b16 v[70:71], v59 offset:27136\n\t" \
  "v_mfma_f32_16x16x32_bf16 v[224:227], v[108:111], v[88:91], v[224:227]\n\t" \
  "ds_read_b64_tr_b16 v[76:77], v59 offset:30720\n\t" \
  "v_mfma_f32_16x16x32_bf16 v[224:227], v[148:151], v[128:131], v[224:227]\n\t" \
  "ds_read_b64_tr_b16 v[78:79], v59 offset:31232\n\t" \
  "v_mfma_f32_16x16x32_bf16 v[232:235], v[64:67], v[128:131], v[232:235]\n\t" \
  "ds_read_b64_tr_b16 v[80:81], v58 offset:18432\n\t" \
  "v_mfma_f32_16x16x32_bf16 v[232:235], v[104:107], v[88:91], v[232:235]\n\t" \
  "ds_read_b64_tr_b16 v[82:83], v58 offset:18944\n\t" \
  "v_mfma_f32_16x16x32_bf16 v[240:243], v[112:115], v[88:91], v[240:243]\n\t" \
  "ds_read_b64_tr_b16 v[124:125], v58 offset:22528\n\t" \
  "v_mfma_f32_16x16x32_bf16 v[240:243], v[116:119], v[128:131], v[240:243]\n\t" \
  "ds_read_b64_tr_b16 v[126:127], v58 offset:23040\n\t" \
  "v_mfma_f32_16x16x32_bf16 v[220:223], v[96:99], v[120:123], v[220:223]\n\t" \
  "ds_read_b64_tr_b16 v[84:85], v59 offset:18432\n\t" \
  "s_waitcnt lgkmcnt(7)\n\t" \
  "v_mfma_f32_16x16x32_bf16 v[220:223], v[132:135], v[100:103], v[220:223]\n\t" \
  "ds_read_b64_tr_b16 v[86:87], v59 offset:18944\n\t" \
  "v_mfma_f32_16x16x32_bf16 v[228:231], v[108:111], v[100:103], v[228:231]\n\t" \
  "ds_read_b64_tr_b16 v[92:93], v59 offset:22528\n\t" \
  "v_mfma_f32_16x16x32_bf16 v[228:231], v[148:151], v[120:123], v[228:231]\n\t" \
  "ds_read_b64_tr_b16 v[94:95], v59 offset:23040\n\t" \
  "v_mfma_f32_16x16x32_bf16 v[236:239], v[64:67], v[120:123], v[236:239]\n\t" \
  "v_mfma_f32_16x16x32_bf16 v[236:239], v[104:107], v[100:103], v[236:239]\n\t" \
  "v_mfma_f32_16x16x32_bf16 v[244:247], v[112:115], v[100:103], v[244:247]\n\t" \
  "v_mfma_f32_16x16x32_bf16 v[244:247], v[116:119], v[120:123], v[244:247]\n\t" \
  "v_mfma_f32_16x16x32_bf16 v[184:187], v[136:139], v[96:99], v[184:187]\n\t" \
  "v_mfma_f32_16x16x32_bf16 v[184:187], v[144:147], v[132:135], v[184:187]\n\t" \
  "v_mfma_f32_16x16x32_bf16 v[192:195], v[144:147], v[108:111], v[192:195]\n\t" \
  "v_mfma_f32_16x16x32_bf16 v[192:195], v[136:139], v[148:151], v[192:195]\n\t" \
  "v_mfma_f32_16x16x32_bf16 v[200:203], v[136:139], v[64:67], v[200:203]\n\t" \
  "global_store_dwordx4 v2, v[42:45], %24 offset:1024 sc1\n\t" \
  "v_add_u32 v2, 0x20000, v2\n\t" \
  "v_mfma_f32_16x16x32_bf16 v[200:203], v[144:147], v[104:107], v[200:203]\n\t" \
  "v_mfma_f32_16x16x32_bf16 v[208:211], v[144:147], v[112:115], v[208:211]\n\t" \
  "v_mfma_f32_16x16x32_bf16 v[42:45], v[26:29], v[18:21], 0\n\t" \
  "v_mfma_f32_16x16x32_bf16 v[42:45], v[30:33], v[22:25], v[42:45]\n\t" \
  "v_mfma_f32_16x16x32_bf16 v[46:49], v[30:33], v[18:21], 0\n\t" \
  "v_mfma_f32_16x16x32_bf16 v[46:49], v[34:37], v[22:25], v[46:49]\n\t" \
  "ds_read_b128 v[26:29], v0 offset:1840\n\t" \
  "ds_read_b128 v[30:33], v0 offset:1328\n\t" \
  "v_mfma_f32_16x16x32_bf16 v[208:211], v[136:139], v[116:119], v[208:211]\n\t" \
  "ds_read_b128 v[34:37], v0 offset:800\n\t" \
  "v_mfma_f32_16x16x32_bf16 v[248:251], v[136:139], v[72:75], v[248:251]\n\t" \
  "v_mfma_f32_16x16x32_bf16 v[248:251], v[144:147], v[140:143], v[248:251]\n\t" \
  "v_lshlrev_b32 v50, 16, v38\n\t" \
  "v_lshlrev_b32 v51, 16, v40\n\t" \
  "v_mfma_f32_16x16x32_bf16 v[252:255], v[68:71], v[72:75], v[252:255]\n\t" \
  "v_mul_f32 v42, v42, v50\n\t" \
  "v_mul_f32 v46, v46, v51\n\t" \
  "s_waitcnt lgkmcnt(3)\n\t" \
  "v_mfma_f32_16x16x32_bf16 v[252:255], v[76:79], v[140:143], v[252:255]\n\t" \
  "v_cvt_pk_bf16_f32 v42, v42, v46\n\t" \
  "v_and_b32 v50, 0xffff0000, v38\n\t" \
  "v_mfma_f32_16x16x32_bf16 v[188:191], v[76:79], v[132:135], v[188:191]\n\t" \
  "v_and_b32 v51, 0xffff0000, v40\n\t" \
  "v_mul_f32 v43, v43, v50\n\t" \
  "v_mfma_f32_16x16x32_bf16 v[188:191], v[68:71], v[96:99], v[188:191]\n\t" \
  "v_mul_f32 v47, v47, v51\n\t" \
  "v_cvt_pk_bf16_f32 v43, v43, v47\n\t" \
  "s_waitcnt lgkmcnt(0)\n\t" \
  "s_barrier\n\t" \
  "v_mfma_f32_16x16x32_bf16 v[196:199], v[68:71], v[148:151], v[196:199]\n\t" \
  "ds_read_b64_tr_b16 v[88:89], v62 offset:34816\n\t" \
  "v_lshlrev_b32 v50, 16, v39\n\t" \
  "v_mfma_f32_16x16x32_bf16 v[196:199], v[76:79], v[108:111], v[196:199]\n\t" \
  "ds_read_b64_tr_b16 v[90:91], v62 offset:35328\n\t" \
  "v_lshlrev_b32 v51, 16, v41\n\t" \
  "v_mfma_f32_16x16x32_bf16 v[204:207], v[76:79], v[104:107], v[204:207]\n\t" \
  "ds_read_b128 v[128:131], v60 offset:2048\n\t" \
  "v_mul_f32 v44, v44, v50\n\t" \
  "v_mfma_f32_16x16x32_bf16 v[204:207], v[68:71], v[64:67], v[204:207]\n\t" \
  "ds_read_b64_tr_b16 v[100:101], v62 offset:38912\n\t" \
  "v_mul_f32 v48, v48, v51\n\t" \
  "v_mfma_f32_16x16x32_bf16 v[212:215], v[68:71], v[116:119], v[212:215]\n\t" \
  "ds_read_b64_tr_b16 v[102:103], v62 offset:39424\n\t" \
  "v_cvt_pk_bf16_f32 v44, v44, v48\n\t" \
  "v_mfma_f32_16x16x32_bf16 v[212:215], v[76:79], v[112:115], v[212:215]\n\t" \
  "ds_read_b128 v[120:123], v61 offset:2048\n\t" \
  "v_and_b32 v50, 0xffff0000, v39\n\t" \
  "v_mfma_f32_16x16x32_bf16 v[152:155], v[80:83], v[96:99], v[152:155]\n\t" \
  "ds_read_b128 v[136:139], v61 offset:4096\n\t" \
  "v_and_b32 v51, 0xffff0000, v41\n\t" \
  "v_mfma_f32_16x16x32_bf16 v[152:155], v[124:127], v[132:135], v[152:155]\n\t" \
  "ds_read_b128 v[144:147], v60 offset:4096\n\t" \
  "v_mul_f32 v45, v45, v50\n\t" \
  "v_mfma_f32_16x16x32_bf16 v[160:163], v[124:127], v[108:111], v[160:163]\n\t" \
  "ds_read_b128 v[72:75], v60 offset:6144\n\t" \
  "v_mul_f32 v49, v49, v51\n\t" \
  "v_mfma_f32_16x16x32_bf16 v[160:163], v[80:83], v[148:151], v[160:163]\n\t" \
  "ds_read_b128 v[140:143], v61 offset:6144\n\t" \
  "ds_read_b64_tr_b16 v[38:39], v1 offset:800\n\t" \
  "v_mfma_f32_16x16x32_bf16 v[168:171], v[80:83], v[64:67], v[168:171]\n\t" \
  "ds_read_b128 v[68:71], v61 offset:8192\n\t" \
  "ds_read_b64_tr_b16 v[40:41], v1 offset:288\n\t" \
  "v_mfma_f32_16x16x32_bf16 v[168:171], v[124:127], v[104:107], v[168:171]\n\t" \
  "ds_read_b128 v[76:79], v60 offset:8192\n\t" \
  "v_cvt_pk_bf16_f32 v45, v45, v49\n\t" \
  "v_mfma_f32_16x16x32_bf16 v[176:179], v[124:127], v[112:115], v[176:179]\n\t" \
  "ds_read_b64_tr_b16 v[124:125], v63 offset:34816\n\t" \
  "v_mfma_f32_16x16x32_bf16 v[176:179], v[80:83], v[116:119], v[176:179]\n\t" \
  "ds_read_b64_tr_b16 v[126:127], v63 offset:35328\n\t" \
  "v_mfma_f32_16x16x32_bf16 v[156:159], v[84:87], v[96:99], v[156:159]\n\t" \
  "ds_read_b64_tr_b16 v[80:81], v63 offset:38912\n\t" \
  "v_mfma_f32_16x16x32_bf16 v[156:159], v[92:95], v[132:135], v[156:159]\n\t" \
  "ds_read_b64_tr_b16 v[82:83], v63 offset:39424\n\t" \
  "v_mfma_f32_16x16x32_bf16 v[164:167], v[92:95], v[108:111], v[164:167]\n\t" \
  "ds_read_b64_tr_b16 v[96:97], v62 offset:26624\n\t" \
  "v_mfma_f32_16x16x32_bf16 v[164:167], v[84:87], v[148:151], v[164:167]\n\t" \
  "ds_read_b64_tr_b16 v[98:99], v62 offset:27136\n\t" \
  "v_mfma_f32_16x16x32_bf16 v[172:175], v[84:87], v[64:67], v[172:175]\n\t" \
  "ds_read_b64_tr_b16 v[132:133], v62 offset:30720\n\t" \
  "v_mfma_f32_16x16x32_bf16 v[172:175], v[92:95], v[104:107], v[172:175]\n\t" \
  "ds_read_b64_tr_b16 v[134:135], v62 offset:31232\n\t" \
  "v_mfma_f32_16x16x32_bf16 v[180:183], v[92:95], v[112:115], v[180:183]\n\t" \
  "ds_read_b128 v[108:111], v60 offset:0\n\t" \
  "v_mfma_f32_16x16x32_bf16 v[180:183], v[84:87], v[116:119], v[180:183]\n\t" \
  "ds_read_b128 v[148:151], v61 offset:0\n\t" \
  "s_waitcnt lgkmcnt(7)\n\t" \
  "v_mfma_f32_16x16x32_bf16 v[216:219], v[128:131], v[88:91], v[216:219]\n\t" \
  "ds_read_b64_tr_b16 v[64:65], v63 offset:26624\n\t" \
  "v_mfma_f32_16x16x32_bf16 v[216:219], v[120:123], v[100:103], v[216:219]\n\t" \
  "ds_read_b64_tr_b16 v[66:67], v63 offset:27136\n\t" \
  "v_mfma_f32_16x16x32_bf16 v[224:227], v[136:139], v[100:103], v[224:227]\n\t" \
  "ds_read_b64_tr_b16 v[104:105], v63 offset:30720\n\t" \
  "v_mfma_f32_16x16x32_bf16 v[224:227], v[144:147], v[88:91], v[224:227]\n\t" \
  "ds_read_b64_tr_b16 v[106:107], v63 offset:31232\n\t" \
  "v_mfma_f32_16x16x32_bf16 v[232:235], v[72:75], v[88:91], v[232:235]\n\t" \
  "ds_read_b64_tr_b16 v[92:93], v62 offset:18432\n\t" \
  "v_mfma_f32_16x16x32_bf16 v[232:235], v[140:143], v[100:103], v[232:235]\n\t" \
  "ds_read_b64_tr_b16 v[94:95], v62 offset:18944\n\t" \
  "v_mfma_f32_16x16x32_bf16 v[240:243], v[68:71], v[100:103], v[240:243]\n\t" \
  "ds_read_b64_tr_b16 v[112:113], v62 offset:22528\n\t" \
  "v_mfma_f32_16x16x32_bf16 v[240:243], v[76:79], v[88:91], v[240:243]\n\t" \
  "ds_read_b64_tr_b16 v[114:115], v62 offset:23040\n\t" \
  "v_mfma_f32_16x16x32_bf16 v[220:223], v[128:131], v[124:127], v[220:223]\n\t" \
  "ds_read_b64_tr_b16 v[84:85], v63 offset:18432\n\t" \
  "s_waitcnt lgkmcnt(7)\n\t" \
  "v_mfma_f32_16x16x32_bf16 v[220:223], v[120:123], v[80:83], v[220:223]\n\t" \
  "ds_read_b64_tr_b16 v[86:87], v63 offset:18944\n\t" \
  "v_mfma_f32_16x16x32_bf16 v[228:231], v[136:139], v[80:83], v[228:231]\n\t" \
  "ds_read_b64_tr_b16 v[116:117], v63 offset:22528\n\t" \
  "v_mfma_f32_16x16x32_bf16 v[228:231], v[144:147], v[124:127], v[228:231]\n\t" \
  "ds_read_b64_tr_b16 v[118:119], v63 offset:23040\n\t" \
  "v_mfma_f32_16x16x32_bf16 v[236:239], v[72:75], v[124:127], v[236:239]\n\t" \
  "v_mfma_f32_16x16x32_bf16 v[236:239], v[140:143], v[80:83], v[236:239]\n\t" \
  "v_mfma_f32_16x16x32_bf16 v[244:247], v[68:71], v[80:83], v[244:247]\n\t" \
  "v_mfma_f32_16x16x32_bf16 v[244:247], v[76:79], v[124:127], v[244:247]\n\t" \
  "v_mfma_f32_16x16x32_bf16 v[184:187], v[96:99], v[128:131], v[184:187]\n\t" \
  "v_mfma_f32_16x16x32_bf16 v[184:187], v[132:135], v[120:123], v[184:187]\n\t" \
  "v_mfma_f32_16x16x32_bf16 v[192:195], v[132:135], v[136:139], v[192:195]\n\t" \
  "v_mfma_f32_16x16x32_bf16 v[192:195], v[96:99], v[144:147], v[192:195]\n\t" \
  "v_mfma_f32_16x16x32_bf16 v[200:203], v[96:99], v[72:75], v[200:203]\n\t" \
  "global_store_dwordx4 v2, v[42:45], %24 offset:0 sc1\n\t" \
  "v_mfma_f32_16x16x32_bf16 v[200:203], v[132:135], v[140:143], v[200:203]\n\t" \
  "s_nop 0\n\t" \
  "v_mfma_f32_16x16x32_bf16 v[42:45], v[26:29], v[18:21], 0\n\t" \
  "v_mfma_f32_16x16x32_bf16 v[42:45], v[30:33], v[22:25], v[42:45]\n\t" \
  "v_mfma_f32_16x16x32_bf16 v[46:49], v[30:33], v[18:21], 0\n\t" \
  "v_mfma_f32_16x16x32_bf16 v[46:49], v[34:37], v[22:25], v[46:49]\n\t" \
  "v_mfma_f32_16x16x32_bf16 v[208:211], v[132:135], v[68:71], v[208:211]\n\t" \
  "v_mfma_f32_16x16x32_bf16 v[208:211], v[96:99], v[76:79], v[208:211]\n\t" \
  "v_lshlrev_b32 v50, 16, v38\n\t" \
  "v_lshlrev_b32 v51, 16, v40\n\t" \
  "v_mfma_f32_16x16x32_bf16 v[248:251], v[96:99], v[108:111], v[248:251]\n\t" \
  "s_nop 0\n\t" \
  "v_mul_f32 v42, v42, v50\n\t" \
  "s_nop 0\n\t" \
  "v_mul_f32 v46, v46, v51\n\t" \
  "v_mfma_f32_16x16x32_bf16 v[248:251], v[132:135], v[148:151], v[248:251]\n\t" \
  "v_cvt_pk_bf16_f32 v42, v42, v46\n\t" \
  "v_and_b32 v50, 0xffff0000, v38\n\t" \
  "v_mfma_f32_16x16x32_bf16 v[252:255], v[64:67], v[108:111], v[252:255]\n\t" \
  "v_and_b32 v51, 0xffff0000, v40\n\t" \
  "v_mul_f32 v43, v43, v50\n\t" \
  "s_waitcnt lgkmcnt(0)\n\t" \
  "v_mfma_f32_16x16x32_bf16 v[252:255], v[104:107], v[148:151], v[252:255]\n\t" \
  "v_mul_f32 v47, v47, v51\n\t" \
  "v_cvt_pk_bf16_f32 v43, v43, v47\n\t" \
  "v_mfma_f32_16x16x32_bf16 v[188:191], v[104:107], v[120:123], v[188:191]\n\t" \
  "v_lshlrev_b32 v50, 16, v39\n\t" \
  "v_lshlrev_b32 v51, 16, v41\n\t" \
  "v_mfma_f32_16x16x32_bf16 v[188:191], v[64:67], v[128:131], v[188:191]\n\t" \
  "v_mul_f32 v44, v44, v50\n\t" \
  "v_mul_f32 v48, v48, v51\n\t" \
  "s_barrier\n\t" \
  "v_mfma_f32_16x16x32_bf16 v[196:199], v[64:67], v[144:147], v[196:199]\n\t" \
  "ds_read_b64_tr_b16 v[100:101], v54 offset:34816\n\t" \
  "v_cvt_pk_bf16_f32 v44, v44, v48\n\t" \
  "v_mfma_f32_16x16x32_bf16 v[196:199], v[104:107], v[136:139], v[196:199]\n\t" \
  "ds_read_b64_tr_b16 v[102:103], v54 offset:35328\n\t" \
  "v_and_b32 v50, 0xffff0000, v39\n\t" \
  "v_mfma_f32_16x16x32_bf16 v[204:207], v[104:107], v[140:143], v[204:207]\n\t" \
  "ds_read_b128 v[88:91], v52 offset:2048\n\t" \
  "v_and_b32 v51, 0xffff0000, v41\n\t" \
  "v_mfma_f32_16x16x32_bf16 v[204:207], v[64:67], v[72:75], v[204:207]\n\t" \
  "ds_read_b64_tr_b16 v[80:81], v54 offset:38912\n\t" \
  "v_mul_f32 v45, v45, v50\n\t" \
  "v_mfma_f32_16x16x32_bf16 v[212:215], v[64:67], v[76:79], v[212:215]\n\t" \
  "ds_read_b64_tr_b16 v[82:83], v54 offset:39424\n\t" \
  "v_mul_f32 v49, v49, v51\n\t" \
  "v_mfma_f32_16x16x32_bf16 v[212:215], v[104:107], v[68:71], v[212:215]\n\t" \
  "ds_read_b128 v[124:127], v53 offset:2048\n\t" \
  "v_cvt_pk_bf16_f32 v45, v45, v49\n\t" \
  "v_mfma_f32_16x16x32_bf16 v[152:155], v[92:95], v[128:131], v[152:155]\n\t" \
  "ds_read_b128 v[96:99], v53 offset:4096\n\t" \
  "v_mfma_f32_16x16x32_bf16 v[152:155], v[112:115], v[120:123], v[152:155]\n\t" \
  "ds_read_b128 v[132:135], v52 offset:4096\n\t" \
  "v_mfma_f32_16x16x32_bf16 v[160:163], v[112:115], v[136:139], v[160:163]\n\t" \
  "ds_read_b128 v[108:111], v52 offset:6144\n\t" \
  "504:\n\t" \
  "s_cbranch_execz 508f\n\t" \
  "v_mfma_f32_16x16x32_bf16 v[160:163], v[92:95], v[144:147], v[160:163]\n\t" \
  "ds_read_b128 v[148:151], v53 offset:6144\n\t" \
  "v_mfma_f32_16x16x32_bf16 v[168:171], v[92:95], v[72:75], v[168:171]\n\t" \
  "ds_read_b128 v[64:67], v53 offset:8192\n\t" \
  "v_mfma_f32_16x16x32_bf16 v[168:171], v[112:115], v[140:143], v[168:171]\n\t" \
  "ds_read_b128 v[104:107], v52 offset:8192\n\t" \
  "v_mfma_f32_16x16x32_bf16 v[176:179], v[112:115], v[68:71], v[176:179]\n\t" \
  "ds_read_b64_tr_b16 v[112:113], v55 offset:34816\n\t" \
  "v_mfma_f32_16x16x32_bf16 v[176:179], v[92:95], v[76:79], v[176:179]\n\t" \
  "ds_read_b64_tr_b16 v[114:115], v55 offset:35328\n\t" \
  "v_mfma_f32_16x16x32_bf16 v[156:159], v[84:87], v[128:131], v[156:159]\n\t" \
  "ds_read_b64_tr_b16 v[92:93], v55 offset:38912\n\t" \
  "v_mfma_f32_16x16x32_bf16 v[156:159], v[116:119], v[120:123], v[156:159]\n\t" \
  "ds_read_b64_tr_b16 v[94:95], v55 offset:39424\n\t" \
  "v_mfma_f32_16x16x32_bf16 v[164:167], v[116:119], v[136:139], v[164:167]\n\t" \
  "ds_read_b64_tr_b16 v[128:129], v54 offset:26624\n\t" \
  "v_mfma_f32_16x16x32_bf16 v[164:167], v[84:87], v[144:147], v[164:167]\n\t" \
  "ds_read_b64_tr_b16 v[130:131], v54 offset:27136\n\t" \
  "v_mfma_f32_16x16x32_bf16 v[172:175], v[84:87], v[72:75], v[172:175]\n\t" \
  "ds_read_b64_tr_b16 v[120:121], v54 offset:30720\n\t" \
  "v_mfma_f32_16x16x32_bf16 v[172:175], v[116:119], v[140:143], v[172:175]\n\t" \
  "ds_read_b64_tr_b16 v[122:123], v54 offset:31232\n\t" \
  "v_mfma_f32_16x16x32_bf16 v[180:183], v[116:119], v[68:71], v[180:183]\n\t" \
  "ds_read_b128 v[136:139], v52 offset:0\n\t" \
  "v_mfma_f32_16x16x32_bf16 v[180:183], v[84:87], v[76:79], v[180:183]\n\t" \
  "ds_read_b128 v[144:147], v53 offset:0\n\t" \
  "s_waitcnt lgkmcnt(7)\n\t" \
  "v_mfma_f32_16x16x32_bf16 v[216:219], v[88:91], v[100:103], v[216:219]\n\t" \
  "ds_read_b64_tr_b16 v[72:73], v55 offset:26624\n\t" \
  "v_mfma_f32_16x16x32_bf16 v[216:219], v[124:127], v[80:83], v[216:219]\n\t" \
  "ds_read_b64_tr_b16 v[74:75], v55 offset:27136\n\t" \
  "v_mfma_f32_16x16x32_bf16 v[224:227], v[96:99], v[80:83], v[224:227]\n\t" \
  "ds_read_b64_tr_b16 v[140:141], v55 offset:30720\n\t" \
  "v_mfma_f32_16x16x32_bf16 v[224:227], v[132:135], v[100:103], v[224:227]\n\t" \
  "ds_read_b64_tr_b16 v[142:143], v55 offset:31232\n\t" \
  "v_mfma_f32_16x16x32_bf16 v[232:235], v[108:111], v[100:103], v[232:235]\n\t" \
  "ds_read_b64_tr_b16 v[68:69], v54 offset:18432\n\t" \
  "v_mfma_f32_16x16x32_bf16 v[232:235], v[148:151], v[80:83], v[232:235]\n\t" \
  "ds_read_b64_tr_b16 v[70:71], v54 offset:18944\n\t" \
  "v_mfma_f32_16x16x32_bf16 v[240:243], v[64:67], v[80:83], v[240:243]\n\t" \
  "ds_read_b64_tr_b16 v[116:117], v54 offset:22528\n\t" \
  "v_mfma_f32_16x16x32_bf16 v[240:243], v[104:107], v[100:103], v[240:243]\n\t" \
  "ds_read_b64_tr_b16 v[118:119], v54 offset:23040\n\t" \
  "v_mfma_f32_16x16x32_bf16 v[220:223], v[88:91], v[112:115], v[220:223]\n\t" \
  "ds_read_b64_tr_b16 v[76:77], v55 offset:18432\n\t" \
  "s_waitcnt lgkmcnt(7)\n\t" \
  "v_mfma_f32_16x16x32_bf16 v[220:223], v[124:127], v[92:95], v[220:223]\n\t" \
  "ds_read_b64_tr_b16 v[78:79], v55 offset:18944\n\t" \
  "v_mfma_f32_16x16x32_bf16 v[228:231], v[96:99], v[92:95], v[228:231]\n\t" \
  "ds_read_b64_tr_b16 v[84:85], v55 offset:22528\n\t" \
  "v_mfma_f32_16x16x32_bf16 v[228:231], v[132:135], v[112:115], v[228:231]\n\t" \
  "ds_read_b64_tr_b16 v[86:87], v55 offset:23040\n\t" \
  "v_mfma_f32_16x16x32_bf16 v[236:239], v[108:111], v[112:115], v[236:239]\n\t" \
  "v_mfma_f32_16x16x32_bf16 v[236:239], v[148:151], v[92:95], v[236:239]\n\t" \
  "v_mfma_f32_16x16x32_bf16 v[244:247], v[64:67], v[92:95], v[244:247]\n\t" \
  "v_mfma_f32_16x16x32_bf16 v[244:247], v[104:107], v[112:115], v[244:247]\n\t" \
  "v_mfma_f32_16x16x32_bf16 v[184:187], v[128:131], v[88:91], v[184:187]\n\t" \
  "v_mfma_f32_16x16x32_bf16 v[184:187], v[120:123], v[124:127], v[184:187]\n\t" \
  "v_mfma_f32_16x16x32_bf16 v[192:195], v[120:123], v[96:99], v[192:195]\n\t" \
  "v_mfma_f32_16x16x32_bf16 v[192:195], v[128:131], v[132:135], v[192:195]\n\t" \
  "v_mfma_f32_16x16x32_bf16 v[200:203], v[128:131], v[108:111], v[200:203]\n\t" \
  "global_store_dwordx4 v2, v[42:45], %24 offset:1024 sc1\n\t" \
  "v_mfma_f32_16x16x32_bf16 v[200:203], v[120:123], v[148:151], v[200:203]\n\t" \
  "v_mfma_f32_16x16x32_bf16 v[208:211], v[120:123], v[64:67], v[208:211]\n\t" \
  "v_mfma_f32_16x16x32_bf16 v[208:211], v[128:131], v[104:107], v[208:211]\n\t" \
  "v_mov_b32 v30, 0xff61b1e6\n\t" \
  "v_bfe_i32 v34, v3, 0, 1\n\t" \
  "v_mfma_f32_16x16x32_bf16 v[248:251], v[128:131], v[136:139], v[248:251]\n\t" \
  "v_and_b32 v34, v30, v34\n\t" \
  "v_bfe_i32 v38, v3, 4, 1\n\t" \
  "v_mfma_f32_16x16x32_bf16 v[248:251], v[120:123], v[144:147], v[248:251]\n\t" \
  "v_and_b32 v38, v30, v38\n\t" \
  "v_bfe_i32 v42, v3, 8, 1\n\t" \
  "v_mfma_f32_16x16x32_bf16 v[252:255], v[72:75], v[136:139], v[252:255]\n\t" \
  "v_bfi_b32 v42, v42, v30, %15\n\t" \
  "v_mov_b32 v48, 0x3f803f80\n\t" \
  "s_waitcnt lgkmcnt(0)\n\t" \
  "v_mfma_f32_16x16x32_bf16 v[252:255], v[140:143], v[144:147], v[252:255]\n\t" \
  "v_bfe_i32 v35, v3, 1, 1\n\t" \
  "v_and_b32 v35, v30, v35\n\t" \
  "v_mfma_f32_16x16x32_bf16 v[188:191], v[140:143], v[124:127], v[188:191]\n\t" \
  "v_bfe_i32 v39, v3, 5, 1\n\t" \
  "v_and_b32 v39, v30, v39\n\t" \
  "v_mfma_f32_16x16x32_bf16 v[188:191], v[72:75], v[88:91], v[188:191]\n\t" \
  "v_bfe_i32 v43, v3, 9, 1\n\t" \
  "v_bfi_b32 v43, v43, v30, %15\n\t" \
  "s_barrier\n\t" \
  "v_mfma_f32_16x16x32_bf16 v[196:199], v[72:75], v[132:135], v[196:199]\n\t" \
  "ds_read_b64_tr_b16 v[80:81], v58 offset:34816\n\t" \
  "v_mov_b32 v49, 0x3f803f80\n\t" \
  "v_mfma_f32_16x16x32_bf16 v[196:199], v[140:143], v[96:99], v[196:199]\n\t" \
  "ds_read_b64_tr_b16 v[82:83], v58 offset:35328\n\t" \
  "v_bfe_i32 v36, v3, 2, 1\n\t" \
  "v_mfma_f32_16x16x32_bf16 v[204:207], v[140:143], v[148:151], v[204:207]\n\t" \
  "ds_read_b128 v[100:103], v56 offset:2048\n\t" \
  "v_and_b32 v36, v30, v36\n\t" \
  "v_mfma_f32_16x16x32_bf16 v[204:207], v[72:75], v[108:111], v[204:207]\n\t" \
  "ds_read_b64_tr_b16 v[92:93], v58 offset:38912\n\t" \
  "v_bfe_i32 v40, v3, 6, 1\n\t" \
  "v_mfma_f32_16x16x32_bf16 v[212:215], v[72:75], v[104:107], v[212:215]\n\t" \
  "ds_read_b64_tr_b16 v[94:95], v58 offset:39424\n\t" \
  "v_and_b32 v40, v30, v40\n\t" \
  "v_mfma_f32_16x16x32_bf16 v[212:215], v[140:143], v[64:67], v[212:215]\n\t" \
  "ds_read_b128 v[112:115], v57 offset:2048\n\t" \
  "v_bfe_i32 v44, v3, 10, 1\n\t" \
  "v_mfma_f32_16x16x32_bf16 v[152:155], v[68:71], v[88:91], v[152:155]\n\t" \
  "ds_read_b128 v[128:131], v57 offset:4096\n\t" \
  "v_bfi_b32 v44, v44, v30, %15\n\t" \
  "v_mfma_f32_16x16x32_bf16 v[152:155], v[116:119], v[124:127], v[152:155]\n\t" \
  "ds_read_b128 v[120:123], v56 offset:4096\n\t" \
  "v_mov_b32 v50, 0x3f803f80\n\t" \
  "v_mfma_f32_16x16x32_bf16 v[160:163], v[116:119], v[96:99], v[160:163]\n\t" \
  "ds_read_b128 v[136:139], v56 offset:6144\n\t" \
  "v_bfe_i32 v37, v3, 3, 1\n\t" \
  "v_mfma_f32_16x16x32_bf16 v[160:163], v[68:71], v[132:135], v[160:163]\n\t" \
  "ds_read_b128 v[144:147], v57 offset:6144\n\t" \
  "v_and_b32 v37, v30, v37\n\t" \
  "v_mfma_f32_16x16x32_bf16 v[168:171], v[68:71], v[108:111], v[168:171]\n\t" \
  "ds_read_b128 v[72:75], v57 offset:8192\n\t" \
  "v_bfe_i32 v41, v3, 7, 1\n\t" \
  "v_mfma_f32_16x16x32_bf16 v[168:171], v[116:119], v[148:151], v[168:171]\n\t" \
  "ds_read_b128 v[140:143], v56 offset:8192\n\t" \
  "v_and_b32 v41, v30, v41\n\t" \
  "v_mfma_f32_16x16x32_bf16 v[176:179], v[116:119], v[64:67], v[176:179]\n\t" \
  "ds_read_b64_tr_b16 v[116:117], v59 offset:34816\n\t" \
  "v_bfe_i32 v45, v3, 11, 1\n\t" \
  "v_mfma_f32_16x16x32_bf16 v[176:179], v[68:71], v[104:107], v[176:179]\n\t" \
  "ds_read_b64_tr_b16 v[118:119], v59 offset:35328\n\t" \
  "v_bfi_b32 v45, v45, v30, %15\n\t" \
  "v_mfma_f32_16x16x32_bf16 v[156:159], v[76:79], v[88:91], v[156:159]\n\t" \
  "ds_read_b64_tr_b16 v[68:69], v59 offset:38912\n\t" \
  "v_mov_b32 v51, 0x3f803f80\n\t" \
  "v_mfma_f32_16x16x32_bf16 v[156:159], v[84:87], v[124:127], v[156:159]\n\t" \
  "ds_read_b64_tr_b16 v[70:71], v59 offset:39424\n\t" \
  "v_bfe_i32 v46, v3, 12, 1\n\t" \
  "v_mfma_f32_16x16x32_bf16 v[164:167], v[84:87], v[96:99], v[164:167]\n\t" \
  "ds_read_b64_tr_b16 v[88:89], v58 offset:26624\n\t" \
  "v_bfe_i32 v31, v3, 13, 1\n\t" \
  "v_mfma_f32_16x16x32_bf16 v[164:167], v[76:79], v[132:135], v[164:167]\n\t" \
  "ds_read_b64_tr_b16 v[90:91], v58 offset:27136\n\t" \
  "v_perm_b32 v46, v31, v46, vcc_hi\n\t" \
  "v_mfma_f32_16x16x32_bf16 v[172:175], v[76:79], v[108:111], v[172:175]\n\t" \
  "ds_read_b64_tr_b16 v[124:125], v58 offset:30720\n\t" \
  "v_bfe_i32 v47, v3, 14, 1\n\t" \
  "v_mfma_f32_16x16x32_bf16 v[172:175], v[84:87], v[148:151], v[172:175]\n\t" \
  "ds_read_b64_tr_b16 v[126:127], v58 offset:31232\n\t" \
  "v_bfe_i32 v31, v3, 15, 1\n\t" \
  "v_mfma_f32_16x16x32_bf16 v[180:183], v[84:87], v[64:67], v[180:183]\n\t" \
  "ds_read_b128 v[96:99], v56 offset:0\n\t" \
  "v_perm_b32 v47, v31, v47, vcc_hi\n\t" \
  "v_mfma_f32_16x16x32_bf16 v[180:183], v[76:79], v[104:107], v[180:183]\n\t" \
  "ds_read_b128 v[132:135], v57 offset:0\n\t" \
  "v_add_u32 v30, 18432, v4\n\t" \
  "s_waitcnt lgkmcnt(7)\n\t" \
  "v_mfma_f32_16x16x32_bf16 v[216:219], v[100:103], v[80:83], v[216:219]\n\t" \
  "ds_read_b64_tr_b16 v[108:109], v59 offset:26624\n\t" \
  "v_xor_b32 v31, 1024, v30\n\t" \
  "v_mfma_f32_16x16x32_bf16 v[216:219], v[112:115], v[92:95], v[216:219]\n\t" \
  "ds_read_b64_tr_b16 v[110:111], v59 offset:27136\n\t" \
  "v_mov_b32 v32, %12\n\t" \
  "v_mfma_f32_16x16x32_bf16 v[224:227], v[128:131], v[92:95], v[224:227]\n\t" \
  "ds_read_b64_tr_b16 v[148:149], v59 offset:30720\n\t" \
  "v_xor_b32 v33, 4, v32\n\t" \
  "v_mfma_f32_16x16x32_bf16 v[224:227], v[120:123], v[80:83], v[224:227]\n\t" \
  "ds_read_b64_tr_b16 v[150:151], v59 offset:31232\n\t" \
  "v_mov_b32 v0, 1\n\t" \
  "v_mfma_f32_16x16x32_bf16 v[232:235], v[136:139], v[80:83], v[232:235]\n\t" \
  "ds_read_b64_tr_b16 v[64:65], v58 offset:18432\n\t" \
  "v_and_b32 v4, 1023, v4\n\t" \
  "v_mfma_f32_16x16x32_bf16 v[232:235], v[144:147], v[92:95], v[232:235]\n\t" \
  "ds_read_b64_tr_b16 v[66:67], v58 offset:18944\n\t" \
  "v_add_u32 v1, 16384, v4\n\t" \
  "v_mfma_f32_16x16x32_bf16 v[240:243], v[72:75], v[92:95], v[240:243]\n\t" \
  "ds_read_b64_tr_b16 v[84:85], v58 offset:22528\n\t" \
  "v_mfma_f32_16x16x32_bf16 v[240:243], v[140:143], v[80:83], v[240:243]\n\t" \
  "ds_read_b64_tr_b16 v[86:87], v58 offset:23040\n\t" \
  "v_mfma_f32_16x16x32_bf16 v[220:223], v[100:103], v[116:119], v[220:223]\n\t" \
  "ds_read_b64_tr_b16 v[76:77], v59 offset:18432\n\t" \
  "s_waitcnt lgkmcnt(7)\n\t" \
  "v_mfma_f32_16x16x32_bf16 v[220:223], v[112:115], v[68:71], v[220:223]\n\t" \
  "ds_read_b64_tr_b16 v[78:79], v59 offset:18944\n\t" \
  "v_mfma_f32_16x16x32_bf16 v[228:231], v[128:131], v[68:71], v[228:231]\n\t" \
  "ds_read_b64_tr_b16 v[104:105], v59 offset:22528\n\t" \
  "v_mfma_f32_16x16x32_bf16 v[228:231], v[120:123], v[116:119], v[228:231]\n\t" \
  "ds_read_b64_tr_b16 v[106:107], v59 offset:23040\n\t" \
  "v_mfma_f32_16x16x32_bf16 v[236:239], v[136:139], v[116:119], v[236:239]\n\t" \
  "v_mfma_f32_16x16x32_bf16 v[236:239], v[144:147], v[68:71], v[236:239]\n\t" \
  "v_mfma_f32_16x16x32_bf16 v[244:247], v[72:75], v[68:71], v[244:247]\n\t" \
  "v_mfma_f32_16x16x32_bf16 v[244:247], v[140:143], v[116:119], v[244:247]\n\t" \
  "v_mfma_f32_16x16x32_bf16 v[184:187], v[88:91], v[100:103], v[184:187]\n\t" \
  "v_mfma_f32_16x16x32_bf16 v[184:187], v[124:127], v[112:115], v[184:187]\n\t" \
  "v_mfma_f32_16x16x32_bf16 v[192:195], v[124:127], v[128:131], v[192:195]\n\t" \
  "v_mfma_f32_16x16x32_bf16 v[192:195], v[88:91], v[120:123], v[192:195]\n\t" \
  "v_mfma_f32_16x16x32_bf16 v[200:203], v[88:91], v[136:139], v[200:203]\n\t" \
  "v_mfma_f32_16x16x32_bf16 v[200:203], v[124:127], v[144:147], v[200:203]\n\t" \
  "v_mfma_f32_16x16x32_bf16 v[208:211], v[124:127], v[72:75], v[208:211]\n\t" \
  "v_mfma_f32_16x16x32_bf16 v[208:211], v[88:91], v[140:143], v[208:211]\n\t" \
  "v_mfma_f32_16x16x32_bf16 v[248:251], v[88:91], v[96:99], v[248:251]\n\t" \
  "v_mfma_f32_16x16x32_bf16 v[248:251], v[124:127], v[132:135], v[248:251]\n\t" \
  "v_mfma_f32_16x16x32_bf16 v[252:255], v[108:111], v[96:99], v[252:255]\n\t" \
  "s_waitcnt lgkmcnt(0)\n\t" \
  "v_mfma_f32_16x16x32_bf16 v[252:255], v[148:151], v[132:135], v[252:255]\n\t" \
  "v_mfma_f32_16x16x32_bf16 v[188:191], v[148:151], v[112:115], v[188:191]\n\t" \
  "v_mfma_f32_16x16x32_bf16 v[188:191], v[108:111], v[100:103], v[188:191]\n\t" \
  "s_barrier\n\t" \
  "v_mfma_f32_16x16x32_bf16 v[196:199], v[108:111], v[120:123], v[196:199]\n\t" \
  "ds_read_b64_tr_b16 v[92:93], v62 offset:34816\n\t" \
  "v_mfma_f32_16x16x32_bf16 v[196:199], v[148:151], v[128:131], v[196:199]\n\t" \
  "ds_read_b64_tr_b16 v[94:95], v62 offset:35328\n\t" \
  "v_mfma_f32_16x16x32_bf16 v[204:207], v[148:151], v[144:147], v[204:207]\n\t" \
  "ds_read_b128 v[80:83], v60 offset:2048\n\t" \
  "v_mfma_f32_16x16x32_bf16 v[204:207], v[108:111], v[136:139], v[204:207]\n\t" \
  "ds_read_b64_tr_b16 v[68:69], v62 offset:38912\n\t" \
  "v_mfma_f32_16x16x32_bf16 v[212:215], v[108:111], v[140:143], v[212:215]\n\t" \
  "ds_read_b64_tr_b16 v[70:71], v62 offset:39424\n\t" \
  "v_mfma_f32_16x16x32_bf16 v[212:215], v[148:151], v[72:75], v[212:215]\n\t" \
  "ds_read_b128 v[116:119], v61 offset:2048\n\t" \
  "v_mfma_f32_16x16x32_bf16 v[152:155], v[64:67], v[100:103], v[152:155]\n\t" \
  "ds_read_b128 v[88:91], v61 offset:4096\n\t" \
  "v_mfma_f32_16x16x32_bf16 v[152:155], v[84:87], v[112:115], v[152:155]\n\t" \
  "ds_read_b128 v[124:127], v60 offset:4096\n\t" \
  "v_mfma_f32_16x16x32_bf16 v[160:163], v[84:87], v[128:131], v[160:163]\n\t" \
  "ds_read_b128 v[96:99], v60 offset:6144\n\t" \
  "v_mfma_f32_16x16x32_bf16 v[160:163], v[64:67], v[120:123], v[160:163]\n\t" \
  "ds_read_b128 v[132:135], v61 offset:6144\n\t" \
  "v_mfma_f32_16x16x32_bf16 v[168:171], v[64:67], v[136:139], v[168:171]\n\t" \
  "ds_read_b128 v[108:111], v61 offset:8192\n\t" \
  "v_mfma_f32_16x16x32_bf16 v[168:171], v[84:87], v[144:147], v[168:171]\n\t" \
  "ds_read_b128 v[148:151], v60 offset:8192\n\t" \
  "v_mfma_f32_16x16x32_bf16 v[176:179], v[84:87], v[72:75], v[176:179]\n\t" \
  "ds_read_b64_tr_b16 v[84:85], v63 offset:34816\n\t" \
  "v_mfma_f32_16x16x32_bf16 v[176:179], v[64:67], v[140:143], v[176:179]\n\t" \
  "ds_read_b64_tr_b16 v[86:87], v63 offset:35328\n\t" \
  "v_mfma_f32_16x16x32_bf16 v[156:159], v[76:79], v[100:103], v[156:159]\n\t" \
  "ds_read_b64_tr_b16 v[64:65], v63 offset:38912\n\t" \
  "v_mfma_f32_16x16x32_bf16 v[156:159], v[104:107], v[112:115], v[156:159]\n\t" \
  "ds_read_b64_tr_b16 v[66:67], v63 offset:39424\n\t" \
  "v_mfma_f32_16x16x32_bf16 v[164:167], v[104:107], v[128:131], v[164:167]\n\t" \
  "ds_read_b64_tr_b16 v[100:101], v62 offset:26624\n\t" \
  "v_mfma_f32_16x16x32_bf16 v[164:167], v[76:79], v[120:123], v[164:167]\n\t" \
  "ds_read_b64_tr_b16 v[102:103], v62 offset:27136\n\t" \
  "v_mfma_f32_16x16x32_bf16 v[172:175], v[76:79], v[136:139], v[172:175]\n\t" \
  "ds_read_b64_tr_b16 v[112:113], v62 offset:30720\n\t" \
  "v_mfma_f32_16x16x32_bf16 v[172:175], v[104:107], v[144:147], v[172:175]\n\t" \
  "ds_read_b64_tr_b16 v[114:115], v62 offset:31232\n\t" \
  "v_mfma_f32_16x16x32_bf16 v[180:183], v[104:107], v[72:75], v[180:183]\n\t" \
  "ds_read_b128 v[128:131], v60 offset:0\n\t" \
  "v_mfma_f32_16x16x32_bf16 v[180:183], v[76:79], v[140:143], v[180:183]\n\t" \
  "ds_read_b128 v[120:123], v61 offset:0\n\t" \
  "s_waitcnt lgkmcnt(7)\n\t" \
  "v_mfma_f32_16x16x32_bf16 v[216:219], v[80:83], v[92:95], v[216:219]\n\t" \
  "ds_read_b64_tr_b16 v[136:137], v63 offset:26624\n\t" \
  "v_mfma_f32_16x16x32_bf16 v[216:219], v[116:119], v[68:71], v[216:219]\n\t" \
  "ds_read_b64_tr_b16 v[138:139], v63 offset:27136\n\t" \
  "v_mfma_f32_16x16x32_bf16 v[224:227], v[88:91], v[68:71], v[224:227]\n\t" \
  "ds_read_b64_tr_b16 v[144:145], v63 offset:30720\n\t" \
  "v_mfma_f32_16x16x32_bf16 v[224:227], v[124:127], v[92:95], v[224:227]\n\t" \
  "ds_read_b64_tr_b16 v[146:147], v63 offset:31232\n\t" \
  "v_mfma_f32_16x16x32_bf16 v[232:235], v[96:99], v[92:95], v[232:235]\n\t" \
  "ds_read_b64_tr_b16 v[72:73], v62 offset:18432\n\t" \
  "v_mfma_f32_16x16x32_bf16 v[232:235], v[132:135], v[68:71], v[232:235]\n\t" \
  "ds_read_b64_tr_b16 v[74:75], v62 offset:18944\n\t" \
  "v_mfma_f32_16x16x32_bf16 v[240:243], v[108:111], v[68:71], v[240:243]\n\t" \
  "ds_read_b64_tr_b16 v[104:105], v62 offset:22528\n\t" \
  "v_mfma_f32_16x16x32_bf16 v[240:243], v[148:151], v[92:95], v[240:243]\n\t" \
  "ds_read_b64_tr_b16 v[106:107], v62 offset:23040\n\t" \
  "v_mfma_f32_16x16x32_bf16 v[220:223], v[80:83], v[84:87], v[220:223]\n\t" \
  "ds_read_b64_tr_b16 v[76:77], v63 offset:18432\n\t" \
  "s_waitcnt lgkmcnt(7)\n\t" \
  "v_mfma_f32_16x16x32_bf16 v[220:223], v[116:119], v[64:67], v[220:223]\n\t" \
  "ds_read_b64_tr_b16 v[78:79], v63 offset:18944\n\t" \
  "v_mfma_f32_16x16x32_bf16 v[228:231], v[88:91], v[64:67], v[228:231]\n\t" \
  "ds_read_b64_tr_b16 v[140:141], v63 offset:22528\n\t" \
  "v_mfma_f32_16x16x32_bf16 v[228:231], v[124:127], v[84:87], v[228:231]\n\t" \
  "ds_read_b64_tr_b16 v[142:143], v63 offset:23040\n\t" \
  "v_mfma_f32_16x16x32_bf16 v[236:239], v[96:99], v[84:87], v[236:239]\n\t" \
  "v_mfma_f32_16x16x32_bf16 v[236:239], v[132:135], v[64:67], v[236:239]\n\t" \
  "v_mfma_f32_16x16x32_bf16 v[244:247], v[108:111], v[64:67], v[244:247]\n\t" \
  "v_mfma_f32_16x16x32_bf16 v[244:247], v[148:151], v[84:87], v[244:247]\n\t" \
  "v_mfma_f32_16x16x32_bf16 v[184:187], v[100:103], v[80:83], v[184:187]\n\t" \
  "v_mfma_f32_16x16x32_bf16 v[184:187], v[112:115], v[116:119], v[184:187]\n\t" \
  "v_mfma_f32_16x16x32_bf16 v[192:195], v[112:115], v[88:91], v[192:195]\n\t" \
  "v_mfma_f32_16x16x32_bf16 v[192:195], v[100:103], v[124:127], v[192:195]\n\t" \
  "v_mfma_f32_16x16x32_bf16 v[200:203], v[100:103], v[96:99], v[200:203]\n\t" \
  "v_mfma_f32_16x16x32_bf16 v[200:203], v[112:115], v[132:135], v[200:203]\n\t" \
  "v_mfma_f32_16x16x32_bf16 v[208:211], v[112:115], v[108:111], v[208:211]\n\t" \
  "v_mfma_f32_16x16x32_bf16 v[208:211], v[100:103], v[148:151], v[208:211]\n\t" \
  "v_mfma_f32_16x16x32_bf16 v[248:251], v[100:103], v[128:131], v[248:251]\n\t" \
  "v_mfma_f32_16x16x32_bf16 v[248:251], v[112:115], v[120:123], v[248:251]\n\t" \
  "v_mfma_f32_16x16x32_bf16 v[252:255], v[136:139], v[128:131], v[252:255]\n\t" \
  "s_waitcnt lgkmcnt(0)\n\t" \
  "v_mfma_f32_16x16x32_bf16 v[252:255], v[144:147], v[120:123], v[252:255]\n\t" \
  "v_mfma_f32_16x16x32_bf16 v[188:191], v[144:147], v[116:119], v[188:191]\n\t" \
  "v_mfma_f32_16x16x32_bf16 v[188:191], v[136:139], v[80:83], v[188:191]\n\t" \
  "s_waitcnt vmcnt(0)\n\t" \
  "s_barrier\n\t" \
  "v_mfma_f32_16x16x32_bf16 v[196:199], v[136:139], v[124:127], v[196:199]\n\t" \
  "ds_read_b64_tr_b16 v[68:69], v54 offset:34816\n\t" \
  "v_mfma_f32_16x16x32_bf16 v[196:199], v[144:147], v[88:91], v[196:199]\n\t" \
  "ds_read_b64_tr_b16 v[70:71], v54 offset:35328\n\t" \
  "v_mfma_f32_16x16x32_bf16 v[204:207], v[144:147], v[132:135], v[204:207]\n\t" \
  "ds_read_b128 v[92:95], v52 offset:2048\n\t" \
  "v_mfma_f32_16x16x32_bf16 v[204:207], v[136:139], v[96:99], v[204:207]\n\t" \
  "ds_read_b64_tr_b16 v[64:65], v54 offset:38912\n\t" \
  "v_mfma_f32_16x16x32_bf16 v[212:215], v[136:139], v[148:151], v[212:215]\n\t" \
  "ds_read_b64_tr_b16 v[66:67], v54 offset:39424\n\t" \
  "v_mfma_f32_16x16x32_bf16 v[212:215], v[144:147], v[108:111], v[212:215]\n\t" \
  "ds_read_b128 v[84:87], v53 offset:2048\n\t" \
  "v_mfma_f32_16x16x32_bf16 v[152:155], v[72:75], v[80:83], v[152:155]\n\t" \
  "ds_read_b128 v[100:103], v53 offset:4096\n\t" \
  "v_mfma_f32_16x16x32_bf16 v[152:155], v[104:107], v[116:119], v[152:155]\n\t" \
  "ds_read_b128 v[112:115], v52 offset:4096\n\t" \
  "v_mfma_f32_16x16x32_bf16 v[160:163], v[104:107], v[88:91], v[160:163]\n\t" \
  "ds_read_b128 v[128:131], v52 offset:6144\n\t" \
  "v_mfma_f32_16x16x32_bf16 v[160:163], v[72:75], v[124:127], v[160:163]\n\t" \
  "ds_read_b128 v[120:123], v53 offset:6144\n\t" \
  "v_mfma_f32_16x16x32_bf16 v[168:171], v[72:75], v[96:99], v[168:171]\n\t" \
  "ds_read_b128 v[136:139], v53 offset:8192\n\t" \
  "v_mfma_f32_16x16x32_bf16 v[168:171], v[104:107], v[132:135], v[168:171]\n\t" \
  "ds_read_b128 v[144:147], v52 offset:8192\n\t" \
  "v_mfma_f32_16x16x32_bf16 v[176:179], v[104:107], v[108:111], v[176:179]\n\t" \
  "ds_read_b64_tr_b16 v[104:105], v55 offset:34816\n\t" \
  "v_mfma_f32_16x16x32_bf16 v[176:179], v[72:75], v[148:151], v[176:179]\n\t" \
  "ds_read_b64_tr_b16 v[106:107], v55 offset:35328\n\t" \
  "v_mfma_f32_16x16x32_bf16 v[156:159], v[76:79], v[80:83], v[156:159]\n\t" \
  "ds_read_b64_tr_b16 v[72:73], v55 offset:38912\n\t" \
  "v_mfma_f32_16x16x32_bf16 v[156:159], v[140:143], v[116:119], v[156:159]\n\t" \
  "ds_read_b64_tr_b16 v[74:75], v55 offset:39424\n\t" \
  "v_mfma_f32_16x16x32_bf16 v[164:167], v[140:143], v[88:91], v[164:167]\n\t" \
  "ds_read_b64_tr_b16 v[80:81], v54 offset:26624\n\t" \
  "v_mfma_f32_16x16x32_bf16 v[164:167], v[76:79], v[124:127], v[164:167]\n\t" \
  "ds_read_b64_tr_b16 v[82:83], v54 offset:27136\n\t" \
  "v_mfma_f32_16x16x32_bf16 v[172:175], v[76:79], v[96:99], v[172:175]\n\t" \
  "ds_read_b64_tr_b16 v[116:117], v54 offset:30720\n\t" \
  "v_mfma_f32_16x16x32_bf16 v[172:175], v[140:143], v[132:135], v[172:175]\n\t" \
  "ds_read_b64_tr_b16 v[118:119], v54 offset:31232\n\t" \
  "v_mfma_f32_16x16x32_bf16 v[180:183], v[140:143], v[108:111], v[180:183]\n\t" \
  "ds_read_b128 v[88:91], v52 offset:0\n\t" \
  "v_mfma_f32_16x16x32_bf16 v[180:183], v[76:79], v[148:151], v[180:183]\n\t" \
  "ds_read_b128 v[124:127], v53 offset:0\n\t" \
  "s_waitcnt lgkmcnt(7)\n\t" \
  "v_mfma_f32_16x16x32_bf16 v[216:219], v[92:95], v[68:71], v[216:219]\n\t" \
  "ds_read_b64_tr_b16 v[96:97], v55 offset:26624\n\t" \
  "v_mfma_f32_16x16x32_bf16 v[216:219], v[84:87], v[64:67], v[216:219]\n\t" \
  "ds_read_b64_tr_b16 v[98:99], v55 offset:27136\n\t" \
  "v_mfma_f32_16x16x32_bf16 v[224:227], v[100:103], v[64:67], v[224:227]\n\t" \
  "ds_read_b64_tr_b16 v[132:133], v55 offset:30720\n\t" \
  "v_mfma_f32_16x16x32_bf16 v[224:227], v[112:115], v[68:71], v[224:227]\n\t" \
  "ds_read_b64_tr_b16 v[134:135], v55 offset:31232\n\t" \
  "v_mfma_f32_16x16x32_bf16 v[232:235], v[128:131], v[68:71], v[232:235]\n\t" \
  "ds_read_b64_tr_b16 v[108:109], v54 offset:18432\n\t" \
  "v_mfma_f32_16x16x32_bf16 v[232:235], v[120:123], v[64:67], v[232:235]\n\t" \
  "ds_read_b64_tr_b16 v[110:111], v54 offset:18944\n\t" \
  "v_mfma_f32_16x16x32_bf16 v[240:243], v[136:139], v[64:67], v[240:243]\n\t" \
  "ds_read_b64_tr_b16 v[140:141], v54 offset:22528\n\t" \
  "v_mfma_f32_16x16x32_bf16 v[240:243], v[144:147], v[68:71], v[240:243]\n\t" \
  "ds_read_b64_tr_b16 v[142:143], v54 offset:23040\n\t" \
  "v_mfma_f32_16x16x32_bf16 v[220:223], v[92:95], v[104:107], v[220:223]\n\t" \
  "ds_read_b64_tr_b16 v[76:77], v55 offset:18432\n\t" \
  "s_waitcnt lgkmcnt(7)\n\t" \
  "v_mfma_f32_16x16x32_bf16 v[220:223], v[84:87], v[72:75], v[220:223]\n\t" \
  "ds_read_b64_tr_b16 v[78:79], v55 offset:18944\n\t" \
  "v_mfma_f32_16x16x32_bf16 v[228:231], v[100:103], v[72:75], v[228:231]\n\t" \
  "ds_read_b64_tr_b16 v[148:149], v55 offset:22528\n\t" \
  "v_mfma_f32_16x16x32_bf16 v[228:231], v[112:115], v[104:107], v[228:231]\n\t" \
  "ds_read_b64_tr_b16 v[150:151], v55 offset:23040\n\t" \
  "v_mfma_f32_16x16x32_bf16 v[236:239], v[128:131], v[104:107], v[236:239]\n\t" \
  "v_mfma_f32_16x16x32_bf16 v[236:239], v[120:123], v[72:75], v[236:239]\n\t" \
  "v_mfma_f32_16x16x32_bf16 v[244:247], v[136:139], v[72:75], v[244:247]\n\t" \
  "v_mfma_f32_16x16x32_bf16 v[244:247], v[144:147], v[104:107], v[244:247]\n\t" \
  "v_mfma_f32_16x16x32_bf16 v[184:187], v[80:83], v[92:95], v[184:187]\n\t" \
  "ds_read_b32 v10, v32 offset:104\n\t" \
  "v_mfma_f32_16x16x32_bf16 v[184:187], v[116:119], v[84:87], v[184:187]\n\t" \
  "v_lshlrev_b32 v11, 3, v4\n\t" \
  "v_mfma_f32_16x16x32_bf16 v[192:195], v[116:119], v[100:103], v[192:195]\n\t" \
  "v_mfma_f32_16x16x32_bf16 v[192:195], v[80:83], v[112:115], v[192:195]\n\t" \
  "v_mfma_f32_16x16x32_bf16 v[200:203], v[80:83], v[128:131], v[200:203]\n\t" \
  "s_waitcnt lgkmcnt(0)\n\t" \
  "s_cmp_lg_u32 %13, 0\n\t" \
  "s_cbranch_scc1 27f\n\t" \
  "s_mov_b32 exec_hi, 0\n\t" \
  "global_store_dword v11, v10, %25 sc1\n\t" \
  "s_mov_b32 exec_hi, -1\n\t" \
  "27:\n\t" \
  "v_mfma_f32_16x16x32_bf16 v[200:203], v[116:119], v[120:123], v[200:203]\n\t" \
  "v_mfma_f32_16x16x32_bf16 v[208:211], v[116:119], v[136:139], v[208:211]\n\t" \
  "v_mfma_f32_16x16x32_bf16 v[208:211], v[80:83], v[144:147], v[208:211]\n\t" \
  "v_mfma_f32_16x16x32_bf16 v[248:251], v[80:83], v[88:91], v[248:251]\n\t" \
  "v_mfma_f32_16x16x32_bf16 v[248:251], v[116:119], v[124:127], v[248:251]\n\t" \
  "v_mfma_f32_16x16x32_bf16 v[252:255], v[96:99], v[88:91], v[252:255]\n\t" \
  "v_mfma_f32_16x16x32_bf16 v[252:255], v[132:135], v[124:127], v[252:255]\n\t" \
  "v_mfma_f32_16x16x32_bf16 v[188:191], v[132:135], v[84:87], v[188:191]\n\t" \
  "v_mfma_f32_16x16x32_bf16 v[188:191], v[96:99], v[92:95], v[188:191]\n\t" \
  "s_barrier\n\t" \
  "v_mfma_f32_16x16x32_bf16 v[196:199], v[96:99], v[112:115], v[196:199]\n\t" \
  "ds_read_b64_tr_b16 v[64:65], v58 offset:34816\n\t" \
  "v_mfma_f32_16x16x32_bf16 v[196:199], v[132:135], v[100:103], v[196:199]\n\t" \
  "ds_read_b64_tr_b16 v[66:67], v58 offset:35328\n\t" \
  "v_mfma_f32_16x16x32_bf16 v[204:207], v[132:135], v[120:123], v[204:207]\n\t" \
  "ds_read_b128 v[68:71], v56 offset:2048\n\t" \
  "v_mfma_f32_16x16x32_bf16 v[204:207], v[96:99], v[128:131], v[204:207]\n\t" \
  "ds_read_b64_tr_b16 v[72:73], v58 offset:38912\n\t" \
  "v_mfma_f32_16x16x32_bf16 v[212:215], v[96:99], v[144:147], v[212:215]\n\t" \
  "ds_read_b64_tr_b16 v[74:75], v58 offset:39424\n\t" \
  "v_mfma_f32_16x16x32_bf16 v[212:215], v[132:135], v[136:139], v[212:215]\n\t" \
  "ds_read_b128 v[104:107], v57 offset:2048\n\t" \
  "v_mfma_f32_16x16x32_bf16 v[152:155], v[108:111], v[92:95], v[152:155]\n\t" \
  "ds_read_b128 v[80:83], v57 offset:4096\n\t" \
  "v_mfma_f32_16x16x32_bf16 v[152:155], v[140:143], v[84:87], v[152:155]\n\t" \
  "ds_read_b128 v[116:119], v56 offset:4096\n\t" \
  "v_mfma_f32_16x16x32_bf16 v[160:163], v[140:143], v[100:103], v[160:163]\n\t" \
  "ds_read_b128 v[88:91], v56 offset:6144\n\t" \
  "v_mfma_f32_16x16x32_bf16 v[160:163], v[108:111], v[112:115], v[160:163]\n\t" \
  "ds_read_b128 v[124:127], v57 offset:6144\n\t" \
  "v_mfma_f32_16x16x32_bf16 v[168:171], v[108:111], v[128:131], v[168:171]\n\t" \
  "ds_read_b128 v[96:99], v57 offset:8192\n\t" \
  "v_mfma_f32_16x16x32_bf16 v[168:171], v[140:143], v[120:123], v[168:171]\n\t" \
  "ds_read_b128 v[132:135], v56 offset:8192\n\t" \
  "v_mfma_f32_16x16x32_bf16 v[176:179], v[140:143], v[136:139], v[176:179]\n\t" \
  "ds_read_b64_tr_b16 v[140:141], v59 offset:34816\n\t" \
  "v_mfma_f32_16x16x32_bf16 v[176:179], v[108:111], v[144:147], v[176:179]\n\t" \
  "ds_read_b64_tr_b16 v[142:143], v59 offset:35328\n\t" \
  "v_mfma_f32_16x16x32_bf16 v[156:159], v[76:79], v[92:95], v[156:159]\n\t" \
  "ds_read_b64_tr_b16 v[108:109], v59 offset:38912\n\t" \
  "v_mfma_f32_16x16x32_bf16 v[156:159], v[148:151], v[84:87], v[156:159]\n\t" \
  "ds_read_b64_tr_b16 v[110:111], v59 offset:39424\n\t" \
  "v_mfma_f32_16x16x32_bf16 v[164:167], v[148:151], v[100:103], v[164:167]\n\t" \
  "ds_read_b64_tr_b16 v[92:93], v58 offset:26624\n\t" \
  "v_mfma_f32_16x16x32_bf16 v[164:167], v[76:79], v[112:115], v[164:167]\n\t" \
  "ds_read_b64_tr_b16 v[94:95], v58 offset:27136\n\t" \
  "v_mfma_f32_16x16x32_bf16 v[172:175], v[76:79], v[128:131], v[172:175]\n\t" \
  "ds_read_b64_tr_b16 v[84:85], v58 offset:30720\n\t" \
  "v_mfma_f32_16x16x32_bf16 v[172:175], v[148:151], v[120:123], v[172:175]\n\t" \
  "ds_read_b64_tr_b16 v[86:87], v58 offset:31232\n\t" \
  "v_mfma_f32_16x16x32_bf16 v[180:183], v[148:151], v[136:139], v[180:183]\n\t" \
  "ds_read_b128 v[100:103], v56 offset:0\n\t" \
  "v_mfma_f32_16x16x32_bf16 v[180:183], v[76:79], v[144:147], v[180:183]\n\t" \
  "ds_read_b128 v[112:115], v57 offset:0\n\t" \
  "s_waitcnt lgkmcnt(7)\n\t" \
  "v_mfma_f32_16x16x32_bf16 v[216:219], v[68:71], v[64:67], v[216:219]\n\t" \
  "ds_read_b64_tr_b16 v[128:129], v59 offset:26624\n\t" \
  "v_mfma_f32_16x16x32_bf16 v[216:219], v[104:107], v[72:75], v[216:219]\n\t" \
  "ds_read_b64_tr_b16 v[130:131], v59 offset:27136\n\t" \
  "v_mfma_f32_16x16x32_bf16 v[224:227], v[80:83], v[72:75], v[224:227]\n\t" \
  "ds_read_b64_tr_b16 v[120:121], v59 offset:30720\n\t" \
  "v_mfma_f32_16x16x32_bf16 v[224:227], v[116:119], v[64:67], v[224:227]\n\t" \
  "ds_read_b64_tr_b16 v[122:123], v59 offset:31232\n\t" \
  "v_mfma_f32_16x16x32_bf16 v[232:235], v[88:91], v[64:67], v[232:235]\n\t" \
  "ds_read_b64_tr_b16 v[136:137], v58 offset:18432\n\t" \
  "v_mfma_f32_16x16x32_bf16 v[232:235], v[124:127], v[72:75], v[232:235]\n\t" \
  "ds_read_b64_tr_b16 v[138:139], v58 offset:18944\n\t" \
  "v_mfma_f32_16x16x32_bf16 v[240:243], v[96:99], v[72:75], v[240:243]\n\t" \
  "ds_read_b64_tr_b16 v[148:149], v58 offset:22528\n\t" \
  "v_mfma_f32_16x16x32_bf16 v[240:243], v[132:135], v[64:67], v[240:243]\n\t" \
  "ds_read_b64_tr_b16 v[150:151], v58 offset:23040\n\t" \
  "v_mfma_f32_16x16x32_bf16 v[220:223], v[68:71], v[140:143], v[220:223]\n\t" \
  "ds_read_b64_tr_b16 v[76:77], v59 offset:18432\n\t" \
  "s_waitcnt lgkmcnt(7)\n\t" \
  "v_mfma_f32_16x16x32_bf16 v[220:223], v[104:107], v[108:111], v[220:223]\n\t" \
  "ds_read_b64_tr_b16 v[78:79], v59 offset:18944\n\t" \
  "v_mfma_f32_16x16x32_bf16 v[228:231], v[80:83], v[108:111], v[228:231]\n\t" \
  "ds_read_b64_tr_b16 v[144:145], v59 offset:22528\n\t" \
  "v_mfma_f32_16x16x32_bf16 v[228:231], v[116:119], v[140:143], v[228:231]\n\t" \
  "ds_read_b64_tr_b16 v[146:147], v59 offset:23040\n\t" \
  "v_mfma_f32_16x16x32_bf16 v[236:239], v[88:91], v[140:143], v[236:239]\n\t" \
  "v_mfma_f32_16x16x32_bf16 v[236:239], v[124:127], v[108:111], v[236:239]\n\t" \
  "v_mfma_f32_16x16x32_bf16 v[244:247], v[96:99], v[108:111], v[244:247]\n\t" \
  "v_mfma_f32_16x16x32_bf16 v[244:247], v[132:135], v[140:143], v[244:247]\n\t" \
  "v_mfma_f32_16x16x32_bf16 v[184:187], v[92:95], v[68:71], v[184:187]\n\t" \
  "v_mfma_f32_16x16x32_bf16 v[184:187], v[84:87], v[104:107], v[184:187]\n\t" \
  "v_mfma_f32_16x16x32_bf16 v[192:195], v[84:87], v[80:83], v[192:195]\n\t" \
  "v_mfma_f32_16x16x32_bf16 v[192:195], v[92:95], v[116:119], v[192:195]\n\t" \
  "v_mfma_f32_16x16x32_bf16 v[200:203], v[92:95], v[88:91], v[200:203]\n\t" \
  "v_mfma_f32_16x16x32_bf16 v[200:203], v[84:87], v[124:127], v[200:203]\n\t" \
  "v_mfma_f32_16x16x32_bf16 v[208:211], v[84:87], v[96:99], v[208:211]\n\t" \
  "v_mfma_f32_16x16x32_bf16 v[208:211], v[92:95], v[132:135], v[208:211]\n\t" \
  "v_mfma_f32_16x16x32_bf16 v[248:251], v[92:95], v[100:103], v[248:251]\n\t" \
  "v_mfma_f32_16x16x32_bf16 v[248:251], v[84:87], v[112:115], v[248:251]\n\t" \
  "v_mfma_f32_16x16x32_bf16 v[252:255], v[128:131], v[100:103], v[252:255]\n\t" \
  "s_waitcnt lgkmcnt(0)\n\t" \
  "v_mfma_f32_16x16x32_bf16 v[252:255], v[120:123], v[112:115], v[252:255]\n\t" \
  "v_mfma_f32_16x16x32_bf16 v[188:191], v[120:123], v[104:107], v[188:191]\n\t" \
  "v_mfma_f32_16x16x32_bf16 v[188:191], v[128:131], v[68:71], v[188:191]\n\t" \
  "s_barrier\n\t" \
  "v_mfma_f32_16x16x32_bf16 v[196:199], v[128:131], v[116:119], v[196:199]\n\t" \
  "ds_read_b64_tr_b16 v[72:73], v62 offset:34816\n\t" \
  "v_mfma_f32_16x16x32_bf16 v[196:199], v[120:123], v[80:83], v[196:199]\n\t" \
  "ds_read_b64_tr_b16 v[74:75], v62 offset:35328\n\t" \
  "v_mfma_f32_16x16x32_bf16 v[204:207], v[120:123], v[124:127], v[204:207]\n\t" \
  "ds_read_b128 v[64:67], v60 offset:2048\n\t" \
  "v_mfma_f32_16x16x32_bf16 v[204:207], v[128:131], v[88:91], v[204:207]\n\t" \
  "ds_read_b64_tr_b16 v[108:109], v62 offset:38912\n\t" \
  "v_mfma_f32_16x16x32_bf16 v[212:215], v[128:131], v[132:135], v[212:215]\n\t" \
  "ds_read_b64_tr_b16 v[110:111], v62 offset:39424\n\t" \
  "v_mfma_f32_16x16x32_bf16 v[212:215], v[120:123], v[96:99], v[212:215]\n\t" \
  "ds_read_b128 v[140:143], v61 offset:2048\n\t" \
  "v_mfma_f32_16x16x32_bf16 v[152:155], v[136:139], v[68:71], v[152:155]\n\t" \
  "ds_read_b128 v[92:95], v61 offset:4096\n\t" \
  "v_mfma_f32_16x16x32_bf16 v[152:155], v[148:151], v[104:107], v[152:155]\n\t" \
  "ds_read_b128 v[84:87], v60 offset:4096\n\t" \
  "v_mfma_f32_16x16x32_bf16 v[160:163], v[148:151], v[80:83], v[160:163]\n\t" \
  "ds_read_b128 v[100:103], v60 offset:6144\n\t" \
  "v_mfma_f32_16x16x32_bf16 v[160:163], v[136:139], v[116:119], v[160:163]\n\t" \
  "ds_read_b128 v[112:115], v61 offset:6144\n\t" \
  "v_mfma_f32_16x16x32_bf16 v[168:171], v[136:139], v[88:91], v[168:171]\n\t" \
  "ds_read_b128 v[128:131], v61 offset:8192\n\t" \
  "v_mfma_f32_16x16x32_bf16 v[168:171], v[148:151], v[124:127], v[168:171]\n\t" \
  "ds_read_b128 v[120:123], v60 offset:8192\n\t" \
  "v_mfma_f32_16x16x32_bf16 v[176:179], v[148:151], v[96:99], v[176:179]\n\t" \
  "ds_read_b64_tr_b16 v[148:149], v63 offset:34816\n\t" \
  "v_mfma_f32_16x16x32_bf16 v[176:179], v[136:139], v[132:135], v[176:179]\n\t" \
  "ds_read_b64_tr_b16 v[150:151], v63 offset:35328\n\t" \
  "v_mfma_f32_16x16x32_bf16 v[156:159], v[76:79], v[68:71], v[156:159]\n\t" \
  "ds_read_b64_tr_b16 v[136:137], v63 offset:38912\n\t" \
  "v_mfma_f32_16x16x32_bf16 v[156:159], v[144:147], v[104:107], v[156:159]\n\t" \
  "ds_read_b64_tr_b16 v[138:139], v63 offset:39424\n\t" \
  "v_mfma_f32_16x16x32_bf16 v[164:167], v[144:147], v[80:83], v[164:167]\n\t" \
  "ds_read_b64_tr_b16 v[68:69], v62 offset:26624\n\t" \
  "v_mfma_f32_16x16x32_bf16 v[164:167], v[76:79], v[116:119], v[164:167]\n\t" \
  "ds_read_b64_tr_b16 v[70:71], v62 offset:27136\n\t" \
  "v_mfma_f32_16x16x32_bf16 v[172:175], v[76:79], v[88:91], v[172:175]\n\t" \
  "ds_read_b64_tr_b16 v[104:105], v62 offset:30720\n\t" \
  "v_mfma_f32_16x16x32_bf16 v[172:175], v[144:147], v[124:127], v[172:175]\n\t" \
  "ds_read_b64_tr_b16 v[106:107], v62 offset:31232\n\t" \
  "v_mfma_f32_16x16x32_bf16 v[180:183], v[144:147], v[96:99], v[180:183]\n\t" \
  "ds_read_b128 v[80:83], v60 offset:0\n\t" \
  "v_mfma_f32_16x16x32_bf16 v[180:183], v[76:79], v[132:135], v[180:183]\n\t" \
  "ds_read_b128 v[116:119], v61 offset:0\n\t" \
  "s_waitcnt lgkmcnt(7)\n\t" \
  "v_mfma_f32_16x16x32_bf16 v[216:219], v[64:67], v[72:75], v[216:219]\n\t" \
  "ds_read_b64_tr_b16 v[88:89], v63 offset:26624\n\t" \
  "v_mfma_f32_16x16x32_bf16 v[216:219], v[140:143], v[108:111], v[216:219]\n\t" \
  "505:\n\t" \
  "s_cbranch_execz 509f\n\t" \
  "ds_read_b64_tr_b16 v[90:91], v63 offset:27136\n\t" \
  "v_mfma_f32_16x16x32_bf16 v[224:227], v[92:95], v[108:111], v[224:227]\n\t" \
  "ds_read_b64_tr_b16 v[124:125], v63 offset:30720\n\t" \
  "v_mfma_f32_16x16x32_bf16 v[224:227], v[84:87], v[72:75], v[224:227]\n\t" \
  "ds_read_b64_tr_b16 v[126:127], v63 offset:31232\n\t" \
  "v_mfma_f32_16x16x32_bf16 v[232:235], v[100:103], v[72:75], v[232:235]\n\t" \
  "ds_read_b64_tr_b16 v[96:97], v62 offset:18432\n\t" \
  "v_mfma_f32_16x16x32_bf16 v[232:235], v[112:115], v[108:111], v[232:235]\n\t" \
  "ds_read_b64_tr_b16 v[98:99], v62 offset:18944\n\t" \
  "v_mfma_f32_16x16x32_bf16 v[240:243], v[128:131], v[108:111], v[240:243]\n\t" \
  "ds_read_b64_tr_b16 v[144:145], v62 offset:22528\n\t" \
  "v_mfma_f32_16x16x32_bf16 v[240:243], v[120:123], v[72:75], v[240:243]\n\t" \
  "ds_read_b64_tr_b16 v[146:147], v62 offset:23040\n\t" \
  "v_mfma_f32_16x16x32_bf16 v[220:223], v[64:67], v[148:151], v[220:223]\n\t" \
  "ds_read_b64_tr_b16 v[76:77], v63 offset:18432\n\t" \
  "s_waitcnt lgkmcnt(7)\n\t" \
  "v_mfma_f32_16x16x32_bf16 v[220:223], v[140:143], v[136:139], v[220:223]\n\t" \
  "ds_read_b64_tr_b16 v[78:79], v63 offset:18944\n\t" \
  "v_mfma_f32_16x16x32_bf16 v[228:231], v[92:95], v[136:139], v[228:231]\n\t" \
  "ds_read_b64_tr_b16 v[132:133], v63 offset:22528\n\t" \
  "v_mfma_f32_16x16x32_bf16 v[228:231], v[84:87], v[148:151], v[228:231]\n\t" \
  "ds_read_b64_tr_b16 v[134:135], v63 offset:23040\n\t" \
  "v_mfma_f32_16x16x32_bf16 v[236:239], v[100:103], v[148:151], v[236:239]\n\t" \
  "v_mfma_f32_16x16x32_bf16 v[236:239], v[112:115], v[136:139], v[236:239]\n\t" \
  "v_mfma_f32_16x16x32_bf16 v[244:247], v[128:131], v[136:139], v[244:247]\n\t" \
  "v_mfma_f32_16x16x32_bf16 v[244:247], v[120:123], v[148:151], v[244:247]\n\t" \
  "v_mfma_f32_16x16x32_bf16 v[184:187], v[68:71], v[64:67], v[184:187]\n\t" \
  "v_mfma_f32_16x16x32_bf16 v[184:187], v[104:107], v[140:143], v[184:187]\n\t" \
  "v_mfma_f32_16x16x32_bf16 v[192:195], v[104:107], v[92:95], v[192:195]\n\t" \
  "v_mfma_f32_16x16x32_bf16 v[192:195], v[68:71], v[84:87], v[192:195]\n\t" \
  "v_mfma_f32_16x16x32_bf16 v[200:203], v[68:71], v[100:103], v[200:203]\n\t" \
  "v_mfma_f32_16x16x32_bf16 v[200:203], v[104:107], v[112:115], v[200:203]\n\t" \
  "v_mfma_f32_16x16x32_bf16 v[208:211], v[104:107], v[128:131], v[208:211]\n\t" \
  "v_mfma_f32_16x16x32_bf16 v[208:211], v[68:71], v[120:123], v[208:211]\n\t" \
  "v_mfma_f32_16x16x32_bf16 v[248:251], v[68:71], v[80:83], v[248:251]\n\t" \
  "v_mfma_f32_16x16x32_bf16 v[248:251], v[104:107], v[116:119], v[248:251]\n\t" \
  "v_mfma_f32_16x16x32_bf16 v[252:255], v[88:91], v[80:83], v[252:255]\n\t" \
  "s_waitcnt lgkmcnt(0)\n\t" \
  "v_mfma_f32_16x16x32_bf16 v[252:255], v[124:127], v[116:119], v[252:255]\n\t" \
  "v_mfma_f32_16x16x32_bf16 v[188:191], v[124:127], v[140:143], v[188:191]\n\t" \
  "v_mfma_f32_16x16x32_bf16 v[188:191], v[88:91], v[64:67], v[188:191]\n\t" \
  "s_barrier\n\t" \
  "v_mfma_f32_16x16x32_bf16 v[196:199], v[88:91], v[84:87], v[196:199]\n\t" \
  "ds_read_b64_tr_b16 v[108:109], v54 offset:34816\n\t" \
  "v_mfma_f32_16x16x32_bf16 v[196:199], v[124:127], v[92:95], v[196:199]\n\t" \
  "ds_read_b64_tr_b16 v[110:111], v54 offset:35328\n\t" \
  "v_mfma_f32_16x16x32_bf16 v[204:207], v[124:127], v[112:115], v[204:207]\n\t" \
  "ds_read_b128 v[72:75], v52 offset:2048\n\t" \
  "v_mfma_f32_16x16x32_bf16 v[204:207], v[88:91], v[100:103], v[204:207]\n\t" \
  "ds_read_b64_tr_b16 v[136:137], v54 offset:38912\n\t" \
  "v_mfma_f32_16x16x32_bf16 v[212:215], v[88:91], v[120:123], v[212:215]\n\t" \
  "ds_read_b64_tr_b16 v[138:139], v54 offset:39424\n\t" \
  "v_mfma_f32_16x16x32_bf16 v[212:215], v[124:127], v[128:131], v[212:215]\n\t" \
  "ds_read_b128 v[148:151], v53 offset:2048\n\t" \
  "v_mfma_f32_16x16x32_bf16 v[152:155], v[96:99], v[64:67], v[152:155]\n\t" \
  "ds_read_b128 v[68:71], v53 offset:4096\n\t" \
  "v_mfma_f32_16x16x32_bf16 v[152:155], v[144:147], v[140:143], v[152:155]\n\t" \
  "ds_read_b128 v[104:107], v52 offset:4096\n\t" \
  "v_mfma_f32_16x16x32_bf16 v[160:163], v[144:147], v[92:95], v[160:163]\n\t" \
  "ds_read_b128 v[80:83], v52 offset:6144\n\t" \
  "v_mfma_f32_16x16x32_bf16 v[160:163], v[96:99], v[84:87], v[160:163]\n\t" \
  "ds_read_b128 v[116:119], v53 offset:6144\n\t" \
  "v_mfma_f32_16x16x32_bf16 v[168:171], v[96:99], v[100:103], v[168:171]\n\t" \
  "ds_read_b128 v[88:91], v53 offset:8192\n\t" \
  "v_mfma_f32_16x16x32_bf16 v[168:171], v[144:147], v[112:115], v[168:171]\n\t" \
  "ds_read_b128 v[124:127], v52 offset:8192\n\t" \
  "v_mfma_f32_16x16x32_bf16 v[176:179], v[144:147], v[128:131], v[176:179]\n\t" \
  "ds_read_b64_tr_b16 v[144:145], v55 offset:34816\n\t" \
  "v_mfma_f32_16x16x32_bf16 v[176:179], v[96:99], v[120:123], v[176:179]\n\t" \
  "ds_read_b64_tr_b16 v[146:147], v55 offset:35328\n\t" \
  "v_mfma_f32_16x16x32_bf16 v[156:159], v[76:79], v[64:67], v[156:159]\n\t" \
  "ds_read_b64_tr_b16 v[96:97], v55 offset:38912\n\t" \
  "v_mfma_f32_16x16x32_bf16 v[156:159], v[132:135], v[140:143], v[156:159]\n\t" \
  "ds_read_b64_tr_b16 v[98:99], v55 offset:39424\n\t" \
  "v_mfma_f32_16x16x32_bf16 v[164:167], v[132:135], v[92:95], v[164:167]\n\t" \
  "ds_read_b64_tr_b16 v[64:65], v54 offset:26624\n\t" \
  "v_mfma_f32_16x16x32_bf16 v[164:167], v[76:79], v[84:87], v[164:167]\n\t" \
  "ds_read_b64_tr_b16 v[66:67], v54 offset:27136\n\t" \
  "v_mfma_f32_16x16x32_bf16 v[172:175], v[76:79], v[100:103], v[172:175]\n\t" \
  "ds_read_b64_tr_b16 v[140:141], v54 offset:30720\n\t" \
  "v_mfma_f32_16x16x32_bf16 v[172:175], v[132:135], v[112:115], v[172:175]\n\t" \
  "ds_read_b64_tr_b16 v[142:143], v54 offset:31232\n\t" \
  "v_mfma_f32_16x16x32_bf16 v[180:183], v[132:135], v[128:131], v[180:183]\n\t" \
  "ds_read_b128 v[92:95], v52 offset:0\n\t" \
  "v_mfma_f32_16x16x32_bf16 v[180:183], v[76:79], v[120:123], v[180:183]\n\t" \
  "ds_read_b128 v[84:87], v53 offset:0\n\t" \
  "s_waitcnt lgkmcnt(7)\n\t" \
  "v_mfma_f32_16x16x32_bf16 v[216:219], v[72:75], v[108:111], v[216:219]\n\t" \
  "ds_read_b64_tr_b16 v[100:101], v55 offset:26624\n\t" \
  "v_mfma_f32_16x16x32_bf16 v[216:219], v[148:151], v[136:139], v[216:219]\n\t" \
  "ds_read_b64_tr_b16 v[102:103], v55 offset:27136\n\t" \
  "v_mfma_f32_16x16x32_bf16 v[224:227], v[68:71], v[136:139], v[224:227]\n\t" \
  "ds_read_b64_tr_b16 v[112:113], v55 offset:30720\n\t" \
  "v_mfma_f32_16x16x32_bf16 v[224:227], v[104:107], v[108:111], v[224:227]\n\t" \
  "ds_read_b64_tr_b16 v[114:115], v55 offset:31232\n\t" \
  "v_mfma_f32_16x16x32_bf16 v[232:235], v[80:83], v[108:111], v[232:235]\n\t" \
  "ds_read_b64_tr_b16 v[128:129], v54 offset:18432\n\t" \
  "v_mfma_f32_16x16x32_bf16 v[232:235], v[116:119], v[136:139], v[232:235]\n\t" \
  "ds_read_b64_tr_b16 v[130:131], v54 offset:18944\n\t" \
  "v_mfma_f32_16x16x32_bf16 v[240:243], v[88:91], v[136:139], v[240:243]\n\t" \
  "ds_read_b64_tr_b16 v[132:133], v54 offset:22528\n\t" \
  "v_mfma_f32_16x16x32_bf16 v[240:243], v[124:127], v[108:111], v[240:243]\n\t" \
  "ds_read_b64_tr_b16 v[134:135], v54 offset:23040\n\t" \
  "v_mfma_f32_16x16x32_bf16 v[220:223], v[72:75], v[144:147], v[220:223]\n\t" \
  "ds_read_b64_tr_b16 v[76:77], v55 offset:18432\n\t" \
  "s_waitcnt lgkmcnt(7)\n\t" \
  "v_mfma_f32_16x16x32_bf16 v[220:223], v[148:151], v[96:99], v[220:223]\n\t" \
  "ds_read_b64_tr_b16 v[78:79], v55 offset:18944\n\t" \
  "v_mfma_f32_16x16x32_bf16 v[228:231], v[68:71], v[96:99], v[228:231]\n\t" \
  "ds_read_b64_tr_b16 v[120:121], v55 offset:22528\n\t" \
  "v_mfma_f32_16x16x32_bf16 v[228:231], v[104:107], v[144:147], v[228:231]\n\t" \
  "ds_read_b64_tr_b16 v[122:123], v55 offset:23040\n\t" \
  "v_mfma_f32_16x16x32_bf16 v[236:239], v[80:83], v[144:147], v[236:239]\n\t" \
  "v_mfma_f32_16x16x32_bf16 v[236:239], v[116:119], v[96:99], v[236:239]\n\t" \
  "v_mfma_f32_16x16x32_bf16 v[244:247], v[88:91], v[96:99], v[244:247]\n\t" \
  "v_mfma_f32_16x16x32_bf16 v[244:247], v[124:127], v[144:147], v[244:247]\n\t" \
  "v_mfma_f32_16x16x32_bf16 v[184:187], v[64:67], v[72:75], v[184:187]\n\t" \
  "v_mfma_f32_16x16x32_bf16 v[184:187], v[140:143], v[148:151], v[184:187]\n\t" \
  "v_mfma_f32_16x16x32_bf16 v[192:195], v[140:143], v[68:71], v[192:195]\n\t" \
  "v_mfma_f32_16x16x32_bf16 v[192:195], v[64:67], v[104:107], v[192:195]\n\t" \
  "v_mfma_f32_16x16x32_bf16 v[200:203], v[64:67], v[80:83], v[200:203]\n\t" \
  "v_mfma_f32_16x16x32_bf16 v[200:203], v[140:143], v[116:119], v[200:203]\n\t" \
  "v_mfma_f32_16x16x32_bf16 v[208:211], v[140:143], v[88:91], v[208:211]\n\t" \
  "v_mfma_f32_16x16x32_bf16 v[208:211], v[64:67], v[124:127], v[208:211]\n\t" \
  "v_mfma_f32_16x16x32_bf16 v[248:251], v[64:67], v[92:95], v[248:251]\n\t" \
  "v_mfma_f32_16x16x32_bf16 v[248:251], v[140:143], v[84:87], v[248:251]\n\t" \
  "v_mfma_f32_16x16x32_bf16 v[252:255], v[100:103], v[92:95], v[252:255]\n\t" \
  "s_waitcnt lgkmcnt(0)\n\t" \
  "v_mfma_f32_16x16x32_bf16 v[252:255], v[112:115], v[84:87], v[252:255]\n\t" \
  "v_mfma_f32_16x16x32_bf16 v[188:191], v[112:115], v[148:151], v[188:191]\n\t" \
  "v_mfma_f32_16x16x32_bf16 v[188:191], v[100:103], v[72:75], v[188:191]\n\t" \
  "s_barrier\n\t" \
  "v_mfma_f32_16x16x32_bf16 v[196:199], v[100:103], v[104:107], v[196:199]\n\t" \
  "ds_read_b64_tr_b16 v[96:97], v58 offset:34816\n\t" \
  "v_mfma_f32_16x16x32_bf16 v[196:199], v[112:115], v[68:71], v[196:199]\n\t" \
  "ds_read_b64_tr_b16 v[98:99], v58 offset:35328\n\t" \
  "v_mfma_f32_16x16x32_bf16 v[204:207], v[112:115], v[116:119], v[204:207]\n\t" \
  "ds_read_b128 v[136:139], v56 offset:2048\n\t" \
  "v_mfma_f32_16x16x32_bf16 v[204:207], v[100:103], v[80:83], v[204:207]\n\t" \
  "ds_read_b64_tr_b16 v[64:65], v58 offset:38912\n\t" \
  "v_mfma_f32_16x16x32_bf16 v[212:215], v[100:103], v[124:127], v[212:215]\n\t" \
  "ds_read_b64_tr_b16 v[66:67], v58 offset:39424\n\t" \
  "v_mfma_f32_16x16x32_bf16 v[212:215], v[112:115], v[88:91], v[212:215]\n\t" \
  "ds_read_b128 v[108:111], v57 offset:2048\n\t" \
  "v_mfma_f32_16x16x32_bf16 v[152:155], v[128:131], v[72:75], v[152:155]\n\t" \
  "ds_read_b128 v[144:147], v57 offset:4096\n\t" \
  "v_mfma_f32_16x16x32_bf16 v[152:155], v[132:135], v[148:151], v[152:155]\n\t" \
  "ds_read_b128 v[140:143], v56 offset:4096\n\t" \
  "v_mfma_f32_16x16x32_bf16 v[160:163], v[132:135], v[68:71], v[160:163]\n\t" \
  "ds_read_b128 v[112:115], v56 offset:6144\n\t" \
  "v_mfma_f32_16x16x32_bf16 v[160:163], v[128:131], v[104:107], v[160:163]\n\t" \
  "ds_read_b64_tr_b16 v[92:93], v59 offset:34816\n\t" \
  "v_mfma_f32_16x16x32_bf16 v[168:171], v[128:131], v[80:83], v[168:171]\n\t" \
  "ds_read_b64_tr_b16 v[94:95], v59 offset:35328\n\t" \
  "v_mfma_f32_16x16x32_bf16 v[168:171], v[132:135], v[116:119], v[168:171]\n\t" \
  "ds_read_b64_tr_b16 v[84:85], v59 offset:38912\n\t" \
  "v_mfma_f32_16x16x32_bf16 v[176:179], v[132:135], v[88:91], v[176:179]\n\t" \
  "ds_read_b128 v[132:135], v57 offset:6144\n\t" \
  "v_mfma_f32_16x16x32_bf16 v[176:179], v[128:131], v[124:127], v[176:179]\n\t" \
  "ds_read_b128 v[128:131], v57 offset:8192\n\t" \
  "v_mfma_f32_16x16x32_bf16 v[156:159], v[76:79], v[72:75], v[156:159]\n\t" \
  "ds_read_b64_tr_b16 v[86:87], v59 offset:39424\n\t" \
  "v_mfma_f32_16x16x32_bf16 v[156:159], v[120:123], v[148:151], v[156:159]\n\t" \
  "ds_read_b128 v[148:151], v56 offset:8192\n\t" \
  "v_mfma_f32_16x16x32_bf16 v[164:167], v[120:123], v[68:71], v[164:167]\n\t" \
  "ds_read_b64_tr_b16 v[100:101], v58 offset:26624\n\t" \
  "v_mfma_f32_16x16x32_bf16 v[164:167], v[76:79], v[104:107], v[164:167]\n\t" \
  "ds_read_b64_tr_b16 v[102:103], v58 offset:27136\n\t" \
  "v_mfma_f32_16x16x32_bf16 v[172:175], v[76:79], v[80:83], v[172:175]\n\t" \
  "ds_read_b64_tr_b16 v[72:73], v58 offset:30720\n\t" \
  "v_mfma_f32_16x16x32_bf16 v[172:175], v[120:123], v[116:119], v[172:175]\n\t" \
  "ds_read_b64_tr_b16 v[74:75], v58 offset:31232\n\t" \
  "v_mfma_f32_16x16x32_bf16 v[180:183], v[120:123], v[88:91], v[180:183]\n\t" \
  "ds_read_b128 v[68:71], v56 offset:0\n\t" \
  "v_mfma_f32_16x16x32_bf16 v[180:183], v[76:79], v[124:127], v[180:183]\n\t" \
  "ds_read_b128 v[104:107], v57 offset:0\n\t" \
  "s_waitcnt lgkmcnt(7)\n\t" \
  "v_mfma_f32_16x16x32_bf16 v[216:219], v[136:139], v[96:99], v[216:219]\n\t" \
  "ds_read_b64_tr_b16 v[80:81], v59 offset:26624\n\t" \
  "v_mfma_f32_16x16x32_bf16 v[216:219], v[108:111], v[64:67], v[216:219]\n\t" \
  "ds_read_b64_tr_b16 v[82:83], v59 offset:27136\n\t" \
  "v_mfma_f32_16x16x32_bf16 v[224:227], v[144:147], v[64:67], v[224:227]\n\t" \
  "ds_read_b64_tr_b16 v[88:89], v59 offset:30720\n\t" \
  "v_mfma_f32_16x16x32_bf16 v[224:227], v[140:143], v[96:99], v[224:227]\n\t" \
  "ds_read_b64_tr_b16 v[90:91], v59 offset:31232\n\t" \
  "v_mfma_f32_16x16x32_bf16 v[232:235], v[112:115], v[96:99], v[232:235]\n\t" \
  "ds_read_b64_tr_b16 v[116:117], v58 offset:18432\n\t" \
  "v_mfma_f32_16x16x32_bf16 v[232:235], v[132:135], v[64:67], v[232:235]\n\t" \
  "ds_read_b64_tr_b16 v[118:119], v58 offset:18944\n\t" \
  "v_mfma_f32_16x16x32_bf16 v[240:243], v[128:131], v[64:67], v[240:243]\n\t" \
  "ds_read_b64_tr_b16 v[120:121], v58 offset:22528\n\t" \
  "s_waitcnt lgkmcnt(7)\n\t" \
  "v_mfma_f32_16x16x32_bf16 v[240:243], v[148:151], v[96:99], v[240:243]\n\t" \
  "ds_read_b64_tr_b16 v[122:123], v58 offset:23040\n\t" \
  "v_mfma_f32_16x16x32_bf16 v[220:223], v[136:139], v[92:95], v[220:223]\n\t" \
  "ds_read_b64_tr_b16 v[124:125], v59 offset:18432\n\t" \
  "v_mfma_f32_16x16x32_bf16 v[220:223], v[108:111], v[84:87], v[220:223]\n\t" \
  "ds_read_b64_tr_b16 v[126:127], v59 offset:18944\n\t" \
  "v_mfma_f32_16x16x32_bf16 v[228:231], v[144:147], v[84:87], v[228:231]\n\t" \
  "ds_read_b64_tr_b16 v[76:77], v59 offset:22528\n\t" \
  "v_mfma_f32_16x16x32_bf16 v[228:231], v[140:143], v[92:95], v[228:231]\n\t" \
  "ds_read_b64_tr_b16 v[78:79], v59 offset:23040\n\t" \
  "v_mfma_f32_16x16x32_bf16 v[236:239], v[112:115], v[92:95], v[236:239]\n\t" \
  "v_mfma_f32_16x16x32_bf16 v[236:239], v[132:135], v[84:87], v[236:239]\n\t" \
  "v_mfma_f32_16x16x32_bf16 v[244:247], v[128:131], v[84:87], v[244:247]\n\t" \
  "v_mfma_f32_16x16x32_bf16 v[244:247], v[148:151], v[92:95], v[244:247]\n\t" \
  "v_mfma_f32_16x16x32_bf16 v[184:187], v[100:103], v[136:139], v[184:187]\n\t" \
  "v_mfma_f32_16x16x32_bf16 v[184:187], v[72:75], v[108:111], v[184:187]\n\t" \
  "v_mfma_f32_16x16x32_bf16 v[192:195], v[72:75], v[144:147], v[192:195]\n\t" \
  "v_and_b32 v14, -16, v32\n\t" \
  "v_mfma_f32_16x16x32_bf16 v[192:195], v[100:103], v[140:143], v[192:195]\n\t" \
  "v_mfma_f32_16x16x32_bf16 v[200:203], v[100:103], v[112:115], v[200:203]\n\t" \
  "ds_read_b128 v[10:13], v14 offset:128\n\t" \
  "v_mfma_f32_16x16x32_bf16 v[200:203], v[72:75], v[132:135], v[200:203]\n\t" \
  "v_mfma_f32_16x16x32_bf16 v[208:211], v[72:75], v[128:131], v[208:211]\n\t" \
  "v_mfma_f32_16x16x32_bf16 v[208:211], v[100:103], v[148:151], v[208:211]\n\t" \
  "v_mfma_f32_16x16x32_bf16 v[248:251], v[100:103], v[68:71], v[248:251]\n\t" \
  "v_mfma_f32_16x16x32_bf16 v[248:251], v[72:75], v[104:107], v[248:251]\n\t" \
  "s_waitcnt lgkmcnt(1)\n\t" \
  "v_mfma_f32_16x16x32_bf16 v[252:255], v[80:83], v[68:71], v[252:255]\n\t" \
  "s_waitcnt lgkmcnt(0)\n\t" \
  "v_and_b32 v10, v10, v11\n\t" \
  "v_and_b32 v12, v12, v13\n\t" \
  "v_mfma_f32_16x16x32_bf16 v[252:255], v[88:91], v[104:107], v[252:255]\n\t" \
  "v_and_b32 v10, v10, v12\n\t" \
  "v_mfma_f32_16x16x32_bf16 v[188:191], v[88:91], v[108:111], v[188:191]\n\t" \
  "v_readfirstlane_b32 vcc_lo, v10\n\t" \
  "v_mfma_f32_16x16x32_bf16 v[188:191], v[80:83], v[136:139], v[188:191]\n\t" \
  "ds_write_b32 v32, v0 offset:144\n\t" \
  "s_cmp_eq_u32 vcc_lo, 0\n\t" \
  "s_cbranch_scc1 15f\n\t" \
  "14:\n\t" \
  "v_mfma_f32_16x16x32_bf16 v[196:199], v[80:83], v[140:143], v[196:199]\n\t" \
  "ds_read_b64_tr_b16 v[64:65], v62 offset:26624\n\t" \
  "v_mfma_f32_16x16x32_bf16 v[196:199], v[88:91], v[144:147], v[196:199]\n\t" \
  "ds_read_b64_tr_b16 v[66:67], v62 offset:27136\n\t" \
  "v_mfma_f32_16x16x32_bf16 v[204:207], v[88:91], v[132:135], v[204:207]\n\t" \
  "ds_read_b128 v[96:99], v60 offset:0\n\t" \
  "v_mfma_f32_16x16x32_bf16 v[204:207], v[80:83], v[112:115], v[204:207]\n\t" \
  "ds_read_b128 v[84:87], v60 offset:2048\n\t" \
  "v_mfma_f32_16x16x32_bf16 v[212:215], v[80:83], v[148:151], v[212:215]\n\t" \
  "ds_read_b128 v[92:95], v60 offset:4096\n\t" \
  "v_mfma_f32_16x16x32_bf16 v[212:215], v[88:91], v[128:131], v[212:215]\n\t" \
  "ds_read_b128 v[100:103], v60 offset:6144\n\t" \
  "v_mfma_f32_16x16x32_bf16 v[152:155], v[116:119], v[136:139], v[152:155]\n\t" \
  "ds_read_b128 v[72:75], v60 offset:8192\n\t" \
  "v_mfma_f32_16x16x32_bf16 v[152:155], v[120:123], v[108:111], v[152:155]\n\t" \
  "ds_read_b64_tr_b16 v[68:69], v63 offset:26624\n\t" \
  "v_mfma_f32_16x16x32_bf16 v[160:163], v[120:123], v[144:147], v[160:163]\n\t" \
  "ds_read_b64_tr_b16 v[70:71], v63 offset:27136\n\t" \
  "v_mfma_f32_16x16x32_bf16 v[160:163], v[116:119], v[140:143], v[160:163]\n\t" \
  "ds_read_b64_tr_b16 v[104:105], v62 offset:34816\n\t" \
  "v_mfma_f32_16x16x32_bf16 v[168:171], v[116:119], v[112:115], v[168:171]\n\t" \
  "ds_read_b64_tr_b16 v[106:107], v62 offset:35328\n\t" \
  "v_mfma_f32_16x16x32_bf16 v[168:171], v[120:123], v[132:135], v[168:171]\n\t" \
  "ds_read_b64_tr_b16 v[80:81], v63 offset:34816\n\t" \
  "v_mfma_f32_16x16x32_bf16 v[176:179], v[120:123], v[128:131], v[176:179]\n\t" \
  "ds_read_b64_tr_b16 v[82:83], v63 offset:35328\n\t" \
  "v_mfma_f32_16x16x32_bf16 v[176:179], v[116:119], v[148:151], v[176:179]\n\t" \
  "ds_read_b64_tr_b16 v[88:89], v62 offset:18432\n\t" \
  "v_mfma_f32_16x16x32_bf16 v[156:159], v[124:127], v[136:139], v[156:159]\n\t" \
  "ds_read_b64_tr_b16 v[90:91], v62 offset:18944\n\t" \
  "v_mfma_f32_16x16x32_bf16 v[156:159], v[76:79], v[108:111], v[156:159]\n\t" \
  "ds_read_b64_tr_b16 v[120:121], v62 offset:30720\n\t" \
  "v_mfma_f32_16x16x32_bf16 v[164:167], v[76:79], v[144:147], v[164:167]\n\t" \
  "ds_read_b64_tr_b16 v[122:123], v62 offset:31232\n\t" \
  "v_mfma_f32_16x16x32_bf16 v[164:167], v[124:127], v[140:143], v[164:167]\n\t" \
  "ds_read_b128 v[116:119], v61 offset:0\n\t" \
  "v_mfma_f32_16x16x32_bf16 v[172:175], v[124:127], v[112:115], v[172:175]\n\t" \
  "ds_read_b128 v[136:139], v61 offset:2048\n\t" \
  "v_mfma_f32_16x16x32_bf16 v[172:175], v[76:79], v[132:135], v[172:175]\n\t" \
  "ds_read_b128 v[108:111], v61 offset:4096\n\t" \
  "v_mfma_f32_16x16x32_bf16 v[180:183], v[76:79], v[128:131], v[180:183]\n\t" \
  "ds_read_b64_tr_b16 v[76:77], v63 offset:18432\n\t" \
  "v_mfma_f32_16x16x32_bf16 v[180:183], v[124:127], v[148:151], v[180:183]\n\t" \
  "ds_read_b64_tr_b16 v[78:79], v63 offset:18944\n\t" \
  "s_waitcnt lgkmcnt(7)\n\t" \
  "v_mfma_f32_16x16x32_bf16 v[248:251], v[64:67], v[96:99], v[248:251]\n\t" \
  "ds_read_b128 v[144:147], v61 offset:6144\n\t" \
  "v_mfma_f32_16x16x32_bf16 v[184:187], v[64:67], v[84:87], v[184:187]\n\t" \
  "ds_read_b128 v[140:143], v61 offset:8192\n\t" \
  "v_mfma_f32_16x16x32_bf16 v[192:195], v[64:67], v[92:95], v[192:195]\n\t" \
  "ds_read_b64_tr_b16 v[112:113], v63 offset:30720\n\t" \
  "v_mfma_f32_16x16x32_bf16 v[200:203], v[64:67], v[100:103], v[200:203]\n\t" \
  "ds_read_b64_tr_b16 v[114:115], v63 offset:31232\n\t" \
  "v_mfma_f32_16x16x32_bf16 v[208:211], v[64:67], v[72:75], v[208:211]\n\t" \
  "ds_read_b64_tr_b16 v[132:133], v62 offset:22528\n\t" \
  "v_mfma_f32_16x16x32_bf16 v[252:255], v[68:71], v[96:99], v[252:255]\n\t" \
  "ds_read_b64_tr_b16 v[134:135], v62 offset:23040\n\t" \
  "v_mfma_f32_16x16x32_bf16 v[188:191], v[68:71], v[84:87], v[188:191]\n\t" \
  "ds_read_b64_tr_b16 v[128:129], v63 offset:22528\n\t" \
  "v_mfma_f32_16x16x32_bf16 v[196:199], v[68:71], v[92:95], v[196:199]\n\t" \
  "ds_read_b64_tr_b16 v[130:131], v63 offset:23040\n\t" \
  "v_mfma_f32_16x16x32_bf16 v[204:207], v[68:71], v[100:103], v[204:207]\n\t" \
  "ds_read_b64_tr_b16 v[124:125], v62 offset:38912\n\t" \
  "v_mfma_f32_16x16x32_bf16 v[212:215], v[68:71], v[72:75], v[212:215]\n\t" \
  "ds_read_b64_tr_b16 v[126:127], v62 offset:39424\n\t" \
  "v_mfma_f32_16x16x32_bf16 v[216:219], v[84:87], v[104:107], v[216:219]\n\t" \
  "ds_read_b64_tr_b16 v[148:149], v63 offset:38912\n\t" \
  "v_mfma_f32_16x16x32_bf16 v[224:227], v[92:95], v[104:107], v[224:227]\n\t" \
  "ds_read_b64_tr_b16 v[150:151], v63 offset:39424\n\t" \
  "v_mfma_f32_16x16x32_bf16 v[232:235], v[100:103], v[104:107], v[232:235]\n\t" \
  "v_mfma_f32_16x16x32_bf16 v[240:243], v[72:75], v[104:107], v[240:243]\n\t" \
  "v_mfma_f32_16x16x32_bf16 v[220:223], v[84:87], v[80:83], v[220:223]\n\t" \
  "v_mfma_f32_16x16x32_bf16 v[228:231], v[92:95], v[80:83], v[228:231]\n\t" \
  "v_mfma_f32_16x16x32_bf16 v[236:239], v[100:103], v[80:83], v[236:239]\n\t" \
  "v_mfma_f32_16x16x32_bf16 v[244:247], v[72:75], v[80:83], v[244:247]\n\t" \
  "v_mfma_f32_16x16x32_bf16 v[152:155], v[88:91], v[84:87], v[152:155]\n\t" \
  "v_mfma_f32_16x16x32_bf16 v[160:163], v[88:91], v[92:95], v[160:163]\n\t" \
  "v_mfma_f32_16x16x32_bf16 v[168:171], v[88:91], v[100:103], v[168:171]\n\t" \
  "v_mfma_f32_16x16x32_bf16 v[176:179], v[88:91], v[72:75], v[176:179]\n\t" \
  "s_waitcnt lgkmcnt(0)\n\t" \
  "v_mfma_f32_16x16x32_bf16 v[156:159], v[76:79], v[84:87], v[156:159]\n\t" \
  "v_mfma_f32_16x16x32_bf16 v[164:167], v[76:79], v[92:95], v[164:167]\n\t" \
  "ds_write_b32 v32, v0\n\t" \
  "v_mfma_f32_16x16x32_bf16 v[172:175], v[76:79], v[100:103], v[172:175]\n\t" \
  "v_mfma_f32_16x16x32_bf16 v[180:183], v[76:79], v[72:75], v[180:183]\n\t" \
  "v_mfma_f32_16x16x32_bf16 v[248:251], v[120:123], v[116:119], v[248:251]\n\t" \
  "v_and_b32 v56, -16, v32\n\t" \
  "v_mfma_f32_16x16x32_bf16 v[184:187], v[120:123], v[136:139], v[184:187]\n\t" \
  "v_mfma_f32_16x16x32_bf16 v[192:195], v[120:123], v[108:111], v[192:195]\n\t" \
  "v_mfma_f32_16x16x32_bf16 v[200:203], v[120:123], v[144:147], v[200:203]\n\t" \
  "v_mfma_f32_16x16x32_bf16 v[208:211], v[120:123], v[140:143], v[208:211]\n\t" \
  "v_mfma_f32_16x16x32_bf16 v[252:255], v[112:115], v[116:119], v[252:255]\n\t" \
  "v_mfma_f32_16x16x32_bf16 v[188:191], v[112:115], v[136:139], v[188:191]\n\t" \
  "v_mfma_f32_16x16x32_bf16 v[196:199], v[112:115], v[108:111], v[196:199]\n\t" \
  "v_cvt_pk_bf16_f32 v10, v248, v249\n\t" \
  "v_cvt_pk_bf16_f32 v11, v250, v251\n\t" \
  "v_mfma_f32_16x16x32_bf16 v[204:207], v[112:115], v[144:147], v[204:207]\n\t" \
  "v_cvt_pk_bf16_f32 v14, v184, v185\n\t" \
  "v_cvt_pk_bf16_f32 v15, v186, v187\n\t" \
  "v_mfma_f32_16x16x32_bf16 v[212:215], v[112:115], v[140:143], v[212:215]\n\t" \
  "v_cvt_pk_bf16_f32 v12, v252, v253\n\t" \
  "v_cvt_pk_bf16_f32 v13, v254, v255\n\t" \
  "v_mfma_f32_16x16x32_bf16 v[152:155], v[132:135], v[136:139], v[152:155]\n\t" \
  "ds_write_b128 v30, v[10:13]\n\t" \
  "ds_write_b32 v32, v0 offset:48\n\t" \
  "v_mfma_f32_16x16x32_bf16 v[160:163], v[132:135], v[108:111], v[160:163]\n\t" \
  "v_cvt_pk_bf16_f32 v16, v188, v189\n\t" \
  "v_cvt_pk_bf16_f32 v17, v190, v191\n\t" \
  "v_mfma_f32_16x16x32_bf16 v[168:171], v[132:135], v[144:147], v[168:171]\n\t" \
  "v_cvt_pk_bf16_f32 v18, v192, v193\n\t" \
  "v_cvt_pk_bf16_f32 v19, v194, v195\n\t" \
  "v_mfma_f32_16x16x32_bf16 v[176:179], v[132:135], v[140:143], v[176:179]\n\t" \
  "v_cvt_pk_bf16_f32 v20, v196, v197\n\t" \
  "v_cvt_pk_bf16_f32 v21, v198, v199\n\t" \
  "v_mfma_f32_16x16x32_bf16 v[156:159], v[128:131], v[136:139], v[156:159]\n\t" \
  "v_cvt_pk_bf16_f32 v22, v200, v201\n\t" \
  "v_cvt_pk_bf16_f32 v23, v202, v203\n\t" \
  "v_mfma_f32_16x16x32_bf16 v[164:167], v[128:131], v[108:111], v[164:167]\n\t" \
  "v_cvt_pk_bf16_f32 v24, v204, v205\n\t" \
  "v_cvt_pk_bf16_f32 v25, v206, v207\n\t" \
  "v_mfma_f32_16x16x32_bf16 v[172:175], v[128:131], v[144:147], v[172:175]\n\t" \
  "v_cvt_pk_bf16_f32 v26, v208, v209\n\t" \
  "v_cvt_pk_bf16_f32 v27, v210, v211\n\t" \
  "v_mfma_f32_16x16x32_bf16 v[180:183], v[128:131], v[140:143], v[180:183]\n\t" \
  "v_cvt_pk_bf16_f32 v28, v212, v213\n\t" \
  "v_cvt_pk_bf16_f32 v29, v214, v215\n\t" \
  "v_mfma_f32_16x16x32_bf16 v[216:219], v[136:139], v[124:127], v[216:219]\n\t" \
  "v_cvt_pk_bf16_f32 v84, v152, v153\n\t" \
  "v_cvt_pk_bf16_f32 v85, v154, v155\n\t" \
  "v_mfma_f32_16x16x32_bf16 v[224:227], v[108:111], v[124:127], v[224:227]\n\t" \
  "v_cvt_pk_bf16_f32 v86, v156, v157\n\t" \
  "v_cvt_pk_bf16_f32 v87, v158, v159\n\t" \
  "v_mfma_f32_16x16x32_bf16 v[232:235], v[144:147], v[124:127], v[232:235]\n\t" \
  "v_cvt_pk_bf16_f32 v88, v160, v161\n\t" \
  "v_cvt_pk_bf16_f32 v89, v162, v163\n\t" \
  "v_mfma_f32_16x16x32_bf16 v[240:243], v[140:143], v[124:127], v[240:243]\n\t" \
  "v_cvt_pk_bf16_f32 v90, v164, v165\n\t" \
  "v_cvt_pk_bf16_f32 v91, v166, v167\n\t" \
  "v_mfma_f32_16x16x32_bf16 v[220:223], v[136:139], v[148:151], v[220:223]\n\t" \
  "v_cvt_pk_bf16_f32 v92, v168, v169\n\t" \
  "v_cvt_pk_bf16_f32 v93, v170, v171\n\t" \
  "v_mfma_f32_16x16x32_bf16 v[228:231], v[108:111], v[148:151], v[228:231]\n\t" \
  "v_cvt_pk_bf16_f32 v94, v172, v173\n\t" \
  "v_cvt_pk_bf16_f32 v95, v174, v175\n\t" \
  "v_mfma_f32_16x16x32_bf16 v[236:239], v[144:147], v[148:151], v[236:239]\n\t" \
  "v_cvt_pk_bf16_f32 v96, v176, v177\n\t" \
  "v_cvt_pk_bf16_f32 v97, v178, v179\n\t" \
  "v_mfma_f32_16x16x32_bf16 v[244:247], v[140:143], v[148:151], v[244:247]\n\t" \
  "v_cvt_pk_bf16_f32 v98, v180, v181\n\t" \
  "v_cvt_pk_bf16_f32 v99, v182, v183\n\t" \
  "v_mfma_f32_16x16x32_bf16 v[200:203], v[22:25], v[92:95], v[34:37]\n\t" \
  "v_cvt_pk_bf16_f32 v66, v232, v233\n\t" \
  "v_cvt_pk_bf16_f32 v67, v234, v235\n\t" \
  "v_mfma_f32_16x16x32_bf16 v[208:211], v[26:29], v[96:99], v[34:37]\n\t" \
  "v_cvt_pk_bf16_f32 v76, v236, v237\n\t" \
  "v_cvt_pk_bf16_f32 v77, v238, v239\n\t" \
  "v_mfma_f32_16x16x32_bf16 v[212:215], v[22:25], v[96:99], v[38:41]\n\t" \
  "v_cvt_pk_bf16_f32 v64, v240, v241\n\t" \
  "v_cvt_pk_bf16_f32 v65, v242, v243\n\t" \
  "v_mfma_f32_16x16x32_bf16 v[204:207], v[18:21], v[92:95], v[38:41]\n\t" \
  "v_cvt_pk_bf16_f32 v74, v244, v245\n\t" \
  "v_cvt_pk_bf16_f32 v75, v246, v247\n\t" \
  "v_mfma_f32_16x16x32_bf16 v[192:195], v[18:21], v[88:91], v[34:37]\n\t" \
  "v_cvt_pk_bf16_f32 v68, v224, v225\n\t" \
  "v_cvt_pk_bf16_f32 v69, v226, v227\n\t" \
  "v_mfma_f32_16x16x32_bf16 v[196:199], v[14:17], v[88:91], v[38:41]\n\t" \
  "v_max_f32 v208, v208, v212\n\t" \
  "v_max_f32 v209, v209, v213\n\t" \
  "v_mfma_f32_16x16x32_bf16 v[184:187], v[14:17], v[84:87], v[34:37]\n\t" \
  "v_max_f32 v210, v210, v214\n\t" \
  "v_max_f32 v200, v200, v204\n\t" \
  "v_mfma_f32_16x16x32_bf16 v[188:191], v[10:13], v[84:87], v[42:45]\n\t" \
  "v_max_f32 v201, v201, v205\n\t" \
  "v_max_f32 v202, v202, v206\n\t" \
  "v_max_f32 v192, v192, v196\n\t" \
  "v_max_f32 v193, v193, v197\n\t" \
  "v_max_f32 v194, v194, v198\n\t" \
  "v_max_f32 v211, v211, v215\n\t" \
  "ds_read_b32 v52, v33 offset:48\n\t" \
  "ds_read_b64 v[72:73], v31\n\t" \
  "ds_read_b64 v[82:83], v31 offset:8\n\t" \
  "v_max3_f32 v106, v208, v209, v210\n\t" \
  "v_max_f32 v184, v184, v188\n\t" \
  "v_max_f32 v185, v185, v189\n\t" \
  "v_max_f32 v186, v186, v190\n\t" \
  "v_max_f32 v203, v203, v207\n\t" \
  "v_max3_f32 v104, v200, v201, v202\n\t" \
  "v_max_f32 v195, v195, v199\n\t" \
  "v_max3_f32 v102, v192, v193, v194\n\t" \
  "v_max_f32 v187, v187, v191\n\t" \
  "v_max3_f32 v100, v184, v185, v186\n\t" \
  "v_max_f32 v106, v106, v211\n\t" \
  "v_max_f32 v104, v104, v203\n\t" \
  "v_max_f32 v102, v102, v195\n\t" \
  "v_max_f32 v100, v100, v187\n\t" \
  "v_permlane16_swap_b32 v104, v106\n\t" \
  "v_cvt_pk_bf16_f32 v78, v228, v229\n\t" \
  "v_permlane16_swap_b32 v100, v102\n\t" \
  "v_max_f32 v104, v104, v106\n\t" \
  "v_max_f32 v100, v100, v102\n\t" \
  "v_cvt_pk_bf16_f32 v79, v230, v231\n\t" \
  "v_cvt_pk_bf16_f32 v70, v216, v217\n\t" \
  "v_permlane32_swap_b32 v100, v104\n\t" \
  "v_cvt_pk_bf16_f32 v71, v218, v219\n\t" \
  "v_max_f32 v100, v100, v104\n\t" \
  "v_mul_f32 v100, 0xbe8293ee, v100\n\t" \
  "v_mov_b32 v104, v100\n\t" \
  "v_cvt_pk_bf16_f32 v80, v220, v221\n\t" \
  "v_cvt_pk_bf16_f32 v81, v222, v223\n\t" \
  "v_permlane32_swap_b32 v100, v104\n\t" \
  "s_waitcnt lgkmcnt(0)\n\t" \
  "v_readfirstlane_b32 vcc_lo, v52\n\t" \
  "s_nop 1\n\t" \
  "s_cmp_eq_u32 vcc_lo, 0\n\t" \
  "s_cbranch_scc1 5f\n\t" \
  "4:\n\t" \
  "v_mov_b32 v106, v104\n\t" \
  "v_mov_b32 v102, v100\n\t" \
  "s_nop 0\n\t" \
  "v_permlane16_swap_b32 v104, v106\n\t" \
  "v_permlane16_swap_b32 v100, v102\n\t" \
  "v_fma_f32 v208, v208, %16, v106\n\t" \
  "v_fma_f32 v209, v209, %16, v106\n\t" \
  "v_fma_f32 v210, v210, %16, v106\n\t" \
  "v_fma_f32 v211, v211, %16, v106\n\t" \
  "v_exp_f32 v208, v208\n\t" \
  "v_exp_f32 v209, v209\n\t" \
  "v_exp_f32 v210, v210\n\t" \
  "v_exp_f32 v211, v211\n\t" \
  "v_cvt_pk_bf16_f32 v212, v208, v209\n\t" \
  "v_cvt_pk_bf16_f32 v213, v210, v211\n\t" \
  "v_bfi_b32 v214, v46, 0, v212\n\t" \
  "v_bfi_b32 v215, v47, 0, v213\n\t" \
  "v_and_b32 v212, v46, v212\n\t" \
  "v_and_b32 v213, v47, v213\n\t" \
  "v_fma_f32 v200, v200, %16, v104\n\t" \
  "v_fma_f32 v201, v201, %16, v104\n\t" \
  "v_mfma_f32_16x16x32_bf16 v[208:211], v[48:51], v[212:215], 0\n\t" \
  "v_fma_f32 v202, v202, %16, v104\n\t" \
  "v_fma_f32 v203, v203, %16, v104\n\t" \
  "v_mfma_f32_16x16x32_bf16 v[176:179], v[64:67], v[212:215], 0\n\t" \
  "v_exp_f32 v200, v200\n\t" \
  "v_mfma_f32_16x16x32_bf16 v[180:183], v[74:77], v[212:215], 0\n\t" \
  "v_exp_f32 v201, v201\n\t" \
  "v_exp_f32 v202, v202\n\t" \
  "v_exp_f32 v203, v203\n\t" \
  "v_cvt_pk_bf16_f32 v204, v200, v201\n\t" \
  "v_cvt_pk_bf16_f32 v205, v202, v203\n\t" \
  "v_bfi_b32 v206, v46, 0, v204\n\t" \
  "v_bfi_b32 v207, v47, 0, v205\n\t" \
  "v_rcp_f32 v106, v208\n\t" \
  "v_and_b32 v204, v46, v204\n\t" \
  "v_and_b32 v205, v47, v205\n\t" \
  "v_pk_mul_f32 v[176:177], v[176:177], v[106:107] op_sel_hi:[1,0]\n\t" \
  "v_pk_mul_f32 v[178:179], v[178:179], v[106:107] op_sel_hi:[1,0]\n\t" \
  "v_mfma_f32_16x16x32_bf16 v[200:203], v[48:51], v[204:207], 0\n\t" \
  "v_pk_mul_f32 v[180:181], v[180:181], v[106:107] op_sel_hi:[1,0]\n\t" \
  "v_pk_mul_f32 v[182:183], v[182:183], v[106:107] op_sel_hi:[1,0]\n\t" \
  "v_mfma_f32_16x16x32_bf16 v[168:171], v[66:69], v[204:207], 0\n\t" \
  "v_cvt_pk_bf16_f32 v240, v176, v177\n\t" \
  "v_cvt_pk_bf16_f32 v241, v178, v179\n\t" \
  "v_mfma_f32_16x16x32_bf16 v[172:175], v[76:79], v[204:207], 0\n\t" \
  "v_cvt_pk_bf16_f32 v242, v180, v181\n\t" \
  "v_cvt_pk_bf16_f32 v243, v182, v183\n\t" \
  "global_store_dwordx4 v1, v[240:243], %14 offset:0 sc1\n\t" \
  "v_fma_f32 v192, v192, %16, v102\n\t" \
  "v_fma_f32 v193, v193, %16, v102\n\t" \
  "v_fma_f32 v194, v194, %16, v102\n\t" \
  "v_fma_f32 v195, v195, %16, v102\n\t" \
  "v_exp_f32 v192, v192\n\t" \
  "v_exp_f32 v193, v193\n\t" \
  "v_exp_f32 v194, v194\n\t" \
  "v_exp_f32 v195, v195\n\t" \
  "v_cvt_pk_bf16_f32 v196, v192, v193\n\t" \
  "v_cvt_pk_bf16_f32 v197, v194, v195\n\t" \
  "v_bfi_b32 v198, v46, 0, v196\n\t" \
  "v_bfi_b32 v199, v47, 0, v197\n\t" \
  "v_rcp_f32 v104, v200\n\t" \
  "v_and_b32 v196, v46, v196\n\t" \
  "v_and_b32 v197, v47, v197\n\t" \
  "v_pk_mul_f32 v[168:169], v[168:169], v[104:105] op_sel_hi:[1,0]\n\t" \
  "v_pk_mul_f32 v[170:171], v[170:171], v[104:105] op_sel_hi:[1,0]\n\t" \
  "v_mfma_f32_16x16x32_bf16 v[192:195], v[48:51], v[196:199], 0\n\t" \
  "v_pk_mul_f32 v[172:173], v[172:173], v[104:105] op_sel_hi:[1,0]\n\t" \
  "v_pk_mul_f32 v[174:175], v[174:175], v[104:105] op_sel_hi:[1,0]\n\t" \
  "v_mfma_f32_16x16x32_bf16 v[160:163], v[68:71], v[196:199], 0\n\t" \
  "v_cvt_pk_bf16_f32 v232, v168, v169\n\t" \
  "v_cvt_pk_bf16_f32 v233, v170, v171\n\t" \
  "v_mfma_f32_16x16x32_bf16 v[164:167], v[78:81], v[196:199], 0\n\t" \
  "v_cvt_pk_bf16_f32 v234, v172, v173\n\t" \
  "v_cvt_pk_bf16_f32 v235, v174, v175\n\t" \
  "global_store_dwordx4 v1, v[232:235], %14 offset:-4096 sc1\n\t" \
  "v_fma_f32 v184, v184, %16, v100\n\t" \
  "v_fma_f32 v185, v185, %16, v100\n\t" \
  "v_fma_f32 v186, v186, %16, v100\n\t" \
  "v_fma_f32 v187, v187, %16, v100\n\t" \
  "v_exp_f32 v184, v184\n\t" \
  "v_exp_f32 v185, v185\n\t" \
  "v_exp_f32 v186, v186\n\t" \
  "v_exp_f32 v187, v187\n\t" \
  "v_cvt_pk_bf16_f32 v188, v184, v185\n\t" \
  "v_cvt_pk_bf16_f32 v189, v186, v187\n\t" \
  "v_bfi_b32 v190, v46, 0, v188\n\t" \
  "v_bfi_b32 v191, v47, 0, v189\n\t" \
  "v_rcp_f32 v102, v192\n\t" \
  "v_and_b32 v188, v46, v188\n\t" \
  "v_and_b32 v189, v47, v189\n\t" \
  "v_pk_mul_f32 v[160:161], v[160:161], v[102:103] op_sel_hi:[1,0]\n\t" \
  "v_pk_mul_f32 v[162:163], v[162:163], v[102:103] op_sel_hi:[1,0]\n\t" \
  "v_mfma_f32_16x16x32_bf16 v[184:187], v[48:51], v[188:191], 0\n\t" \
  "v_pk_mul_f32 v[164:165], v[164:165], v[102:103] op_sel_hi:[1,0]\n\t" \
  "v_pk_mul_f32 v[166:167], v[166:167], v[102:103] op_sel_hi:[1,0]\n\t" \
  "v_mfma_f32_16x16x32_bf16 v[152:155], v[70:73], v[188:191], 0\n\t" \
  "v_cvt_pk_bf16_f32 v224, v160, v161\n\t" \
  "v_cvt_pk_bf16_f32 v225, v162, v163\n\t" \
  "v_mfma_f32_16x16x32_bf16 v[156:159], v[80:83], v[188:191], 0\n\t" \
  "v_cvt_pk_bf16_f32 v226, v164, v165\n\t" \
  "v_cvt_pk_bf16_f32 v227, v166, v167\n\t" \
  "global_store_dwordx4 v4, v[224:227], %14 offset:0 sc1\n\t" \
  "v_rcp_f32 v100, v184\n\t" \
  "ds_read_b128 v[60:63], v56 offset:64\n\t" \
  "ds_read_b64_tr_b16 v[108:109], v5 offset:0\n\t" \
  "ds_read_b64_tr_b16 v[110:111], v5 offset:16\n\t" \
  "ds_read_b128 v[112:115], v6 offset:16384\n\t" \
  "ds_read_b128 v[116:119], v6 offset:15360\n\t" \
  "506:\n\t" \
  "s_cbranch_execz 510f\n\t" \
  "ds_read_b64_tr_b16 v[120:121], v5 offset:512\n\t" \
  "ds_read_b64_tr_b16 v[122:123], v5 offset:528\n\t" \
  "ds_read_b128 v[124:127], v6 offset:14336\n\t" \
  "ds_read_b64_tr_b16 v[128:129], v5 offset:1024\n\t" \
  "ds_read_b64_tr_b16 v[130:131], v5 offset:1040\n\t" \
  "ds_read_b128 v[132:135], v6 offset:13312\n\t" \
  "ds_read_b64_tr_b16 v[136:137], v5 offset:1536\n\t" \
  "ds_read_b64_tr_b16 v[138:139], v5 offset:1552\n\t" \
  "ds_read_b128 v[140:143], v6 offset:12288\n\t" \
  "ds_read_b64_tr_b16 v[144:145], v5 offset:2048\n\t" \
  "v_pk_mul_f32 v[152:153], v[152:153], v[100:101] op_sel_hi:[1,0]\n\t" \
  "v_pk_mul_f32 v[154:155], v[154:155], v[100:101] op_sel_hi:[1,0]\n\t" \
  "v_cvt_pk_bf16_f32 v216, v152, v153\n\t" \
  "v_pk_mul_f32 v[156:157], v[156:157], v[100:101] op_sel_hi:[1,0]\n\t" \
  "v_pk_mul_f32 v[158:159], v[158:159], v[100:101] op_sel_hi:[1,0]\n\t" \
  "v_cvt_pk_bf16_f32 v217, v154, v155\n\t" \
  "v_cvt_pk_bf16_f32 v218, v156, v157\n\t" \
  "v_cvt_pk_bf16_f32 v219, v158, v159\n\t" \
  "global_store_dwordx4 v4, v[216:219], %14 offset:-4096 sc1\n\t" \
  "ds_write_b32 v32, v0 offset:112\n\t" \
  "s_branch 9f\n\t" \
  "5:\n\t" \
  "ds_read_b32 v52, v33 offset:48\n\t" \
  "ds_read_b64 v[72:73], v31\n\t" \
  "ds_read_b64 v[82:83], v31 offset:8\n\t" \
  "s_waitcnt lgkmcnt(0)\n\t" \
  "v_readfirstlane_b32 vcc_lo, v52\n\t" \
  "s_nop 1\n\t" \
  "s_cmp_eq_u32 vcc_lo, 0\n\t" \
  "s_cbranch_scc1 5b\n\t" \
  "s_branch 4b\n\t" \
  "15:\n\t" \
  "ds_read_b128 v[10:13], v14 offset:128\n\t" \
  "s_waitcnt lgkmcnt(0)\n\t" \
  "v_and_b32 v10, v10, v11\n\t" \
  "v_and_b32 v12, v12, v13\n\t" \
  "v_and_b32 v10, v10, v12\n\t" \
  "s_nop 1\n\t" \
  "v_readfirstlane_b32 vcc_lo, v10\n\t" \
  "s_nop 1\n\t" \
  "s_cmp_eq_u32 vcc_lo, 0\n\t" \
  "s_cbranch_scc1 15b\n\t" \
  "s_branch 14b\n\t" \
  "9:\n\t" \
  "507:\n\t" \
  "s_cbranch_execz 511f\n\t" \
  "s_waitcnt lgkmcnt(14)\n\t" \
  "v_and_b32 v60, v60, v61\n\t" \
  "v_and_b32 v62, v62, v63\n\t" \
  "v_mov_b32 v12, %13\n\t" \
  "v_and_b32 v60, v60, v62\n\t" \
  "s_nop 0\n\t" \
  "v_readfirstlane_b32 vcc_lo, v60\n\t" \
  "s_nop 1\n\t" \
  "s_cmp_eq_u32 vcc_lo, 0\n\t" \
  "s_cbranch_scc1 7f\n\t" \
  "6:\n\t" \
  "s_waitcnt lgkmcnt(11)\n\t" \
  "v_mfma_f32_16x16x32_bf16 v[192:195], v[108:111], v[112:115], 0\n\t" \
  "ds_read_b64_tr_b16 v[146:147], v5 offset:2064\n\t" \
  "ds_read_b128 v[148:151], v6 offset:11264\n\t" \
  "s_waitcnt lgkmcnt(12)\n\t" \
  "v_mfma_f32_16x16x32_bf16 v[196:199], v[108:111], v[116:119], 0\n\t" \
  "ds_read_b64_tr_b16 v[64:65], v5 offset:2560\n\t" \
  "ds_read_b64_tr_b16 v[66:67], v5 offset:2576\n\t" \
  "s_waitcnt lgkmcnt(12)\n\t" \
  "v_mfma_f32_16x16x32_bf16 v[196:199], v[120:123], v[112:115], v[196:199]\n\t" \
  "ds_read_b128 v[68:71], v6 offset:10240\n\t" \
  "ds_read_b64_tr_b16 v[72:73], v5 offset:3072\n\t" \
  "s_waitcnt lgkmcnt(13)\n\t" \
  "v_mfma_f32_16x16x32_bf16 v[200:203], v[108:111], v[124:127], 0\n\t" \
  "ds_read_b64_tr_b16 v[74:75], v5 offset:3088\n\t" \
  "ds_read_b128 v[76:79], v6 offset:9216\n\t" \
  "v_mfma_f32_16x16x32_bf16 v[200:203], v[120:123], v[116:119], v[200:203]\n\t" \
  "ds_read_b64_tr_b16 v[80:81], v5 offset:3584\n\t" \
  "ds_read_b64_tr_b16 v[82:83], v5 offset:3600\n\t" \
  "s_waitcnt lgkmcnt(15)\n\t" \
  "v_mfma_f32_16x16x32_bf16 v[200:203], v[128:131], v[112:115], v[200:203]\n\t" \
  "ds_read_b128 v[84:87], v6 offset:0\n\t" \
  "ds_read_b128 v[88:91], v6 offset:1024\n\t" \
  "s_waitcnt lgkmcnt(15)\n\t" \
  "v_mfma_f32_16x16x32_bf16 v[204:207], v[108:111], v[132:135], 0\n\t" \
  "ds_read_b128 v[92:95], v6 offset:2048\n\t" \
  "ds_read_b128 v[96:99], v6 offset:3072\n\t" \
  "v_mfma_f32_16x16x32_bf16 v[204:207], v[120:123], v[124:127], v[204:207]\n\t" \
  "ds_read_b128 v[100:103], v6 offset:4096\n\t" \
  "ds_read_b128 v[104:107], v6 offset:5120\n\t" \
  "v_mfma_f32_16x16x32_bf16 v[204:207], v[128:131], v[116:119], v[204:207]\n\t" \
  "ds_read_b128 v[152:155], v6 offset:6144\n\t" \
  "ds_read_b128 v[156:159], v6 offset:7168\n\t" \
  "s_waitcnt lgkmcnt(14)\n\t" \
  "v_mfma_f32_16x16x32_bf16 v[204:207], v[136:139], v[112:115], v[204:207]\n\t" \
  "v_lshl_add_u32 v12, v12, 12, v4\n\t" \
  "s_lshl_b32 s88, %13, 5\n\t" \
  "v_mfma_f32_16x16x32_bf16 v[208:211], v[108:111], v[140:143], 0\n\t" \
  "v_mfma_f32_16x16x32_bf16 v[208:211], v[120:123], v[132:135], v[208:211]\n\t" \
  "v_mfma_f32_16x16x32_bf16 v[208:211], v[128:131], v[124:127], v[208:211]\n\t" \
  "s_and_b32 s88, s88, 64\n\t" \
  "v_xor_b32 v13, s88, v54\n\t" \
  "v_mfma_f32_16x16x32_bf16 v[208:211], v[136:139], v[116:119], v[208:211]\n\t" \
  "v_mfma_f32_16x16x32_bf16 v[208:211], v[144:147], v[112:115], v[208:211]\n\t" \
  "v_mfma_f32_16x16x32_bf16 v[212:215], v[108:111], v[148:151], 0\n\t" \
  "ds_read_b32 v55, v56 offset:104\n\t" \
  "v_mfma_f32_16x16x32_bf16 v[212:215], v[120:123], v[140:143], v[212:215]\n\t" \
  "v_mfma_f32_16x16x32_bf16 v[212:215], v[128:131], v[132:135], v[212:215]\n\t" \
  "v_bfe_u32 v11, v12, 4, 4\n\t" \
  "s_mov_b32 s74, 0\n\t" \
  "v_mfma_f32_16x16x32_bf16 v[212:215], v[136:139], v[124:127], v[212:215]\n\t" \
  "v_lshlrev_b32 v11, 7, v11\n\t" \
  "v_mfma_f32_16x16x32_bf16 v[212:215], v[144:147], v[116:119], v[212:215]\n\t" \
  "v_mov_b32 v10, 1\n\t" \
  "v_mfma_f32_16x16x32_bf16 v[212:215], v[64:67], v[112:115], v[212:215]\n\t" \
  "s_getpc_b64 s[70:71]\n\t" \
  "24:\n\t" \
  "s_add_u32 s70, s70, 23f-24b\n\t" \
  "s_addc_u32 s71, s71, 0\n\t" \
  "s_waitcnt lgkmcnt(1)\n\t" \
  "v_mfma_f32_16x16x32_bf16 v[216:219], v[108:111], v[68:71], 0\n\t" \
  "v_mfma_f32_16x16x32_bf16 v[216:219], v[120:123], v[148:151], v[216:219]\n\t" \
  "v_and_b32 v1, 0x180, v4\n\t" \
  "v_mfma_f32_16x16x32_bf16 v[216:219], v[128:131], v[140:143], v[216:219]\n\t" \
  "v_and_b32 v2, 0x70, v4\n\t" \
  "v_mfma_f32_16x16x32_bf16 v[216:219], v[136:139], v[132:135], v[216:219]\n\t" \
  "v_lshlrev_b32 v2, 3, v2\n\t" \
  "v_mfma_f32_16x16x32_bf16 v[216:219], v[144:147], v[124:127], v[216:219]\n\t" \
  "v_lshl_add_u32 v1, v1, 5, v2\n\t" \
  "v_mfma_f32_16x16x32_bf16 v[216:219], v[64:67], v[116:119], v[216:219]\n\t" \
  "v_and_b32 v2, 0x2000, v1\n\t" \
  "v_mfma_f32_16x16x32_bf16 v[216:219], v[72:75], v[112:115], v[216:219]\n\t" \
  "v_add_u32 v1, v1, v2\n\t" \
  "v_mfma_f32_16x16x32_bf16 v[220:223], v[108:111], v[76:79], 0\n\t" \
  "v_mfma_f32_16x16x32_bf16 v[220:223], v[120:123], v[68:71], v[220:223]\n\t" \
  "v_mfma_f32_16x16x32_bf16 v[220:223], v[128:131], v[148:151], v[220:223]\n\t" \
  "v_mfma_f32_16x16x32_bf16 v[220:223], v[136:139], v[140:143], v[220:223]\n\t" \
  "v_mfma_f32_16x16x32_bf16 v[220:223], v[144:147], v[132:135], v[220:223]\n\t" \
  "ds_read_b64_tr_b16 v[160:161], v5 offset:128\n\t" \
  "v_mfma_f32_16x16x32_bf16 v[220:223], v[64:67], v[124:127], v[220:223]\n\t" \
  "ds_read_b64_tr_b16 v[162:163], v5 offset:144\n\t" \
  "v_mfma_f32_16x16x32_bf16 v[220:223], v[72:75], v[116:119], v[220:223]\n\t" \
  "ds_read_b128 v[164:167], v7 offset:16384\n\t" \
  "v_mfma_f32_16x16x32_bf16 v[220:223], v[80:83], v[112:115], v[220:223]\n\t" \
  "ds_read_b128 v[168:171], v7 offset:15360\n\t" \
  "v_mfma_f32_16x16x32_bf16 v[252:255], v[108:111], v[84:87], 0\n\t" \
  "ds_read_b64_tr_b16 v[172:173], v5 offset:640\n\t" \
  "v_mfma_f32_16x16x32_bf16 v[252:255], v[120:123], v[88:91], v[252:255]\n\t" \
  "ds_read_b64_tr_b16 v[174:175], v5 offset:656\n\t" \
  "v_mfma_f32_16x16x32_bf16 v[252:255], v[128:131], v[92:95], v[252:255]\n\t" \
  "ds_read_b128 v[176:179], v7 offset:14336\n\t" \
  "v_mfma_f32_16x16x32_bf16 v[252:255], v[136:139], v[96:99], v[252:255]\n\t" \
  "ds_read_b64_tr_b16 v[180:181], v5 offset:1152\n\t" \
  "v_mfma_f32_16x16x32_bf16 v[252:255], v[144:147], v[100:103], v[252:255]\n\t" \
  "ds_read_b64_tr_b16 v[182:183], v5 offset:1168\n\t" \
  "v_mfma_f32_16x16x32_bf16 v[252:255], v[64:67], v[104:107], v[252:255]\n\t" \
  "ds_read_b128 v[184:187], v7 offset:13312\n\t" \
  "v_mfma_f32_16x16x32_bf16 v[252:255], v[72:75], v[152:155], v[252:255]\n\t" \
  "ds_read_b64_tr_b16 v[188:189], v5 offset:1664\n\t" \
  "v_mfma_f32_16x16x32_bf16 v[252:255], v[80:83], v[156:159], v[252:255]\n\t" \
  "ds_read_b64_tr_b16 v[190:191], v5 offset:1680\n\t" \
  "v_mfma_f32_16x16x32_bf16 v[248:251], v[108:111], v[88:91], 0\n\t" \
  "ds_read_b128 v[76:79], v7 offset:12288\n\t" \
  "v_mfma_f32_16x16x32_bf16 v[248:251], v[120:123], v[92:95], v[248:251]\n\t" \
  "v_mfma_f32_16x16x32_bf16 v[248:251], v[128:131], v[96:99], v[248:251]\n\t" \
  "ds_read_b64_tr_b16 v[68:69], v5 offset:2176\n\t" \
  "v_mfma_f32_16x16x32_bf16 v[248:251], v[136:139], v[100:103], v[248:251]\n\t" \
  "ds_read_b64_tr_b16 v[70:71], v5 offset:2192\n\t" \
  "v_mfma_f32_16x16x32_bf16 v[248:251], v[144:147], v[104:107], v[248:251]\n\t" \
  "ds_read_b128 v[148:151], v7 offset:11264\n\t" \
  "v_mfma_f32_16x16x32_bf16 v[248:251], v[64:67], v[152:155], v[248:251]\n\t" \
  "v_mfma_f32_16x16x32_bf16 v[248:251], v[72:75], v[156:159], v[248:251]\n\t" \
  "v_mfma_f32_16x16x32_bf16 v[244:247], v[108:111], v[92:95], 0\n\t" \
  "v_mfma_f32_16x16x32_bf16 v[244:247], v[120:123], v[96:99], v[244:247]\n\t" \
  "ds_read_b64_tr_b16 v[140:141], v5 offset:2688\n\t" \
  "v_mfma_f32_16x16x32_bf16 v[244:247], v[128:131], v[100:103], v[244:247]\n\t" \
  "ds_read_b64_tr_b16 v[142:143], v5 offset:2704\n\t" \
  "v_mfma_f32_16x16x32_bf16 v[244:247], v[136:139], v[104:107], v[244:247]\n\t" \
  "ds_read_b128 v[132:135], v7 offset:10240\n\t" \
  "v_mfma_f32_16x16x32_bf16 v[244:247], v[144:147], v[152:155], v[244:247]\n\t" \
  "v_mfma_f32_16x16x32_bf16 v[244:247], v[64:67], v[156:159], v[244:247]\n\t" \
  "v_mfma_f32_16x16x32_bf16 v[240:243], v[108:111], v[96:99], 0\n\t" \
  "v_mfma_f32_16x16x32_bf16 v[240:243], v[120:123], v[100:103], v[240:243]\n\t" \
  "v_mfma_f32_16x16x32_bf16 v[240:243], v[128:131], v[104:107], v[240:243]\n\t" \
  "ds_read_b64_tr_b16 v[124:125], v5 offset:3200\n\t" \
  "v_mfma_f32_16x16x32_bf16 v[240:243], v[136:139], v[152:155], v[240:243]\n\t" \
  "ds_read_b64_tr_b16 v[126:127], v5 offset:3216\n\t" \
  "v_mfma_f32_16x16x32_bf16 v[240:243], v[144:147], v[156:159], v[240:243]\n\t" \
  "ds_read_b128 v[116:119], v7 offset:9216\n\t" \
  "v_mfma_f32_16x16x32_bf16 v[236:239], v[108:111], v[100:103], 0\n\t" \
  "v_mfma_f32_16x16x32_bf16 v[236:239], v[120:123], v[104:107], v[236:239]\n\t" \
  "s_getreg_b32 vcc_lo, hwreg(HW_REG_IB_STS, 0, 4)\n\t" \
  "v_mfma_f32_16x16x32_bf16 v[236:239], v[128:131], v[152:155], v[236:239]\n\t" \
  "s_or_b32 vcc_lo, vcc_lo, s74\n\t" \
  "s_cbranch_scc1 30f\n\t" \
  "s_swappc_b64 s[68:69], s[70:71]\n\t" \
  "30:\n\t" \
  "v_mfma_f32_16x16x32_bf16 v[236:239], v[136:139], v[156:159], v[236:239]\n\t" \
  "v_mfma_f32_16x16x32_bf16 v[232:235], v[108:111], v[104:107], 0\n\t" \
  "v_mfma_f32_16x16x32_bf16 v[232:235], v[120:123], v[152:155], v[232:235]\n\t" \
  "ds_read_b64_tr_b16 v[64:65], v5 offset:3712\n\t" \
  "v_mfma_f32_16x16x32_bf16 v[232:235], v[128:131], v[156:159], v[232:235]\n\t" \
  "ds_read_b64_tr_b16 v[66:67], v5 offset:3728\n\t" \
  "v_mfma_f32_16x16x32_bf16 v[228:231], v[108:111], v[152:155], 0\n\t" \
  "ds_read_b128 v[72:75], v7 offset:0\n\t" \
  "v_mfma_f32_16x16x32_bf16 v[228:231], v[120:123], v[156:159], v[228:231]\n\t" \
  "ds_read_b128 v[80:83], v7 offset:1024\n\t" \
  "v_mfma_f32_16x16x32_bf16 v[224:227], v[108:111], v[156:159], 0\n\t" \
  "ds_read_b128 v[84:87], v7 offset:2048\n\t" \
  "s_waitcnt lgkmcnt(5)\n\t" \
  "v_mfma_f32_16x16x32_bf16 v[192:195], v[160:163], v[164:167], v[192:195]\n\t" \
  "ds_read_b128 v[88:91], v7 offset:3072\n\t" \
  "v_mfma_f32_16x16x32_bf16 v[196:199], v[160:163], v[168:171], v[196:199]\n\t" \
  "ds_read_b128 v[92:95], v7 offset:4096\n\t" \
  "v_mfma_f32_16x16x32_bf16 v[196:199], v[172:175], v[164:167], v[196:199]\n\t" \
  "ds_read_b128 v[108:111], v7 offset:5120\n\t" \
  "v_mfma_f32_16x16x32_bf16 v[200:203], v[160:163], v[176:179], v[200:203]\n\t" \
  "ds_read_b128 v[112:115], v7 offset:6144\n\t" \
  "v_mfma_f32_16x16x32_bf16 v[200:203], v[172:175], v[168:171], v[200:203]\n\t" \
  "ds_read_b128 v[120:123], v7 offset:7168\n\t" \
  "v_mfma_f32_16x16x32_bf16 v[200:203], v[180:183], v[164:167], v[200:203]\n\t" \
  "v_mfma_f32_16x16x32_bf16 v[204:207], v[160:163], v[184:187], v[204:207]\n\t" \
  "v_mfma_f32_16x16x32_bf16 v[204:207], v[172:175], v[176:179], v[204:207]\n\t" \
  "v_mfma_f32_16x16x32_bf16 v[204:207], v[180:183], v[168:171], v[204:207]\n\t" \
  "v_mfma_f32_16x16x32_bf16 v[204:207], v[188:191], v[164:167], v[204:207]\n\t" \
  "v_mfma_f32_16x16x32_bf16 v[208:211], v[160:163], v[76:79], v[208:211]\n\t" \
  "v_mfma_f32_16x16x32_bf16 v[208:211], v[172:175], v[184:187], v[208:211]\n\t" \
  "s_getreg_b32 vcc_lo, hwreg(HW_REG_IB_STS, 0, 4)\n\t" \
  "v_mfma_f32_16x16x32_bf16 v[208:211], v[180:183], v[176:179], v[208:211]\n\t" \
  "s_or_b32 vcc_lo, vcc_lo, s74\n\t" \
  "s_cbranch_scc1 31f\n\t" \
  "s_swappc_b64 s[68:69], s[70:71]\n\t" \
  "31:\n\t" \
  "v_mfma_f32_16x16x32_bf16 v[208:211], v[188:191], v[168:171], v[208:211]\n\t" \
  "v_mfma_f32_16x16x32_bf16 v[208:211], v[68:71], v[164:167], v[208:211]\n\t" \
  "v_mfma_f32_16x16x32_bf16 v[212:215], v[160:163], v[148:151], v[212:215]\n\t" \
  "v_mfma_f32_16x16x32_bf16 v[212:215], v[172:175], v[76:79], v[212:215]\n\t" \
  "v_mfma_f32_16x16x32_bf16 v[212:215], v[180:183], v[184:187], v[212:215]\n\t" \
  "v_mfma_f32_16x16x32_bf16 v[212:215], v[188:191], v[176:179], v[212:215]\n\t" \
  "v_mfma_f32_16x16x32_bf16 v[212:215], v[68:71], v[168:171], v[212:215]\n\t" \
  "v_mfma_f32_16x16x32_bf16 v[212:215], v[140:143], v[164:167], v[212:215]\n\t" \
  "v_mfma_f32_16x16x32_bf16 v[216:219], v[160:163], v[132:135], v[216:219]\n\t" \
  "v_mfma_f32_16x16x32_bf16 v[216:219], v[172:175], v[148:151], v[216:219]\n\t" \
  "v_mfma_f32_16x16x32_bf16 v[216:219], v[180:183], v[76:79], v[216:219]\n\t" \
  "v_mfma_f32_16x16x32_bf16 v[216:219], v[188:191], v[184:187], v[216:219]\n\t" \
  "v_mfma_f32_16x16x32_bf16 v[216:219], v[68:71], v[176:179], v[216:219]\n\t" \
  "v_mfma_f32_16x16x32_bf16 v[216:219], v[140:143], v[168:171], v[216:219]\n\t" \
  "v_mfma_f32_16x16x32_bf16 v[216:219], v[124:127], v[164:167], v[216:219]\n\t" \
  "v_mfma_f32_16x16x32_bf16 v[220:223], v[160:163], v[116:119], v[220:223]\n\t" \
  "v_mfma_f32_16x16x32_bf16 v[220:223], v[172:175], v[132:135], v[220:223]\n\t" \
  "v_mfma_f32_16x16x32_bf16 v[220:223], v[180:183], v[148:151], v[220:223]\n\t" \
  "v_mfma_f32_16x16x32_bf16 v[220:223], v[188:191], v[76:79], v[220:223]\n\t" \
  "v_mfma_f32_16x16x32_bf16 v[220:223], v[68:71], v[184:187], v[220:223]\n\t" \
  "s_cmp_lg_u32 s74, 0\n\t" \
  "s_cbranch_scc1 32f\n\t" \
  "s_swappc_b64 s[68:69], s[70:71]\n\t" \
  "32:\n\t" \
  "v_mfma_f32_16x16x32_bf16 v[220:223], v[140:143], v[176:179], v[220:223]\n\t" \
  "ds_read_b64_tr_b16 v[76:77], v5 offset:256\n\t" \
  "v_mfma_f32_16x16x32_bf16 v[220:223], v[124:127], v[168:171], v[220:223]\n\t" \
  "ds_read_b64_tr_b16 v[78:79], v5 offset:272\n\t" \
  "s_waitcnt lgkmcnt(2)\n\t" \
  "v_mfma_f32_16x16x32_bf16 v[220:223], v[64:67], v[164:167], v[220:223]\n\t" \
  "s_getpc_b64 s[70:71]\n\t" \
  "24:\n\t" \
  "s_add_u32 s70, s70, 25f-24b\n\t" \
  "s_addc_u32 s71, s71, 0\n\t" \
  "v_mfma_f32_16x16x32_bf16 v[252:255], v[160:163], v[72:75], v[252:255]\n\t" \
  "ds_read_b128 v[128:131], v8 offset:16384\n\t" \
  "v_mfma_f32_16x16x32_bf16 v[252:255], v[172:175], v[80:83], v[252:255]\n\t" \
  "ds_read_b128 v[136:139], v8 offset:15360\n\t" \
  "v_mfma_f32_16x16x32_bf16 v[252:255], v[180:183], v[84:87], v[252:255]\n\t" \
  "v_and_b32 v53, 0x2000, v12\n\t" \
  "ds_read_b64_tr_b16 v[144:145], v5 offset:768\n\t" \
  "v_mfma_f32_16x16x32_bf16 v[252:255], v[188:191], v[88:91], v[252:255]\n\t" \
  "ds_read_b64_tr_b16 v[146:147], v5 offset:784\n\t" \
  "v_mfma_f32_16x16x32_bf16 v[252:255], v[68:71], v[92:95], v[252:255]\n\t" \
  "v_add_u32 v53, v53, v12\n\t" \
  "ds_read_b128 v[96:99], v8 offset:14336\n\t" \
  "v_mfma_f32_16x16x32_bf16 v[252:255], v[140:143], v[108:111], v[252:255]\n\t" \
  "ds_read_b64_tr_b16 v[72:73], v5 offset:1280\n\t" \
  "v_mfma_f32_16x16x32_bf16 v[252:255], v[124:127], v[112:115], v[252:255]\n\t" \
  "v_lshrrev_b32 v14, 1, v4\n\t" \
  "ds_read_b64_tr_b16 v[74:75], v5 offset:1296\n\t" \
  "v_mfma_f32_16x16x32_bf16 v[252:255], v[64:67], v[120:123], v[252:255]\n\t" \
  "ds_read_b128 v[100:103], v8 offset:13312\n\t" \
  "v_mfma_f32_16x16x32_bf16 v[248:251], v[160:163], v[80:83], v[248:251]\n\t" \
  "v_lshrrev_b32 v15, 24, v3\n\t" \
  "ds_read_b64_tr_b16 v[104:105], v5 offset:1792\n\t" \
  "v_mfma_f32_16x16x32_bf16 v[248:251], v[172:175], v[84:87], v[248:251]\n\t" \
  "ds_read_b64_tr_b16 v[106:107], v5 offset:1808\n\t" \
  "v_mfma_f32_16x16x32_bf16 v[248:251], v[180:183], v[88:91], v[248:251]\n\t" \
  "v_add_u32 v14, %20, v14\n\t" \
  "ds_read_b128 v[152:155], v8 offset:12288\n\t" \
  "v_mfma_f32_16x16x32_bf16 v[248:251], v[188:191], v[92:95], v[248:251]\n\t" \
  "ds_read_b64_tr_b16 v[64:65], v5 offset:2304\n\t" \
  "v_mfma_f32_16x16x32_bf16 v[248:251], v[68:71], v[108:111], v[248:251]\n\t" \
  "v_lshrrev_b32 v18, 10, v12\n\t" \
  "ds_read_b64_tr_b16 v[66:67], v5 offset:2320\n\t" \
  "v_mfma_f32_16x16x32_bf16 v[248:251], v[140:143], v[112:115], v[248:251]\n\t" \
  "ds_read_b128 v[80:83], v8 offset:11264\n\t" \
  "v_mfma_f32_16x16x32_bf16 v[248:251], v[124:127], v[120:123], v[248:251]\n\t" \
  "v_and_b32 v18, 12, v18\n\t" \
  "v_mfma_f32_16x16x32_bf16 v[244:247], v[160:163], v[84:87], v[244:247]\n\t" \
  "v_mfma_f32_16x16x32_bf16 v[244:247], v[172:175], v[88:91], v[244:247]\n\t" \
  "v_mov_b32 v17, 16\n\t" \
  "ds_read_b64_tr_b16 v[84:85], v5 offset:2816\n\t" \
  "v_mfma_f32_16x16x32_bf16 v[244:247], v[180:183], v[92:95], v[244:247]\n\t" \
  "ds_read_b64_tr_b16 v[86:87], v5 offset:2832\n\t" \
  "v_mfma_f32_16x16x32_bf16 v[244:247], v[188:191], v[108:111], v[244:247]\n\t" \
  "v_add_u32 v18, v56, v18\n\t" \
  "ds_read_b128 v[156:159], v8 offset:10240\n\t" \
  "v_mfma_f32_16x16x32_bf16 v[244:247], v[68:71], v[112:115], v[244:247]\n\t" \
  "v_mfma_f32_16x16x32_bf16 v[244:247], v[140:143], v[120:123], v[244:247]\n\t" \
  "s_mov_b32 s76, 0\n\t" \
  "v_mfma_f32_16x16x32_bf16 v[240:243], v[160:163], v[88:91], v[240:243]\n\t" \
  "v_mfma_f32_16x16x32_bf16 v[240:243], v[172:175], v[92:95], v[240:243]\n\t" \
  "s_brev_b32 s77, 1\n\t" \
  "v_mfma_f32_16x16x32_bf16 v[240:243], v[180:183], v[108:111], v[240:243]\n\t" \
  "ds_read_b64_tr_b16 v[88:89], v5 offset:3328\n\t" \
  "v_mfma_f32_16x16x32_bf16 v[240:243], v[188:191], v[112:115], v[240:243]\n\t" \
  "s_mov_b32 s75, 0\n\t" \
  "ds_read_b64_tr_b16 v[90:91], v5 offset:3344\n\t" \
  "v_mfma_f32_16x16x32_bf16 v[240:243], v[68:71], v[120:123], v[240:243]\n\t" \
  "ds_read_b128 v[164:167], v8 offset:9216\n\t" \
  "v_mfma_f32_16x16x32_bf16 v[236:239], v[160:163], v[92:95], v[236:239]\n\t" \
  "v_add_u32 v28, 0, v13\n\t" \
  "v_mfma_f32_16x16x32_bf16 v[236:239], v[172:175], v[108:111], v[236:239]\n\t" \
  "v_mfma_f32_16x16x32_bf16 v[236:239], v[180:183], v[112:115], v[236:239]\n\t" \
  "v_xor_b32 v29, 32, v28\n\t" \
  "v_mfma_f32_16x16x32_bf16 v[236:239], v[188:191], v[120:123], v[236:239]\n\t" \
  "v_mfma_f32_16x16x32_bf16 v[232:235], v[160:163], v[108:111], v[232:235]\n\t" \
  "v_xor_b32 v30, 64, v28\n\t" \
  "v_mfma_f32_16x16x32_bf16 v[232:235], v[172:175], v[112:115], v[232:235]\n\t" \
  "ds_read_b64_tr_b16 v[68:69], v5 offset:3840\n\t" \
  "v_mfma_f32_16x16x32_bf16 v[232:235], v[180:183], v[120:123], v[232:235]\n\t" \
  "v_xor_b32 v31, 96, v28\n\t" \
  "ds_read_b64_tr_b16 v[70:71], v5 offset:3856\n\t" \
  "v_mfma_f32_16x16x32_bf16 v[228:231], v[160:163], v[112:115], v[228:231]\n\t" \
  "ds_read_b128 v[92:95], v8 offset:0\n\t" \
  "v_mfma_f32_16x16x32_bf16 v[228:231], v[172:175], v[120:123], v[228:231]\n\t" \
  "v_add_u32 v32, 65536, v13\n\t" \
  "ds_read_b128 v[168:171], v8 offset:1024\n\t" \
  "v_mfma_f32_16x16x32_bf16 v[224:227], v[160:163], v[120:123], v[224:227]\n\t" \
  "ds_read_b128 v[172:175], v8 offset:2048\n\t" \
  "s_waitcnt lgkmcnt(5)\n\t" \
  "v_mfma_f32_16x16x32_bf16 v[192:195], v[76:79], v[128:131], v[192:195]\n\t" \
  "v_xor_b32 v33, 32, v32\n\t" \
  "ds_read_b128 v[160:163], v8 offset:3072\n\t" \
  "v_mfma_f32_16x16x32_bf16 v[196:199], v[76:79], v[136:139], v[196:199]\n\t" \
  "ds_read_b128 v[176:179], v8 offset:4096\n\t" \
  "v_mfma_f32_16x16x32_bf16 v[196:199], v[144:147], v[128:131], v[196:199]\n\t" \
  "v_xor_b32 v34, 64, v32\n\t" \
  "ds_read_b128 v[180:183], v8 offset:5120\n\t" \
  "v_mfma_f32_16x16x32_bf16 v[200:203], v[76:79], v[96:99], v[200:203]\n\t" \
  "ds_read_b128 v[184:187], v8 offset:6144\n\t" \
  "v_mfma_f32_16x16x32_bf16 v[200:203], v[144:147], v[136:139], v[200:203]\n\t" \
  "v_xor_b32 v35, 96, v32\n\t" \
  "ds_read_b128 v[188:191], v8 offset:7168\n\t" \
  "v_mfma_f32_16x16x32_bf16 v[200:203], v[72:75], v[128:131], v[200:203]\n\t" \
  "v_mfma_f32_16x16x32_bf16 v[204:207], v[76:79], v[100:103], v[204:207]\n\t" \
  "v_add_u32 v36, 131072, v13\n\t" \
  "v_mfma_f32_16x16x32_bf16 v[204:207], v[144:147], v[96:99], v[204:207]\n\t" \
  "v_mfma_f32_16x16x32_bf16 v[204:207], v[72:75], v[136:139], v[204:207]\n\t" \
  "v_xor_b32 v37, 32, v36\n\t" \
  "v_mfma_f32_16x16x32_bf16 v[204:207], v[104:107], v[128:131], v[204:207]\n\t" \
  "v_mfma_f32_16x16x32_bf16 v[208:211], v[76:79], v[152:155], v[208:211]\n\t" \
  "v_xor_b32 v38, 64, v36\n\t" \
  "v_mfma_f32_16x16x32_bf16 v[208:211], v[144:147], v[100:103], v[208:211]\n\t" \
  "v_mfma_f32_16x16x32_bf16 v[208:211], v[72:75], v[96:99], v[208:211]\n\t" \
  "v_xor_b32 v39, 96, v36\n\t" \
  "v_mfma_f32_16x16x32_bf16 v[208:211], v[104:107], v[136:139], v[208:211]\n\t" \
  "v_mfma_f32_16x16x32_bf16 v[208:211], v[64:67], v[128:131], v[208:211]\n\t" \
  "v_lshrrev_b32 v57, 5, v4\n\t" \
  "v_mfma_f32_16x16x32_bf16 v[212:215], v[76:79], v[80:83], v[212:215]\n\t" \
  "v_mfma_f32_16x16x32_bf16 v[212:215], v[144:147], v[152:155], v[212:215]\n\t" \
  "v_and_b32 v57, 0x18, v57\n\t" \
  "v_mfma_f32_16x16x32_bf16 v[212:215], v[72:75], v[100:103], v[212:215]\n\t" \
  "v_mfma_f32_16x16x32_bf16 v[212:215], v[104:107], v[96:99], v[212:215]\n\t" \
  "v_and_b32 v62, 0x70, v4\n\t" \
  "v_mfma_f32_16x16x32_bf16 v[212:215], v[64:67], v[136:139], v[212:215]\n\t" \
  "v_mfma_f32_16x16x32_bf16 v[212:215], v[84:87], v[128:131], v[212:215]\n\t" \
  "v_xor_b32 v57, v57, v62\n\t" \
  "v_mfma_f32_16x16x32_bf16 v[216:219], v[76:79], v[156:159], v[216:219]\n\t" \
  "v_mfma_f32_16x16x32_bf16 v[216:219], v[144:147], v[80:83], v[216:219]\n\t" \
  "v_and_b32 v58, 0xf0, v4\n\t" \
  "v_mfma_f32_16x16x32_bf16 v[216:219], v[72:75], v[152:155], v[216:219]\n\t" \
  "v_mfma_f32_16x16x32_bf16 v[216:219], v[104:107], v[100:103], v[216:219]\n\t" \
  "v_lshl_add_u32 v57, v58, 3, v57\n\t" \
  "v_mfma_f32_16x16x32_bf16 v[216:219], v[64:67], v[96:99], v[216:219]\n\t" \
  "v_mfma_f32_16x16x32_bf16 v[216:219], v[84:87], v[136:139], v[216:219]\n\t" \
  "v_add_u32 v57, %23, v57\n\t" \
  "v_mfma_f32_16x16x32_bf16 v[216:219], v[88:91], v[128:131], v[216:219]\n\t" \
  "v_mfma_f32_16x16x32_bf16 v[220:223], v[76:79], v[164:167], v[220:223]\n\t" \
  "v_xor_b32 v58, 32, v57\n\t" \
  "v_mfma_f32_16x16x32_bf16 v[220:223], v[144:147], v[156:159], v[220:223]\n\t" \
  "v_mfma_f32_16x16x32_bf16 v[220:223], v[72:75], v[80:83], v[220:223]\n\t" \
  "v_xor_b32 v59, 64, v57\n\t" \
  "v_mfma_f32_16x16x32_bf16 v[220:223], v[104:107], v[152:155], v[220:223]\n\t" \
  "v_mfma_f32_16x16x32_bf16 v[220:223], v[64:67], v[100:103], v[220:223]\n\t" \
  "v_xor_b32 v60, 96, v57\n\t" \
  "ds_read_b64_tr_b16 v[148:149], v5 offset:384\n\t" \
  "v_mfma_f32_16x16x32_bf16 v[220:223], v[84:87], v[96:99], v[220:223]\n\t" \
  "ds_read_b64_tr_b16 v[150:151], v5 offset:400\n\t" \
  "v_mfma_f32_16x16x32_bf16 v[220:223], v[88:91], v[136:139], v[220:223]\n\t" \
  "v_lshrrev_b32 v61, 3, v4\n\t" \
  "ds_read_b128 v[80:83], v9 offset:16384\n\t" \
  "s_waitcnt lgkmcnt(3)\n\t" \
  "v_mfma_f32_16x16x32_bf16 v[220:223], v[68:71], v[128:131], v[220:223]\n\t" \
  "ds_read_b128 v[140:143], v9 offset:15360\n\t" \
  "v_mfma_f32_16x16x32_bf16 v[252:255], v[76:79], v[92:95], v[252:255]\n\t" \
  "v_and_b32 v61, 0x70, v61\n\t" \
  "ds_read_b64_tr_b16 v[132:133], v5 offset:896\n\t" \
  "v_mfma_f32_16x16x32_bf16 v[252:255], v[144:147], v[168:171], v[252:255]\n\t" \
  "ds_read_b64_tr_b16 v[134:135], v5 offset:912\n\t" \
  "v_mfma_f32_16x16x32_bf16 v[252:255], v[72:75], v[172:175], v[252:255]\n\t" \
  "v_xor_b32 v61, v61, v4\n\t" \
  "ds_read_b128 v[124:127], v9 offset:14336\n\t" \
  "v_mfma_f32_16x16x32_bf16 v[252:255], v[104:107], v[160:163], v[252:255]\n\t" \
  "ds_read_b64_tr_b16 v[116:117], v5 offset:1408\n\t" \
  "v_mfma_f32_16x16x32_bf16 v[252:255], v[64:67], v[176:179], v[252:255]\n\t" \
  "v_add_u32 v61, %23, v61\n\t" \
  "ds_read_b64_tr_b16 v[118:119], v5 offset:1424\n\t" \
  "v_mfma_f32_16x16x32_bf16 v[252:255], v[84:87], v[180:183], v[252:255]\n\t" \
  "ds_read_b128 v[92:95], v9 offset:13312\n\t" \
  "v_mfma_f32_16x16x32_bf16 v[252:255], v[88:91], v[184:187], v[252:255]\n\t" \
  "v_and_b32 v63, 0x380, v4\n\t" \
  "ds_read_b64_tr_b16 v[108:109], v5 offset:1920\n\t" \
  "v_mfma_f32_16x16x32_bf16 v[252:255], v[68:71], v[188:191], v[252:255]\n\t" \
  "ds_read_b64_tr_b16 v[110:111], v5 offset:1936\n\t" \
  "v_mfma_f32_16x16x32_bf16 v[248:251], v[76:79], v[168:171], v[248:251]\n\t" \
  "v_lshl_add_u32 v62, v63, 6, v62\n\t" \
  "ds_read_b128 v[112:115], v9 offset:12288\n\t" \
  "v_mfma_f32_16x16x32_bf16 v[248:251], v[144:147], v[172:175], v[248:251]\n\t" \
  "v_mfma_f32_16x16x32_bf16 v[248:251], v[72:75], v[160:163], v[248:251]\n\t" \
  "v_add_u32 v63, 0x10000, v62\n\t" \
  "ds_read_b64_tr_b16 v[120:121], v5 offset:2432\n\t" \
  "v_mfma_f32_16x16x32_bf16 v[248:251], v[104:107], v[176:179], v[248:251]\n\t" \
  "ds_read_b64_tr_b16 v[122:123], v5 offset:2448\n\t" \
  "v_mfma_f32_16x16x32_bf16 v[248:251], v[64:67], v[180:183], v[248:251]\n\t" \
  "ds_read_b128 v[68:71], v9 offset:11264\n\t" \
  "v_mfma_f32_16x16x32_bf16 v[248:251], v[84:87], v[184:187], v[248:251]\n\t" \
  "v_mfma_f32_16x16x32_bf16 v[248:251], v[88:91], v[188:191], v[248:251]\n\t" \
  "v_mfma_f32_16x16x32_bf16 v[244:247], v[76:79], v[172:175], v[244:247]\n\t" \
  "v_mfma_f32_16x16x32_bf16 v[244:247], v[144:147], v[160:163], v[244:247]\n\t" \
  "ds_read_b64_tr_b16 v[128:129], v5 offset:2944\n\t" \
  "v_mfma_f32_16x16x32_bf16 v[244:247], v[72:75], v[176:179], v[244:247]\n\t" \
  "ds_read_b64_tr_b16 v[130:131], v5 offset:2960\n\t" \
  "v_mfma_f32_16x16x32_bf16 v[244:247], v[104:107], v[180:183], v[244:247]\n\t" \
  "ds_read_b128 v[88:91], v9 offset:10240\n\t" \
  "v_mfma_f32_16x16x32_bf16 v[244:247], v[64:67], v[184:187], v[244:247]\n\t" \
  "v_mfma_f32_16x16x32_bf16 v[244:247], v[84:87], v[188:191], v[244:247]\n\t" \
  "v_mfma_f32_16x16x32_bf16 v[240:243], v[76:79], v[160:163], v[240:243]\n\t" \
  "v_mfma_f32_16x16x32_bf16 v[240:243], v[144:147], v[176:179], v[240:243]\n\t" \
  "v_mfma_f32_16x16x32_bf16 v[240:243], v[72:75], v[180:183], v[240:243]\n\t" \
  "ds_read_b64_tr_b16 v[136:137], v5 offset:3456\n\t" \
  "v_mfma_f32_16x16x32_bf16 v[240:243], v[104:107], v[184:187], v[240:243]\n\t" \
  "ds_read_b64_tr_b16 v[138:139], v5 offset:3472\n\t" \
  "v_mfma_f32_16x16x32_bf16 v[240:243], v[64:67], v[188:191], v[240:243]\n\t" \
  "ds_read_b128 v[84:87], v9 offset:9216\n\t" \
  "v_mfma_f32_16x16x32_bf16 v[236:239], v[76:79], v[176:179], v[236:239]\n\t" \
  "v_mfma_f32_16x16x32_bf16 v[236:239], v[144:147], v[180:183], v[236:239]\n\t" \
  "v_mfma_f32_16x16x32_bf16 v[236:239], v[72:75], v[184:187], v[236:239]\n\t" \
  "v_mfma_f32_16x16x32_bf16 v[236:239], v[104:107], v[188:191], v[236:239]\n\t" \
  "v_mfma_f32_16x16x32_bf16 v[232:235], v[76:79], v[180:183], v[232:235]\n\t" \
  "v_mfma_f32_16x16x32_bf16 v[232:235], v[144:147], v[184:187], v[232:235]\n\t" \
  "ds_read_b64_tr_b16 v[96:97], v5 offset:3968\n\t" \
  "v_mfma_f32_16x16x32_bf16 v[232:235], v[72:75], v[188:191], v[232:235]\n\t" \
  "ds_read_b64_tr_b16 v[98:99], v5 offset:3984\n\t" \
  "ds_write_b32 v18, v17 offset:176\n\t" \
  "v_mfma_f32_16x16x32_bf16 v[228:231], v[76:79], v[184:187], v[228:231]\n\t" \
  "ds_read_b128 v[100:103], v9 offset:7168\n\t" \
  "v_mfma_f32_16x16x32_bf16 v[228:231], v[144:147], v[188:191], v[228:231]\n\t" \
  "ds_read_b128 v[104:107], v9 offset:6144\n\t" \
  "v_mfma_f32_16x16x32_bf16 v[224:227], v[76:79], v[188:191], v[224:227]\n\t" \
  "ds_read_b128 v[144:147], v9 offset:5120\n\t" \
  "s_waitcnt lgkmcnt(6)\n\t" \
  "v_mfma_f32_16x16x32_bf16 v[192:195], v[148:151], v[80:83], v[192:195]\n\t" \
  "v_mfma_f32_16x16x32_bf16 v[196:199], v[148:151], v[140:143], v[196:199]\n\t" \
  "v_mfma_f32_16x16x32_bf16 v[196:199], v[132:135], v[80:83], v[196:199]\n\t" \
  "ds_read_b128 v[152:155], v9 offset:4096\n\t" \
  "v_mfma_f32_16x16x32_bf16 v[200:203], v[148:151], v[124:127], v[200:203]\n\t" \
  "v_mfma_f32_16x16x32_bf16 v[200:203], v[132:135], v[140:143], v[200:203]\n\t" \
  "v_mfma_f32_16x16x32_bf16 v[200:203], v[116:119], v[80:83], v[200:203]\n\t" \
  "v_mfma_f32_16x16x32_bf16 v[204:207], v[148:151], v[92:95], v[204:207]\n\t" \
  "ds_read_b128 v[156:159], v9 offset:3072\n\t" \
  "v_cndmask_b32 v20, v195, 0, s[76:77]\n\t" \
  "v_mfma_f32_16x16x32_bf16 v[204:207], v[132:135], v[124:127], v[204:207]\n\t" \
  "ds_bpermute_b32 v20, v15, v20\n\t" \
  "v_add_f32 v225, v225, v192\n\t" \
  "v_mfma_f32_16x16x32_bf16 v[204:207], v[116:119], v[140:143], v[204:207]\n\t" \
  "v_add_f32 v226, v226, v193\n\t" \
  "v_add_f32 v227, v227, v194\n\t" \
  "v_mfma_f32_16x16x32_bf16 v[204:207], v[108:111], v[80:83], v[204:207]\n\t" \
  "v_cndmask_b32 v21, v199, v195, s[76:77]\n\t" \
  "ds_bpermute_b32 v21, v15, v21\n\t" \
  "v_mfma_f32_16x16x32_bf16 v[208:211], v[148:151], v[112:115], v[208:211]\n\t" \
  "v_add_f32 v229, v229, v196\n\t" \
  "v_add_f32 v230, v230, v197\n\t" \
  "v_mfma_f32_16x16x32_bf16 v[208:211], v[132:135], v[92:95], v[208:211]\n\t" \
  "ds_read_b128 v[164:167], v9 offset:2048\n\t" \
  "v_add_f32 v231, v231, v198\n\t" \
  "v_mfma_f32_16x16x32_bf16 v[208:211], v[116:119], v[124:127], v[208:211]\n\t" \
  "v_cndmask_b32 v22, v203, v199, s[76:77]\n\t" \
  "ds_bpermute_b32 v22, v15, v22\n\t" \
  "v_mfma_f32_16x16x32_bf16 v[208:211], v[108:111], v[140:143], v[208:211]\n\t" \
  "v_add_f32 v233, v233, v200\n\t" \
  "v_add_f32 v234, v234, v201\n\t" \
  "v_mfma_f32_16x16x32_bf16 v[208:211], v[120:123], v[80:83], v[208:211]\n\t" \
  "v_add_f32 v235, v235, v202\n\t" \
  "s_waitcnt lgkmcnt(3)\n\t" \
  "v_add_f32 v224, v224, v20\n\t" \
  "v_mfma_f32_16x16x32_bf16 v[212:215], v[148:151], v[68:71], v[212:215]\n\t" \
  "v_cndmask_b32 v23, v207, v203, s[76:77]\n\t" \
  "ds_bpermute_b32 v23, v15, v23\n\t" \
  "v_mfma_f32_16x16x32_bf16 v[212:215], v[132:135], v[112:115], v[212:215]\n\t" \
  "v_add_f32 v237, v237, v204\n\t" \
  "v_add_f32 v238, v238, v205\n\t" \
  "v_mfma_f32_16x16x32_bf16 v[212:215], v[116:119], v[92:95], v[212:215]\n\t" \
  "ds_read_b128 v[168:171], v9 offset:1024\n\t" \
  "v_add_f32 v239, v239, v206\n\t" \
  "v_mfma_f32_16x16x32_bf16 v[212:215], v[108:111], v[124:127], v[212:215]\n\t" \
  "s_waitcnt lgkmcnt(4)\n\t" \
  "v_add_f32 v228, v228, v21\n\t" \
  "v_mfma_f32_16x16x32_bf16 v[212:215], v[120:123], v[140:143], v[212:215]\n\t" \
  "s_waitcnt lgkmcnt(2)\n\t" \
  "v_add_f32 v232, v232, v22\n\t" \
  "v_mfma_f32_16x16x32_bf16 v[212:215], v[128:131], v[80:83], v[212:215]\n\t" \
  "v_cndmask_b32 v24, v211, v207, s[76:77]\n\t" \
  "ds_bpermute_b32 v24, v15, v24\n\t" \
  "v_mfma_f32_16x16x32_bf16 v[216:219], v[148:151], v[88:91], v[216:219]\n\t" \
  "v_add_f32 v241, v241, v208\n\t" \
  "v_add_f32 v242, v242, v209\n\t" \
  "v_mfma_f32_16x16x32_bf16 v[216:219], v[132:135], v[68:71], v[216:219]\n\t" \
  "v_add_f32 v243, v243, v210\n\t" \
  "v_mfma_f32_16x16x32_bf16 v[216:219], v[116:119], v[112:115], v[216:219]\n\t" \
  "s_waitcnt lgkmcnt(2)\n\t" \
  "v_add_f32 v236, v236, v23\n\t" \
  "v_mfma_f32_16x16x32_bf16 v[216:219], v[108:111], v[92:95], v[216:219]\n\t" \
  "ds_read_b128 v[172:175], v9 offset:0\n\t" \
  "ds_write_b32 v18, v17 offset:192\n\t" \
  "v_mfma_f32_16x16x32_bf16 v[216:219], v[120:123], v[124:127], v[216:219]\n\t" \
  "v_mfma_f32_16x16x32_bf16 v[216:219], v[128:131], v[140:143], v[216:219]\n\t" \
  "ds_read_b32 v16, v56 offset:96\n\t" \
  "v_cndmask_b32 v25, v215, v211, s[76:77]\n\t" \
  "v_mfma_f32_16x16x32_bf16 v[216:219], v[136:139], v[80:83], v[216:219]\n\t" \
  "ds_bpermute_b32 v25, v15, v25\n\t" \
  "v_add_f32 v245, v245, v212\n\t" \
  "v_mfma_f32_16x16x32_bf16 v[220:223], v[148:151], v[84:87], v[220:223]\n\t" \
  "v_add_f32 v246, v246, v213\n\t" \
  "v_add_f32 v247, v247, v214\n\t" \
  "v_mfma_f32_16x16x32_bf16 v[220:223], v[132:135], v[88:91], v[220:223]\n\t" \
  "s_waitcnt lgkmcnt(4)\n\t" \
  "v_add_f32 v240, v240, v24\n\t" \
  "v_mfma_f32_16x16x32_bf16 v[220:223], v[116:119], v[68:71], v[220:223]\n\t" \
  "v_mfma_f32_16x16x32_bf16 v[220:223], v[108:111], v[112:115], v[220:223]\n\t" \
  "v_mfma_f32_16x16x32_bf16 v[220:223], v[120:123], v[92:95], v[220:223]\n\t" \
  "v_mfma_f32_16x16x32_bf16 v[220:223], v[128:131], v[124:127], v[220:223]\n\t" \
  "s_waitcnt lgkmcnt(1)\n\t" \
  "v_readfirstlane_b32 vcc_lo, v16\n\t" \
  "v_mfma_f32_16x16x32_bf16 v[220:223], v[136:139], v[140:143], v[220:223]\n\t" \
  "s_cmp_eq_u32 vcc_lo, 0\n\t" \
  "s_cbranch_scc1 33f\n\t" \
  "s_lshr_b64 s[90:91], %18, s75\n\t" \
  "s_and_b32 s90, s90, 15\n\t" \
  "s_lshl_b32 s90, s90, 15\n\t" \
  "s_add_u32 s90, s90, s92\n\t" \
  "s_addc_u32 s91, s93, 0\n\t" \
  "s_add_u32 s75, s75, 4\n\t" \
  "s_swappc_b64 s[68:69], s[70:71]\n\t" \
  "33:\n\t" \
  "v_mfma_f32_16x16x32_bf16 v[220:223], v[96:99], v[80:83], v[220:223]\n\t" \
  "v_cndmask_b32 v26, v219, v215, s[76:77]\n\t" \
  "ds_bpermute_b32 v26, v15, v26\n\t" \
  "v_mfma_f32_16x16x32_bf16 v[224:227], v[148:151], v[100:103], v[224:227]\n\t" \
  "v_add_f32 v249, v249, v216\n\t" \
  "v_add_f32 v250, v250, v217\n\t" \
  "v_mfma_f32_16x16x32_bf16 v[228:231], v[148:151], v[104:107], v[228:231]\n\t" \
  "ds_read_b128 v[40:43], v56 offset:80\n\t" \
  "v_add_f32 v251, v251, v218\n\t" \
  "v_mfma_f32_16x16x32_bf16 v[228:231], v[132:135], v[100:103], v[228:231]\n\t" \
  "s_waitcnt lgkmcnt(2)\n\t" \
  "v_add_f32 v244, v244, v25\n\t" \
  "v_mfma_f32_16x16x32_bf16 v[232:235], v[148:151], v[144:147], v[232:235]\n\t" \
  "v_mfma_f32_16x16x32_bf16 v[232:235], v[132:135], v[104:107], v[232:235]\n\t" \
  "v_mfma_f32_16x16x32_bf16 v[232:235], v[116:119], v[100:103], v[232:235]\n\t" \
  "s_waitcnt lgkmcnt(1)\n\t" \
  "v_add_f32 v248, v248, v26\n\t" \
  "v_cndmask_b32 v27, v223, v219, s[76:77]\n\t" \
  "v_mfma_f32_16x16x32_bf16 v[236:239], v[148:151], v[152:155], v[236:239]\n\t" \
  "ds_bpermute_b32 v27, v15, v27\n\t" \
  "v_add_f32 v253, v253, v220\n\t" \
  "v_mfma_f32_16x16x32_bf16 v[236:239], v[132:135], v[144:147], v[236:239]\n\t" \
  "v_add_f32 v254, v254, v221\n\t" \
  "v_add_f32 v255, v255, v222\n\t" \
  "508:\n\t" \
  "s_cbranch_execz 26b\n\t" \
  "v_mfma_f32_16x16x32_bf16 v[236:239], v[116:119], v[104:107], v[236:239]\n\t" \
  "s_waitcnt lgkmcnt(1)\n\t" \
  "v_min_u32 v40, v40, v41\n\t" \
  "v_min3_u32 v40, v40, v42, v43\n\t" \
  "v_mfma_f32_16x16x32_bf16 v[236:239], v[108:111], v[100:103], v[236:239]\n\t" \
  "v_readfirstlane_b32 vcc_lo, v40\n\t" \
  "v_cvt_pk_bf16_f32 v45, v226, v227\n\t" \
  "v_mfma_f32_16x16x32_bf16 v[240:243], v[148:151], v[156:159], v[240:243]\n\t" \
  "ds_read_b32 v16, v56 offset:96\n\t" \
  "s_cmp_ge_u32 vcc_lo, 7\n\t" \
  "s_cbranch_scc0 36f\n\t" \
  "37:\n\t" \
  "v_mfma_f32_16x16x32_bf16 v[240:243], v[132:135], v[152:155], v[240:243]\n\t" \
  "v_cvt_pk_bf16_f32 v44, v224, v225\n\t" \
  "ds_write_b64 v14, v[44:45] offset:0\n\t" \
  "v_mfma_f32_16x16x32_bf16 v[240:243], v[116:119], v[144:147], v[240:243]\n\t" \
  "v_cvt_pk_bf16_f32 v47, v230, v231\n\t" \
  "v_cvt_pk_bf16_f32 v46, v228, v229\n\t" \
  "v_mfma_f32_16x16x32_bf16 v[240:243], v[108:111], v[104:107], v[240:243]\n\t" \
  "v_mov_b32 v19, 1\n\t" \
  "ds_write_b64 v14, v[46:47] offset:512\n\t" \
  "v_mfma_f32_16x16x32_bf16 v[240:243], v[120:123], v[100:103], v[240:243]\n\t" \
  "ds_write_b32 v18, v19 offset:32\n\t" \
  "v_cvt_pk_bf16_f32 v49, v234, v235\n\t" \
  "v_mfma_f32_16x16x32_bf16 v[244:247], v[148:151], v[164:167], v[244:247]\n\t" \
  "v_cvt_pk_bf16_f32 v48, v232, v233\n\t" \
  "ds_write_b64 v14, v[48:49] offset:1024\n\t" \
  "v_mfma_f32_16x16x32_bf16 v[244:247], v[132:135], v[156:159], v[244:247]\n\t" \
  "s_waitcnt lgkmcnt(5)\n\t" \
  "v_add_f32 v252, v252, v27\n\t" \
  "v_cvt_pk_bf16_f32 v51, v238, v239\n\t" \
  "v_mfma_f32_16x16x32_bf16 v[244:247], v[116:119], v[152:155], v[244:247]\n\t" \
  "s_waitcnt lgkmcnt(4)\n\t" \
  "v_readfirstlane_b32 vcc_lo, v16\n\t" \
  "v_mfma_f32_16x16x32_bf16 v[244:247], v[108:111], v[144:147], v[244:247]\n\t" \
  "s_cmp_eq_u32 vcc_lo, 0\n\t" \
  "s_cbranch_scc1 34f\n\t" \
  "s_lshr_b64 s[90:91], %18, s75\n\t" \
  "s_and_b32 s90, s90, 15\n\t" \
  "s_lshl_b32 s90, s90, 15\n\t" \
  "s_add_u32 s90, s90, s92\n\t" \
  "s_addc_u32 s91, s93, 0\n\t" \
  "s_add_u32 s75, s75, 4\n\t" \
  "s_swappc_b64 s[68:69], s[70:71]\n\t" \
  "34:\n\t" \
  "v_mfma_f32_16x16x32_bf16 v[244:247], v[120:123], v[104:107], v[244:247]\n\t" \
  "v_cvt_pk_bf16_f32 v50, v236, v237\n\t" \
  "v_mov_b32 v19, 3\n\t" \
  "v_mfma_f32_16x16x32_bf16 v[244:247], v[128:131], v[100:103], v[244:247]\n\t" \
  "ds_write_b64 v14, v[50:51] offset:1536\n\t" \
  "ds_write_b32 v18, v19 offset:32\n\t" \
  "v_mfma_f32_16x16x32_bf16 v[248:251], v[148:151], v[168:171], v[248:251]\n\t" \
  "v_cvt_pk_bf16_f32 v45, v242, v243\n\t" \
  "v_cvt_pk_bf16_f32 v44, v240, v241\n\t" \
  "v_mfma_f32_16x16x32_bf16 v[248:251], v[132:135], v[164:167], v[248:251]\n\t" \
  "ds_read_b32 v16, v56 offset:96\n\t" \
  "ds_write_b64 v14, v[44:45] offset:2048\n\t" \
  "v_mfma_f32_16x16x32_bf16 v[248:251], v[116:119], v[156:159], v[248:251]\n\t" \
  "ds_read_b64_tr_b16 v[192:193], v28 offset:22528\n\t" \
  "v_mfma_f32_16x16x32_bf16 v[248:251], v[108:111], v[152:155], v[248:251]\n\t" \
  "ds_read_b64_tr_b16 v[194:195], v28 offset:23040\n\t" \
  "v_mfma_f32_16x16x32_bf16 v[248:251], v[120:123], v[144:147], v[248:251]\n\t" \
  "ds_read_b64_tr_b16 v[196:197], v28 offset:26624\n\t" \
  "v_mfma_f32_16x16x32_bf16 v[248:251], v[128:131], v[104:107], v[248:251]\n\t" \
  "v_cvt_pk_bf16_f32 v47, v246, v247\n\t" \
  "v_cvt_pk_bf16_f32 v46, v244, v245\n\t" \
  "v_mfma_f32_16x16x32_bf16 v[248:251], v[136:139], v[100:103], v[248:251]\n\t" \
  "v_mov_b32 v19, 7\n\t" \
  "ds_write_b64 v14, v[46:47] offset:2560\n\t" \
  "v_mfma_f32_16x16x32_bf16 v[252:255], v[148:151], v[172:175], v[252:255]\n\t" \
  "ds_write_b32 v18, v19 offset:32\n\t" \
  "ds_read_b64_tr_b16 v[198:199], v28 offset:27136\n\t" \
  "v_mfma_f32_16x16x32_bf16 v[252:255], v[132:135], v[168:171], v[252:255]\n\t" \
  "s_waitcnt lgkmcnt(7)\n\t" \
  "v_readfirstlane_b32 vcc_lo, v16\n\t" \
  "v_mfma_f32_16x16x32_bf16 v[252:255], v[116:119], v[164:167], v[252:255]\n\t" \
  "s_cmp_eq_u32 vcc_lo, 0\n\t" \
  "s_cbranch_scc1 35f\n\t" \
  "s_cmp_ge_u32 s75, 8\n\t" \
  "s_cbranch_scc1 35f\n\t" \
  "s_lshr_b64 s[90:91], %18, s75\n\t" \
  "s_and_b32 s90, s90, 15\n\t" \
  "s_lshl_b32 s90, s90, 15\n\t" \
  "s_add_u32 s90, s90, s92\n\t" \
  "s_addc_u32 s91, s93, 0\n\t" \
  "s_add_u32 s75, s75, 4\n\t" \
  "s_swappc_b64 s[68:69], s[70:71]\n\t" \
  "35:\n\t" \
  "v_mfma_f32_16x16x32_bf16 v[252:255], v[108:111], v[156:159], v[252:255]\n\t" \
  "ds_read_b64_tr_b16 v[200:201], v29 offset:22528\n\t" \
  "v_mfma_f32_16x16x32_bf16 v[252:255], v[120:123], v[152:155], v[252:255]\n\t" \
  "ds_read_b64_tr_b16 v[202:203], v29 offset:23040\n\t" \
  "v_mfma_f32_16x16x32_bf16 v[252:255], v[128:131], v[144:147], v[252:255]\n\t" \
  "v_cvt_pk_bf16_f32 v49, v250, v251\n\t" \
  "v_cvt_pk_bf16_f32 v48, v248, v249\n\t" \
  "v_mfma_f32_16x16x32_bf16 v[252:255], v[136:139], v[104:107], v[252:255]\n\t" \
  "ds_write_b64 v14, v[48:49] offset:3072\n\t" \
  "ds_read_b64_tr_b16 v[204:205], v29 offset:26624\n\t" \
  "v_mfma_f32_16x16x32_bf16 v[252:255], v[96:99], v[100:103], v[252:255]\n\t" \
  "ds_read_b64_tr_b16 v[206:207], v29 offset:27136\n\t" \
  "ds_read_b32 v16, v56 offset:96\n\t" \
  "ds_read_b64_tr_b16 v[208:209], v30 offset:22528\n\t" \
  "ds_read_b64_tr_b16 v[210:211], v30 offset:23040\n\t" \
  "ds_read_b64_tr_b16 v[212:213], v30 offset:26624\n\t" \
  "ds_read_b64_tr_b16 v[214:215], v30 offset:27136\n\t" \
  "ds_read_b64_tr_b16 v[216:217], v31 offset:22528\n\t" \
  "ds_read_b64_tr_b16 v[218:219], v31 offset:23040\n\t" \
  "ds_read_b64_tr_b16 v[220:221], v31 offset:26624\n\t" \
  "ds_read_b64_tr_b16 v[222:223], v31 offset:27136\n\t" \
  "v_cvt_pk_bf16_f32 v51, v254, v255\n\t" \
  "v_cvt_pk_bf16_f32 v50, v252, v253\n\t" \
  "v_mov_b32 v19, 15\n\t" \
  "ds_write_b64 v14, v[50:51] offset:3584\n\t" \
  "ds_write_b32 v18, v19 offset:32\n\t" \
  "s_cmp_lg_u32 s75, 0\n\t" \
  "s_cbranch_scc1 44f\n\t" \
  "s_waitcnt lgkmcnt(10)\n\t" \
  "v_readfirstlane_b32 vcc_lo, v16\n\t" \
  "s_nop 1\n\t" \
  "s_cmp_ge_u32 vcc_lo, 1\n\t" \
  "s_cbranch_scc0 46f\n\t" \
  "47:\n\t" \
  "44:\n\t" \
  "s_cmp_ge_u32 s75, 8\n\t" \
  "s_cbranch_scc1 45f\n\t" \
  "s_lshr_b64 s[90:91], %18, s75\n\t" \
  "s_and_b32 s90, s90, 15\n\t" \
  "s_lshl_b32 s90, s90, 15\n\t" \
  "s_add_u32 s90, s90, s92\n\t" \
  "s_addc_u32 s91, s93, 0\n\t" \
  "s_add_u32 s75, s75, 4\n\t" \
  "s_swappc_b64 s[68:69], s[70:71]\n\t" \
  "s_branch 44b\n\t" \
  "45:\n\t" \
  "s_waitcnt lgkmcnt(15)\n\t" \
  "s_waitcnt vmcnt(6)\n\t" \
  "v_mfma_f32_16x16x32_bf16 v[224:227], v[192:195], v[64:67], 0\n\t" \
  "v_mfma_f32_16x16x32_bf16 v[224:227], v[196:199], v[68:71], v[224:227]\n\t" \
  "s_lshr_b64 s[90:91], %18, 8\n\t" \
  "s_and_b32 s90, s90, 15\n\t" \
  "s_lshl_b32 s90, s90, 15\n\t" \
  "s_add_u32 s90, s90, s92\n\t" \
  "s_addc_u32 s91, s93, 0\n\t" \
  "global_load_dwordx4 v[96:99], v53, s[90:91] offset:0 sc1\n\t" \
  "s_waitcnt vmcnt(5)\n\t" \
  "v_mfma_f32_16x16x32_bf16 v[228:231], v[196:199], v[76:79], 0\n\t" \
  "v_mfma_f32_16x16x32_bf16 v[228:231], v[192:195], v[72:75], v[228:231]\n\t" \
  "global_load_dwordx4 v[100:103], v53, s[90:91] offset:1024 sc1\n\t" \
  "s_waitcnt lgkmcnt(14)\n\t" \
  "v_mfma_f32_16x16x32_bf16 v[232:235], v[200:203], v[64:67], 0\n\t" \
  "ds_read_b64_tr_b16 v[192:193], v28 offset:30720\n\t" \
  "s_waitcnt lgkmcnt(12)\n\t" \
  "v_mfma_f32_16x16x32_bf16 v[232:235], v[204:207], v[68:71], v[232:235]\n\t" \
  "ds_read_b64_tr_b16 v[194:195], v28 offset:31232\n\t" \
  "global_load_dwordx4 v[104:107], v53, s[90:91] offset:2048 sc1\n\t" \
  "v_mfma_f32_16x16x32_bf16 v[236:239], v[204:207], v[76:79], 0\n\t" \
  "ds_read_b64_tr_b16 v[196:197], v28 offset:34816\n\t" \
  "v_mfma_f32_16x16x32_bf16 v[236:239], v[200:203], v[72:75], v[236:239]\n\t" \
  "ds_read_b64_tr_b16 v[198:199], v28 offset:35328\n\t" \
  "global_load_dwordx4 v[108:111], v53, s[90:91] offset:3072 sc1\n\t" \
  "s_waitcnt lgkmcnt(6)\n\t" \
  "v_mfma_f32_16x16x32_bf16 v[240:243], v[208:211], v[64:67], 0\n\t" \
  "ds_read_b64_tr_b16 v[200:201], v29 offset:30720\n\t" \
  "v_mfma_f32_16x16x32_bf16 v[240:243], v[212:215], v[68:71], v[240:243]\n\t" \
  "ds_read_b64_tr_b16 v[202:203], v29 offset:31232\n\t" \
  "s_lshr_b64 s[90:91], %18, 12\n\t" \
  "s_and_b32 s90, s90, 15\n\t" \
  "s_lshl_b32 s90, s90, 15\n\t" \
  "s_add_u32 s90, s90, s92\n\t" \
  "s_addc_u32 s91, s93, 0\n\t" \
  "global_load_dwordx4 v[112:115], v53, s[90:91] offset:0 sc1\n\t" \
  "v_mfma_f32_16x16x32_bf16 v[244:247], v[212:215], v[76:79], 0\n\t" \
  "ds_read_b64_tr_b16 v[204:205], v29 offset:34816\n\t" \
  "v_mfma_f32_16x16x32_bf16 v[244:247], v[208:211], v[72:75], v[244:247]\n\t" \
  "ds_read_b64_tr_b16 v[206:207], v29 offset:35328\n\t" \
  "global_load_dwordx4 v[116:119], v53, s[90:91] offset:1024 sc1\n\t" \
  "v_mfma_f32_16x16x32_bf16 v[248:251], v[216:219], v[64:67], 0\n\t" \
  "ds_read_b64_tr_b16 v[208:209], v30 offset:30720\n\t" \
  "v_mfma_f32_16x16x32_bf16 v[248:251], v[220:223], v[68:71], v[248:251]\n\t" \
  "ds_read_b64_tr_b16 v[210:211], v30 offset:31232\n\t" \
  "global_load_dwordx4 v[120:123], v53, s[90:91] offset:2048 sc1\n\t" \
  "v_mfma_f32_16x16x32_bf16 v[252:255], v[220:223], v[76:79], 0\n\t" \
  "ds_read_b64_tr_b16 v[212:213], v30 offset:34816\n\t" \
  "v_mfma_f32_16x16x32_bf16 v[252:255], v[216:219], v[72:75], v[252:255]\n\t" \
  "ds_read_b64_tr_b16 v[214:215], v30 offset:35328\n\t" \
  "global_load_dwordx4 v[124:127], v53, s[90:91] offset:3072 sc1\n\t" \
  "s_waitcnt lgkmcnt(6)\n\t" \
  "s_waitcnt vmcnt(10)\n\t" \
  "v_mfma_f32_16x16x32_bf16 v[224:227], v[192:195], v[80:83], v[224:227]\n\t" \
  "ds_read_b64_tr_b16 v[216:217], v31 offset:30720\n\t" \
  "v_mfma_f32_16x16x32_bf16 v[224:227], v[196:199], v[84:87], v[224:227]\n\t" \
  "ds_read_b64_tr_b16 v[218:219], v31 offset:31232\n\t" \
  "s_waitcnt vmcnt(8)\n\t" \
  "v_mfma_f32_16x16x32_bf16 v[228:231], v[196:199], v[92:95], v[228:231]\n\t" \
  "ds_read_b64_tr_b16 v[220:221], v31 offset:34816\n\t" \
  "s_lshr_b64 s[90:91], %18, 16\n\t" \
  "s_and_b32 s90, s90, 15\n\t" \
  "s_lshl_b32 s90, s90, 15\n\t" \
  "s_add_u32 s90, s90, s92\n\t" \
  "s_addc_u32 s91, s93, 0\n\t" \
  "global_load_dwordx4 v[128:131], v53, s[90:91] offset:0 sc1\n\t" \
  "v_mfma_f32_16x16x32_bf16 v[228:231], v[192:195], v[88:91], v[228:231]\n\t" \
  "ds_read_b64_tr_b16 v[222:223], v31 offset:35328\n\t" \
  "v_mfma_f32_16x16x32_bf16 v[232:235], v[200:203], v[80:83], v[232:235]\n\t" \
  "ds_read_b64_tr_b16 v[192:193], v28 offset:38912\n\t" \
  "s_waitcnt lgkmcnt(6)\n\t" \
  "v_mfma_f32_16x16x32_bf16 v[232:235], v[204:207], v[84:87], v[232:235]\n\t" \
  "ds_read_b64_tr_b16 v[194:195], v28 offset:39424\n\t" \
  "ds_write_b32 v18, v17 offset:160\n\t" \
  "v_mfma_f32_16x16x32_bf16 v[236:239], v[204:207], v[92:95], v[236:239]\n\t" \
  "ds_read_b64_tr_b16 v[196:197], v28 offset:43008\n\t" \
  "global_load_dwordx4 v[132:135], v53, s[90:91] offset:1024 sc1\n\t" \
  "v_mfma_f32_16x16x32_bf16 v[236:239], v[200:203], v[88:91], v[236:239]\n\t" \
  "ds_read_b64_tr_b16 v[198:199], v28 offset:43520\n\t" \
  "v_mfma_f32_16x16x32_bf16 v[240:243], v[208:211], v[80:83], v[240:243]\n\t" \
  "ds_read_b64_tr_b16 v[200:201], v29 offset:38912\n\t" \
  "s_waitcnt lgkmcnt(7)\n\t" \
  "v_mfma_f32_16x16x32_bf16 v[240:243], v[212:215], v[84:87], v[240:243]\n\t" \
  "ds_read_b64_tr_b16 v[202:203], v29 offset:39424\n\t" \
  "v_mfma_f32_16x16x32_bf16 v[244:247], v[212:215], v[92:95], v[244:247]\n\t" \
  "ds_read_b64_tr_b16 v[204:205], v29 offset:43008\n\t" \
  "global_load_dwordx4 v[136:139], v53, s[90:91] offset:2048 sc1\n\t" \
  "v_mfma_f32_16x16x32_bf16 v[244:247], v[208:211], v[88:91], v[244:247]\n\t" \
  "ds_read_b64_tr_b16 v[206:207], v29 offset:43520\n\t" \
  "v_mfma_f32_16x16x32_bf16 v[248:251], v[216:219], v[80:83], v[248:251]\n\t" \
  "ds_read_b64_tr_b16 v[208:209], v30 offset:38912\n\t" \
  "s_waitcnt lgkmcnt(6)\n\t" \
  "v_mfma_f32_16x16x32_bf16 v[248:251], v[220:223], v[84:87], v[248:251]\n\t" \
  "ds_read_b64_tr_b16 v[210:211], v30 offset:39424\n\t" \
  "v_mfma_f32_16x16x32_bf16 v[252:255], v[220:223], v[92:95], v[252:255]\n\t" \
  "ds_read_b64_tr_b16 v[212:213], v30 offset:43008\n\t" \
  "global_load_dwordx4 v[140:143], v53, s[90:91] offset:3072 sc1\n\t" \
  "v_mfma_f32_16x16x32_bf16 v[252:255], v[216:219], v[88:91], v[252:255]\n\t" \
  "ds_read_b64_tr_b16 v[214:215], v30 offset:43520\n\t" \
  "s_waitcnt vmcnt(10)\n\t" \
  "v_mfma_f32_16x16x32_bf16 v[224:227], v[192:195], v[96:99], v[224:227]\n\t" \
  "ds_read_b64_tr_b16 v[216:217], v31 offset:38912\n\t" \
  "s_waitcnt lgkmcnt(6)\n\t" \
  "v_mfma_f32_16x16x32_bf16 v[224:227], v[196:199], v[100:103], v[224:227]\n\t" \
  "ds_read_b64_tr_b16 v[218:219], v31 offset:39424\n\t" \
  "s_waitcnt vmcnt(8)\n\t" \
  "v_mfma_f32_16x16x32_bf16 v[228:231], v[196:199], v[108:111], v[228:231]\n\t" \
  "ds_read_b64_tr_b16 v[220:221], v31 offset:43008\n\t" \
  "s_lshr_b64 s[90:91], %18, 20\n\t" \
  "s_and_b32 s90, s90, 15\n\t" \
  "s_lshl_b32 s90, s90, 15\n\t" \
  "s_add_u32 s90, s90, s92\n\t" \
  "s_addc_u32 s91, s93, 0\n\t" \
  "global_load_dwordx4 v[144:147], v53, s[90:91] offset:0 sc1\n\t" \
  "v_mfma_f32_16x16x32_bf16 v[228:231], v[192:195], v[104:107], v[228:231]\n\t" \
  "ds_read_b64_tr_b16 v[222:223], v31 offset:43520\n\t" \
  "v_mfma_f32_16x16x32_bf16 v[232:235], v[200:203], v[96:99], v[232:235]\n\t" \
  "ds_read_b64_tr_b16 v[192:193], v28 offset:47104\n\t" \
  "s_waitcnt lgkmcnt(6)\n\t" \
  "v_mfma_f32_16x16x32_bf16 v[232:235], v[204:207], v[100:103], v[232:235]\n\t" \
  "ds_read_b64_tr_b16 v[194:195], v28 offset:47616\n\t" \
  "v_mfma_f32_16x16x32_bf16 v[236:239], v[204:207], v[108:111], v[236:239]\n\t" \
  "ds_read_b64_tr_b16 v[196:197], v28 offset:51200\n\t" \
  "global_load_dwordx4 v[148:151], v53, s[90:91] offset:1024 sc1\n\t" \
  "v_mfma_f32_16x16x32_bf16 v[236:239], v[200:203], v[104:107], v[236:239]\n\t" \
  "ds_read_b64_tr_b16 v[198:199], v28 offset:51712\n\t" \
  "v_mfma_f32_16x16x32_bf16 v[240:243], v[208:211], v[96:99], v[240:243]\n\t" \
  "ds_read_b64_tr_b16 v[200:201], v29 offset:47104\n\t" \
  "s_waitcnt lgkmcnt(6)\n\t" \
  "v_mfma_f32_16x16x32_bf16 v[240:243], v[212:215], v[100:103], v[240:243]\n\t" \
  "ds_read_b64_tr_b16 v[202:203], v29 offset:47616\n\t" \
  "v_mfma_f32_16x16x32_bf16 v[244:247], v[212:215], v[108:111], v[244:247]\n\t" \
  "ds_read_b64_tr_b16 v[204:205], v29 offset:51200\n\t" \
  "global_load_dwordx4 v[152:155], v53, s[90:91] offset:2048 sc1\n\t" \
  "v_mfma_f32_16x16x32_bf16 v[244:247], v[208:211], v[104:107], v[244:247]\n\t" \
  "ds_read_b64_tr_b16 v[206:207], v29 offset:51712\n\t" \
  "v_mfma_f32_16x16x32_bf16 v[248:251], v[216:219], v[96:99], v[248:251]\n\t" \
  "ds_read_b64_tr_b16 v[208:209], v30 offset:47104\n\t" \
  "s_waitcnt lgkmcnt(6)\n\t" \
  "v_mfma_f32_16x16x32_bf16 v[248:251], v[220:223], v[100:103], v[248:251]\n\t" \
  "ds_read_b64_tr_b16 v[210:211], v30 offset:47616\n\t" \
  "v_mfma_f32_16x16x32_bf16 v[252:255], v[220:223], v[108:111], v[252:255]\n\t" \
  "ds_read_b64_tr_b16 v[212:213], v30 offset:51200\n\t" \
  "global_load_dwordx4 v[156:159], v53, s[90:91] offset:3072 sc1\n\t" \
  "v_mfma_f32_16x16x32_bf16 v[252:255], v[216:219], v[104:107], v[252:255]\n\t" \
  "ds_read_b64_tr_b16 v[214:215], v30 offset:51712\n\t" \
  "s_waitcnt vmcnt(10)\n\t" \
  "v_mfma_f32_16x16x32_bf16 v[224:227], v[192:195], v[112:115], v[224:227]\n\t" \
  "ds_read_b64_tr_b16 v[216:217], v31 offset:47104\n\t" \
  "s_waitcnt lgkmcnt(6)\n\t" \
  "v_mfma_f32_16x16x32_bf16 v[224:227], v[196:199], v[116:119], v[224:227]\n\t" \
  "ds_read_b64_tr_b16 v[218:219], v31 offset:47616\n\t" \
  "s_waitcnt vmcnt(8)\n\t" \
  "v_mfma_f32_16x16x32_bf16 v[228:231], v[196:199], v[124:127], v[228:231]\n\t" \
  "ds_read_b64_tr_b16 v[220:221], v31 offset:51200\n\t" \
  "s_lshr_b64 s[90:91], %18, 24\n\t" \
  "s_and_b32 s90, s90, 15\n\t" \
  "s_lshl_b32 s90, s90, 15\n\t" \
  "s_add_u32 s90, s90, s92\n\t" \
  "s_addc_u32 s91, s93, 0\n\t" \
  "global_load_dwordx4 v[160:163], v53, s[90:91] offset:0 sc1\n\t" \
  "v_mfma_f32_16x16x32_bf16 v[228:231], v[192:195], v[120:123], v[228:231]\n\t" \
  "ds_read_b64_tr_b16 v[222:223], v31 offset:51712\n\t" \
  "v_mfma_f32_16x16x32_bf16 v[232:235], v[200:203], v[112:115], v[232:235]\n\t" \
  "ds_read_b64_tr_b16 v[192:193], v28 offset:55296\n\t" \
  "s_waitcnt lgkmcnt(6)\n\t" \
  "v_mfma_f32_16x16x32_bf16 v[232:235], v[204:207], v[116:119], v[232:235]\n\t" \
  "ds_read_b64_tr_b16 v[194:195], v28 offset:55808\n\t" \
  "v_mfma_f32_16x16x32_bf16 v[236:239], v[204:207], v[124:127], v[236:239]\n\t" \
  "ds_read_b64_tr_b16 v[196:197], v28 offset:59392\n\t" \
  "global_load_dwordx4 v[164:167], v53, s[90:91] offset:1024 sc1\n\t" \
  "ds_read_b32 v16, v56 offset:96\n\t" \
  "v_mfma_f32_16x16x32_bf16 v[236:239], v[200:203], v[120:123], v[236:239]\n\t" \
  "ds_read_b64_tr_b16 v[198:199], v28 offset:59904\n\t" \
  "v_mfma_f32_16x16x32_bf16 v[240:243], v[208:211], v[112:115], v[240:243]\n\t" \
  "ds_read_b64_tr_b16 v[200:201], v29 offset:55296\n\t" \
  "s_waitcnt lgkmcnt(7)\n\t" \
  "v_mfma_f32_16x16x32_bf16 v[240:243], v[212:215], v[116:119], v[240:243]\n\t" \
  "ds_read_b64_tr_b16 v[202:203], v29 offset:55808\n\t" \
  "v_mfma_f32_16x16x32_bf16 v[244:247], v[212:215], v[124:127], v[244:247]\n\t" \
  "ds_read_b64_tr_b16 v[204:205], v29 offset:59392\n\t" \
  "global_load_dwordx4 v[168:171], v53, s[90:91] offset:2048 sc1\n\t" \
  "v_mfma_f32_16x16x32_bf16 v[244:247], v[208:211], v[120:123], v[244:247]\n\t" \
  "ds_read_b64_tr_b16 v[206:207], v29 offset:59904\n\t" \
  "v_mfma_f32_16x16x32_bf16 v[248:251], v[216:219], v[112:115], v[248:251]\n\t" \
  "ds_read_b64_tr_b16 v[208:209], v30 offset:55296\n\t" \
  "s_waitcnt lgkmcnt(6)\n\t" \
  "v_mfma_f32_16x16x32_bf16 v[248:251], v[220:223], v[116:119], v[248:251]\n\t" \
  "ds_read_b64_tr_b16 v[210:211], v30 offset:55808\n\t" \
  "v_mfma_f32_16x16x32_bf16 v[252:255], v[220:223], v[124:127], v[252:255]\n\t" \
  "ds_read_b64_tr_b16 v[212:213], v30 offset:59392\n\t" \
  "global_load_dwordx4 v[172:175], v53, s[90:91] offset:3072 sc1\n\t" \
  "v_readfirstlane_b32 vcc_lo, v16\n\t" \
  "v_mfma_f32_16x16x32_bf16 v[252:255], v[216:219], v[120:123], v[252:255]\n\t" \
  "ds_read_b64_tr_b16 v[214:215], v30 offset:59904\n\t" \
  "s_cmp_ge_u32 vcc_lo, 2\n\t" \
  "s_cbranch_scc0 49f\n\t" \
  "50:\n\t" \
  "s_waitcnt vmcnt(10)\n\t" \
  "v_mfma_f32_16x16x32_bf16 v[224:227], v[192:195], v[128:131], v[224:227]\n\t" \
  "ds_read_b64_tr_b16 v[216:217], v31 offset:55296\n\t" \
  "s_waitcnt lgkmcnt(6)\n\t" \
  "v_mfma_f32_16x16x32_bf16 v[224:227], v[196:199], v[132:135], v[224:227]\n\t" \
  "ds_read_b64_tr_b16 v[218:219], v31 offset:55808\n\t" \
  "s_waitcnt vmcnt(8)\n\t" \
  "v_mfma_f32_16x16x32_bf16 v[228:231], v[196:199], v[140:143], v[228:231]\n\t" \
  "ds_read_b64_tr_b16 v[220:221], v31 offset:59392\n\t" \
  "s_lshr_b64 s[90:91], %18, 28\n\t" \
  "s_and_b32 s90, s90, 15\n\t" \
  "s_lshl_b32 s90, s90, 15\n\t" \
  "s_add_u32 s90, s90, s92\n\t" \
  "s_addc_u32 s91, s93, 0\n\t" \
  "global_load_dwordx4 v[176:179], v53, s[90:91] offset:0 sc1\n\t" \
  "v_mfma_f32_16x16x32_bf16 v[228:231], v[192:195], v[136:139], v[228:231]\n\t" \
  "ds_read_b64_tr_b16 v[222:223], v31 offset:59904\n\t" \
  "v_mfma_f32_16x16x32_bf16 v[232:235], v[200:203], v[128:131], v[232:235]\n\t" \
  "ds_read_b64_tr_b16 v[192:193], v28 offset:63488\n\t" \
  "s_waitcnt lgkmcnt(6)\n\t" \
  "v_mfma_f32_16x16x32_bf16 v[232:235], v[204:207], v[132:135], v[232:235]\n\t" \
  "ds_read_b64_tr_b16 v[194:195], v28 offset:64000\n\t" \
  "v_mfma_f32_16x16x32_bf16 v[236:239], v[204:207], v[140:143], v[236:239]\n\t" \
  "ds_read_b64_tr_b16 v[196:197], v32 offset:2048\n\t" \
  "global_load_dwordx4 v[180:183], v53, s[90:91] offset:1024 sc1\n\t" \
  "v_mfma_f32_16x16x32_bf16 v[236:239], v[200:203], v[136:139], v[236:239]\n\t" \
  "ds_read_b64_tr_b16 v[198:199], v32 offset:2560\n\t" \
  "v_mfma_f32_16x16x32_bf16 v[240:243], v[208:211], v[128:131], v[240:243]\n\t" \
  "ds_read_b64_tr_b16 v[200:201], v29 offset:63488\n\t" \
  "s_waitcnt lgkmcnt(6)\n\t" \
  "v_mfma_f32_16x16x32_bf16 v[240:243], v[212:215], v[132:135], v[240:243]\n\t" \
  "ds_read_b64_tr_b16 v[202:203], v29 offset:64000\n\t" \
  "v_mfma_f32_16x16x32_bf16 v[244:247], v[212:215], v[140:143], v[244:247]\n\t" \
  "ds_read_b64_tr_b16 v[204:205], v33 offset:2048\n\t" \
  "global_load_dwordx4 v[184:187], v53, s[90:91] offset:2048 sc1\n\t" \
  "v_mfma_f32_16x16x32_bf16 v[244:247], v[208:211], v[136:139], v[244:247]\n\t" \
  "ds_read_b64_tr_b16 v[206:207], v33 offset:2560\n\t" \
  "v_mfma_f32_16x16x32_bf16 v[248:251], v[216:219], v[128:131], v[248:251]\n\t" \
  "ds_read_b64_tr_b16 v[208:209], v30 offset:63488\n\t" \
  "s_waitcnt lgkmcnt(6)\n\t" \
  "v_mfma_f32_16x16x32_bf16 v[248:251], v[220:223], v[132:135], v[248:251]\n\t" \
  "ds_read_b64_tr_b16 v[210:211], v30 offset:64000\n\t" \
  "v_mfma_f32_16x16x32_bf16 v[252:255], v[220:223], v[140:143], v[252:255]\n\t" \
  "ds_read_b64_tr_b16 v[212:213], v34 offset:2048\n\t" \
  "global_load_dwordx4 v[188:191], v53, s[90:91] offset:3072 sc1\n\t" \
  "v_mfma_f32_16x16x32_bf16 v[252:255], v[216:219], v[136:139], v[252:255]\n\t" \
  "ds_read_b64_tr_b16 v[214:215], v34 offset:2560\n\t" \
  "s_waitcnt vmcnt(10)\n\t" \
  "v_mfma_f32_16x16x32_bf16 v[224:227], v[192:195], v[144:147], v[224:227]\n\t" \
  "ds_read_b64_tr_b16 v[216:217], v31 offset:63488\n\t" \
  "s_waitcnt lgkmcnt(6)\n\t" \
  "v_mfma_f32_16x16x32_bf16 v[224:227], v[196:199], v[148:151], v[224:227]\n\t" \
  "ds_read_b64_tr_b16 v[218:219], v31 offset:64000\n\t" \
  "s_waitcnt vmcnt(8)\n\t" \
  "v_mfma_f32_16x16x32_bf16 v[228:231], v[196:199], v[156:159], v[228:231]\n\t" \
  "ds_read_b64_tr_b16 v[220:221], v35 offset:2048\n\t" \
  "s_lshr_b64 s[90:91], %18, 32\n\t" \
  "s_and_b32 s90, s90, 15\n\t" \
  "s_lshl_b32 s90, s90, 15\n\t" \
  "s_add_u32 s90, s90, s92\n\t" \
  "s_addc_u32 s91, s93, 0\n\t" \
  "global_load_dwordx4 v[64:67], v53, s[90:91] offset:0 sc1\n\t" \
  "v_mfma_f32_16x16x32_bf16 v[228:231], v[192:195], v[152:155], v[228:231]\n\t" \
  "ds_read_b64_tr_b16 v[222:223], v35 offset:2560\n\t" \
  "v_mfma_f32_16x16x32_bf16 v[232:235], v[200:203], v[144:147], v[232:235]\n\t" \
  "ds_read_b64_tr_b16 v[192:193], v32 offset:6144\n\t" \
  "s_waitcnt lgkmcnt(6)\n\t" \
  "v_mfma_f32_16x16x32_bf16 v[232:235], v[204:207], v[148:151], v[232:235]\n\t" \
  "ds_read_b64_tr_b16 v[194:195], v32 offset:6656\n\t" \
  "v_mfma_f32_16x16x32_bf16 v[236:239], v[204:207], v[156:159], v[236:239]\n\t" \
  "ds_read_b64_tr_b16 v[196:197], v32 offset:10240\n\t" \
  "global_load_dwordx4 v[68:71], v53, s[90:91] offset:1024 sc1\n\t" \
  "v_mfma_f32_16x16x32_bf16 v[236:239], v[200:203], v[152:155], v[236:239]\n\t" \
  "ds_read_b64_tr_b16 v[198:199], v32 offset:10752\n\t" \
  "v_mfma_f32_16x16x32_bf16 v[240:243], v[208:211], v[144:147], v[240:243]\n\t" \
  "ds_read_b64_tr_b16 v[200:201], v33 offset:6144\n\t" \
  "s_waitcnt lgkmcnt(6)\n\t" \
  "v_mfma_f32_16x16x32_bf16 v[240:243], v[212:215], v[148:151], v[240:243]\n\t" \
  "ds_read_b128 v[40:43], v56 offset:80\n\t" \
  "ds_read_b64_tr_b16 v[202:203], v33 offset:6656\n\t" \
  "v_mfma_f32_16x16x32_bf16 v[244:247], v[212:215], v[156:159], v[244:247]\n\t" \
  "ds_read_b64_tr_b16 v[204:205], v33 offset:10240\n\t" \
  "global_load_dwordx4 v[72:75], v53, s[90:91] offset:2048 sc1\n\t" \
  "v_mfma_f32_16x16x32_bf16 v[244:247], v[208:211], v[152:155], v[244:247]\n\t" \
  "ds_read_b64_tr_b16 v[206:207], v33 offset:10752\n\t" \
  "v_mfma_f32_16x16x32_bf16 v[248:251], v[216:219], v[144:147], v[248:251]\n\t" \
  "ds_read_b64_tr_b16 v[208:209], v34 offset:6144\n\t" \
  "s_waitcnt lgkmcnt(7)\n\t" \
  "v_mfma_f32_16x16x32_bf16 v[248:251], v[220:223], v[148:151], v[248:251]\n\t" \
  "ds_read_b64_tr_b16 v[210:211], v34 offset:6656\n\t" \
  "v_mfma_f32_16x16x32_bf16 v[252:255], v[220:223], v[156:159], v[252:255]\n\t" \
  "ds_read_b64_tr_b16 v[212:213], v34 offset:10240\n\t" \
  "global_load_dwordx4 v[76:79], v53, s[90:91] offset:3072 sc1\n\t" \
  "v_mfma_f32_16x16x32_bf16 v[252:255], v[216:219], v[152:155], v[252:255]\n\t" \
  "ds_read_b64_tr_b16 v[214:215], v34 offset:10752\n\t" \
  "s_waitcnt vmcnt(10)\n\t" \
  "v_mfma_f32_16x16x32_bf16 v[224:227], v[192:195], v[160:163], v[224:227]\n\t" \
  "s_waitcnt lgkmcnt(7)\n\t" \
  "v_min_u32 v40, v40, v41\n\t" \
  "v_min3_u32 v40, v40, v42, v43\n\t" \
  "ds_read_b64_tr_b16 v[216:217], v35 offset:6144\n\t" \
  "v_mfma_f32_16x16x32_bf16 v[224:227], v[196:199], v[164:167], v[224:227]\n\t" \
  "v_readfirstlane_b32 vcc_lo, v40\n\t" \
  "ds_read_b64_tr_b16 v[218:219], v35 offset:6656\n\t" \
  "s_waitcnt vmcnt(8)\n\t" \
  "v_mfma_f32_16x16x32_bf16 v[228:231], v[196:199], v[172:175], v[228:231]\n\t" \
  "s_cmp_ge_u32 vcc_lo, 13\n\t" \
  "s_cbranch_scc0 38f\n\t" \
  "39:\n\t" \
  "ds_read_b64_tr_b16 v[220:221], v35 offset:10240\n\t" \
  "s_lshr_b64 s[90:91], %18, 36\n\t" \
  "s_and_b32 s90, s90, 15\n\t" \
  "s_lshl_b32 s90, s90, 15\n\t" \
  "s_add_u32 s90, s90, s92\n\t" \
  "s_addc_u32 s91, s93, 0\n\t" \
  "global_load_dwordx4 v[80:83], v53, s[90:91] offset:0 sc1\n\t" \
  "v_mfma_f32_16x16x32_bf16 v[228:231], v[192:195], v[168:171], v[228:231]\n\t" \
  "ds_read_b64_tr_b16 v[222:223], v35 offset:10752\n\t" \
  "s_waitcnt lgkmcnt(6)\n\t" \
  "v_mfma_f32_16x16x32_bf16 v[232:235], v[200:203], v[160:163], v[232:235]\n\t" \
  "ds_read_b64_tr_b16 v[192:193], v32 offset:14336\n\t" \
  "v_mfma_f32_16x16x32_bf16 v[232:235], v[204:207], v[164:167], v[232:235]\n\t" \
  "ds_read_b64_tr_b16 v[194:195], v32 offset:14848\n\t" \
  "v_mfma_f32_16x16x32_bf16 v[236:239], v[204:207], v[172:175], v[236:239]\n\t" \
  "ds_read_b64_tr_b16 v[196:197], v32 offset:18432\n\t" \
  "global_load_dwordx4 v[84:87], v53, s[90:91] offset:1024 sc1\n\t" \
  "v_mfma_f32_16x16x32_bf16 v[236:239], v[200:203], v[168:171], v[236:239]\n\t" \
  "ds_read_b64_tr_b16 v[198:199], v32 offset:18944\n\t" \
  "v_mfma_f32_16x16x32_bf16 v[240:243], v[208:211], v[160:163], v[240:243]\n\t" \
  "ds_read_b64_tr_b16 v[200:201], v33 offset:14336\n\t" \
  "s_waitcnt lgkmcnt(6)\n\t" \
  "v_mfma_f32_16x16x32_bf16 v[240:243], v[212:215], v[164:167], v[240:243]\n\t" \
  "ds_read_b64_tr_b16 v[202:203], v33 offset:14848\n\t" \
  "v_mfma_f32_16x16x32_bf16 v[244:247], v[212:215], v[172:175], v[244:247]\n\t" \
  "ds_read_b64_tr_b16 v[204:205], v33 offset:18432\n\t" \
  "global_load_dwordx4 v[88:91], v53, s[90:91] offset:2048 sc1\n\t" \
  "v_mfma_f32_16x16x32_bf16 v[244:247], v[208:211], v[168:171], v[244:247]\n\t" \
  "ds_read_b64_tr_b16 v[206:207], v33 offset:18944\n\t" \
  "v_mfma_f32_16x16x32_bf16 v[248:251], v[216:219], v[160:163], v[248:251]\n\t" \
  "ds_read_b64_tr_b16 v[208:209], v34 offset:14336\n\t" \
  "s_waitcnt lgkmcnt(6)\n\t" \
  "v_mfma_f32_16x16x32_bf16 v[248:251], v[220:223], v[164:167], v[248:251]\n\t" \
  "ds_read_b64_tr_b16 v[210:211], v34 offset:14848\n\t" \
  "v_mfma_f32_16x16x32_bf16 v[252:255], v[220:223], v[172:175], v[252:255]\n\t" \
  "ds_read_b64_tr_b16 v[212:213], v34 offset:18432\n\t" \
  "global_load_dwordx4 v[92:95], v53, s[90:91] offset:3072 sc1\n\t" \
  "v_mfma_f32_16x16x32_bf16 v[252:255], v[216:219], v[168:171], v[252:255]\n\t" \
  "ds_read_b64_tr_b16 v[214:215], v34 offset:18944\n\t" \
  "s_waitcnt vmcnt(10)\n\t" \
  "v_mfma_f32_16x16x32_bf16 v[224:227], v[192:195], v[176:179], v[224:227]\n\t" \
  "ds_read_b64_tr_b16 v[216:217], v35 offset:14336\n\t" \
  "s_waitcnt lgkmcnt(6)\n\t" \
  "v_mfma_f32_16x16x32_bf16 v[224:227], v[196:199], v[180:183], v[224:227]\n\t" \
  "ds_read_b64_tr_b16 v[218:219], v35 offset:14848\n\t" \
  "s_waitcnt vmcnt(8)\n\t" \
  "v_mfma_f32_16x16x32_bf16 v[228:231], v[196:199], v[188:191], v[228:231]\n\t" \
  "ds_read_b64_tr_b16 v[220:221], v35 offset:18432\n\t" \
  "s_lshr_b64 s[90:91], %18, 40\n\t" \
  "s_and_b32 s90, s90, 15\n\t" \
  "s_lshl_b32 s90, s90, 15\n\t" \
  "s_add_u32 s90, s90, s92\n\t" \
  "s_addc_u32 s91, s93, 0\n\t" \
  "global_load_dwordx4 v[96:99], v53, s[90:91] offset:0 sc1\n\t" \
  "v_mfma_f32_16x16x32_bf16 v[228:231], v[192:195], v[184:187], v[228:231]\n\t" \
  "ds_read_b64_tr_b16 v[222:223], v35 offset:18944\n\t" \
  "v_mfma_f32_16x16x32_bf16 v[232:235], v[200:203], v[176:179], v[232:235]\n\t" \
  "ds_read_b64_tr_b16 v[192:193], v32 offset:22528\n\t" \
  "s_waitcnt lgkmcnt(6)\n\t" \
  "v_mfma_f32_16x16x32_bf16 v[232:235], v[204:207], v[180:183], v[232:235]\n\t" \
  "ds_read_b64_tr_b16 v[194:195], v32 offset:23040\n\t" \
  "v_mfma_f32_16x16x32_bf16 v[236:239], v[204:207], v[188:191], v[236:239]\n\t" \
  "ds_read_b64_tr_b16 v[196:197], v32 offset:26624\n\t" \
  "global_load_dwordx4 v[100:103], v53, s[90:91] offset:1024 sc1\n\t" \
  "v_mfma_f32_16x16x32_bf16 v[236:239], v[200:203], v[184:187], v[236:239]\n\t" \
  "ds_read_b64_tr_b16 v[198:199], v32 offset:27136\n\t" \
  "v_mfma_f32_16x16x32_bf16 v[240:243], v[208:211], v[176:179], v[240:243]\n\t" \
  "ds_read_b64_tr_b16 v[200:201], v33 offset:22528\n\t" \
  "s_waitcnt lgkmcnt(6)\n\t" \
  "v_mfma_f32_16x16x32_bf16 v[240:243], v[212:215], v[180:183], v[240:243]\n\t" \
  "ds_read_b64_tr_b16 v[202:203], v33 offset:23040\n\t" \
  "v_mfma_f32_16x16x32_bf16 v[244:247], v[212:215], v[188:191], v[244:247]\n\t" \
  "ds_read_b64_tr_b16 v[204:205], v33 offset:26624\n\t" \
  "global_load_dwordx4 v[104:107], v53, s[90:91] offset:2048 sc1\n\t" \
  "v_mfma_f32_16x16x32_bf16 v[244:247], v[208:211], v[184:187], v[244:247]\n\t" \
  "ds_read_b64_tr_b16 v[206:207], v33 offset:27136\n\t" \
  "v_mfma_f32_16x16x32_bf16 v[248:251], v[216:219], v[176:179], v[248:251]\n\t" \
  "ds_read_b64_tr_b16 v[208:209], v34 offset:22528\n\t" \
  "s_waitcnt lgkmcnt(6)\n\t" \
  "v_mfma_f32_16x16x32_bf16 v[248:251], v[220:223], v[180:183], v[248:251]\n\t" \
  "ds_read_b64_tr_b16 v[210:211], v34 offset:23040\n\t" \
  "v_mfma_f32_16x16x32_bf16 v[252:255], v[220:223], v[188:191], v[252:255]\n\t" \
  "ds_read_b64_tr_b16 v[212:213], v34 offset:26624\n\t" \
  "global_load_dwordx4 v[108:111], v53, s[90:91] offset:3072 sc1\n\t" \
  "ds_write_b32 v56, v56 offset:108\n\t" \
  "v_mfma_f32_16x16x32_bf16 v[252:255], v[216:219], v[184:187], v[252:255]\n\t" \
  "ds_read_b64_tr_b16 v[214:215], v34 offset:27136\n\t" \
  "s_waitcnt vmcnt(10)\n\t" \
  "v_mfma_f32_16x16x32_bf16 v[224:227], v[192:195], v[64:67], v[224:227]\n\t" \
  "ds_read_b64_tr_b16 v[216:217], v35 offset:22528\n\t" \
  "s_waitcnt lgkmcnt(7)\n\t" \
  "v_mfma_f32_16x16x32_bf16 v[224:227], v[196:199], v[68:71], v[224:227]\n\t" \
  "ds_read_b64_tr_b16 v[218:219], v35 offset:23040\n\t" \
  "s_waitcnt vmcnt(8)\n\t" \
  "v_mfma_f32_16x16x32_bf16 v[228:231], v[196:199], v[76:79], v[228:231]\n\t" \
  "ds_read_b64_tr_b16 v[220:221], v35 offset:26624\n\t" \
  "s_lshr_b64 s[90:91], %18, 44\n\t" \
  "s_and_b32 s90, s90, 15\n\t" \
  "s_lshl_b32 s90, s90, 15\n\t" \
  "s_add_u32 s90, s90, s92\n\t" \
  "s_addc_u32 s91, s93, 0\n\t" \
  "global_load_dwordx4 v[112:115], v53, s[90:91] offset:0 sc1\n\t" \
  "v_mfma_f32_16x16x32_bf16 v[228:231], v[192:195], v[72:75], v[228:231]\n\t" \
  "ds_read_b64_tr_b16 v[222:223], v35 offset:27136\n\t" \
  "v_mfma_f32_16x16x32_bf16 v[232:235], v[200:203], v[64:67], v[232:235]\n\t" \
  "ds_read_b64_tr_b16 v[192:193], v32 offset:30720\n\t" \
  "s_waitcnt lgkmcnt(6)\n\t" \
  "v_mfma_f32_16x16x32_bf16 v[232:235], v[204:207], v[68:71], v[232:235]\n\t" \
  "ds_read_b64_tr_b16 v[194:195], v32 offset:31232\n\t" \
  "v_mfma_f32_16x16x32_bf16 v[236:239], v[204:207], v[76:79], v[236:239]\n\t" \
  "ds_read_b64_tr_b16 v[196:197], v32 offset:34816\n\t" \
  "global_load_dwordx4 v[116:119], v53, s[90:91] offset:1024 sc1\n\t" \
  "v_mfma_f32_16x16x32_bf16 v[236:239], v[200:203], v[72:75], v[236:239]\n\t" \
  "ds_read_b64_tr_b16 v[198:199], v32 offset:35328\n\t" \
  "v_mfma_f32_16x16x32_bf16 v[240:243], v[208:211], v[64:67], v[240:243]\n\t" \
  "ds_read_b64_tr_b16 v[200:201], v33 offset:30720\n\t" \
  "s_waitcnt lgkmcnt(6)\n\t" \
  "v_mfma_f32_16x16x32_bf16 v[240:243], v[212:215], v[68:71], v[240:243]\n\t" \
  "ds_read_b64_tr_b16 v[202:203], v33 offset:31232\n\t" \
  "v_mfma_f32_16x16x32_bf16 v[244:247], v[212:215], v[76:79], v[244:247]\n\t" \
  "ds_read_b64_tr_b16 v[204:205], v33 offset:34816\n\t" \
  "global_load_dwordx4 v[120:123], v53, s[90:91] offset:2048 sc1\n\t" \
  "v_mfma_f32_16x16x32_bf16 v[244:247], v[208:211], v[72:75], v[244:247]\n\t" \
  "ds_read_b64_tr_b16 v[206:207], v33 offset:35328\n\t" \
  "v_mfma_f32_16x16x32_bf16 v[248:251], v[216:219], v[64:67], v[248:251]\n\t" \
  "ds_read_b64_tr_b16 v[208:209], v34 offset:30720\n\t" \
  "s_waitcnt lgkmcnt(6)\n\t" \
  "v_mfma_f32_16x16x32_bf16 v[248:251], v[220:223], v[68:71], v[248:251]\n\t" \
  "ds_read_b64_tr_b16 v[210:211], v34 offset:31232\n\t" \
  "v_mfma_f32_16x16x32_bf16 v[252:255], v[220:223], v[76:79], v[252:255]\n\t" \
  "ds_read_b64_tr_b16 v[212:213], v34 offset:34816\n\t" \
  "global_load_dwordx4 v[124:127], v53, s[90:91] offset:3072 sc1\n\t" \
  "v_mfma_f32_16x16x32_bf16 v[252:255], v[216:219], v[72:75], v[252:255]\n\t" \
  "ds_read_b64_tr_b16 v[214:215], v34 offset:35328\n\t" \
  "s_waitcnt vmcnt(10)\n\t" \
  "v_mfma_f32_16x16x32_bf16 v[224:227], v[192:195], v[80:83], v[224:227]\n\t" \
  "ds_read_b64_tr_b16 v[216:217], v35 offset:30720\n\t" \
  "s_waitcnt lgkmcnt(6)\n\t" \
  "v_mfma_f32_16x16x32_bf16 v[224:227], v[196:199], v[84:87], v[224:227]\n\t" \
  "ds_read_b64_tr_b16 v[218:219], v35 offset:31232\n\t" \
  "s_waitcnt vmcnt(8)\n\t" \
  "v_mfma_f32_16x16x32_bf16 v[228:231], v[196:199], v[92:95], v[228:231]\n\t" \
  "ds_read_b64_tr_b16 v[220:221], v35 offset:34816\n\t" \
  "s_lshr_b64 s[90:91], %18, 48\n\t" \
  "s_and_b32 s90, s90, 15\n\t" \
  "s_lshl_b32 s90, s90, 15\n\t" \
  "s_add_u32 s90, s90, s92\n\t" \
  "s_addc_u32 s91, s93, 0\n\t" \
  "global_load_dwordx4 v[128:131], v53, s[90:91] offset:0 sc1\n\t" \
  "v_mfma_f32_16x16x32_bf16 v[228:231], v[192:195], v[88:91], v[228:231]\n\t" \
  "ds_read_b64_tr_b16 v[222:223], v35 offset:35328\n\t" \
  "v_mfma_f32_16x16x32_bf16 v[232:235], v[200:203], v[80:83], v[232:235]\n\t" \
  "ds_read_b64_tr_b16 v[192:193], v32 offset:38912\n\t" \
  "s_waitcnt lgkmcnt(6)\n\t" \
  "v_mfma_f32_16x16x32_bf16 v[232:235], v[204:207], v[84:87], v[232:235]\n\t" \
  "ds_read_b64_tr_b16 v[194:195], v32 offset:39424\n\t" \
  "v_mfma_f32_16x16x32_bf16 v[236:239], v[204:207], v[92:95], v[236:239]\n\t" \
  "ds_read_b64_tr_b16 v[196:197], v32 offset:43008\n\t" \
  "global_load_dwordx4 v[132:135], v53, s[90:91] offset:1024 sc1\n\t" \
  "v_mfma_f32_16x16x32_bf16 v[236:239], v[200:203], v[88:91], v[236:239]\n\t" \
  "ds_read_b64_tr_b16 v[198:199], v32 offset:43520\n\t" \
  "v_mfma_f32_16x16x32_bf16 v[240:243], v[208:211], v[80:83], v[240:243]\n\t" \
  "ds_read_b64_tr_b16 v[200:201], v33 offset:38912\n\t" \
  "s_waitcnt lgkmcnt(6)\n\t" \
  "509:\n\t" \
  "s_cbranch_execz 26b\n\t" \
  "v_mfma_f32_16x16x32_bf16 v[240:243], v[212:215], v[84:87], v[240:243]\n\t" \
  "ds_read_b64_tr_b16 v[202:203], v33 offset:39424\n\t" \
  "v_mfma_f32_16x16x32_bf16 v[244:247], v[212:215], v[92:95], v[244:247]\n\t" \
  "ds_read_b64_tr_b16 v[204:205], v33 offset:43008\n\t" \
  "global_load_dwordx4 v[136:139], v53, s[90:91] offset:2048 sc1\n\t" \
  "v_mfma_f32_16x16x32_bf16 v[244:247], v[208:211], v[88:91], v[244:247]\n\t" \
  "ds_read_b64_tr_b16 v[206:207], v33 offset:43520\n\t" \
  "v_mfma_f32_16x16x32_bf16 v[248:251], v[216:219], v[80:83], v[248:251]\n\t" \
  "ds_read_b64_tr_b16 v[208:209], v34 offset:38912\n\t" \
  "s_waitcnt lgkmcnt(6)\n\t" \
  "v_mfma_f32_16x16x32_bf16 v[248:251], v[220:223], v[84:87], v[248:251]\n\t" \
  "ds_read_b64_tr_b16 v[210:211], v34 offset:39424\n\t" \
  "v_mfma_f32_16x16x32_bf16 v[252:255], v[220:223], v[92:95], v[252:255]\n\t" \
  "ds_read_b64_tr_b16 v[212:213], v34 offset:43008\n\t" \
  "global_load_dwordx4 v[140:143], v53, s[90:91] offset:3072 sc1\n\t" \
  "v_mfma_f32_16x16x32_bf16 v[252:255], v[216:219], v[88:91], v[252:255]\n\t" \
  "ds_read_b64_tr_b16 v[214:215], v34 offset:43520\n\t" \
  "s_waitcnt vmcnt(10)\n\t" \
  "v_mfma_f32_16x16x32_bf16 v[224:227], v[192:195], v[96:99], v[224:227]\n\t" \
  "ds_read_b64_tr_b16 v[216:217], v35 offset:38912\n\t" \
  "s_waitcnt lgkmcnt(6)\n\t" \
  "v_mfma_f32_16x16x32_bf16 v[224:227], v[196:199], v[100:103], v[224:227]\n\t" \
  "ds_read_b64_tr_b16 v[218:219], v35 offset:39424\n\t" \
  "s_waitcnt vmcnt(8)\n\t" \
  "v_mfma_f32_16x16x32_bf16 v[228:231], v[196:199], v[108:111], v[228:231]\n\t" \
  "ds_read_b64_tr_b16 v[220:221], v35 offset:43008\n\t" \
  "s_lshr_b64 s[90:91], %18, 52\n\t" \
  "s_and_b32 s90, s90, 15\n\t" \
  "s_lshl_b32 s90, s90, 15\n\t" \
  "s_add_u32 s90, s90, s92\n\t" \
  "s_addc_u32 s91, s93, 0\n\t" \
  "global_load_dwordx4 v[144:147], v53, s[90:91] offset:0 sc1\n\t" \
  "v_mfma_f32_16x16x32_bf16 v[228:231], v[192:195], v[104:107], v[228:231]\n\t" \
  "ds_read_b64_tr_b16 v[222:223], v35 offset:43520\n\t" \
  "v_mfma_f32_16x16x32_bf16 v[232:235], v[200:203], v[96:99], v[232:235]\n\t" \
  "ds_read_b64_tr_b16 v[192:193], v32 offset:47104\n\t" \
  "s_waitcnt lgkmcnt(6)\n\t" \
  "v_mfma_f32_16x16x32_bf16 v[232:235], v[204:207], v[100:103], v[232:235]\n\t" \
  "ds_read_b64_tr_b16 v[194:195], v32 offset:47616\n\t" \
  "v_mfma_f32_16x16x32_bf16 v[236:239], v[204:207], v[108:111], v[236:239]\n\t" \
  "ds_read_b64_tr_b16 v[196:197], v32 offset:51200\n\t" \
  "global_load_dwordx4 v[148:151], v53, s[90:91] offset:1024 sc1\n\t" \
  "v_mfma_f32_16x16x32_bf16 v[236:239], v[200:203], v[104:107], v[236:239]\n\t" \
  "ds_read_b64_tr_b16 v[198:199], v32 offset:51712\n\t" \
  "v_mfma_f32_16x16x32_bf16 v[240:243], v[208:211], v[96:99], v[240:243]\n\t" \
  "ds_read_b64_tr_b16 v[200:201], v33 offset:47104\n\t" \
  "s_waitcnt lgkmcnt(6)\n\t" \
  "v_mfma_f32_16x16x32_bf16 v[240:243], v[212:215], v[100:103], v[240:243]\n\t" \
  "ds_read_b64_tr_b16 v[202:203], v33 offset:47616\n\t" \
  "v_mfma_f32_16x16x32_bf16 v[244:247], v[212:215], v[108:111], v[244:247]\n\t" \
  "ds_read_b64_tr_b16 v[204:205], v33 offset:51200\n\t" \
  "global_load_dwordx4 v[152:155], v53, s[90:91] offset:2048 sc1\n\t" \
  "v_mfma_f32_16x16x32_bf16 v[244:247], v[208:211], v[104:107], v[244:247]\n\t" \
  "ds_read_b64_tr_b16 v[206:207], v33 offset:51712\n\t" \
  "v_mfma_f32_16x16x32_bf16 v[248:251], v[216:219], v[96:99], v[248:251]\n\t" \
  "ds_read_b64_tr_b16 v[208:209], v34 offset:47104\n\t" \
  "s_waitcnt lgkmcnt(6)\n\t" \
  "v_mfma_f32_16x16x32_bf16 v[248:251], v[220:223], v[100:103], v[248:251]\n\t" \
  "ds_read_b64_tr_b16 v[210:211], v34 offset:47616\n\t" \
  "v_mfma_f32_16x16x32_bf16 v[252:255], v[220:223], v[108:111], v[252:255]\n\t" \
  "ds_read_b64_tr_b16 v[212:213], v34 offset:51200\n\t" \
  "global_load_dwordx4 v[156:159], v53, s[90:91] offset:3072 sc1\n\t" \
  "v_mfma_f32_16x16x32_bf16 v[252:255], v[216:219], v[104:107], v[252:255]\n\t" \
  "ds_read_b64_tr_b16 v[214:215], v34 offset:51712\n\t" \
  "s_waitcnt vmcnt(10)\n\t" \
  "v_mfma_f32_16x16x32_bf16 v[224:227], v[192:195], v[112:115], v[224:227]\n\t" \
  "ds_read_b64_tr_b16 v[216:217], v35 offset:47104\n\t" \
  "s_waitcnt lgkmcnt(6)\n\t" \
  "v_mfma_f32_16x16x32_bf16 v[224:227], v[196:199], v[116:119], v[224:227]\n\t" \
  "ds_read_b64_tr_b16 v[218:219], v35 offset:47616\n\t" \
  "s_waitcnt vmcnt(8)\n\t" \
  "v_mfma_f32_16x16x32_bf16 v[228:231], v[196:199], v[124:127], v[228:231]\n\t" \
  "ds_read_b64_tr_b16 v[220:221], v35 offset:51200\n\t" \
  "s_lshr_b64 s[90:91], %18, 56\n\t" \
  "s_and_b32 s90, s90, 15\n\t" \
  "s_lshl_b32 s90, s90, 15\n\t" \
  "s_add_u32 s90, s90, s92\n\t" \
  "s_addc_u32 s91, s93, 0\n\t" \
  "global_load_dwordx4 v[160:163], v53, s[90:91] offset:0 sc1\n\t" \
  "v_mfma_f32_16x16x32_bf16 v[228:231], v[192:195], v[120:123], v[228:231]\n\t" \
  "ds_read_b64_tr_b16 v[222:223], v35 offset:51712\n\t" \
  "v_mfma_f32_16x16x32_bf16 v[232:235], v[200:203], v[112:115], v[232:235]\n\t" \
  "ds_read_b64_tr_b16 v[192:193], v32 offset:55296\n\t" \
  "s_waitcnt lgkmcnt(6)\n\t" \
  "v_mfma_f32_16x16x32_bf16 v[232:235], v[204:207], v[116:119], v[232:235]\n\t" \
  "ds_read_b64_tr_b16 v[194:195], v32 offset:55808\n\t" \
  "v_mfma_f32_16x16x32_bf16 v[236:239], v[204:207], v[124:127], v[236:239]\n\t" \
  "ds_read_b64_tr_b16 v[196:197], v32 offset:59392\n\t" \
  "global_load_dwordx4 v[164:167], v53, s[90:91] offset:1024 sc1\n\t" \
  "v_mfma_f32_16x16x32_bf16 v[236:239], v[200:203], v[120:123], v[236:239]\n\t" \
  "ds_read_b64_tr_b16 v[198:199], v32 offset:59904\n\t" \
  "v_mfma_f32_16x16x32_bf16 v[240:243], v[208:211], v[112:115], v[240:243]\n\t" \
  "ds_read_b64_tr_b16 v[200:201], v33 offset:55296\n\t" \
  "s_waitcnt lgkmcnt(6)\n\t" \
  "v_mfma_f32_16x16x32_bf16 v[240:243], v[212:215], v[116:119], v[240:243]\n\t" \
  "ds_read_b128 v[40:43], v56 offset:80\n\t" \
  "ds_read_b64_tr_b16 v[202:203], v33 offset:55808\n\t" \
  "v_mfma_f32_16x16x32_bf16 v[244:247], v[212:215], v[124:127], v[244:247]\n\t" \
  "ds_read_b64_tr_b16 v[204:205], v33 offset:59392\n\t" \
  "global_load_dwordx4 v[168:171], v53, s[90:91] offset:2048 sc1\n\t" \
  "v_mfma_f32_16x16x32_bf16 v[244:247], v[208:211], v[120:123], v[244:247]\n\t" \
  "ds_read_b64_tr_b16 v[206:207], v33 offset:59904\n\t" \
  "v_mfma_f32_16x16x32_bf16 v[248:251], v[216:219], v[112:115], v[248:251]\n\t" \
  "ds_read_b64_tr_b16 v[208:209], v34 offset:55296\n\t" \
  "s_waitcnt lgkmcnt(7)\n\t" \
  "v_mfma_f32_16x16x32_bf16 v[248:251], v[220:223], v[116:119], v[248:251]\n\t" \
  "ds_read_b64_tr_b16 v[210:211], v34 offset:55808\n\t" \
  "v_mfma_f32_16x16x32_bf16 v[252:255], v[220:223], v[124:127], v[252:255]\n\t" \
  "ds_read_b64_tr_b16 v[212:213], v34 offset:59392\n\t" \
  "global_load_dwordx4 v[172:175], v53, s[90:91] offset:3072 sc1\n\t" \
  "v_mfma_f32_16x16x32_bf16 v[252:255], v[216:219], v[120:123], v[252:255]\n\t" \
  "ds_read_b64_tr_b16 v[214:215], v34 offset:59904\n\t" \
  "s_waitcnt vmcnt(10)\n\t" \
  "v_mfma_f32_16x16x32_bf16 v[224:227], v[192:195], v[128:131], v[224:227]\n\t" \
  "s_waitcnt lgkmcnt(7)\n\t" \
  "v_min_u32 v40, v40, v41\n\t" \
  "v_min3_u32 v40, v40, v42, v43\n\t" \
  "ds_read_b64_tr_b16 v[216:217], v35 offset:55296\n\t" \
  "v_mfma_f32_16x16x32_bf16 v[224:227], v[196:199], v[132:135], v[224:227]\n\t" \
  "v_readfirstlane_b32 vcc_lo, v40\n\t" \
  "ds_read_b64_tr_b16 v[218:219], v35 offset:55808\n\t" \
  "s_waitcnt vmcnt(8)\n\t" \
  "v_mfma_f32_16x16x32_bf16 v[228:231], v[196:199], v[140:143], v[228:231]\n\t" \
  "s_cmp_ge_u32 vcc_lo, 16\n\t" \
  "s_cbranch_scc0 40f\n\t" \
  "41:\n\t" \
  "ds_read_b64_tr_b16 v[220:221], v35 offset:59392\n\t" \
  "s_lshr_b64 s[90:91], %18, 60\n\t" \
  "s_and_b32 s90, s90, 15\n\t" \
  "s_lshl_b32 s90, s90, 15\n\t" \
  "s_add_u32 s90, s90, s92\n\t" \
  "s_addc_u32 s91, s93, 0\n\t" \
  "global_load_dwordx4 v[176:179], v53, s[90:91] offset:0 sc1\n\t" \
  "v_mfma_f32_16x16x32_bf16 v[228:231], v[192:195], v[136:139], v[228:231]\n\t" \
  "ds_read_b64_tr_b16 v[222:223], v35 offset:59904\n\t" \
  "s_waitcnt lgkmcnt(6)\n\t" \
  "v_mfma_f32_16x16x32_bf16 v[232:235], v[200:203], v[128:131], v[232:235]\n\t" \
  "ds_read_b64_tr_b16 v[192:193], v36 offset:2304\n\t" \
  "v_mfma_f32_16x16x32_bf16 v[232:235], v[204:207], v[132:135], v[232:235]\n\t" \
  "ds_read_b64_tr_b16 v[194:195], v36 offset:2816\n\t" \
  "v_mfma_f32_16x16x32_bf16 v[236:239], v[204:207], v[140:143], v[236:239]\n\t" \
  "ds_read_b64_tr_b16 v[196:197], v36 offset:10752\n\t" \
  "global_load_dwordx4 v[180:183], v53, s[90:91] offset:1024 sc1\n\t" \
  "v_mfma_f32_16x16x32_bf16 v[236:239], v[200:203], v[136:139], v[236:239]\n\t" \
  "ds_read_b64_tr_b16 v[198:199], v36 offset:11264\n\t" \
  "v_mfma_f32_16x16x32_bf16 v[240:243], v[208:211], v[128:131], v[240:243]\n\t" \
  "ds_read_b64_tr_b16 v[200:201], v37 offset:2304\n\t" \
  "s_waitcnt lgkmcnt(6)\n\t" \
  "v_mfma_f32_16x16x32_bf16 v[240:243], v[212:215], v[132:135], v[240:243]\n\t" \
  "ds_read_b64_tr_b16 v[202:203], v37 offset:2816\n\t" \
  "v_mfma_f32_16x16x32_bf16 v[244:247], v[212:215], v[140:143], v[244:247]\n\t" \
  "ds_read_b64_tr_b16 v[204:205], v37 offset:10752\n\t" \
  "global_load_dwordx4 v[184:187], v53, s[90:91] offset:2048 sc1\n\t" \
  "v_mfma_f32_16x16x32_bf16 v[244:247], v[208:211], v[136:139], v[244:247]\n\t" \
  "ds_read_b64_tr_b16 v[206:207], v37 offset:11264\n\t" \
  "v_mfma_f32_16x16x32_bf16 v[248:251], v[216:219], v[128:131], v[248:251]\n\t" \
  "ds_read_b64_tr_b16 v[208:209], v38 offset:2304\n\t" \
  "s_waitcnt lgkmcnt(6)\n\t" \
  "v_mfma_f32_16x16x32_bf16 v[248:251], v[220:223], v[132:135], v[248:251]\n\t" \
  "ds_read_b64_tr_b16 v[210:211], v38 offset:2816\n\t" \
  "v_mfma_f32_16x16x32_bf16 v[252:255], v[220:223], v[140:143], v[252:255]\n\t" \
  "ds_read_b64_tr_b16 v[212:213], v38 offset:10752\n\t" \
  "global_load_dwordx4 v[188:191], v53, s[90:91] offset:3072 sc1\n\t" \
  "v_mfma_f32_16x16x32_bf16 v[252:255], v[216:219], v[136:139], v[252:255]\n\t" \
  "ds_read_b64_tr_b16 v[214:215], v38 offset:11264\n\t" \
  "s_waitcnt vmcnt(10)\n\t" \
  "v_mfma_f32_16x16x32_bf16 v[224:227], v[192:195], v[144:147], v[224:227]\n\t" \
  "ds_read_b64_tr_b16 v[216:217], v39 offset:2304\n\t" \
  "s_waitcnt lgkmcnt(6)\n\t" \
  "v_mfma_f32_16x16x32_bf16 v[224:227], v[196:199], v[148:151], v[224:227]\n\t" \
  "ds_read_b64_tr_b16 v[218:219], v39 offset:2816\n\t" \
  "s_waitcnt vmcnt(8)\n\t" \
  "v_mfma_f32_16x16x32_bf16 v[228:231], v[196:199], v[156:159], v[228:231]\n\t" \
  "ds_read_b64_tr_b16 v[220:221], v39 offset:10752\n\t" \
  "v_mfma_f32_16x16x32_bf16 v[228:231], v[192:195], v[152:155], v[228:231]\n\t" \
  "ds_read_b64_tr_b16 v[222:223], v39 offset:11264\n\t" \
  "v_mfma_f32_16x16x32_bf16 v[232:235], v[200:203], v[144:147], v[232:235]\n\t" \
  "ds_read_b64_tr_b16 v[192:193], v36 offset:19200\n\t" \
  "s_waitcnt lgkmcnt(6)\n\t" \
  "v_mfma_f32_16x16x32_bf16 v[232:235], v[204:207], v[148:151], v[232:235]\n\t" \
  "ds_read_b64_tr_b16 v[194:195], v36 offset:19712\n\t" \
  "v_mfma_f32_16x16x32_bf16 v[236:239], v[204:207], v[156:159], v[236:239]\n\t" \
  "ds_read_b64_tr_b16 v[196:197], v36 offset:27648\n\t" \
  "v_mfma_f32_16x16x32_bf16 v[236:239], v[200:203], v[152:155], v[236:239]\n\t" \
  "ds_read_b64_tr_b16 v[198:199], v36 offset:28160\n\t" \
  "v_mfma_f32_16x16x32_bf16 v[240:243], v[208:211], v[144:147], v[240:243]\n\t" \
  "ds_read_b64_tr_b16 v[200:201], v37 offset:19200\n\t" \
  "s_waitcnt lgkmcnt(6)\n\t" \
  "v_mfma_f32_16x16x32_bf16 v[240:243], v[212:215], v[148:151], v[240:243]\n\t" \
  "ds_read_b128 v[40:43], v56 offset:160\n\t" \
  "ds_read_b64_tr_b16 v[202:203], v37 offset:19712\n\t" \
  "v_mfma_f32_16x16x32_bf16 v[244:247], v[212:215], v[156:159], v[244:247]\n\t" \
  "ds_read_b64_tr_b16 v[204:205], v37 offset:27648\n\t" \
  "v_mfma_f32_16x16x32_bf16 v[244:247], v[208:211], v[152:155], v[244:247]\n\t" \
  "ds_read_b64_tr_b16 v[206:207], v37 offset:28160\n\t" \
  "v_mfma_f32_16x16x32_bf16 v[248:251], v[216:219], v[144:147], v[248:251]\n\t" \
  "ds_read_b64_tr_b16 v[208:209], v38 offset:19200\n\t" \
  "s_waitcnt lgkmcnt(7)\n\t" \
  "v_mfma_f32_16x16x32_bf16 v[248:251], v[220:223], v[148:151], v[248:251]\n\t" \
  "ds_read_b64_tr_b16 v[210:211], v38 offset:19712\n\t" \
  "v_mfma_f32_16x16x32_bf16 v[252:255], v[220:223], v[156:159], v[252:255]\n\t" \
  "ds_read_b64_tr_b16 v[212:213], v38 offset:27648\n\t" \
  "v_mfma_f32_16x16x32_bf16 v[252:255], v[216:219], v[152:155], v[252:255]\n\t" \
  "ds_read_b64_tr_b16 v[214:215], v38 offset:28160\n\t" \
  "s_waitcnt vmcnt(6)\n\t" \
  "v_mfma_f32_16x16x32_bf16 v[224:227], v[192:195], v[160:163], v[224:227]\n\t" \
  "s_waitcnt lgkmcnt(7)\n\t" \
  "v_min_u32 v40, v40, v41\n\t" \
  "v_min3_u32 v40, v40, v42, v43\n\t" \
  "ds_read_b64_tr_b16 v[216:217], v39 offset:19200\n\t" \
  "v_mfma_f32_16x16x32_bf16 v[224:227], v[196:199], v[164:167], v[224:227]\n\t" \
  "v_readfirstlane_b32 vcc_lo, v40\n\t" \
  "ds_read_b64_tr_b16 v[218:219], v39 offset:19712\n\t" \
  "s_waitcnt vmcnt(4)\n\t" \
  "v_mfma_f32_16x16x32_bf16 v[228:231], v[196:199], v[172:175], v[228:231]\n\t" \
  "s_cmp_ge_u32 vcc_lo, 16\n\t" \
  "s_cbranch_scc0 42f\n\t" \
  "43:\n\t" \
  "ds_read_b64_tr_b16 v[220:221], v39 offset:27648\n\t" \
  "v_mfma_f32_16x16x32_bf16 v[228:231], v[192:195], v[168:171], v[228:231]\n\t" \
  "ds_read_b64_tr_b16 v[222:223], v39 offset:28160\n\t" \
  "s_waitcnt lgkmcnt(6)\n\t" \
  "v_mfma_f32_16x16x32_bf16 v[232:235], v[200:203], v[160:163], v[232:235]\n\t" \
  "ds_read_b64_tr_b16 v[192:193], v28 offset:0\n\t" \
  "v_mfma_f32_16x16x32_bf16 v[232:235], v[204:207], v[164:167], v[232:235]\n\t" \
  "ds_read_b64_tr_b16 v[194:195], v28 offset:512\n\t" \
  "v_mfma_f32_16x16x32_bf16 v[236:239], v[204:207], v[172:175], v[236:239]\n\t" \
  "ds_read_b64_tr_b16 v[196:197], v28 offset:4096\n\t" \
  "v_mfma_f32_16x16x32_bf16 v[236:239], v[200:203], v[168:171], v[236:239]\n\t" \
  "ds_read_b64_tr_b16 v[198:199], v28 offset:4608\n\t" \
  "v_mfma_f32_16x16x32_bf16 v[240:243], v[208:211], v[160:163], v[240:243]\n\t" \
  "ds_read_b64_tr_b16 v[200:201], v29 offset:0\n\t" \
  "s_waitcnt lgkmcnt(6)\n\t" \
  "v_mfma_f32_16x16x32_bf16 v[240:243], v[212:215], v[164:167], v[240:243]\n\t" \
  "ds_read_b64_tr_b16 v[202:203], v29 offset:512\n\t" \
  "v_mfma_f32_16x16x32_bf16 v[244:247], v[212:215], v[172:175], v[244:247]\n\t" \
  "ds_read_b64_tr_b16 v[204:205], v29 offset:4096\n\t" \
  "v_mfma_f32_16x16x32_bf16 v[244:247], v[208:211], v[168:171], v[244:247]\n\t" \
  "ds_read_b64_tr_b16 v[206:207], v29 offset:4608\n\t" \
  "v_mfma_f32_16x16x32_bf16 v[248:251], v[216:219], v[160:163], v[248:251]\n\t" \
  "ds_read_b64_tr_b16 v[208:209], v30 offset:0\n\t" \
  "s_waitcnt lgkmcnt(6)\n\t" \
  "v_mfma_f32_16x16x32_bf16 v[248:251], v[220:223], v[164:167], v[248:251]\n\t" \
  "ds_read_b64_tr_b16 v[210:211], v30 offset:512\n\t" \
  "v_mfma_f32_16x16x32_bf16 v[252:255], v[220:223], v[172:175], v[252:255]\n\t" \
  "ds_read_b64_tr_b16 v[212:213], v30 offset:4096\n\t" \
  "v_mfma_f32_16x16x32_bf16 v[252:255], v[216:219], v[168:171], v[252:255]\n\t" \
  "ds_read_b64_tr_b16 v[214:215], v30 offset:4608\n\t" \
  "s_waitcnt vmcnt(2)\n\t" \
  "v_mfma_f32_16x16x32_bf16 v[224:227], v[192:195], v[176:179], v[224:227]\n\t" \
  "ds_read_b64_tr_b16 v[216:217], v31 offset:0\n\t" \
  "s_waitcnt lgkmcnt(6)\n\t" \
  "v_mfma_f32_16x16x32_bf16 v[224:227], v[196:199], v[180:183], v[224:227]\n\t" \
  "ds_read_b64_tr_b16 v[218:219], v31 offset:512\n\t" \
  "s_waitcnt vmcnt(0)\n\t" \
  "v_mfma_f32_16x16x32_bf16 v[228:231], v[196:199], v[188:191], v[228:231]\n\t" \
  "ds_read_b64_tr_b16 v[220:221], v31 offset:4096\n\t" \
  "v_mfma_f32_16x16x32_bf16 v[228:231], v[192:195], v[184:187], v[228:231]\n\t" \
  "ds_read_b64_tr_b16 v[222:223], v31 offset:4608\n\t" \
  "v_mfma_f32_16x16x32_bf16 v[232:235], v[200:203], v[176:179], v[232:235]\n\t" \
  "s_waitcnt lgkmcnt(5)\n\t" \
  "v_mfma_f32_16x16x32_bf16 v[232:235], v[204:207], v[180:183], v[232:235]\n\t" \
  "v_mfma_f32_16x16x32_bf16 v[236:239], v[204:207], v[188:191], v[236:239]\n\t" \
  "v_cvt_pk_bf16_f32 v224, v224, v225\n\t" \
  "v_cvt_pk_bf16_f32 v225, v226, v227\n\t" \
  "v_mfma_f32_16x16x32_bf16 v[236:239], v[200:203], v[184:187], v[236:239]\n\t" \
  "ds_write_b64 v57, v[224:225] offset:0\n\t" \
  "v_mfma_f32_16x16x32_bf16 v[240:243], v[208:211], v[176:179], v[240:243]\n\t" \
  "v_cvt_pk_bf16_f32 v228, v228, v229\n\t" \
  "v_cvt_pk_bf16_f32 v229, v230, v231\n\t" \
  "s_waitcnt lgkmcnt(2)\n\t" \
  "v_mfma_f32_16x16x32_bf16 v[240:243], v[212:215], v[180:183], v[240:243]\n\t" \
  "ds_write_b64 v57, v[228:229] offset:2048\n\t" \
  "v_mfma_f32_16x16x32_bf16 v[244:247], v[212:215], v[188:191], v[244:247]\n\t" \
  "v_cvt_pk_bf16_f32 v232, v232, v233\n\t" \
  "v_cvt_pk_bf16_f32 v233, v234, v235\n\t" \
  "v_mfma_f32_16x16x32_bf16 v[244:247], v[208:211], v[184:187], v[244:247]\n\t" \
  "ds_write_b64 v58, v[232:233] offset:0\n\t" \
  "v_mfma_f32_16x16x32_bf16 v[248:251], v[216:219], v[176:179], v[248:251]\n\t" \
  "v_cvt_pk_bf16_f32 v236, v236, v237\n\t" \
  "v_cvt_pk_bf16_f32 v237, v238, v239\n\t" \
  "s_waitcnt lgkmcnt(3)\n\t" \
  "v_mfma_f32_16x16x32_bf16 v[248:251], v[220:223], v[180:183], v[248:251]\n\t" \
  "ds_write_b64 v58, v[236:237] offset:2048\n\t" \
  "v_mfma_f32_16x16x32_bf16 v[252:255], v[220:223], v[188:191], v[252:255]\n\t" \
  "v_cvt_pk_bf16_f32 v240, v240, v241\n\t" \
  "v_cvt_pk_bf16_f32 v241, v242, v243\n\t" \
  "v_mfma_f32_16x16x32_bf16 v[252:255], v[216:219], v[184:187], v[252:255]\n\t" \
  "ds_write_b64 v59, v[240:241] offset:0\n\t" \
  "v_cvt_pk_bf16_f32 v244, v244, v245\n\t" \
  "v_cvt_pk_bf16_f32 v245, v246, v247\n\t" \
  "ds_write_b64 v59, v[244:245] offset:2048\n\t" \
  "v_cvt_pk_bf16_f32 v248, v248, v249\n\t" \
  "v_cvt_pk_bf16_f32 v249, v250, v251\n\t" \
  "ds_write_b64 v60, v[248:249] offset:0\n\t" \
  "s_nop 0\n\t" \
  "v_cvt_pk_bf16_f32 v252, v252, v253\n\t" \
  "v_cvt_pk_bf16_f32 v253, v254, v255\n\t" \
  "ds_write_b64 v60, v[252:253] offset:2048\n\t" \
  "ds_read_b128 v[64:67], v61 offset:0\n\t" \
  "ds_read_b128 v[68:71], v61 offset:1024\n\t" \
  "ds_read_b128 v[72:75], v61 offset:2048\n\t" \
  "ds_read_b128 v[76:79], v61 offset:3072\n\t" \
  "s_waitcnt lgkmcnt(3)\n\t" \
  "global_store_dwordx4 v62, v[64:67], %22 offset:-4096" OUTPOL "\n\t" \
  "s_waitcnt lgkmcnt(2)\n\t" \
  "global_store_dwordx4 v63, v[68:71], %22 offset:-4096" OUTPOL "\n\t" \
  "s_waitcnt lgkmcnt(1)\n\t" \
  "global_store_dwordx4 v62, v[72:75], %22 offset:0" OUTPOL "\n\t" \
  "s_waitcnt lgkmcnt(0)\n\t" \
  "global_store_dwordx4 v63, v[76:79], %22 offset:0" OUTPOL "\n\t" \
  "s_branch 19f\n\t" \
  "23:\n\t" \
  "510:\n\t" \
  "s_cbranch_execz 26b\n\t" \
  "s_waitcnt vmcnt(0)\n\t" \
  "s_mov_b64 exec, 1\n\t" \
  "ds_add_rtn_u32 v10, v56, v10 offset:16\n\t" \
  "s_mov_b32 s74, 16\n\t" \
  "s_waitcnt lgkmcnt(0)\n\t" \
  "v_readfirstlane_b32 vcc_lo, v10\n\t" \
  "s_mov_b64 exec, 0xffff\n\t" \
  "s_nop 0\n\t" \
  "s_cmp_eq_u32 vcc_lo, 3\n\t" \
  "s_cbranch_scc0 22f\n\t" \
  "global_store_dword v11, v55, %19 sc1\n\t" \
  "22:\n\t" \
  "s_mov_b32 exec_lo, -1\n\t" \
  "global_load_dword v2, v1, %14 offset:-4096 sc1\n\t" \
  "s_mov_b64 exec, -1\n\t" \
  "s_setpc_b64 s[68:69]\n\t" \
  "7:\n\t" \
  "s_sleep 1\n\t" \
  "ds_read_b128 v[60:63], v56 offset:64\n\t" \
  "ds_read_b64_tr_b16 v[108:109], v5 offset:0\n\t" \
  "ds_read_b64_tr_b16 v[110:111], v5 offset:16\n\t" \
  "ds_read_b128 v[112:115], v6 offset:16384\n\t" \
  "ds_read_b128 v[116:119], v6 offset:15360\n\t" \
  "ds_read_b64_tr_b16 v[120:121], v5 offset:512\n\t" \
  "ds_read_b64_tr_b16 v[122:123], v5 offset:528\n\t" \
  "ds_read_b128 v[124:127], v6 offset:14336\n\t" \
  "ds_read_b64_tr_b16 v[128:129], v5 offset:1024\n\t" \
  "ds_read_b64_tr_b16 v[130:131], v5 offset:1040\n\t" \
  "ds_read_b128 v[132:135], v6 offset:13312\n\t" \
  "ds_read_b64_tr_b16 v[136:137], v5 offset:1536\n\t" \
  "ds_read_b64_tr_b16 v[138:139], v5 offset:1552\n\t" \
  "ds_read_b128 v[140:143], v6 offset:12288\n\t" \
  "ds_read_b64_tr_b16 v[144:145], v5 offset:2048\n\t" \
  "s_waitcnt lgkmcnt(0)\n\t" \
  "v_and_b32 v60, v60, v61\n\t" \
  "v_and_b32 v62, v62, v63\n\t" \
  "v_mov_b32 v12, %13\n\t" \
  "v_and_b32 v60, v60, v62\n\t" \
  "s_nop 0\n\t" \
  "v_readfirstlane_b32 vcc_lo, v60\n\t" \
  "s_nop 1\n\t" \
  "s_cmp_eq_u32 vcc_lo, 0\n\t" \
  "s_cbranch_scc1 7b\n\t" \
  "s_branch 6b\n\t" \
  "25:\n\t" \
  "511:\n\t" \
  "s_cbranch_execz 26b\n\t" \
  "global_load_dwordx4 v[64:67], v53, s[90:91] offset:0 sc1\n\t" \
  "global_load_dwordx4 v[68:71], v53, s[90:91] offset:1024 sc1\n\t" \
  "global_load_dwordx4 v[72:75], v53, s[90:91] offset:2048 sc1\n\t" \
  "global_load_dwordx4 v[76:79], v53, s[90:91] offset:3072 sc1\n\t" \
  "s_swappc_b64 s[70:71], s[68:69]\n\t" \
  "global_load_dwordx4 v[80:83], v53, s[90:91] offset:0 sc1\n\t" \
  "global_load_dwordx4 v[84:87], v53, s[90:91] offset:1024 sc1\n\t" \
  "global_load_dwordx4 v[88:91], v53, s[90:91] offset:2048 sc1\n\t" \
  "global_load_dwordx4 v[92:95], v53, s[90:91] offset:3072 sc1\n\t" \
  "s_swappc_b64 s[70:71], s[68:69]\n\t" \
  "36:\n\t" \
  "ds_read_b128 v[40:43], v56 offset:80\n\t" \
  "s_waitcnt lgkmcnt(0)\n\t" \
  "v_min_u32 v40, v40, v41\n\t" \
  "v_min3_u32 v40, v40, v42, v43\n\t" \
  "s_nop 1\n\t" \
  "v_readfirstlane_b32 vcc_lo, v40\n\t" \
  "s_nop 1\n\t" \
  "s_cmp_ge_u32 vcc_lo, 7\n\t" \
  "s_cbranch_scc0 36b\n\t" \
  "s_branch 37b\n\t" \
  "38:\n\t" \
  "ds_read_b128 v[40:43], v56 offset:80\n\t" \
  "s_waitcnt lgkmcnt(0)\n\t" \
  "v_min_u32 v40, v40, v41\n\t" \
  "v_min3_u32 v40, v40, v42, v43\n\t" \
  "s_nop 1\n\t" \
  "v_readfirstlane_b32 vcc_lo, v40\n\t" \
  "s_nop 1\n\t" \
  "s_cmp_ge_u32 vcc_lo, 13\n\t" \
  "s_cbranch_scc0 38b\n\t" \
  "s_branch 39b\n\t" \
  "40:\n\t" \
  "ds_read_b128 v[40:43], v56 offset:80\n\t" \
  "s_waitcnt lgkmcnt(0)\n\t" \
  "v_min_u32 v40, v40, v41\n\t" \
  "v_min3_u32 v40, v40, v42, v43\n\t" \
  "s_nop 1\n\t" \
  "v_readfirstlane_b32 vcc_lo, v40\n\t" \
  "s_nop 1\n\t" \
  "s_cmp_ge_u32 vcc_lo, 16\n\t" \
  "s_cbranch_scc0 40b\n\t" \
  "s_branch 41b\n\t" \
  "42:\n\t" \
  "ds_read_b128 v[40:43], v56 offset:160\n\t" \
  "s_waitcnt lgkmcnt(0)\n\t" \
  "v_min_u32 v40, v40, v41\n\t" \
  "v_min3_u32 v40, v40, v42, v43\n\t" \
  "s_nop 1\n\t" \
  "v_readfirstlane_b32 vcc_lo, v40\n\t" \
  "s_nop 1\n\t" \
  "s_cmp_ge_u32 vcc_lo, 16\n\t" \
  "s_cbranch_scc0 42b\n\t" \
  "s_branch 43b\n\t" \
  "46:\n\t" \
  "s_cmp_eq_u32 %13, 0\n\t" \
  "s_cbranch_scc1 48f\n\t" \
  "s_sleep 1\n\t" \
  "ds_read_b32 v16, v56 offset:96\n\t" \
  "s_waitcnt lgkmcnt(0)\n\t" \
  "v_readfirstlane_b32 vcc_lo, v16\n\t" \
  "s_nop 1\n\t" \
  "s_cmp_ge_u32 vcc_lo, 1\n\t" \
  "s_cbranch_scc0 46b\n\t" \
  "s_branch 47b\n\t" \
  "48:\n\t" \
  "v_bfe_u32 v19, v12, 4, 4\n\t" \
  "v_lshlrev_b32 v19, 2, v19\n\t" \
  "s_mov_b32 s90, 0\n\t" \
  "148:\n\t" \
  "global_load_dword v16, v19, %21 sc1\n\t" \
  "s_add_u32 s90, s90, 1\n\t" \
  "s_waitcnt vmcnt(0)\n\t" \
  "v_cmp_ne_u32 vcc, v55, v16\n\t" \
  "s_mov_b32 s88, %26\n\t" \
  "s_mov_b32 s89, s88\n\t" \
  "s_and_b64 vcc, vcc, s[88:89]\n\t" \
  "s_cbranch_scc0 248f\n\t" \
  "s_sleep 2\n\t" \
  "s_cmp_lt_u32 s90, 0x400000\n\t" \
  "s_cbranch_scc1 148b\n\t" \
  "248:\n\t" \
  "v_mov_b32 v16, 1\n\t" \
  "ds_max_u32 v56, v16 offset:96\n\t" \
  "s_branch 47b\n\t" \
  "49:\n\t" \
  "s_cmp_eq_u32 %13, 0\n\t" \
  "s_cbranch_scc1 51f\n\t" \
  "s_sleep 1\n\t" \
  "ds_read_b32 v16, v56 offset:96\n\t" \
  "s_waitcnt lgkmcnt(0)\n\t" \
  "v_readfirstlane_b32 vcc_lo, v16\n\t" \
  "s_nop 1\n\t" \
  "s_cmp_ge_u32 vcc_lo, 2\n\t" \
  "s_cbranch_scc0 49b\n\t" \
  "s_branch 50b\n\t" \
  "51:\n\t" \
  "v_bfe_u32 v19, v12, 4, 4\n\t" \
  "v_lshlrev_b32 v19, 2, v19\n\t" \
  "s_mov_b32 s90, 0\n\t" \
  "151:\n\t" \
  "global_load_dword v16, v19, %21 sc1\n\t" \
  "s_add_u32 s90, s90, 1\n\t" \
  "s_waitcnt vmcnt(0)\n\t" \
  "v_cmp_ne_u32 vcc, v55, v16\n\t" \
  "s_mov_b32 s88, %26\n\t" \
  "s_mov_b32 s89, s88\n\t" \
  "s_and_b64 vcc, vcc, -1\n\t" \
  "s_cbranch_scc0 251f\n\t" \
  "s_sleep 2\n\t" \
  "s_cmp_lt_u32 s90, 0x400000\n\t" \
  "s_cbranch_scc1 151b\n\t" \
  "251:\n\t" \
  "v_mov_b32 v16, 2\n\t" \
  "ds_max_u32 v56, v16 offset:96\n\t" \
  "s_branch 50b\n\t" \
  "19:\n\t"
#define C_CLOB "v10","v11","v12","v13","v18","v19","v20","v21","v22","v23","v24","v25","v26","v27","v28","v29","v30","v31","v32","v33","v34","v35","v36","v37","v38","v39","v40","v41","v42","v43","v44","v45","v46","v47","v48","v49","v50","v53","v55","v56","v57","v58","v59","v60","v61","v62","v63","v64","v65","v66","v67","v68","v69","v70","v71","v72","v73","v74","v75","v76","v77","v78","v79","v80","v81","v82","v83","v84","v85","v86","v87","v88","v89","v90","v91","v92","v93","v94","v95","v96","v97","v98","v99","v100","v101","v102","v103","v104","v105","v106","v107","v108","v109","v110","v111","v112","v113","v114","v115","v116","v117","v118","v119","v120","v121","v122","v123","v124","v125","v126","v127","v128","v129","v130","v131","v132","v133","v134","v135","v136","v137","v138","v139","v140","v141","v142","v143","v144","v145","v146","v147","v148","v149","v150","v151","v152","v153","v154","v155","v156","v157","v158","v159","v160","v161","v162","v163","v164","v165","v166","v167","v168","v169","v170","v171","v172","v173","v174","v175","v176","v177","v178","v179","v180","v181","v182","v183","v184","v185","v186","v187","v188","v189","v190","v191","v192","v193","v194","v195","v196","v197","v198","v199","v200","v201","v202","v203","v204","v205","v206","v207","v208","v209","v210","v211","v212","v213","v214","v215","v216","v217","v218","v219","v220","v221","v222","v223","v224","v225","v226","v227","v228","v229","v230","v231","v232","v233","v234","v235","v236","v237","v238","v239","v240","v241","v242","v243","v244","v245","v246","v247","v248","v249","v250","v251","v252","v253","v254","v255"

static_assert(KVP<13 && KVP>TGS+1, "");

#define TS(i)
#define TSDUMP()
#define TSB(i)
#define TSR(i)
#define TSH(i)
#define TSDECL()

#define TSF(i)
typedef unsigned long long u64;
typedef u64 u64x8 __attribute__((ext_vector_type(8)));
typedef u64 u64x4 __attribute__((ext_vector_type(4)));
typedef unsigned u32x8 __attribute__((ext_vector_type(8)));
typedef unsigned u32x16 __attribute__((ext_vector_type(16)));
template<class T> __device__ __forceinline__ T sg(T v){
  if constexpr(sizeof(T)==4) return (T)__builtin_amdgcn_readfirstlane((unsigned)v); else return v; }
#define GP(T,v) ((T*)(__attribute__((address_space(1))) T*)(v))
__global__ __launch_bounds__(512,2) __attribute__((aligned(4096))) void fused_kernel(const u16* x_, const u16* wqkv_, const u16* kin_, const u16* vin_, const u16* shk_, const u16* hk_,
    const u16* wo_, const u16* qin_, const u16* sD_, u16* out_, u16* ws_, u16* unused_)
{
#define ST0()
#define ST1()
#define ARGS0() u64x8 ka; u64x4 kb; asm volatile("s_load_dwordx16 %0, %2, 0\n\ts_load_dwordx8 %1, %2, 64" : "=&s"(ka), "=&s"(kb) : "s"(__builtin_amdgcn_kernarg_segment_ptr()) : "memory")
#define ARGS() asm volatile("s_waitcnt lgkmcnt(0)" : "+s"(ka), "+s"(kb) :: "memory"); ST1() \
  const u16 *x=GP(const u16,ka[0]), *wqkv=GP(const u16,ka[1]), *kin=GP(const u16,ka[2]), *vin=GP(const u16,ka[3]), *shk=GP(const u16,ka[4]), *hk=GP(const u16,ka[5]), *wo=GP(const u16,ka[6]), *qin=GP(const u16,ka[7]), *sD=GP(const u16,kb[0]); \
  u16 *out=GP(u16,kb[1]), *o=GP(u16,kb[2]); char* ws=(char*)o; unsigned cpar=xcd>>2; \
  auto loc=[&](unsigned off)->unsigned{ return off|(((cpar^(off>>21))&1)<<13); }; \
  (void)x; (void)wqkv; (void)kin; (void)vin; (void)shk; (void)hk; (void)wo; (void)qin; (void)sD; (void)out; (void)loc
  ST0()
  __shared__ __attribute__((aligned(1024))) char smem[ALL_LDS];
  const int tid=threadIdx.x;
  const int w8=__builtin_amdgcn_readfirstlane(tid>>6);
  asm volatile("s_sub_u32 vcc_lo, %0, 4\n\ts_cbranch_scc1 1f\n\ts_cmp_lg_u32 %0, 7\n\ts_cbranch_scc1 2f\n\t"
      "s_lshr_b32 vcc_lo, %2, 3\n\ts_mul_i32 vcc_lo, vcc_lo, %5\n\tv_and_b32 v254, 15, %1\n\tv_lshl_add_u32 v254, v254, 7, vcc_lo\n\ts_getpc_b64 vcc\n\tglobal_load_dword v255, v254, vcc\n\ts_branch 1f\n"
      "2:\n\ts_and_b32 vcc_hi, %2, 7\n\ts_cbranch_scc1 1f\n\ts_cmp_gt_u32 %2, 32\n\ts_cbranch_scc1 1f\n\ts_mul_i32 vcc_hi, %2, 3\n\ts_lshr_b32 vcc_hi, vcc_hi, 3\n\ts_add_u32 vcc_lo, vcc_lo, vcc_hi\n\t"
      "v_and_b32 v253, 3, %1\n\tv_and_b32 v254, 4, %1\n\tv_lshlrev_b32 v253, 7, v253\n\tv_lshl_add_u32 v253, v254, 11, v253\n\tv_lshl_add_u32 v253, vcc_lo, 9, v253\n\tv_add_u32 v253, %4, v253\n\t"
      "s_load_dwordx2 vcc, %3, 80\n\ts_mov_b64 exec, 255\n\ts_waitcnt lgkmcnt(0)\n\tglobal_store_dword v253, v253, vcc sc1\n\ts_mov_b64 exec, -1\n1:"
      :: "s"(w8), "v"(tid), "s"(blockIdx.x), "s"(__builtin_amdgcn_kernarg_segment_ptr()), "n"(WS_GEN+16384), "n"(CODE_ST) : "memory", "scc", "vcc", "v253", "v254", "v255");
  LDS_AS unsigned* const ct=(LDS_AS unsigned*)(LDS_AS char*)(smem+CT_OFF);
  volatile LDS_AS unsigned* const vct=ct;
  auto all4=[&](int i)->unsigned{ u32x4 v=*(volatile LDS_AS u32x4*)(ct+i); return __builtin_amdgcn_readfirstlane(v[0]&v[1]&v[2]&v[3]); };
#define ROTF(b) ((4+4*xr+4*xc+8*(b))&15)
#define BLOCK() int bid=blockIdx.x, w=w8&3; asm volatile("" : "+s"(bid), "+s"(w)); __builtin_assume(bid>=0 && bid<256 && w>=0 && w<4); DERIVE()
#define DERIVE() const int xcd=bid&7, idx=bid>>3, ra=idx&3, rbb=idx>>2, xr=xcd>>1, xc=xcd&1, rb=xr*4+ra, hp=xc*8+rbb, s0=rb*128, tm=rb, tn=hp^TNX, cb=(xcd>>2)*128+idx*4+(xcd&3), gi=cb^16; (void)s0; (void)gi; \
  auto korder=[&]()->unsigned long long{ const unsigned N=0x7654210u, sh=4*(KR), q=(((N>>sh)|(N<<(28-sh)))&0xfffffffu)|0x30000000u; \
    return ((unsigned long long)q<<32|q)^(xc? 0x88888888ull : 0x8888888800000000ull); }; \
  const unsigned l1m=0xf7u<<(8*xc); (void)l1m; (void)korder

  if(__builtin_expect(w8>=4,0)){
  BLOCK(); constexpr int WB=4; (void)WB;
  const int hd=w>>1, par=w&1;
  __builtin_amdgcn_s_setprio(CPRIO);
  {
    ARGS0(); LANES();
    if(w==0){ if(l<52) ct[l]=0u; if(idx==0) *(u32x4*)(smem+AUX+l*16)=u32x4{0,0,0,0}; }
    unsigned a0b=fr*128+((g^(fr&7))*16);
    const int r0=8*g+tq;
    unsigned bbb=r0*128+(((2*hd)^wswz(r0))*32)+8*tp;
    unsigned kA=AUX+(fr^3)*KVP_+w*64+(g&1)*16-(g>>1)*256, kB=AUX+(20+4*g-tq)*KVP_+w*64+8*tp;
    const int cch=127-4*idx-g;
    unsigned kvo=(8*w+tq)*32768+(cch>>6)*16384+((cch>>4)&3)*2048+(cch&15)*64+((tp^((cch>>2)&3))*16);
    unsigned sho=(xcd*128+32*w+fr)*8; const unsigned ctl=CT_OFF+4*w;
    u32x4 sel;
    { const unsigned b2=(g>>1)? 0x0302u : 0x0100u, sv=(fr&1)? (b2<<16|0x0c0cu) : (0x0c0c0000u|b2);
      #pragma unroll
      for(int i=0;i<4;i++) sel[i]= (fr>>1)==(g&1)*4+i ? sv : 0x0c0c0c0cu; }
    unsigned msk=0, lx=w*1024+l*16;
    #pragma unroll
    for(int e=0;e<4;e++){ const int d=4*g+e-fr; const unsigned pv=d<9, hv=par? pv : (unsigned)(d<1 || 4*g+e>7);
      msk|=((unsigned)(d+7)>7u?1u:0u)<<e | pv<<(4+e) | hv<<(8+e) | (d<=0?1u:0u)<<(12+e); }
    msk|=(unsigned)(g? l-16 : 48+((l-1)&15))<<26;
    const unsigned hb=__builtin_amdgcn_readfirstlane(rb==0 ? 0xcfa8965fu : 0u);
    unsigned ap=AUX+w*CV_W+(tq&1)*(CV_CP-4)+8+32*g+4*tq+8*tp;
    u32x4 bp;
    #pragma unroll
    for(int b=0;b<4;b++){ const int c=15+4*b-fr+g; bp[b]=c*64+((w^((c>>2)&3))*16); }
    asm volatile("" : "+v"(a0b), "+v"(bbb), "+v"(kA), "+v"(kB), "+v"(kvo), "+v"(sho), "+v"(msk), "+v"(lx), "+v"(ap), "+v"(bp), "+v"(sel));
    ARGS();
    const char* ow=ws+loc((rb*16+hp)*32768)+(par*2+hd)*1024+4096;
    asm volatile("" : "+s"(shk));
    asm volatile(C_ASM : "+{v52}"(a0b), "+{v54}"(bbb), "+{v0}"(kA), "+{v1}"(kB), "+{v2}"(kvo), "+{v51}"(sho), "+{v3}"(msk), "+{v4}"(lx), "+{v5}"(ap), "+{v[6:9]}"(bp), "+{v[14:17]}"(sel)
       : "s"(shk), "s"(ctl), "s"(w), "s"(ow), "s"(hb), "s"(0.17677669529663687f*1.44269504088896341f), "{s[92:93]}"(ws+loc(tm*16*32768)), "s"(korder()),
         "s"(ws+loc(flo(1,rb*16))+hp*4), "s"(AUX+w*(CV_W+64)), "s"(ws+loc(flo(1,tm*16+tn))),
         "s"(out+(size_t)(tm*128+w*32+1)*2048+1024+tn*64), "s"(8192+w*4096), "s"(ws+loc(WS_KV+xcd*32*32768)), "s"(ws+loc(flo(0,xcd*32))+idx*4), "s"(l1m), "s"(xcd==0 && idx<5 && w<3? (u64)(ws+WS_GEN+idx*3+w) : 0ull) : "memory", "scc", "vcc", "s68", "s69", "s70", "s71", "s74", "s75", "s76", "s77", "s88", "s89", "s90", "s91",
       C_CLOB);
    TSDECL();
    TS(20); TSDUMP();
  }
  return;
  }
  {
    ARGS0(); BLOCK(); constexpr int WB=0; (void)WB; LANES(); TSDECL();
    const unsigned vx=(l>>3)*4096+(((l&7)^(l>>3))*16), vxs=vx+(s0+32*w)*2048, vw=(l>>3)*6144+((((l&7)>>1)^((l>>4)&1))*32)+(l&1)*16+hp*128+w*98304, vw1=(vw^64)+49152-1024;
    u32x8 va; u32x2 vb; unsigned vsp;
    #pragma unroll
    for(int i=0;i<6;i++) va[i]=((i&1)? vw1 : vw)+(i>>1)*2048;
    va[6]=vxs; va[7]=vxs+32768-1024; vb[0]=vxs+2048; vb[1]=vxs+2048+32768-1024;
    vsp=vx+(rb? s0-16+w : 0)*2048;
    const unsigned sw1=w*1024, sw2=w*2048;
    unsigned rot=ROTF(rbb);
    asm volatile("" : "+v"(va), "+v"(vb), "+v"(vsp));
    asm volatile("" : "+s"(rot));
    unsigned vt=(w&1)? (xr*512+idx*16+(l>>1))*2048+(l&1)*1024 : (2*idx+(l&1))*6144+((l>>1)&3)*2048+xc*1024;
    const int kt0=idx*64; int kvs=KS0+KVSEL;
    unsigned kvl=(kt0+16*w+g)*2048+xcd*256+fr*16, l16=l*16;
    const bool hl= w==0 && kt0;
    unsigned c46=sg(w<2? 128u : w==2? 393216u : 0u), c49=sg(w<2? sw1 : (unsigned)ALL_LDS-96), c64=sg(hl? 8192u : 0u), c68=sg(hl? AUX : ALL_LDS-96);
    unsigned long long c50= w<2? ~0ull : w==2? 63ull : 1ull, c66= hl? ~0ull : 1ull;
    asm volatile("" : "+v"(vt), "+v"(kvl), "+v"(l16));
    asm volatile("" : "+s"(c46), "+s"(c49), "+s"(c64), "+s"(c68), "+s"(c50), "+s"(c66), "+s"(kvs));
    ARGS();
    TSB(16);
    asm volatile(H0_ASM : "+{s[40:41]}"(x), "+{s[42:43]}"(wqkv) : "{v[0:7]}"(va), "{v[8:9]}"(vb), "{v10}"(vsp), "{s52}"(rot), "{s53}"(sw2), "{s59}"(w)
      : "memory", "scc", "s76", "s77", "s78", "s79", "s82", "s83");
    const u16* spb= w==2? wqkv : x;
    const int hl3=(3-(int)((size_t)spb>>7))&7, roth=ROTF(hl3), xs=((hl3-ROTF(0))&7)-LEAD;
    unsigned i11=sg((w==2? roth+LEAD+1 : rot+1)&15), sbo, sst;
    if(w>=2){
      vsp=vt+hl3*128;
      touch(spb, vsp+((roth+(l>>3))&15)*c46, w==2? 0x3f3f3f3f3f3f3f3full>>(56-8*LEAD) : xs<=1? 0xffffffffull : 1ull);
    }
    TSB(17);
    unsigned gof=(((gi&15)*128+64*(w&1)+4*(w>>1)+16*((l>>5)&1)+((l>>3)&3))*DM+(gi>>4)*64)*2+(l16&127);
    unsigned wv[2];
    wv[0]=(((l>>5)*16+w*4+((l>>3)&3))*DM+tn*64)*2+((l16&127)^((l&16)*2)^((w&1)*64)); wv[1]=wv[0]+32*DM*2;
    unsigned zpa=(w>>1)*9216+(w&1)*512+l16, hda=AUX+w*CV_W+(l&1)*CV_CP+(l>>1)*16, yra=AUX+tq*(CV_W+64)+(g>>1)*128+(g&1)*32+tp*8+w*1024;
    const unsigned f0=loc(flo(0,cb)), f1=loc(flo(1,tm*16+tn)), f2=loc(flo(2,gi));
    unsigned vc=(w&1)? (g==0? WS_GEN : (g==1? f0 : g==2? f1 : f2)^((l&1)<<13)) : idx*CODE_ST+fr*128u;
    unsigned kfo=f0+(l&31)*4, ofo= w==0? f1+fr*4 : WS_GEN;
    asm volatile("" : "+v"(kfo), "+v"(ofo), "+v"(gof), "+v"(wv[0]), "+v"(wv[1]), "+v"(hda), "+v"(yra), "+v"(zpa), "+v"(vc));
    unsigned long long kt=korder();
    asm volatile("" : "+s"(kt));
#define HDBG_O
#define HDBG_I
#define H_IN "{v[0:7]}"(va), "{v[8:9]}"(vb), "{v10}"(vsp), "{v11}"(kvl), "{s[40:41]}"(x), "{s[42:43]}"(wqkv), "{s[44:45]}"(spb), "{s46}"(c46), "{s49}"(c49), "{s[50:51]}"(c50), "{s53}"(sw2), \
      "{s58}"(kvs), "{s59}"(w), "{s[60:61]}"(kin), "{s[62:63]}"(vin), "{s64}"(c64), "{s[66:67]}"(c66), "{s68}"(c68) HDBG_I
#define H_CLOB "memory", "scc", "vcc", "s76", "s77", "s78", "s79", "s80", "s81", "s82", "s83", "s84", "s85", "v20", "v21", "v22", "v253", "v254", "v255"
    asm volatile(H1_ASM : "+{s47}"(i11), "+{s52}"(rot), "={s54}"(sbo), "={s55}"(sst) HDBG_O : H_IN : H_CLOB);
    const u16* hrow=hk+(size_t)(cb*4+w)*S;
    u32x4 h0; unsigned dvu;
    asm volatile("s_load_dwordx4 %0, %2, 0\n\ts_load_dword %1, %3, %4" : "=&s"(h0), "=&s"(dvu) : "s"(hrow), "s"(sD), "s"(cb*8+(w&2)*2) : "memory");
    unsigned long long pc; asm volatile("s_getpc_b64 %0" : "=s"(pc)); pc&=~4095ull;
    asm volatile("" : "+s"(wo), "+s"(qin), "+s"(out), "+s"(ws));
    __builtin_amdgcn_s_setprio(HP1);
    unsigned kf2=kfo;
    asm volatile(H_ASM : "+{v16}"(kf2), "+{s47}"(i11), "+{s52}"(rot), "+{s54}"(sbo), "+{s55}"(sst) HDBG_O
      : H_IN, "{v14}"(vc), "{v13}"(WS_GEN+256+(l&31)*4), "{s56}"(sg(3u<<(kvs-1) | 1u<<15)),
        "{s57}"(sg(((w&1)? (1u<<TGS | 3u<<PLS | 1u<<14 | (xs>1? 8u : 0u)) : 1u<<CPS)&~(unsigned)(c50>>32))), "{s[70:71]}"(ws), "{s[72:73]}"(pc) : H_CLOB);
    asm volatile("s_waitcnt lgkmcnt(0)" : "+s"(h0), "+s"(dvu) :: "memory");
    asm volatile("" : "+s"(bid), "+s"(w)); __builtin_assume(bid>=0 && bid<256 && w>=0 && w<4);
    { DERIVE(); const unsigned sw1=w*1024, sw2=w*2048;
    u32x4 qv[4];
    BAR(); TSB(14);
    __builtin_amdgcn_s_setprio(HP2);
    { const unsigned fa=AUX+w*CV_W+32;
      asm volatile("s_mov_b64 exec, -2\n\ts_add_u32 m0, %3, %4\n\ts_nop 0\n\tglobal_load_lds_dwordx4 %0, %1" LDPOL "\n\ts_mov_b32 m0, %3\n\ts_nop 0\n\tglobal_load_lds_dwordx4 %0, %2" LDPOL "\n\ts_mov_b64 exec, -1"
        :: "v"(l16), "s"(hrow), "s"(hrow-2), "s"(fa), "n"(CV_CP) : "memory", "scc");
      #pragma unroll
      for(int i=1;i<4;i++){ gldsn(hrow+i*512, l16, fa, CV_CP+i*1024); gldsn(hrow-2+i*512, l16, fa, i*1024); } }
    WAITV(8);
    vct[32+w]=1u;
    TSH(6);
    const unsigned tag=__builtin_amdgcn_readfirstlane(vct[26]); const bool kvok=__builtin_amdgcn_readfirstlane(vct[5])!=0;
    { const unsigned pl=__builtin_amdgcn_readfirstlane(vct[6]); cpar=(xcd>>2)^(pl>>13); kfo^=pl; if(w==0) ofo^=pl; }
    u32x4 hv;
    { const unsigned d0=(h0[0]&0xffff0000u)|(pack2(bflo(h0[0])+((w&1)? bfhi(dvu) : bflo(dvu)),0.f)&0xffffu);
      hv= l==4? u32x4{0,d0,h0[1],h0[2]} : l==5? u32x4{d0,h0[1],h0[2],h0[3]} : u32x4{0,0,0,0}; }
    asm volatile("s_mov_b64 exec, 63\n\tds_write_b128 %0, %1\n\ts_mov_b64 exec, %3\n\tds_write_b128 %2, %1 offset:7680\n\ts_mov_b64 exec, -1"
      :: "v"(hda), "v"(hv), "v"(zpa), "s"(0xffffffff00000000ull) : "memory");
#define QLD() asm volatile("global_load_dwordx4 %0, %4, %5" LDPOL "\n\tglobal_load_dwordx4 %1, %4, %6" LDPOL "\n\tglobal_load_dwordx4 %2, %4, %7" LDPOL "\n\tglobal_load_dwordx4 %3, %4, %8" LDPOL \
        : "=&v"(qv[0]), "=&v"(qv[1]), "=&v"(qv[2]), "=&v"(qv[3]) : "v"(gof), "s"(qin), "s"(qin+8*DM), "s"(qin+32*DM), "s"(qin+40*DM) : "memory")
    auto kvt=[&](){
      #pragma unroll
      for(int i=0;i<4;i++) gldsc(ws+loc(WS_KV+cb*32768)+(w>>1)*16384+(w&1)*4096+i*1024, l16, sw2, (i&1)*9216+(i>>1)*1024); };
    if(!kvok) wait_flag((const unsigned*)(ws+kfo), tag);
    kvt();
    TSH(7);
    auto wos=[&](int st){ const u16* b=wo+(size_t)((kt>>(4*st))&15)*64*DM; glds(b,wv[0],sw1,WOB+st*8192); glds(b,wv[1],sw1,WOB+st*8192+4096); };
    wos(0); wos(1);
    while(!all4(36)) __builtin_amdgcn_s_sleep(1);
    #pragma unroll
    for(int st=2;st<NE;st++) wos(st);
    WAITV(2*NE); WAITL();
    vct[16+w]=1u;
    TSH(8);
    __builtin_amdgcn_s_setprio(HP4);
    if(GATE==1) while(!all4(28)) __builtin_amdgcn_s_sleep(1);
    if(GATE==2) while(vct[4]<4u) __builtin_amdgcn_s_sleep(1);
    TSF(24);
    QLD();
    #pragma unroll
    for(int st=NE;st<13;st++){ if(GATE==0 && st==7) while(!all4(0)) __builtin_amdgcn_s_sleep(1);
      WAITV(W1); TSF(24+st);
      if(st==EP+W1/2+1 || st==EP2+W1/2+1)
        asm volatile("s_cmp_lg_u32 %0, 0\n\ts_cbranch_scc1 1f\n\tv_cmp_ne_u32 vcc, %1, v253\n\tv_mov_b32 v253, %3\n\ts_nop 1\n\ts_and_b32 vcc_lo, vcc_lo, %2\n\ts_cbranch_scc1 1f\n\tv_mov_b32 v254, 1\n\tds_max_u32 v253, v254\n1:"
          :: "s"(w), "s"(tag), "s"(l1m), "n"(CT_OFF+96) : "memory", "scc", "vcc", "v253", "v254");
      if(st==EP || st==EP2) asm volatile("global_load_dword v253, %0, %1 sc1" :: "v"(ofo), "s"(ws) : "memory", "v253");
      wos(st); if(st==7+W1/2) vct[20+w]=7u; }
    unsigned op;
    asm volatile("global_load_dword %0, %1, %2 sc1" : "=v"(op) : "v"(ofo), "s"(ws) : "memory");
    WAITV(1); vct[20+w]=13u;
    TSH(9);
    asm volatile("s_waitcnt vmcnt(0)" : "+v"(qv[0]), "+v"(qv[1]), "+v"(qv[2]), "+v"(qv[3]), "+v"(op) :: "memory");
    unsigned oseen=0; bool ptch=false;
    auto olevel=[&](unsigned v){
      const unsigned ne=(unsigned)__ballot(v!=tag), lv= ne==0 ? 2u : (ne&l1m)==0 ? 1u : 0u;
      if(lv>oseen){ if(oseen==0){ TSH(10); } oseen=lv; if(l==0) __hip_atomic_fetch_max(ct+24, lv, __ATOMIC_RELAXED, __HIP_MEMORY_SCOPE_WORKGROUP); }
      if(PTCH && !ptch && !((ne>>(hp^8))&1)){ ptch=true; const char* b=ws+loc((tm*16+(hp^8))*32768);
        asm volatile("global_load_dword v255, %0, %1 sc1\n\tglobal_load_dword v255, %0, %2 sc1" :: "v"(l16*8), "s"(b), "s"(b+16384) : "memory", "v255"); } };
    auto opoll=[&](){ olevel(__hip_atomic_load((const unsigned*)(ws+ofo), __ATOMIC_RELAXED, __HIP_MEMORY_SCOPE_AGENT)); };
    if(w==0) olevel(op);
    float qf[32];
    auto qun=[&](){
    #pragma unroll
    for(int i=0;i<16;i++){ qf[2*i]=bflo(qv[i>>2][i&3]); qf[2*i+1]=bfhi(qv[i>>2][i&3]); }
    #pragma unroll
    for(int i=0;i<32;i+=8) asm volatile("" : "+v"(qf[i]), "+v"(qf[i+1]), "+v"(qf[i+2]), "+v"(qf[i+3]), "+v"(qf[i+4]), "+v"(qf[i+5]), "+v"(qf[i+6]), "+v"(qf[i+7]));
    };
    qun();
    auto wol=[&](int i){ glds(wo+(size_t)((kt>>(52+4*(i>>1)))&15)*64*DM, wv[i&1], sw1, i<4? AUX+4352+i*CV_W : (i&1)*4096); };
    while(!all4(44)){ if(w==0 && oseen<1) opoll(); else __builtin_amdgcn_s_sleep(1); }
    wol(0); wol(1); wol(2); wol(3);
    while(!all4(48)){ if(w==0 && oseen<1) opoll(); else __builtin_amdgcn_s_sleep(1); }
    wol(4); wol(5);
    __builtin_amdgcn_s_setprio(HP3);
    bool gen= !(bid==0 && w==1);
    auto trygen=[&](){
      unsigned v[4];
      #pragma unroll
      for(int i=0;i<4;i++) v[i]=__hip_atomic_load((const unsigned*)(ws+(loc(flo(0,i*64))^((i>>1)<<13))+(l>>5)*4096+(l&31)*4), __ATOMIC_RELAXED, __HIP_MEMORY_SCOPE_AGENT);
      if(__all(v[0]==tag && v[1]==tag && v[2]==tag && v[3]==tag)){ if(l==0) st4c(ws+WS_GEN, tag); gen=true; } };
    if(!gen) trygen();
    while(!((all4(8)>>w)&1)){
      if(w==0 && oseen<2) opoll();
      __builtin_amdgcn_s_sleep(OPOLLS);
    }
    TSH(11);
    __builtin_amdgcn_s_setprio(HP5);
    {
      const char* yb=smem+yra;
      s16x4 ya[4], yc[4];
      #pragma unroll
      for(int i=0;i<4;i++){ ya[i]=trread(yb+(i>>1)*512+(i&1)*64); yc[i]=trread(yb+(i>>1)*512+(i&1)*64+256); }
      #pragma unroll
      for(int i=0;i<4;i++) if(NOY!=1) st16c(ws+loc(WS_Y+(cb&~15)*32768)+(w*4+i)*32768+((cb>>3)&1)*16384+(cb&7)*1024, l16, __builtin_bit_cast(u32x4,__builtin_shufflevector(ya[i],yc[i],0,1,2,3,4,5,6,7)));
    }
    TSH(12);
    if(NOY){ WAITV(0); vct[20+w]=16u; for(int sp_=0; !gen && sp_<(1<<22); sp_++) trygen(); TS(20); TSDUMP(); return; }
    unsigned po=gof+(gof&~2047u);
    const char* p0=ws+loc(WS_Y+gi*32768);
    unsigned y0=g*1024+w*256+((fr^(g&2))*16);
    asm volatile("" : "+v"(y0), "+v"(po));
    WAITV(0);
    vct[20+w]=16u;
    if(l<4) st4c(ws+loc(flo(2,cb&~15))+(w*4+l)*128+(cb&15)*4, tag);
    TSH(13);
    if(w==0){ int sp_=0; while(oseen<2 && vct[24]<2u && ++sp_<(1<<22)){ opoll(); __builtin_amdgcn_s_sleep(8); } }
    if(w==0){
      const unsigned* fp=(const unsigned*)(ws+loc(flo(2,gi)))+(l&15); int spins=0;
      while(vct[27]==0u) __builtin_amdgcn_s_sleep(2);
      while(true){
        unsigned v=__hip_atomic_load(fp, __ATOMIC_RELAXED, __HIP_MEMORY_SCOPE_AGENT);
        if(__all(v==tag) || ++spins>(1<<20)) break;
        __builtin_amdgcn_s_sleep(POLLS);
      }
      { u32x4 v; do{ v=*(volatile LDS_AS u32x4*)(ct+20); }while(__builtin_amdgcn_readfirstlane(v[0]&v[1]&v[2]&v[3]&16u)==0); }
      vct[25]=1u;
    } else { while(vct[25]==0u) __builtin_amdgcn_s_sleep(2); }
    TSH(14);
    {
      u32x4 yv[4];
      #pragma unroll
      for(int i=0;i<4;i++) gldsc(p0+(i>>1)*16384+(i&1)*4096, y0^(i*64), w*CV_W, AUX+i*1024);
      const char* ys=smem+AUX+w*CV_W+(l&7)*512;
      const int yi=((l>>3)^((l&7)*2))*16;
      WAITV(0);
      yv[0]=*(const u32x4*)(ys+yi); yv[1]=*(const u32x4*)(ys+yi+256); yv[2]=*(const u32x4*)(ys+(yi^128)); yv[3]=*(const u32x4*)(ys+(yi^128)+256);
      TSH(15);
      #pragma unroll
      for(int r=0;r<4;r++){
        u32x4 ov;
        #pragma unroll
        for(int e=0;e<4;e++){ const unsigned y2=yv[(r>>1)*2+(e>>1)][(r&1)*2+(e&1)]; ov[e]=pack2(qf[r*8+2*e]*bflo(y2), qf[r*8+2*e+1]*bfhi(y2)); }
        stos(out+((r&1)*8+(r>>1)*32)*2048, po, ov);
      }
    }
    for(int sp_=0; !gen && sp_<(1<<22); sp_++) trygen();
    TS(20); TSDUMP();
    }
  }

}

extern "C" void kernel_launch(void* const* d_in, const int* in_sizes, int n_in, void* d_out, int out_size, void* d_ws, size_t ws_size, hipStream_t stream){
  const u16* q=(const u16*)d_in[0]; const u16* k=(const u16*)d_in[1]; const u16* v=(const u16*)d_in[2];
  const u16* shk=(const u16*)d_in[3]; const u16* hk=(const u16*)d_in[4]; const u16* sD=(const u16*)d_in[5];
  const u16* x=(const u16*)d_in[6]; const u16* wqkv=(const u16*)d_in[7]; const u16* wo=(const u16*)d_in[8];
  u16* ws=(u16*)(((size_t)d_ws+16383)&~(size_t)16383);
  fused_kernel<<<256,512,0,stream>>>(x,wqkv,k,v,shk,hk,wo,q,sD,(u16*)d_out,ws,ws);
}
```

```cpp
#include <hip/hip_runtime.h>
#include <hip/hip_bf16.h>
#include <stdint.h>
#define DBG_OFF (28u<<20)
#define SHOWC

typedef unsigned short u16;
typedef short s16x4 __attribute__((ext_vector_type(4)));
typedef float f32x32 __attribute__((ext_vector_type(32)));
typedef unsigned u32x4 __attribute__((ext_vector_type(4)));
typedef unsigned u32x2 __attribute__((ext_vector_type(2)));
#define LDS_AS __attribute__((address_space(3)))

__device__ __forceinline__ float bflo(unsigned x){ return __uint_as_float(x<<16); }
__device__ __forceinline__ float bfhi(unsigned x){ return __uint_as_float(x&0xffff0000u); }
typedef float f32x2 __attribute__((ext_vector_type(2)));
typedef __bf16 bf16x2_t __attribute__((ext_vector_type(2)));
__device__ __forceinline__ unsigned pack2(float lo, float hi){ return __builtin_bit_cast(unsigned, __builtin_convertvector(f32x2{lo,hi}, bf16x2_t)); }

#define GLDS(name,pol) __device__ __forceinline__ void name(const void* sb, unsigned voff, unsigned ldsb, int imm){ \
  asm volatile("s_add_u32 m0, %2, %3\n\ts_nop 0\n\tglobal_load_lds_dwordx4 %0, %1" pol :: "v"(voff), "s"(sb), "s"(ldsb), "n"(imm) : "memory", "scc"); }
#define LDPOL " nt"
GLDS(glds,"") GLDS(gldsn,LDPOL) GLDS(gldsc," sc1")
#define PACE 63
__device__ __forceinline__ void gldsp(const void* sb, unsigned voff, unsigned ldsb, int imm){
  asm volatile("s_add_u32 m0, %2, %3\n\ts_waitcnt vmcnt(%4)\n\tglobal_load_lds_dwordx4 %0, %1" :: "v"(voff), "s"(sb), "s"(ldsb), "n"(imm), "n"(PACE) : "memory", "scc"); }
__device__ __forceinline__ void touch(const void* sb, unsigned voff, unsigned long long mask){
  asm volatile("s_mov_b64 exec, %2\n\tglobal_load_dword v255, %0, %1\n\ts_mov_b64 exec, -1" :: "v"(voff), "s"(sb), "s"(mask) : "memory", "v255"); }
__device__ __forceinline__ void st16c(void* sb, unsigned vo, u32x4 v){ asm volatile("global_store_dwordx4 %0, %1, %2 sc1\n\ts_nop 1" :: "v"(vo), "v"(v), "s"(sb) : "memory"); }
#define OUTPOL " sc1 nt"
__device__ __forceinline__ void stos(void* sb, unsigned vo, u32x4 v){ asm volatile("global_store_dwordx4 %0, %1, %2" OUTPOL "\n\ts_nop 1" :: "v"(vo), "v"(v), "s"(sb) : "memory"); }
__device__ __forceinline__ void stoa(void* p, u32x4 v){ asm volatile("global_store_dwordx4 %0, %1, off" OUTPOL "\n\ts_nop 1" :: "v"(p), "v"(v) : "memory"); }
__device__ __forceinline__ void st4c(void* p, unsigned v){ asm volatile("global_store_dword %0, %1, off sc1" :: "v"(p), "v"(v) : "memory"); }
__device__ __forceinline__ void wait_flag(const unsigned* p, unsigned tag){
  int spins=0;
  while(true){
    unsigned v=__hip_atomic_load(p, __ATOMIC_RELAXED, __HIP_MEMORY_SCOPE_AGENT);
    if(__all(v==tag)) break;
    __builtin_amdgcn_s_sleep(2);
    if(++spins>(1<<22)) break;
  }
}
#define WAITV(n) asm volatile("s_waitcnt vmcnt(%0)"::"n"(n):"memory")
#define WAITL() asm volatile("s_waitcnt lgkmcnt(0)":::"memory")
#define BAR() do{ asm volatile("s_waitcnt lgkmcnt(0)":::"memory"); __builtin_amdgcn_s_barrier(); asm volatile("":::"memory"); }while(0)
#define LANES() int l; asm volatile("v_mbcnt_lo_u32_b32 %0, -1, 0\n\tv_mbcnt_hi_u32_b32 %0, -1, %0" : "=v"(l)); __builtin_assume(l>=0 && l<64); const int g=l>>4, fr=l&15, tq=(l&15)>>2, tp=l&3; (void)g; (void)fr; (void)tq; (void)tp;

__device__ __forceinline__ s16x4 trread(const char* p){ return __builtin_amdgcn_ds_read_tr16_b64_v4i16((LDS_AS s16x4*)p); }
__device__ __forceinline__ int wswz(int row){ return ((row>>1)&1) | (((row>>3)&1)<<1); }
__device__ __forceinline__ u32x4 widen(u32x2 a, u32x2 b){
  auto r0=__builtin_amdgcn_permlane16_swap(a[0],b[0],false,false);
  auto r1=__builtin_amdgcn_permlane16_swap(a[1],b[1],false,false);
  return u32x4{r0[0],r1[0],r0[1],r1[1]};
}

constexpr int S=2048, DM=1024;
constexpr int K1_XB=144*128, K1_WB=3*64*128, K1_ST=K1_XB+K1_WB;
constexpr int AUX=3*K1_ST, KVP_=1040;
constexpr int WOB=22528, CV_W=8448, CV_CP=4224;
constexpr int CT_OFF=AUX+33*KVP_;
constexpr int ALL_LDS=CT_OFF+208+96;
constexpr unsigned WS_KV=8u<<20, WS_Y=16u<<20, WS_FL=24u<<20, WS_GEN=WS_FL+(1u<<20);
__device__ __forceinline__ unsigned flo(int kind, int c){ return WS_FL+kind*65536+(c>>6)*16384+(c&63)*128; }

#define CPRIO 2
#define CODE_ST 1610
#define LEAD 3
#define HP3 3
#define HP2 3
#define HP4 3
#define QP 1
#define TNX 1
#define HWS 99
#define HP1 0
#define HUNR 15
#define NE 2
#define WOC 0
#define NS 4
#define PTCH 1
#define GATE 0
#define WRD 2
#define EP 99
#define EP2 99
#define HP5 3
#define NOWO 0
#define W0 6
#define OPD 8
#define WT0 99
#define W1 2
#define W2 2
#define W3 4
#define OPOLLS 1
#define POLLS 3
#define HPRE 0
#define CPS2 99
#define KR ((rbb+2*ra)%7)
#define NOY 0
#define YLDS 1
#define OLDS 0
#define KVSEL (rbb==3? 0 : idx%KSN)
#define KS0 1
#define KSN 3
#define KST 10
#define KVP 5
#define TGS 2
#define CPS 13
#define PLS 4
#define H0_ASM \
  "s_mul_i32 s76, s52, 0x60000\n\t" \
  "s_add_u32 s76, s42, s76\n\t" \
  "s_addc_u32 s77, s43, 0\n\t" \
  "s_add_u32 s82, s53, 86016\n\t" \
  "s_add_u32 m0, s82, 18432\n\t" \
  "s_lshl_b32 s78, s52, 7\n\t" \
  "global_load_lds_dwordx4 v0, s[76:77]\n\t" \
  "global_load_lds_dwordx4 v1, s[76:77] offset:1024\n\t" \
  "s_add_u32 s78, s40, s78\n\t" \
  "s_addc_u32 s79, s41, 0\n\t" \
  "s_add_u32 m0, s82, 2048\n\t" \
  "s_nop 0\n\t" \
  "global_load_lds_dwordx4 v6, s[78:79]\n\t" \
  "global_load_lds_dwordx4 v7, s[78:79] offset:1024\n\t" \
  "s_add_u32 m0, s82, 26624\n\t" \
  "s_nop 0\n\t" \
  "global_load_lds_dwordx4 v2, s[76:77]\n\t" \
  "global_load_lds_dwordx4 v3, s[76:77] offset:1024\n\t" \
  "s_add_u32 m0, s82, 10240\n\t" \
  "s_nop 0\n\t" \
  "global_load_lds_dwordx4 v8, s[78:79]\n\t" \
  "global_load_lds_dwordx4 v9, s[78:79] offset:1024\n\t" \
  "s_add_u32 m0, s82, 34816\n\t" \
  "s_nop 0\n\t" \
  "global_load_lds_dwordx4 v4, s[76:77]\n\t" \
  "global_load_lds_dwordx4 v5, s[76:77] offset:1024\n\t" \
  "s_cmp_gt_u32 s59, 1\n\t" \
  "s_cbranch_scc1 1f\n\t" \
  "s_lshr_b32 s83, s53, 1\n\t" \
  "s_add_u32 m0, s83, 86016\n\t" \
  "s_nop 0\n\t" \
  "global_load_lds_dwordx4 v10, s[78:79]\n\t" \
  "1:\n\t"
#define H1_ASM \
  "s_mov_b32 s55, 0\n\t" \
  "s_mov_b32 s54, 0\n\t" \
  "s_add_u32 s52, s52, 1\n\t" \
  "s_and_b32 s52, s52, 15\n\t" \
  "s_cmp_eq_u32 s58, 1\n\t" \
  "s_cbranch_scc1 1f\n\t" \
  "s_mul_i32 s76, s52, 0x60000\n\t" \
  "s_add_u32 s76, s42, s76\n\t" \
  "s_addc_u32 s77, s43, 0\n\t" \
  "s_lshl_b32 s78, s52, 7\n\t" \
  "s_add_u32 s78, s40, s78\n\t" \
  "s_addc_u32 s79, s41, 0\n\t" \
  "s_mul_i32 s80, s47, s46\n\t" \
  "s_add_u32 s80, s44, s80\n\t" \
  "s_addc_u32 s81, s45, 0\n\t" \
  "s_mov_b32 s82, s53\n\t" \
  "s_add_u32 m0, s82, 18432\n\t" \
  "s_add_u32 s52, s52, 1\n\t" \
  "global_load_lds_dwordx4 v0, s[76:77]\n\t" \
  "global_load_lds_dwordx4 v1, s[76:77] offset:1024\n\t" \
  "s_add_u32 m0, s82, 2048\n\t" \
  "s_and_b32 s52, s52, 15\n\t" \
  "global_load_lds_dwordx4 v6, s[78:79]\n\t" \
  "global_load_lds_dwordx4 v7, s[78:79] offset:1024\n\t" \
  "s_add_u32 m0, s82, 26624\n\t" \
  "s_add_u32 s47, s47, 1\n\t" \
  "global_load_lds_dwordx4 v2, s[76:77]\n\t" \
  "global_load_lds_dwordx4 v3, s[76:77] offset:1024\n\t" \
  "s_add_u32 m0, s82, 10240\n\t" \
  "s_and_b32 s47, s47, 15\n\t" \
  "global_load_lds_dwordx4 v8, s[78:79]\n\t" \
  "global_load_lds_dwordx4 v9, s[78:79] offset:1024\n\t" \
  "s_add_u32 m0, s82, 34816\n\t" \
  "s_and_b32 s83, s54, s51\n\t" \
  "global_load_lds_dwordx4 v4, s[76:77]\n\t" \
  "global_load_lds_dwordx4 v5, s[76:77] offset:1024\n\t" \
  "s_add_u32 m0, s83, s49\n\t" \
  "s_mov_b64 exec, s[50:51]\n\t" \
  "global_load_lds_dwordx4 v10, s[80:81]\n\t" \
  "s_mov_b64 exec, -1\n\t" \
  "s_mov_b32 s55, 1\n\t" \
  "s_mov_b32 s54, 43008\n\t" \
  "s_waitcnt vmcnt(11)\n\t" \
  "s_branch 2f\n\t" \
  "1:\n\t" \
  "s_mul_i32 s83, s59, 4160\n\t" \
  "v_add_u32 v20, 7152, v11\n\t" \
  "v_add_u32 v21, 14304, v11\n\t" \
  "v_add_u32 v22, 21456, v11\n\t" \
  "s_add_u32 m0, s83, 130064\n\t" \
  "s_nop 0\n\t" \
  "global_load_lds_dwordx4 v11, s[60:61]" LDPOL "\n\t" \
  "global_load_lds_dwordx4 v20, s[60:61] offset:1040" LDPOL "\n\t" \
  "global_load_lds_dwordx4 v21, s[60:61] offset:2080" LDPOL "\n\t" \
  "global_load_lds_dwordx4 v22, s[60:61] offset:3120" LDPOL "\n\t" \
  "s_add_u32 m0, s83, 146704\n\t" \
  "s_nop 0\n\t" \
  "global_load_lds_dwordx4 v11, s[62:63]" LDPOL "\n\t" \
  "global_load_lds_dwordx4 v20, s[62:63] offset:1040" LDPOL "\n\t" \
  "global_load_lds_dwordx4 v21, s[62:63] offset:2080" LDPOL "\n\t" \
  "global_load_lds_dwordx4 v22, s[62:63] offset:3120" LDPOL "\n\t" \
  "v_subrev_u32 v20, s64, v11\n\t" \
  "s_mov_b32 m0, s68\n\t" \
  "s_mov_b64 exec, s[66:67]\n\t" \
  "global_load_lds_dwordx4 v20, s[60:61]" LDPOL "\n\t" \
  "s_mov_b64 exec, -1\n\t" \
  "s_waitcnt vmcnt(9)\n\t" \
  "2:\n\t" \
  "s_waitcnt lgkmcnt(0)\n\t" \
  "s_barrier\n\t"
#define H_ASM \
  "s_mul_i32 s76, s52, 0x60000\n\t" \
  "s_add_u32 s76, s42, s76\n\t" \
  "s_addc_u32 s77, s43, 0\n\t" \
  "s_lshl_b32 s78, s52, 7\n\t" \
  "s_add_u32 s78, s40, s78\n\t" \
  "s_addc_u32 s79, s41, 0\n\t" \
  "s_mul_i32 s80, s47, s46\n\t" \
  "s_add_u32 s80, s44, s80\n\t" \
  "s_addc_u32 s81, s45, 0\n\t" \
  "s_add_u32 s82, s54, s53\n\t" \
  "s_cmp_eq_u32 s58, 2\n\t" \
  "s_cbranch_scc1 5f\n\t" \
  "s_branch 3f\n\t" \
  "1:\n\t" \
  "s_waitcnt vmcnt(11)\n\t" \
  "2:\n\t" \
  "s_barrier\n\t" \
  "3:\n\t" \
  "s_add_u32 s55, s55, 1\n\t" \
  "s_add_u32 m0, s82, 18432\n\t" \
  "s_add_u32 s52, s52, 1\n\t" \
  "global_load_lds_dwordx4 v0, s[76:77]\n\t" \
  "global_load_lds_dwordx4 v1, s[76:77] offset:1024\n\t" \
  "s_add_u32 m0, s82, 2048\n\t" \
  "s_and_b32 s52, s52, 15\n\t" \
  "global_load_lds_dwordx4 v6, s[78:79]\n\t" \
  "global_load_lds_dwordx4 v7, s[78:79] offset:1024\n\t" \
  "s_add_u32 m0, s82, 26624\n\t" \
  "s_add_u32 s47, s47, 1\n\t" \
  "global_load_lds_dwordx4 v2, s[76:77]\n\t" \
  "global_load_lds_dwordx4 v3, s[76:77] offset:1024\n\t" \
  "s_add_u32 m0, s82, 10240\n\t" \
  "s_and_b32 s47, s47, 15\n\t" \
  "global_load_lds_dwordx4 v8, s[78:79]\n\t" \
  "global_load_lds_dwordx4 v9, s[78:79] offset:1024\n\t" \
  "s_add_u32 m0, s82, 34816\n\t" \
  "s_and_b32 s83, s54, s51\n\t" \
  "global_load_lds_dwordx4 v4, s[76:77]\n\t" \
  "global_load_lds_dwordx4 v5, s[76:77] offset:1024\n\t" \
  "s_bitcmp1_b32 s57, s55\n\t" \
  "s_cbranch_scc1 10f\n\t" \
  "8:\n\t" \
  "s_add_u32 m0, s83, s49\n\t" \
  "s_mov_b64 exec, s[50:51]\n\t" \
  "global_load_lds_dwordx4 v10, s[80:81]\n\t" \
  "s_mov_b64 exec, -1\n\t" \
  "4:\n\t" \
  "s_mul_i32 s76, s52, 0x60000\n\t" \
  "s_add_u32 s76, s42, s76\n\t" \
  "s_addc_u32 s77, s43, 0\n\t" \
  "s_lshl_b32 s78, s52, 7\n\t" \
  "s_add_u32 s78, s40, s78\n\t" \
  "s_addc_u32 s79, s41, 0\n\t" \
  "s_mul_i32 s80, s47, s46\n\t" \
  "s_add_u32 s80, s44, s80\n\t" \
  "s_addc_u32 s81, s45, 0\n\t" \
  "s_add_u32 s54, s54, 43008\n\t" \
  "s_cmp_eq_u32 s54, 129024\n\t" \
  "s_cselect_b32 s54, 0, s54\n\t" \
  "s_add_u32 s82, s54, s53\n\t" \
  "s_bitcmp0_b32 s56, s55\n\t" \
  "s_cbranch_scc1 1b\n\t" \
  "s_cmp_eq_u32 s55, 15\n\t" \
  "s_cbranch_scc1 19f\n\t" \
  "s_cmp_eq_u32 s55, s58\n\t" \
  "s_cbranch_scc1 3b\n\t" \
  "s_waitcnt vmcnt(11)\n\t" \
  "s_barrier\n\t" \
  "5:\n\t" \
  "s_mul_i32 s83, s59, 4160\n\t" \
  "v_add_u32 v20, 7152, v11\n\t" \
  "v_add_u32 v21, 14304, v11\n\t" \
  "v_add_u32 v22, 21456, v11\n\t" \
  "s_add_u32 m0, s83, 130064\n\t" \
  "s_nop 0\n\t" \
  "global_load_lds_dwordx4 v11, s[60:61]" LDPOL "\n\t" \
  "global_load_lds_dwordx4 v20, s[60:61] offset:1040" LDPOL "\n\t" \
  "global_load_lds_dwordx4 v21, s[60:61] offset:2080" LDPOL "\n\t" \
  "global_load_lds_dwordx4 v22, s[60:61] offset:3120" LDPOL "\n\t" \
  "s_add_u32 m0, s83, 146704\n\t" \
  "s_nop 0\n\t" \
  "global_load_lds_dwordx4 v11, s[62:63]" LDPOL "\n\t" \
  "global_load_lds_dwordx4 v20, s[62:63] offset:1040" LDPOL "\n\t" \
  "global_load_lds_dwordx4 v21, s[62:63] offset:2080" LDPOL "\n\t" \
  "global_load_lds_dwordx4 v22, s[62:63] offset:3120" LDPOL "\n\t" \
  "v_subrev_u32 v20, s64, v11\n\t" \
  "s_mov_b32 m0, s68\n\t" \
  "s_mov_b64 exec, s[66:67]\n\t" \
  "global_load_lds_dwordx4 v20, s[60:61]" LDPOL "\n\t" \
  "s_mov_b64 exec, -1\n\t" \
  "s_waitcnt vmcnt(9)\n\t" \
  "s_branch 2b\n\t" \
  "10:\n\t" \
  "s_cmp_eq_u32 s59, 2\n\t" \
  "s_cbranch_scc0 11f\n\t" \
  "s_mov_b64 exec, 0xffff\n\t" \
  "global_load_dword v255, v14, s[72:73]\n\t" \
  "s_mov_b64 exec, -1\n\t" \
  "s_branch 4b\n\t" \
  "11:\n\t" \
  "s_cmp_eq_u32 s55, 2\n\t" \
  "s_cbranch_scc0 12f\n\t" \
  "global_load_dword v254, v14, s[70:71] sc1\n\t" \
  "s_branch 4b\n\t" \
  "12:\n\t" \
  "s_cmp_eq_u32 s55, 4\n\t" \
  "s_cbranch_scc0 15f\n\t" \
  "global_load_dword v253, v13, s[70:71] sc1\n\t" \
  "s_branch 4b\n\t" \
  "15:\n\t" \
  "s_cmp_eq_u32 s55, 5\n\t" \
  "s_cbranch_scc0 13f\n\t" \
  "v_readfirstlane_b32 s84, v254\n\t" \
  "s_mov_b32 m0, 0x400000\n\t" \
  "s_waitcnt vmcnt(10)\n\t" \
  "s_add_u32 s84, s84, 1\n\t" \
  "v_mov_b32 v254, s84\n\t" \
  "16:\n\t" \
  "v_cmp_eq_u32 vcc, v253, v254\n\t" \
  "s_nop 1\n\t" \
  "s_and_b32 s84, vcc_lo, 0x3fffffff\n\t" \
  "s_bcnt1_i32_b32 s85, s84\n\t" \
  "s_cmp_ge_u32 s85, 15\n\t" \
  "s_cbranch_scc1 17f\n\t" \
  "s_sub_u32 m0, m0, 1\n\t" \
  "s_cbranch_scc1 17f\n\t" \
  "global_load_dword v253, v13, s[70:71] sc1\n\t" \
  "s_waitcnt vmcnt(0)\n\t" \
  "s_branch 16b\n\t" \
  "17:\n\t" \
  "s_and_b32 s84, s84, 0xaaaaaaaa\n\t" \
  "s_bcnt1_i32_b32 s84, s84\n\t" \
  "s_and_b32 s84, s84, 8\n\t" \
  "s_lshl_b32 s84, s84, 10\n\t" \
  "v_xor_b32 v16, s84, v16\n\t" \
  "v_mov_b32 v20, s84\n\t" \
  "v_mov_b32 v253, 163344\n\t" \
  "ds_write_b32 v253, v254 offset:104\n\t" \
  "ds_write_b32 v253, v20 offset:24\n\t" \
  "s_waitcnt lgkmcnt(0)\n\t" \
  "s_branch 8b\n\t" \
  "13:\n\t" \
  "s_cmp_eq_u32 s55, 14\n\t" \
  "s_cbranch_scc0 14f\n\t" \
  "global_load_dword v253, v16, s[70:71] sc1\n\t" \
  "s_branch 4b\n\t" \
  "14:\n\t" \
  "s_mov_b32 exec_hi, 0\n\t" \
  "global_load_dword v255, v10, s[44:45]\n\t" \
  "s_mov_b64 exec, -1\n\t" \
  "s_branch 4b\n\t" \
  "19:\n\t" \
  "s_waitcnt vmcnt(11)\n\t" \
  "s_cmp_lg_u32 s59, 3\n\t" \
  "s_cbranch_scc1 18f\n\t" \
  "v_cmp_ne_u32 vcc, v253, v254\n\t" \
  "v_mov_b32 v253, 163364\n\t" \
  "s_nop 1\n\t" \
  "s_cmp_eq_u64 vcc, 0\n\t" \
  "s_cselect_b32 s83, 1, 0\n\t" \
  "v_mov_b32 v254, s83\n\t" \
  "ds_write_b32 v253, v254\n\t" \
  "18:\n\t"
#define C_ASM \
  "s_branch 29f\n\t" \
  "21:\n\t" \
  "s_mov_b64 s[68:69], %27\n\t" \
  "s_and_b32 s74, s68, 15\n\t" \
  "s_andn2_b32 s68, s68, 15\n\t" \
  "s_load_dword vcc_lo, s[68:69], 0 glc\n\t" \
  "s_and_b32 s75, s74, 1\n\t" \
  "s_lshl_b32 s70, s74, 9\n\t" \
  "s_lshl_b32 s71, s75, 13\n\t" \
  "s_or_b32 s70, s70, s71\n\t" \
  "s_waitcnt lgkmcnt(0)\n\t" \
  "s_or_b32 s68, s68, s70\n\t" \
  "s_add_u32 vcc_lo, vcc_lo, 1\n\t" \
  "v_mov_b32 v18, vcc_lo\n\t" \
  "s_memtime s[70:71]\n\t" \
  "s_waitcnt lgkmcnt(0)\n\t" \
  "s_load_dword s76, s[68:69], 16384 glc\n\t" \
  "s_waitcnt lgkmcnt(0)\n\t" \
  "s_xor_b32 s68, s68, 0x2000\n\t" \
  "s_memtime s[90:91]\n\t" \
  "s_waitcnt lgkmcnt(0)\n\t" \
  "s_load_dword s76, s[68:69], 16384 glc\n\t" \
  "s_waitcnt lgkmcnt(0)\n\t" \
  "s_sub_u32 vcc_lo, s90, s70\n\t" \
  "s_memtime s[70:71]\n\t" \
  "s_waitcnt lgkmcnt(0)\n\t" \
  "s_sub_u32 s70, s70, s90\n\t" \
  "s_cmp_gt_u32 vcc_lo, s70\n\t" \
  "s_cselect_b32 s70, 1, 0\n\t" \
  "s_xor_b32 s70, s70, s75\n\t" \
  "s_lshl1_add_u32 s70, s74, s70\n\t" \
  "s_lshl_b32 s70, s70, 2\n\t" \
  "v_mov_b32 v19, s70\n\t" \
  "s_and_b32 s68, s68, 0xffffc000\n\t" \
  "s_mov_b64 exec, 1\n\t" \
  "global_store_dword v19, v18, s[68:69] offset:256 sc1\n\t" \
  "s_branch 26f\n\t" \
  "28:\n\t" \
  "s_cmp_lg_u64 %27, 0\n\t" \
  "s_cbranch_scc1 21b\n\t" \
  "s_mov_b64 exec, 0\n\t" \
  "s_cmp_eq_u32 %13, 0\n\t" \
  "s_cbranch_scc1 500f\n\t" \
  "s_cmp_eq_u32 %13, 1\n\t" \
  "s_cbranch_scc1 501f\n\t" \
  "s_cmp_eq_u32 %13, 2\n\t" \
  "s_cbranch_scc1 502f\n\t" \
  "s_branch 503f\n\t" \
  "26:\n\t" \
  "s_mov_b64 exec, -1\n\t" \
  "s_add_u32 s88, s88, 4\n\t" \
  "s_addc_u32 s89, s89, 0\n\t" \
  "s_setpc_b64 s[88:89]\n\t" \
  ".p2align 12\n\t" \
  "29:\n\t" \
  "s_bitcmp0_b32 %13, 0\n\t" \
  "s_cbranch_scc1 8f\n\t" \
  "global_load_dwordx2 v[10:11], v51, %11\n\t" \
  "global_load_dwordx2 v[12:13], v51, %11 offset:128\n\t" \
  "v_xor_b32 v53, 64, v52\n\t" \
  "v_xor_b32 v55, 32, v54\n\t" \
  "v_add_u32 v56, 43008, v52\n\t" \
  "v_add_u32 v58, 43008, v54\n\t" \
  "v_xor_b32 v57, 64, v56\n\t" \
  "v_xor_b32 v59, 32, v58\n\t" \
  "v_add_u32 v60, 86016, v52\n\t" \
  "v_add_u32 v62, 86016, v54\n\t" \
  "v_xor_b32 v61, 64, v60\n\t" \
  "v_xor_b32 v63, 32, v62\n\t" \
  "s_mov_b32 vcc_hi, 0x05040100\n\t" \
  "s_waitcnt vmcnt(2)\n\t" \
  "s_barrier\n\t" \
  "ds_read_b64_tr_b16 v[64:65], v62 offset:26624\n\t" \
  "ds_read_b64_tr_b16 v[66:67], v62 offset:27136\n\t" \
  "ds_read_b128 v[68:71], v60 offset:10240\n\t" \
  "ds_read_b64_tr_b16 v[72:73], v62 offset:30720\n\t" \
  "ds_read_b64_tr_b16 v[74:75], v62 offset:31232\n\t" \
  "ds_read_b128 v[76:79], v61 offset:10240\n\t" \
  "ds_read_b128 v[80:83], v61 offset:12288\n\t" \
  "ds_read_b128 v[84:87], v60 offset:12288\n\t" \
  "ds_read_b128 v[88:91], v60 offset:14336\n\t" \
  "ds_read_b128 v[92:95], v61 offset:14336\n\t" \
  "ds_read_b128 v[96:99], v61 offset:16384\n\t" \
  "ds_read_b128 v[100:103], v60 offset:16384\n\t" \
  "ds_read_b64_tr_b16 v[104:105], v63 offset:26624\n\t" \
  "ds_read_b64_tr_b16 v[106:107], v63 offset:27136\n\t" \
  "ds_read_b64_tr_b16 v[108:109], v63 offset:30720\n\t" \
  "ds_read_b64_tr_b16 v[110:111], v63 offset:31232\n\t" \
  "s_waitcnt lgkmcnt(13)\n\t" \
  "v_mfma_f32_16x16x32_bf16 v[184:187], v[64:67], v[68:71], 0\n\t" \
  "ds_read_b64_tr_b16 v[112:113], v62 offset:34816\n\t" \
  "ds_read_b64_tr_b16 v[114:115], v62 offset:35328\n\t" \
  "s_waitcnt lgkmcnt(12)\n\t" \
  "v_mfma_f32_16x16x32_bf16 v[184:187], v[72:75], v[76:79], v[184:187]\n\t" \
  "ds_read_b64_tr_b16 v[116:117], v62 offset:38912\n\t" \
  "ds_read_b64_tr_b16 v[118:119], v62 offset:39424\n\t" \
  "s_waitcnt lgkmcnt(13)\n\t" \
  "v_mfma_f32_16x16x32_bf16 v[192:195], v[72:75], v[80:83], 0\n\t" \
  "ds_read_b128 v[120:123], v60 offset:0\n\t" \
  "s_waitcnt lgkmcnt(13)\n\t" \
  "v_mfma_f32_16x16x32_bf16 v[192:195], v[64:67], v[84:87], v[192:195]\n\t" \
  "ds_read_b128 v[124:127], v61 offset:0\n\t" \
  "s_waitcnt lgkmcnt(13)\n\t" \
  "v_mfma_f32_16x16x32_bf16 v[200:203], v[64:67], v[88:91], 0\n\t" \
  "ds_read_b64_tr_b16 v[128:129], v63 offset:34816\n\t" \
  "s_waitcnt lgkmcnt(13)\n\t" \
  "v_mfma_f32_16x16x32_bf16 v[200:203], v[72:75], v[92:95], v[200:203]\n\t" \
  "ds_read_b64_tr_b16 v[130:131], v63 offset:35328\n\t" \
  "s_waitcnt lgkmcnt(13)\n\t" \
  "v_mfma_f32_16x16x32_bf16 v[208:211], v[72:75], v[96:99], 0\n\t" \
  "ds_read_b64_tr_b16 v[132:133], v63 offset:38912\n\t" \
  "s_waitcnt lgkmcnt(9)\n\t" \
  "v_mfma_f32_16x16x32_bf16 v[208:211], v[64:67], v[100:103], v[208:211]\n\t" \
  "ds_read_b64_tr_b16 v[134:135], v63 offset:39424\n\t" \
  "v_mfma_f32_16x16x32_bf16 v[188:191], v[104:107], v[68:71], 0\n\t" \
  "ds_read_b64_tr_b16 v[136:137], v62 offset:18432\n\t" \
  "v_mfma_f32_16x16x32_bf16 v[188:191], v[108:111], v[76:79], v[188:191]\n\t" \
  "ds_read_b64_tr_b16 v[138:139], v62 offset:18944\n\t" \
  "v_mfma_f32_16x16x32_bf16 v[196:199], v[108:111], v[80:83], 0\n\t" \
  "ds_read_b64_tr_b16 v[140:141], v62 offset:22528\n\t" \
  "v_mfma_f32_16x16x32_bf16 v[196:199], v[104:107], v[84:87], v[196:199]\n\t" \
  "ds_read_b64_tr_b16 v[142:143], v62 offset:23040\n\t" \
  "v_mfma_f32_16x16x32_bf16 v[204:207], v[104:107], v[88:91], 0\n\t" \
  "ds_read_b64_tr_b16 v[144:145], v63 offset:18432\n\t" \
  "v_mfma_f32_16x16x32_bf16 v[204:207], v[108:111], v[92:95], v[204:207]\n\t" \
  "ds_read_b64_tr_b16 v[146:147], v63 offset:18944\n\t" \
  "v_mfma_f32_16x16x32_bf16 v[212:215], v[108:111], v[96:99], 0\n\t" \
  "ds_read_b64_tr_b16 v[148:149], v63 offset:22528\n\t" \
  "v_mfma_f32_16x16x32_bf16 v[212:215], v[104:107], v[100:103], v[212:215]\n\t" \
  "ds_read_b64_tr_b16 v[150:151], v63 offset:23040\n\t" \
  "s_waitcnt lgkmcnt(7)\n\t" \
  "v_mfma_f32_16x16x32_bf16 v[216:219], v[68:71], v[112:115], 0\n\t" \
  "v_mfma_f32_16x16x32_bf16 v[216:219], v[76:79], v[116:119], v[216:219]\n\t" \
  "v_mfma_f32_16x16x32_bf16 v[224:227], v[80:83], v[116:119], 0\n\t" \
  "v_mfma_f32_16x16x32_bf16 v[224:227], v[84:87], v[112:115], v[224:227]\n\t" \
  "v_mfma_f32_16x16x32_bf16 v[232:235], v[88:91], v[112:115], 0\n\t" \
  "v_mfma_f32_16x16x32_bf16 v[232:235], v[92:95], v[116:119], v[232:235]\n\t" \
  "v_mfma_f32_16x16x32_bf16 v[240:243], v[96:99], v[116:119], 0\n\t" \
  "v_mfma_f32_16x16x32_bf16 v[240:243], v[100:103], v[112:115], v[240:243]\n\t" \
  "v_mfma_f32_16x16x32_bf16 v[248:251], v[120:123], v[112:115], 0\n\t" \
  "v_mfma_f32_16x16x32_bf16 v[248:251], v[124:127], v[116:119], v[248:251]\n\t" \
  "v_mfma_f32_16x16x32_bf16 v[252:255], v[120:123], v[128:131], 0\n\t" \
  "v_mfma_f32_16x16x32_bf16 v[252:255], v[124:127], v[132:135], v[252:255]\n\t" \
  "v_mfma_f32_16x16x32_bf16 v[220:223], v[76:79], v[132:135], 0\n\t" \
  "v_mfma_f32_16x16x32_bf16 v[220:223], v[68:71], v[128:131], v[220:223]\n\t" \
  "s_waitcnt lgkmcnt(0)\n\t" \
  "s_getpc_b64 s[88:89]\n\t" \
  "s_branch 28b\n\t" \
  "s_barrier\n\t" \
  "v_mfma_f32_16x16x32_bf16 v[228:231], v[84:87], v[128:131], 0\n\t" \
  "ds_read_b64_tr_b16 v[72:73], v54 offset:26624\n\t" \
  "v_mfma_f32_16x16x32_bf16 v[228:231], v[80:83], v[132:135], v[228:231]\n\t" \
  "ds_read_b64_tr_b16 v[74:75], v54 offset:27136\n\t" \
  "v_mfma_f32_16x16x32_bf16 v[236:239], v[92:95], v[132:135], 0\n\t" \
  "ds_read_b128 v[64:67], v52 offset:10240\n\t" \
  "v_mfma_f32_16x16x32_bf16 v[236:239], v[88:91], v[128:131], v[236:239]\n\t" \
  "ds_read_b64_tr_b16 v[108:109], v54 offset:30720\n\t" \
  "v_mfma_f32_16x16x32_bf16 v[244:247], v[100:103], v[128:131], 0\n\t" \
  "ds_read_b64_tr_b16 v[110:111], v54 offset:31232\n\t" \
  "v_mfma_f32_16x16x32_bf16 v[244:247], v[96:99], v[132:135], v[244:247]\n\t" \
  "ds_read_b128 v[104:107], v53 offset:10240\n\t" \
  "v_mfma_f32_16x16x32_bf16 v[152:155], v[136:139], v[68:71], 0\n\t" \
  "ds_read_b128 v[112:115], v53 offset:12288\n\t" \
  "v_mfma_f32_16x16x32_bf16 v[152:155], v[140:143], v[76:79], v[152:155]\n\t" \
  "ds_read_b128 v[116:119], v52 offset:12288\n\t" \
  "v_mfma_f32_16x16x32_bf16 v[160:163], v[140:143], v[80:83], 0\n\t" \
  "ds_read_b128 v[120:123], v52 offset:14336\n\t" \
  "v_mfma_f32_16x16x32_bf16 v[160:163], v[136:139], v[84:87], v[160:163]\n\t" \
  "ds_read_b128 v[124:127], v53 offset:14336\n\t" \
  "v_mfma_f32_16x16x32_bf16 v[168:171], v[136:139], v[88:91], 0\n\t" \
  "ds_read_b128 v[128:131], v53 offset:16384\n\t" \
  "v_mfma_f32_16x16x32_bf16 v[168:171], v[140:143], v[92:95], v[168:171]\n\t" \
  "ds_read_b128 v[132:135], v52 offset:16384\n\t" \
  "v_mfma_f32_16x16x32_bf16 v[176:179], v[140:143], v[96:99], 0\n\t" \
  "ds_read_b64_tr_b16 v[140:141], v55 offset:26624\n\t" \
  "v_mfma_f32_16x16x32_bf16 v[176:179], v[136:139], v[100:103], v[176:179]\n\t" \
  "ds_read_b64_tr_b16 v[142:143], v55 offset:27136\n\t" \
  "v_mfma_f32_16x16x32_bf16 v[156:159], v[144:147], v[68:71], 0\n\t" \
  "ds_read_b64_tr_b16 v[136:137], v55 offset:30720\n\t" \
  "v_mfma_f32_16x16x32_bf16 v[156:159], v[148:151], v[76:79], v[156:159]\n\t" \
  "ds_read_b64_tr_b16 v[138:139], v55 offset:31232\n\t" \
  "v_mfma_f32_16x16x32_bf16 v[164:167], v[148:151], v[80:83], 0\n\t" \
  "ds_read_b64_tr_b16 v[68:69], v54 offset:34816\n\t" \
  "v_mfma_f32_16x16x32_bf16 v[164:167], v[144:147], v[84:87], v[164:167]\n\t" \
  "ds_read_b64_tr_b16 v[70:71], v54 offset:35328\n\t" \
  "v_mfma_f32_16x16x32_bf16 v[172:175], v[144:147], v[88:91], 0\n\t" \
  "ds_read_b64_tr_b16 v[76:77], v54 offset:38912\n\t" \
  "v_mfma_f32_16x16x32_bf16 v[172:175], v[148:151], v[92:95], v[172:175]\n\t" \
  "ds_read_b64_tr_b16 v[78:79], v54 offset:39424\n\t" \
  "v_mfma_f32_16x16x32_bf16 v[180:183], v[148:151], v[96:99], 0\n\t" \
  "ds_read_b128 v[80:83], v52 offset:0\n\t" \
  "v_mfma_f32_16x16x32_bf16 v[180:183], v[144:147], v[100:103], v[180:183]\n\t" \
  "ds_read_b128 v[84:87], v53 offset:0\n\t" \
  "s_waitcnt lgkmcnt(7)\n\t" \
  "v_mfma_f32_16x16x32_bf16 v[184:187], v[72:75], v[64:67], v[184:187]\n\t" \
  "ds_read_b64_tr_b16 v[88:89], v55 offset:34816\n\t" \
  "v_mfma_f32_16x16x32_bf16 v[184:187], v[108:111], v[104:107], v[184:187]\n\t" \
  "ds_read_b64_tr_b16 v[90:91], v55 offset:35328\n\t" \
  "v_mfma_f32_16x16x32_bf16 v[192:195], v[108:111], v[112:115], v[192:195]\n\t" \
  "ds_read_b64_tr_b16 v[92:93], v55 offset:38912\n\t" \
  "v_mfma_f32_16x16x32_bf16 v[192:195], v[72:75], v[116:119], v[192:195]\n\t" \
  "ds_read_b64_tr_b16 v[94:95], v55 offset:39424\n\t" \
  "v_mfma_f32_16x16x32_bf16 v[200:203], v[72:75], v[120:123], v[200:203]\n\t" \
  "ds_read_b64_tr_b16 v[96:97], v54 offset:18432\n\t" \
  "v_mfma_f32_16x16x32_bf16 v[200:203], v[108:111], v[124:127], v[200:203]\n\t" \
  "ds_read_b64_tr_b16 v[98:99], v54 offset:18944\n\t" \
  "v_mfma_f32_16x16x32_bf16 v[208:211], v[108:111], v[128:131], v[208:211]\n\t" \
  "ds_read_b64_tr_b16 v[148:149], v54 offset:22528\n\t" \
  "v_mfma_f32_16x16x32_bf16 v[208:211], v[72:75], v[132:135], v[208:211]\n\t" \
  "ds_read_b64_tr_b16 v[150:151], v54 offset:23040\n\t" \
  "v_mfma_f32_16x16x32_bf16 v[188:191], v[140:143], v[64:67], v[188:191]\n\t" \
  "ds_read_b64_tr_b16 v[100:101], v55 offset:18432\n\t" \
  "s_waitcnt lgkmcnt(7)\n\t" \
  "v_mfma_f32_16x16x32_bf16 v[188:191], v[136:139], v[104:107], v[188:191]\n\t" \
  "ds_read_b64_tr_b16 v[102:103], v55 offset:18944\n\t" \
  "v_mfma_f32_16x16x32_bf16 v[196:199], v[136:139], v[112:115], v[196:199]\n\t" \
  "ds_read_b64_tr_b16 v[144:145], v55 offset:22528\n\t" \
  "v_mfma_f32_16x16x32_bf16 v[196:199], v[140:143], v[116:119], v[196:199]\n\t" \
  "ds_read_b64_tr_b16 v[146:147], v55 offset:23040\n\t" \
  "v_mfma_f32_16x16x32_bf16 v[204:207], v[140:143], v[120:123], v[204:207]\n\t" \
  "v_mfma_f32_16x16x32_bf16 v[204:207], v[136:139], v[124:127], v[204:207]\n\t" \
  "v_mfma_f32_16x16x32_bf16 v[212:215], v[136:139], v[128:131], v[212:215]\n\t" \
  "v_mfma_f32_16x16x32_bf16 v[212:215], v[140:143], v[132:135], v[212:215]\n\t" \
  "v_mfma_f32_16x16x32_bf16 v[216:219], v[64:67], v[68:71], v[216:219]\n\t" \
  "v_mfma_f32_16x16x32_bf16 v[216:219], v[104:107], v[76:79], v[216:219]\n\t" \
  "v_mfma_f32_16x16x32_bf16 v[224:227], v[112:115], v[76:79], v[224:227]\n\t" \
  "v_mfma_f32_16x16x32_bf16 v[224:227], v[116:119], v[68:71], v[224:227]\n\t" \
  "v_mfma_f32_16x16x32_bf16 v[232:235], v[120:123], v[68:71], v[232:235]\n\t" \
  "v_mfma_f32_16x16x32_bf16 v[232:235], v[124:127], v[76:79], v[232:235]\n\t" \
  "v_mfma_f32_16x16x32_bf16 v[240:243], v[128:131], v[76:79], v[240:243]\n\t" \
  "v_mfma_f32_16x16x32_bf16 v[240:243], v[132:135], v[68:71], v[240:243]\n\t" \
  "v_mfma_f32_16x16x32_bf16 v[248:251], v[80:83], v[68:71], v[248:251]\n\t" \
  "v_mfma_f32_16x16x32_bf16 v[248:251], v[84:87], v[76:79], v[248:251]\n\t" \
  "v_mfma_f32_16x16x32_bf16 v[252:255], v[80:83], v[88:91], v[252:255]\n\t" \
  "s_waitcnt lgkmcnt(0)\n\t" \
  "v_mfma_f32_16x16x32_bf16 v[252:255], v[84:87], v[92:95], v[252:255]\n\t" \
  "v_mfma_f32_16x16x32_bf16 v[220:223], v[104:107], v[92:95], v[220:223]\n\t" \
  "v_mfma_f32_16x16x32_bf16 v[220:223], v[64:67], v[88:91], v[220:223]\n\t" \
  "s_barrier\n\t" \
  "v_mfma_f32_16x16x32_bf16 v[228:231], v[116:119], v[88:91], v[228:231]\n\t" \
  "ds_read_b64_tr_b16 v[108:109], v58 offset:26624\n\t" \
  "v_mfma_f32_16x16x32_bf16 v[228:231], v[112:115], v[92:95], v[228:231]\n\t" \
  "ds_read_b64_tr_b16 v[110:111], v58 offset:27136\n\t" \
  "v_mfma_f32_16x16x32_bf16 v[236:239], v[124:127], v[92:95], v[236:239]\n\t" \
  "ds_read_b128 v[72:75], v56 offset:10240\n\t" \
  "v_mfma_f32_16x16x32_bf16 v[236:239], v[120:123], v[88:91], v[236:239]\n\t" \
  "ds_read_b64_tr_b16 v[136:137], v58 offset:30720\n\t" \
  "v_mfma_f32_16x16x32_bf16 v[244:247], v[132:135], v[88:91], v[244:247]\n\t" \
  "ds_read_b64_tr_b16 v[138:139], v58 offset:31232\n\t" \
  "v_mfma_f32_16x16x32_bf16 v[244:247], v[128:131], v[92:95], v[244:247]\n\t" \
  "ds_read_b128 v[140:143], v57 offset:10240\n\t" \
  "v_mfma_f32_16x16x32_bf16 v[152:155], v[96:99], v[64:67], v[152:155]\n\t" \
  "ds_read_b128 v[68:71], v57 offset:12288\n\t" \
  "v_mfma_f32_16x16x32_bf16 v[152:155], v[148:151], v[104:107], v[152:155]\n\t" \
  "ds_read_b128 v[76:79], v56 offset:12288\n\t" \
  "v_mfma_f32_16x16x32_bf16 v[160:163], v[148:151], v[112:115], v[160:163]\n\t" \
  "ds_read_b128 v[80:83], v56 offset:14336\n\t" \
  "v_mfma_f32_16x16x32_bf16 v[160:163], v[96:99], v[116:119], v[160:163]\n\t" \
  "ds_read_b128 v[84:87], v57 offset:14336\n\t" \
  "v_mfma_f32_16x16x32_bf16 v[168:171], v[96:99], v[120:123], v[168:171]\n\t" \
  "ds_read_b128 v[88:91], v57 offset:16384\n\t" \
  "v_mfma_f32_16x16x32_bf16 v[168:171], v[148:151], v[124:127], v[168:171]\n\t" \
  "ds_read_b128 v[92:95], v56 offset:16384\n\t" \
  "v_mfma_f32_16x16x32_bf16 v[176:179], v[148:151], v[128:131], v[176:179]\n\t" \
  "ds_read_b64_tr_b16 v[148:149], v59 offset:26624\n\t" \
  "v_mfma_f32_16x16x32_bf16 v[176:179], v[96:99], v[132:135], v[176:179]\n\t" \
  "ds_read_b64_tr_b16 v[150:151], v59 offset:27136\n\t" \
  "v_mfma_f32_16x16x32_bf16 v[156:159], v[100:103], v[64:67], v[156:159]\n\t" \
  "ds_read_b64_tr_b16 v[96:97], v59 offset:30720\n\t" \
  "v_mfma_f32_16x16x32_bf16 v[156:159], v[144:147], v[104:107], v[156:159]\n\t" \
  "ds_read_b64_tr_b16 v[98:99], v59 offset:31232\n\t" \
  "v_mfma_f32_16x16x32_bf16 v[164:167], v[144:147], v[112:115], v[164:167]\n\t" \
  "ds_read_b64_tr_b16 v[64:65], v58 offset:34816\n\t" \
  "v_mfma_f32_16x16x32_bf16 v[164:167], v[100:103], v[116:119], v[164:167]\n\t" \
  "ds_read_b64_tr_b16 v[66:67], v58 offset:35328\n\t" \
  "v_mfma_f32_16x16x32_bf16 v[172:175], v[100:103], v[120:123], v[172:175]\n\t" \
  "ds_read_b64_tr_b16 v[104:105], v58 offset:38912\n\t" \
  "v_mfma_f32_16x16x32_bf16 v[172:175], v[144:147], v[124:127], v[172:175]\n\t" \
  "ds_read_b64_tr_b16 v[106:107], v58 offset:39424\n\t" \
  "v_mfma_f32_16x16x32_bf16 v[180:183], v[144:147], v[128:131], v[180:183]\n\t" \
  "ds_read_b128 v[112:115], v56 offset:0\n\t" \
  "v_mfma_f32_16x16x32_bf16 v[180:183], v[100:103], v[132:135], v[180:183]\n\t" \
  "ds_read_b128 v[116:119], v57 offset:0\n\t" \
  "s_waitcnt lgkmcnt(7)\n\t" \
  "v_mfma_f32_16x16x32_bf16 v[184:187], v[108:111], v[72:75], v[184:187]\n\t" \
  "ds_read_b64_tr_b16 v[120:121], v59 offset:34816\n\t" \
  "v_mfma_f32_16x16x32_bf16 v[184:187], v[136:139], v[140:143], v[184:187]\n\t" \
  "ds_read_b64_tr_b16 v[122:123], v59 offset:35328\n\t" \
  "v_mfma_f32_16x16x32_bf16 v[192:195], v[136:139], v[68:71], v[192:195]\n\t" \
  "ds_read_b64_tr_b16 v[124:125], v59 offset:38912\n\t" \
  "v_mfma_f32_16x16x32_bf16 v[192:195], v[108:111], v[76:79], v[192:195]\n\t" \
  "ds_read_b64_tr_b16 v[126:127], v59 offset:39424\n\t" \
  "v_mfma_f32_16x16x32_bf16 v[200:203], v[108:111], v[80:83], v[200:203]\n\t" \
  "ds_read_b64_tr_b16 v[128:129], v58 offset:18432\n\t" \
  "v_mfma_f32_16x16x32_bf16 v[200:203], v[136:139], v[84:87], v[200:203]\n\t" \
  "ds_read_b64_tr_b16 v[130:131], v58 offset:18944\n\t" \
  "v_mfma_f32_16x16x32_bf16 v[208:211], v[136:139], v[88:91], v[208:211]\n\t" \
  "ds_read_b64_tr_b16 v[144:145], v58 offset:22528\n\t" \
  "v_mfma_f32_16x16x32_bf16 v[208:211], v[108:111], v[92:95], v[208:211]\n\t" \
  "ds_read_b64_tr_b16 v[146:147], v58 offset:23040\n\t" \
  "v_mfma_f32_16x16x32_bf16 v[188:191], v[148:151], v[72:75], v[188:191]\n\t" \
  "ds_read_b64_tr_b16 v[100:101], v59 offset:18432\n\t" \
  "s_waitcnt lgkmcnt(7)\n\t" \
  "v_mfma_f32_16x16x32_bf16 v[188:191], v[96:99], v[140:143], v[188:191]\n\t" \
  "ds_read_b64_tr_b16 v[102:103], v59 offset:18944\n\t" \
  "v_mfma_f32_16x16x32_bf16 v[196:199], v[96:99], v[68:71], v[196:199]\n\t" \
  "ds_read_b64_tr_b16 v[132:133], v59 offset:22528\n\t" \
  "v_mfma_f32_16x16x32_bf16 v[196:199], v[148:151], v[76:79], v[196:199]\n\t" \
  "ds_read_b64_tr_b16 v[134:135], v59 offset:23040\n\t" \
  "v_mfma_f32_16x16x32_bf16 v[204:207], v[148:151], v[80:83], v[204:207]\n\t" \
  "v_mfma_f32_16x16x32_bf16 v[204:207], v[96:99], v[84:87], v[204:207]\n\t" \
  "v_mfma_f32_16x16x32_bf16 v[212:215], v[96:99], v[88:91], v[212:215]\n\t" \
  "v_mfma_f32_16x16x32_bf16 v[212:215], v[148:151], v[92:95], v[212:215]\n\t" \
  "v_mfma_f32_16x16x32_bf16 v[216:219], v[72:75], v[64:67], v[216:219]\n\t" \
  "v_mfma_f32_16x16x32_bf16 v[216:219], v[140:143], v[104:107], v[216:219]\n\t" \
  "v_mfma_f32_16x16x32_bf16 v[224:227], v[68:71], v[104:107], v[224:227]\n\t" \
  "v_mfma_f32_16x16x32_bf16 v[224:227], v[76:79], v[64:67], v[224:227]\n\t" \
  "v_mfma_f32_16x16x32_bf16 v[232:235], v[80:83], v[64:67], v[232:235]\n\t" \
  "v_mfma_f32_16x16x32_bf16 v[232:235], v[84:87], v[104:107], v[232:235]\n\t" \
  "v_mfma_f32_16x16x32_bf16 v[240:243], v[88:91], v[104:107], v[240:243]\n\t" \
  "v_mfma_f32_16x16x32_bf16 v[240:243], v[92:95], v[64:67], v[240:243]\n\t" \
  "v_mfma_f32_16x16x32_bf16 v[248:251], v[112:115], v[64:67], v[248:251]\n\t" \
  "v_mfma_f32_16x16x32_bf16 v[248:251], v[116:119], v[104:107], v[248:251]\n\t" \
  "v_mfma_f32_16x16x32_bf16 v[252:255], v[112:115], v[120:123], v[252:255]\n\t" \
  "s_waitcnt lgkmcnt(0)\n\t" \
  "v_mfma_f32_16x16x32_bf16 v[252:255], v[116:119], v[124:127], v[252:255]\n\t" \
  "v_mfma_f32_16x16x32_bf16 v[220:223], v[140:143], v[124:127], v[220:223]\n\t" \
  "v_mfma_f32_16x16x32_bf16 v[220:223], v[72:75], v[120:123], v[220:223]\n\t" \
  "s_barrier\n\t" \
  "v_mfma_f32_16x16x32_bf16 v[228:231], v[76:79], v[120:123], v[228:231]\n\t" \
  "ds_read_b64_tr_b16 v[136:137], v62 offset:26624\n\t" \
  "v_bfe_u32 v30, v4, 9, 1\n\t" \
  "v_mfma_f32_16x16x32_bf16 v[228:231], v[68:71], v[124:127], v[228:231]\n\t" \
  "ds_read_b64_tr_b16 v[138:139], v62 offset:27136\n\t" \
  "v_mad_i32_i24 v30, v30, -16, v0\n\t" \
  "v_mfma_f32_16x16x32_bf16 v[236:239], v[84:87], v[124:127], v[236:239]\n\t" \
  "ds_read_b128 v[108:111], v60 offset:10240\n\t" \
  "ds_read_b128 v[26:29], v0 offset:1552\n\t" \
  "v_mfma_f32_16x16x32_bf16 v[236:239], v[80:83], v[120:123], v[236:239]\n\t" \
  "ds_read_b64_tr_b16 v[96:97], v62 offset:30720\n\t" \
  "ds_read_b128 v[30:33], v30 offset:1040\n\t" \
  "v_mfma_f32_16x16x32_bf16 v[244:247], v[92:95], v[120:123], v[244:247]\n\t" \
  "ds_read_b64_tr_b16 v[98:99], v62 offset:31232\n\t" \
  "ds_read_b128 v[34:37], v0 offset:512\n\t" \
  "v_mfma_f32_16x16x32_bf16 v[244:247], v[88:91], v[124:127], v[244:247]\n\t" \
  "ds_read_b128 v[148:151], v61 offset:10240\n\t" \
  "ds_read_b64_tr_b16 v[38:39], v1 offset:512\n\t" \
  "v_mfma_f32_16x16x32_bf16 v[152:155], v[128:131], v[72:75], v[152:155]\n\t" \
  "ds_read_b128 v[64:67], v61 offset:12288\n\t" \
  "ds_read_b64_tr_b16 v[40:41], v1 offset:0\n\t" \
  "v_mfma_f32_16x16x32_bf16 v[152:155], v[144:147], v[140:143], v[152:155]\n\t" \
  "ds_read_b128 v[104:107], v60 offset:12288\n\t" \
  "s_waitcnt vmcnt(0)\n\t" \
  "v_mfma_f32_16x16x32_bf16 v[160:163], v[144:147], v[68:71], v[160:163]\n\t" \
  "ds_read_b128 v[112:115], v60 offset:14336\n\t" \
  "v_perm_b32 v18, v10, v10, v14\n\t" \
  "v_mfma_f32_16x16x32_bf16 v[160:163], v[128:131], v[76:79], v[160:163]\n\t" \
  "ds_read_b128 v[116:119], v61 offset:14336\n\t" \
  "v_perm_b32 v19, v10, v10, v15\n\t" \
  "v_mfma_f32_16x16x32_bf16 v[168:171], v[128:131], v[80:83], v[168:171]\n\t" \
  "ds_read_b128 v[120:123], v61 offset:16384\n\t" \
  "v_perm_b32 v20, v10, v10, v16\n\t" \
  "v_mfma_f32_16x16x32_bf16 v[168:171], v[144:147], v[84:87], v[168:171]\n\t" \
  "ds_read_b128 v[124:127], v60 offset:16384\n\t" \
  "v_perm_b32 v21, v10, v10, v17\n\t" \
  "v_mfma_f32_16x16x32_bf16 v[176:179], v[144:147], v[88:91], v[176:179]\n\t" \
  "ds_read_b64_tr_b16 v[144:145], v63 offset:26624\n\t" \
  "v_perm_b32 v22, v11, v11, v14\n\t" \
  "v_mfma_f32_16x16x32_bf16 v[176:179], v[128:131], v[92:95], v[176:179]\n\t" \
  "ds_read_b64_tr_b16 v[146:147], v63 offset:27136\n\t" \
  "v_perm_b32 v23, v11, v11, v15\n\t" \
  "v_mfma_f32_16x16x32_bf16 v[156:159], v[100:103], v[72:75], v[156:159]\n\t" \
  "ds_read_b64_tr_b16 v[128:129], v63 offset:30720\n\t" \
  "v_perm_b32 v24, v11, v11, v16\n\t" \
  "v_mfma_f32_16x16x32_bf16 v[156:159], v[132:135], v[140:143], v[156:159]\n\t" \
  "ds_read_b64_tr_b16 v[130:131], v63 offset:31232\n\t" \
  "v_perm_b32 v25, v11, v11, v17\n\t" \
  "v_mfma_f32_16x16x32_bf16 v[164:167], v[132:135], v[68:71], v[164:167]\n\t" \
  "ds_read_b64_tr_b16 v[72:73], v62 offset:34816\n\t" \
  "v_mfma_f32_16x16x32_bf16 v[164:167], v[100:103], v[76:79], v[164:167]\n\t" \
  "ds_read_b64_tr_b16 v[74:75], v62 offset:35328\n\t" \
  "v_mfma_f32_16x16x32_bf16 v[172:175], v[100:103], v[80:83], v[172:175]\n\t" \
  "ds_read_b64_tr_b16 v[140:141], v62 offset:38912\n\t" \
  "s_waitcnt lgkmcnt(6)\n\t" \
  "v_mfma_f32_16x16x32_bf16 v[42:45], v[26:29], v[18:21], 0\n\t" \
  "v_mfma_f32_16x16x32_bf16 v[42:45], v[30:33], v[22:25], v[42:45]\n\t" \
  "v_mfma_f32_16x16x32_bf16 v[46:49], v[30:33], v[18:21], 0\n\t" \
  "v_mfma_f32_16x16x32_bf16 v[46:49], v[34:37], v[22:25], v[46:49]\n\t" \
  "ds_read_b128 v[26:29], v0 offset:1808\n\t" \
  "ds_read_b128 v[30:33], v0 offset:1296\n\t" \
  "v_mfma_f32_16x16x32_bf16 v[172:175], v[132:135], v[84:87], v[172:175]\n\t" \
  "ds_read_b64_tr_b16 v[142:143], v62 offset:39424\n\t" \
  "ds_read_b128 v[34:37], v0 offset:768\n\t" \
  "v_mfma_f32_16x16x32_bf16 v[180:183], v[132:135], v[88:91], v[180:183]\n\t" \
  "ds_read_b128 v[68:71], v60 offset:0\n\t" \
  "v_mfma_f32_16x16x32_bf16 v[180:183], v[100:103], v[92:95], v[180:183]\n\t" \
  "ds_read_b128 v[76:79], v61 offset:0\n\t" \
  "v_mfma_f32_16x16x32_bf16 v[184:187], v[136:139], v[108:111], v[184:187]\n\t" \
  "ds_read_b64_tr_b16 v[80:81], v63 offset:34816\n\t" \
  "v_lshlrev_b32 v50, 16, v38\n\t" \
  "v_mfma_f32_16x16x32_bf16 v[184:187], v[96:99], v[148:151], v[184:187]\n\t" \
  "ds_read_b64_tr_b16 v[82:83], v63 offset:35328\n\t" \
  "v_lshlrev_b32 v51, 16, v40\n\t" \
  "v_mfma_f32_16x16x32_bf16 v[192:195], v[96:99], v[64:67], v[192:195]\n\t" \
  "ds_read_b64_tr_b16 v[84:85], v63 offset:38912\n\t" \
  "v_mul_f32 v42, v42, v50\n\t" \
  "v_mfma_f32_16x16x32_bf16 v[192:195], v[136:139], v[104:107], v[192:195]\n\t" \
  "ds_read_b64_tr_b16 v[86:87], v63 offset:39424\n\t" \
  "v_mul_f32 v46, v46, v51\n\t" \
  "v_mfma_f32_16x16x32_bf16 v[200:203], v[136:139], v[112:115], v[200:203]\n\t" \
  "ds_read_b64_tr_b16 v[88:89], v62 offset:18432\n\t" \
  "v_cvt_pk_bf16_f32 v42, v42, v46\n\t" \
  "v_mfma_f32_16x16x32_bf16 v[200:203], v[96:99], v[116:119], v[200:203]\n\t" \
  "ds_read_b64_tr_b16 v[90:91], v62 offset:18944\n\t" \
  "v_and_b32 v50, 0xffff0000, v38\n\t" \
  "v_mfma_f32_16x16x32_bf16 v[208:211], v[96:99], v[120:123], v[208:211]\n\t" \
  "ds_read_b64_tr_b16 v[132:133], v62 offset:22528\n\t" \
  "v_and_b32 v51, 0xffff0000, v40\n\t" \
  "v_mfma_f32_16x16x32_bf16 v[208:211], v[136:139], v[124:127], v[208:211]\n\t" \
  "ds_read_b64_tr_b16 v[134:135], v62 offset:23040\n\t" \
  "v_mul_f32 v43, v43, v50\n\t" \
  "s_waitcnt lgkmcnt(7)\n\t" \
  "v_mfma_f32_16x16x32_bf16 v[188:191], v[144:147], v[108:111], v[188:191]\n\t" \
  "ds_read_b64_tr_b16 v[92:93], v63 offset:18432\n\t" \
  "v_mul_f32 v47, v47, v51\n\t" \
  "v_mfma_f32_16x16x32_bf16 v[188:191], v[128:131], v[148:151], v[188:191]\n\t" \
  "ds_read_b64_tr_b16 v[94:95], v63 offset:18944\n\t" \
  "v_cvt_pk_bf16_f32 v43, v43, v47\n\t" \
  "v_mfma_f32_16x16x32_bf16 v[196:199], v[128:131], v[64:67], v[196:199]\n\t" \
  "ds_read_b64_tr_b16 v[100:101], v63 offset:22528\n\t" \
  "v_lshlrev_b32 v50, 16, v39\n\t" \
  "v_mfma_f32_16x16x32_bf16 v[196:199], v[144:147], v[104:107], v[196:199]\n\t" \
  "ds_read_b64_tr_b16 v[102:103], v63 offset:23040\n\t" \
  "v_lshlrev_b32 v51, 16, v41\n\t" \
  "v_mfma_f32_16x16x32_bf16 v[204:207], v[144:147], v[112:115], v[204:207]\n\t" \
  "v_mul_f32 v44, v44, v50\n\t" \
  "v_mul_f32 v48, v48, v51\n\t" \
  "v_mfma_f32_16x16x32_bf16 v[204:207], v[128:131], v[116:119], v[204:207]\n\t" \
  "v_cvt_pk_bf16_f32 v44, v44, v48\n\t" \
  "v_and_b32 v50, 0xffff0000, v39\n\t" \
  "v_mfma_f32_16x16x32_bf16 v[212:215], v[128:131], v[120:123], v[212:215]\n\t" \
  "v_and_b32 v51, 0xffff0000, v41\n\t" \
  "v_mul_f32 v45, v45, v50\n\t" \
  "v_mfma_f32_16x16x32_bf16 v[212:215], v[144:147], v[124:127], v[212:215]\n\t" \
  "v_mul_f32 v49, v49, v51\n\t" \
  "ds_read_b64_tr_b16 v[38:39], v1 offset:768\n\t" \
  "v_mfma_f32_16x16x32_bf16 v[216:219], v[108:111], v[72:75], v[216:219]\n\t" \
  "ds_read_b64_tr_b16 v[40:41], v1 offset:256\n\t" \
  "v_cvt_pk_bf16_f32 v45, v45, v49\n\t" \
  "v_mfma_f32_16x16x32_bf16 v[216:219], v[148:151], v[140:143], v[216:219]\n\t" \
  "v_mfma_f32_16x16x32_bf16 v[224:227], v[64:67], v[140:143], v[224:227]\n\t" \
  "v_mfma_f32_16x16x32_bf16 v[224:227], v[104:107], v[72:75], v[224:227]\n\t" \
  "v_mfma_f32_16x16x32_bf16 v[232:235], v[112:115], v[72:75], v[232:235]\n\t" \
  "v_mfma_f32_16x16x32_bf16 v[232:235], v[116:119], v[140:143], v[232:235]\n\t" \
  "v_mfma_f32_16x16x32_bf16 v[240:243], v[120:123], v[140:143], v[240:243]\n\t" \
  "v_mfma_f32_16x16x32_bf16 v[240:243], v[124:127], v[72:75], v[240:243]\n\t" \
  "v_mfma_f32_16x16x32_bf16 v[248:251], v[68:71], v[72:75], v[248:251]\n\t" \
  "v_mfma_f32_16x16x32_bf16 v[248:251], v[76:79], v[140:143], v[248:251]\n\t" \
  "s_waitcnt lgkmcnt(0)\n\t" \
  "v_mfma_f32_16x16x32_bf16 v[252:255], v[68:71], v[80:83], v[252:255]\n\t" \
  "v_mfma_f32_16x16x32_bf16 v[252:255], v[76:79], v[84:87], v[252:255]\n\t" \
  "v_mfma_f32_16x16x32_bf16 v[220:223], v[148:151], v[84:87], v[220:223]\n\t" \
  "v_mfma_f32_16x16x32_bf16 v[220:223], v[108:111], v[80:83], v[220:223]\n\t" \
  "s_barrier\n\t" \
  "v_mfma_f32_16x16x32_bf16 v[228:231], v[104:107], v[80:83], v[228:231]\n\t" \
  "ds_read_b64_tr_b16 v[96:97], v54 offset:26624\n\t" \
  "v_mfma_f32_16x16x32_bf16 v[228:231], v[64:67], v[84:87], v[228:231]\n\t" \
  "v_mov_b32 v50, 163344\n\t" \
  "ds_read_b64_tr_b16 v[98:99], v54 offset:27136\n\t" \
  "v_mfma_f32_16x16x32_bf16 v[236:239], v[116:119], v[84:87], v[236:239]\n\t" \
  "ds_read_b32 v50, v50 offset:24\n\t" \
  "ds_read_b128 v[136:139], v52 offset:10240\n\t" \
  "v_mfma_f32_16x16x32_bf16 v[236:239], v[112:115], v[80:83], v[236:239]\n\t" \
  "ds_read_b64_tr_b16 v[128:129], v54 offset:30720\n\t" \
  "v_mfma_f32_16x16x32_bf16 v[244:247], v[124:127], v[80:83], v[244:247]\n\t" \
  "ds_read_b64_tr_b16 v[130:131], v54 offset:31232\n\t" \
  "v_mfma_f32_16x16x32_bf16 v[244:247], v[120:123], v[84:87], v[244:247]\n\t" \
  "ds_read_b128 v[144:147], v53 offset:10240\n\t" \
  "v_mfma_f32_16x16x32_bf16 v[152:155], v[88:91], v[108:111], v[152:155]\n\t" \
  "ds_read_b128 v[72:75], v53 offset:12288\n\t" \
  "v_mfma_f32_16x16x32_bf16 v[152:155], v[132:135], v[148:151], v[152:155]\n\t" \
  "ds_read_b128 v[140:143], v52 offset:12288\n\t" \
  "v_mfma_f32_16x16x32_bf16 v[160:163], v[132:135], v[64:67], v[160:163]\n\t" \
  "ds_read_b128 v[68:71], v52 offset:14336\n\t" \
  "v_mfma_f32_16x16x32_bf16 v[160:163], v[88:91], v[104:107], v[160:163]\n\t" \
  "ds_read_b128 v[76:79], v53 offset:14336\n\t" \
  "v_mfma_f32_16x16x32_bf16 v[168:171], v[88:91], v[112:115], v[168:171]\n\t" \
  "ds_read_b128 v[80:83], v53 offset:16384\n\t" \
  "v_mfma_f32_16x16x32_bf16 v[168:171], v[132:135], v[116:119], v[168:171]\n\t" \
  "ds_read_b128 v[84:87], v52 offset:16384\n\t" \
  "v_mfma_f32_16x16x32_bf16 v[176:179], v[132:135], v[120:123], v[176:179]\n\t" \
  "s_waitcnt lgkmcnt(10)\n\t" \
  "v_readfirstlane_b32 s88, v50\n\t" \
  "s_mov_b32 s89, 0\n\t" \
  "ds_read_b64_tr_b16 v[132:133], v55 offset:26624\n\t" \
  "v_mfma_f32_16x16x32_bf16 v[176:179], v[88:91], v[124:127], v[176:179]\n\t" \
  "ds_read_b64_tr_b16 v[134:135], v55 offset:27136\n\t" \
  "v_mfma_f32_16x16x32_bf16 v[156:159], v[92:95], v[108:111], v[156:159]\n\t" \
  "s_xor_b64 %24, %24, s[88:89]\n\t" \
  "ds_read_b64_tr_b16 v[88:89], v55 offset:30720\n\t" \
  "v_mfma_f32_16x16x32_bf16 v[156:159], v[100:103], v[148:151], v[156:159]\n\t" \
  "s_xor_b64 %25, %25, s[88:89]\n\t" \
  "ds_read_b64_tr_b16 v[90:91], v55 offset:31232\n\t" \
  "v_mfma_f32_16x16x32_bf16 v[164:167], v[100:103], v[64:67], v[164:167]\n\t" \
  "s_xor_b64 %14, %14, s[88:89]\n\t" \
  "ds_read_b64_tr_b16 v[108:109], v54 offset:34816\n\t" \
  "v_mfma_f32_16x16x32_bf16 v[164:167], v[92:95], v[104:107], v[164:167]\n\t" \
  "s_xor_b64 %19, %19, s[88:89]\n\t" \
  "ds_read_b64_tr_b16 v[110:111], v54 offset:35328\n\t" \
  "v_mfma_f32_16x16x32_bf16 v[172:175], v[92:95], v[112:115], v[172:175]\n\t" \
  "s_xor_b64 %21, %21, s[88:89]\n\t" \
  "ds_read_b64_tr_b16 v[148:149], v54 offset:38912\n\t" \
  "v_mfma_f32_16x16x32_bf16 v[172:175], v[100:103], v[116:119], v[172:175]\n\t" \
  "s_xor_b64 s[92:93], s[92:93], s[88:89]\n\t" \
  "ds_read_b64_tr_b16 v[150:151], v54 offset:39424\n\t" \
  "v_mfma_f32_16x16x32_bf16 v[180:183], v[100:103], v[120:123], v[180:183]\n\t" \
  "ds_read_b128 v[64:67], v52 offset:0\n\t" \
  "v_mfma_f32_16x16x32_bf16 v[180:183], v[92:95], v[124:127], v[180:183]\n\t" \
  "ds_read_b128 v[104:107], v53 offset:0\n\t" \
  "s_waitcnt lgkmcnt(7)\n\t" \
  "v_mfma_f32_16x16x32_bf16 v[184:187], v[96:99], v[136:139], v[184:187]\n\t" \
  "ds_read_b64_tr_b16 v[112:113], v55 offset:34816\n\t" \
  "v_mfma_f32_16x16x32_bf16 v[184:187], v[128:131], v[144:147], v[184:187]\n\t" \
  "ds_read_b64_tr_b16 v[114:115], v55 offset:35328\n\t" \
  "v_mfma_f32_16x16x32_bf16 v[192:195], v[128:131], v[72:75], v[192:195]\n\t" \
  "ds_read_b64_tr_b16 v[116:117], v55 offset:38912\n\t" \
  "v_mfma_f32_16x16x32_bf16 v[192:195], v[96:99], v[140:143], v[192:195]\n\t" \
  "ds_read_b64_tr_b16 v[118:119], v55 offset:39424\n\t" \
  "v_mfma_f32_16x16x32_bf16 v[200:203], v[96:99], v[68:71], v[200:203]\n\t" \
  "ds_read_b64_tr_b16 v[100:101], v54 offset:18432\n\t" \
  "v_mfma_f32_16x16x32_bf16 v[200:203], v[128:131], v[76:79], v[200:203]\n\t" \
  "ds_read_b64_tr_b16 v[102:103], v54 offset:18944\n\t" \
  "v_mfma_f32_16x16x32_bf16 v[208:211], v[128:131], v[80:83], v[208:211]\n\t" \
  "ds_read_b64_tr_b16 v[120:121], v54 offset:22528\n\t" \
  "v_mfma_f32_16x16x32_bf16 v[208:211], v[96:99], v[84:87], v[208:211]\n\t" \
  "ds_read_b64_tr_b16 v[122:123], v54 offset:23040\n\t" \
  "v_mfma_f32_16x16x32_bf16 v[188:191], v[132:135], v[136:139], v[188:191]\n\t" \
  "ds_read_b64_tr_b16 v[92:93], v55 offset:18432\n\t" \
  "s_waitcnt lgkmcnt(7)\n\t" \
  "v_mfma_f32_16x16x32_bf16 v[188:191], v[88:91], v[144:147], v[188:191]\n\t" \
  "ds_read_b64_tr_b16 v[94:95], v55 offset:18944\n\t" \
  "v_mfma_f32_16x16x32_bf16 v[196:199], v[88:91], v[72:75], v[196:199]\n\t" \
  "ds_read_b64_tr_b16 v[124:125], v55 offset:22528\n\t" \
  "v_mfma_f32_16x16x32_bf16 v[196:199], v[132:135], v[140:143], v[196:199]\n\t" \
  "ds_read_b64_tr_b16 v[126:127], v55 offset:23040\n\t" \
  "v_mfma_f32_16x16x32_bf16 v[204:207], v[132:135], v[68:71], v[204:207]\n\t" \
  "v_mfma_f32_16x16x32_bf16 v[204:207], v[88:91], v[76:79], v[204:207]\n\t" \
  "v_mfma_f32_16x16x32_bf16 v[212:215], v[88:91], v[80:83], v[212:215]\n\t" \
  "v_mfma_f32_16x16x32_bf16 v[212:215], v[132:135], v[84:87], v[212:215]\n\t" \
  "v_mfma_f32_16x16x32_bf16 v[216:219], v[136:139], v[108:111], v[216:219]\n\t" \
  "v_mfma_f32_16x16x32_bf16 v[216:219], v[144:147], v[148:151], v[216:219]\n\t" \
  "v_mfma_f32_16x16x32_bf16 v[224:227], v[72:75], v[148:151], v[224:227]\n\t" \
  "v_mfma_f32_16x16x32_bf16 v[224:227], v[140:143], v[108:111], v[224:227]\n\t" \
  "v_mfma_f32_16x16x32_bf16 v[232:235], v[68:71], v[108:111], v[232:235]\n\t" \
  "global_store_dwordx4 v2, v[42:45], %24 offset:0 sc1\n\t" \
  "v_mfma_f32_16x16x32_bf16 v[232:235], v[76:79], v[148:151], v[232:235]\n\t" \
  "s_nop 0\n\t" \
  "v_mfma_f32_16x16x32_bf16 v[42:45], v[26:29], v[18:21], 0\n\t" \
  "v_mfma_f32_16x16x32_bf16 v[42:45], v[30:33], v[22:25], v[42:45]\n\t" \
  "v_mfma_f32_16x16x32_bf16 v[46:49], v[30:33], v[18:21], 0\n\t" \
  "v_mfma_f32_16x16x32_bf16 v[46:49], v[34:37], v[22:25], v[46:49]\n\t" \
  "v_bfe_u32 v30, v4, 9, 1\n\t" \
  "v_mad_i32_i24 v30, v30, -16, v0\n\t" \
  "v_mfma_f32_16x16x32_bf16 v[240:243], v[80:83], v[148:151], v[240:243]\n\t" \
  "ds_read_b128 v[26:29], v0 offset:1584\n\t" \
  "ds_read_b128 v[30:33], v30 offset:1072\n\t" \
  "v_mfma_f32_16x16x32_bf16 v[240:243], v[84:87], v[108:111], v[240:243]\n\t" \
  "ds_read_b128 v[34:37], v0 offset:544\n\t" \
  "v_perm_b32 v18, v12, v12, v14\n\t" \
  "v_mfma_f32_16x16x32_bf16 v[248:251], v[64:67], v[108:111], v[248:251]\n\t" \
  "v_perm_b32 v19, v12, v12, v15\n\t" \
  "v_perm_b32 v20, v12, v12, v16\n\t" \
  "v_mfma_f32_16x16x32_bf16 v[248:251], v[104:107], v[148:151], v[248:251]\n\t" \
  "v_perm_b32 v21, v12, v12, v17\n\t" \
  "v_perm_b32 v22, v13, v13, v14\n\t" \
  "v_mfma_f32_16x16x32_bf16 v[252:255], v[64:67], v[112:115], v[252:255]\n\t" \
  "v_perm_b32 v23, v13, v13, v15\n\t" \
  "v_perm_b32 v24, v13, v13, v16\n\t" \
  "s_waitcnt lgkmcnt(3)\n\t" \
  "v_mfma_f32_16x16x32_bf16 v[252:255], v[104:107], v[116:119], v[252:255]\n\t" \
  "v_perm_b32 v25, v13, v13, v17\n\t" \
  "v_lshlrev_b32 v50, 16, v38\n\t" \
  "v_mfma_f32_16x16x32_bf16 v[220:223], v[144:147], v[116:119], v[220:223]\n\t" \
  "v_lshlrev_b32 v51, 16, v40\n\t" \
  "v_mul_f32 v42, v42, v50\n\t" \
  "v_mfma_f32_16x16x32_bf16 v[220:223], v[136:139], v[112:115], v[220:223]\n\t" \
  "v_mul_f32 v46, v46, v51\n\t" \
  "v_cvt_pk_bf16_f32 v42, v42, v46\n\t" \
  "s_waitcnt lgkmcnt(0)\n\t" \
  "s_barrier\n\t" \
  "v_mfma_f32_16x16x32_bf16 v[228:231], v[140:143], v[112:115], v[228:231]\n\t" \
  "ds_read_b64_tr_b16 v[128:129], v58 offset:26624\n\t" \
  "v_and_b32 v50, 0xffff0000, v38\n\t" \
  "v_mfma_f32_16x16x32_bf16 v[228:231], v[72:75], v[116:119], v[228:231]\n\t" \
  "ds_read_b64_tr_b16 v[130:131], v58 offset:27136\n\t" \
  "v_and_b32 v51, 0xffff0000, v40\n\t" \
  "v_mfma_f32_16x16x32_bf16 v[236:239], v[76:79], v[116:119], v[236:239]\n\t" \
  "ds_read_b128 v[96:99], v56 offset:10240\n\t" \
  "v_mul_f32 v43, v43, v50\n\t" \
  "v_mfma_f32_16x16x32_bf16 v[236:239], v[68:71], v[112:115], v[236:239]\n\t" \
  "ds_read_b64_tr_b16 v[88:89], v58 offset:30720\n\t" \
  "500:\n\t" \
  "s_cbranch_execz 504f\n\t" \
  "v_mul_f32 v47, v47, v51\n\t" \
  "v_mfma_f32_16x16x32_bf16 v[244:247], v[84:87], v[112:115], v[244:247]\n\t" \
  "ds_read_b64_tr_b16 v[90:91], v58 offset:31232\n\t" \
  "v_cvt_pk_bf16_f32 v43, v43, v47\n\t" \
  "v_mfma_f32_16x16x32_bf16 v[244:247], v[80:83], v[116:119], v[244:247]\n\t" \
  "ds_read_b128 v[132:135], v57 offset:10240\n\t" \
  "v_lshlrev_b32 v50, 16, v39\n\t" \
  "v_mfma_f32_16x16x32_bf16 v[152:155], v[100:103], v[136:139], v[152:155]\n\t" \
  "ds_read_b128 v[108:111], v57 offset:12288\n\t" \
  "v_lshlrev_b32 v51, 16, v41\n\t" \
  "v_mfma_f32_16x16x32_bf16 v[152:155], v[120:123], v[144:147], v[152:155]\n\t" \
  "ds_read_b128 v[148:151], v56 offset:12288\n\t" \
  "v_mul_f32 v44, v44, v50\n\t" \
  "v_mfma_f32_16x16x32_bf16 v[160:163], v[120:123], v[72:75], v[160:163]\n\t" \
  "ds_read_b128 v[64:67], v56 offset:14336\n\t" \
  "v_mul_f32 v48, v48, v51\n\t" \
  "v_mfma_f32_16x16x32_bf16 v[160:163], v[100:103], v[140:143], v[160:163]\n\t" \
  "ds_read_b128 v[104:107], v57 offset:14336\n\t" \
  "v_cvt_pk_bf16_f32 v44, v44, v48\n\t" \
  "v_mfma_f32_16x16x32_bf16 v[168:171], v[100:103], v[68:71], v[168:171]\n\t" \
  "ds_read_b128 v[112:115], v57 offset:16384\n\t" \
  "v_and_b32 v50, 0xffff0000, v39\n\t" \
  "v_mfma_f32_16x16x32_bf16 v[168:171], v[120:123], v[76:79], v[168:171]\n\t" \
  "ds_read_b128 v[116:119], v56 offset:16384\n\t" \
  "v_and_b32 v51, 0xffff0000, v41\n\t" \
  "v_mfma_f32_16x16x32_bf16 v[176:179], v[120:123], v[80:83], v[176:179]\n\t" \
  "ds_read_b64_tr_b16 v[120:121], v59 offset:26624\n\t" \
  "v_mul_f32 v45, v45, v50\n\t" \
  "v_mfma_f32_16x16x32_bf16 v[176:179], v[100:103], v[84:87], v[176:179]\n\t" \
  "ds_read_b64_tr_b16 v[122:123], v59 offset:27136\n\t" \
  "v_mul_f32 v49, v49, v51\n\t" \
  "v_mfma_f32_16x16x32_bf16 v[156:159], v[92:95], v[136:139], v[156:159]\n\t" \
  "ds_read_b64_tr_b16 v[100:101], v59 offset:30720\n\t" \
  "ds_read_b64_tr_b16 v[38:39], v1 offset:544\n\t" \
  "v_mfma_f32_16x16x32_bf16 v[156:159], v[124:127], v[144:147], v[156:159]\n\t" \
  "ds_read_b64_tr_b16 v[102:103], v59 offset:31232\n\t" \
  "ds_read_b64_tr_b16 v[40:41], v1 offset:32\n\t" \
  "v_mfma_f32_16x16x32_bf16 v[164:167], v[124:127], v[72:75], v[164:167]\n\t" \
  "ds_read_b64_tr_b16 v[136:137], v58 offset:34816\n\t" \
  "v_cvt_pk_bf16_f32 v45, v45, v49\n\t" \
  "v_mfma_f32_16x16x32_bf16 v[164:167], v[92:95], v[140:143], v[164:167]\n\t" \
  "ds_read_b64_tr_b16 v[138:139], v58 offset:35328\n\t" \
  "v_mfma_f32_16x16x32_bf16 v[172:175], v[92:95], v[68:71], v[172:175]\n\t" \
  "ds_read_b64_tr_b16 v[144:145], v58 offset:38912\n\t" \
  "v_mfma_f32_16x16x32_bf16 v[172:175], v[124:127], v[76:79], v[172:175]\n\t" \
  "ds_read_b64_tr_b16 v[146:147], v58 offset:39424\n\t" \
  "v_mfma_f32_16x16x32_bf16 v[180:183], v[124:127], v[80:83], v[180:183]\n\t" \
  "ds_read_b128 v[72:75], v56 offset:0\n\t" \
  "v_mfma_f32_16x16x32_bf16 v[180:183], v[92:95], v[84:87], v[180:183]\n\t" \
  "ds_read_b128 v[140:143], v57 offset:0\n\t" \
  "s_waitcnt lgkmcnt(8)\n\t" \
  "v_mfma_f32_16x16x32_bf16 v[184:187], v[128:131], v[96:99], v[184:187]\n\t" \
  "ds_read_b64_tr_b16 v[68:69], v59 offset:34816\n\t" \
  "v_mfma_f32_16x16x32_bf16 v[184:187], v[88:91], v[132:135], v[184:187]\n\t" \
  "ds_read_b64_tr_b16 v[70:71], v59 offset:35328\n\t" \
  "v_mfma_f32_16x16x32_bf16 v[192:195], v[88:91], v[108:111], v[192:195]\n\t" \
  "ds_read_b64_tr_b16 v[76:77], v59 offset:38912\n\t" \
  "v_mfma_f32_16x16x32_bf16 v[192:195], v[128:131], v[148:151], v[192:195]\n\t" \
  "ds_read_b64_tr_b16 v[78:79], v59 offset:39424\n\t" \
  "v_mfma_f32_16x16x32_bf16 v[200:203], v[128:131], v[64:67], v[200:203]\n\t" \
  "ds_read_b64_tr_b16 v[80:81], v58 offset:18432\n\t" \
  "v_mfma_f32_16x16x32_bf16 v[200:203], v[88:91], v[104:107], v[200:203]\n\t" \
  "ds_read_b64_tr_b16 v[82:83], v58 offset:18944\n\t" \
  "v_mfma_f32_16x16x32_bf16 v[208:211], v[88:91], v[112:115], v[208:211]\n\t" \
  "ds_read_b64_tr_b16 v[124:125], v58 offset:22528\n\t" \
  "v_mfma_f32_16x16x32_bf16 v[208:211], v[128:131], v[116:119], v[208:211]\n\t" \
  "ds_read_b64_tr_b16 v[126:127], v58 offset:23040\n\t" \
  "v_mfma_f32_16x16x32_bf16 v[188:191], v[120:123], v[96:99], v[188:191]\n\t" \
  "ds_read_b64_tr_b16 v[84:85], v59 offset:18432\n\t" \
  "s_waitcnt lgkmcnt(7)\n\t" \
  "v_mfma_f32_16x16x32_bf16 v[188:191], v[100:103], v[132:135], v[188:191]\n\t" \
  "ds_read_b64_tr_b16 v[86:87], v59 offset:18944\n\t" \
  "v_mfma_f32_16x16x32_bf16 v[196:199], v[100:103], v[108:111], v[196:199]\n\t" \
  "ds_read_b64_tr_b16 v[92:93], v59 offset:22528\n\t" \
  "v_mfma_f32_16x16x32_bf16 v[196:199], v[120:123], v[148:151], v[196:199]\n\t" \
  "ds_read_b64_tr_b16 v[94:95], v59 offset:23040\n\t" \
  "v_mfma_f32_16x16x32_bf16 v[204:207], v[120:123], v[64:67], v[204:207]\n\t" \
  "v_mfma_f32_16x16x32_bf16 v[204:207], v[100:103], v[104:107], v[204:207]\n\t" \
  "v_mfma_f32_16x16x32_bf16 v[212:215], v[100:103], v[112:115], v[212:215]\n\t" \
  "v_mfma_f32_16x16x32_bf16 v[212:215], v[120:123], v[116:119], v[212:215]\n\t" \
  "v_mfma_f32_16x16x32_bf16 v[216:219], v[96:99], v[136:139], v[216:219]\n\t" \
  "v_mfma_f32_16x16x32_bf16 v[216:219], v[132:135], v[144:147], v[216:219]\n\t" \
  "v_mfma_f32_16x16x32_bf16 v[224:227], v[108:111], v[144:147], v[224:227]\n\t" \
  "v_mfma_f32_16x16x32_bf16 v[224:227], v[148:151], v[136:139], v[224:227]\n\t" \
  "v_mfma_f32_16x16x32_bf16 v[232:235], v[64:67], v[136:139], v[232:235]\n\t" \
  "global_store_dwordx4 v2, v[42:45], %24 offset:1024 sc1\n\t" \
  "v_add_u32 v2, 0x20000, v2\n\t" \
  "v_mfma_f32_16x16x32_bf16 v[232:235], v[104:107], v[144:147], v[232:235]\n\t" \
  "v_mfma_f32_16x16x32_bf16 v[240:243], v[112:115], v[144:147], v[240:243]\n\t" \
  "v_mfma_f32_16x16x32_bf16 v[42:45], v[26:29], v[18:21], 0\n\t" \
  "v_mfma_f32_16x16x32_bf16 v[42:45], v[30:33], v[22:25], v[42:45]\n\t" \
  "v_mfma_f32_16x16x32_bf16 v[46:49], v[30:33], v[18:21], 0\n\t" \
  "v_mfma_f32_16x16x32_bf16 v[46:49], v[34:37], v[22:25], v[46:49]\n\t" \
  "ds_read_b128 v[26:29], v0 offset:1840\n\t" \
  "ds_read_b128 v[30:33], v0 offset:1328\n\t" \
  "v_mfma_f32_16x16x32_bf16 v[240:243], v[116:119], v[136:139], v[240:243]\n\t" \
  "ds_read_b128 v[34:37], v0 offset:800\n\t" \
  "v_mfma_f32_16x16x32_bf16 v[248:251], v[72:75], v[136:139], v[248:251]\n\t" \
  "v_mfma_f32_16x16x32_bf16 v[248:251], v[140:143], v[144:147], v[248:251]\n\t" \
  "v_lshlrev_b32 v50, 16, v38\n\t" \
  "v_lshlrev_b32 v51, 16, v40\n\t" \
  "v_mfma_f32_16x16x32_bf16 v[252:255], v[72:75], v[68:71], v[252:255]\n\t" \
  "v_mul_f32 v42, v42, v50\n\t" \
  "v_mul_f32 v46, v46, v51\n\t" \
  "s_waitcnt lgkmcnt(3)\n\t" \
  "v_mfma_f32_16x16x32_bf16 v[252:255], v[140:143], v[76:79], v[252:255]\n\t" \
  "v_cvt_pk_bf16_f32 v42, v42, v46\n\t" \
  "v_and_b32 v50, 0xffff0000, v38\n\t" \
  "v_mfma_f32_16x16x32_bf16 v[220:223], v[132:135], v[76:79], v[220:223]\n\t" \
  "v_and_b32 v51, 0xffff0000, v40\n\t" \
  "v_mul_f32 v43, v43, v50\n\t" \
  "v_mfma_f32_16x16x32_bf16 v[220:223], v[96:99], v[68:71], v[220:223]\n\t" \
  "v_mul_f32 v47, v47, v51\n\t" \
  "v_cvt_pk_bf16_f32 v43, v43, v47\n\t" \
  "s_waitcnt lgkmcnt(0)\n\t" \
  "s_barrier\n\t" \
  "v_mfma_f32_16x16x32_bf16 v[228:231], v[148:151], v[68:71], v[228:231]\n\t" \
  "ds_read_b64_tr_b16 v[88:89], v62 offset:26624\n\t" \
  "v_lshlrev_b32 v50, 16, v39\n\t" \
  "v_mfma_f32_16x16x32_bf16 v[228:231], v[108:111], v[76:79], v[228:231]\n\t" \
  "ds_read_b64_tr_b16 v[90:91], v62 offset:27136\n\t" \
  "v_lshlrev_b32 v51, 16, v41\n\t" \
  "v_mfma_f32_16x16x32_bf16 v[236:239], v[104:107], v[76:79], v[236:239]\n\t" \
  "ds_read_b128 v[128:131], v60 offset:10240\n\t" \
  "v_mul_f32 v44, v44, v50\n\t" \
  "v_mfma_f32_16x16x32_bf16 v[236:239], v[64:67], v[68:71], v[236:239]\n\t" \
  "ds_read_b64_tr_b16 v[100:101], v62 offset:30720\n\t" \
  "v_mul_f32 v48, v48, v51\n\t" \
  "v_mfma_f32_16x16x32_bf16 v[244:247], v[116:119], v[68:71], v[244:247]\n\t" \
  "ds_read_b64_tr_b16 v[102:103], v62 offset:31232\n\t" \
  "v_cvt_pk_bf16_f32 v44, v44, v48\n\t" \
  "v_mfma_f32_16x16x32_bf16 v[244:247], v[112:115], v[76:79], v[244:247]\n\t" \
  "ds_read_b128 v[120:123], v61 offset:10240\n\t" \
  "v_and_b32 v50, 0xffff0000, v39\n\t" \
  "v_mfma_f32_16x16x32_bf16 v[152:155], v[80:83], v[96:99], v[152:155]\n\t" \
  "ds_read_b128 v[136:139], v61 offset:12288\n\t" \
  "v_and_b32 v51, 0xffff0000, v41\n\t" \
  "v_mfma_f32_16x16x32_bf16 v[152:155], v[124:127], v[132:135], v[152:155]\n\t" \
  "ds_read_b128 v[144:147], v60 offset:12288\n\t" \
  "v_mul_f32 v45, v45, v50\n\t" \
  "v_mfma_f32_16x16x32_bf16 v[160:163], v[124:127], v[108:111], v[160:163]\n\t" \
  "ds_read_b128 v[72:75], v60 offset:14336\n\t" \
  "v_mul_f32 v49, v49, v51\n\t" \
  "v_mfma_f32_16x16x32_bf16 v[160:163], v[80:83], v[148:151], v[160:163]\n\t" \
  "ds_read_b128 v[140:143], v61 offset:14336\n\t" \
  "ds_read_b64_tr_b16 v[38:39], v1 offset:800\n\t" \
  "v_mfma_f32_16x16x32_bf16 v[168:171], v[80:83], v[64:67], v[168:171]\n\t" \
  "ds_read_b128 v[68:71], v61 offset:16384\n\t" \
  "ds_read_b64_tr_b16 v[40:41], v1 offset:288\n\t" \
  "v_mfma_f32_16x16x32_bf16 v[168:171], v[124:127], v[104:107], v[168:171]\n\t" \
  "ds_read_b128 v[76:79], v60 offset:16384\n\t" \
  "v_cvt_pk_bf16_f32 v45, v45, v49\n\t" \
  "v_mfma_f32_16x16x32_bf16 v[176:179], v[124:127], v[112:115], v[176:179]\n\t" \
  "ds_read_b64_tr_b16 v[124:125], v63 offset:26624\n\t" \
  "v_mfma_f32_16x16x32_bf16 v[176:179], v[80:83], v[116:119], v[176:179]\n\t" \
  "ds_read_b64_tr_b16 v[126:127], v63 offset:27136\n\t" \
  "v_mfma_f32_16x16x32_bf16 v[156:159], v[84:87], v[96:99], v[156:159]\n\t" \
  "ds_read_b64_tr_b16 v[80:81], v63 offset:30720\n\t" \
  "v_mfma_f32_16x16x32_bf16 v[156:159], v[92:95], v[132:135], v[156:159]\n\t" \
  "ds_read_b64_tr_b16 v[82:83], v63 offset:31232\n\t" \
  "v_mfma_f32_16x16x32_bf16 v[164:167], v[92:95], v[108:111], v[164:167]\n\t" \
  "ds_read_b64_tr_b16 v[96:97], v62 offset:34816\n\t" \
  "v_mfma_f32_16x16x32_bf16 v[164:167], v[84:87], v[148:151], v[164:167]\n\t" \
  "ds_read_b64_tr_b16 v[98:99], v62 offset:35328\n\t" \
  "v_mfma_f32_16x16x32_bf16 v[172:175], v[84:87], v[64:67], v[172:175]\n\t" \
  "ds_read_b64_tr_b16 v[132:133], v62 offset:38912\n\t" \
  "v_mfma_f32_16x16x32_bf16 v[172:175], v[92:95], v[104:107], v[172:175]\n\t" \
  "ds_read_b64_tr_b16 v[134:135], v62 offset:39424\n\t" \
  "v_mfma_f32_16x16x32_bf16 v[180:183], v[92:95], v[112:115], v[180:183]\n\t" \
  "ds_read_b128 v[108:111], v60 offset:0\n\t" \
  "v_mfma_f32_16x16x32_bf16 v[180:183], v[84:87], v[116:119], v[180:183]\n\t" \
  "ds_read_b128 v[148:151], v61 offset:0\n\t" \
  "s_waitcnt lgkmcnt(7)\n\t" \
  "v_mfma_f32_16x16x32_bf16 v[184:187], v[88:91], v[128:131], v[184:187]\n\t" \
  "ds_read_b64_tr_b16 v[64:65], v63 offset:34816\n\t" \
  "v_mfma_f32_16x16x32_bf16 v[184:187], v[100:103], v[120:123], v[184:187]\n\t" \
  "ds_read_b64_tr_b16 v[66:67], v63 offset:35328\n\t" \
  "v_mfma_f32_16x16x32_bf16 v[192:195], v[100:103], v[136:139], v[192:195]\n\t" \
  "ds_read_b64_tr_b16 v[104:105], v63 offset:38912\n\t" \
  "v_mfma_f32_16x16x32_bf16 v[192:195], v[88:91], v[144:147], v[192:195]\n\t" \
  "ds_read_b64_tr_b16 v[106:107], v63 offset:39424\n\t" \
  "v_mfma_f32_16x16x32_bf16 v[200:203], v[88:91], v[72:75], v[200:203]\n\t" \
  "ds_read_b64_tr_b16 v[92:93], v62 offset:18432\n\t" \
  "v_mfma_f32_16x16x32_bf16 v[200:203], v[100:103], v[140:143], v[200:203]\n\t" \
  "ds_read_b64_tr_b16 v[94:95], v62 offset:18944\n\t" \
  "v_mfma_f32_16x16x32_bf16 v[208:211], v[100:103], v[68:71], v[208:211]\n\t" \
  "ds_read_b64_tr_b16 v[112:113], v62 offset:22528\n\t" \
  "v_mfma_f32_16x16x32_bf16 v[208:211], v[88:91], v[76:79], v[208:211]\n\t" \
  "ds_read_b64_tr_b16 v[114:115], v62 offset:23040\n\t" \
  "v_mfma_f32_16x16x32_bf16 v[188:191], v[124:127], v[128:131], v[188:191]\n\t" \
  "ds_read_b64_tr_b16 v[84:85], v63 offset:18432\n\t" \
  "s_waitcnt lgkmcnt(7)\n\t" \
  "v_mfma_f32_16x16x32_bf16 v[188:191], v[80:83], v[120:123], v[188:191]\n\t" \
  "ds_read_b64_tr_b16 v[86:87], v63 offset:18944\n\t" \
  "v_mfma_f32_16x16x32_bf16 v[196:199], v[80:83], v[136:139], v[196:199]\n\t" \
  "ds_read_b64_tr_b16 v[116:117], v63 offset:22528\n\t" \
  "v_mfma_f32_16x16x32_bf16 v[196:199], v[124:127], v[144:147], v[196:199]\n\t" \
  "ds_read_b64_tr_b16 v[118:119], v63 offset:23040\n\t" \
  "v_mfma_f32_16x16x32_bf16 v[204:207], v[124:127], v[72:75], v[204:207]\n\t" \
  "v_mfma_f32_16x16x32_bf16 v[204:207], v[80:83], v[140:143], v[204:207]\n\t" \
  "v_mfma_f32_16x16x32_bf16 v[212:215], v[80:83], v[68:71], v[212:215]\n\t" \
  "v_mfma_f32_16x16x32_bf16 v[212:215], v[124:127], v[76:79], v[212:215]\n\t" \
  "v_mfma_f32_16x16x32_bf16 v[216:219], v[128:131], v[96:99], v[216:219]\n\t" \
  "v_mfma_f32_16x16x32_bf16 v[216:219], v[120:123], v[132:135], v[216:219]\n\t" \
  "v_mfma_f32_16x16x32_bf16 v[224:227], v[136:139], v[132:135], v[224:227]\n\t" \
  "v_mfma_f32_16x16x32_bf16 v[224:227], v[144:147], v[96:99], v[224:227]\n\t" \
  "v_mfma_f32_16x16x32_bf16 v[232:235], v[72:75], v[96:99], v[232:235]\n\t" \
  "global_store_dwordx4 v2, v[42:45], %24 offset:0 sc1\n\t" \
  "v_mfma_f32_16x16x32_bf16 v[232:235], v[140:143], v[132:135], v[232:235]\n\t" \
  "s_nop 0\n\t" \
  "v_mfma_f32_16x16x32_bf16 v[42:45], v[26:29], v[18:21], 0\n\t" \
  "v_mfma_f32_16x16x32_bf16 v[42:45], v[30:33], v[22:25], v[42:45]\n\t" \
  "v_mfma_f32_16x16x32_bf16 v[46:49], v[30:33], v[18:21], 0\n\t" \
  "v_mfma_f32_16x16x32_bf16 v[46:49], v[34:37], v[22:25], v[46:49]\n\t" \
  "v_mfma_f32_16x16x32_bf16 v[240:243], v[68:71], v[132:135], v[240:243]\n\t" \
  "v_mfma_f32_16x16x32_bf16 v[240:243], v[76:79], v[96:99], v[240:243]\n\t" \
  "v_lshlrev_b32 v50, 16, v38\n\t" \
  "v_lshlrev_b32 v51, 16, v40\n\t" \
  "v_mfma_f32_16x16x32_bf16 v[248:251], v[108:111], v[96:99], v[248:251]\n\t" \
  "s_nop 0\n\t" \
  "v_mul_f32 v42, v42, v50\n\t" \
  "s_nop 0\n\t" \
  "v_mul_f32 v46, v46, v51\n\t" \
  "v_mfma_f32_16x16x32_bf16 v[248:251], v[148:151], v[132:135], v[248:251]\n\t" \
  "v_cvt_pk_bf16_f32 v42, v42, v46\n\t" \
  "v_and_b32 v50, 0xffff0000, v38\n\t" \
  "v_mfma_f32_16x16x32_bf16 v[252:255], v[108:111], v[64:67], v[252:255]\n\t" \
  "v_and_b32 v51, 0xffff0000, v40\n\t" \
  "v_mul_f32 v43, v43, v50\n\t" \
  "s_waitcnt lgkmcnt(0)\n\t" \
  "v_mfma_f32_16x16x32_bf16 v[252:255], v[148:151], v[104:107], v[252:255]\n\t" \
  "v_mul_f32 v47, v47, v51\n\t" \
  "v_cvt_pk_bf16_f32 v43, v43, v47\n\t" \
  "v_mfma_f32_16x16x32_bf16 v[220:223], v[120:123], v[104:107], v[220:223]\n\t" \
  "v_lshlrev_b32 v50, 16, v39\n\t" \
  "v_lshlrev_b32 v51, 16, v41\n\t" \
  "v_mfma_f32_16x16x32_bf16 v[220:223], v[128:131], v[64:67], v[220:223]\n\t" \
  "v_mul_f32 v44, v44, v50\n\t" \
  "v_mul_f32 v48, v48, v51\n\t" \
  "s_barrier\n\t" \
  "v_mfma_f32_16x16x32_bf16 v[228:231], v[144:147], v[64:67], v[228:231]\n\t" \
  "ds_read_b64_tr_b16 v[100:101], v54 offset:26624\n\t" \
  "v_cvt_pk_bf16_f32 v44, v44, v48\n\t" \
  "v_mfma_f32_16x16x32_bf16 v[228:231], v[136:139], v[104:107], v[228:231]\n\t" \
  "ds_read_b64_tr_b16 v[102:103], v54 offset:27136\n\t" \
  "v_and_b32 v50, 0xffff0000, v39\n\t" \
  "v_mfma_f32_16x16x32_bf16 v[236:239], v[140:143], v[104:107], v[236:239]\n\t" \
  "ds_read_b128 v[88:91], v52 offset:10240\n\t" \
  "v_and_b32 v51, 0xffff0000, v41\n\t" \
  "v_mfma_f32_16x16x32_bf16 v[236:239], v[72:75], v[64:67], v[236:239]\n\t" \
  "ds_read_b64_tr_b16 v[80:81], v54 offset:30720\n\t" \
  "v_mul_f32 v45, v45, v50\n\t" \
  "v_mfma_f32_16x16x32_bf16 v[244:247], v[76:79], v[64:67], v[244:247]\n\t" \
  "ds_read_b64_tr_b16 v[82:83], v54 offset:31232\n\t" \
  "v_mul_f32 v49, v49, v51\n\t" \
  "v_mfma_f32_16x16x32_bf16 v[244:247], v[68:71], v[104:107], v[244:247]\n\t" \
  "ds_read_b128 v[124:127], v53 offset:10240\n\t" \
  "v_cvt_pk_bf16_f32 v45, v45, v49\n\t" \
  "v_mfma_f32_16x16x32_bf16 v[152:155], v[92:95], v[128:131], v[152:155]\n\t" \
  "ds_read_b128 v[96:99], v53 offset:12288\n\t" \
  "v_mfma_f32_16x16x32_bf16 v[152:155], v[112:115], v[120:123], v[152:155]\n\t" \
  "ds_read_b128 v[132:135], v52 offset:12288\n\t" \
  "v_mfma_f32_16x16x32_bf16 v[160:163], v[112:115], v[136:139], v[160:163]\n\t" \
  "ds_read_b128 v[108:111], v52 offset:14336\n\t" \
  "v_mfma_f32_16x16x32_bf16 v[160:163], v[92:95], v[144:147], v[160:163]\n\t" \
  "ds_read_b128 v[148:151], v53 offset:14336\n\t" \
  "v_mfma_f32_16x16x32_bf16 v[168:171], v[92:95], v[72:75], v[168:171]\n\t" \
  "ds_read_b128 v[64:67], v53 offset:16384\n\t" \
  "v_mfma_f32_16x16x32_bf16 v[168:171], v[112:115], v[140:143], v[168:171]\n\t" \
  "ds_read_b128 v[104:107], v52 offset:16384\n\t" \
  "v_mfma_f32_16x16x32_bf16 v[176:179], v[112:115], v[68:71], v[176:179]\n\t" \
  "ds_read_b64_tr_b16 v[112:113], v55 offset:26624\n\t" \
  "v_mfma_f32_16x16x32_bf16 v[176:179], v[92:95], v[76:79], v[176:179]\n\t" \
  "ds_read_b64_tr_b16 v[114:115], v55 offset:27136\n\t" \
  "v_mfma_f32_16x16x32_bf16 v[156:159], v[84:87], v[128:131], v[156:159]\n\t" \
  "ds_read_b64_tr_b16 v[92:93], v55 offset:30720\n\t" \
  "v_mfma_f32_16x16x32_bf16 v[156:159], v[116:119], v[120:123], v[156:159]\n\t" \
  "ds_read_b64_tr_b16 v[94:95], v55 offset:31232\n\t" \
  "v_mfma_f32_16x16x32_bf16 v[164:167], v[116:119], v[136:139], v[164:167]\n\t" \
  "ds_read_b64_tr_b16 v[128:129], v54 offset:34816\n\t" \
  "v_mfma_f32_16x16x32_bf16 v[164:167], v[84:87], v[144:147], v[164:167]\n\t" \
  "ds_read_b64_tr_b16 v[130:131], v54 offset:35328\n\t" \
  "v_mfma_f32_16x16x32_bf16 v[172:175], v[84:87], v[72:75], v[172:175]\n\t" \
  "ds_read_b64_tr_b16 v[120:121], v54 offset:38912\n\t" \
  "v_mfma_f32_16x16x32_bf16 v[172:175], v[116:119], v[140:143], v[172:175]\n\t" \
  "ds_read_b64_tr_b16 v[122:123], v54 offset:39424\n\t" \
  "v_mfma_f32_16x16x32_bf16 v[180:183], v[116:119], v[68:71], v[180:183]\n\t" \
  "ds_read_b128 v[136:139], v52 offset:0\n\t" \
  "v_mfma_f32_16x16x32_bf16 v[180:183], v[84:87], v[76:79], v[180:183]\n\t" \
  "ds_read_b128 v[144:147], v53 offset:0\n\t" \
  "s_waitcnt lgkmcnt(7)\n\t" \
  "v_mfma_f32_16x16x32_bf16 v[184:187], v[100:103], v[88:91], v[184:187]\n\t" \
  "ds_read_b64_tr_b16 v[72:73], v55 offset:34816\n\t" \
  "v_mfma_f32_16x16x32_bf16 v[184:187], v[80:83], v[124:127], v[184:187]\n\t" \
  "ds_read_b64_tr_b16 v[74:75], v55 offset:35328\n\t" \
  "v_mfma_f32_16x16x32_bf16 v[192:195], v[80:83], v[96:99], v[192:195]\n\t" \
  "ds_read_b64_tr_b16 v[140:141], v55 offset:38912\n\t" \
  "v_mfma_f32_16x16x32_bf16 v[192:195], v[100:103], v[132:135], v[192:195]\n\t" \
  "ds_read_b64_tr_b16 v[142:143], v55 offset:39424\n\t" \
  "v_mfma_f32_16x16x32_bf16 v[200:203], v[100:103], v[108:111], v[200:203]\n\t" \
  "ds_read_b64_tr_b16 v[68:69], v54 offset:18432\n\t" \
  "v_mfma_f32_16x16x32_bf16 v[200:203], v[80:83], v[148:151], v[200:203]\n\t" \
  "ds_read_b64_tr_b16 v[70:71], v54 offset:18944\n\t" \
  "v_mfma_f32_16x16x32_bf16 v[208:211], v[80:83], v[64:67], v[208:211]\n\t" \
  "ds_read_b64_tr_b16 v[116:117], v54 offset:22528\n\t" \
  "v_mfma_f32_16x16x32_bf16 v[208:211], v[100:103], v[104:107], v[208:211]\n\t" \
  "ds_read_b64_tr_b16 v[118:119], v54 offset:23040\n\t" \
  "v_mfma_f32_16x16x32_bf16 v[188:191], v[112:115], v[88:91], v[188:191]\n\t" \
  "ds_read_b64_tr_b16 v[76:77], v55 offset:18432\n\t" \
  "s_waitcnt lgkmcnt(7)\n\t" \
  "v_mfma_f32_16x16x32_bf16 v[188:191], v[92:95], v[124:127], v[188:191]\n\t" \
  "ds_read_b64_tr_b16 v[78:79], v55 offset:18944\n\t" \
  "v_mfma_f32_16x16x32_bf16 v[196:199], v[92:95], v[96:99], v[196:199]\n\t" \
  "ds_read_b64_tr_b16 v[84:85], v55 offset:22528\n\t" \
  "v_mfma_f32_16x16x32_bf16 v[196:199], v[112:115], v[132:135], v[196:199]\n\t" \
  "ds_read_b64_tr_b16 v[86:87], v55 offset:23040\n\t" \
  "v_mfma_f32_16x16x32_bf16 v[204:207], v[112:115], v[108:111], v[204:207]\n\t" \
  "v_mfma_f32_16x16x32_bf16 v[204:207], v[92:95], v[148:151], v[204:207]\n\t" \
  "v_mfma_f32_16x16x32_bf16 v[212:215], v[92:95], v[64:67], v[212:215]\n\t" \
  "v_mfma_f32_16x16x32_bf16 v[212:215], v[112:115], v[104:107], v[212:215]\n\t" \
  "v_mfma_f32_16x16x32_bf16 v[216:219], v[88:91], v[128:131], v[216:219]\n\t" \
  "v_mfma_f32_16x16x32_bf16 v[216:219], v[124:127], v[120:123], v[216:219]\n\t" \
  "v_mfma_f32_16x16x32_bf16 v[224:227], v[96:99], v[120:123], v[224:227]\n\t" \
  "v_mfma_f32_16x16x32_bf16 v[224:227], v[132:135], v[128:131], v[224:227]\n\t" \
  "v_mfma_f32_16x16x32_bf16 v[232:235], v[108:111], v[128:131], v[232:235]\n\t" \
  "global_store_dwordx4 v2, v[42:45], %24 offset:1024 sc1\n\t" \
  "v_mfma_f32_16x16x32_bf16 v[232:235], v[148:151], v[120:123], v[232:235]\n\t" \
  "v_mfma_f32_16x16x32_bf16 v[240:243], v[64:67], v[120:123], v[240:243]\n\t" \
  "v_mfma_f32_16x16x32_bf16 v[240:243], v[104:107], v[128:131], v[240:243]\n\t" \
  "v_mov_b32 v30, 0xff61b1e6\n\t" \
  "v_bfe_i32 v34, v3, 0, 1\n\t" \
  "v_mfma_f32_16x16x32_bf16 v[248:251], v[136:139], v[128:131], v[248:251]\n\t" \
  "v_and_b32 v34, v30, v34\n\t" \
  "v_bfe_i32 v38, v3, 4, 1\n\t" \
  "v_mfma_f32_16x16x32_bf16 v[248:251], v[144:147], v[120:123], v[248:251]\n\t" \
  "v_and_b32 v38, v30, v38\n\t" \
  "v_bfe_i32 v42, v3, 8, 1\n\t" \
  "v_mfma_f32_16x16x32_bf16 v[252:255], v[136:139], v[72:75], v[252:255]\n\t" \
  "v_bfi_b32 v42, v42, v30, %15\n\t" \
  "v_mov_b32 v48, 0x3f803f80\n\t" \
  "s_waitcnt lgkmcnt(0)\n\t" \
  "v_mfma_f32_16x16x32_bf16 v[252:255], v[144:147], v[140:143], v[252:255]\n\t" \
  "v_bfe_i32 v35, v3, 1, 1\n\t" \
  "v_and_b32 v35, v30, v35\n\t" \
  "v_mfma_f32_16x16x32_bf16 v[220:223], v[124:127], v[140:143], v[220:223]\n\t" \
  "v_bfe_i32 v39, v3, 5, 1\n\t" \
  "v_and_b32 v39, v30, v39\n\t" \
  "v_mfma_f32_16x16x32_bf16 v[220:223], v[88:91], v[72:75], v[220:223]\n\t" \
  "v_bfe_i32 v43, v3, 9, 1\n\t" \
  "v_bfi_b32 v43, v43, v30, %15\n\t" \
  "s_barrier\n\t" \
  "v_mfma_f32_16x16x32_bf16 v[228:231], v[132:135], v[72:75], v[228:231]\n\t" \
  "ds_read_b64_tr_b16 v[80:81], v58 offset:26624\n\t" \
  "v_mov_b32 v49, 0x3f803f80\n\t" \
  "v_mfma_f32_16x16x32_bf16 v[228:231], v[96:99], v[140:143], v[228:231]\n\t" \
  "ds_read_b64_tr_b16 v[82:83], v58 offset:27136\n\t" \
  "v_bfe_i32 v36, v3, 2, 1\n\t" \
  "v_mfma_f32_16x16x32_bf16 v[236:239], v[148:151], v[140:143], v[236:239]\n\t" \
  "ds_read_b128 v[100:103], v56 offset:10240\n\t" \
  "v_and_b32 v36, v30, v36\n\t" \
  "v_mfma_f32_16x16x32_bf16 v[236:239], v[108:111], v[72:75], v[236:239]\n\t" \
  "ds_read_b64_tr_b16 v[92:93], v58 offset:30720\n\t" \
  "v_bfe_i32 v40, v3, 6, 1\n\t" \
  "v_mfma_f32_16x16x32_bf16 v[244:247], v[104:107], v[72:75], v[244:247]\n\t" \
  "ds_read_b64_tr_b16 v[94:95], v58 offset:31232\n\t" \
  "v_and_b32 v40, v30, v40\n\t" \
  "v_mfma_f32_16x16x32_bf16 v[244:247], v[64:67], v[140:143], v[244:247]\n\t" \
  "ds_read_b128 v[112:115], v57 offset:10240\n\t" \
  "v_bfe_i32 v44, v3, 10, 1\n\t" \
  "v_mfma_f32_16x16x32_bf16 v[152:155], v[68:71], v[88:91], v[152:155]\n\t" \
  "ds_read_b128 v[128:131], v57 offset:12288\n\t" \
  "v_bfi_b32 v44, v44, v30, %15\n\t" \
  "v_mfma_f32_16x16x32_bf16 v[152:155], v[116:119], v[124:127], v[152:155]\n\t" \
  "ds_read_b128 v[120:123], v56 offset:12288\n\t" \
  "v_mov_b32 v50, 0x3f803f80\n\t" \
  "v_mfma_f32_16x16x32_bf16 v[160:163], v[116:119], v[96:99], v[160:163]\n\t" \
  "ds_read_b128 v[136:139], v56 offset:14336\n\t" \
  "v_bfe_i32 v37, v3, 3, 1\n\t" \
  "v_mfma_f32_16x16x32_bf16 v[160:163], v[68:71], v[132:135], v[160:163]\n\t" \
  "ds_read_b128 v[144:147], v57 offset:14336\n\t" \
  "v_and_b32 v37, v30, v37\n\t" \
  "v_mfma_f32_16x16x32_bf16 v[168:171], v[68:71], v[108:111], v[168:171]\n\t" \
  "ds_read_b128 v[72:75], v57 offset:16384\n\t" \
  "v_bfe_i32 v41, v3, 7, 1\n\t" \
  "v_mfma_f32_16x16x32_bf16 v[168:171], v[116:119], v[148:151], v[168:171]\n\t" \
  "ds_read_b128 v[140:143], v56 offset:16384\n\t" \
  "v_and_b32 v41, v30, v41\n\t" \
  "v_mfma_f32_16x16x32_bf16 v[176:179], v[116:119], v[64:67], v[176:179]\n\t" \
  "ds_read_b64_tr_b16 v[116:117], v59 offset:26624\n\t" \
  "v_bfe_i32 v45, v3, 11, 1\n\t" \
  "v_mfma_f32_16x16x32_bf16 v[176:179], v[68:71], v[104:107], v[176:179]\n\t" \
  "ds_read_b64_tr_b16 v[118:119], v59 offset:27136\n\t" \
  "v_bfi_b32 v45, v45, v30, %15\n\t" \
  "v_mfma_f32_16x16x32_bf16 v[156:159], v[76:79], v[88:91], v[156:159]\n\t" \
  "ds_read_b64_tr_b16 v[68:69], v59 offset:30720\n\t" \
  "v_mov_b32 v51, 0x3f803f80\n\t" \
  "v_mfma_f32_16x16x32_bf16 v[156:159], v[84:87], v[124:127], v[156:159]\n\t" \
  "ds_read_b64_tr_b16 v[70:71], v59 offset:31232\n\t" \
  "v_bfe_i32 v46, v3, 12, 1\n\t" \
  "v_mfma_f32_16x16x32_bf16 v[164:167], v[84:87], v[96:99], v[164:167]\n\t" \
  "ds_read_b64_tr_b16 v[88:89], v58 offset:34816\n\t" \
  "v_bfe_i32 v31, v3, 13, 1\n\t" \
  "v_mfma_f32_16x16x32_bf16 v[164:167], v[76:79], v[132:135], v[164:167]\n\t" \
  "ds_read_b64_tr_b16 v[90:91], v58 offset:35328\n\t" \
  "v_perm_b32 v46, v31, v46, vcc_hi\n\t" \
  "v_mfma_f32_16x16x32_bf16 v[172:175], v[76:79], v[108:111], v[172:175]\n\t" \
  "ds_read_b64_tr_b16 v[124:125], v58 offset:38912\n\t" \
  "v_bfe_i32 v47, v3, 14, 1\n\t" \
  "v_mfma_f32_16x16x32_bf16 v[172:175], v[84:87], v[148:151], v[172:175]\n\t" \
  "ds_read_b64_tr_b16 v[126:127], v58 offset:39424\n\t" \
  "v_bfe_i32 v31, v3, 15, 1\n\t" \
  "v_mfma_f32_16x16x32_bf16 v[180:183], v[84:87], v[64:67], v[180:183]\n\t" \
  "ds_read_b128 v[96:99], v56 offset:0\n\t" \
  "v_perm_b32 v47, v31, v47, vcc_hi\n\t" \
  "v_mfma_f32_16x16x32_bf16 v[180:183], v[76:79], v[104:107], v[180:183]\n\t" \
  "ds_read_b128 v[132:135], v57 offset:0\n\t" \
  "v_add_u32 v30, 18432, v4\n\t" \
  "s_waitcnt lgkmcnt(7)\n\t" \
  "v_mfma_f32_16x16x32_bf16 v[184:187], v[80:83], v[100:103], v[184:187]\n\t" \
  "ds_read_b64_tr_b16 v[108:109], v59 offset:34816\n\t" \
  "v_xor_b32 v31, 1024, v30\n\t" \
  "v_mfma_f32_16x16x32_bf16 v[184:187], v[92:95], v[112:115], v[184:187]\n\t" \
  "ds_read_b64_tr_b16 v[110:111], v59 offset:35328\n\t" \
  "v_mov_b32 v32, %12\n\t" \
  "v_mfma_f32_16x16x32_bf16 v[192:195], v[92:95], v[128:131], v[192:195]\n\t" \
  "ds_read_b64_tr_b16 v[148:149], v59 offset:38912\n\t" \
  "v_xor_b32 v33, 4, v32\n\t" \
  "v_mfma_f32_16x16x32_bf16 v[192:195], v[80:83], v[120:123], v[192:195]\n\t" \
  "ds_read_b64_tr_b16 v[150:151], v59 offset:39424\n\t" \
  "v_mov_b32 v0, 1\n\t" \
  "v_mfma_f32_16x16x32_bf16 v[200:203], v[80:83], v[136:139], v[200:203]\n\t" \
  "ds_read_b64_tr_b16 v[64:65], v58 offset:18432\n\t" \
  "v_and_b32 v4, 1023, v4\n\t" \
  "v_mfma_f32_16x16x32_bf16 v[200:203], v[92:95], v[144:147], v[200:203]\n\t" \
  "ds_read_b64_tr_b16 v[66:67], v58 offset:18944\n\t" \
  "v_add_u32 v1, 16384, v4\n\t" \
  "v_mfma_f32_16x16x32_bf16 v[208:211], v[92:95], v[72:75], v[208:211]\n\t" \
  "ds_read_b64_tr_b16 v[84:85], v58 offset:22528\n\t" \
  "v_mfma_f32_16x16x32_bf16 v[208:211], v[80:83], v[140:143], v[208:211]\n\t" \
  "ds_read_b64_tr_b16 v[86:87], v58 offset:23040\n\t" \
  "v_mfma_f32_16x16x32_bf16 v[188:191], v[116:119], v[100:103], v[188:191]\n\t" \
  "ds_read_b64_tr_b16 v[76:77], v59 offset:18432\n\t" \
  "s_waitcnt lgkmcnt(7)\n\t" \
  "v_mfma_f32_16x16x32_bf16 v[188:191], v[68:71], v[112:115], v[188:191]\n\t" \
  "ds_read_b64_tr_b16 v[78:79], v59 offset:18944\n\t" \
  "v_mfma_f32_16x16x32_bf16 v[196:199], v[68:71], v[128:131], v[196:199]\n\t" \
  "ds_read_b64_tr_b16 v[104:105], v59 offset:22528\n\t" \
  "v_mfma_f32_16x16x32_bf16 v[196:199], v[116:119], v[120:123], v[196:199]\n\t" \
  "ds_read_b64_tr_b16 v[106:107], v59 offset:23040\n\t" \
  "v_mfma_f32_16x16x32_bf16 v[204:207], v[116:119], v[136:139], v[204:207]\n\t" \
  "v_mfma_f32_16x16x32_bf16 v[204:207], v[68:71], v[144:147], v[204:207]\n\t" \
  "v_mfma_f32_16x16x32_bf16 v[212:215], v[68:71], v[72:75], v[212:215]\n\t" \
  "v_mfma_f32_16x16x32_bf16 v[212:215], v[116:119], v[140:143], v[212:215]\n\t" \
  "v_mfma_f32_16x16x32_bf16 v[216:219], v[100:103], v[88:91], v[216:219]\n\t" \
  "v_mfma_f32_16x16x32_bf16 v[216:219], v[112:115], v[124:127], v[216:219]\n\t" \
  "v_mfma_f32_16x16x32_bf16 v[224:227], v[128:131], v[124:127], v[224:227]\n\t" \
  "v_mfma_f32_16x16x32_bf16 v[224:227], v[120:123], v[88:91], v[224:227]\n\t" \
  "v_mfma_f32_16x16x32_bf16 v[232:235], v[136:139], v[88:91], v[232:235]\n\t" \
  "v_mfma_f32_16x16x32_bf16 v[232:235], v[144:147], v[124:127], v[232:235]\n\t" \
  "v_mfma_f32_16x16x32_bf16 v[240:243], v[72:75], v[124:127], v[240:243]\n\t" \
  "v_mfma_f32_16x16x32_bf16 v[240:243], v[140:143], v[88:91], v[240:243]\n\t" \
  "v_mfma_f32_16x16x32_bf16 v[248:251], v[96:99], v[88:91], v[248:251]\n\t" \
  "v_mfma_f32_16x16x32_bf16 v[248:251], v[132:135], v[124:127], v[248:251]\n\t" \
  "v_mfma_f32_16x16x32_bf16 v[252:255], v[96:99], v[108:111], v[252:255]\n\t" \
  "s_waitcnt lgkmcnt(0)\n\t" \
  "v_mfma_f32_16x16x32_bf16 v[252:255], v[132:135], v[148:151], v[252:255]\n\t" \
  "v_mfma_f32_16x16x32_bf16 v[220:223], v[112:115], v[148:151], v[220:223]\n\t" \
  "v_mfma_f32_16x16x32_bf16 v[220:223], v[100:103], v[108:111], v[220:223]\n\t" \
  "s_barrier\n\t" \
  "v_mfma_f32_16x16x32_bf16 v[228:231], v[120:123], v[108:111], v[228:231]\n\t" \
  "ds_read_b64_tr_b16 v[92:93], v62 offset:26624\n\t" \
  "v_mfma_f32_16x16x32_bf16 v[228:231], v[128:131], v[148:151], v[228:231]\n\t" \
  "ds_read_b64_tr_b16 v[94:95], v62 offset:27136\n\t" \
  "v_mfma_f32_16x16x32_bf16 v[236:239], v[144:147], v[148:151], v[236:239]\n\t" \
  "ds_read_b128 v[80:83], v60 offset:10240\n\t" \
  "v_mfma_f32_16x16x32_bf16 v[236:239], v[136:139], v[108:111], v[236:239]\n\t" \
  "ds_read_b64_tr_b16 v[68:69], v62 offset:30720\n\t" \
  "v_mfma_f32_16x16x32_bf16 v[244:247], v[140:143], v[108:111], v[244:247]\n\t" \
  "ds_read_b64_tr_b16 v[70:71], v62 offset:31232\n\t" \
  "v_mfma_f32_16x16x32_bf16 v[244:247], v[72:75], v[148:151], v[244:247]\n\t" \
  "ds_read_b128 v[116:119], v61 offset:10240\n\t" \
  "v_mfma_f32_16x16x32_bf16 v[152:155], v[64:67], v[100:103], v[152:155]\n\t" \
  "ds_read_b128 v[88:91], v61 offset:12288\n\t" \
  "v_mfma_f32_16x16x32_bf16 v[152:155], v[84:87], v[112:115], v[152:155]\n\t" \
  "ds_read_b128 v[124:127], v60 offset:12288\n\t" \
  "v_mfma_f32_16x16x32_bf16 v[160:163], v[84:87], v[128:131], v[160:163]\n\t" \
  "ds_read_b128 v[96:99], v60 offset:14336\n\t" \
  "v_mfma_f32_16x16x32_bf16 v[160:163], v[64:67], v[120:123], v[160:163]\n\t" \
  "ds_read_b128 v[132:135], v61 offset:14336\n\t" \
  "v_mfma_f32_16x16x32_bf16 v[168:171], v[64:67], v[136:139], v[168:171]\n\t" \
  "ds_read_b128 v[108:111], v61 offset:16384\n\t" \
  "v_mfma_f32_16x16x32_bf16 v[168:171], v[84:87], v[144:147], v[168:171]\n\t" \
  "ds_read_b128 v[148:151], v60 offset:16384\n\t" \
  "v_mfma_f32_16x16x32_bf16 v[176:179], v[84:87], v[72:75], v[176:179]\n\t" \
  "ds_read_b64_tr_b16 v[84:85], v63 offset:26624\n\t" \
  "v_mfma_f32_16x16x32_bf16 v[176:179], v[64:67], v[140:143], v[176:179]\n\t" \
  "ds_read_b64_tr_b16 v[86:87], v63 offset:27136\n\t" \
  "v_mfma_f32_16x16x32_bf16 v[156:159], v[76:79], v[100:103], v[156:159]\n\t" \
  "ds_read_b64_tr_b16 v[64:65], v63 offset:30720\n\t" \
  "v_mfma_f32_16x16x32_bf16 v[156:159], v[104:107], v[112:115], v[156:159]\n\t" \
  "ds_read_b64_tr_b16 v[66:67], v63 offset:31232\n\t" \
  "v_mfma_f32_16x16x32_bf16 v[164:167], v[104:107], v[128:131], v[164:167]\n\t" \
  "ds_read_b64_tr_b16 v[100:101], v62 offset:34816\n\t" \
  "v_mfma_f32_16x16x32_bf16 v[164:167], v[76:79], v[120:123], v[164:167]\n\t" \
  "ds_read_b64_tr_b16 v[102:103], v62 offset:35328\n\t" \
  "v_mfma_f32_16x16x32_bf16 v[172:175], v[76:79], v[136:139], v[172:175]\n\t" \
  "ds_read_b64_tr_b16 v[112:113], v62 offset:38912\n\t" \
  "v_mfma_f32_16x16x32_bf16 v[172:175], v[104:107], v[144:147], v[172:175]\n\t" \
  "ds_read_b64_tr_b16 v[114:115], v62 offset:39424\n\t" \
  "v_mfma_f32_16x16x32_bf16 v[180:183], v[104:107], v[72:75], v[180:183]\n\t" \
  "ds_read_b128 v[128:131], v60 offset:0\n\t" \
  "v_mfma_f32_16x16x32_bf16 v[180:183], v[76:79], v[140:143], v[180:183]\n\t" \
  "ds_read_b128 v[120:123], v61 offset:0\n\t" \
  "s_waitcnt lgkmcnt(7)\n\t" \
  "v_mfma_f32_16x16x32_bf16 v[184:187], v[92:95], v[80:83], v[184:187]\n\t" \
  "ds_read_b64_tr_b16 v[136:137], v63 offset:34816\n\t" \
  "v_mfma_f32_16x16x32_bf16 v[184:187], v[68:71], v[116:119], v[184:187]\n\t" \
  "ds_read_b64_tr_b16 v[138:139], v63 offset:35328\n\t" \
  "v_mfma_f32_16x16x32_bf16 v[192:195], v[68:71], v[88:91], v[192:195]\n\t" \
  "ds_read_b64_tr_b16 v[144:145], v63 offset:38912\n\t" \
  "v_mfma_f32_16x16x32_bf16 v[192:195], v[92:95], v[124:127], v[192:195]\n\t" \
  "ds_read_b64_tr_b16 v[146:147], v63 offset:39424\n\t" \
  "v_mfma_f32_16x16x32_bf16 v[200:203], v[92:95], v[96:99], v[200:203]\n\t" \
  "ds_read_b64_tr_b16 v[72:73], v62 offset:18432\n\t" \
  "v_mfma_f32_16x16x32_bf16 v[200:203], v[68:71], v[132:135], v[200:203]\n\t" \
  "ds_read_b64_tr_b16 v[74:75], v62 offset:18944\n\t" \
  "v_mfma_f32_16x16x32_bf16 v[208:211], v[68:71], v[108:111], v[208:211]\n\t" \
  "ds_read_b64_tr_b16 v[104:105], v62 offset:22528\n\t" \
  "v_mfma_f32_16x16x32_bf16 v[208:211], v[92:95], v[148:151], v[208:211]\n\t" \
  "ds_read_b64_tr_b16 v[106:107], v62 offset:23040\n\t" \
  "v_mfma_f32_16x16x32_bf16 v[188:191], v[84:87], v[80:83], v[188:191]\n\t" \
  "ds_read_b64_tr_b16 v[76:77], v63 offset:18432\n\t" \
  "s_waitcnt lgkmcnt(7)\n\t" \
  "v_mfma_f32_16x16x32_bf16 v[188:191], v[64:67], v[116:119], v[188:191]\n\t" \
  "ds_read_b64_tr_b16 v[78:79], v63 offset:18944\n\t" \
  "v_mfma_f32_16x16x32_bf16 v[196:199], v[64:67], v[88:91], v[196:199]\n\t" \
  "ds_read_b64_tr_b16 v[140:141], v63 offset:22528\n\t" \
  "v_mfma_f32_16x16x32_bf16 v[196:199], v[84:87], v[124:127], v[196:199]\n\t" \
  "ds_read_b64_tr_b16 v[142:143], v63 offset:23040\n\t" \
  "v_mfma_f32_16x16x32_bf16 v[204:207], v[84:87], v[96:99], v[204:207]\n\t" \
  "v_mfma_f32_16x16x32_bf16 v[204:207], v[64:67], v[132:135], v[204:207]\n\t" \
  "v_mfma_f32_16x16x32_bf16 v[212:215], v[64:67], v[108:111], v[212:215]\n\t" \
  "v_mfma_f32_16x16x32_bf16 v[212:215], v[84:87], v[148:151], v[212:215]\n\t" \
  "v_mfma_f32_16x16x32_bf16 v[216:219], v[80:83], v[100:103], v[216:219]\n\t" \
  "501:\n\t" \
  "s_cbranch_execz 505f\n\t" \
  "v_mfma_f32_16x16x32_bf16 v[216:219], v[116:119], v[112:115], v[216:219]\n\t" \
  "v_mfma_f32_16x16x32_bf16 v[224:227], v[88:91], v[112:115], v[224:227]\n\t" \
  "v_mfma_f32_16x16x32_bf16 v[224:227], v[124:127], v[100:103], v[224:227]\n\t" \
  "v_mfma_f32_16x16x32_bf16 v[232:235], v[96:99], v[100:103], v[232:235]\n\t" \
  "v_mfma_f32_16x16x32_bf16 v[232:235], v[132:135], v[112:115], v[232:235]\n\t" \
  "v_mfma_f32_16x16x32_bf16 v[240:243], v[108:111], v[112:115], v[240:243]\n\t" \
  "v_mfma_f32_16x16x32_bf16 v[240:243], v[148:151], v[100:103], v[240:243]\n\t" \
  "v_mfma_f32_16x16x32_bf16 v[248:251], v[128:131], v[100:103], v[248:251]\n\t" \
  "v_mfma_f32_16x16x32_bf16 v[248:251], v[120:123], v[112:115], v[248:251]\n\t" \
  "v_mfma_f32_16x16x32_bf16 v[252:255], v[128:131], v[136:139], v[252:255]\n\t" \
  "s_waitcnt lgkmcnt(0)\n\t" \
  "v_mfma_f32_16x16x32_bf16 v[252:255], v[120:123], v[144:147], v[252:255]\n\t" \
  "v_mfma_f32_16x16x32_bf16 v[220:223], v[116:119], v[144:147], v[220:223]\n\t" \
  "v_mfma_f32_16x16x32_bf16 v[220:223], v[80:83], v[136:139], v[220:223]\n\t" \
  "s_waitcnt vmcnt(0)\n\t" \
  "s_barrier\n\t" \
  "v_mfma_f32_16x16x32_bf16 v[228:231], v[124:127], v[136:139], v[228:231]\n\t" \
  "ds_read_b64_tr_b16 v[68:69], v54 offset:26624\n\t" \
  "v_mfma_f32_16x16x32_bf16 v[228:231], v[88:91], v[144:147], v[228:231]\n\t" \
  "ds_read_b64_tr_b16 v[70:71], v54 offset:27136\n\t" \
  "v_mfma_f32_16x16x32_bf16 v[236:239], v[132:135], v[144:147], v[236:239]\n\t" \
  "ds_read_b128 v[92:95], v52 offset:10240\n\t" \
  "v_mfma_f32_16x16x32_bf16 v[236:239], v[96:99], v[136:139], v[236:239]\n\t" \
  "ds_read_b64_tr_b16 v[64:65], v54 offset:30720\n\t" \
  "v_mfma_f32_16x16x32_bf16 v[244:247], v[148:151], v[136:139], v[244:247]\n\t" \
  "ds_read_b64_tr_b16 v[66:67], v54 offset:31232\n\t" \
  "v_mfma_f32_16x16x32_bf16 v[244:247], v[108:111], v[144:147], v[244:247]\n\t" \
  "ds_read_b128 v[84:87], v53 offset:10240\n\t" \
  "v_mfma_f32_16x16x32_bf16 v[152:155], v[72:75], v[80:83], v[152:155]\n\t" \
  "ds_read_b128 v[100:103], v53 offset:12288\n\t" \
  "v_mfma_f32_16x16x32_bf16 v[152:155], v[104:107], v[116:119], v[152:155]\n\t" \
  "ds_read_b128 v[112:115], v52 offset:12288\n\t" \
  "v_mfma_f32_16x16x32_bf16 v[160:163], v[104:107], v[88:91], v[160:163]\n\t" \
  "ds_read_b128 v[128:131], v52 offset:14336\n\t" \
  "v_mfma_f32_16x16x32_bf16 v[160:163], v[72:75], v[124:127], v[160:163]\n\t" \
  "ds_read_b128 v[120:123], v53 offset:14336\n\t" \
  "v_mfma_f32_16x16x32_bf16 v[168:171], v[72:75], v[96:99], v[168:171]\n\t" \
  "ds_read_b128 v[136:139], v53 offset:16384\n\t" \
  "v_mfma_f32_16x16x32_bf16 v[168:171], v[104:107], v[132:135], v[168:171]\n\t" \
  "ds_read_b128 v[144:147], v52 offset:16384\n\t" \
  "v_mfma_f32_16x16x32_bf16 v[176:179], v[104:107], v[108:111], v[176:179]\n\t" \
  "ds_read_b64_tr_b16 v[104:105], v55 offset:26624\n\t" \
  "v_mfma_f32_16x16x32_bf16 v[176:179], v[72:75], v[148:151], v[176:179]\n\t" \
  "ds_read_b64_tr_b16 v[106:107], v55 offset:27136\n\t" \
  "v_mfma_f32_16x16x32_bf16 v[156:159], v[76:79], v[80:83], v[156:159]\n\t" \
  "ds_read_b64_tr_b16 v[72:73], v55 offset:30720\n\t" \
  "v_mfma_f32_16x16x32_bf16 v[156:159], v[140:143], v[116:119], v[156:159]\n\t" \
  "ds_read_b64_tr_b16 v[74:75], v55 offset:31232\n\t" \
  "v_mfma_f32_16x16x32_bf16 v[164:167], v[140:143], v[88:91], v[164:167]\n\t" \
  "ds_read_b64_tr_b16 v[80:81], v54 offset:34816\n\t" \
  "v_mfma_f32_16x16x32_bf16 v[164:167], v[76:79], v[124:127], v[164:167]\n\t" \
  "ds_read_b64_tr_b16 v[82:83], v54 offset:35328\n\t" \
  "v_mfma_f32_16x16x32_bf16 v[172:175], v[76:79], v[96:99], v[172:175]\n\t" \
  "ds_read_b64_tr_b16 v[116:117], v54 offset:38912\n\t" \
  "v_mfma_f32_16x16x32_bf16 v[172:175], v[140:143], v[132:135], v[172:175]\n\t" \
  "ds_read_b64_tr_b16 v[118:119], v54 offset:39424\n\t" \
  "v_mfma_f32_16x16x32_bf16 v[180:183], v[140:143], v[108:111], v[180:183]\n\t" \
  "ds_read_b128 v[88:91], v52 offset:0\n\t" \
  "v_mfma_f32_16x16x32_bf16 v[180:183], v[76:79], v[148:151], v[180:183]\n\t" \
  "ds_read_b128 v[124:127], v53 offset:0\n\t" \
  "s_waitcnt lgkmcnt(7)\n\t" \
  "v_mfma_f32_16x16x32_bf16 v[184:187], v[68:71], v[92:95], v[184:187]\n\t" \
  "ds_read_b64_tr_b16 v[96:97], v55 offset:34816\n\t" \
  "v_mfma_f32_16x16x32_bf16 v[184:187], v[64:67], v[84:87], v[184:187]\n\t" \
  "ds_read_b64_tr_b16 v[98:99], v55 offset:35328\n\t" \
  "v_mfma_f32_16x16x32_bf16 v[192:195], v[64:67], v[100:103], v[192:195]\n\t" \
  "ds_read_b64_tr_b16 v[132:133], v55 offset:38912\n\t" \
  "v_mfma_f32_16x16x32_bf16 v[192:195], v[68:71], v[112:115], v[192:195]\n\t" \
  "ds_read_b64_tr_b16 v[134:135], v55 offset:39424\n\t" \
  "v_mfma_f32_16x16x32_bf16 v[200:203], v[68:71], v[128:131], v[200:203]\n\t" \
  "ds_read_b64_tr_b16 v[108:109], v54 offset:18432\n\t" \
  "v_mfma_f32_16x16x32_bf16 v[200:203], v[64:67], v[120:123], v[200:203]\n\t" \
  "ds_read_b64_tr_b16 v[110:111], v54 offset:18944\n\t" \
  "v_mfma_f32_16x16x32_bf16 v[208:211], v[64:67], v[136:139], v[208:211]\n\t" \
  "ds_read_b64_tr_b16 v[140:141], v54 offset:22528\n\t" \
  "v_mfma_f32_16x16x32_bf16 v[208:211], v[68:71], v[144:147], v[208:211]\n\t" \
  "ds_read_b64_tr_b16 v[142:143], v54 offset:23040\n\t" \
  "v_mfma_f32_16x16x32_bf16 v[188:191], v[104:107], v[92:95], v[188:191]\n\t" \
  "ds_read_b64_tr_b16 v[76:77], v55 offset:18432\n\t" \
  "s_waitcnt lgkmcnt(7)\n\t" \
  "v_mfma_f32_16x16x32_bf16 v[188:191], v[72:75], v[84:87], v[188:191]\n\t" \
  "ds_read_b64_tr_b16 v[78:79], v55 offset:18944\n\t" \
  "v_mfma_f32_16x16x32_bf16 v[196:199], v[72:75], v[100:103], v[196:199]\n\t" \
  "ds_read_b64_tr_b16 v[148:149], v55 offset:22528\n\t" \
  "v_mfma_f32_16x16x32_bf16 v[196:199], v[104:107], v[112:115], v[196:199]\n\t" \
  "ds_read_b64_tr_b16 v[150:151], v55 offset:23040\n\t" \
  "v_mfma_f32_16x16x32_bf16 v[204:207], v[104:107], v[128:131], v[204:207]\n\t" \
  "v_mfma_f32_16x16x32_bf16 v[204:207], v[72:75], v[120:123], v[204:207]\n\t" \
  "v_mfma_f32_16x16x32_bf16 v[212:215], v[72:75], v[136:139], v[212:215]\n\t" \
  "v_mfma_f32_16x16x32_bf16 v[212:215], v[104:107], v[144:147], v[212:215]\n\t" \
  "v_mfma_f32_16x16x32_bf16 v[216:219], v[92:95], v[80:83], v[216:219]\n\t" \
  "v_mfma_f32_16x16x32_bf16 v[216:219], v[84:87], v[116:119], v[216:219]\n\t" \
  "v_mfma_f32_16x16x32_bf16 v[224:227], v[100:103], v[116:119], v[224:227]\n\t" \
  "v_mfma_f32_16x16x32_bf16 v[224:227], v[112:115], v[80:83], v[224:227]\n\t" \
  "v_mfma_f32_16x16x32_bf16 v[232:235], v[128:131], v[80:83], v[232:235]\n\t" \
  "v_mfma_f32_16x16x32_bf16 v[232:235], v[120:123], v[116:119], v[232:235]\n\t" \
  "v_mfma_f32_16x16x32_bf16 v[240:243], v[136:139], v[116:119], v[240:243]\n\t" \
  "v_mfma_f32_16x16x32_bf16 v[240:243], v[144:147], v[80:83], v[240:243]\n\t" \
  "v_mfma_f32_16x16x32_bf16 v[248:251], v[88:91], v[80:83], v[248:251]\n\t" \
  "v_mfma_f32_16x16x32_bf16 v[248:251], v[124:127], v[116:119], v[248:251]\n\t" \
  "v_mfma_f32_16x16x32_bf16 v[252:255], v[88:91], v[96:99], v[252:255]\n\t" \
  "s_waitcnt lgkmcnt(0)\n\t" \
  "v_mfma_f32_16x16x32_bf16 v[252:255], v[124:127], v[132:135], v[252:255]\n\t" \
  "v_mfma_f32_16x16x32_bf16 v[220:223], v[84:87], v[132:135], v[220:223]\n\t" \
  "v_mfma_f32_16x16x32_bf16 v[220:223], v[92:95], v[96:99], v[220:223]\n\t" \
  "s_barrier\n\t" \
  "v_mfma_f32_16x16x32_bf16 v[228:231], v[112:115], v[96:99], v[228:231]\n\t" \
  "ds_read_b64_tr_b16 v[64:65], v58 offset:26624\n\t" \
  "v_mfma_f32_16x16x32_bf16 v[228:231], v[100:103], v[132:135], v[228:231]\n\t" \
  "ds_read_b64_tr_b16 v[66:67], v58 offset:27136\n\t" \
  "v_mfma_f32_16x16x32_bf16 v[236:239], v[120:123], v[132:135], v[236:239]\n\t" \
  "ds_read_b128 v[68:71], v56 offset:10240\n\t" \
  "v_mfma_f32_16x16x32_bf16 v[236:239], v[128:131], v[96:99], v[236:239]\n\t" \
  "ds_read_b64_tr_b16 v[72:73], v58 offset:30720\n\t" \
  "v_mfma_f32_16x16x32_bf16 v[244:247], v[144:147], v[96:99], v[244:247]\n\t" \
  "ds_read_b64_tr_b16 v[74:75], v58 offset:31232\n\t" \
  "v_mfma_f32_16x16x32_bf16 v[244:247], v[136:139], v[132:135], v[244:247]\n\t" \
  "ds_read_b128 v[104:107], v57 offset:10240\n\t" \
  "v_mfma_f32_16x16x32_bf16 v[152:155], v[108:111], v[92:95], v[152:155]\n\t" \
  "ds_read_b128 v[80:83], v57 offset:12288\n\t" \
  "v_mfma_f32_16x16x32_bf16 v[152:155], v[140:143], v[84:87], v[152:155]\n\t" \
  "ds_read_b128 v[116:119], v56 offset:12288\n\t" \
  "v_mfma_f32_16x16x32_bf16 v[160:163], v[140:143], v[100:103], v[160:163]\n\t" \
  "ds_read_b128 v[88:91], v56 offset:14336\n\t" \
  "v_mfma_f32_16x16x32_bf16 v[160:163], v[108:111], v[112:115], v[160:163]\n\t" \
  "ds_read_b128 v[124:127], v57 offset:14336\n\t" \
  "v_mfma_f32_16x16x32_bf16 v[168:171], v[108:111], v[128:131], v[168:171]\n\t" \
  "ds_read_b128 v[96:99], v57 offset:16384\n\t" \
  "v_mfma_f32_16x16x32_bf16 v[168:171], v[140:143], v[120:123], v[168:171]\n\t" \
  "ds_read_b128 v[132:135], v56 offset:16384\n\t" \
  "v_mfma_f32_16x16x32_bf16 v[176:179], v[140:143], v[136:139], v[176:179]\n\t" \
  "ds_read_b64_tr_b16 v[140:141], v59 offset:26624\n\t" \
  "v_mfma_f32_16x16x32_bf16 v[176:179], v[108:111], v[144:147], v[176:179]\n\t" \
  "ds_read_b64_tr_b16 v[142:143], v59 offset:27136\n\t" \
  "v_mfma_f32_16x16x32_bf16 v[156:159], v[76:79], v[92:95], v[156:159]\n\t" \
  "ds_read_b64_tr_b16 v[108:109], v59 offset:30720\n\t" \
  "v_mfma_f32_16x16x32_bf16 v[156:159], v[148:151], v[84:87], v[156:159]\n\t" \
  "ds_read_b64_tr_b16 v[110:111], v59 offset:31232\n\t" \
  "v_mfma_f32_16x16x32_bf16 v[164:167], v[148:151], v[100:103], v[164:167]\n\t" \
  "ds_read_b64_tr_b16 v[92:93], v58 offset:34816\n\t" \
  "v_mfma_f32_16x16x32_bf16 v[164:167], v[76:79], v[112:115], v[164:167]\n\t" \
  "ds_read_b64_tr_b16 v[94:95], v58 offset:35328\n\t" \
  "v_mfma_f32_16x16x32_bf16 v[172:175], v[76:79], v[128:131], v[172:175]\n\t" \
  "ds_read_b64_tr_b16 v[84:85], v58 offset:38912\n\t" \
  "v_mfma_f32_16x16x32_bf16 v[172:175], v[148:151], v[120:123], v[172:175]\n\t" \
  "ds_read_b64_tr_b16 v[86:87], v58 offset:39424\n\t" \
  "v_mfma_f32_16x16x32_bf16 v[180:183], v[148:151], v[136:139], v[180:183]\n\t" \
  "ds_read_b128 v[100:103], v56 offset:0\n\t" \
  "v_mfma_f32_16x16x32_bf16 v[180:183], v[76:79], v[144:147], v[180:183]\n\t" \
  "ds_read_b128 v[112:115], v57 offset:0\n\t" \
  "s_waitcnt lgkmcnt(7)\n\t" \
  "v_mfma_f32_16x16x32_bf16 v[184:187], v[64:67], v[68:71], v[184:187]\n\t" \
  "ds_read_b64_tr_b16 v[128:129], v59 offset:34816\n\t" \
  "v_mfma_f32_16x16x32_bf16 v[184:187], v[72:75], v[104:107], v[184:187]\n\t" \
  "ds_read_b64_tr_b16 v[130:131], v59 offset:35328\n\t" \
  "v_mfma_f32_16x16x32_bf16 v[192:195], v[72:75], v[80:83], v[192:195]\n\t" \
  "ds_read_b64_tr_b16 v[120:121], v59 offset:38912\n\t" \
  "v_mfma_f32_16x16x32_bf16 v[192:195], v[64:67], v[116:119], v[192:195]\n\t" \
  "ds_read_b64_tr_b16 v[122:123], v59 offset:39424\n\t" \
  "v_mfma_f32_16x16x32_bf16 v[200:203], v[64:67], v[88:91], v[200:203]\n\t" \
  "ds_read_b64_tr_b16 v[136:137], v58 offset:18432\n\t" \
  "v_mfma_f32_16x16x32_bf16 v[200:203], v[72:75], v[124:127], v[200:203]\n\t" \
  "ds_read_b64_tr_b16 v[138:139], v58 offset:18944\n\t" \
  "v_mfma_f32_16x16x32_bf16 v[208:211], v[72:75], v[96:99], v[208:211]\n\t" \
  "ds_read_b64_tr_b16 v[148:149], v58 offset:22528\n\t" \
  "v_mfma_f32_16x16x32_bf16 v[208:211], v[64:67], v[132:135], v[208:211]\n\t" \
  "ds_read_b64_tr_b16 v[150:151], v58 offset:23040\n\t" \
  "v_mfma_f32_16x16x32_bf16 v[188:191], v[140:143], v[68:71], v[188:191]\n\t" \
  "ds_read_b64_tr_b16 v[76:77], v59 offset:18432\n\t" \
  "s_waitcnt lgkmcnt(7)\n\t" \
  "v_mfma_f32_16x16x32_bf16 v[188:191], v[108:111], v[104:107], v[188:191]\n\t" \
  "ds_read_b64_tr_b16 v[78:79], v59 offset:18944\n\t" \
  "v_mfma_f32_16x16x32_bf16 v[196:199], v[108:111], v[80:83], v[196:199]\n\t" \
  "ds_read_b64_tr_b16 v[144:145], v59 offset:22528\n\t" \
  "v_mfma_f32_16x16x32_bf16 v[196:199], v[140:143], v[116:119], v[196:199]\n\t" \
  "ds_read_b64_tr_b16 v[146:147], v59 offset:23040\n\t" \
  "v_mfma_f32_16x16x32_bf16 v[204:207], v[140:143], v[88:91], v[204:207]\n\t" \
  "v_mfma_f32_16x16x32_bf16 v[204:207], v[108:111], v[124:127], v[204:207]\n\t" \
  "v_mfma_f32_16x16x32_bf16 v[212:215], v[108:111], v[96:99], v[212:215]\n\t" \
  "v_mfma_f32_16x16x32_bf16 v[212:215], v[140:143], v[132:135], v[212:215]\n\t" \
  "v_mfma_f32_16x16x32_bf16 v[216:219], v[68:71], v[92:95], v[216:219]\n\t" \
  "v_mfma_f32_16x16x32_bf16 v[216:219], v[104:107], v[84:87], v[216:219]\n\t" \
  "v_mfma_f32_16x16x32_bf16 v[224:227], v[80:83], v[84:87], v[224:227]\n\t" \
  "v_mfma_f32_16x16x32_bf16 v[224:227], v[116:119], v[92:95], v[224:227]\n\t" \
  "v_mfma_f32_16x16x32_bf16 v[232:235], v[88:91], v[92:95], v[232:235]\n\t" \
  "v_mfma_f32_16x16x32_bf16 v[232:235], v[124:127], v[84:87], v[232:235]\n\t" \
  "v_mfma_f32_16x16x32_bf16 v[240:243], v[96:99], v[84:87], v[240:243]\n\t" \
  "v_mfma_f32_16x16x32_bf16 v[240:243], v[132:135], v[92:95], v[240:243]\n\t" \
  "v_mfma_f32_16x16x32_bf16 v[248:251], v[100:103], v[92:95], v[248:251]\n\t" \
  "v_mfma_f32_16x16x32_bf16 v[248:251], v[112:115], v[84:87], v[248:251]\n\t" \
  "v_mfma_f32_16x16x32_bf16 v[252:255], v[100:103], v[128:131], v[252:255]\n\t" \
  "s_waitcnt lgkmcnt(0)\n\t" \
  "v_mfma_f32_16x16x32_bf16 v[252:255], v[112:115], v[120:123], v[252:255]\n\t" \
  "v_mfma_f32_16x16x32_bf16 v[220:223], v[104:107], v[120:123], v[220:223]\n\t" \
  "v_mfma_f32_16x16x32_bf16 v[220:223], v[68:71], v[128:131], v[220:223]\n\t" \
  "s_barrier\n\t" \
  "v_mfma_f32_16x16x32_bf16 v[228:231], v[116:119], v[128:131], v[228:231]\n\t" \
  "ds_read_b64_tr_b16 v[72:73], v62 offset:26624\n\t" \
  "v_mfma_f32_16x16x32_bf16 v[228:231], v[80:83], v[120:123], v[228:231]\n\t" \
  "ds_read_b64_tr_b16 v[74:75], v62 offset:27136\n\t" \
  "v_mfma_f32_16x16x32_bf16 v[236:239], v[124:127], v[120:123], v[236:239]\n\t" \
  "ds_read_b128 v[64:67], v60 offset:10240\n\t" \
  "v_mfma_f32_16x16x32_bf16 v[236:239], v[88:91], v[128:131], v[236:239]\n\t" \
  "ds_read_b64_tr_b16 v[108:109], v62 offset:30720\n\t" \
  "v_mfma_f32_16x16x32_bf16 v[244:247], v[132:135], v[128:131], v[244:247]\n\t" \
  "ds_read_b64_tr_b16 v[110:111], v62 offset:31232\n\t" \
  "v_mfma_f32_16x16x32_bf16 v[244:247], v[96:99], v[120:123], v[244:247]\n\t" \
  "ds_read_b128 v[140:143], v61 offset:10240\n\t" \
  "v_mfma_f32_16x16x32_bf16 v[152:155], v[136:139], v[68:71], v[152:155]\n\t" \
  "ds_read_b128 v[92:95], v61 offset:12288\n\t" \
  "v_mfma_f32_16x16x32_bf16 v[152:155], v[148:151], v[104:107], v[152:155]\n\t" \
  "ds_read_b128 v[84:87], v60 offset:12288\n\t" \
  "v_mfma_f32_16x16x32_bf16 v[160:163], v[148:151], v[80:83], v[160:163]\n\t" \
  "ds_read_b128 v[100:103], v60 offset:14336\n\t" \
  "v_mfma_f32_16x16x32_bf16 v[160:163], v[136:139], v[116:119], v[160:163]\n\t" \
  "ds_read_b128 v[112:115], v61 offset:14336\n\t" \
  "v_mfma_f32_16x16x32_bf16 v[168:171], v[136:139], v[88:91], v[168:171]\n\t" \
  "ds_read_b128 v[128:131], v61 offset:16384\n\t" \
  "v_mfma_f32_16x16x32_bf16 v[168:171], v[148:151], v[124:127], v[168:171]\n\t" \
  "ds_read_b128 v[120:123], v60 offset:16384\n\t" \
  "v_mfma_f32_16x16x32_bf16 v[176:179], v[148:151], v[96:99], v[176:179]\n\t" \
  "ds_read_b64_tr_b16 v[148:149], v63 offset:26624\n\t" \
  "v_mfma_f32_16x16x32_bf16 v[176:179], v[136:139], v[132:135], v[176:179]\n\t" \
  "ds_read_b64_tr_b16 v[150:151], v63 offset:27136\n\t" \
  "v_mfma_f32_16x16x32_bf16 v[156:159], v[76:79], v[68:71], v[156:159]\n\t" \
  "ds_read_b64_tr_b16 v[136:137], v63 offset:30720\n\t" \
  "v_mfma_f32_16x16x32_bf16 v[156:159], v[144:147], v[104:107], v[156:159]\n\t" \
  "ds_read_b64_tr_b16 v[138:139], v63 offset:31232\n\t" \
  "v_mfma_f32_16x16x32_bf16 v[164:167], v[144:147], v[80:83], v[164:167]\n\t" \
  "ds_read_b64_tr_b16 v[68:69], v62 offset:34816\n\t" \
  "v_mfma_f32_16x16x32_bf16 v[164:167], v[76:79], v[116:119], v[164:167]\n\t" \
  "ds_read_b64_tr_b16 v[70:71], v62 offset:35328\n\t" \
  "v_mfma_f32_16x16x32_bf16 v[172:175], v[76:79], v[88:91], v[172:175]\n\t" \
  "ds_read_b64_tr_b16 v[104:105], v62 offset:38912\n\t" \
  "v_mfma_f32_16x16x32_bf16 v[172:175], v[144:147], v[124:127], v[172:175]\n\t" \
  "ds_read_b64_tr_b16 v[106:107], v62 offset:39424\n\t" \
  "v_mfma_f32_16x16x32_bf16 v[180:183], v[144:147], v[96:99], v[180:183]\n\t" \
  "ds_read_b128 v[80:83], v60 offset:0\n\t" \
  "v_mfma_f32_16x16x32_bf16 v[180:183], v[76:79], v[132:135], v[180:183]\n\t" \
  "ds_read_b128 v[116:119], v61 offset:0\n\t" \
  "s_waitcnt lgkmcnt(7)\n\t" \
  "v_mfma_f32_16x16x32_bf16 v[184:187], v[72:75], v[64:67], v[184:187]\n\t" \
  "ds_read_b64_tr_b16 v[88:89], v63 offset:34816\n\t" \
  "v_mfma_f32_16x16x32_bf16 v[184:187], v[108:111], v[140:143], v[184:187]\n\t" \
  "ds_read_b64_tr_b16 v[90:91], v63 offset:35328\n\t" \
  "v_mfma_f32_16x16x32_bf16 v[192:195], v[108:111], v[92:95], v[192:195]\n\t" \
  "ds_read_b64_tr_b16 v[124:125], v63 offset:38912\n\t" \
  "v_mfma_f32_16x16x32_bf16 v[192:195], v[72:75], v[84:87], v[192:195]\n\t" \
  "ds_read_b64_tr_b16 v[126:127], v63 offset:39424\n\t" \
  "v_mfma_f32_16x16x32_bf16 v[200:203], v[72:75], v[100:103], v[200:203]\n\t" \
  "ds_read_b64_tr_b16 v[96:97], v62 offset:18432\n\t" \
  "v_mfma_f32_16x16x32_bf16 v[200:203], v[108:111], v[112:115], v[200:203]\n\t" \
  "ds_read_b64_tr_b16 v[98:99], v62 offset:18944\n\t" \
  "v_mfma_f32_16x16x32_bf16 v[208:211], v[108:111], v[128:131], v[208:211]\n\t" \
  "ds_read_b64_tr_b16 v[144:145], v62 offset:22528\n\t" \
  "v_mfma_f32_16x16x32_bf16 v[208:211], v[72:75], v[120:123], v[208:211]\n\t" \
  "ds_read_b64_tr_b16 v[146:147], v62 offset:23040\n\t" \
  "v_mfma_f32_16x16x32_bf16 v[188:191], v[148:151], v[64:67], v[188:191]\n\t" \
  "ds_read_b64_tr_b16 v[76:77], v63 offset:18432\n\t" \
  "s_waitcnt lgkmcnt(7)\n\t" \
  "v_mfma_f32_16x16x32_bf16 v[188:191], v[136:139], v[140:143], v[188:191]\n\t" \
  "ds_read_b64_tr_b16 v[78:79], v63 offset:18944\n\t" \
  "v_mfma_f32_16x16x32_bf16 v[196:199], v[136:139], v[92:95], v[196:199]\n\t" \
  "ds_read_b64_tr_b16 v[132:133], v63 offset:22528\n\t" \
  "v_mfma_f32_16x16x32_bf16 v[196:199], v[148:151], v[84:87], v[196:199]\n\t" \
  "ds_read_b64_tr_b16 v[134:135], v63 offset:23040\n\t" \
  "v_mfma_f32_16x16x32_bf16 v[204:207], v[148:151], v[100:103], v[204:207]\n\t" \
  "v_mfma_f32_16x16x32_bf16 v[204:207], v[136:139], v[112:115], v[204:207]\n\t" \
  "v_mfma_f32_16x16x32_bf16 v[212:215], v[136:139], v[128:131], v[212:215]\n\t" \
  "v_mfma_f32_16x16x32_bf16 v[212:215], v[148:151], v[120:123], v[212:215]\n\t" \
  "v_mfma_f32_16x16x32_bf16 v[216:219], v[64:67], v[68:71], v[216:219]\n\t" \
  "v_mfma_f32_16x16x32_bf16 v[216:219], v[140:143], v[104:107], v[216:219]\n\t" \
  "v_mfma_f32_16x16x32_bf16 v[224:227], v[92:95], v[104:107], v[224:227]\n\t" \
  "v_mfma_f32_16x16x32_bf16 v[224:227], v[84:87], v[68:71], v[224:227]\n\t" \
  "v_mfma_f32_16x16x32_bf16 v[232:235], v[100:103], v[68:71], v[232:235]\n\t" \
  "v_mfma_f32_16x16x32_bf16 v[232:235], v[112:115], v[104:107], v[232:235]\n\t" \
  "v_mfma_f32_16x16x32_bf16 v[240:243], v[128:131], v[104:107], v[240:243]\n\t" \
  "v_mfma_f32_16x16x32_bf16 v[240:243], v[120:123], v[68:71], v[240:243]\n\t" \
  "v_mfma_f32_16x16x32_bf16 v[248:251], v[80:83], v[68:71], v[248:251]\n\t" \
  "v_mfma_f32_16x16x32_bf16 v[248:251], v[116:119], v[104:107], v[248:251]\n\t" \
  "v_mfma_f32_16x16x32_bf16 v[252:255], v[80:83], v[88:91], v[252:255]\n\t" \
  "s_waitcnt lgkmcnt(0)\n\t" \
  "v_mfma_f32_16x16x32_bf16 v[252:255], v[116:119], v[124:127], v[252:255]\n\t" \
  "v_mfma_f32_16x16x32_bf16 v[220:223], v[140:143], v[124:127], v[220:223]\n\t" \
  "v_mfma_f32_16x16x32_bf16 v[220:223], v[64:67], v[88:91], v[220:223]\n\t" \
  "s_barrier\n\t" \
  "v_mfma_f32_16x16x32_bf16 v[228:231], v[84:87], v[88:91], v[228:231]\n\t" \
  "ds_read_b64_tr_b16 v[108:109], v54 offset:26624\n\t" \
  "v_mfma_f32_16x16x32_bf16 v[228:231], v[92:95], v[124:127], v[228:231]\n\t" \
  "ds_read_b64_tr_b16 v[110:111], v54 offset:27136\n\t" \
  "v_mfma_f32_16x16x32_bf16 v[236:239], v[112:115], v[124:127], v[236:239]\n\t" \
  "ds_read_b128 v[72:75], v52 offset:10240\n\t" \
  "v_mfma_f32_16x16x32_bf16 v[236:239], v[100:103], v[88:91], v[236:239]\n\t" \
  "ds_read_b64_tr_b16 v[136:137], v54 offset:30720\n\t" \
  "v_mfma_f32_16x16x32_bf16 v[244:247], v[120:123], v[88:91], v[244:247]\n\t" \
  "ds_read_b64_tr_b16 v[138:139], v54 offset:31232\n\t" \
  "v_mfma_f32_16x16x32_bf16 v[244:247], v[128:131], v[124:127], v[244:247]\n\t" \
  "ds_read_b128 v[148:151], v53 offset:10240\n\t" \
  "v_mfma_f32_16x16x32_bf16 v[152:155], v[96:99], v[64:67], v[152:155]\n\t" \
  "ds_read_b128 v[68:71], v53 offset:12288\n\t" \
  "v_mfma_f32_16x16x32_bf16 v[152:155], v[144:147], v[140:143], v[152:155]\n\t" \
  "ds_read_b128 v[104:107], v52 offset:12288\n\t" \
  "v_mfma_f32_16x16x32_bf16 v[160:163], v[144:147], v[92:95], v[160:163]\n\t" \
  "ds_read_b128 v[80:83], v52 offset:14336\n\t" \
  "v_mfma_f32_16x16x32_bf16 v[160:163], v[96:99], v[84:87], v[160:163]\n\t" \
  "ds_read_b128 v[116:119], v53 offset:14336\n\t" \
  "v_mfma_f32_16x16x32_bf16 v[168:171], v[96:99], v[100:103], v[168:171]\n\t" \
  "ds_read_b128 v[88:91], v53 offset:16384\n\t" \
  "v_mfma_f32_16x16x32_bf16 v[168:171], v[144:147], v[112:115], v[168:171]\n\t" \
  "ds_read_b128 v[124:127], v52 offset:16384\n\t" \
  "v_mfma_f32_16x16x32_bf16 v[176:179], v[144:147], v[128:131], v[176:179]\n\t" \
  "ds_read_b64_tr_b16 v[144:145], v55 offset:26624\n\t" \
  "v_mfma_f32_16x16x32_bf16 v[176:179], v[96:99], v[120:123], v[176:179]\n\t" \
  "ds_read_b64_tr_b16 v[146:147], v55 offset:27136\n\t" \
  "v_mfma_f32_16x16x32_bf16 v[156:159], v[76:79], v[64:67], v[156:159]\n\t" \
  "ds_read_b64_tr_b16 v[96:97], v55 offset:30720\n\t" \
  "v_mfma_f32_16x16x32_bf16 v[156:159], v[132:135], v[140:143], v[156:159]\n\t" \
  "ds_read_b64_tr_b16 v[98:99], v55 offset:31232\n\t" \
  "v_mfma_f32_16x16x32_bf16 v[164:167], v[132:135], v[92:95], v[164:167]\n\t" \
  "ds_read_b64_tr_b16 v[64:65], v54 offset:34816\n\t" \
  "v_mfma_f32_16x16x32_bf16 v[164:167], v[76:79], v[84:87], v[164:167]\n\t" \
  "ds_read_b64_tr_b16 v[66:67], v54 offset:35328\n\t" \
  "v_mfma_f32_16x16x32_bf16 v[172:175], v[76:79], v[100:103], v[172:175]\n\t" \
  "ds_read_b64_tr_b16 v[140:141], v54 offset:38912\n\t" \
  "v_mfma_f32_16x16x32_bf16 v[172:175], v[132:135], v[112:115], v[172:175]\n\t" \
  "ds_read_b64_tr_b16 v[142:143], v54 offset:39424\n\t" \
  "v_mfma_f32_16x16x32_bf16 v[180:183], v[132:135], v[128:131], v[180:183]\n\t" \
  "ds_read_b128 v[92:95], v52 offset:0\n\t" \
  "v_mfma_f32_16x16x32_bf16 v[180:183], v[76:79], v[120:123], v[180:183]\n\t" \
  "ds_read_b128 v[84:87], v53 offset:0\n\t" \
  "s_waitcnt lgkmcnt(7)\n\t" \
  "v_mfma_f32_16x16x32_bf16 v[184:187], v[108:111], v[72:75], v[184:187]\n\t" \
  "ds_read_b64_tr_b16 v[100:101], v55 offset:34816\n\t" \
  "v_mfma_f32_16x16x32_bf16 v[184:187], v[136:139], v[148:151], v[184:187]\n\t" \
  "ds_read_b64_tr_b16 v[102:103], v55 offset:35328\n\t" \
  "v_mfma_f32_16x16x32_bf16 v[192:195], v[136:139], v[68:71], v[192:195]\n\t" \
  "ds_read_b64_tr_b16 v[112:113], v55 offset:38912\n\t" \
  "v_mfma_f32_16x16x32_bf16 v[192:195], v[108:111], v[104:107], v[192:195]\n\t" \
  "ds_read_b64_tr_b16 v[114:115], v55 offset:39424\n\t" \
  "v_mfma_f32_16x16x32_bf16 v[200:203], v[108:111], v[80:83], v[200:203]\n\t" \
  "ds_read_b64_tr_b16 v[128:129], v54 offset:18432\n\t" \
  "v_mfma_f32_16x16x32_bf16 v[200:203], v[136:139], v[116:119], v[200:203]\n\t" \
  "ds_read_b64_tr_b16 v[130:131], v54 offset:18944\n\t" \
  "v_mfma_f32_16x16x32_bf16 v[208:211], v[136:139], v[88:91], v[208:211]\n\t" \
  "ds_read_b64_tr_b16 v[132:133], v54 offset:22528\n\t" \
  "v_mfma_f32_16x16x32_bf16 v[208:211], v[108:111], v[124:127], v[208:211]\n\t" \
  "ds_read_b64_tr_b16 v[134:135], v54 offset:23040\n\t" \
  "v_mfma_f32_16x16x32_bf16 v[188:191], v[144:147], v[72:75], v[188:191]\n\t" \
  "ds_read_b64_tr_b16 v[76:77], v55 offset:18432\n\t" \
  "s_waitcnt lgkmcnt(7)\n\t" \
  "v_mfma_f32_16x16x32_bf16 v[188:191], v[96:99], v[148:151], v[188:191]\n\t" \
  "ds_read_b64_tr_b16 v[78:79], v55 offset:18944\n\t" \
  "v_mfma_f32_16x16x32_bf16 v[196:199], v[96:99], v[68:71], v[196:199]\n\t" \
  "ds_read_b64_tr_b16 v[120:121], v55 offset:22528\n\t" \
  "v_mfma_f32_16x16x32_bf16 v[196:199], v[144:147], v[104:107], v[196:199]\n\t" \
  "ds_read_b64_tr_b16 v[122:123], v55 offset:23040\n\t" \
  "v_mfma_f32_16x16x32_bf16 v[204:207], v[144:147], v[80:83], v[204:207]\n\t" \
  "v_mfma_f32_16x16x32_bf16 v[204:207], v[96:99], v[116:119], v[204:207]\n\t" \
  "v_mfma_f32_16x16x32_bf16 v[212:215], v[96:99], v[88:91], v[212:215]\n\t" \
  "v_mfma_f32_16x16x32_bf16 v[212:215], v[144:147], v[124:127], v[212:215]\n\t" \
  "v_mfma_f32_16x16x32_bf16 v[216:219], v[72:75], v[64:67], v[216:219]\n\t" \
  "v_mfma_f32_16x16x32_bf16 v[216:219], v[148:151], v[140:143], v[216:219]\n\t" \
  "v_mfma_f32_16x16x32_bf16 v[224:227], v[68:71], v[140:143], v[224:227]\n\t" \
  "v_mfma_f32_16x16x32_bf16 v[224:227], v[104:107], v[64:67], v[224:227]\n\t" \
  "v_mfma_f32_16x16x32_bf16 v[232:235], v[80:83], v[64:67], v[232:235]\n\t" \
  "v_mfma_f32_16x16x32_bf16 v[232:235], v[116:119], v[140:143], v[232:235]\n\t" \
  "v_mfma_f32_16x16x32_bf16 v[240:243], v[88:91], v[140:143], v[240:243]\n\t" \
  "v_mfma_f32_16x16x32_bf16 v[240:243], v[124:127], v[64:67], v[240:243]\n\t" \
  "v_mfma_f32_16x16x32_bf16 v[248:251], v[92:95], v[64:67], v[248:251]\n\t" \
  "v_mfma_f32_16x16x32_bf16 v[248:251], v[84:87], v[140:143], v[248:251]\n\t" \
  "v_mfma_f32_16x16x32_bf16 v[252:255], v[92:95], v[100:103], v[252:255]\n\t" \
  "s_waitcnt lgkmcnt(0)\n\t" \
  "v_mfma_f32_16x16x32_bf16 v[252:255], v[84:87], v[112:115], v[252:255]\n\t" \
  "v_mfma_f32_16x16x32_bf16 v[220:223], v[148:151], v[112:115], v[220:223]\n\t" \
  "v_mfma_f32_16x16x32_bf16 v[220:223], v[72:75], v[100:103], v[220:223]\n\t" \
  "s_barrier\n\t" \
  "v_mfma_f32_16x16x32_bf16 v[228:231], v[104:107], v[100:103], v[228:231]\n\t" \
  "ds_read_b64_tr_b16 v[96:97], v58 offset:26624\n\t" \
  "v_mfma_f32_16x16x32_bf16 v[228:231], v[68:71], v[112:115], v[228:231]\n\t" \
  "ds_read_b64_tr_b16 v[98:99], v58 offset:27136\n\t" \
  "v_mfma_f32_16x16x32_bf16 v[236:239], v[116:119], v[112:115], v[236:239]\n\t" \
  "ds_read_b128 v[136:139], v56 offset:10240\n\t" \
  "v_mfma_f32_16x16x32_bf16 v[236:239], v[80:83], v[100:103], v[236:239]\n\t" \
  "ds_read_b64_tr_b16 v[64:65], v58 offset:30720\n\t" \
  "v_mfma_f32_16x16x32_bf16 v[244:247], v[124:127], v[100:103], v[244:247]\n\t" \
  "ds_read_b64_tr_b16 v[66:67], v58 offset:31232\n\t" \
  "v_mfma_f32_16x16x32_bf16 v[244:247], v[88:91], v[112:115], v[244:247]\n\t" \
  "ds_read_b128 v[108:111], v57 offset:10240\n\t" \
  "v_mfma_f32_16x16x32_bf16 v[152:155], v[128:131], v[72:75], v[152:155]\n\t" \
  "ds_read_b128 v[144:147], v57 offset:12288\n\t" \
  "v_mfma_f32_16x16x32_bf16 v[152:155], v[132:135], v[148:151], v[152:155]\n\t" \
  "ds_read_b128 v[140:143], v56 offset:12288\n\t" \
  "v_mfma_f32_16x16x32_bf16 v[160:163], v[132:135], v[68:71], v[160:163]\n\t" \
  "ds_read_b128 v[112:115], v56 offset:14336\n\t" \
  "v_mfma_f32_16x16x32_bf16 v[160:163], v[128:131], v[104:107], v[160:163]\n\t" \
  "ds_read_b64_tr_b16 v[92:93], v59 offset:26624\n\t" \
  "v_mfma_f32_16x16x32_bf16 v[168:171], v[128:131], v[80:83], v[168:171]\n\t" \
  "ds_read_b64_tr_b16 v[94:95], v59 offset:27136\n\t" \
  "v_mfma_f32_16x16x32_bf16 v[168:171], v[132:135], v[116:119], v[168:171]\n\t" \
  "ds_read_b64_tr_b16 v[84:85], v59 offset:30720\n\t" \
  "v_mfma_f32_16x16x32_bf16 v[176:179], v[132:135], v[88:91], v[176:179]\n\t" \
  "ds_read_b128 v[132:135], v57 offset:14336\n\t" \
  "v_mfma_f32_16x16x32_bf16 v[176:179], v[128:131], v[124:127], v[176:179]\n\t" \
  "ds_read_b128 v[128:131], v57 offset:16384\n\t" \
  "v_mfma_f32_16x16x32_bf16 v[156:159], v[76:79], v[72:75], v[156:159]\n\t" \
  "ds_read_b64_tr_b16 v[86:87], v59 offset:31232\n\t" \
  "v_mfma_f32_16x16x32_bf16 v[156:159], v[120:123], v[148:151], v[156:159]\n\t" \
  "ds_read_b128 v[148:151], v56 offset:16384\n\t" \
  "v_mfma_f32_16x16x32_bf16 v[164:167], v[120:123], v[68:71], v[164:167]\n\t" \
  "ds_read_b64_tr_b16 v[100:101], v58 offset:34816\n\t" \
  "v_mfma_f32_16x16x32_bf16 v[164:167], v[76:79], v[104:107], v[164:167]\n\t" \
  "ds_read_b64_tr_b16 v[102:103], v58 offset:35328\n\t" \
  "v_mfma_f32_16x16x32_bf16 v[172:175], v[76:79], v[80:83], v[172:175]\n\t" \
  "ds_read_b64_tr_b16 v[72:73], v58 offset:38912\n\t" \
  "v_mfma_f32_16x16x32_bf16 v[172:175], v[120:123], v[116:119], v[172:175]\n\t" \
  "ds_read_b64_tr_b16 v[74:75], v58 offset:39424\n\t" \
  "v_mfma_f32_16x16x32_bf16 v[180:183], v[120:123], v[88:91], v[180:183]\n\t" \
  "ds_read_b128 v[68:71], v56 offset:0\n\t" \
  "v_mfma_f32_16x16x32_bf16 v[180:183], v[76:79], v[124:127], v[180:183]\n\t" \
  "ds_read_b128 v[104:107], v57 offset:0\n\t" \
  "s_waitcnt lgkmcnt(7)\n\t" \
  "v_mfma_f32_16x16x32_bf16 v[184:187], v[96:99], v[136:139], v[184:187]\n\t" \
  "ds_read_b64_tr_b16 v[80:81], v59 offset:34816\n\t" \
  "v_mfma_f32_16x16x32_bf16 v[184:187], v[64:67], v[108:111], v[184:187]\n\t" \
  "ds_read_b64_tr_b16 v[82:83], v59 offset:35328\n\t" \
  "v_mfma_f32_16x16x32_bf16 v[192:195], v[64:67], v[144:147], v[192:195]\n\t" \
  "ds_read_b64_tr_b16 v[88:89], v59 offset:38912\n\t" \
  "v_mfma_f32_16x16x32_bf16 v[192:195], v[96:99], v[140:143], v[192:195]\n\t" \
  "ds_read_b64_tr_b16 v[90:91], v59 offset:39424\n\t" \
  "v_mfma_f32_16x16x32_bf16 v[200:203], v[96:99], v[112:115], v[200:203]\n\t" \
  "ds_read_b64_tr_b16 v[116:117], v58 offset:18432\n\t" \
  "v_mfma_f32_16x16x32_bf16 v[200:203], v[64:67], v[132:135], v[200:203]\n\t" \
  "ds_read_b64_tr_b16 v[118:119], v58 offset:18944\n\t" \
  "v_mfma_f32_16x16x32_bf16 v[208:211], v[64:67], v[128:131], v[208:211]\n\t" \
  "ds_read_b64_tr_b16 v[120:121], v58 offset:22528\n\t" \
  "s_waitcnt lgkmcnt(7)\n\t" \
  "v_mfma_f32_16x16x32_bf16 v[208:211], v[96:99], v[148:151], v[208:211]\n\t" \
  "ds_read_b64_tr_b16 v[122:123], v58 offset:23040\n\t" \
  "v_mfma_f32_16x16x32_bf16 v[188:191], v[92:95], v[136:139], v[188:191]\n\t" \
  "ds_read_b64_tr_b16 v[124:125], v59 offset:18432\n\t" \
  "v_mfma_f32_16x16x32_bf16 v[188:191], v[84:87], v[108:111], v[188:191]\n\t" \
  "ds_read_b64_tr_b16 v[126:127], v59 offset:18944\n\t" \
  "v_mfma_f32_16x16x32_bf16 v[196:199], v[84:87], v[144:147], v[196:199]\n\t" \
  "ds_read_b64_tr_b16 v[76:77], v59 offset:22528\n\t" \
  "v_mfma_f32_16x16x32_bf16 v[196:199], v[92:95], v[140:143], v[196:199]\n\t" \
  "ds_read_b64_tr_b16 v[78:79], v59 offset:23040\n\t" \
  "v_mfma_f32_16x16x32_bf16 v[204:207], v[92:95], v[112:115], v[204:207]\n\t" \
  "v_mfma_f32_16x16x32_bf16 v[204:207], v[84:87], v[132:135], v[204:207]\n\t" \
  "v_mfma_f32_16x16x32_bf16 v[212:215], v[84:87], v[128:131], v[212:215]\n\t" \
  "v_mfma_f32_16x16x32_bf16 v[212:215], v[92:95], v[148:151], v[212:215]\n\t" \
  "v_mfma_f32_16x16x32_bf16 v[216:219], v[136:139], v[100:103], v[216:219]\n\t" \
  "v_mfma_f32_16x16x32_bf16 v[216:219], v[108:111], v[72:75], v[216:219]\n\t" \
  "v_mfma_f32_16x16x32_bf16 v[224:227], v[144:147], v[72:75], v[224:227]\n\t" \
  "v_and_b32 v14, -16, v32\n\t" \
  "v_mfma_f32_16x16x32_bf16 v[224:227], v[140:143], v[100:103], v[224:227]\n\t" \
  "v_mfma_f32_16x16x32_bf16 v[232:235], v[112:115], v[100:103], v[232:235]\n\t" \
  "ds_read_b128 v[10:13], v14 offset:128\n\t" \
  "v_mfma_f32_16x16x32_bf16 v[232:235], v[132:135], v[72:75], v[232:235]\n\t" \
  "v_mfma_f32_16x16x32_bf16 v[240:243], v[128:131], v[72:75], v[240:243]\n\t" \
  "v_mfma_f32_16x16x32_bf16 v[240:243], v[148:151], v[100:103], v[240:243]\n\t" \
  "v_mfma_f32_16x16x32_bf16 v[248:251], v[68:71], v[100:103], v[248:251]\n\t" \
  "v_mfma_f32_16x16x32_bf16 v[248:251], v[104:107], v[72:75], v[248:251]\n\t" \
  "s_waitcnt lgkmcnt(1)\n\t" \
  "v_mfma_f32_16x16x32_bf16 v[252:255], v[68:71], v[80:83], v[252:255]\n\t" \
  "s_waitcnt lgkmcnt(0)\n\t" \
  "v_and_b32 v10, v10, v11\n\t" \
  "v_and_b32 v12, v12, v13\n\t" \
  "v_mfma_f32_16x16x32_bf16 v[252:255], v[104:107], v[88:91], v[252:255]\n\t" \
  "v_and_b32 v10, v10, v12\n\t" \
  "v_mfma_f32_16x16x32_bf16 v[220:223], v[108:111], v[88:91], v[220:223]\n\t" \
  "v_readfirstlane_b32 vcc_lo, v10\n\t" \
  "v_mfma_f32_16x16x32_bf16 v[220:223], v[136:139], v[80:83], v[220:223]\n\t" \
  "ds_write_b32 v32, v0 offset:144\n\t" \
  "s_cmp_eq_u32 vcc_lo, 0\n\t" \
  "s_cbranch_scc1 15f\n\t" \
  "14:\n\t" \
  "v_mfma_f32_16x16x32_bf16 v[228:231], v[140:143], v[80:83], v[228:231]\n\t" \
  "ds_read_b64_tr_b16 v[64:65], v62 offset:26624\n\t" \
  "v_mfma_f32_16x16x32_bf16 v[228:231], v[144:147], v[88:91], v[228:231]\n\t" \
  "ds_read_b64_tr_b16 v[66:67], v62 offset:27136\n\t" \
  "v_mfma_f32_16x16x32_bf16 v[236:239], v[132:135], v[88:91], v[236:239]\n\t" \
  "ds_read_b128 v[96:99], v60 offset:10240\n\t" \
  "v_mfma_f32_16x16x32_bf16 v[236:239], v[112:115], v[80:83], v[236:239]\n\t" \
  "ds_read_b128 v[84:87], v60 offset:12288\n\t" \
  "v_mfma_f32_16x16x32_bf16 v[244:247], v[148:151], v[80:83], v[244:247]\n\t" \
  "ds_read_b128 v[92:95], v60 offset:14336\n\t" \
  "v_mfma_f32_16x16x32_bf16 v[244:247], v[128:131], v[88:91], v[244:247]\n\t" \
  "ds_read_b128 v[100:103], v60 offset:16384\n\t" \
  "v_mfma_f32_16x16x32_bf16 v[152:155], v[116:119], v[136:139], v[152:155]\n\t" \
  "ds_read_b64_tr_b16 v[72:73], v63 offset:26624\n\t" \
  "v_mfma_f32_16x16x32_bf16 v[152:155], v[120:123], v[108:111], v[152:155]\n\t" \
  "ds_read_b64_tr_b16 v[74:75], v63 offset:27136\n\t" \
  "v_mfma_f32_16x16x32_bf16 v[160:163], v[120:123], v[144:147], v[160:163]\n\t" \
  "ds_read_b64_tr_b16 v[68:69], v62 offset:34816\n\t" \
  "v_mfma_f32_16x16x32_bf16 v[160:163], v[116:119], v[140:143], v[160:163]\n\t" \
  "ds_read_b64_tr_b16 v[70:71], v62 offset:35328\n\t" \
  "v_mfma_f32_16x16x32_bf16 v[168:171], v[116:119], v[112:115], v[168:171]\n\t" \
  "ds_read_b128 v[104:107], v60 offset:0\n\t" \
  "v_mfma_f32_16x16x32_bf16 v[168:171], v[120:123], v[132:135], v[168:171]\n\t" \
  "ds_read_b64_tr_b16 v[80:81], v63 offset:34816\n\t" \
  "v_mfma_f32_16x16x32_bf16 v[176:179], v[120:123], v[128:131], v[176:179]\n\t" \
  "ds_read_b64_tr_b16 v[82:83], v63 offset:35328\n\t" \
  "v_mfma_f32_16x16x32_bf16 v[176:179], v[116:119], v[148:151], v[176:179]\n\t" \
  "ds_read_b64_tr_b16 v[88:89], v62 offset:18432\n\t" \
  "v_mfma_f32_16x16x32_bf16 v[156:159], v[124:127], v[136:139], v[156:159]\n\t" \
  "ds_read_b64_tr_b16 v[90:91], v62 offset:18944\n\t" \
  "v_mfma_f32_16x16x32_bf16 v[156:159], v[76:79], v[108:111], v[156:159]\n\t" \
  "ds_read_b64_tr_b16 v[120:121], v62 offset:30720\n\t" \
  "v_mfma_f32_16x16x32_bf16 v[164:167], v[76:79], v[144:147], v[164:167]\n\t" \
  "ds_read_b64_tr_b16 v[122:123], v62 offset:31232\n\t" \
  "v_mfma_f32_16x16x32_bf16 v[164:167], v[124:127], v[140:143], v[164:167]\n\t" \
  "ds_read_b128 v[116:119], v61 offset:10240\n\t" \
  "v_mfma_f32_16x16x32_bf16 v[172:175], v[124:127], v[112:115], v[172:175]\n\t" \
  "ds_read_b128 v[136:139], v61 offset:12288\n\t" \
  "v_mfma_f32_16x16x32_bf16 v[172:175], v[76:79], v[132:135], v[172:175]\n\t" \
  "ds_read_b128 v[108:111], v61 offset:14336\n\t" \
  "v_mfma_f32_16x16x32_bf16 v[180:183], v[76:79], v[128:131], v[180:183]\n\t" \
  "ds_read_b64_tr_b16 v[76:77], v63 offset:18432\n\t" \
  "v_mfma_f32_16x16x32_bf16 v[180:183], v[124:127], v[148:151], v[180:183]\n\t" \
  "ds_read_b64_tr_b16 v[78:79], v63 offset:18944\n\t" \
  "s_waitcnt lgkmcnt(7)\n\t" \
  "v_mfma_f32_16x16x32_bf16 v[184:187], v[64:67], v[96:99], v[184:187]\n\t" \
  "ds_read_b128 v[144:147], v61 offset:16384\n\t" \
  "v_mfma_f32_16x16x32_bf16 v[192:195], v[64:67], v[84:87], v[192:195]\n\t" \
  "ds_read_b64_tr_b16 v[140:141], v63 offset:30720\n\t" \
  "v_mfma_f32_16x16x32_bf16 v[200:203], v[64:67], v[92:95], v[200:203]\n\t" \
  "ds_read_b64_tr_b16 v[142:143], v63 offset:31232\n\t" \
  "v_mfma_f32_16x16x32_bf16 v[208:211], v[64:67], v[100:103], v[208:211]\n\t" \
  "502:\n\t" \
  "s_cbranch_execz 506f\n\t" \
  "ds_read_b64_tr_b16 v[112:113], v62 offset:22528\n\t" \
  "v_mfma_f32_16x16x32_bf16 v[188:191], v[72:75], v[96:99], v[188:191]\n\t" \
  "ds_read_b64_tr_b16 v[114:115], v62 offset:23040\n\t" \
  "v_mfma_f32_16x16x32_bf16 v[196:199], v[72:75], v[84:87], v[196:199]\n\t" \
  "ds_read_b64_tr_b16 v[132:133], v63 offset:22528\n\t" \
  "v_mfma_f32_16x16x32_bf16 v[204:207], v[72:75], v[92:95], v[204:207]\n\t" \
  "ds_read_b64_tr_b16 v[134:135], v63 offset:23040\n\t" \
  "v_mfma_f32_16x16x32_bf16 v[212:215], v[72:75], v[100:103], v[212:215]\n\t" \
  "ds_read_b64_tr_b16 v[128:129], v62 offset:38912\n\t" \
  "v_mfma_f32_16x16x32_bf16 v[248:251], v[104:107], v[68:71], v[248:251]\n\t" \
  "ds_read_b64_tr_b16 v[130:131], v62 offset:39424\n\t" \
  "v_mfma_f32_16x16x32_bf16 v[216:219], v[96:99], v[68:71], v[216:219]\n\t" \
  "ds_read_b128 v[124:127], v61 offset:0\n\t" \
  "v_mfma_f32_16x16x32_bf16 v[224:227], v[84:87], v[68:71], v[224:227]\n\t" \
  "ds_read_b64_tr_b16 v[148:149], v63 offset:38912\n\t" \
  "v_mfma_f32_16x16x32_bf16 v[232:235], v[92:95], v[68:71], v[232:235]\n\t" \
  "ds_read_b64_tr_b16 v[150:151], v63 offset:39424\n\t" \
  "v_mfma_f32_16x16x32_bf16 v[240:243], v[100:103], v[68:71], v[240:243]\n\t" \
  "v_mfma_f32_16x16x32_bf16 v[252:255], v[104:107], v[80:83], v[252:255]\n\t" \
  "v_mfma_f32_16x16x32_bf16 v[220:223], v[96:99], v[80:83], v[220:223]\n\t" \
  "v_mfma_f32_16x16x32_bf16 v[228:231], v[84:87], v[80:83], v[228:231]\n\t" \
  "v_mfma_f32_16x16x32_bf16 v[236:239], v[92:95], v[80:83], v[236:239]\n\t" \
  "v_mfma_f32_16x16x32_bf16 v[244:247], v[100:103], v[80:83], v[244:247]\n\t" \
  "v_mfma_f32_16x16x32_bf16 v[152:155], v[88:91], v[96:99], v[152:155]\n\t" \
  "v_mfma_f32_16x16x32_bf16 v[160:163], v[88:91], v[84:87], v[160:163]\n\t" \
  "v_mfma_f32_16x16x32_bf16 v[168:171], v[88:91], v[92:95], v[168:171]\n\t" \
  "v_mfma_f32_16x16x32_bf16 v[176:179], v[88:91], v[100:103], v[176:179]\n\t" \
  "s_waitcnt lgkmcnt(0)\n\t" \
  "v_mfma_f32_16x16x32_bf16 v[156:159], v[76:79], v[96:99], v[156:159]\n\t" \
  "v_mfma_f32_16x16x32_bf16 v[164:167], v[76:79], v[84:87], v[164:167]\n\t" \
  "ds_write_b32 v32, v0\n\t" \
  "v_mfma_f32_16x16x32_bf16 v[172:175], v[76:79], v[92:95], v[172:175]\n\t" \
  "v_mfma_f32_16x16x32_bf16 v[180:183], v[76:79], v[100:103], v[180:183]\n\t" \
  "v_mfma_f32_16x16x32_bf16 v[184:187], v[120:123], v[116:119], v[184:187]\n\t" \
  "v_and_b32 v56, -16, v32\n\t" \
  "v_mfma_f32_16x16x32_bf16 v[192:195], v[120:123], v[136:139], v[192:195]\n\t" \
  "v_mfma_f32_16x16x32_bf16 v[200:203], v[120:123], v[108:111], v[200:203]\n\t" \
  "v_mfma_f32_16x16x32_bf16 v[208:211], v[120:123], v[144:147], v[208:211]\n\t" \
  "v_mfma_f32_16x16x32_bf16 v[188:191], v[140:143], v[116:119], v[188:191]\n\t" \
  "v_mfma_f32_16x16x32_bf16 v[196:199], v[140:143], v[136:139], v[196:199]\n\t" \
  "v_mfma_f32_16x16x32_bf16 v[204:207], v[140:143], v[108:111], v[204:207]\n\t" \
  "v_mfma_f32_16x16x32_bf16 v[212:215], v[140:143], v[144:147], v[212:215]\n\t" \
  "v_cvt_pk_bf16_f32 v14, v184, v185\n\t" \
  "v_cvt_pk_bf16_f32 v15, v186, v187\n\t" \
  "v_mfma_f32_16x16x32_bf16 v[152:155], v[112:115], v[116:119], v[152:155]\n\t" \
  "v_cvt_pk_bf16_f32 v18, v192, v193\n\t" \
  "v_cvt_pk_bf16_f32 v19, v194, v195\n\t" \
  "v_mfma_f32_16x16x32_bf16 v[160:163], v[112:115], v[136:139], v[160:163]\n\t" \
  "v_cvt_pk_bf16_f32 v16, v188, v189\n\t" \
  "v_cvt_pk_bf16_f32 v17, v190, v191\n\t" \
  "v_mfma_f32_16x16x32_bf16 v[168:171], v[112:115], v[108:111], v[168:171]\n\t" \
  "v_cvt_pk_bf16_f32 v20, v196, v197\n\t" \
  "v_cvt_pk_bf16_f32 v21, v198, v199\n\t" \
  "v_mfma_f32_16x16x32_bf16 v[176:179], v[112:115], v[144:147], v[176:179]\n\t" \
  "v_cvt_pk_bf16_f32 v22, v200, v201\n\t" \
  "v_cvt_pk_bf16_f32 v23, v202, v203\n\t" \
  "v_mfma_f32_16x16x32_bf16 v[156:159], v[132:135], v[116:119], v[156:159]\n\t" \
  "v_cvt_pk_bf16_f32 v24, v204, v205\n\t" \
  "v_cvt_pk_bf16_f32 v25, v206, v207\n\t" \
  "v_mfma_f32_16x16x32_bf16 v[164:167], v[132:135], v[136:139], v[164:167]\n\t" \
  "v_cvt_pk_bf16_f32 v26, v208, v209\n\t" \
  "v_cvt_pk_bf16_f32 v27, v210, v211\n\t" \
  "v_mfma_f32_16x16x32_bf16 v[172:175], v[132:135], v[108:111], v[172:175]\n\t" \
  "v_cvt_pk_bf16_f32 v28, v212, v213\n\t" \
  "v_cvt_pk_bf16_f32 v29, v214, v215\n\t" \
  "v_mfma_f32_16x16x32_bf16 v[180:183], v[132:135], v[144:147], v[180:183]\n\t" \
  "v_cvt_pk_bf16_f32 v84, v152, v153\n\t" \
  "v_cvt_pk_bf16_f32 v85, v154, v155\n\t" \
  "v_mfma_f32_16x16x32_bf16 v[248:251], v[124:127], v[128:131], v[248:251]\n\t" \
  "v_cvt_pk_bf16_f32 v86, v156, v157\n\t" \
  "v_cvt_pk_bf16_f32 v87, v158, v159\n\t" \
  "v_mfma_f32_16x16x32_bf16 v[216:219], v[116:119], v[128:131], v[216:219]\n\t" \
  "v_cvt_pk_bf16_f32 v88, v160, v161\n\t" \
  "v_cvt_pk_bf16_f32 v89, v162, v163\n\t" \
  "v_mfma_f32_16x16x32_bf16 v[224:227], v[136:139], v[128:131], v[224:227]\n\t" \
  "v_cvt_pk_bf16_f32 v90, v164, v165\n\t" \
  "v_cvt_pk_bf16_f32 v91, v166, v167\n\t" \
  "v_mfma_f32_16x16x32_bf16 v[232:235], v[108:111], v[128:131], v[232:235]\n\t" \
  "v_cvt_pk_bf16_f32 v72, v248, v249\n\t" \
  "v_cvt_pk_bf16_f32 v73, v250, v251\n\t" \
  "v_mfma_f32_16x16x32_bf16 v[240:243], v[144:147], v[128:131], v[240:243]\n\t" \
  "ds_read_b32 v52, v33 offset:48\n\t" \
  "ds_read_b128 v[10:13], v31\n\t" \
  "v_cvt_pk_bf16_f32 v92, v168, v169\n\t" \
  "v_mfma_f32_16x16x32_bf16 v[252:255], v[124:127], v[148:151], v[252:255]\n\t" \
  "v_cvt_pk_bf16_f32 v93, v170, v171\n\t" \
  "v_cvt_pk_bf16_f32 v94, v172, v173\n\t" \
  "v_mfma_f32_16x16x32_bf16 v[220:223], v[116:119], v[148:151], v[220:223]\n\t" \
  "v_cvt_pk_bf16_f32 v95, v174, v175\n\t" \
  "v_cvt_pk_bf16_f32 v96, v176, v177\n\t" \
  "v_mfma_f32_16x16x32_bf16 v[228:231], v[136:139], v[148:151], v[228:231]\n\t" \
  "v_cvt_pk_bf16_f32 v97, v178, v179\n\t" \
  "v_cvt_pk_bf16_f32 v98, v180, v181\n\t" \
  "v_mfma_f32_16x16x32_bf16 v[236:239], v[108:111], v[148:151], v[236:239]\n\t" \
  "v_cvt_pk_bf16_f32 v82, v252, v253\n\t" \
  "v_cvt_pk_bf16_f32 v83, v254, v255\n\t" \
  "v_mfma_f32_16x16x32_bf16 v[244:247], v[144:147], v[148:151], v[244:247]\n\t" \
  "ds_write_b64 v30, v[72:73]\n\t" \
  "ds_write_b64 v30, v[82:83] offset:8\n\t" \
  "ds_write_b32 v32, v0 offset:48\n\t" \
  "v_mfma_f32_16x16x32_bf16 v[200:203], v[22:25], v[92:95], v[34:37]\n\t" \
  "v_cvt_pk_bf16_f32 v99, v182, v183\n\t" \
  "s_waitcnt lgkmcnt(0)\n\t" \
  "v_readfirstlane_b32 vcc_lo, v52\n\t" \
  "s_nop 1\n\t" \
  "s_cmp_eq_u32 vcc_lo, 0\n\t" \
  "s_cbranch_scc1 5f\n\t" \
  "4:\n\t" \
  "v_mfma_f32_16x16x32_bf16 v[204:207], v[18:21], v[92:95], v[38:41]\n\t" \
  "v_cvt_pk_bf16_f32 v66, v232, v233\n\t" \
  "v_cvt_pk_bf16_f32 v67, v234, v235\n\t" \
  "v_mfma_f32_16x16x32_bf16 v[208:211], v[26:29], v[96:99], v[34:37]\n\t" \
  "v_cvt_pk_bf16_f32 v76, v236, v237\n\t" \
  "v_cvt_pk_bf16_f32 v77, v238, v239\n\t" \
  "v_mfma_f32_16x16x32_bf16 v[212:215], v[22:25], v[96:99], v[38:41]\n\t" \
  "v_cvt_pk_bf16_f32 v64, v240, v241\n\t" \
  "v_cvt_pk_bf16_f32 v65, v242, v243\n\t" \
  "v_mfma_f32_16x16x32_bf16 v[192:195], v[18:21], v[88:91], v[34:37]\n\t" \
  "v_max_f32 v200, v200, v204\n\t" \
  "v_max_f32 v201, v201, v205\n\t" \
  "v_mfma_f32_16x16x32_bf16 v[196:199], v[14:17], v[88:91], v[38:41]\n\t" \
  "v_max_f32 v202, v202, v206\n\t" \
  "v_max_f32 v203, v203, v207\n\t" \
  "v_mfma_f32_16x16x32_bf16 v[184:187], v[14:17], v[84:87], v[34:37]\n\t" \
  "v_max_f32 v208, v208, v212\n\t" \
  "v_max_f32 v209, v209, v213\n\t" \
  "v_mfma_f32_16x16x32_bf16 v[188:191], v[10:13], v[84:87], v[42:45]\n\t" \
  "v_max_f32 v210, v210, v214\n\t" \
  "v_max_f32 v211, v211, v215\n\t" \
  "v_max_f32 v192, v192, v196\n\t" \
  "v_max_f32 v193, v193, v197\n\t" \
  "v_max_f32 v194, v194, v198\n\t" \
  "v_max3_f32 v106, v208, v209, v210\n\t" \
  "v_max3_f32 v104, v200, v201, v202\n\t" \
  "v_max_f32 v195, v195, v199\n\t" \
  "v_max_f32 v184, v184, v188\n\t" \
  "v_max_f32 v185, v185, v189\n\t" \
  "v_max_f32 v186, v186, v190\n\t" \
  "v_max3_f32 v102, v192, v193, v194\n\t" \
  "v_max_f32 v187, v187, v191\n\t" \
  "v_max3_f32 v100, v184, v185, v186\n\t" \
  "v_max_f32 v106, v106, v211\n\t" \
  "v_max_f32 v104, v104, v203\n\t" \
  "v_max_f32 v102, v102, v195\n\t" \
  "v_max_f32 v100, v100, v187\n\t" \
  "v_permlane16_swap_b32 v104, v106\n\t" \
  "v_cvt_pk_bf16_f32 v74, v244, v245\n\t" \
  "v_permlane16_swap_b32 v100, v102\n\t" \
  "v_max_f32 v104, v104, v106\n\t" \
  "v_max_f32 v100, v100, v102\n\t" \
  "v_cvt_pk_bf16_f32 v75, v246, v247\n\t" \
  "v_cvt_pk_bf16_f32 v68, v224, v225\n\t" \
  "v_permlane32_swap_b32 v100, v104\n\t" \
  "v_cvt_pk_bf16_f32 v69, v226, v227\n\t" \
  "v_max_f32 v100, v100, v104\n\t" \
  "v_mul_f32 v100, 0xbe8293ee, v100\n\t" \
  "v_mov_b32 v104, v100\n\t" \
  "v_cvt_pk_bf16_f32 v78, v228, v229\n\t" \
  "v_cvt_pk_bf16_f32 v79, v230, v231\n\t" \
  "v_permlane32_swap_b32 v100, v104\n\t" \
  "v_cvt_pk_bf16_f32 v70, v216, v217\n\t" \
  "v_mov_b32 v106, v104\n\t" \
  "v_mov_b32 v102, v100\n\t" \
  "v_cvt_pk_bf16_f32 v71, v218, v219\n\t" \
  "v_permlane16_swap_b32 v104, v106\n\t" \
  "v_permlane16_swap_b32 v100, v102\n\t" \
  "v_fma_f32 v208, v208, %16, v106\n\t" \
  "v_fma_f32 v209, v209, %16, v106\n\t" \
  "v_fma_f32 v210, v210, %16, v106\n\t" \
  "v_fma_f32 v211, v211, %16, v106\n\t" \
  "v_exp_f32 v208, v208\n\t" \
  "v_exp_f32 v209, v209\n\t" \
  "v_exp_f32 v210, v210\n\t" \
  "v_exp_f32 v211, v211\n\t" \
  "v_cvt_pk_bf16_f32 v212, v208, v209\n\t" \
  "v_cvt_pk_bf16_f32 v213, v210, v211\n\t" \
  "v_bfi_b32 v214, v46, 0, v212\n\t" \
  "v_bfi_b32 v215, v47, 0, v213\n\t" \
  "v_and_b32 v212, v46, v212\n\t" \
  "v_and_b32 v213, v47, v213\n\t" \
  "v_fma_f32 v200, v200, %16, v104\n\t" \
  "v_fma_f32 v201, v201, %16, v104\n\t" \
  "v_mfma_f32_16x16x32_bf16 v[208:211], v[48:51], v[212:215], 0\n\t" \
  "v_fma_f32 v202, v202, %16, v104\n\t" \
  "v_fma_f32 v203, v203, %16, v104\n\t" \
  "v_mfma_f32_16x16x32_bf16 v[176:179], v[64:67], v[212:215], 0\n\t" \
  "v_exp_f32 v200, v200\n\t" \
  "v_mfma_f32_16x16x32_bf16 v[180:183], v[74:77], v[212:215], 0\n\t" \
  "v_exp_f32 v201, v201\n\t" \
  "v_exp_f32 v202, v202\n\t" \
  "v_exp_f32 v203, v203\n\t" \
  "v_cvt_pk_bf16_f32 v204, v200, v201\n\t" \
  "v_cvt_pk_bf16_f32 v205, v202, v203\n\t" \
  "v_bfi_b32 v206, v46, 0, v204\n\t" \
  "v_bfi_b32 v207, v47, 0, v205\n\t" \
  "v_rcp_f32 v106, v208\n\t" \
  "v_and_b32 v204, v46, v204\n\t" \
  "v_and_b32 v205, v47, v205\n\t" \
  "v_pk_mul_f32 v[176:177], v[176:177], v[106:107] op_sel_hi:[1,0]\n\t" \
  "v_pk_mul_f32 v[178:179], v[178:179], v[106:107] op_sel_hi:[1,0]\n\t" \
  "v_mfma_f32_16x16x32_bf16 v[200:203], v[48:51], v[204:207], 0\n\t" \
  "v_pk_mul_f32 v[180:181], v[180:181], v[106:107] op_sel_hi:[1,0]\n\t" \
  "v_pk_mul_f32 v[182:183], v[182:183], v[106:107] op_sel_hi:[1,0]\n\t" \
  "v_mfma_f32_16x16x32_bf16 v[168:171], v[66:69], v[204:207], 0\n\t" \
  "v_cvt_pk_bf16_f32 v240, v176, v177\n\t" \
  "v_cvt_pk_bf16_f32 v241, v178, v179\n\t" \
  "v_mfma_f32_16x16x32_bf16 v[172:175], v[76:79], v[204:207], 0\n\t" \
  "v_cvt_pk_bf16_f32 v242, v180, v181\n\t" \
  "v_cvt_pk_bf16_f32 v243, v182, v183\n\t" \
  "global_store_dwordx4 v1, v[240:243], %14 offset:0 sc1\n\t" \
  "v_fma_f32 v192, v192, %16, v102\n\t" \
  "v_fma_f32 v193, v193, %16, v102\n\t" \
  "v_fma_f32 v194, v194, %16, v102\n\t" \
  "v_fma_f32 v195, v195, %16, v102\n\t" \
  "v_exp_f32 v192, v192\n\t" \
  "v_exp_f32 v193, v193\n\t" \
  "v_exp_f32 v194, v194\n\t" \
  "v_exp_f32 v195, v195\n\t" \
  "v_cvt_pk_bf16_f32 v196, v192, v193\n\t" \
  "v_cvt_pk_bf16_f32 v197, v194, v195\n\t" \
  "v_bfi_b32 v198, v46, 0, v196\n\t" \
  "v_bfi_b32 v199, v47, 0, v197\n\t" \
  "v_rcp_f32 v104, v200\n\t" \
  "v_and_b32 v196, v46, v196\n\t" \
  "v_and_b32 v197, v47, v197\n\t" \
  "v_cvt_pk_bf16_f32 v80, v220, v221\n\t" \
  "v_cvt_pk_bf16_f32 v81, v222, v223\n\t" \
  "v_mfma_f32_16x16x32_bf16 v[192:195], v[48:51], v[196:199], 0\n\t" \
  "v_pk_mul_f32 v[168:169], v[168:169], v[104:105] op_sel_hi:[1,0]\n\t" \
  "v_pk_mul_f32 v[170:171], v[170:171], v[104:105] op_sel_hi:[1,0]\n\t" \
  "v_mfma_f32_16x16x32_bf16 v[160:163], v[68:71], v[196:199], 0\n\t" \
  "v_pk_mul_f32 v[172:173], v[172:173], v[104:105] op_sel_hi:[1,0]\n\t" \
  "v_pk_mul_f32 v[174:175], v[174:175], v[104:105] op_sel_hi:[1,0]\n\t" \
  "v_mfma_f32_16x16x32_bf16 v[164:167], v[78:81], v[196:199], 0\n\t" \
  "v_cvt_pk_bf16_f32 v232, v168, v169\n\t" \
  "v_cvt_pk_bf16_f32 v233, v170, v171\n\t" \
  "v_cvt_pk_bf16_f32 v234, v172, v173\n\t" \
  "v_cvt_pk_bf16_f32 v235, v174, v175\n\t" \
  "global_store_dwordx4 v1, v[232:235], %14 offset:-4096 sc1\n\t" \
  "v_fma_f32 v184, v184, %16, v100\n\t" \
  "v_fma_f32 v185, v185, %16, v100\n\t" \
  "v_fma_f32 v186, v186, %16, v100\n\t" \
  "v_fma_f32 v187, v187, %16, v100\n\t" \
  "v_exp_f32 v184, v184\n\t" \
  "v_exp_f32 v185, v185\n\t" \
  "v_exp_f32 v186, v186\n\t" \
  "v_exp_f32 v187, v187\n\t" \
  "v_cvt_pk_bf16_f32 v188, v184, v185\n\t" \
  "v_cvt_pk_bf16_f32 v189, v186, v187\n\t" \
  "v_bfi_b32 v190, v46, 0, v188\n\t" \
  "v_bfi_b32 v191, v47, 0, v189\n\t" \
  "v_rcp_f32 v102, v192\n\t" \
  "v_and_b32 v188, v46, v188\n\t" \
  "v_and_b32 v189, v47, v189\n\t" \
  "v_pk_mul_f32 v[160:161], v[160:161], v[102:103] op_sel_hi:[1,0]\n\t" \
  "v_pk_mul_f32 v[162:163], v[162:163], v[102:103] op_sel_hi:[1,0]\n\t" \
  "v_mfma_f32_16x16x32_bf16 v[184:187], v[48:51], v[188:191], 0\n\t" \
  "v_pk_mul_f32 v[164:165], v[164:165], v[102:103] op_sel_hi:[1,0]\n\t" \
  "v_pk_mul_f32 v[166:167], v[166:167], v[102:103] op_sel_hi:[1,0]\n\t" \
  "v_mfma_f32_16x16x32_bf16 v[152:155], v[70:73], v[188:191], 0\n\t" \
  "v_cvt_pk_bf16_f32 v224, v160, v161\n\t" \
  "v_cvt_pk_bf16_f32 v225, v162, v163\n\t" \
  "v_mfma_f32_16x16x32_bf16 v[156:159], v[80:83], v[188:191], 0\n\t" \
  "v_cvt_pk_bf16_f32 v226, v164, v165\n\t" \
  "v_cvt_pk_bf16_f32 v227, v166, v167\n\t" \
  "global_store_dwordx4 v4, v[224:227], %14 offset:0 sc1\n\t" \
  "v_rcp_f32 v100, v184\n\t" \
  "ds_read_b128 v[60:63], v56 offset:64\n\t" \
  "ds_read_b64_tr_b16 v[108:109], v5 offset:0\n\t" \
  "ds_read_b64_tr_b16 v[110:111], v5 offset:16\n\t" \
  "ds_read_b128 v[112:115], v6 offset:16384\n\t" \
  "ds_read_b128 v[116:119], v6 offset:15360\n\t" \
  "ds_read_b64_tr_b16 v[120:121], v5 offset:512\n\t" \
  "ds_read_b64_tr_b16 v[122:123], v5 offset:528\n\t" \
  "ds_read_b128 v[124:127], v6 offset:14336\n\t" \
  "ds_read_b64_tr_b16 v[128:129], v5 offset:1024\n\t" \
  "ds_read_b64_tr_b16 v[130:131], v5 offset:1040\n\t" \
  "ds_read_b128 v[132:135], v6 offset:13312\n\t" \
  "ds_read_b64_tr_b16 v[136:137], v5 offset:1536\n\t" \
  "ds_read_b64_tr_b16 v[138:139], v5 offset:1552\n\t" \
  "ds_read_b128 v[140:143], v6 offset:12288\n\t" \
  "ds_read_b64_tr_b16 v[144:145], v5 offset:2048\n\t" \
  "v_pk_mul_f32 v[152:153], v[152:153], v[100:101] op_sel_hi:[1,0]\n\t" \
  "v_pk_mul_f32 v[154:155], v[154:155], v[100:101] op_sel_hi:[1,0]\n\t" \
  "v_cvt_pk_bf16_f32 v216, v152, v153\n\t" \
  "v_pk_mul_f32 v[156:157], v[156:157], v[100:101] op_sel_hi:[1,0]\n\t" \
  "v_pk_mul_f32 v[158:159], v[158:159], v[100:101] op_sel_hi:[1,0]\n\t" \
  "v_cvt_pk_bf16_f32 v217, v154, v155\n\t" \
  "v_cvt_pk_bf16_f32 v218, v156, v157\n\t" \
  "v_cvt_pk_bf16_f32 v219, v158, v159\n\t" \
  "global_store_dwordx4 v4, v[216:219], %14 offset:-4096 sc1\n\t" \
  "ds_write_b32 v32, v0 offset:112\n\t" \
  "s_branch 9f\n\t" \
  "5:\n\t" \
  "ds_read_b32 v52, v33 offset:48\n\t" \
  "ds_read_b128 v[10:13], v31\n\t" \
  "s_waitcnt lgkmcnt(0)\n\t" \
  "v_readfirstlane_b32 vcc_lo, v52\n\t" \
  "s_nop 1\n\t" \
  "s_cmp_eq_u32 vcc_lo, 0\n\t" \
  "s_cbranch_scc1 5b\n\t" \
  "s_branch 4b\n\t" \
  "15:\n\t" \
  "ds_read_b128 v[10:13], v14 offset:128\n\t" \
  "s_waitcnt lgkmcnt(0)\n\t" \
  "v_and_b32 v10, v10, v11\n\t" \
  "v_and_b32 v12, v12, v13\n\t" \
  "v_and_b32 v10, v10, v12\n\t" \
  "s_nop 1\n\t" \
  "v_readfirstlane_b32 vcc_lo, v10\n\t" \
  "s_nop 1\n\t" \
  "s_cmp_eq_u32 vcc_lo, 0\n\t" \
  "s_cbranch_scc1 15b\n\t" \
  "s_branch 14b\n\t" \
  "8:\n\t" \
  "global_load_dwordx2 v[10:11], v51, %11\n\t" \
  "global_load_dwordx2 v[12:13], v51, %11 offset:128\n\t" \
  "v_xor_b32 v53, 64, v52\n\t" \
  "v_xor_b32 v55, 32, v54\n\t" \
  "v_add_u32 v56, 43008, v52\n\t" \
  "v_add_u32 v58, 43008, v54\n\t" \
  "v_xor_b32 v57, 64, v56\n\t" \
  "v_xor_b32 v59, 32, v58\n\t" \
  "v_add_u32 v60, 86016, v52\n\t" \
  "v_add_u32 v62, 86016, v54\n\t" \
  "v_xor_b32 v61, 64, v60\n\t" \
  "v_xor_b32 v63, 32, v62\n\t" \
  "s_mov_b32 vcc_hi, 0x05040100\n\t" \
  "s_waitcnt vmcnt(2)\n\t" \
  "s_barrier\n\t" \
  "ds_read_b64_tr_b16 v[64:65], v62 offset:34816\n\t" \
  "ds_read_b64_tr_b16 v[66:67], v62 offset:35328\n\t" \
  "ds_read_b128 v[68:71], v60 offset:2048\n\t" \
  "ds_read_b64_tr_b16 v[72:73], v62 offset:38912\n\t" \
  "ds_read_b64_tr_b16 v[74:75], v62 offset:39424\n\t" \
  "ds_read_b128 v[76:79], v61 offset:2048\n\t" \
  "ds_read_b128 v[80:83], v61 offset:4096\n\t" \
  "ds_read_b128 v[84:87], v60 offset:4096\n\t" \
  "ds_read_b128 v[88:91], v60 offset:6144\n\t" \
  "ds_read_b128 v[92:95], v61 offset:6144\n\t" \
  "ds_read_b128 v[96:99], v61 offset:8192\n\t" \
  "ds_read_b128 v[100:103], v60 offset:8192\n\t" \
  "ds_read_b64_tr_b16 v[104:105], v63 offset:34816\n\t" \
  "ds_read_b64_tr_b16 v[106:107], v63 offset:35328\n\t" \
  "ds_read_b64_tr_b16 v[108:109], v63 offset:38912\n\t" \
  "ds_read_b64_tr_b16 v[110:111], v63 offset:39424\n\t" \
  "s_waitcnt lgkmcnt(13)\n\t" \
  "v_mfma_f32_16x16x32_bf16 v[216:219], v[68:71], v[64:67], 0\n\t" \
  "ds_read_b64_tr_b16 v[112:113], v62 offset:26624\n\t" \
  "ds_read_b64_tr_b16 v[114:115], v62 offset:27136\n\t" \
  "s_waitcnt lgkmcnt(12)\n\t" \
  "v_mfma_f32_16x16x32_bf16 v[216:219], v[76:79], v[72:75], v[216:219]\n\t" \
  "ds_read_b64_tr_b16 v[116:117], v62 offset:30720\n\t" \
  "ds_read_b64_tr_b16 v[118:119], v62 offset:31232\n\t" \
  "s_waitcnt lgkmcnt(13)\n\t" \
  "v_mfma_f32_16x16x32_bf16 v[224:227], v[80:83], v[72:75], 0\n\t" \
  "ds_read_b128 v[120:123], v60 offset:0\n\t" \
  "s_waitcnt lgkmcnt(13)\n\t" \
  "v_mfma_f32_16x16x32_bf16 v[224:227], v[84:87], v[64:67], v[224:227]\n\t" \
  "ds_read_b128 v[124:127], v61 offset:0\n\t" \
  "s_waitcnt lgkmcnt(13)\n\t" \
  "v_mfma_f32_16x16x32_bf16 v[232:235], v[88:91], v[64:67], 0\n\t" \
  "ds_read_b64_tr_b16 v[128:129], v63 offset:26624\n\t" \
  "s_waitcnt lgkmcnt(13)\n\t" \
  "v_mfma_f32_16x16x32_bf16 v[232:235], v[92:95], v[72:75], v[232:235]\n\t" \
  "ds_read_b64_tr_b16 v[130:131], v63 offset:27136\n\t" \
  "s_waitcnt lgkmcnt(13)\n\t" \
  "v_mfma_f32_16x16x32_bf16 v[240:243], v[96:99], v[72:75], 0\n\t" \
  "ds_read_b64_tr_b16 v[132:133], v63 offset:30720\n\t" \
  "s_waitcnt lgkmcnt(9)\n\t" \
  "v_mfma_f32_16x16x32_bf16 v[240:243], v[100:103], v[64:67], v[240:243]\n\t" \
  "ds_read_b64_tr_b16 v[134:135], v63 offset:31232\n\t" \
  "v_mfma_f32_16x16x32_bf16 v[220:223], v[68:71], v[104:107], 0\n\t" \
  "ds_read_b64_tr_b16 v[136:137], v62 offset:18432\n\t" \
  "v_mfma_f32_16x16x32_bf16 v[220:223], v[76:79], v[108:111], v[220:223]\n\t" \
  "ds_read_b64_tr_b16 v[138:139], v62 offset:18944\n\t" \
  "v_mfma_f32_16x16x32_bf16 v[228:231], v[80:83], v[108:111], 0\n\t" \
  "ds_read_b64_tr_b16 v[140:141], v62 offset:22528\n\t" \
  "v_mfma_f32_16x16x32_bf16 v[228:231], v[84:87], v[104:107], v[228:231]\n\t" \
  "ds_read_b64_tr_b16 v[142:143], v62 offset:23040\n\t" \
  "v_mfma_f32_16x16x32_bf16 v[236:239], v[88:91], v[104:107], 0\n\t" \
  "ds_read_b64_tr_b16 v[144:145], v63 offset:18432\n\t" \
  "v_mfma_f32_16x16x32_bf16 v[236:239], v[92:95], v[108:111], v[236:239]\n\t" \
  "ds_read_b64_tr_b16 v[146:147], v63 offset:18944\n\t" \
  "v_mfma_f32_16x16x32_bf16 v[244:247], v[96:99], v[108:111], 0\n\t" \
  "ds_read_b64_tr_b16 v[148:149], v63 offset:22528\n\t" \
  "v_mfma_f32_16x16x32_bf16 v[244:247], v[100:103], v[104:107], v[244:247]\n\t" \
  "ds_read_b64_tr_b16 v[150:151], v63 offset:23040\n\t" \
  "s_waitcnt lgkmcnt(7)\n\t" \
  "v_mfma_f32_16x16x32_bf16 v[184:187], v[112:115], v[68:71], 0\n\t" \
  "v_mfma_f32_16x16x32_bf16 v[184:187], v[116:119], v[76:79], v[184:187]\n\t" \
  "v_mfma_f32_16x16x32_bf16 v[192:195], v[116:119], v[80:83], 0\n\t" \
  "v_mfma_f32_16x16x32_bf16 v[192:195], v[112:115], v[84:87], v[192:195]\n\t" \
  "v_mfma_f32_16x16x32_bf16 v[200:203], v[112:115], v[88:91], 0\n\t" \
  "v_mfma_f32_16x16x32_bf16 v[200:203], v[116:119], v[92:95], v[200:203]\n\t" \
  "v_mfma_f32_16x16x32_bf16 v[208:211], v[116:119], v[96:99], 0\n\t" \
  "v_mfma_f32_16x16x32_bf16 v[208:211], v[112:115], v[100:103], v[208:211]\n\t" \
  "v_mfma_f32_16x16x32_bf16 v[248:251], v[112:115], v[120:123], 0\n\t" \
  "v_mfma_f32_16x16x32_bf16 v[248:251], v[116:119], v[124:127], v[248:251]\n\t" \
  "v_mfma_f32_16x16x32_bf16 v[252:255], v[128:131], v[120:123], 0\n\t" \
  "v_mfma_f32_16x16x32_bf16 v[252:255], v[132:135], v[124:127], v[252:255]\n\t" \
  "v_mfma_f32_16x16x32_bf16 v[188:191], v[132:135], v[76:79], 0\n\t" \
  "v_mfma_f32_16x16x32_bf16 v[188:191], v[128:131], v[68:71], v[188:191]\n\t" \
  "s_waitcnt lgkmcnt(0)\n\t" \
  "s_getpc_b64 s[88:89]\n\t" \
  "s_branch 28b\n\t" \
  "s_barrier\n\t" \
  "v_mfma_f32_16x16x32_bf16 v[196:199], v[128:131], v[84:87], 0\n\t" \
  "ds_read_b64_tr_b16 v[72:73], v54 offset:34816\n\t" \
  "v_mfma_f32_16x16x32_bf16 v[196:199], v[132:135], v[80:83], v[196:199]\n\t" \
  "ds_read_b64_tr_b16 v[74:75], v54 offset:35328\n\t" \
  "v_mfma_f32_16x16x32_bf16 v[204:207], v[132:135], v[92:95], 0\n\t" \
  "ds_read_b128 v[64:67], v52 offset:2048\n\t" \
  "v_mfma_f32_16x16x32_bf16 v[204:207], v[128:131], v[88:91], v[204:207]\n\t" \
  "ds_read_b64_tr_b16 v[108:109], v54 offset:38912\n\t" \
  "v_mfma_f32_16x16x32_bf16 v[212:215], v[128:131], v[100:103], 0\n\t" \
  "ds_read_b64_tr_b16 v[110:111], v54 offset:39424\n\t" \
  "v_mfma_f32_16x16x32_bf16 v[212:215], v[132:135], v[96:99], v[212:215]\n\t" \
  "ds_read_b128 v[104:107], v53 offset:2048\n\t" \
  "v_mfma_f32_16x16x32_bf16 v[152:155], v[136:139], v[68:71], 0\n\t" \
  "ds_read_b128 v[112:115], v53 offset:4096\n\t" \
  "v_mfma_f32_16x16x32_bf16 v[152:155], v[140:143], v[76:79], v[152:155]\n\t" \
  "ds_read_b128 v[116:119], v52 offset:4096\n\t" \
  "v_mfma_f32_16x16x32_bf16 v[160:163], v[140:143], v[80:83], 0\n\t" \
  "ds_read_b128 v[120:123], v52 offset:6144\n\t" \
  "v_mfma_f32_16x16x32_bf16 v[160:163], v[136:139], v[84:87], v[160:163]\n\t" \
  "ds_read_b128 v[124:127], v53 offset:6144\n\t" \
  "v_mfma_f32_16x16x32_bf16 v[168:171], v[136:139], v[88:91], 0\n\t" \
  "ds_read_b128 v[128:131], v53 offset:8192\n\t" \
  "v_mfma_f32_16x16x32_bf16 v[168:171], v[140:143], v[92:95], v[168:171]\n\t" \
  "ds_read_b128 v[132:135], v52 offset:8192\n\t" \
  "v_mfma_f32_16x16x32_bf16 v[176:179], v[140:143], v[96:99], 0\n\t" \
  "ds_read_b64_tr_b16 v[140:141], v55 offset:34816\n\t" \
  "v_mfma_f32_16x16x32_bf16 v[176:179], v[136:139], v[100:103], v[176:179]\n\t" \
  "ds_read_b64_tr_b16 v[142:143], v55 offset:35328\n\t" \
  "v_mfma_f32_16x16x32_bf16 v[156:159], v[144:147], v[68:71], 0\n\t" \
  "ds_read_b64_tr_b16 v[136:137], v55 offset:38912\n\t" \
  "v_mfma_f32_16x16x32_bf16 v[156:159], v[148:151], v[76:79], v[156:159]\n\t" \
  "ds_read_b64_tr_b16 v[138:139], v55 offset:39424\n\t" \
  "v_mfma_f32_16x16x32_bf16 v[164:167], v[148:151], v[80:83], 0\n\t" \
  "ds_read_b64_tr_b16 v[68:69], v54 offset:26624\n\t" \
  "v_mfma_f32_16x16x32_bf16 v[164:167], v[144:147], v[84:87], v[164:167]\n\t" \
  "ds_read_b64_tr_b16 v[70:71], v54 offset:27136\n\t" \
  "v_mfma_f32_16x16x32_bf16 v[172:175], v[144:147], v[88:91], 0\n\t" \
  "ds_read_b64_tr_b16 v[76:77], v54 offset:30720\n\t" \
  "v_mfma_f32_16x16x32_bf16 v[172:175], v[148:151], v[92:95], v[172:175]\n\t" \
  "ds_read_b64_tr_b16 v[78:79], v54 offset:31232\n\t" \
  "v_mfma_f32_16x16x32_bf16 v[180:183], v[148:151], v[96:99], 0\n\t" \
  "ds_read_b128 v[80:83], v52 offset:0\n\t" \
  "v_mfma_f32_16x16x32_bf16 v[180:183], v[144:147], v[100:103], v[180:183]\n\t" \
  "ds_read_b128 v[84:87], v53 offset:0\n\t" \
  "s_waitcnt lgkmcnt(7)\n\t" \
  "v_mfma_f32_16x16x32_bf16 v[216:219], v[64:67], v[72:75], v[216:219]\n\t" \
  "ds_read_b64_tr_b16 v[88:89], v55 offset:26624\n\t" \
  "v_mfma_f32_16x16x32_bf16 v[216:219], v[104:107], v[108:111], v[216:219]\n\t" \
  "ds_read_b64_tr_b16 v[90:91], v55 offset:27136\n\t" \
  "v_mfma_f32_16x16x32_bf16 v[224:227], v[112:115], v[108:111], v[224:227]\n\t" \
  "ds_read_b64_tr_b16 v[92:93], v55 offset:30720\n\t" \
  "v_mfma_f32_16x16x32_bf16 v[224:227], v[116:119], v[72:75], v[224:227]\n\t" \
  "ds_read_b64_tr_b16 v[94:95], v55 offset:31232\n\t" \
  "v_mfma_f32_16x16x32_bf16 v[232:235], v[120:123], v[72:75], v[232:235]\n\t" \
  "ds_read_b64_tr_b16 v[96:97], v54 offset:18432\n\t" \
  "v_mfma_f32_16x16x32_bf16 v[232:235], v[124:127], v[108:111], v[232:235]\n\t" \
  "ds_read_b64_tr_b16 v[98:99], v54 offset:18944\n\t" \
  "v_mfma_f32_16x16x32_bf16 v[240:243], v[128:131], v[108:111], v[240:243]\n\t" \
  "ds_read_b64_tr_b16 v[148:149], v54 offset:22528\n\t" \
  "v_mfma_f32_16x16x32_bf16 v[240:243], v[132:135], v[72:75], v[240:243]\n\t" \
  "ds_read_b64_tr_b16 v[150:151], v54 offset:23040\n\t" \
  "v_mfma_f32_16x16x32_bf16 v[220:223], v[64:67], v[140:143], v[220:223]\n\t" \
  "ds_read_b64_tr_b16 v[100:101], v55 offset:18432\n\t" \
  "s_waitcnt lgkmcnt(7)\n\t" \
  "v_mfma_f32_16x16x32_bf16 v[220:223], v[104:107], v[136:139], v[220:223]\n\t" \
  "ds_read_b64_tr_b16 v[102:103], v55 offset:18944\n\t" \
  "v_mfma_f32_16x16x32_bf16 v[228:231], v[112:115], v[136:139], v[228:231]\n\t" \
  "ds_read_b64_tr_b16 v[144:145], v55 offset:22528\n\t" \
  "v_mfma_f32_16x16x32_bf16 v[228:231], v[116:119], v[140:143], v[228:231]\n\t" \
  "ds_read_b64_tr_b16 v[146:147], v55 offset:23040\n\t" \
  "v_mfma_f32_16x16x32_bf16 v[236:239], v[120:123], v[140:143], v[236:239]\n\t" \
  "v_mfma_f32_16x16x32_bf16 v[236:239], v[124:127], v[136:139], v[236:239]\n\t" \
  "v_mfma_f32_16x16x32_bf16 v[244:247], v[128:131], v[136:139], v[244:247]\n\t" \
  "v_mfma_f32_16x16x32_bf16 v[244:247], v[132:135], v[140:143], v[244:247]\n\t" \
  "v_mfma_f32_16x16x32_bf16 v[184:187], v[68:71], v[64:67], v[184:187]\n\t" \
  "v_mfma_f32_16x16x32_bf16 v[184:187], v[76:79], v[104:107], v[184:187]\n\t" \
  "v_mfma_f32_16x16x32_bf16 v[192:195], v[76:79], v[112:115], v[192:195]\n\t" \
  "v_mfma_f32_16x16x32_bf16 v[192:195], v[68:71], v[116:119], v[192:195]\n\t" \
  "v_mfma_f32_16x16x32_bf16 v[200:203], v[68:71], v[120:123], v[200:203]\n\t" \
  "v_mfma_f32_16x16x32_bf16 v[200:203], v[76:79], v[124:127], v[200:203]\n\t" \
  "v_mfma_f32_16x16x32_bf16 v[208:211], v[76:79], v[128:131], v[208:211]\n\t" \
  "v_mfma_f32_16x16x32_bf16 v[208:211], v[68:71], v[132:135], v[208:211]\n\t" \
  "v_mfma_f32_16x16x32_bf16 v[248:251], v[68:71], v[80:83], v[248:251]\n\t" \
  "v_mfma_f32_16x16x32_bf16 v[248:251], v[76:79], v[84:87], v[248:251]\n\t" \
  "v_mfma_f32_16x16x32_bf16 v[252:255], v[88:91], v[80:83], v[252:255]\n\t" \
  "s_waitcnt lgkmcnt(0)\n\t" \
  "v_mfma_f32_16x16x32_bf16 v[252:255], v[92:95], v[84:87], v[252:255]\n\t" \
  "v_mfma_f32_16x16x32_bf16 v[188:191], v[92:95], v[104:107], v[188:191]\n\t" \
  "v_mfma_f32_16x16x32_bf16 v[188:191], v[88:91], v[64:67], v[188:191]\n\t" \
  "s_barrier\n\t" \
  "v_mfma_f32_16x16x32_bf16 v[196:199], v[88:91], v[116:119], v[196:199]\n\t" \
  "ds_read_b64_tr_b16 v[108:109], v58 offset:34816\n\t" \
  "v_mfma_f32_16x16x32_bf16 v[196:199], v[92:95], v[112:115], v[196:199]\n\t" \
  "ds_read_b64_tr_b16 v[110:111], v58 offset:35328\n\t" \
  "v_mfma_f32_16x16x32_bf16 v[204:207], v[92:95], v[124:127], v[204:207]\n\t" \
  "ds_read_b128 v[72:75], v56 offset:2048\n\t" \
  "v_mfma_f32_16x16x32_bf16 v[204:207], v[88:91], v[120:123], v[204:207]\n\t" \
  "ds_read_b64_tr_b16 v[136:137], v58 offset:38912\n\t" \
  "v_mfma_f32_16x16x32_bf16 v[212:215], v[88:91], v[132:135], v[212:215]\n\t" \
  "ds_read_b64_tr_b16 v[138:139], v58 offset:39424\n\t" \
  "v_mfma_f32_16x16x32_bf16 v[212:215], v[92:95], v[128:131], v[212:215]\n\t" \
  "ds_read_b128 v[140:143], v57 offset:2048\n\t" \
  "v_mfma_f32_16x16x32_bf16 v[152:155], v[96:99], v[64:67], v[152:155]\n\t" \
  "ds_read_b128 v[68:71], v57 offset:4096\n\t" \
  "v_mfma_f32_16x16x32_bf16 v[152:155], v[148:151], v[104:107], v[152:155]\n\t" \
  "ds_read_b128 v[76:79], v56 offset:4096\n\t" \
  "v_mfma_f32_16x16x32_bf16 v[160:163], v[148:151], v[112:115], v[160:163]\n\t" \
  "ds_read_b128 v[80:83], v56 offset:6144\n\t" \
  "v_mfma_f32_16x16x32_bf16 v[160:163], v[96:99], v[116:119], v[160:163]\n\t" \
  "ds_read_b128 v[84:87], v57 offset:6144\n\t" \
  "v_mfma_f32_16x16x32_bf16 v[168:171], v[96:99], v[120:123], v[168:171]\n\t" \
  "ds_read_b128 v[88:91], v57 offset:8192\n\t" \
  "v_mfma_f32_16x16x32_bf16 v[168:171], v[148:151], v[124:127], v[168:171]\n\t" \
  "ds_read_b128 v[92:95], v56 offset:8192\n\t" \
  "v_mfma_f32_16x16x32_bf16 v[176:179], v[148:151], v[128:131], v[176:179]\n\t" \
  "ds_read_b64_tr_b16 v[148:149], v59 offset:34816\n\t" \
  "v_mfma_f32_16x16x32_bf16 v[176:179], v[96:99], v[132:135], v[176:179]\n\t" \
  "ds_read_b64_tr_b16 v[150:151], v59 offset:35328\n\t" \
  "v_mfma_f32_16x16x32_bf16 v[156:159], v[100:103], v[64:67], v[156:159]\n\t" \
  "ds_read_b64_tr_b16 v[96:97], v59 offset:38912\n\t" \
  "v_mfma_f32_16x16x32_bf16 v[156:159], v[144:147], v[104:107], v[156:159]\n\t" \
  "ds_read_b64_tr_b16 v[98:99], v59 offset:39424\n\t" \
  "v_mfma_f32_16x16x32_bf16 v[164:167], v[144:147], v[112:115], v[164:167]\n\t" \
  "ds_read_b64_tr_b16 v[64:65], v58 offset:26624\n\t" \
  "v_mfma_f32_16x16x32_bf16 v[164:167], v[100:103], v[116:119], v[164:167]\n\t" \
  "ds_read_b64_tr_b16 v[66:67], v58 offset:27136\n\t" \
  "v_mfma_f32_16x16x32_bf16 v[172:175], v[100:103], v[120:123], v[172:175]\n\t" \
  "ds_read_b64_tr_b16 v[104:105], v58 offset:30720\n\t" \
  "v_mfma_f32_16x16x32_bf16 v[172:175], v[144:147], v[124:127], v[172:175]\n\t" \
  "ds_read_b64_tr_b16 v[106:107], v58 offset:31232\n\t" \
  "v_mfma_f32_16x16x32_bf16 v[180:183], v[144:147], v[128:131], v[180:183]\n\t" \
  "ds_read_b128 v[112:115], v56 offset:0\n\t" \
  "v_mfma_f32_16x16x32_bf16 v[180:183], v[100:103], v[132:135], v[180:183]\n\t" \
  "ds_read_b128 v[116:119], v57 offset:0\n\t" \
  "s_waitcnt lgkmcnt(7)\n\t" \
  "v_mfma_f32_16x16x32_bf16 v[216:219], v[72:75], v[108:111], v[216:219]\n\t" \
  "ds_read_b64_tr_b16 v[120:121], v59 offset:26624\n\t" \
  "v_mfma_f32_16x16x32_bf16 v[216:219], v[140:143], v[136:139], v[216:219]\n\t" \
  "ds_read_b64_tr_b16 v[122:123], v59 offset:27136\n\t" \
  "v_mfma_f32_16x16x32_bf16 v[224:227], v[68:71], v[136:139], v[224:227]\n\t" \
  "ds_read_b64_tr_b16 v[124:125], v59 offset:30720\n\t" \
  "v_mfma_f32_16x16x32_bf16 v[224:227], v[76:79], v[108:111], v[224:227]\n\t" \
  "ds_read_b64_tr_b16 v[126:127], v59 offset:31232\n\t" \
  "v_mfma_f32_16x16x32_bf16 v[232:235], v[80:83], v[108:111], v[232:235]\n\t" \
  "ds_read_b64_tr_b16 v[128:129], v58 offset:18432\n\t" \
  "v_mfma_f32_16x16x32_bf16 v[232:235], v[84:87], v[136:139], v[232:235]\n\t" \
  "ds_read_b64_tr_b16 v[130:131], v58 offset:18944\n\t" \
  "v_mfma_f32_16x16x32_bf16 v[240:243], v[88:91], v[136:139], v[240:243]\n\t" \
  "ds_read_b64_tr_b16 v[144:145], v58 offset:22528\n\t" \
  "v_mfma_f32_16x16x32_bf16 v[240:243], v[92:95], v[108:111], v[240:243]\n\t" \
  "ds_read_b64_tr_b16 v[146:147], v58 offset:23040\n\t" \
  "v_mfma_f32_16x16x32_bf16 v[220:223], v[72:75], v[148:151], v[220:223]\n\t" \
  "ds_read_b64_tr_b16 v[100:101], v59 offset:18432\n\t" \
  "s_waitcnt lgkmcnt(7)\n\t" \
  "v_mfma_f32_16x16x32_bf16 v[220:223], v[140:143], v[96:99], v[220:223]\n\t" \
  "ds_read_b64_tr_b16 v[102:103], v59 offset:18944\n\t" \
  "v_mfma_f32_16x16x32_bf16 v[228:231], v[68:71], v[96:99], v[228:231]\n\t" \
  "ds_read_b64_tr_b16 v[132:133], v59 offset:22528\n\t" \
  "v_mfma_f32_16x16x32_bf16 v[228:231], v[76:79], v[148:151], v[228:231]\n\t" \
  "ds_read_b64_tr_b16 v[134:135], v59 offset:23040\n\t" \
  "v_mfma_f32_16x16x32_bf16 v[236:239], v[80:83], v[148:151], v[236:239]\n\t" \
  "v_mfma_f32_16x16x32_bf16 v[236:239], v[84:87], v[96:99], v[236:239]\n\t" \
  "v_mfma_f32_16x16x32_bf16 v[244:247], v[88:91], v[96:99], v[244:247]\n\t" \
  "v_mfma_f32_16x16x32_bf16 v[244:247], v[92:95], v[148:151], v[244:247]\n\t" \
  "503:\n\t" \
  "s_cbranch_execz 507f\n\t" \
  "v_mfma_f32_16x16x32_bf16 v[184:187], v[64:67], v[72:75], v[184:187]\n\t" \
  "v_mfma_f32_16x16x32_bf16 v[184:187], v[104:107], v[140:143], v[184:187]\n\t" \
  "v_mfma_f32_16x16x32_bf16 v[192:195], v[104:107], v[68:71], v[192:195]\n\t" \
  "v_mfma_f32_16x16x32_bf16 v[192:195], v[64:67], v[76:79], v[192:195]\n\t" \
  "v_mfma_f32_16x16x32_bf16 v[200:203], v[64:67], v[80:83], v[200:203]\n\t" \
  "v_mfma_f32_16x16x32_bf16 v[200:203], v[104:107], v[84:87], v[200:203]\n\t" \
  "v_mfma_f32_16x16x32_bf16 v[208:211], v[104:107], v[88:91], v[208:211]\n\t" \
  "v_mfma_f32_16x16x32_bf16 v[208:211], v[64:67], v[92:95], v[208:211]\n\t" \
  "v_mfma_f32_16x16x32_bf16 v[248:251], v[64:67], v[112:115], v[248:251]\n\t" \
  "v_mfma_f32_16x16x32_bf16 v[248:251], v[104:107], v[116:119], v[248:251]\n\t" \
  "v_mfma_f32_16x16x32_bf16 v[252:255], v[120:123], v[112:115], v[252:255]\n\t" \
  "s_waitcnt lgkmcnt(0)\n\t" \
  "v_mfma_f32_16x16x32_bf16 v[252:255], v[124:127], v[116:119], v[252:255]\n\t" \
  "v_mfma_f32_16x16x32_bf16 v[188:191], v[124:127], v[140:143], v[188:191]\n\t" \
  "v_mfma_f32_16x16x32_bf16 v[188:191], v[120:123], v[72:75], v[188:191]\n\t" \
  "s_barrier\n\t" \
  "v_mfma_f32_16x16x32_bf16 v[196:199], v[120:123], v[76:79], v[196:199]\n\t" \
  "ds_read_b64_tr_b16 v[136:137], v62 offset:34816\n\t" \
  "v_bfe_u32 v30, v4, 9, 1\n\t" \
  "v_mfma_f32_16x16x32_bf16 v[196:199], v[124:127], v[68:71], v[196:199]\n\t" \
  "ds_read_b64_tr_b16 v[138:139], v62 offset:35328\n\t" \
  "v_mad_i32_i24 v30, v30, -16, v0\n\t" \
  "v_mfma_f32_16x16x32_bf16 v[204:207], v[124:127], v[84:87], v[204:207]\n\t" \
  "ds_read_b128 v[108:111], v60 offset:2048\n\t" \
  "ds_read_b128 v[26:29], v0 offset:1552\n\t" \
  "v_mfma_f32_16x16x32_bf16 v[204:207], v[120:123], v[80:83], v[204:207]\n\t" \
  "ds_read_b64_tr_b16 v[96:97], v62 offset:38912\n\t" \
  "ds_read_b128 v[30:33], v30 offset:1040\n\t" \
  "v_mfma_f32_16x16x32_bf16 v[212:215], v[120:123], v[92:95], v[212:215]\n\t" \
  "ds_read_b64_tr_b16 v[98:99], v62 offset:39424\n\t" \
  "ds_read_b128 v[34:37], v0 offset:512\n\t" \
  "v_mfma_f32_16x16x32_bf16 v[212:215], v[124:127], v[88:91], v[212:215]\n\t" \
  "ds_read_b128 v[148:151], v61 offset:2048\n\t" \
  "ds_read_b64_tr_b16 v[38:39], v1 offset:512\n\t" \
  "v_mfma_f32_16x16x32_bf16 v[152:155], v[128:131], v[72:75], v[152:155]\n\t" \
  "ds_read_b128 v[64:67], v61 offset:4096\n\t" \
  "ds_read_b64_tr_b16 v[40:41], v1 offset:0\n\t" \
  "v_mfma_f32_16x16x32_bf16 v[152:155], v[144:147], v[140:143], v[152:155]\n\t" \
  "ds_read_b128 v[104:107], v60 offset:4096\n\t" \
  "s_waitcnt vmcnt(0)\n\t" \
  "v_mfma_f32_16x16x32_bf16 v[160:163], v[144:147], v[68:71], v[160:163]\n\t" \
  "ds_read_b128 v[112:115], v60 offset:6144\n\t" \
  "v_perm_b32 v18, v10, v10, v14\n\t" \
  "v_mfma_f32_16x16x32_bf16 v[160:163], v[128:131], v[76:79], v[160:163]\n\t" \
  "ds_read_b128 v[116:119], v61 offset:6144\n\t" \
  "v_perm_b32 v19, v10, v10, v15\n\t" \
  "v_mfma_f32_16x16x32_bf16 v[168:171], v[128:131], v[80:83], v[168:171]\n\t" \
  "ds_read_b128 v[120:123], v61 offset:8192\n\t" \
  "v_perm_b32 v20, v10, v10, v16\n\t" \
  "v_mfma_f32_16x16x32_bf16 v[168:171], v[144:147], v[84:87], v[168:171]\n\t" \
  "ds_read_b128 v[124:127], v60 offset:8192\n\t" \
  "v_perm_b32 v21, v10, v10, v17\n\t" \
  "v_mfma_f32_16x16x32_bf16 v[176:179], v[144:147], v[88:91], v[176:179]\n\t" \
  "ds_read_b64_tr_b16 v[144:145], v63 offset:34816\n\t" \
  "v_perm_b32 v22, v11, v11, v14\n\t" \
  "v_mfma_f32_16x16x32_bf16 v[176:179], v[128:131], v[92:95], v[176:179]\n\t" \
  "ds_read_b64_tr_b16 v[146:147], v63 offset:35328\n\t" \
  "v_perm_b32 v23, v11, v11, v15\n\t" \
  "v_mfma_f32_16x16x32_bf16 v[156:159], v[100:103], v[72:75], v[156:159]\n\t" \
  "ds_read_b64_tr_b16 v[128:129], v63 offset:38912\n\t" \
  "v_perm_b32 v24, v11, v11, v16\n\t" \
  "v_mfma_f32_16x16x32_bf16 v[156:159], v[132:135], v[140:143], v[156:159]\n\t" \
  "ds_read_b64_tr_b16 v[130:131], v63 offset:39424\n\t" \
  "v_perm_b32 v25, v11, v11, v17\n\t" \
  "v_mfma_f32_16x16x32_bf16 v[164:167], v[132:135], v[68:71], v[164:167]\n\t" \
  "ds_read_b64_tr_b16 v[72:73], v62 offset:26624\n\t" \
  "v_mfma_f32_16x16x32_bf16 v[164:167], v[100:103], v[76:79], v[164:167]\n\t" \
  "ds_read_b64_tr_b16 v[74:75], v62 offset:27136\n\t" \
  "v_mfma_f32_16x16x32_bf16 v[172:175], v[100:103], v[80:83], v[172:175]\n\t" \
  "ds_read_b64_tr_b16 v[140:141], v62 offset:30720\n\t" \
  "s_waitcnt lgkmcnt(6)\n\t" \
  "v_mfma_f32_16x16x32_bf16 v[42:45], v[26:29], v[18:21], 0\n\t" \
  "v_mfma_f32_16x16x32_bf16 v[42:45], v[30:33], v[22:25], v[42:45]\n\t" \
  "v_mfma_f32_16x16x32_bf16 v[46:49], v[30:33], v[18:21], 0\n\t" \
  "v_mfma_f32_16x16x32_bf16 v[46:49], v[34:37], v[22:25], v[46:49]\n\t" \
  "ds_read_b128 v[26:29], v0 offset:1808\n\t" \
  "ds_read_b128 v[30:33], v0 offset:1296\n\t" \
  "v_mfma_f32_16x16x32_bf16 v[172:175], v[132:135], v[84:87], v[172:175]\n\t" \
  "ds_read_b64_tr_b16 v[142:143], v62 offset:31232\n\t" \
  "ds_read_b128 v[34:37], v0 offset:768\n\t" \
  "v_mfma_f32_16x16x32_bf16 v[180:183], v[132:135], v[88:91], v[180:183]\n\t" \
  "ds_read_b128 v[68:71], v60 offset:0\n\t" \
  "v_mfma_f32_16x16x32_bf16 v[180:183], v[100:103], v[92:95], v[180:183]\n\t" \
  "ds_read_b128 v[76:79], v61 offset:0\n\t" \
  "v_mfma_f32_16x16x32_bf16 v[216:219], v[108:111], v[136:139], v[216:219]\n\t" \
  "ds_read_b64_tr_b16 v[80:81], v63 offset:26624\n\t" \
  "v_lshlrev_b32 v50, 16, v38\n\t" \
  "v_mfma_f32_16x16x32_bf16 v[216:219], v[148:151], v[96:99], v[216:219]\n\t" \
  "ds_read_b64_tr_b16 v[82:83], v63 offset:27136\n\t" \
  "v_lshlrev_b32 v51, 16, v40\n\t" \
  "v_mfma_f32_16x16x32_bf16 v[224:227], v[64:67], v[96:99], v[224:227]\n\t" \
  "ds_read_b64_tr_b16 v[84:85], v63 offset:30720\n\t" \
  "v_mul_f32 v42, v42, v50\n\t" \
  "v_mfma_f32_16x16x32_bf16 v[224:227], v[104:107], v[136:139], v[224:227]\n\t" \
  "ds_read_b64_tr_b16 v[86:87], v63 offset:31232\n\t" \
  "v_mul_f32 v46, v46, v51\n\t" \
  "v_mfma_f32_16x16x32_bf16 v[232:235], v[112:115], v[136:139], v[232:235]\n\t" \
  "ds_read_b64_tr_b16 v[88:89], v62 offset:18432\n\t" \
  "v_cvt_pk_bf16_f32 v42, v42, v46\n\t" \
  "v_mfma_f32_16x16x32_bf16 v[232:235], v[116:119], v[96:99], v[232:235]\n\t" \
  "ds_read_b64_tr_b16 v[90:91], v62 offset:18944\n\t" \
  "v_and_b32 v50, 0xffff0000, v38\n\t" \
  "v_mfma_f32_16x16x32_bf16 v[240:243], v[120:123], v[96:99], v[240:243]\n\t" \
  "ds_read_b64_tr_b16 v[132:133], v62 offset:22528\n\t" \
  "v_and_b32 v51, 0xffff0000, v40\n\t" \
  "v_mfma_f32_16x16x32_bf16 v[240:243], v[124:127], v[136:139], v[240:243]\n\t" \
  "ds_read_b64_tr_b16 v[134:135], v62 offset:23040\n\t" \
  "v_mul_f32 v43, v43, v50\n\t" \
  "s_waitcnt lgkmcnt(7)\n\t" \
  "v_mfma_f32_16x16x32_bf16 v[220:223], v[108:111], v[144:147], v[220:223]\n\t" \
  "ds_read_b64_tr_b16 v[92:93], v63 offset:18432\n\t" \
  "v_mul_f32 v47, v47, v51\n\t" \
  "v_mfma_f32_16x16x32_bf16 v[220:223], v[148:151], v[128:131], v[220:223]\n\t" \
  "ds_read_b64_tr_b16 v[94:95], v63 offset:18944\n\t" \
  "v_cvt_pk_bf16_f32 v43, v43, v47\n\t" \
  "v_mfma_f32_16x16x32_bf16 v[228:231], v[64:67], v[128:131], v[228:231]\n\t" \
  "ds_read_b64_tr_b16 v[100:101], v63 offset:22528\n\t" \
  "v_lshlrev_b32 v50, 16, v39\n\t" \
  "v_mfma_f32_16x16x32_bf16 v[228:231], v[104:107], v[144:147], v[228:231]\n\t" \
  "ds_read_b64_tr_b16 v[102:103], v63 offset:23040\n\t" \
  "v_lshlrev_b32 v51, 16, v41\n\t" \
  "v_mfma_f32_16x16x32_bf16 v[236:239], v[112:115], v[144:147], v[236:239]\n\t" \
  "v_mul_f32 v44, v44, v50\n\t" \
  "v_mul_f32 v48, v48, v51\n\t" \
  "v_mfma_f32_16x16x32_bf16 v[236:239], v[116:119], v[128:131], v[236:239]\n\t" \
  "v_cvt_pk_bf16_f32 v44, v44, v48\n\t" \
  "v_and_b32 v50, 0xffff0000, v39\n\t" \
  "v_mfma_f32_16x16x32_bf16 v[244:247], v[120:123], v[128:131], v[244:247]\n\t" \
  "v_and_b32 v51, 0xffff0000, v41\n\t" \
  "v_mul_f32 v45, v45, v50\n\t" \
  "v_mfma_f32_16x16x32_bf16 v[244:247], v[124:127], v[144:147], v[244:247]\n\t" \
  "v_mul_f32 v49, v49, v51\n\t" \
  "ds_read_b64_tr_b16 v[38:39], v1 offset:768\n\t" \
  "v_mfma_f32_16x16x32_bf16 v[184:187], v[72:75], v[108:111], v[184:187]\n\t" \
  "ds_read_b64_tr_b16 v[40:41], v1 offset:256\n\t" \
  "v_cvt_pk_bf16_f32 v45, v45, v49\n\t" \
  "v_mfma_f32_16x16x32_bf16 v[184:187], v[140:143], v[148:151], v[184:187]\n\t" \
  "v_mfma_f32_16x16x32_bf16 v[192:195], v[140:143], v[64:67], v[192:195]\n\t" \
  "v_mfma_f32_16x16x32_bf16 v[192:195], v[72:75], v[104:107], v[192:195]\n\t" \
  "v_mfma_f32_16x16x32_bf16 v[200:203], v[72:75], v[112:115], v[200:203]\n\t" \
  "v_mfma_f32_16x16x32_bf16 v[200:203], v[140:143], v[116:119], v[200:203]\n\t" \
  "v_mfma_f32_16x16x32_bf16 v[208:211], v[140:143], v[120:123], v[208:211]\n\t" \
  "v_mfma_f32_16x16x32_bf16 v[208:211], v[72:75], v[124:127], v[208:211]\n\t" \
  "v_mfma_f32_16x16x32_bf16 v[248:251], v[72:75], v[68:71], v[248:251]\n\t" \
  "v_mfma_f32_16x16x32_bf16 v[248:251], v[140:143], v[76:79], v[248:251]\n\t" \
  "s_waitcnt lgkmcnt(0)\n\t" \
  "v_mfma_f32_16x16x32_bf16 v[252:255], v[80:83], v[68:71], v[252:255]\n\t" \
  "v_mfma_f32_16x16x32_bf16 v[252:255], v[84:87], v[76:79], v[252:255]\n\t" \
  "v_mfma_f32_16x16x32_bf16 v[188:191], v[84:87], v[148:151], v[188:191]\n\t" \
  "v_mfma_f32_16x16x32_bf16 v[188:191], v[80:83], v[108:111], v[188:191]\n\t" \
  "s_barrier\n\t" \
  "v_mfma_f32_16x16x32_bf16 v[196:199], v[80:83], v[104:107], v[196:199]\n\t" \
  "ds_read_b64_tr_b16 v[96:97], v54 offset:34816\n\t" \
  "v_mfma_f32_16x16x32_bf16 v[196:199], v[84:87], v[64:67], v[196:199]\n\t" \
  "v_mov_b32 v50, 163344\n\t" \
  "ds_read_b64_tr_b16 v[98:99], v54 offset:35328\n\t" \
  "v_mfma_f32_16x16x32_bf16 v[204:207], v[84:87], v[116:119], v[204:207]\n\t" \
  "ds_read_b32 v50, v50 offset:24\n\t" \
  "ds_read_b128 v[136:139], v52 offset:2048\n\t" \
  "v_mfma_f32_16x16x32_bf16 v[204:207], v[80:83], v[112:115], v[204:207]\n\t" \
  "ds_read_b64_tr_b16 v[128:129], v54 offset:38912\n\t" \
  "v_mfma_f32_16x16x32_bf16 v[212:215], v[80:83], v[124:127], v[212:215]\n\t" \
  "ds_read_b64_tr_b16 v[130:131], v54 offset:39424\n\t" \
  "v_mfma_f32_16x16x32_bf16 v[212:215], v[84:87], v[120:123], v[212:215]\n\t" \
  "ds_read_b128 v[144:147], v53 offset:2048\n\t" \
  "v_mfma_f32_16x16x32_bf16 v[152:155], v[88:91], v[108:111], v[152:155]\n\t" \
  "ds_read_b128 v[72:75], v53 offset:4096\n\t" \
  "v_mfma_f32_16x16x32_bf16 v[152:155], v[132:135], v[148:151], v[152:155]\n\t" \
  "ds_read_b128 v[140:143], v52 offset:4096\n\t" \
  "v_mfma_f32_16x16x32_bf16 v[160:163], v[132:135], v[64:67], v[160:163]\n\t" \
  "ds_read_b128 v[68:71], v52 offset:6144\n\t" \
  "v_mfma_f32_16x16x32_bf16 v[160:163], v[88:91], v[104:107], v[160:163]\n\t" \
  "ds_read_b128 v[76:79], v53 offset:6144\n\t" \
  "v_mfma_f32_16x16x32_bf16 v[168:171], v[88:91], v[112:115], v[168:171]\n\t" \
  "ds_read_b128 v[80:83], v53 offset:8192\n\t" \
  "v_mfma_f32_16x16x32_bf16 v[168:171], v[132:135], v[116:119], v[168:171]\n\t" \
  "ds_read_b128 v[84:87], v52 offset:8192\n\t" \
  "v_mfma_f32_16x16x32_bf16 v[176:179], v[132:135], v[120:123], v[176:179]\n\t" \
  "s_waitcnt lgkmcnt(10)\n\t" \
  "v_readfirstlane_b32 s88, v50\n\t" \
  "s_mov_b32 s89, 0\n\t" \
  "ds_read_b64_tr_b16 v[132:133], v55 offset:34816\n\t" \
  "v_mfma_f32_16x16x32_bf16 v[176:179], v[88:91], v[124:127], v[176:179]\n\t" \
  "ds_read_b64_tr_b16 v[134:135], v55 offset:35328\n\t" \
  "v_mfma_f32_16x16x32_bf16 v[156:159], v[92:95], v[108:111], v[156:159]\n\t" \
  "s_xor_b64 %24, %24, s[88:89]\n\t" \
  "ds_read_b64_tr_b16 v[88:89], v55 offset:38912\n\t" \
  "v_mfma_f32_16x16x32_bf16 v[156:159], v[100:103], v[148:151], v[156:159]\n\t" \
  "s_xor_b64 %25, %25, s[88:89]\n\t" \
  "ds_read_b64_tr_b16 v[90:91], v55 offset:39424\n\t" \
  "v_mfma_f32_16x16x32_bf16 v[164:167], v[100:103], v[64:67], v[164:167]\n\t" \
  "s_xor_b64 %14, %14, s[88:89]\n\t" \
  "ds_read_b64_tr_b16 v[108:109], v54 offset:26624\n\t" \
  "v_mfma_f32_16x16x32_bf16 v[164:167], v[92:95], v[104:107], v[164:167]\n\t" \
  "s_xor_b64 %19, %19, s[88:89]\n\t" \
  "ds_read_b64_tr_b16 v[110:111], v54 offset:27136\n\t" \
  "v_mfma_f32_16x16x32_bf16 v[172:175], v[92:95], v[112:115], v[172:175]\n\t" \
  "s_xor_b64 %21, %21, s[88:89]\n\t" \
  "ds_read_b64_tr_b16 v[148:149], v54 offset:30720\n\t" \
  "v_mfma_f32_16x16x32_bf16 v[172:175], v[100:103], v[116:119], v[172:175]\n\t" \
  "s_xor_b64 s[92:93], s[92:93], s[88:89]\n\t" \
  "ds_read_b64_tr_b16 v[150:151], v54 offset:31232\n\t" \
  "v_mfma_f32_16x16x32_bf16 v[180:183], v[100:103], v[120:123], v[180:183]\n\t" \
  "ds_read_b128 v[64:67], v52 offset:0\n\t" \
  "v_mfma_f32_16x16x32_bf16 v[180:183], v[92:95], v[124:127], v[180:183]\n\t" \
  "ds_read_b128 v[104:107], v53 offset:0\n\t" \
  "s_waitcnt lgkmcnt(7)\n\t" \
  "v_mfma_f32_16x16x32_bf16 v[216:219], v[136:139], v[96:99], v[216:219]\n\t" \
  "ds_read_b64_tr_b16 v[112:113], v55 offset:26624\n\t" \
  "v_mfma_f32_16x16x32_bf16 v[216:219], v[144:147], v[128:131], v[216:219]\n\t" \
  "ds_read_b64_tr_b16 v[114:115], v55 offset:27136\n\t" \
  "v_mfma_f32_16x16x32_bf16 v[224:227], v[72:75], v[128:131], v[224:227]\n\t" \
  "ds_read_b64_tr_b16 v[116:117], v55 offset:30720\n\t" \
  "v_mfma_f32_16x16x32_bf16 v[224:227], v[140:143], v[96:99], v[224:227]\n\t" \
  "ds_read_b64_tr_b16 v[118:119], v55 offset:31232\n\t" \
  "v_mfma_f32_16x16x32_bf16 v[232:235], v[68:71], v[96:99], v[232:235]\n\t" \
  "ds_read_b64_tr_b16 v[100:101], v54 offset:18432\n\t" \
  "v_mfma_f32_16x16x32_bf16 v[232:235], v[76:79], v[128:131], v[232:235]\n\t" \
  "ds_read_b64_tr_b16 v[102:103], v54 offset:18944\n\t" \
  "v_mfma_f32_16x16x32_bf16 v[240:243], v[80:83], v[128:131], v[240:243]\n\t" \
  "ds_read_b64_tr_b16 v[120:121], v54 offset:22528\n\t" \
  "v_mfma_f32_16x16x32_bf16 v[240:243], v[84:87], v[96:99], v[240:243]\n\t" \
  "ds_read_b64_tr_b16 v[122:123], v54 offset:23040\n\t" \
  "v_mfma_f32_16x16x32_bf16 v[220:223], v[136:139], v[132:135], v[220:223]\n\t" \
  "ds_read_b64_tr_b16 v[92:93], v55 offset:18432\n\t" \
  "s_waitcnt lgkmcnt(7)\n\t" \
  "v_mfma_f32_16x16x32_bf16 v[220:223], v[144:147], v[88:91], v[220:223]\n\t" \
  "ds_read_b64_tr_b16 v[94:95], v55 offset:18944\n\t" \
  "v_mfma_f32_16x16x32_bf16 v[228:231], v[72:75], v[88:91], v[228:231]\n\t" \
  "ds_read_b64_tr_b16 v[124:125], v55 offset:22528\n\t" \
  "v_mfma_f32_16x16x32_bf16 v[228:231], v[140:143], v[132:135], v[228:231]\n\t" \
  "ds_read_b64_tr_b16 v[126:127], v55 offset:23040\n\t" \
  "v_mfma_f32_16x16x32_bf16 v[236:239], v[68:71], v[132:135], v[236:239]\n\t" \
  "v_mfma_f32_16x16x32_bf16 v[236:239], v[76:79], v[88:91], v[236:239]\n\t" \
  "v_mfma_f32_16x16x32_bf16 v[244:247], v[80:83], v[88:91], v[244:247]\n\t" \
  "v_mfma_f32_16x16x32_bf16 v[244:247], v[84:87], v[132:135], v[244:247]\n\t" \
  "v_mfma_f32_16x16x32_bf16 v[184:187], v[108:111], v[136:139], v[184:187]\n\t" \
  "v_mfma_f32_16x16x32_bf16 v[184:187], v[148:151], v[144:147], v[184:187]\n\t" \
  "v_mfma_f32_16x16x32_bf16 v[192:195], v[148:151], v[72:75], v[192:195]\n\t" \
  "v_mfma_f32_16x16x32_bf16 v[192:195], v[108:111], v[140:143], v[192:195]\n\t" \
  "v_mfma_f32_16x16x32_bf16 v[200:203], v[108:111], v[68:71], v[200:203]\n\t" \
  "global_store_dwordx4 v2, v[42:45], %24 offset:0 sc1\n\t" \
  "v_mfma_f32_16x16x32_bf16 v[200:203], v[148:151], v[76:79], v[200:203]\n\t" \
  "s_nop 0\n\t" \
  "v_mfma_f32_16x16x32_bf16 v[42:45], v[26:29], v[18:21], 0\n\t" \
  "v_mfma_f32_16x16x32_bf16 v[42:45], v[30:33], v[22:25], v[42:45]\n\t" \
  "v_mfma_f32_16x16x32_bf16 v[46:49], v[30:33], v[18:21], 0\n\t" \
  "v_mfma_f32_16x16x32_bf16 v[46:49], v[34:37], v[22:25], v[46:49]\n\t" \
  "v_bfe_u32 v30, v4, 9, 1\n\t" \
  "v_mad_i32_i24 v30, v30, -16, v0\n\t" \
  "v_mfma_f32_16x16x32_bf16 v[208:211], v[148:151], v[80:83], v[208:211]\n\t" \
  "ds_read_b128 v[26:29], v0 offset:1584\n\t" \
  "ds_read_b128 v[30:33], v30 offset:1072\n\t" \
  "v_mfma_f32_16x16x32_bf16 v[208:211], v[108:111], v[84:87], v[208:211]\n\t" \
  "ds_read_b128 v[34:37], v0 offset:544\n\t" \
  "v_perm_b32 v18, v12, v12, v14\n\t" \
  "v_mfma_f32_16x16x32_bf16 v[248:251], v[108:111], v[64:67], v[248:251]\n\t" \
  "v_perm_b32 v19, v12, v12, v15\n\t" \
  "v_perm_b32 v20, v12, v12, v16\n\t" \
  "v_mfma_f32_16x16x32_bf16 v[248:251], v[148:151], v[104:107], v[248:251]\n\t" \
  "v_perm_b32 v21, v12, v12, v17\n\t" \
  "v_perm_b32 v22, v13, v13, v14\n\t" \
  "v_mfma_f32_16x16x32_bf16 v[252:255], v[112:115], v[64:67], v[252:255]\n\t" \
  "v_perm_b32 v23, v13, v13, v15\n\t" \
  "v_perm_b32 v24, v13, v13, v16\n\t" \
  "s_waitcnt lgkmcnt(3)\n\t" \
  "v_mfma_f32_16x16x32_bf16 v[252:255], v[116:119], v[104:107], v[252:255]\n\t" \
  "v_perm_b32 v25, v13, v13, v17\n\t" \
  "v_lshlrev_b32 v50, 16, v38\n\t" \
  "v_mfma_f32_16x16x32_bf16 v[188:191], v[116:119], v[144:147], v[188:191]\n\t" \
  "v_lshlrev_b32 v51, 16, v40\n\t" \
  "v_mul_f32 v42, v42, v50\n\t" \
  "v_mfma_f32_16x16x32_bf16 v[188:191], v[112:115], v[136:139], v[188:191]\n\t" \
  "v_mul_f32 v46, v46, v51\n\t" \
  "v_cvt_pk_bf16_f32 v42, v42, v46\n\t" \
  "s_waitcnt lgkmcnt(0)\n\t" \
  "s_barrier\n\t" \
  "v_mfma_f32_16x16x32_bf16 v[196:199], v[112:115], v[140:143], v[196:199]\n\t" \
  "ds_read_b64_tr_b16 v[128:129], v58 offset:34816\n\t" \
  "v_and_b32 v50, 0xffff0000, v38\n\t" \
  "v_mfma_f32_16x16x32_bf16 v[196:199], v[116:119], v[72:75], v[196:199]\n\t" \
  "ds_read_b64_tr_b16 v[130:131], v58 offset:35328\n\t" \
  "v_and_b32 v51, 0xffff0000, v40\n\t" \
  "v_mfma_f32_16x16x32_bf16 v[204:207], v[116:119], v[76:79], v[204:207]\n\t" \
  "ds_read_b128 v[96:99], v56 offset:2048\n\t" \
  "v_mul_f32 v43, v43, v50\n\t" \
  "v_mfma_f32_16x16x32_bf16 v[204:207], v[112:115], v[68:71], v[204:207]\n\t" \
  "ds_read_b64_tr_b16 v[88:89], v58 offset:38912\n\t" \
  "v_mul_f32 v47, v47, v51\n\t" \
  "v_mfma_f32_16x16x32_bf16 v[212:215], v[112:115], v[84:87], v[212:215]\n\t" \
  "ds_read_b64_tr_b16 v[90:91], v58 offset:39424\n\t" \
  "v_cvt_pk_bf16_f32 v43, v43, v47\n\t" \
  "v_mfma_f32_16x16x32_bf16 v[212:215], v[116:119], v[80:83], v[212:215]\n\t" \
  "ds_read_b128 v[132:135], v57 offset:2048\n\t" \
  "v_lshlrev_b32 v50, 16, v39\n\t" \
  "v_mfma_f32_16x16x32_bf16 v[152:155], v[100:103], v[136:139], v[152:155]\n\t" \
  "ds_read_b128 v[108:111], v57 offset:4096\n\t" \
  "v_lshlrev_b32 v51, 16, v41\n\t" \
  "v_mfma_f32_16x16x32_bf16 v[152:155], v[120:123], v[144:147], v[152:155]\n\t" \
  "ds_read_b128 v[148:151], v56 offset:4096\n\t" \
  "v_mul_f32 v44, v44, v50\n\t" \
  "v_mfma_f32_16x16x32_bf16 v[160:163], v[120:123], v[72:75], v[160:163]\n\t" \
  "ds_read_b128 v[64:67], v56 offset:6144\n\t" \
  "v_mul_f32 v48, v48, v51\n\t" \
  "v_mfma_f32_16x16x32_bf16 v[160:163], v[100:103], v[140:143], v[160:163]\n\t" \
  "ds_read_b128 v[104:107], v57 offset:6144\n\t" \
  "v_cvt_pk_bf16_f32 v44, v44, v48\n\t" \
  "v_mfma_f32_16x16x32_bf16 v[168:171], v[100:103], v[68:71], v[168:171]\n\t" \
  "ds_read_b128 v[112:115], v57 offset:8192\n\t" \
  "v_and_b32 v50, 0xffff0000, v39\n\t" \
  "v_mfma_f32_16x16x32_bf16 v[168:171], v[120:123], v[76:79], v[168:171]\n\t" \
  "ds_read_b128 v[116:119], v56 offset:8192\n\t" \
  "v_and_b32 v51, 0xffff0000, v41\n\t" \
  "v_mfma_f32_16x16x32_bf16 v[176:179], v[120:123], v[80:83], v[176:179]\n\t" \
  "ds_read_b64_tr_b16 v[120:121], v59 offset:34816\n\t" \
  "v_mul_f32 v45, v45, v50\n\t" \
  "v_mfma_f32_16x16x32_bf16 v[176:179], v[100:103], v[84:87], v[176:179]\n\t" \
  "ds_read_b64_tr_b16 v[122:123], v59 offset:35328\n\t" \
  "v_mul_f32 v49, v49, v51\n\t" \
  "v_mfma_f32_16x16x32_bf16 v[156:159], v[92:95], v[136:139], v[156:159]\n\t" \
  "ds_read_b64_tr_b16 v[100:101], v59 offset:38912\n\t" \
  "ds_read_b64_tr_b16 v[38:39], v1 offset:544\n\t" \
  "v_mfma_f32_16x16x32_bf16 v[156:159], v[124:127], v[144:147], v[156:159]\n\t" \
  "ds_read_b64_tr_b16 v[102:103], v59 offset:39424\n\t" \
  "ds_read_b64_tr_b16 v[40:41], v1 offset:32\n\t" \
  "v_mfma_f32_16x16x32_bf16 v[164:167], v[124:127], v[72:75], v[164:167]\n\t" \
  "ds_read_b64_tr_b16 v[136:137], v58 offset:26624\n\t" \
  "v_cvt_pk_bf16_f32 v45, v45, v49\n\t" \
  "v_mfma_f32_16x16x32_bf16 v[164:167], v[92:95], v[140:143], v[164:167]\n\t" \
  "ds_read_b64_tr_b16 v[138:139], v58 offset:27136\n\t" \
  "v_mfma_f32_16x16x32_bf16 v[172:175], v[92:95], v[68:71], v[172:175]\n\t" \
  "ds_read_b64_tr_b16 v[144:145], v58 offset:30720\n\t" \
  "v_mfma_f32_16x16x32_bf16 v[172:175], v[124:127], v[76:79], v[172:175]\n\t" \
  "ds_read_b64_tr_b16 v[146:147], v58 offset:31232\n\t" \
  "v_mfma_f32_16x16x32_bf16 v[180:183], v[124:127], v[80:83], v[180:183]\n\t" \
  "ds_read_b128 v[72:75], v56 offset:0\n\t" \
  "v_mfma_f32_16x16x32_bf16 v[180:183], v[92:95], v[84:87], v[180:183]\n\t" \
  "ds_read_b128 v[140:143], v57 offset:0\n\t" \
  "s_waitcnt lgkmcnt(8)\n\t" \
  "v_mfma_f32_16x16x32_bf16 v[216:219], v[96:99], v[128:131], v[216:219]\n\t" \
  "ds_read_b64_tr_b16 v[68:69], v59 offset:26624\n\t" \
  "v_mfma_f32_16x16x32_bf16 v[216:219], v[132:135], v[88:91], v[216:219]\n\t" \
  "ds_read_b64_tr_b16 v[70:71], v59 offset:27136\n\t" \
  "v_mfma_f32_16x16x32_bf16 v[224:227], v[108:111], v[88:91], v[224:227]\n\t" \
  "ds_read_b64_tr_b16 v[76:77], v59 offset:30720\n\t" \
  "v_mfma_f32_16x16x32_bf16 v[224:227], v[148:151], v[128:131], v[224:227]\n\t" \
  "ds_read_b64_tr_b16 v[78:79], v59 offset:31232\n\t" \
  "v_mfma_f32_16x16x32_bf16 v[232:235], v[64:67], v[128:131], v[232:235]\n\t" \
  "ds_read_b64_tr_b16 v[80:81], v58 offset:18432\n\t" \
  "v_mfma_f32_16x16x32_bf16 v[232:235], v[104:107], v[88:91], v[232:235]\n\t" \
  "ds_read_b64_tr_b16 v[82:83], v58 offset:18944\n\t" \
  "v_mfma_f32_16x16x32_bf16 v[240:243], v[112:115], v[88:91], v[240:243]\n\t" \
  "ds_read_b64_tr_b16 v[124:125], v58 offset:22528\n\t" \
  "v_mfma_f32_16x16x32_bf16 v[240:243], v[116:119], v[128:131], v[240:243]\n\t" \
  "ds_read_b64_tr_b16 v[126:127], v58 offset:23040\n\t" \
  "v_mfma_f32_16x16x32_bf16 v[220:223], v[96:99], v[120:123], v[220:223]\n\t" \
  "ds_read_b64_tr_b16 v[84:85], v59 offset:18432\n\t" \
  "s_waitcnt lgkmcnt(7)\n\t" \
  "v_mfma_f32_16x16x32_bf16 v[220:223], v[132:135], v[100:103], v[220:223]\n\t" \
  "ds_read_b64_tr_b16 v[86:87], v59 offset:18944\n\t" \
  "v_mfma_f32_16x16x32_bf16 v[228:231], v[108:111], v[100:103], v[228:231]\n\t" \
  "ds_read_b64_tr_b16 v[92:93], v59 offset:22528\n\t" \
  "v_mfma_f32_16x16x32_bf16 v[228:231], v[148:151], v[120:123], v[228:231]\n\t" \
  "ds_read_b64_tr_b16 v[94:95], v59 offset:23040\n\t" \
  "v_mfma_f32_16x16x32_bf16 v[236:239], v[64:67], v[120:123], v[236:239]\n\t" \
  "v_mfma_f32_16x16x32_bf16 v[236:239], v[104:107], v[100:103], v[236:239]\n\t" \
  "v_mfma_f32_16x16x32_bf16 v[244:247], v[112:115], v[100:103], v[244:247]\n\t" \
  "v_mfma_f32_16x16x32_bf16 v[244:247], v[116:119], v[120:123], v[244:247]\n\t" \
  "v_mfma_f32_16x16x32_bf16 v[184:187], v[136:139], v[96:99], v[184:187]\n\t" \
  "v_mfma_f32_16x16x32_bf16 v[184:187], v[144:147], v[132:135], v[184:187]\n\t" \
  "v_mfma_f32_16x16x32_bf16 v[192:195], v[144:147], v[108:111], v[192:195]\n\t" \
  "v_mfma_f32_16x16x32_bf16 v[192:195], v[136:139], v[148:151], v[192:195]\n\t" \
  "v_mfma_f32_16x16x32_bf16 v[200:203], v[136:139], v[64:67], v[200:203]\n\t" \
  "global_store_dwordx4 v2, v[42:45], %24 offset:1024 sc1\n\t" \
  "v_add_u32 v2, 0x20000, v2\n\t" \
  "v_mfma_f32_16x16x32_bf16 v[200:203], v[144:147], v[104:107], v[200:203]\n\t" \
  "v_mfma_f32_16x16x32_bf16 v[208:211], v[144:147], v[112:115], v[208:211]\n\t" \
  "v_mfma_f32_16x16x32_bf16 v[42:45], v[26:29], v[18:21], 0\n\t" \
  "v_mfma_f32_16x16x32_bf16 v[42:45], v[30:33], v[22:25], v[42:45]\n\t" \
  "v_mfma_f32_16x16x32_bf16 v[46:49], v[30:33], v[18:21], 0\n\t" \
  "v_mfma_f32_16x16x32_bf16 v[46:49], v[34:37], v[22:25], v[46:49]\n\t" \
  "ds_read_b128 v[26:29], v0 offset:1840\n\t" \
  "ds_read_b128 v[30:33], v0 offset:1328\n\t" \
  "v_mfma_f32_16x16x32_bf16 v[208:211], v[136:139], v[116:119], v[208:211]\n\t" \
  "ds_read_b128 v[34:37], v0 offset:800\n\t" \
  "v_mfma_f32_16x16x32_bf16 v[248:251], v[136:139], v[72:75], v[248:251]\n\t" \
  "v_mfma_f32_16x16x32_bf16 v[248:251], v[144:147], v[140:143], v[248:251]\n\t" \
  "v_lshlrev_b32 v50, 16, v38\n\t" \
  "v_lshlrev_b32 v51, 16, v40\n\t" \
  "v_mfma_f32_16x16x32_bf16 v[252:255], v[68:71], v[72:75], v[252:255]\n\t" \
  "v_mul_f32 v42, v42, v50\n\t" \
  "v_mul_f32 v46, v46, v51\n\t" \
  "s_waitcnt lgkmcnt(3)\n\t" \
  "v_mfma_f32_16x16x32_bf16 v[252:255], v[76:79], v[140:143], v[252:255]\n\t" \
  "v_cvt_pk_bf16_f32 v42, v42, v46\n\t" \
  "v_and_b32 v50, 0xffff0000, v38\n\t" \
  "v_mfma_f32_16x16x32_bf16 v[188:191], v[76:79], v[132:135], v[188:191]\n\t" \
  "v_and_b32 v51, 0xffff0000, v40\n\t" \
  "v_mul_f32 v43, v43, v50\n\t" \
  "v_mfma_f32_16x16x32_bf16 v[188:191], v[68:71], v[96:99], v[188:191]\n\t" \
  "v_mul_f32 v47, v47, v51\n\t" \
  "v_cvt_pk_bf16_f32 v43, v43, v47\n\t" \
  "s_waitcnt lgkmcnt(0)\n\t" \
  "s_barrier\n\t" \
  "v_mfma_f32_16x16x32_bf16 v[196:199], v[68:71], v[148:151], v[196:199]\n\t" \
  "ds_read_b64_tr_b16 v[88:89], v62 offset:34816\n\t" \
  "v_lshlrev_b32 v50, 16, v39\n\t" \
  "v_mfma_f32_16x16x32_bf16 v[196:199], v[76:79], v[108:111], v[196:199]\n\t" \
  "ds_read_b64_tr_b16 v[90:91], v62 offset:35328\n\t" \
  "v_lshlrev_b32 v51, 16, v41\n\t" \
  "v_mfma_f32_16x16x32_bf16 v[204:207], v[76:79], v[104:107], v[204:207]\n\t" \
  "ds_read_b128 v[128:131], v60 offset:2048\n\t" \
  "v_mul_f32 v44, v44, v50\n\t" \
  "v_mfma_f32_16x16x32_bf16 v[204:207], v[68:71], v[64:67], v[204:207]\n\t" \
  "ds_read_b64_tr_b16 v[100:101], v62 offset:38912\n\t" \
  "v_mul_f32 v48, v48, v51\n\t" \
  "v_mfma_f32_16x16x32_bf16 v[212:215], v[68:71], v[116:119], v[212:215]\n\t" \
  "ds_read_b64_tr_b16 v[102:103], v62 offset:39424\n\t" \
  "v_cvt_pk_bf16_f32 v44, v44, v48\n\t" \
  "v_mfma_f32_16x16x32_bf16 v[212:215], v[76:79], v[112:115], v[212:215]\n\t" \
  "ds_read_b128 v[120:123], v61 offset:2048\n\t" \
  "v_and_b32 v50, 0xffff0000, v39\n\t" \
  "v_mfma_f32_16x16x32_bf16 v[152:155], v[80:83], v[96:99], v[152:155]\n\t" \
  "ds_read_b128 v[136:139], v61 offset:4096\n\t" \
  "v_and_b32 v51, 0xffff0000, v41\n\t" \
  "v_mfma_f32_16x16x32_bf16 v[152:155], v[124:127], v[132:135], v[152:155]\n\t" \
  "ds_read_b128 v[144:147], v60 offset:4096\n\t" \
  "v_mul_f32 v45, v45, v50\n\t" \
  "v_mfma_f32_16x16x32_bf16 v[160:163], v[124:127], v[108:111], v[160:163]\n\t" \
  "ds_read_b128 v[72:75], v60 offset:6144\n\t" \
  "v_mul_f32 v49, v49, v51\n\t" \
  "v_mfma_f32_16x16x32_bf16 v[160:163], v[80:83], v[148:151], v[160:163]\n\t" \
  "ds_read_b128 v[140:143], v61 offset:6144\n\t" \
  "ds_read_b64_tr_b16 v[38:39], v1 offset:800\n\t" \
  "v_mfma_f32_16x16x32_bf16 v[168:171], v[80:83], v[64:67], v[168:171]\n\t" \
  "ds_read_b128 v[68:71], v61 offset:8192\n\t" \
  "ds_read_b64_tr_b16 v[40:41], v1 offset:288\n\t" \
  "v_mfma_f32_16x16x32_bf16 v[168:171], v[124:127], v[104:107], v[168:171]\n\t" \
  "ds_read_b128 v[76:79], v60 offset:8192\n\t" \
  "v_cvt_pk_bf16_f32 v45, v45, v49\n\t" \
  "v_mfma_f32_16x16x32_bf16 v[176:179], v[124:127], v[112:115], v[176:179]\n\t" \
  "ds_read_b64_tr_b16 v[124:125], v63 offset:34816\n\t" \
  "v_mfma_f32_16x16x32_bf16 v[176:179], v[80:83], v[116:119], v[176:179]\n\t" \
  "ds_read_b64_tr_b16 v[126:127], v63 offset:35328\n\t" \
  "v_mfma_f32_16x16x32_bf16 v[156:159], v[84:87], v[96:99], v[156:159]\n\t" \
  "ds_read_b64_tr_b16 v[80:81], v63 offset:38912\n\t" \
  "v_mfma_f32_16x16x32_bf16 v[156:159], v[92:95], v[132:135], v[156:159]\n\t" \
  "ds_read_b64_tr_b16 v[82:83], v63 offset:39424\n\t" \
  "v_mfma_f32_16x16x32_bf16 v[164:167], v[92:95], v[108:111], v[164:167]\n\t" \
  "ds_read_b64_tr_b16 v[96:97], v62 offset:26624\n\t" \
  "v_mfma_f32_16x16x32_bf16 v[164:167], v[84:87], v[148:151], v[164:167]\n\t" \
  "ds_read_b64_tr_b16 v[98:99], v62 offset:27136\n\t" \
  "v_mfma_f32_16x16x32_bf16 v[172:175], v[84:87], v[64:67], v[172:175]\n\t" \
  "ds_read_b64_tr_b16 v[132:133], v62 offset:30720\n\t" \
  "v_mfma_f32_16x16x32_bf16 v[172:175], v[92:95], v[104:107], v[172:175]\n\t" \
  "ds_read_b64_tr_b16 v[134:135], v62 offset:31232\n\t" \
  "v_mfma_f32_16x16x32_bf16 v[180:183], v[92:95], v[112:115], v[180:183]\n\t" \
  "ds_read_b128 v[108:111], v60 offset:0\n\t" \
  "v_mfma_f32_16x16x32_bf16 v[180:183], v[84:87], v[116:119], v[180:183]\n\t" \
  "ds_read_b128 v[148:151], v61 offset:0\n\t" \
  "s_waitcnt lgkmcnt(7)\n\t" \
  "v_mfma_f32_16x16x32_bf16 v[216:219], v[128:131], v[88:91], v[216:219]\n\t" \
  "ds_read_b64_tr_b16 v[64:65], v63 offset:26624\n\t" \
  "v_mfma_f32_16x16x32_bf16 v[216:219], v[120:123], v[100:103], v[216:219]\n\t" \
  "ds_read_b64_tr_b16 v[66:67], v63 offset:27136\n\t" \
  "v_mfma_f32_16x16x32_bf16 v[224:227], v[136:139], v[100:103], v[224:227]\n\t" \
  "ds_read_b64_tr_b16 v[104:105], v63 offset:30720\n\t" \
  "v_mfma_f32_16x16x32_bf16 v[224:227], v[144:147], v[88:91], v[224:227]\n\t" \
  "ds_read_b64_tr_b16 v[106:107], v63 offset:31232\n\t" \
  "v_mfma_f32_16x16x32_bf16 v[232:235], v[72:75], v[88:91], v[232:235]\n\t" \
  "ds_read_b64_tr_b16 v[92:93], v62 offset:18432\n\t" \
  "v_mfma_f32_16x16x32_bf16 v[232:235], v[140:143], v[100:103], v[232:235]\n\t" \
  "ds_read_b64_tr_b16 v[94:95], v62 offset:18944\n\t" \
  "v_mfma_f32_16x16x32_bf16 v[240:243], v[68:71], v[100:103], v[240:243]\n\t" \
  "ds_read_b64_tr_b16 v[112:113], v62 offset:22528\n\t" \
  "v_mfma_f32_16x16x32_bf16 v[240:243], v[76:79], v[88:91], v[240:243]\n\t" \
  "ds_read_b64_tr_b16 v[114:115], v62 offset:23040\n\t" \
  "v_mfma_f32_16x16x32_bf16 v[220:223], v[128:131], v[124:127], v[220:223]\n\t" \
  "ds_read_b64_tr_b16 v[84:85], v63 offset:18432\n\t" \
  "s_waitcnt lgkmcnt(7)\n\t" \
  "v_mfma_f32_16x16x32_bf16 v[220:223], v[120:123], v[80:83], v[220:223]\n\t" \
  "ds_read_b64_tr_b16 v[86:87], v63 offset:18944\n\t" \
  "v_mfma_f32_16x16x32_bf16 v[228:231], v[136:139], v[80:83], v[228:231]\n\t" \
  "ds_read_b64_tr_b16 v[116:117], v63 offset:22528\n\t" \
  "v_mfma_f32_16x16x32_bf16 v[228:231], v[144:147], v[124:127], v[228:231]\n\t" \
  "ds_read_b64_tr_b16 v[118:119], v63 offset:23040\n\t" \
  "v_mfma_f32_16x16x32_bf16 v[236:239], v[72:75], v[124:127], v[236:239]\n\t" \
  "v_mfma_f32_16x16x32_bf16 v[236:239], v[140:143], v[80:83], v[236:239]\n\t" \
  "v_mfma_f32_16x16x32_bf16 v[244:247], v[68:71], v[80:83], v[244:247]\n\t" \
  "v_mfma_f32_16x16x32_bf16 v[244:247], v[76:79], v[124:127], v[244:247]\n\t" \
  "v_mfma_f32_16x16x32_bf16 v[184:187], v[96:99], v[128:131], v[184:187]\n\t" \
  "v_mfma_f32_16x16x32_bf16 v[184:187], v[132:135], v[120:123], v[184:187]\n\t" \
  "v_mfma_f32_16x16x32_bf16 v[192:195], v[132:135], v[136:139], v[192:195]\n\t" \
  "v_mfma_f32_16x16x32_bf16 v[192:195], v[96:99], v[144:147], v[192:195]\n\t" \
  "v_mfma_f32_16x16x32_bf16 v[200:203], v[96:99], v[72:75], v[200:203]\n\t" \
  "global_store_dwordx4 v2, v[42:45], %24 offset:0 sc1\n\t" \
  "v_mfma_f32_16x16x32_bf16 v[200:203], v[132:135], v[140:143], v[200:203]\n\t" \
  "s_nop 0\n\t" \
  "v_mfma_f32_16x16x32_bf16 v[42:45], v[26:29], v[18:21], 0\n\t" \
  "v_mfma_f32_16x16x32_bf16 v[42:45], v[30:33], v[22:25], v[42:45]\n\t" \
  "v_mfma_f32_16x16x32_bf16 v[46:49], v[30:33], v[18:21], 0\n\t" \
  "v_mfma_f32_16x16x32_bf16 v[46:49], v[34:37], v[22:25], v[46:49]\n\t" \
  "v_mfma_f32_16x16x32_bf16 v[208:211], v[132:135], v[68:71], v[208:211]\n\t" \
  "v_mfma_f32_16x16x32_bf16 v[208:211], v[96:99], v[76:79], v[208:211]\n\t" \
  "v_lshlrev_b32 v50, 16, v38\n\t" \
  "v_lshlrev_b32 v51, 16, v40\n\t" \
  "v_mfma_f32_16x16x32_bf16 v[248:251], v[96:99], v[108:111], v[248:251]\n\t" \
  "s_nop 0\n\t" \
  "v_mul_f32 v42, v42, v50\n\t" \
  "s_nop 0\n\t" \
  "v_mul_f32 v46, v46, v51\n\t" \
  "v_mfma_f32_16x16x32_bf16 v[248:251], v[132:135], v[148:151], v[248:251]\n\t" \
  "v_cvt_pk_bf16_f32 v42, v42, v46\n\t" \
  "v_and_b32 v50, 0xffff0000, v38\n\t" \
  "v_mfma_f32_16x16x32_bf16 v[252:255], v[64:67], v[108:111], v[252:255]\n\t" \
  "v_and_b32 v51, 0xffff0000, v40\n\t" \
  "v_mul_f32 v43, v43, v50\n\t" \
  "s_waitcnt lgkmcnt(0)\n\t" \
  "v_mfma_f32_16x16x32_bf16 v[252:255], v[104:107], v[148:151], v[252:255]\n\t" \
  "v_mul_f32 v47, v47, v51\n\t" \
  "v_cvt_pk_bf16_f32 v43, v43, v47\n\t" \
  "v_mfma_f32_16x16x32_bf16 v[188:191], v[104:107], v[120:123], v[188:191]\n\t" \
  "v_lshlrev_b32 v50, 16, v39\n\t" \
  "v_lshlrev_b32 v51, 16, v41\n\t" \
  "v_mfma_f32_16x16x32_bf16 v[188:191], v[64:67], v[128:131], v[188:191]\n\t" \
  "v_mul_f32 v44, v44, v50\n\t" \
  "v_mul_f32 v48, v48, v51\n\t" \
  "s_barrier\n\t" \
  "v_mfma_f32_16x16x32_bf16 v[196:199], v[64:67], v[144:147], v[196:199]\n\t" \
  "ds_read_b64_tr_b16 v[100:101], v54 offset:34816\n\t" \
  "v_cvt_pk_bf16_f32 v44, v44, v48\n\t" \
  "v_mfma_f32_16x16x32_bf16 v[196:199], v[104:107], v[136:139], v[196:199]\n\t" \
  "ds_read_b64_tr_b16 v[102:103], v54 offset:35328\n\t" \
  "v_and_b32 v50, 0xffff0000, v39\n\t" \
  "v_mfma_f32_16x16x32_bf16 v[204:207], v[104:107], v[140:143], v[204:207]\n\t" \
  "ds_read_b128 v[88:91], v52 offset:2048\n\t" \
  "v_and_b32 v51, 0xffff0000, v41\n\t" \
  "v_mfma_f32_16x16x32_bf16 v[204:207], v[64:67], v[72:75], v[204:207]\n\t" \
  "ds_read_b64_tr_b16 v[80:81], v54 offset:38912\n\t" \
  "v_mul_f32 v45, v45, v50\n\t" \
  "v_mfma_f32_16x16x32_bf16 v[212:215], v[64:67], v[76:79], v[212:215]\n\t" \
  "ds_read_b64_tr_b16 v[82:83], v54 offset:39424\n\t" \
  "v_mul_f32 v49, v49, v51\n\t" \
  "v_mfma_f32_16x16x32_bf16 v[212:215], v[104:107], v[68:71], v[212:215]\n\t" \
  "ds_read_b128 v[124:127], v53 offset:2048\n\t" \
  "v_cvt_pk_bf16_f32 v45, v45, v49\n\t" \
  "v_mfma_f32_16x16x32_bf16 v[152:155], v[92:95], v[128:131], v[152:155]\n\t" \
  "ds_read_b128 v[96:99], v53 offset:4096\n\t" \
  "v_mfma_f32_16x16x32_bf16 v[152:155], v[112:115], v[120:123], v[152:155]\n\t" \
  "ds_read_b128 v[132:135], v52 offset:4096\n\t" \
  "v_mfma_f32_16x16x32_bf16 v[160:163], v[112:115], v[136:139], v[160:163]\n\t" \
  "ds_read_b128 v[108:111], v52 offset:6144\n\t" \
  "504:\n\t" \
  "s_cbranch_execz 508f\n\t" \
  "v_mfma_f32_16x16x32_bf16 v[160:163], v[92:95], v[144:147], v[160:163]\n\t" \
  "ds_read_b128 v[148:151], v53 offset:6144\n\t" \
  "v_mfma_f32_16x16x32_bf16 v[168:171], v[92:95], v[72:75], v[168:171]\n\t" \
  "ds_read_b128 v[64:67], v53 offset:8192\n\t" \
  "v_mfma_f32_16x16x32_bf16 v[168:171], v[112:115], v[140:143], v[168:171]\n\t" \
  "ds_read_b128 v[104:107], v52 offset:8192\n\t" \
  "v_mfma_f32_16x16x32_bf16 v[176:179], v[112:115], v[68:71], v[176:179]\n\t" \
  "ds_read_b64_tr_b16 v[112:113], v55 offset:34816\n\t" \
  "v_mfma_f32_16x16x32_bf16 v[176:179], v[92:95], v[76:79], v[176:179]\n\t" \
  "ds_read_b64_tr_b16 v[114:115], v55 offset:35328\n\t" \
  "v_mfma_f32_16x16x32_bf16 v[156:159], v[84:87], v[128:131], v[156:159]\n\t" \
  "ds_read_b64_tr_b16 v[92:93], v55 offset:38912\n\t" \
  "v_mfma_f32_16x16x32_bf16 v[156:159], v[116:119], v[120:123], v[156:159]\n\t" \
  "ds_read_b64_tr_b16 v[94:95], v55 offset:39424\n\t" \
  "v_mfma_f32_16x16x32_bf16 v[164:167], v[116:119], v[136:139], v[164:167]\n\t" \
  "ds_read_b64_tr_b16 v[128:129], v54 offset:26624\n\t" \
  "v_mfma_f32_16x16x32_bf16 v[164:167], v[84:87], v[144:147], v[164:167]\n\t" \
  "ds_read_b64_tr_b16 v[130:131], v54 offset:27136\n\t" \
  "v_mfma_f32_16x16x32_bf16 v[172:175], v[84:87], v[72:75], v[172:175]\n\t" \
  "ds_read_b64_tr_b16 v[120:121], v54 offset:30720\n\t" \
  "v_mfma_f32_16x16x32_bf16 v[172:175], v[116:119], v[140:143], v[172:175]\n\t" \
  "ds_read_b64_tr_b16 v[122:123], v54 offset:31232\n\t" \
  "v_mfma_f32_16x16x32_bf16 v[180:183], v[116:119], v[68:71], v[180:183]\n\t" \
  "ds_read_b128 v[136:139], v52 offset:0\n\t" \
  "v_mfma_f32_16x16x32_bf16 v[180:183], v[84:87], v[76:79], v[180:183]\n\t" \
  "ds_read_b128 v[144:147], v53 offset:0\n\t" \
  "s_waitcnt lgkmcnt(7)\n\t" \
  "v_mfma_f32_16x16x32_bf16 v[216:219], v[88:91], v[100:103], v[216:219]\n\t" \
  "ds_read_b64_tr_b16 v[72:73], v55 offset:26624\n\t" \
  "v_mfma_f32_16x16x32_bf16 v[216:219], v[124:127], v[80:83], v[216:219]\n\t" \
  "ds_read_b64_tr_b16 v[74:75], v55 offset:27136\n\t" \
  "v_mfma_f32_16x16x32_bf16 v[224:227], v[96:99], v[80:83], v[224:227]\n\t" \
  "ds_read_b64_tr_b16 v[140:141], v55 offset:30720\n\t" \
  "v_mfma_f32_16x16x32_bf16 v[224:227], v[132:135], v[100:103], v[224:227]\n\t" \
  "ds_read_b64_tr_b16 v[142:143], v55 offset:31232\n\t" \
  "v_mfma_f32_16x16x32_bf16 v[232:235], v[108:111], v[100:103], v[232:235]\n\t" \
  "ds_read_b64_tr_b16 v[68:69], v54 offset:18432\n\t" \
  "v_mfma_f32_16x16x32_bf16 v[232:235], v[148:151], v[80:83], v[232:235]\n\t" \
  "ds_read_b64_tr_b16 v[70:71], v54 offset:18944\n\t" \
  "v_mfma_f32_16x16x32_bf16 v[240:243], v[64:67], v[80:83], v[240:243]\n\t" \
  "ds_read_b64_tr_b16 v[116:117], v54 offset:22528\n\t" \
  "v_mfma_f32_16x16x32_bf16 v[240:243], v[104:107], v[100:103], v[240:243]\n\t" \
  "ds_read_b64_tr_b16 v[118:119], v54 offset:23040\n\t" \
  "v_mfma_f32_16x16x32_bf16 v[220:223], v[88:91], v[112:115], v[220:223]\n\t" \
  "ds_read_b64_tr_b16 v[76:77], v55 offset:18432\n\t" \
  "s_waitcnt lgkmcnt(7)\n\t" \
  "v_mfma_f32_16x16x32_bf16 v[220:223], v[124:127], v[92:95], v[220:223]\n\t" \
  "ds_read_b64_tr_b16 v[78:79], v55 offset:18944\n\t" \
  "v_mfma_f32_16x16x32_bf16 v[228:231], v[96:99], v[92:95], v[228:231]\n\t" \
  "ds_read_b64_tr_b16 v[84:85], v55 offset:22528\n\t" \
  "v_mfma_f32_16x16x32_bf16 v[228:231], v[132:135], v[112:115], v[228:231]\n\t" \
  "ds_read_b64_tr_b16 v[86:87], v55 offset:23040\n\t" \
  "v_mfma_f32_16x16x32_bf16 v[236:239], v[108:111], v[112:115], v[236:239]\n\t" \
  "v_mfma_f32_16x16x32_bf16 v[236:239], v[148:151], v[92:95], v[236:239]\n\t" \
  "v_mfma_f32_16x16x32_bf16 v[244:247], v[64:67], v[92:95], v[244:247]\n\t" \
  "v_mfma_f32_16x16x32_bf16 v[244:247], v[104:107], v[112:115], v[244:247]\n\t" \
  "v_mfma_f32_16x16x32_bf16 v[184:187], v[128:131], v[88:91], v[184:187]\n\t" \
  "v_mfma_f32_16x16x32_bf16 v[184:187], v[120:123], v[124:127], v[184:187]\n\t" \
  "v_mfma_f32_16x16x32_bf16 v[192:195], v[120:123], v[96:99], v[192:195]\n\t" \
  "v_mfma_f32_16x16x32_bf16 v[192:195], v[128:131], v[132:135], v[192:195]\n\t" \
  "v_mfma_f32_16x16x32_bf16 v[200:203], v[128:131], v[108:111], v[200:203]\n\t" \
  "global_store_dwordx4 v2, v[42:45], %24 offset:1024 sc1\n\t" \
  "v_mfma_f32_16x16x32_bf16 v[200:203], v[120:123], v[148:151], v[200:203]\n\t" \
  "v_mfma_f32_16x16x32_bf16 v[208:211], v[120:123], v[64:67], v[208:211]\n\t" \
  "v_mfma_f32_16x16x32_bf16 v[208:211], v[128:131], v[104:107], v[208:211]\n\t" \
  "v_mov_b32 v30, 0xff61b1e6\n\t" \
  "v_bfe_i32 v34, v3, 0, 1\n\t" \
  "v_mfma_f32_16x16x32_bf16 v[248:251], v[128:131], v[136:139], v[248:251]\n\t" \
  "v_and_b32 v34, v30, v34\n\t" \
  "v_bfe_i32 v38, v3, 4, 1\n\t" \
  "v_mfma_f32_16x16x32_bf16 v[248:251], v[120:123], v[144:147], v[248:251]\n\t" \
  "v_and_b32 v38, v30, v38\n\t" \
  "v_bfe_i32 v42, v3, 8, 1\n\t" \
  "v_mfma_f32_16x16x32_bf16 v[252:255], v[72:75], v[136:139], v[252:255]\n\t" \
  "v_bfi_b32 v42, v42, v30, %15\n\t" \
  "v_mov_b32 v48, 0x3f803f80\n\t" \
  "s_waitcnt lgkmcnt(0)\n\t" \
  "v_mfma_f32_16x16x32_bf16 v[252:255], v[140:143], v[144:147], v[252:255]\n\t" \
  "v_bfe_i32 v35, v3, 1, 1\n\t" \
  "v_and_b32 v35, v30, v35\n\t" \
  "v_mfma_f32_16x16x32_bf16 v[188:191], v[140:143], v[124:127], v[188:191]\n\t" \
  "v_bfe_i32 v39, v3, 5, 1\n\t" \
  "v_and_b32 v39, v30, v39\n\t" \
  "v_mfma_f32_16x16x32_bf16 v[188:191], v[72:75], v[88:91], v[188:191]\n\t" \
  "v_bfe_i32 v43, v3, 9, 1\n\t" \
  "v_bfi_b32 v43, v43, v30, %15\n\t" \
  "s_barrier\n\t" \
  "v_mfma_f32_16x16x32_bf16 v[196:199], v[72:75], v[132:135], v[196:199]\n\t" \
  "ds_read_b64_tr_b16 v[80:81], v58 offset:34816\n\t" \
  "v_mov_b32 v49, 0x3f803f80\n\t" \
  "v_mfma_f32_16x16x32_bf16 v[196:199], v[140:143], v[96:99], v[196:199]\n\t" \
  "ds_read_b64_tr_b16 v[82:83], v58 offset:35328\n\t" \
  "v_bfe_i32 v36, v3, 2, 1\n\t" \
  "v_mfma_f32_16x16x32_bf16 v[204:207], v[140:143], v[148:151], v[204:207]\n\t" \
  "ds_read_b128 v[100:103], v56 offset:2048\n\t" \
  "v_and_b32 v36, v30, v36\n\t" \
  "v_mfma_f32_16x16x32_bf16 v[204:207], v[72:75], v[108:111], v[204:207]\n\t" \
  "ds_read_b64_tr_b16 v[92:93], v58 offset:38912\n\t" \
  "v_bfe_i32 v40, v3, 6, 1\n\t" \
  "v_mfma_f32_16x16x32_bf16 v[212:215], v[72:75], v[104:107], v[212:215]\n\t" \
  "ds_read_b64_tr_b16 v[94:95], v58 offset:39424\n\t" \
  "v_and_b32 v40, v30, v40\n\t" \
  "v_mfma_f32_16x16x32_bf16 v[212:215], v[140:143], v[64:67], v[212:215]\n\t" \
  "ds_read_b128 v[112:115], v57 offset:2048\n\t" \
  "v_bfe_i32 v44, v3, 10, 1\n\t" \
  "v_mfma_f32_16x16x32_bf16 v[152:155], v[68:71], v[88:91], v[152:155]\n\t" \
  "ds_read_b128 v[128:131], v57 offset:4096\n\t" \
  "v_bfi_b32 v44, v44, v30, %15\n\t" \
  "v_mfma_f32_16x16x32_bf16 v[152:155], v[116:119], v[124:127], v[152:155]\n\t" \
  "ds_read_b128 v[120:123], v56 offset:4096\n\t" \
  "v_mov_b32 v50, 0x3f803f80\n\t" \
  "v_mfma_f32_16x16x32_bf16 v[160:163], v[116:119], v[96:99], v[160:163]\n\t" \
  "ds_read_b128 v[136:139], v56 offset:6144\n\t" \
  "v_bfe_i32 v37, v3, 3, 1\n\t" \
  "v_mfma_f32_16x16x32_bf16 v[160:163], v[68:71], v[132:135], v[160:163]\n\t" \
  "ds_read_b128 v[144:147], v57 offset:6144\n\t" \
  "v_and_b32 v37, v30, v37\n\t" \
  "v_mfma_f32_16x16x32_bf16 v[168:171], v[68:71], v[108:111], v[168:171]\n\t" \
  "ds_read_b128 v[72:75], v57 offset:8192\n\t" \
  "v_bfe_i32 v41, v3, 7, 1\n\t" \
  "v_mfma_f32_16x16x32_bf16 v[168:171], v[116:119], v[148:151], v[168:171]\n\t" \
  "ds_read_b128 v[140:143], v56 offset:8192\n\t" \
  "v_and_b32 v41, v30, v41\n\t" \
  "v_mfma_f32_16x16x32_bf16 v[176:179], v[116:119], v[64:67], v[176:179]\n\t" \
  "ds_read_b64_tr_b16 v[116:117], v59 offset:34816\n\t" \
  "v_bfe_i32 v45, v3, 11, 1\n\t" \
  "v_mfma_f32_16x16x32_bf16 v[176:179], v[68:71], v[104:107], v[176:179]\n\t" \
  "ds_read_b64_tr_b16 v[118:119], v59 offset:35328\n\t" \
  "v_bfi_b32 v45, v45, v30, %15\n\t" \
  "v_mfma_f32_16x16x32_bf16 v[156:159], v[76:79], v[88:91], v[156:159]\n\t" \
  "ds_read_b64_tr_b16 v[68:69], v59 offset:38912\n\t" \
  "v_mov_b32 v51, 0x3f803f80\n\t" \
  "v_mfma_f32_16x16x32_bf16 v[156:159], v[84:87], v[124:127], v[156:159]\n\t" \
  "ds_read_b64_tr_b16 v[70:71], v59 offset:39424\n\t" \
  "v_bfe_i32 v46, v3, 12, 1\n\t" \
  "v_mfma_f32_16x16x32_bf16 v[164:167], v[84:87], v[96:99], v[164:167]\n\t" \
  "ds_read_b64_tr_b16 v[88:89], v58 offset:26624\n\t" \
  "v_bfe_i32 v31, v3, 13, 1\n\t" \
  "v_mfma_f32_16x16x32_bf16 v[164:167], v[76:79], v[132:135], v[164:167]\n\t" \
  "ds_read_b64_tr_b16 v[90:91], v58 offset:27136\n\t" \
  "v_perm_b32 v46, v31, v46, vcc_hi\n\t" \
  "v_mfma_f32_16x16x32_bf16 v[172:175], v[76:79], v[108:111], v[172:175]\n\t" \
  "ds_read_b64_tr_b16 v[124:125], v58 offset:30720\n\t" \
  "v_bfe_i32 v47, v3, 14, 1\n\t" \
  "v_mfma_f32_16x16x32_bf16 v[172:175], v[84:87], v[148:151], v[172:175]\n\t" \
  "ds_read_b64_tr_b16 v[126:127], v58 offset:31232\n\t" \
  "v_bfe_i32 v31, v3, 15, 1\n\t" \
  "v_mfma_f32_16x16x32_bf16 v[180:183], v[84:87], v[64:67], v[180:183]\n\t" \
  "ds_read_b128 v[96:99], v56 offset:0\n\t" \
  "v_perm_b32 v47, v31, v47, vcc_hi\n\t" \
  "v_mfma_f32_16x16x32_bf16 v[180:183], v[76:79], v[104:107], v[180:183]\n\t" \
  "ds_read_b128 v[132:135], v57 offset:0\n\t" \
  "v_add_u32 v30, 18432, v4\n\t" \
  "s_waitcnt lgkmcnt(7)\n\t" \
  "v_mfma_f32_16x16x32_bf16 v[216:219], v[100:103], v[80:83], v[216:219]\n\t" \
  "ds_read_b64_tr_b16 v[108:109], v59 offset:26624\n\t" \
  "v_xor_b32 v31, 1024, v30\n\t" \
  "v_mfma_f32_16x16x32_bf16 v[216:219], v[112:115], v[92:95], v[216:219]\n\t" \
  "ds_read_b64_tr_b16 v[110:111], v59 offset:27136\n\t" \
  "v_mov_b32 v32, %12\n\t" \
  "v_mfma_f32_16x16x32_bf16 v[224:227], v[128:131], v[92:95], v[224:227]\n\t" \
  "ds_read_b64_tr_b16 v[148:149], v59 offset:30720\n\t" \
  "v_xor_b32 v33, 4, v32\n\t" \
  "v_mfma_f32_16x16x32_bf16 v[224:227], v[120:123], v[80:83], v[224:227]\n\t" \
  "ds_read_b64_tr_b16 v[150:151], v59 offset:31232\n\t" \
  "v_mov_b32 v0, 1\n\t" \
  "v_mfma_f32_16x16x32_bf16 v[232:235], v[136:139], v[80:83], v[232:235]\n\t" \
  "ds_read_b64_tr_b16 v[64:65], v58 offset:18432\n\t" \
  "v_and_b32 v4, 1023, v4\n\t" \
  "v_mfma_f32_16x16x32_bf16 v[232:235], v[144:147], v[92:95], v[232:235]\n\t" \
  "ds_read_b64_tr_b16 v[66:67], v58 offset:18944\n\t" \
  "v_add_u32 v1, 16384, v4\n\t" \
  "v_mfma_f32_16x16x32_bf16 v[240:243], v[72:75], v[92:95], v[240:243]\n\t" \
  "ds_read_b64_tr_b16 v[84:85], v58 offset:22528\n\t" \
  "v_mfma_f32_16x16x32_bf16 v[240:243], v[140:143], v[80:83], v[240:243]\n\t" \
  "ds_read_b64_tr_b16 v[86:87], v58 offset:23040\n\t" \
  "v_mfma_f32_16x16x32_bf16 v[220:223], v[100:103], v[116:119], v[220:223]\n\t" \
  "ds_read_b64_tr_b16 v[76:77], v59 offset:18432\n\t" \
  "s_waitcnt lgkmcnt(7)\n\t" \
  "v_mfma_f32_16x16x32_bf16 v[220:223], v[112:115], v[68:71], v[220:223]\n\t" \
  "ds_read_b64_tr_b16 v[78:79], v59 offset:18944\n\t" \
  "v_mfma_f32_16x16x32_bf16 v[228:231], v[128:131], v[68:71], v[228:231]\n\t" \
  "ds_read_b64_tr_b16 v[104:105], v59 offset:22528\n\t" \
  "v_mfma_f32_16x16x32_bf16 v[228:231], v[120:123], v[116:119], v[228:231]\n\t" \
  "ds_read_b64_tr_b16 v[106:107], v59 offset:23040\n\t" \
  "v_mfma_f32_16x16x32_bf16 v[236:239], v[136:139], v[116:119], v[236:239]\n\t" \
  "v_mfma_f32_16x16x32_bf16 v[236:239], v[144:147], v[68:71], v[236:239]\n\t" \
  "v_mfma_f32_16x16x32_bf16 v[244:247], v[72:75], v[68:71], v[244:247]\n\t" \
  "v_mfma_f32_16x16x32_bf16 v[244:247], v[140:143], v[116:119], v[244:247]\n\t" \
  "v_mfma_f32_16x16x32_bf16 v[184:187], v[88:91], v[100:103], v[184:187]\n\t" \
  "v_mfma_f32_16x16x32_bf16 v[184:187], v[124:127], v[112:115], v[184:187]\n\t" \
  "v_mfma_f32_16x16x32_bf16 v[192:195], v[124:127], v[128:131], v[192:195]\n\t" \
  "v_mfma_f32_16x16x32_bf16 v[192:195], v[88:91], v[120:123], v[192:195]\n\t" \
  "v_mfma_f32_16x16x32_bf16 v[200:203], v[88:91], v[136:139], v[200:203]\n\t" \
  "v_mfma_f32_16x16x32_bf16 v[200:203], v[124:127], v[144:147], v[200:203]\n\t" \
  "v_mfma_f32_16x16x32_bf16 v[208:211], v[124:127], v[72:75], v[208:211]\n\t" \
  "v_mfma_f32_16x16x32_bf16 v[208:211], v[88:91], v[140:143], v[208:211]\n\t" \
  "v_mfma_f32_16x16x32_bf16 v[248:251], v[88:91], v[96:99], v[248:251]\n\t" \
  "v_mfma_f32_16x16x32_bf16 v[248:251], v[124:127], v[132:135], v[248:251]\n\t" \
  "v_mfma_f32_16x16x32_bf16 v[252:255], v[108:111], v[96:99], v[252:255]\n\t" \
  "s_waitcnt lgkmcnt(0)\n\t" \
  "v_mfma_f32_16x16x32_bf16 v[252:255], v[148:151], v[132:135], v[252:255]\n\t" \
  "v_mfma_f32_16x16x32_bf16 v[188:191], v[148:151], v[112:115], v[188:191]\n\t" \
  "v_mfma_f32_16x16x32_bf16 v[188:191], v[108:111], v[100:103], v[188:191]\n\t" \
  "s_barrier\n\t" \
  "v_mfma_f32_16x16x32_bf16 v[196:199], v[108:111], v[120:123], v[196:199]\n\t" \
  "ds_read_b64_tr_b16 v[92:93], v62 offset:34816\n\t" \
  "v_mfma_f32_16x16x32_bf16 v[196:199], v[148:151], v[128:131], v[196:199]\n\t" \
  "ds_read_b64_tr_b16 v[94:95], v62 offset:35328\n\t" \
  "v_mfma_f32_16x16x32_bf16 v[204:207], v[148:151], v[144:147], v[204:207]\n\t" \
  "ds_read_b128 v[80:83], v60 offset:2048\n\t" \
  "v_mfma_f32_16x16x32_bf16 v[204:207], v[108:111], v[136:139], v[204:207]\n\t" \
  "ds_read_b64_tr_b16 v[68:69], v62 offset:38912\n\t" \
  "v_mfma_f32_16x16x32_bf16 v[212:215], v[108:111], v[140:143], v[212:215]\n\t" \
  "ds_read_b64_tr_b16 v[70:71], v62 offset:39424\n\t" \
  "v_mfma_f32_16x16x32_bf16 v[212:215], v[148:151], v[72:75], v[212:215]\n\t" \
  "ds_read_b128 v[116:119], v61 offset:2048\n\t" \
  "v_mfma_f32_16x16x32_bf16 v[152:155], v[64:67], v[100:103], v[152:155]\n\t" \
  "ds_read_b128 v[88:91], v61 offset:4096\n\t" \
  "v_mfma_f32_16x16x32_bf16 v[152:155], v[84:87], v[112:115], v[152:155]\n\t" \
  "ds_read_b128 v[124:127], v60 offset:4096\n\t" \
  "v_mfma_f32_16x16x32_bf16 v[160:163], v[84:87], v[128:131], v[160:163]\n\t" \
  "ds_read_b128 v[96:99], v60 offset:6144\n\t" \
  "v_mfma_f32_16x16x32_bf16 v[160:163], v[64:67], v[120:123], v[160:163]\n\t" \
  "ds_read_b128 v[132:135], v61 offset:6144\n\t" \
  "v_mfma_f32_16x16x32_bf16 v[168:171], v[64:67], v[136:139], v[168:171]\n\t" \
  "ds_read_b128 v[108:111], v61 offset:8192\n\t" \
  "v_mfma_f32_16x16x32_bf16 v[168:171], v[84:87], v[144:147], v[168:171]\n\t" \
  "ds_read_b128 v[148:151], v60 offset:8192\n\t" \
  "v_mfma_f32_16x16x32_bf16 v[176:179], v[84:87], v[72:75], v[176:179]\n\t" \
  "ds_read_b64_tr_b16 v[84:85], v63 offset:34816\n\t" \
  "v_mfma_f32_16x16x32_bf16 v[176:179], v[64:67], v[140:143], v[176:179]\n\t" \
  "ds_read_b64_tr_b16 v[86:87], v63 offset:35328\n\t" \
  "v_mfma_f32_16x16x32_bf16 v[156:159], v[76:79], v[100:103], v[156:159]\n\t" \
  "ds_read_b64_tr_b16 v[64:65], v63 offset:38912\n\t" \
  "v_mfma_f32_16x16x32_bf16 v[156:159], v[104:107], v[112:115], v[156:159]\n\t" \
  "ds_read_b64_tr_b16 v[66:67], v63 offset:39424\n\t" \
  "v_mfma_f32_16x16x32_bf16 v[164:167], v[104:107], v[128:131], v[164:167]\n\t" \
  "ds_read_b64_tr_b16 v[100:101], v62 offset:26624\n\t" \
  "v_mfma_f32_16x16x32_bf16 v[164:167], v[76:79], v[120:123], v[164:167]\n\t" \
  "ds_read_b64_tr_b16 v[102:103], v62 offset:27136\n\t" \
  "v_mfma_f32_16x16x32_bf16 v[172:175], v[76:79], v[136:139], v[172:175]\n\t" \
  "ds_read_b64_tr_b16 v[112:113], v62 offset:30720\n\t" \
  "v_mfma_f32_16x16x32_bf16 v[172:175], v[104:107], v[144:147], v[172:175]\n\t" \
  "ds_read_b64_tr_b16 v[114:115], v62 offset:31232\n\t" \
  "v_mfma_f32_16x16x32_bf16 v[180:183], v[104:107], v[72:75], v[180:183]\n\t" \
  "ds_read_b128 v[128:131], v60 offset:0\n\t" \
  "v_mfma_f32_16x16x32_bf16 v[180:183], v[76:79], v[140:143], v[180:183]\n\t" \
  "ds_read_b128 v[120:123], v61 offset:0\n\t" \
  "s_waitcnt lgkmcnt(7)\n\t" \
  "v_mfma_f32_16x16x32_bf16 v[216:219], v[80:83], v[92:95], v[216:219]\n\t" \
  "ds_read_b64_tr_b16 v[136:137], v63 offset:26624\n\t" \
  "v_mfma_f32_16x16x32_bf16 v[216:219], v[116:119], v[68:71], v[216:219]\n\t" \
  "ds_read_b64_tr_b16 v[138:139], v63 offset:27136\n\t" \
  "v_mfma_f32_16x16x32_bf16 v[224:227], v[88:91], v[68:71], v[224:227]\n\t" \
  "ds_read_b64_tr_b16 v[144:145], v63 offset:30720\n\t" \
  "v_mfma_f32_16x16x32_bf16 v[224:227], v[124:127], v[92:95], v[224:227]\n\t" \
  "ds_read_b64_tr_b16 v[146:147], v63 offset:31232\n\t" \
  "v_mfma_f32_16x16x32_bf16 v[232:235], v[96:99], v[92:95], v[232:235]\n\t" \
  "ds_read_b64_tr_b16 v[72:73], v62 offset:18432\n\t" \
  "v_mfma_f32_16x16x32_bf16 v[232:235], v[132:135], v[68:71], v[232:235]\n\t" \
  "ds_read_b64_tr_b16 v[74:75], v62 offset:18944\n\t" \
  "v_mfma_f32_16x16x32_bf16 v[240:243], v[108:111], v[68:71], v[240:243]\n\t" \
  "ds_read_b64_tr_b16 v[104:105], v62 offset:22528\n\t" \
  "v_mfma_f32_16x16x32_bf16 v[240:243], v[148:151], v[92:95], v[240:243]\n\t" \
  "ds_read_b64_tr_b16 v[106:107], v62 offset:23040\n\t" \
  "v_mfma_f32_16x16x32_bf16 v[220:223], v[80:83], v[84:87], v[220:223]\n\t" \
  "ds_read_b64_tr_b16 v[76:77], v63 offset:18432\n\t" \
  "s_waitcnt lgkmcnt(7)\n\t" \
  "v_mfma_f32_16x16x32_bf16 v[220:223], v[116:119], v[64:67], v[220:223]\n\t" \
  "ds_read_b64_tr_b16 v[78:79], v63 offset:18944\n\t" \
  "v_mfma_f32_16x16x32_bf16 v[228:231], v[88:91], v[64:67], v[228:231]\n\t" \
  "ds_read_b64_tr_b16 v[140:141], v63 offset:22528\n\t" \
  "v_mfma_f32_16x16x32_bf16 v[228:231], v[124:127], v[84:87], v[228:231]\n\t" \
  "ds_read_b64_tr_b16 v[142:143], v63 offset:23040\n\t" \
  "v_mfma_f32_16x16x32_bf16 v[236:239], v[96:99], v[84:87], v[236:239]\n\t" \
  "v_mfma_f32_16x16x32_bf16 v[236:239], v[132:135], v[64:67], v[236:239]\n\t" \
  "v_mfma_f32_16x16x32_bf16 v[244:247], v[108:111], v[64:67], v[244:247]\n\t" \
  "v_mfma_f32_16x16x32_bf16 v[244:247], v[148:151], v[84:87], v[244:247]\n\t" \
  "v_mfma_f32_16x16x32_bf16 v[184:187], v[100:103], v[80:83], v[184:187]\n\t" \
  "v_mfma_f32_16x16x32_bf16 v[184:187], v[112:115], v[116:119], v[184:187]\n\t" \
  "v_mfma_f32_16x16x32_bf16 v[192:195], v[112:115], v[88:91], v[192:195]\n\t" \
  "v_mfma_f32_16x16x32_bf16 v[192:195], v[100:103], v[124:127], v[192:195]\n\t" \
  "v_mfma_f32_16x16x32_bf16 v[200:203], v[100:103], v[96:99], v[200:203]\n\t" \
  "v_mfma_f32_16x16x32_bf16 v[200:203], v[112:115], v[132:135], v[200:203]\n\t" \
  "v_mfma_f32_16x16x32_bf16 v[208:211], v[112:115], v[108:111], v[208:211]\n\t" \
  "v_mfma_f32_16x16x32_bf16 v[208:211], v[100:103], v[148:151], v[208:211]\n\t" \
  "v_mfma_f32_16x16x32_bf16 v[248:251], v[100:103], v[128:131], v[248:251]\n\t" \
  "v_mfma_f32_16x16x32_bf16 v[248:251], v[112:115], v[120:123], v[248:251]\n\t" \
  "v_mfma_f32_16x16x32_bf16 v[252:255], v[136:139], v[128:131], v[252:255]\n\t" \
  "s_waitcnt lgkmcnt(0)\n\t" \
  "v_mfma_f32_16x16x32_bf16 v[252:255], v[144:147], v[120:123], v[252:255]\n\t" \
  "v_mfma_f32_16x16x32_bf16 v[188:191], v[144:147], v[116:119], v[188:191]\n\t" \
  "v_mfma_f32_16x16x32_bf16 v[188:191], v[136:139], v[80:83], v[188:191]\n\t" \
  "s_waitcnt vmcnt(0)\n\t" \
  "s_barrier\n\t" \
  "v_mfma_f32_16x16x32_bf16 v[196:199], v[136:139], v[124:127], v[196:199]\n\t" \
  "ds_read_b64_tr_b16 v[68:69], v54 offset:34816\n\t" \
  "v_mfma_f32_16x16x32_bf16 v[196:199], v[144:147], v[88:91], v[196:199]\n\t" \
  "ds_read_b64_tr_b16 v[70:71], v54 offset:35328\n\t" \
  "v_mfma_f32_16x16x32_bf16 v[204:207], v[144:147], v[132:135], v[204:207]\n\t" \
  "ds_read_b128 v[92:95], v52 offset:2048\n\t" \
  "v_mfma_f32_16x16x32_bf16 v[204:207], v[136:139], v[96:99], v[204:207]\n\t" \
  "ds_read_b64_tr_b16 v[64:65], v54 offset:38912\n\t" \
  "v_mfma_f32_16x16x32_bf16 v[212:215], v[136:139], v[148:151], v[212:215]\n\t" \
  "ds_read_b64_tr_b16 v[66:67], v54 offset:39424\n\t" \
  "v_mfma_f32_16x16x32_bf16 v[212:215], v[144:147], v[108:111], v[212:215]\n\t" \
  "ds_read_b128 v[84:87], v53 offset:2048\n\t" \
  "v_mfma_f32_16x16x32_bf16 v[152:155], v[72:75], v[80:83], v[152:155]\n\t" \
  "ds_read_b128 v[100:103], v53 offset:4096\n\t" \
  "v_mfma_f32_16x16x32_bf16 v[152:155], v[104:107], v[116:119], v[152:155]\n\t" \
  "ds_read_b128 v[112:115], v52 offset:4096\n\t" \
  "v_mfma_f32_16x16x32_bf16 v[160:163], v[104:107], v[88:91], v[160:163]\n\t" \
  "ds_read_b128 v[128:131], v52 offset:6144\n\t" \
  "v_mfma_f32_16x16x32_bf16 v[160:163], v[72:75], v[124:127], v[160:163]\n\t" \
  "ds_read_b128 v[120:123], v53 offset:6144\n\t" \
  "v_mfma_f32_16x16x32_bf16 v[168:171], v[72:75], v[96:99], v[168:171]\n\t" \
  "ds_read_b128 v[136:139], v53 offset:8192\n\t" \
  "v_mfma_f32_16x16x32_bf16 v[168:171], v[104:107], v[132:135], v[168:171]\n\t" \
  "ds_read_b128 v[144:147], v52 offset:8192\n\t" \
  "v_mfma_f32_16x16x32_bf16 v[176:179], v[104:107], v[108:111], v[176:179]\n\t" \
  "ds_read_b64_tr_b16 v[104:105], v55 offset:34816\n\t" \
  "v_mfma_f32_16x16x32_bf16 v[176:179], v[72:75], v[148:151], v[176:179]\n\t" \
  "ds_read_b64_tr_b16 v[106:107], v55 offset:35328\n\t" \
  "v_mfma_f32_16x16x32_bf16 v[156:159], v[76:79], v[80:83], v[156:159]\n\t" \
  "ds_read_b64_tr_b16 v[72:73], v55 offset:38912\n\t" \
  "v_mfma_f32_16x16x32_bf16 v[156:159], v[140:143], v[116:119], v[156:159]\n\t" \
  "ds_read_b64_tr_b16 v[74:75], v55 offset:39424\n\t" \
  "v_mfma_f32_16x16x32_bf16 v[164:167], v[140:143], v[88:91], v[164:167]\n\t" \
  "ds_read_b64_tr_b16 v[80:81], v54 offset:26624\n\t" \
  "v_mfma_f32_16x16x32_bf16 v[164:167], v[76:79], v[124:127], v[164:167]\n\t" \
  "ds_read_b64_tr_b16 v[82:83], v54 offset:27136\n\t" \
  "v_mfma_f32_16x16x32_bf16 v[172:175], v[76:79], v[96:99], v[172:175]\n\t" \
  "ds_read_b64_tr_b16 v[116:117], v54 offset:30720\n\t" \
  "v_mfma_f32_16x16x32_bf16 v[172:175], v[140:143], v[132:135], v[172:175]\n\t" \
  "ds_read_b64_tr_b16 v[118:119], v54 offset:31232\n\t" \
  "v_mfma_f32_16x16x32_bf16 v[180:183], v[140:143], v[108:111], v[180:183]\n\t" \
  "ds_read_b128 v[88:91], v52 offset:0\n\t" \
  "v_mfma_f32_16x16x32_bf16 v[180:183], v[76:79], v[148:151], v[180:183]\n\t" \
  "ds_read_b128 v[124:127], v53 offset:0\n\t" \
  "s_waitcnt lgkmcnt(7)\n\t" \
  "v_mfma_f32_16x16x32_bf16 v[216:219], v[92:95], v[68:71], v[216:219]\n\t" \
  "ds_read_b64_tr_b16 v[96:97], v55 offset:26624\n\t" \
  "v_mfma_f32_16x16x32_bf16 v[216:219], v[84:87], v[64:67], v[216:219]\n\t" \
  "ds_read_b64_tr_b16 v[98:99], v55 offset:27136\n\t" \
  "v_mfma_f32_16x16x32_bf16 v[224:227], v[100:103], v[64:67], v[224:227]\n\t" \
  "ds_read_b64_tr_b16 v[132:133], v55 offset:30720\n\t" \
  "v_mfma_f32_16x16x32_bf16 v[224:227], v[112:115], v[68:71], v[224:227]\n\t" \
  "ds_read_b64_tr_b16 v[134:135], v55 offset:31232\n\t" \
  "v_mfma_f32_16x16x32_bf16 v[232:235], v[128:131], v[68:71], v[232:235]\n\t" \
  "ds_read_b64_tr_b16 v[108:109], v54 offset:18432\n\t" \
  "v_mfma_f32_16x16x32_bf16 v[232:235], v[120:123], v[64:67], v[232:235]\n\t" \
  "ds_read_b64_tr_b16 v[110:111], v54 offset:18944\n\t" \
  "v_mfma_f32_16x16x32_bf16 v[240:243], v[136:139], v[64:67], v[240:243]\n\t" \
  "ds_read_b64_tr_b16 v[140:141], v54 offset:22528\n\t" \
  "v_mfma_f32_16x16x32_bf16 v[240:243], v[144:147], v[68:71], v[240:243]\n\t" \
  "ds_read_b64_tr_b16 v[142:143], v54 offset:23040\n\t" \
  "v_mfma_f32_16x16x32_bf16 v[220:223], v[92:95], v[104:107], v[220:223]\n\t" \
  "ds_read_b64_tr_b16 v[76:77], v55 offset:18432\n\t" \
  "s_waitcnt lgkmcnt(7)\n\t" \
  "v_mfma_f32_16x16x32_bf16 v[220:223], v[84:87], v[72:75], v[220:223]\n\t" \
  "ds_read_b64_tr_b16 v[78:79], v55 offset:18944\n\t" \
  "v_mfma_f32_16x16x32_bf16 v[228:231], v[100:103], v[72:75], v[228:231]\n\t" \
  "ds_read_b64_tr_b16 v[148:149], v55 offset:22528\n\t" \
  "v_mfma_f32_16x16x32_bf16 v[228:231], v[112:115], v[104:107], v[228:231]\n\t" \
  "ds_read_b64_tr_b16 v[150:151], v55 offset:23040\n\t" \
  "v_mfma_f32_16x16x32_bf16 v[236:239], v[128:131], v[104:107], v[236:239]\n\t" \
  "v_mfma_f32_16x16x32_bf16 v[236:239], v[120:123], v[72:75], v[236:239]\n\t" \
  "v_mfma_f32_16x16x32_bf16 v[244:247], v[136:139], v[72:75], v[244:247]\n\t" \
  "v_mfma_f32_16x16x32_bf16 v[244:247], v[144:147], v[104:107], v[244:247]\n\t" \
  "v_mfma_f32_16x16x32_bf16 v[184:187], v[80:83], v[92:95], v[184:187]\n\t" \
  "ds_read_b32 v10, v32 offset:104\n\t" \
  "v_mfma_f32_16x16x32_bf16 v[184:187], v[116:119], v[84:87], v[184:187]\n\t" \
  "v_lshlrev_b32 v11, 3, v4\n\t" \
  "v_mfma_f32_16x16x32_bf16 v[192:195], v[116:119], v[100:103], v[192:195]\n\t" \
  "v_mfma_f32_16x16x32_bf16 v[192:195], v[80:83], v[112:115], v[192:195]\n\t" \
  "v_mfma_f32_16x16x32_bf16 v[200:203], v[80:83], v[128:131], v[200:203]\n\t" \
  "s_waitcnt lgkmcnt(0)\n\t" \
  "s_cmp_lg_u32 %13, 0\n\t" \
  "s_cbranch_scc1 27f\n\t" \
  "s_mov_b32 exec_hi, 0\n\t" \
  "global_store_dword v11, v10, %25 sc1\n\t" \
  "s_mov_b32 exec_hi, -1\n\t" \
  "27:\n\t" \
  "v_mfma_f32_16x16x32_bf16 v[200:203], v[116:119], v[120:123], v[200:203]\n\t" \
  "v_mfma_f32_16x16x32_bf16 v[208:211], v[116:119], v[136:139], v[208:211]\n\t" \
  "v_mfma_f32_16x16x32_bf16 v[208:211], v[80:83], v[144:147], v[208:211]\n\t" \
  "v_mfma_f32_16x16x32_bf16 v[248:251], v[80:83], v[88:91], v[248:251]\n\t" \
  "v_mfma_f32_16x16x32_bf16 v[248:251], v[116:119], v[124:127], v[248:251]\n\t" \
  "v_mfma_f32_16x16x32_bf16 v[252:255], v[96:99], v[88:91], v[252:255]\n\t" \
  "v_mfma_f32_16x16x32_bf16 v[252:255], v[132:135], v[124:127], v[252:255]\n\t" \
  "v_mfma_f32_16x16x32_bf16 v[188:191], v[132:135], v[84:87], v[188:191]\n\t" \
  "v_mfma_f32_16x16x32_bf16 v[188:191], v[96:99], v[92:95], v[188:191]\n\t" \
  "s_barrier\n\t" \
  "v_mfma_f32_16x16x32_bf16 v[196:199], v[96:99], v[112:115], v[196:199]\n\t" \
  "ds_read_b64_tr_b16 v[64:65], v58 offset:34816\n\t" \
  "v_mfma_f32_16x16x32_bf16 v[196:199], v[132:135], v[100:103], v[196:199]\n\t" \
  "ds_read_b64_tr_b16 v[66:67], v58 offset:35328\n\t" \
  "v_mfma_f32_16x16x32_bf16 v[204:207], v[132:135], v[120:123], v[204:207]\n\t" \
  "ds_read_b128 v[68:71], v56 offset:2048\n\t" \
  "v_mfma_f32_16x16x32_bf16 v[204:207], v[96:99], v[128:131], v[204:207]\n\t" \
  "ds_read_b64_tr_b16 v[72:73], v58 offset:38912\n\t" \
  "v_mfma_f32_16x16x32_bf16 v[212:215], v[96:99], v[144:147], v[212:215]\n\t" \
  "ds_read_b64_tr_b16 v[74:75], v58 offset:39424\n\t" \
  "v_mfma_f32_16x16x32_bf16 v[212:215], v[132:135], v[136:139], v[212:215]\n\t" \
  "ds_read_b128 v[104:107], v57 offset:2048\n\t" \
  "v_mfma_f32_16x16x32_bf16 v[152:155], v[108:111], v[92:95], v[152:155]\n\t" \
  "ds_read_b128 v[80:83], v57 offset:4096\n\t" \
  "v_mfma_f32_16x16x32_bf16 v[152:155], v[140:143], v[84:87], v[152:155]\n\t" \
  "ds_read_b128 v[116:119], v56 offset:4096\n\t" \
  "v_mfma_f32_16x16x32_bf16 v[160:163], v[140:143], v[100:103], v[160:163]\n\t" \
  "ds_read_b128 v[88:91], v56 offset:6144\n\t" \
  "v_mfma_f32_16x16x32_bf16 v[160:163], v[108:111], v[112:115], v[160:163]\n\t" \
  "ds_read_b128 v[124:127], v57 offset:6144\n\t" \
  "v_mfma_f32_16x16x32_bf16 v[168:171], v[108:111], v[128:131], v[168:171]\n\t" \
  "ds_read_b128 v[96:99], v57 offset:8192\n\t" \
  "v_mfma_f32_16x16x32_bf16 v[168:171], v[140:143], v[120:123], v[168:171]\n\t" \
  "ds_read_b128 v[132:135], v56 offset:8192\n\t" \
  "v_mfma_f32_16x16x32_bf16 v[176:179], v[140:143], v[136:139], v[176:179]\n\t" \
  "ds_read_b64_tr_b16 v[140:141], v59 offset:34816\n\t" \
  "v_mfma_f32_16x16x32_bf16 v[176:179], v[108:111], v[144:147], v[176:179]\n\t" \
  "ds_read_b64_tr_b16 v[142:143], v59 offset:35328\n\t" \
  "v_mfma_f32_16x16x32_bf16 v[156:159], v[76:79], v[92:95], v[156:159]\n\t" \
  "ds_read_b64_tr_b16 v[108:109], v59 offset:38912\n\t" \
  "v_mfma_f32_16x16x32_bf16 v[156:159], v[148:151], v[84:87], v[156:159]\n\t" \
  "ds_read_b64_tr_b16 v[110:111], v59 offset:39424\n\t" \
  "v_mfma_f32_16x16x32_bf16 v[164:167], v[148:151], v[100:103], v[164:167]\n\t" \
  "ds_read_b64_tr_b16 v[92:93], v58 offset:26624\n\t" \
  "v_mfma_f32_16x16x32_bf16 v[164:167], v[76:79], v[112:115], v[164:167]\n\t" \
  "ds_read_b64_tr_b16 v[94:95], v58 offset:27136\n\t" \
  "v_mfma_f32_16x16x32_bf16 v[172:175], v[76:79], v[128:131], v[172:175]\n\t" \
  "ds_read_b64_tr_b16 v[84:85], v58 offset:30720\n\t" \
  "v_mfma_f32_16x16x32_bf16 v[172:175], v[148:151], v[120:123], v[172:175]\n\t" \
  "ds_read_b64_tr_b16 v[86:87], v58 offset:31232\n\t" \
  "v_mfma_f32_16x16x32_bf16 v[180:183], v[148:151], v[136:139], v[180:183]\n\t" \
  "ds_read_b128 v[100:103], v56 offset:0\n\t" \
  "v_mfma_f32_16x16x32_bf16 v[180:183], v[76:79], v[144:147], v[180:183]\n\t" \
  "ds_read_b128 v[112:115], v57 offset:0\n\t" \
  "s_waitcnt lgkmcnt(7)\n\t" \
  "v_mfma_f32_16x16x32_bf16 v[216:219], v[68:71], v[64:67], v[216:219]\n\t" \
  "ds_read_b64_tr_b16 v[128:129], v59 offset:26624\n\t" \
  "v_mfma_f32_16x16x32_bf16 v[216:219], v[104:107], v[72:75], v[216:219]\n\t" \
  "ds_read_b64_tr_b16 v[130:131], v59 offset:27136\n\t" \
  "v_mfma_f32_16x16x32_bf16 v[224:227], v[80:83], v[72:75], v[224:227]\n\t" \
  "ds_read_b64_tr_b16 v[120:121], v59 offset:30720\n\t" \
  "v_mfma_f32_16x16x32_bf16 v[224:227], v[116:119], v[64:67], v[224:227]\n\t" \
  "ds_read_b64_tr_b16 v[122:123], v59 offset:31232\n\t" \
  "v_mfma_f32_16x16x32_bf16 v[232:235], v[88:91], v[64:67], v[232:235]\n\t" \
  "ds_read_b64_tr_b16 v[136:137], v58 offset:18432\n\t" \
  "v_mfma_f32_16x16x32_bf16 v[232:235], v[124:127], v[72:75], v[232:235]\n\t" \
  "ds_read_b64_tr_b16 v[138:139], v58 offset:18944\n\t" \
  "v_mfma_f32_16x16x32_bf16 v[240:243], v[96:99], v[72:75], v[240:243]\n\t" \
  "ds_read_b64_tr_b16 v[148:149], v58 offset:22528\n\t" \
  "v_mfma_f32_16x16x32_bf16 v[240:243], v[132:135], v[64:67], v[240:243]\n\t" \
  "ds_read_b64_tr_b16 v[150:151], v58 offset:23040\n\t" \
  "v_mfma_f32_16x16x32_bf16 v[220:223], v[68:71], v[140:143], v[220:223]\n\t" \
  "ds_read_b64_tr_b16 v[76:77], v59 offset:18432\n\t" \
  "s_waitcnt lgkmcnt(7)\n\t" \
  "v_mfma_f32_16x16x32_bf16 v[220:223], v[104:107], v[108:111], v[220:223]\n\t" \
  "ds_read_b64_tr_b16 v[78:79], v59 offset:18944\n\t" \
  "v_mfma_f32_16x16x32_bf16 v[228:231], v[80:83], v[108:111], v[228:231]\n\t" \
  "ds_read_b64_tr_b16 v[144:145], v59 offset:22528\n\t" \
  "v_mfma_f32_16x16x32_bf16 v[228:231], v[116:119], v[140:143], v[228:231]\n\t" \
  "ds_read_b64_tr_b16 v[146:147], v59 offset:23040\n\t" \
  "v_mfma_f32_16x16x32_bf16 v[236:239], v[88:91], v[140:143], v[236:239]\n\t" \
  "v_mfma_f32_16x16x32_bf16 v[236:239], v[124:127], v[108:111], v[236:239]\n\t" \
  "v_mfma_f32_16x16x32_bf16 v[244:247], v[96:99], v[108:111], v[244:247]\n\t" \
  "v_mfma_f32_16x16x32_bf16 v[244:247], v[132:135], v[140:143], v[244:247]\n\t" \
  "v_mfma_f32_16x16x32_bf16 v[184:187], v[92:95], v[68:71], v[184:187]\n\t" \
  "v_mfma_f32_16x16x32_bf16 v[184:187], v[84:87], v[104:107], v[184:187]\n\t" \
  "v_mfma_f32_16x16x32_bf16 v[192:195], v[84:87], v[80:83], v[192:195]\n\t" \
  "v_mfma_f32_16x16x32_bf16 v[192:195], v[92:95], v[116:119], v[192:195]\n\t" \
  "v_mfma_f32_16x16x32_bf16 v[200:203], v[92:95], v[88:91], v[200:203]\n\t" \
  "v_mfma_f32_16x16x32_bf16 v[200:203], v[84:87], v[124:127], v[200:203]\n\t" \
  "v_mfma_f32_16x16x32_bf16 v[208:211], v[84:87], v[96:99], v[208:211]\n\t" \
  "v_mfma_f32_16x16x32_bf16 v[208:211], v[92:95], v[132:135], v[208:211]\n\t" \
  "v_mfma_f32_16x16x32_bf16 v[248:251], v[92:95], v[100:103], v[248:251]\n\t" \
  "v_mfma_f32_16x16x32_bf16 v[248:251], v[84:87], v[112:115], v[248:251]\n\t" \
  "v_mfma_f32_16x16x32_bf16 v[252:255], v[128:131], v[100:103], v[252:255]\n\t" \
  "s_waitcnt lgkmcnt(0)\n\t" \
  "v_mfma_f32_16x16x32_bf16 v[252:255], v[120:123], v[112:115], v[252:255]\n\t" \
  "v_mfma_f32_16x16x32_bf16 v[188:191], v[120:123], v[104:107], v[188:191]\n\t" \
  "v_mfma_f32_16x16x32_bf16 v[188:191], v[128:131], v[68:71], v[188:191]\n\t" \
  "s_barrier\n\t" \
  "v_mfma_f32_16x16x32_bf16 v[196:199], v[128:131], v[116:119], v[196:199]\n\t" \
  "ds_read_b64_tr_b16 v[72:73], v62 offset:34816\n\t" \
  "v_mfma_f32_16x16x32_bf16 v[196:199], v[120:123], v[80:83], v[196:199]\n\t" \
  "ds_read_b64_tr_b16 v[74:75], v62 offset:35328\n\t" \
  "v_mfma_f32_16x16x32_bf16 v[204:207], v[120:123], v[124:127], v[204:207]\n\t" \
  "ds_read_b128 v[64:67], v60 offset:2048\n\t" \
  "v_mfma_f32_16x16x32_bf16 v[204:207], v[128:131], v[88:91], v[204:207]\n\t" \
  "ds_read_b64_tr_b16 v[108:109], v62 offset:38912\n\t" \
  "v_mfma_f32_16x16x32_bf16 v[212:215], v[128:131], v[132:135], v[212:215]\n\t" \
  "ds_read_b64_tr_b16 v[110:111], v62 offset:39424\n\t" \
  "v_mfma_f32_16x16x32_bf16 v[212:215], v[120:123], v[96:99], v[212:215]\n\t" \
  "ds_read_b128 v[140:143], v61 offset:2048\n\t" \
  "v_mfma_f32_16x16x32_bf16 v[152:155], v[136:139], v[68:71], v[152:155]\n\t" \
  "ds_read_b128 v[92:95], v61 offset:4096\n\t" \
  "v_mfma_f32_16x16x32_bf16 v[152:155], v[148:151], v[104:107], v[152:155]\n\t" \
  "ds_read_b128 v[84:87], v60 offset:4096\n\t" \
  "v_mfma_f32_16x16x32_bf16 v[160:163], v[148:151], v[80:83], v[160:163]\n\t" \
  "ds_read_b128 v[100:103], v60 offset:6144\n\t" \
  "v_mfma_f32_16x16x32_bf16 v[160:163], v[136:139], v[116:119], v[160:163]\n\t" \
  "ds_read_b128 v[112:115], v61 offset:6144\n\t" \
  "v_mfma_f32_16x16x32_bf16 v[168:171], v[136:139], v[88:91], v[168:171]\n\t" \
  "ds_read_b128 v[128:131], v61 offset:8192\n\t" \
  "v_mfma_f32_16x16x32_bf16 v[168:171], v[148:151], v[124:127], v[168:171]\n\t" \
  "ds_read_b128 v[120:123], v60 offset:8192\n\t" \
  "v_mfma_f32_16x16x32_bf16 v[176:179], v[148:151], v[96:99], v[176:179]\n\t" \
  "ds_read_b64_tr_b16 v[148:149], v63 offset:34816\n\t" \
  "v_mfma_f32_16x16x32_bf16 v[176:179], v[136:139], v[132:135], v[176:179]\n\t" \
  "ds_read_b64_tr_b16 v[150:151], v63 offset:35328\n\t" \
  "v_mfma_f32_16x16x32_bf16 v[156:159], v[76:79], v[68:71], v[156:159]\n\t" \
  "ds_read_b64_tr_b16 v[136:137], v63 offset:38912\n\t" \
  "v_mfma_f32_16x16x32_bf16 v[156:159], v[144:147], v[104:107], v[156:159]\n\t" \
  "ds_read_b64_tr_b16 v[138:139], v63 offset:39424\n\t" \
  "v_mfma_f32_16x16x32_bf16 v[164:167], v[144:147], v[80:83], v[164:167]\n\t" \
  "ds_read_b64_tr_b16 v[68:69], v62 offset:26624\n\t" \
  "v_mfma_f32_16x16x32_bf16 v[164:167], v[76:79], v[116:119], v[164:167]\n\t" \
  "ds_read_b64_tr_b16 v[70:71], v62 offset:27136\n\t" \
  "v_mfma_f32_16x16x32_bf16 v[172:175], v[76:79], v[88:91], v[172:175]\n\t" \
  "ds_read_b64_tr_b16 v[104:105], v62 offset:30720\n\t" \
  "v_mfma_f32_16x16x32_bf16 v[172:175], v[144:147], v[124:127], v[172:175]\n\t" \
  "ds_read_b64_tr_b16 v[106:107], v62 offset:31232\n\t" \
  "v_mfma_f32_16x16x32_bf16 v[180:183], v[144:147], v[96:99], v[180:183]\n\t" \
  "ds_read_b128 v[80:83], v60 offset:0\n\t" \
  "v_mfma_f32_16x16x32_bf16 v[180:183], v[76:79], v[132:135], v[180:183]\n\t" \
  "ds_read_b128 v[116:119], v61 offset:0\n\t" \
  "s_waitcnt lgkmcnt(7)\n\t" \
  "v_mfma_f32_16x16x32_bf16 v[216:219], v[64:67], v[72:75], v[216:219]\n\t" \
  "ds_read_b64_tr_b16 v[88:89], v63 offset:26624\n\t" \
  "v_mfma_f32_16x16x32_bf16 v[216:219], v[140:143], v[108:111], v[216:219]\n\t" \
  "505:\n\t" \
  "s_cbranch_execz 509f\n\t" \
  "ds_read_b64_tr_b16 v[90:91], v63 offset:27136\n\t" \
  "v_mfma_f32_16x16x32_bf16 v[224:227], v[92:95], v[108:111], v[224:227]\n\t" \
  "ds_read_b64_tr_b16 v[124:125], v63 offset:30720\n\t" \
  "v_mfma_f32_16x16x32_bf16 v[224:227], v[84:87], v[72:75], v[224:227]\n\t" \
  "ds_read_b64_tr_b16 v[126:127], v63 offset:31232\n\t" \
  "v_mfma_f32_16x16x32_bf16 v[232:235], v[100:103], v[72:75], v[232:235]\n\t" \
  "ds_read_b64_tr_b16 v[96:97], v62 offset:18432\n\t" \
  "v_mfma_f32_16x16x32_bf16 v[232:235], v[112:115], v[108:111], v[232:235]\n\t" \
  "ds_read_b64_tr_b16 v[98:99], v62 offset:18944\n\t" \
  "v_mfma_f32_16x16x32_bf16 v[240:243], v[128:131], v[108:111], v[240:243]\n\t" \
  "ds_read_b64_tr_b16 v[144:145], v62 offset:22528\n\t" \
  "v_mfma_f32_16x16x32_bf16 v[240:243], v[120:123], v[72:75], v[240:243]\n\t" \
  "ds_read_b64_tr_b16 v[146:147], v62 offset:23040\n\t" \
  "v_mfma_f32_16x16x32_bf16 v[220:223], v[64:67], v[148:151], v[220:223]\n\t" \
  "ds_read_b64_tr_b16 v[76:77], v63 offset:18432\n\t" \
  "s_waitcnt lgkmcnt(7)\n\t" \
  "v_mfma_f32_16x16x32_bf16 v[220:223], v[140:143], v[136:139], v[220:223]\n\t" \
  "ds_read_b64_tr_b16 v[78:79], v63 offset:18944\n\t" \
  "v_mfma_f32_16x16x32_bf16 v[228:231], v[92:95], v[136:139], v[228:231]\n\t" \
  "ds_read_b64_tr_b16 v[132:133], v63 offset:22528\n\t" \
  "v_mfma_f32_16x16x32_bf16 v[228:231], v[84:87], v[148:151], v[228:231]\n\t" \
  "ds_read_b64_tr_b16 v[134:135], v63 offset:23040\n\t" \
  "v_mfma_f32_16x16x32_bf16 v[236:239], v[100:103], v[148:151], v[236:239]\n\t" \
  "v_mfma_f32_16x16x32_bf16 v[236:239], v[112:115], v[136:139], v[236:239]\n\t" \
  "v_mfma_f32_16x16x32_bf16 v[244:247], v[128:131], v[136:139], v[244:247]\n\t" \
  "v_mfma_f32_16x16x32_bf16 v[244:247], v[120:123], v[148:151], v[244:247]\n\t" \
  "v_mfma_f32_16x16x32_bf16 v[184:187], v[68:71], v[64:67], v[184:187]\n\t" \
  "v_mfma_f32_16x16x32_bf16 v[184:187], v[104:107], v[140:143], v[184:187]\n\t" \
  "v_mfma_f32_16x16x32_bf16 v[192:195], v[104:107], v[92:95], v[192:195]\n\t" \
  "v_mfma_f32_16x16x32_bf16 v[192:195], v[68:71], v[84:87], v[192:195]\n\t" \
  "v_mfma_f32_16x16x32_bf16 v[200:203], v[68:71], v[100:103], v[200:203]\n\t" \
  "v_mfma_f32_16x16x32_bf16 v[200:203], v[104:107], v[112:115], v[200:203]\n\t" \
  "v_mfma_f32_16x16x32_bf16 v[208:211], v[104:107], v[128:131], v[208:211]\n\t" \
  "v_mfma_f32_16x16x32_bf16 v[208:211], v[68:71], v[120:123], v[208:211]\n\t" \
  "v_mfma_f32_16x16x32_bf16 v[248:251], v[68:71], v[80:83], v[248:251]\n\t" \
  "v_mfma_f32_16x16x32_bf16 v[248:251], v[104:107], v[116:119], v[248:251]\n\t" \
  "v_mfma_f32_16x16x32_bf16 v[252:255], v[88:91], v[80:83], v[252:255]\n\t" \
  "s_waitcnt lgkmcnt(0)\n\t" \
  "v_mfma_f32_16x16x32_bf16 v[252:255], v[124:127], v[116:119], v[252:255]\n\t" \
  "v_mfma_f32_16x16x32_bf16 v[188:191], v[124:127], v[140:143], v[188:191]\n\t" \
  "v_mfma_f32_16x16x32_bf16 v[188:191], v[88:91], v[64:67], v[188:191]\n\t" \
  "s_barrier\n\t" \
  "v_mfma_f32_16x16x32_bf16 v[196:199], v[88:91], v[84:87], v[196:199]\n\t" \
  "ds_read_b64_tr_b16 v[108:109], v54 offset:34816\n\t" \
  "v_mfma_f32_16x16x32_bf16 v[196:199], v[124:127], v[92:95], v[196:199]\n\t" \
  "ds_read_b64_tr_b16 v[110:111], v54 offset:35328\n\t" \
  "v_mfma_f32_16x16x32_bf16 v[204:207], v[124:127], v[112:115], v[204:207]\n\t" \
  "ds_read_b128 v[72:75], v52 offset:2048\n\t" \
  "v_mfma_f32_16x16x32_bf16 v[204:207], v[88:91], v[100:103], v[204:207]\n\t" \
  "ds_read_b64_tr_b16 v[136:137], v54 offset:38912\n\t" \
  "v_mfma_f32_16x16x32_bf16 v[212:215], v[88:91], v[120:123], v[212:215]\n\t" \
  "ds_read_b64_tr_b16 v[138:139], v54 offset:39424\n\t" \
  "v_mfma_f32_16x16x32_bf16 v[212:215], v[124:127], v[128:131], v[212:215]\n\t" \
  "ds_read_b128 v[148:151], v53 offset:2048\n\t" \
  "v_mfma_f32_16x16x32_bf16 v[152:155], v[96:99], v[64:67], v[152:155]\n\t" \
  "ds_read_b128 v[68:71], v53 offset:4096\n\t" \
  "v_mfma_f32_16x16x32_bf16 v[152:155], v[144:147], v[140:143], v[152:155]\n\t" \
  "ds_read_b128 v[104:107], v52 offset:4096\n\t" \
  "v_mfma_f32_16x16x32_bf16 v[160:163], v[144:147], v[92:95], v[160:163]\n\t" \
  "ds_read_b128 v[80:83], v52 offset:6144\n\t" \
  "v_mfma_f32_16x16x32_bf16 v[160:163], v[96:99], v[84:87], v[160:163]\n\t" \
  "ds_read_b128 v[116:119], v53 offset:6144\n\t" \
  "v_mfma_f32_16x16x32_bf16 v[168:171], v[96:99], v[100:103], v[168:171]\n\t" \
  "ds_read_b128 v[88:91], v53 offset:8192\n\t" \
  "v_mfma_f32_16x16x32_bf16 v[168:171], v[144:147], v[112:115], v[168:171]\n\t" \
  "ds_read_b128 v[124:127], v52 offset:8192\n\t" \
  "v_mfma_f32_16x16x32_bf16 v[176:179], v[144:147], v[128:131], v[176:179]\n\t" \
  "ds_read_b64_tr_b16 v[144:145], v55 offset:34816\n\t" \
  "v_mfma_f32_16x16x32_bf16 v[176:179], v[96:99], v[120:123], v[176:179]\n\t" \
  "ds_read_b64_tr_b16 v[146:147], v55 offset:35328\n\t" \
  "v_mfma_f32_16x16x32_bf16 v[156:159], v[76:79], v[64:67], v[156:159]\n\t" \
  "ds_read_b64_tr_b16 v[96:97], v55 offset:38912\n\t" \
  "v_mfma_f32_16x16x32_bf16 v[156:159], v[132:135], v[140:143], v[156:159]\n\t" \
  "ds_read_b64_tr_b16 v[98:99], v55 offset:39424\n\t" \
  "v_mfma_f32_16x16x32_bf16 v[164:167], v[132:135], v[92:95], v[164:167]\n\t" \
  "ds_read_b64_tr_b16 v[64:65], v54 offset:26624\n\t" \
  "v_mfma_f32_16x16x32_bf16 v[164:167], v[76:79], v[84:87], v[164:167]\n\t" \
  "ds_read_b64_tr_b16 v[66:67], v54 offset:27136\n\t" \
  "v_mfma_f32_16x16x32_bf16 v[172:175], v[76:79], v[100:103], v[172:175]\n\t" \
  "ds_read_b64_tr_b16 v[140:141], v54 offset:30720\n\t" \
  "v_mfma_f32_16x16x32_bf16 v[172:175], v[132:135], v[112:115], v[172:175]\n\t" \
  "ds_read_b64_tr_b16 v[142:143], v54 offset:31232\n\t" \
  "v_mfma_f32_16x16x32_bf16 v[180:183], v[132:135], v[128:131], v[180:183]\n\t" \
  "ds_read_b128 v[92:95], v52 offset:0\n\t" \
  "v_mfma_f32_16x16x32_bf16 v[180:183], v[76:79], v[120:123], v[180:183]\n\t" \
  "ds_read_b128 v[84:87], v53 offset:0\n\t" \
  "s_waitcnt lgkmcnt(7)\n\t" \
  "v_mfma_f32_16x16x32_bf16 v[216:219], v[72:75], v[108:111], v[216:219]\n\t" \
  "ds_read_b64_tr_b16 v[100:101], v55 offset:26624\n\t" \
  "v_mfma_f32_16x16x32_bf16 v[216:219], v[148:151], v[136:139], v[216:219]\n\t" \
  "ds_read_b64_tr_b16 v[102:103], v55 offset:27136\n\t" \
  "v_mfma_f32_16x16x32_bf16 v[224:227], v[68:71], v[136:139], v[224:227]\n\t" \
  "ds_read_b64_tr_b16 v[112:113], v55 offset:30720\n\t" \
  "v_mfma_f32_16x16x32_bf16 v[224:227], v[104:107], v[108:111], v[224:227]\n\t" \
  "ds_read_b64_tr_b16 v[114:115], v55 offset:31232\n\t" \
  "v_mfma_f32_16x16x32_bf16 v[232:235], v[80:83], v[108:111], v[232:235]\n\t" \
  "ds_read_b64_tr_b16 v[128:129], v54 offset:18432\n\t" \
  "v_mfma_f32_16x16x32_bf16 v[232:235], v[116:119], v[136:139], v[232:235]\n\t" \
  "ds_read_b64_tr_b16 v[130:131], v54 offset:18944\n\t" \
  "v_mfma_f32_16x16x32_bf16 v[240:243], v[88:91], v[136:139], v[240:243]\n\t" \
  "ds_read_b64_tr_b16 v[132:133], v54 offset:22528\n\t" \
  "v_mfma_f32_16x16x32_bf16 v[240:243], v[124:127], v[108:111], v[240:243]\n\t" \
  "ds_read_b64_tr_b16 v[134:135], v54 offset:23040\n\t" \
  "v_mfma_f32_16x16x32_bf16 v[220:223], v[72:75], v[144:147], v[220:223]\n\t" \
  "ds_read_b64_tr_b16 v[76:77], v55 offset:18432\n\t" \
  "s_waitcnt lgkmcnt(7)\n\t" \
  "v_mfma_f32_16x16x32_bf16 v[220:223], v[148:151], v[96:99], v[220:223]\n\t" \
  "ds_read_b64_tr_b16 v[78:79], v55 offset:18944\n\t" \
  "v_mfma_f32_16x16x32_bf16 v[228:231], v[68:71], v[96:99], v[228:231]\n\t" \
  "ds_read_b64_tr_b16 v[120:121], v55 offset:22528\n\t" \
  "v_mfma_f32_16x16x32_bf16 v[228:231], v[104:107], v[144:147], v[228:231]\n\t" \
  "ds_read_b64_tr_b16 v[122:123], v55 offset:23040\n\t" \
  "v_mfma_f32_16x16x32_bf16 v[236:239], v[80:83], v[144:147], v[236:239]\n\t" \
  "v_mfma_f32_16x16x32_bf16 v[236:239], v[116:119], v[96:99], v[236:239]\n\t" \
  "v_mfma_f32_16x16x32_bf16 v[244:247], v[88:91], v[96:99], v[244:247]\n\t" \
  "v_mfma_f32_16x16x32_bf16 v[244:247], v[124:127], v[144:147], v[244:247]\n\t" \
  "v_mfma_f32_16x16x32_bf16 v[184:187], v[64:67], v[72:75], v[184:187]\n\t" \
  "v_mfma_f32_16x16x32_bf16 v[184:187], v[140:143], v[148:151], v[184:187]\n\t" \
  "v_mfma_f32_16x16x32_bf16 v[192:195], v[140:143], v[68:71], v[192:195]\n\t" \
  "v_mfma_f32_16x16x32_bf16 v[192:195], v[64:67], v[104:107], v[192:195]\n\t" \
  "v_mfma_f32_16x16x32_bf16 v[200:203], v[64:67], v[80:83], v[200:203]\n\t" \
  "v_mfma_f32_16x16x32_bf16 v[200:203], v[140:143], v[116:119], v[200:203]\n\t" \
  "v_mfma_f32_16x16x32_bf16 v[208:211], v[140:143], v[88:91], v[208:211]\n\t" \
  "v_mfma_f32_16x16x32_bf16 v[208:211], v[64:67], v[124:127], v[208:211]\n\t" \
  "v_mfma_f32_16x16x32_bf16 v[248:251], v[64:67], v[92:95], v[248:251]\n\t" \
  "v_mfma_f32_16x16x32_bf16 v[248:251], v[140:143], v[84:87], v[248:251]\n\t" \
  "v_mfma_f32_16x16x32_bf16 v[252:255], v[100:103], v[92:95], v[252:255]\n\t" \
  "s_waitcnt lgkmcnt(0)\n\t" \
  "v_mfma_f32_16x16x32_bf16 v[252:255], v[112:115], v[84:87], v[252:255]\n\t" \
  "v_mfma_f32_16x16x32_bf16 v[188:191], v[112:115], v[148:151], v[188:191]\n\t" \
  "v_mfma_f32_16x16x32_bf16 v[188:191], v[100:103], v[72:75], v[188:191]\n\t" \
  "s_barrier\n\t" \
  "v_mfma_f32_16x16x32_bf16 v[196:199], v[100:103], v[104:107], v[196:199]\n\t" \
  "ds_read_b64_tr_b16 v[96:97], v58 offset:34816\n\t" \
  "v_mfma_f32_16x16x32_bf16 v[196:199], v[112:115], v[68:71], v[196:199]\n\t" \
  "ds_read_b64_tr_b16 v[98:99], v58 offset:35328\n\t" \
  "v_mfma_f32_16x16x32_bf16 v[204:207], v[112:115], v[116:119], v[204:207]\n\t" \
  "ds_read_b128 v[136:139], v56 offset:2048\n\t" \
  "v_mfma_f32_16x16x32_bf16 v[204:207], v[100:103], v[80:83], v[204:207]\n\t" \
  "ds_read_b64_tr_b16 v[64:65], v58 offset:38912\n\t" \
  "v_mfma_f32_16x16x32_bf16 v[212:215], v[100:103], v[124:127], v[212:215]\n\t" \
  "ds_read_b64_tr_b16 v[66:67], v58 offset:39424\n\t" \
  "v_mfma_f32_16x16x32_bf16 v[212:215], v[112:115], v[88:91], v[212:215]\n\t" \
  "ds_read_b128 v[108:111], v57 offset:2048\n\t" \
  "v_mfma_f32_16x16x32_bf16 v[152:155], v[128:131], v[72:75], v[152:155]\n\t" \
  "ds_read_b128 v[144:147], v57 offset:4096\n\t" \
  "v_mfma_f32_16x16x32_bf16 v[152:155], v[132:135], v[148:151], v[152:155]\n\t" \
  "ds_read_b128 v[140:143], v56 offset:4096\n\t" \
  "v_mfma_f32_16x16x32_bf16 v[160:163], v[132:135], v[68:71], v[160:163]\n\t" \
  "ds_read_b128 v[112:115], v56 offset:6144\n\t" \
  "v_mfma_f32_16x16x32_bf16 v[160:163], v[128:131], v[104:107], v[160:163]\n\t" \
  "ds_read_b64_tr_b16 v[92:93], v59 offset:34816\n\t" \
  "v_mfma_f32_16x16x32_bf16 v[168:171], v[128:131], v[80:83], v[168:171]\n\t" \
  "ds_read_b64_tr_b16 v[94:95], v59 offset:35328\n\t" \
  "v_mfma_f32_16x16x32_bf16 v[168:171], v[132:135], v[116:119], v[168:171]\n\t" \
  "ds_read_b64_tr_b16 v[84:85], v59 offset:38912\n\t" \
  "v_mfma_f32_16x16x32_bf16 v[176:179], v[132:135], v[88:91], v[176:179]\n\t" \
  "ds_read_b128 v[132:135], v57 offset:6144\n\t" \
  "v_mfma_f32_16x16x32_bf16 v[176:179], v[128:131], v[124:127], v[176:179]\n\t" \
  "ds_read_b128 v[128:131], v57 offset:8192\n\t" \
  "v_mfma_f32_16x16x32_bf16 v[156:159], v[76:79], v[72:75], v[156:159]\n\t" \
  "ds_read_b64_tr_b16 v[86:87], v59 offset:39424\n\t" \
  "v_mfma_f32_16x16x32_bf16 v[156:159], v[120:123], v[148:151], v[156:159]\n\t" \
  "ds_read_b128 v[148:151], v56 offset:8192\n\t" \
  "v_mfma_f32_16x16x32_bf16 v[164:167], v[120:123], v[68:71], v[164:167]\n\t" \
  "ds_read_b64_tr_b16 v[100:101], v58 offset:26624\n\t" \
  "v_mfma_f32_16x16x32_bf16 v[164:167], v[76:79], v[104:107], v[164:167]\n\t" \
  "ds_read_b64_tr_b16 v[102:103], v58 offset:27136\n\t" \
  "v_mfma_f32_16x16x32_bf16 v[172:175], v[76:79], v[80:83], v[172:175]\n\t" \
  "ds_read_b64_tr_b16 v[72:73], v58 offset:30720\n\t" \
  "v_mfma_f32_16x16x32_bf16 v[172:175], v[120:123], v[116:119], v[172:175]\n\t" \
  "ds_read_b64_tr_b16 v[74:75], v58 offset:31232\n\t" \
  "v_mfma_f32_16x16x32_bf16 v[180:183], v[120:123], v[88:91], v[180:183]\n\t" \
  "ds_read_b128 v[68:71], v56 offset:0\n\t" \
  "v_mfma_f32_16x16x32_bf16 v[180:183], v[76:79], v[124:127], v[180:183]\n\t" \
  "ds_read_b128 v[104:107], v57 offset:0\n\t" \
  "s_waitcnt lgkmcnt(7)\n\t" \
  "v_mfma_f32_16x16x32_bf16 v[216:219], v[136:139], v[96:99], v[216:219]\n\t" \
  "ds_read_b64_tr_b16 v[80:81], v59 offset:26624\n\t" \
  "v_mfma_f32_16x16x32_bf16 v[216:219], v[108:111], v[64:67], v[216:219]\n\t" \
  "ds_read_b64_tr_b16 v[82:83], v59 offset:27136\n\t" \
  "v_mfma_f32_16x16x32_bf16 v[224:227], v[144:147], v[64:67], v[224:227]\n\t" \
  "ds_read_b64_tr_b16 v[88:89], v59 offset:30720\n\t" \
  "v_mfma_f32_16x16x32_bf16 v[224:227], v[140:143], v[96:99], v[224:227]\n\t" \
  "ds_read_b64_tr_b16 v[90:91], v59 offset:31232\n\t" \
  "v_mfma_f32_16x16x32_bf16 v[232:235], v[112:115], v[96:99], v[232:235]\n\t" \
  "ds_read_b64_tr_b16 v[116:117], v58 offset:18432\n\t" \
  "v_mfma_f32_16x16x32_bf16 v[232:235], v[132:135], v[64:67], v[232:235]\n\t" \
  "ds_read_b64_tr_b16 v[118:119], v58 offset:18944\n\t" \
  "v_mfma_f32_16x16x32_bf16 v[240:243], v[128:131], v[64:67], v[240:243]\n\t" \
  "ds_read_b64_tr_b16 v[120:121], v58 offset:22528\n\t" \
  "s_waitcnt lgkmcnt(7)\n\t" \
  "v_mfma_f32_16x16x32_bf16 v[240:243], v[148:151], v[96:99], v[240:243]\n\t" \
  "ds_read_b64_tr_b16 v[122:123], v58 offset:23040\n\t" \
  "v_mfma_f32_16x16x32_bf16 v[220:223], v[136:139], v[92:95], v[220:223]\n\t" \
  "ds_read_b64_tr_b16 v[124:125], v59 offset:18432\n\t" \
  "v_mfma_f32_16x16x32_bf16 v[220:223], v[108:111], v[84:87], v[220:223]\n\t" \
  "ds_read_b64_tr_b16 v[126:127], v59 offset:18944\n\t" \
  "v_mfma_f32_16x16x32_bf16 v[228:231], v[144:147], v[84:87], v[228:231]\n\t" \
  "ds_read_b64_tr_b16 v[76:77], v59 offset:22528\n\t" \
  "v_mfma_f32_16x16x32_bf16 v[228:231], v[140:143], v[92:95], v[228:231]\n\t" \
  "ds_read_b64_tr_b16 v[78:79], v59 offset:23040\n\t" \
  "v_mfma_f32_16x16x32_bf16 v[236:239], v[112:115], v[92:95], v[236:239]\n\t" \
  "v_mfma_f32_16x16x32_bf16 v[236:239], v[132:135], v[84:87], v[236:239]\n\t" \
  "v_mfma_f32_16x16x32_bf16 v[244:247], v[128:131], v[84:87], v[244:247]\n\t" \
  "v_mfma_f32_16x16x32_bf16 v[244:247], v[148:151], v[92:95], v[244:247]\n\t" \
  "v_mfma_f32_16x16x32_bf16 v[184:187], v[100:103], v[136:139], v[184:187]\n\t" \
  "v_mfma_f32_16x16x32_bf16 v[184:187], v[72:75], v[108:111], v[184:187]\n\t" \
  "v_mfma_f32_16x16x32_bf16 v[192:195], v[72:75], v[144:147], v[192:195]\n\t" \
  "v_and_b32 v14, -16, v32\n\t" \
  "v_mfma_f32_16x16x32_bf16 v[192:195], v[100:103], v[140:143], v[192:195]\n\t" \
  "v_mfma_f32_16x16x32_bf16 v[200:203], v[100:103], v[112:115], v[200:203]\n\t" \
  "ds_read_b128 v[10:13], v14 offset:128\n\t" \
  "v_mfma_f32_16x16x32_bf16 v[200:203], v[72:75], v[132:135], v[200:203]\n\t" \
  "v_mfma_f32_16x16x32_bf16 v[208:211], v[72:75], v[128:131], v[208:211]\n\t" \
  "v_mfma_f32_16x16x32_bf16 v[208:211], v[100:103], v[148:151], v[208:211]\n\t" \
  "v_mfma_f32_16x16x32_bf16 v[248:251], v[100:103], v[68:71], v[248:251]\n\t" \
  "v_mfma_f32_16x16x32_bf16 v[248:251], v[72:75], v[104:107], v[248:251]\n\t" \
  "s_waitcnt lgkmcnt(1)\n\t" \
  "v_mfma_f32_16x16x32_bf16 v[252:255], v[80:83], v[68:71], v[252:255]\n\t" \
  "s_waitcnt lgkmcnt(0)\n\t" \
  "v_and_b32 v10, v10, v11\n\t" \
  "v_and_b32 v12, v12, v13\n\t" \
  "v_mfma_f32_16x16x32_bf16 v[252:255], v[88:91], v[104:107], v[252:255]\n\t" \
  "v_and_b32 v10, v10, v12\n\t" \
  "v_mfma_f32_16x16x32_bf16 v[188:191], v[88:91], v[108:111], v[188:191]\n\t" \
  "v_readfirstlane_b32 vcc_lo, v10\n\t" \
  "v_mfma_f32_16x16x32_bf16 v[188:191], v[80:83], v[136:139], v[188:191]\n\t" \
  "ds_write_b32 v32, v0 offset:144\n\t" \
  "s_cmp_eq_u32 vcc_lo, 0\n\t" \
  "s_cbranch_scc1 15f\n\t" \
  "14:\n\t" \
  "v_mfma_f32_16x16x32_bf16 v[196:199], v[80:83], v[140:143], v[196:199]\n\t" \
  "ds_read_b64_tr_b16 v[64:65], v62 offset:26624\n\t" \
  "v_mfma_f32_16x16x32_bf16 v[196:199], v[88:91], v[144:147], v[196:199]\n\t" \
  "ds_read_b64_tr_b16 v[66:67], v62 offset:27136\n\t" \
  "v_mfma_f32_16x16x32_bf16 v[204:207], v[88:91], v[132:135], v[204:207]\n\t" \
  "ds_read_b128 v[96:99], v60 offset:0\n\t" \
  "v_mfma_f32_16x16x32_bf16 v[204:207], v[80:83], v[112:115], v[204:207]\n\t" \
  "ds_read_b128 v[84:87], v60 offset:2048\n\t" \
  "v_mfma_f32_16x16x32_bf16 v[212:215], v[80:83], v[148:151], v[212:215]\n\t" \
  "ds_read_b128 v[92:95], v60 offset:4096\n\t" \
  "v_mfma_f32_16x16x32_bf16 v[212:215], v[88:91], v[128:131], v[212:215]\n\t" \
  "ds_read_b128 v[100:103], v60 offset:6144\n\t" \
  "v_mfma_f32_16x16x32_bf16 v[152:155], v[116:119], v[136:139], v[152:155]\n\t" \
  "ds_read_b128 v[72:75], v60 offset:8192\n\t" \
  "v_mfma_f32_16x16x32_bf16 v[152:155], v[120:123], v[108:111], v[152:155]\n\t" \
  "ds_read_b64_tr_b16 v[68:69], v63 offset:26624\n\t" \
  "v_mfma_f32_16x16x32_bf16 v[160:163], v[120:123], v[144:147], v[160:163]\n\t" \
  "ds_read_b64_tr_b16 v[70:71], v63 offset:27136\n\t" \
  "v_mfma_f32_16x16x32_bf16 v[160:163], v[116:119], v[140:143], v[160:163]\n\t" \
  "ds_read_b64_tr_b16 v[104:105], v62 offset:34816\n\t" \
  "v_mfma_f32_16x16x32_bf16 v[168:171], v[116:119], v[112:115], v[168:171]\n\t" \
  "ds_read_b64_tr_b16 v[106:107], v62 offset:35328\n\t" \
  "v_mfma_f32_16x16x32_bf16 v[168:171], v[120:123], v[132:135], v[168:171]\n\t" \
  "ds_read_b64_tr_b16 v[80:81], v63 offset:34816\n\t" \
  "v_mfma_f32_16x16x32_bf16 v[176:179], v[120:123], v[128:131], v[176:179]\n\t" \
  "ds_read_b64_tr_b16 v[82:83], v63 offset:35328\n\t" \
  "v_mfma_f32_16x16x32_bf16 v[176:179], v[116:119], v[148:151], v[176:179]\n\t" \
  "ds_read_b64_tr_b16 v[88:89], v62 offset:18432\n\t" \
  "v_mfma_f32_16x16x32_bf16 v[156:159], v[124:127], v[136:139], v[156:159]\n\t" \
  "ds_read_b64_tr_b16 v[90:91], v62 offset:18944\n\t" \
  "v_mfma_f32_16x16x32_bf16 v[156:159], v[76:79], v[108:111], v[156:159]\n\t" \
  "ds_read_b64_tr_b16 v[120:121], v62 offset:30720\n\t" \
  "v_mfma_f32_16x16x32_bf16 v[164:167], v[76:79], v[144:147], v[164:167]\n\t" \
  "ds_read_b64_tr_b16 v[122:123], v62 offset:31232\n\t" \
  "v_mfma_f32_16x16x32_bf16 v[164:167], v[124:127], v[140:143], v[164:167]\n\t" \
  "ds_read_b128 v[116:119], v61 offset:0\n\t" \
  "v_mfma_f32_16x16x32_bf16 v[172:175], v[124:127], v[112:115], v[172:175]\n\t" \
  "ds_read_b128 v[136:139], v61 offset:2048\n\t" \
  "v_mfma_f32_16x16x32_bf16 v[172:175], v[76:79], v[132:135], v[172:175]\n\t" \
  "ds_read_b128 v[108:111], v61 offset:4096\n\t" \
  "v_mfma_f32_16x16x32_bf16 v[180:183], v[76:79], v[128:131], v[180:183]\n\t" \
  "ds_read_b64_tr_b16 v[76:77], v63 offset:18432\n\t" \
  "v_mfma_f32_16x16x32_bf16 v[180:183], v[124:127], v[148:151], v[180:183]\n\t" \
  "ds_read_b64_tr_b16 v[78:79], v63 offset:18944\n\t" \
  "s_waitcnt lgkmcnt(7)\n\t" \
  "v_mfma_f32_16x16x32_bf16 v[248:251], v[64:67], v[96:99], v[248:251]\n\t" \
  "ds_read_b128 v[144:147], v61 offset:6144\n\t" \
  "v_mfma_f32_16x16x32_bf16 v[184:187], v[64:67], v[84:87], v[184:187]\n\t" \
  "ds_read_b128 v[140:143], v61 offset:8192\n\t" \
  "v_mfma_f32_16x16x32_bf16 v[192:195], v[64:67], v[92:95], v[192:195]\n\t" \
  "ds_read_b64_tr_b16 v[112:113], v63 offset:30720\n\t" \
  "v_mfma_f32_16x16x32_bf16 v[200:203], v[64:67], v[100:103], v[200:203]\n\t" \
  "ds_read_b64_tr_b16 v[114:115], v63 offset:31232\n\t" \
  "v_mfma_f32_16x16x32_bf16 v[208:211], v[64:67], v[72:75], v[208:211]\n\t" \
  "ds_read_b64_tr_b16 v[132:133], v62 offset:22528\n\t" \
  "v_mfma_f32_16x16x32_bf16 v[252:255], v[68:71], v[96:99], v[252:255]\n\t" \
  "ds_read_b64_tr_b16 v[134:135], v62 offset:23040\n\t" \
  "v_mfma_f32_16x16x32_bf16 v[188:191], v[68:71], v[84:87], v[188:191]\n\t" \
  "ds_read_b64_tr_b16 v[128:129], v63 offset:22528\n\t" \
  "v_mfma_f32_16x16x32_bf16 v[196:199], v[68:71], v[92:95], v[196:199]\n\t" \
  "ds_read_b64_tr_b16 v[130:131], v63 offset:23040\n\t" \
  "v_mfma_f32_16x16x32_bf16 v[204:207], v[68:71], v[100:103], v[204:207]\n\t" \
  "ds_read_b64_tr_b16 v[124:125], v62 offset:38912\n\t" \
  "v_mfma_f32_16x16x32_bf16 v[212:215], v[68:71], v[72:75], v[212:215]\n\t" \
  "ds_read_b64_tr_b16 v[126:127], v62 offset:39424\n\t" \
  "v_mfma_f32_16x16x32_bf16 v[216:219], v[84:87], v[104:107], v[216:219]\n\t" \
  "ds_read_b64_tr_b16 v[148:149], v63 offset:38912\n\t" \
  "v_mfma_f32_16x16x32_bf16 v[224:227], v[92:95], v[104:107], v[224:227]\n\t" \
  "ds_read_b64_tr_b16 v[150:151], v63 offset:39424\n\t" \
  "v_mfma_f32_16x16x32_bf16 v[232:235], v[100:103], v[104:107], v[232:235]\n\t" \
  "v_mfma_f32_16x16x32_bf16 v[240:243], v[72:75], v[104:107], v[240:243]\n\t" \
  "v_mfma_f32_16x16x32_bf16 v[220:223], v[84:87], v[80:83], v[220:223]\n\t" \
  "v_mfma_f32_16x16x32_bf16 v[228:231], v[92:95], v[80:83], v[228:231]\n\t" \
  "v_mfma_f32_16x16x32_bf16 v[236:239], v[100:103], v[80:83], v[236:239]\n\t" \
  "v_mfma_f32_16x16x32_bf16 v[244:247], v[72:75], v[80:83], v[244:247]\n\t" \
  "v_mfma_f32_16x16x32_bf16 v[152:155], v[88:91], v[84:87], v[152:155]\n\t" \
  "v_mfma_f32_16x16x32_bf16 v[160:163], v[88:91], v[92:95], v[160:163]\n\t" \
  "v_mfma_f32_16x16x32_bf16 v[168:171], v[88:91], v[100:103], v[168:171]\n\t" \
  "v_mfma_f32_16x16x32_bf16 v[176:179], v[88:91], v[72:75], v[176:179]\n\t" \
  "s_waitcnt lgkmcnt(0)\n\t" \
  "v_mfma_f32_16x16x32_bf16 v[156:159], v[76:79], v[84:87], v[156:159]\n\t" \
  "v_mfma_f32_16x16x32_bf16 v[164:167], v[76:79], v[92:95], v[164:167]\n\t" \
  "ds_write_b32 v32, v0\n\t" \
  "v_mfma_f32_16x16x32_bf16 v[172:175], v[76:79], v[100:103], v[172:175]\n\t" \
  "v_mfma_f32_16x16x32_bf16 v[180:183], v[76:79], v[72:75], v[180:183]\n\t" \
  "v_mfma_f32_16x16x32_bf16 v[248:251], v[120:123], v[116:119], v[248:251]\n\t" \
  "v_and_b32 v56, -16, v32\n\t" \
  "v_mfma_f32_16x16x32_bf16 v[184:187], v[120:123], v[136:139], v[184:187]\n\t" \
  "v_mfma_f32_16x16x32_bf16 v[192:195], v[120:123], v[108:111], v[192:195]\n\t" \
  "v_mfma_f32_16x16x32_bf16 v[200:203], v[120:123], v[144:147], v[200:203]\n\t" \
  "v_mfma_f32_16x16x32_bf16 v[208:211], v[120:123], v[140:143], v[208:211]\n\t" \
  "v_mfma_f32_16x16x32_bf16 v[252:255], v[112:115], v[116:119], v[252:255]\n\t" \
  "v_mfma_f32_16x16x32_bf16 v[188:191], v[112:115], v[136:139], v[188:191]\n\t" \
  "v_mfma_f32_16x16x32_bf16 v[196:199], v[112:115], v[108:111], v[196:199]\n\t" \
  "v_cvt_pk_bf16_f32 v10, v248, v249\n\t" \
  "v_cvt_pk_bf16_f32 v11, v250, v251\n\t" \
  "v_mfma_f32_16x16x32_bf16 v[204:207], v[112:115], v[144:147], v[204:207]\n\t" \
  "v_cvt_pk_bf16_f32 v14, v184, v185\n\t" \
  "v_cvt_pk_bf16_f32 v15, v186, v187\n\t" \
  "v_mfma_f32_16x16x32_bf16 v[212:215], v[112:115], v[140:143], v[212:215]\n\t" \
  "v_cvt_pk_bf16_f32 v12, v252, v253\n\t" \
  "v_cvt_pk_bf16_f32 v13, v254, v255\n\t" \
  "v_mfma_f32_16x16x32_bf16 v[152:155], v[132:135], v[136:139], v[152:155]\n\t" \
  "ds_write_b128 v30, v[10:13]\n\t" \
  "ds_write_b32 v32, v0 offset:48\n\t" \
  "v_mfma_f32_16x16x32_bf16 v[160:163], v[132:135], v[108:111], v[160:163]\n\t" \
  "v_cvt_pk_bf16_f32 v16, v188, v189\n\t" \
  "v_cvt_pk_bf16_f32 v17, v190, v191\n\t" \
  "v_mfma_f32_16x16x32_bf16 v[168:171], v[132:135], v[144:147], v[168:171]\n\t" \
  "v_cvt_pk_bf16_f32 v18, v192, v193\n\t" \
  "v_cvt_pk_bf16_f32 v19, v194, v195\n\t" \
  "v_mfma_f32_16x16x32_bf16 v[176:179], v[132:135], v[140:143], v[176:179]\n\t" \
  "v_cvt_pk_bf16_f32 v20, v196, v197\n\t" \
  "v_cvt_pk_bf16_f32 v21, v198, v199\n\t" \
  "v_mfma_f32_16x16x32_bf16 v[156:159], v[128:131], v[136:139], v[156:159]\n\t" \
  "v_cvt_pk_bf16_f32 v22, v200, v201\n\t" \
  "v_cvt_pk_bf16_f32 v23, v202, v203\n\t" \
  "v_mfma_f32_16x16x32_bf16 v[164:167], v[128:131], v[108:111], v[164:167]\n\t" \
  "v_cvt_pk_bf16_f32 v24, v204, v205\n\t" \
  "v_cvt_pk_bf16_f32 v25, v206, v207\n\t" \
  "v_mfma_f32_16x16x32_bf16 v[172:175], v[128:131], v[144:147], v[172:175]\n\t" \
  "v_cvt_pk_bf16_f32 v26, v208, v209\n\t" \
  "v_cvt_pk_bf16_f32 v27, v210, v211\n\t" \
  "v_mfma_f32_16x16x32_bf16 v[180:183], v[128:131], v[140:143], v[180:183]\n\t" \
  "v_cvt_pk_bf16_f32 v28, v212, v213\n\t" \
  "v_cvt_pk_bf16_f32 v29, v214, v215\n\t" \
  "v_mfma_f32_16x16x32_bf16 v[216:219], v[136:139], v[124:127], v[216:219]\n\t" \
  "v_cvt_pk_bf16_f32 v84, v152, v153\n\t" \
  "v_cvt_pk_bf16_f32 v85, v154, v155\n\t" \
  "v_mfma_f32_16x16x32_bf16 v[224:227], v[108:111], v[124:127], v[224:227]\n\t" \
  "v_cvt_pk_bf16_f32 v86, v156, v157\n\t" \
  "v_cvt_pk_bf16_f32 v87, v158, v159\n\t" \
  "v_mfma_f32_16x16x32_bf16 v[232:235], v[144:147], v[124:127], v[232:235]\n\t" \
  "v_cvt_pk_bf16_f32 v88, v160, v161\n\t" \
  "v_cvt_pk_bf16_f32 v89, v162, v163\n\t" \
  "v_mfma_f32_16x16x32_bf16 v[240:243], v[140:143], v[124:127], v[240:243]\n\t" \
  "v_cvt_pk_bf16_f32 v90, v164, v165\n\t" \
  "v_cvt_pk_bf16_f32 v91, v166, v167\n\t" \
  "v_mfma_f32_16x16x32_bf16 v[220:223], v[136:139], v[148:151], v[220:223]\n\t" \
  "v_cvt_pk_bf16_f32 v92, v168, v169\n\t" \
  "v_cvt_pk_bf16_f32 v93, v170, v171\n\t" \
  "v_mfma_f32_16x16x32_bf16 v[228:231], v[108:111], v[148:151], v[228:231]\n\t" \
  "v_cvt_pk_bf16_f32 v94, v172, v173\n\t" \
  "v_cvt_pk_bf16_f32 v95, v174, v175\n\t" \
  "v_mfma_f32_16x16x32_bf16 v[236:239], v[144:147], v[148:151], v[236:239]\n\t" \
  "v_cvt_pk_bf16_f32 v96, v176, v177\n\t" \
  "v_cvt_pk_bf16_f32 v97, v178, v179\n\t" \
  "v_mfma_f32_16x16x32_bf16 v[244:247], v[140:143], v[148:151], v[244:247]\n\t" \
  "v_cvt_pk_bf16_f32 v98, v180, v181\n\t" \
  "v_cvt_pk_bf16_f32 v99, v182, v183\n\t" \
  "v_mfma_f32_16x16x32_bf16 v[200:203], v[22:25], v[92:95], v[34:37]\n\t" \
  "v_cvt_pk_bf16_f32 v66, v232, v233\n\t" \
  "v_cvt_pk_bf16_f32 v67, v234, v235\n\t" \
  "v_mfma_f32_16x16x32_bf16 v[208:211], v[26:29], v[96:99], v[34:37]\n\t" \
  "v_cvt_pk_bf16_f32 v76, v236, v237\n\t" \
  "v_cvt_pk_bf16_f32 v77, v238, v239\n\t" \
  "v_mfma_f32_16x16x32_bf16 v[212:215], v[22:25], v[96:99], v[38:41]\n\t" \
  "v_cvt_pk_bf16_f32 v64, v240, v241\n\t" \
  "v_cvt_pk_bf16_f32 v65, v242, v243\n\t" \
  "v_mfma_f32_16x16x32_bf16 v[204:207], v[18:21], v[92:95], v[38:41]\n\t" \
  "v_cvt_pk_bf16_f32 v74, v244, v245\n\t" \
  "v_cvt_pk_bf16_f32 v75, v246, v247\n\t" \
  "v_mfma_f32_16x16x32_bf16 v[192:195], v[18:21], v[88:91], v[34:37]\n\t" \
  "v_cvt_pk_bf16_f32 v68, v224, v225\n\t" \
  "v_cvt_pk_bf16_f32 v69, v226, v227\n\t" \
  "v_mfma_f32_16x16x32_bf16 v[196:199], v[14:17], v[88:91], v[38:41]\n\t" \
  "v_max_f32 v208, v208, v212\n\t" \
  "v_max_f32 v209, v209, v213\n\t" \
  "v_mfma_f32_16x16x32_bf16 v[184:187], v[14:17], v[84:87], v[34:37]\n\t" \
  "v_max_f32 v210, v210, v214\n\t" \
  "v_max_f32 v200, v200, v204\n\t" \
  "v_mfma_f32_16x16x32_bf16 v[188:191], v[10:13], v[84:87], v[42:45]\n\t" \
  "v_max_f32 v201, v201, v205\n\t" \
  "v_max_f32 v202, v202, v206\n\t" \
  "v_max_f32 v192, v192, v196\n\t" \
  "v_max_f32 v193, v193, v197\n\t" \
  "v_max_f32 v194, v194, v198\n\t" \
  "v_max_f32 v211, v211, v215\n\t" \
  "ds_read_b32 v52, v33 offset:48\n\t" \
  "ds_read_b64 v[72:73], v31\n\t" \
  "ds_read_b64 v[82:83], v31 offset:8\n\t" \
  "v_max3_f32 v106, v208, v209, v210\n\t" \
  "v_max_f32 v184, v184, v188\n\t" \
  "v_max_f32 v185, v185, v189\n\t" \
  "v_max_f32 v186, v186, v190\n\t" \
  "v_max_f32 v203, v203, v207\n\t" \
  "v_max3_f32 v104, v200, v201, v202\n\t" \
  "v_max_f32 v195, v195, v199\n\t" \
  "v_max3_f32 v102, v192, v193, v194\n\t" \
  "v_max_f32 v187, v187, v191\n\t" \
  "v_max3_f32 v100, v184, v185, v186\n\t" \
  "v_max_f32 v106, v106, v211\n\t" \
  "v_max_f32 v104, v104, v203\n\t" \
  "v_max_f32 v102, v102, v195\n\t" \
  "v_max_f32 v100, v100, v187\n\t" \
  "v_permlane16_swap_b32 v104, v106\n\t" \
  "v_cvt_pk_bf16_f32 v78, v228, v229\n\t" \
  "v_permlane16_swap_b32 v100, v102\n\t" \
  "v_max_f32 v104, v104, v106\n\t" \
  "v_max_f32 v100, v100, v102\n\t" \
  "v_cvt_pk_bf16_f32 v79, v230, v231\n\t" \
  "v_cvt_pk_bf16_f32 v70, v216, v217\n\t" \
  "v_permlane32_swap_b32 v100, v104\n\t" \
  "v_cvt_pk_bf16_f32 v71, v218, v219\n\t" \
  "v_max_f32 v100, v100, v104\n\t" \
  "v_mul_f32 v100, 0xbe8293ee, v100\n\t" \
  "v_mov_b32 v104, v100\n\t" \
  "v_cvt_pk_bf16_f32 v80, v220, v221\n\t" \
  "v_cvt_pk_bf16_f32 v81, v222, v223\n\t" \
  "v_permlane32_swap_b32 v100, v104\n\t" \
  "s_waitcnt lgkmcnt(0)\n\t" \
  "v_readfirstlane_b32 vcc_lo, v52\n\t" \
  "s_nop 1\n\t" \
  "s_cmp_eq_u32 vcc_lo, 0\n\t" \
  "s_cbranch_scc1 5f\n\t" \
  "4:\n\t" \
  "v_mov_b32 v106, v104\n\t" \
  "v_mov_b32 v102, v100\n\t" \
  "s_nop 0\n\t" \
  "v_permlane16_swap_b32 v104, v106\n\t" \
  "v_permlane16_swap_b32 v100, v102\n\t" \
  "v_fma_f32 v208, v208, %16, v106\n\t" \
  "v_fma_f32 v209, v209, %16, v106\n\t" \
  "v_fma_f32 v210, v210, %16, v106\n\t" \
  "v_fma_f32 v211, v211, %16, v106\n\t" \
  "v_exp_f32 v208, v208\n\t" \
  "v_exp_f32 v209, v209\n\t" \
  "v_exp_f32 v210, v210\n\t" \
  "v_exp_f32 v211, v211\n\t" \
  "v_cvt_pk_bf16_f32 v212, v208, v209\n\t" \
  "v_cvt_pk_bf16_f32 v213, v210, v211\n\t" \
  "v_bfi_b32 v214, v46, 0, v212\n\t" \
  "v_bfi_b32 v215, v47, 0, v213\n\t" \
  "v_and_b32 v212, v46, v212\n\t" \
  "v_and_b32 v213, v47, v213\n\t" \
  "v_fma_f32 v200, v200, %16, v104\n\t" \
  "v_fma_f32 v201, v201, %16, v104\n\t" \
  "v_mfma_f32_16x16x32_bf16 v[208:211], v[48:51], v[212:215], 0\n\t" \
  "v_fma_f32 v202, v202, %16, v104\n\t" \
  "v_fma_f32 v203, v203, %16, v104\n\t" \
  "v_mfma_f32_16x16x32_bf16 v[176:179], v[64:67], v[212:215], 0\n\t" \
  "v_exp_f32 v200, v200\n\t" \
  "v_mfma_f32_16x16x32_bf16 v[180:183], v[74:77], v[212:215], 0\n\t" \
  "v_exp_f32 v201, v201\n\t" \
  "v_exp_f32 v202, v202\n\t" \
  "v_exp_f32 v203, v203\n\t" \
  "v_cvt_pk_bf16_f32 v204, v200, v201\n\t" \
  "v_cvt_pk_bf16_f32 v205, v202, v203\n\t" \
  "v_bfi_b32 v206, v46, 0, v204\n\t" \
  "v_bfi_b32 v207, v47, 0, v205\n\t" \
  "v_rcp_f32 v106, v208\n\t" \
  "v_and_b32 v204, v46, v204\n\t" \
  "v_and_b32 v205, v47, v205\n\t" \
  "v_pk_mul_f32 v[176:177], v[176:177], v[106:107] op_sel_hi:[1,0]\n\t" \
  "v_pk_mul_f32 v[178:179], v[178:179], v[106:107] op_sel_hi:[1,0]\n\t" \
  "v_mfma_f32_16x16x32_bf16 v[200:203], v[48:51], v[204:207], 0\n\t" \
  "v_pk_mul_f32 v[180:181], v[180:181], v[106:107] op_sel_hi:[1,0]\n\t" \
  "v_pk_mul_f32 v[182:183], v[182:183], v[106:107] op_sel_hi:[1,0]\n\t" \
  "v_mfma_f32_16x16x32_bf16 v[168:171], v[66:69], v[204:207], 0\n\t" \
  "v_cvt_pk_bf16_f32 v240, v176, v177\n\t" \
  "v_cvt_pk_bf16_f32 v241, v178, v179\n\t" \
  "v_mfma_f32_16x16x32_bf16 v[172:175], v[76:79], v[204:207], 0\n\t" \
  "v_cvt_pk_bf16_f32 v242, v180, v181\n\t" \
  "v_cvt_pk_bf16_f32 v243, v182, v183\n\t" \
  "global_store_dwordx4 v1, v[240:243], %14 offset:0 sc1\n\t" \
  "v_fma_f32 v192, v192, %16, v102\n\t" \
  "v_fma_f32 v193, v193, %16, v102\n\t" \
  "v_fma_f32 v194, v194, %16, v102\n\t" \
  "v_fma_f32 v195, v195, %16, v102\n\t" \
  "v_exp_f32 v192, v192\n\t" \
  "v_exp_f32 v193, v193\n\t" \
  "v_exp_f32 v194, v194\n\t" \
  "v_exp_f32 v195, v195\n\t" \
  "v_cvt_pk_bf16_f32 v196, v192, v193\n\t" \
  "v_cvt_pk_bf16_f32 v197, v194, v195\n\t" \
  "v_bfi_b32 v198, v46, 0, v196\n\t" \
  "v_bfi_b32 v199, v47, 0, v197\n\t" \
  "v_rcp_f32 v104, v200\n\t" \
  "v_and_b32 v196, v46, v196\n\t" \
  "v_and_b32 v197, v47, v197\n\t" \
  "v_pk_mul_f32 v[168:169], v[168:169], v[104:105] op_sel_hi:[1,0]\n\t" \
  "v_pk_mul_f32 v[170:171], v[170:171], v[104:105] op_sel_hi:[1,0]\n\t" \
  "v_mfma_f32_16x16x32_bf16 v[192:195], v[48:51], v[196:199], 0\n\t" \
  "v_pk_mul_f32 v[172:173], v[172:173], v[104:105] op_sel_hi:[1,0]\n\t" \
  "v_pk_mul_f32 v[174:175], v[174:175], v[104:105] op_sel_hi:[1,0]\n\t" \
  "v_mfma_f32_16x16x32_bf16 v[160:163], v[68:71], v[196:199], 0\n\t" \
  "v_cvt_pk_bf16_f32 v232, v168, v169\n\t" \
  "v_cvt_pk_bf16_f32 v233, v170, v171\n\t" \
  "v_mfma_f32_16x16x32_bf16 v[164:167], v[78:81], v[196:199], 0\n\t" \
  "v_cvt_pk_bf16_f32 v234, v172, v173\n\t" \
  "v_cvt_pk_bf16_f32 v235, v174, v175\n\t" \
  "global_store_dwordx4 v1, v[232:235], %14 offset:-4096 sc1\n\t" \
  "v_fma_f32 v184, v184, %16, v100\n\t" \
  "v_fma_f32 v185, v185, %16, v100\n\t" \
  "v_fma_f32 v186, v186, %16, v100\n\t" \
  "v_fma_f32 v187, v187, %16, v100\n\t" \
  "v_exp_f32 v184, v184\n\t" \
  "v_exp_f32 v185, v185\n\t" \
  "v_exp_f32 v186, v186\n\t" \
  "v_exp_f32 v187, v187\n\t" \
  "v_cvt_pk_bf16_f32 v188, v184, v185\n\t" \
  "v_cvt_pk_bf16_f32 v189, v186, v187\n\t" \
  "v_bfi_b32 v190, v46, 0, v188\n\t" \
  "v_bfi_b32 v191, v47, 0, v189\n\t" \
  "v_rcp_f32 v102, v192\n\t" \
  "v_and_b32 v188, v46, v188\n\t" \
  "v_and_b32 v189, v47, v189\n\t" \
  "v_pk_mul_f32 v[160:161], v[160:161], v[102:103] op_sel_hi:[1,0]\n\t" \
  "v_pk_mul_f32 v[162:163], v[162:163], v[102:103] op_sel_hi:[1,0]\n\t" \
  "v_mfma_f32_16x16x32_bf16 v[184:187], v[48:51], v[188:191], 0\n\t" \
  "v_pk_mul_f32 v[164:165], v[164:165], v[102:103] op_sel_hi:[1,0]\n\t" \
  "v_pk_mul_f32 v[166:167], v[166:167], v[102:103] op_sel_hi:[1,0]\n\t" \
  "v_mfma_f32_16x16x32_bf16 v[152:155], v[70:73], v[188:191], 0\n\t" \
  "v_cvt_pk_bf16_f32 v224, v160, v161\n\t" \
  "v_cvt_pk_bf16_f32 v225, v162, v163\n\t" \
  "v_mfma_f32_16x16x32_bf16 v[156:159], v[80:83], v[188:191], 0\n\t" \
  "v_cvt_pk_bf16_f32 v226, v164, v165\n\t" \
  "v_cvt_pk_bf16_f32 v227, v166, v167\n\t" \
  "global_store_dwordx4 v4, v[224:227], %14 offset:0 sc1\n\t" \
  "v_rcp_f32 v100, v184\n\t" \
  "ds_read_b128 v[60:63], v56 offset:64\n\t" \
  "ds_read_b64_tr_b16 v[108:109], v5 offset:0\n\t" \
  "ds_read_b64_tr_b16 v[110:111], v5 offset:16\n\t" \
  "ds_read_b128 v[112:115], v6 offset:16384\n\t" \
  "ds_read_b128 v[116:119], v6 offset:15360\n\t" \
  "506:\n\t" \
  "s_cbranch_execz 510f\n\t" \
  "ds_read_b64_tr_b16 v[120:121], v5 offset:512\n\t" \
  "ds_read_b64_tr_b16 v[122:123], v5 offset:528\n\t" \
  "ds_read_b128 v[124:127], v6 offset:14336\n\t" \
  "ds_read_b64_tr_b16 v[128:129], v5 offset:1024\n\t" \
  "ds_read_b64_tr_b16 v[130:131], v5 offset:1040\n\t" \
  "ds_read_b128 v[132:135], v6 offset:13312\n\t" \
  "ds_read_b64_tr_b16 v[136:137], v5 offset:1536\n\t" \
  "ds_read_b64_tr_b16 v[138:139], v5 offset:1552\n\t" \
  "ds_read_b128 v[140:143], v6 offset:12288\n\t" \
  "ds_read_b64_tr_b16 v[144:145], v5 offset:2048\n\t" \
  "v_pk_mul_f32 v[152:153], v[152:153], v[100:101] op_sel_hi:[1,0]\n\t" \
  "v_pk_mul_f32 v[154:155], v[154:155], v[100:101] op_sel_hi:[1,0]\n\t" \
  "v_cvt_pk_bf16_f32 v216, v152, v153\n\t" \
  "v_pk_mul_f32 v[156:157], v[156:157], v[100:101] op_sel_hi:[1,0]\n\t" \
  "v_pk_mul_f32 v[158:159], v[158:159], v[100:101] op_sel_hi:[1,0]\n\t" \
  "v_cvt_pk_bf16_f32 v217, v154, v155\n\t" \
  "v_cvt_pk_bf16_f32 v218, v156, v157\n\t" \
  "v_cvt_pk_bf16_f32 v219, v158, v159\n\t" \
  "global_store_dwordx4 v4, v[216:219], %14 offset:-4096 sc1\n\t" \
  "ds_write_b32 v32, v0 offset:112\n\t" \
  "s_branch 9f\n\t" \
  "5:\n\t" \
  "ds_read_b32 v52, v33 offset:48\n\t" \
  "ds_read_b64 v[72:73], v31\n\t" \
  "ds_read_b64 v[82:83], v31 offset:8\n\t" \
  "s_waitcnt lgkmcnt(0)\n\t" \
  "v_readfirstlane_b32 vcc_lo, v52\n\t" \
  "s_nop 1\n\t" \
  "s_cmp_eq_u32 vcc_lo, 0\n\t" \
  "s_cbranch_scc1 5b\n\t" \
  "s_branch 4b\n\t" \
  "15:\n\t" \
  "ds_read_b128 v[10:13], v14 offset:128\n\t" \
  "s_waitcnt lgkmcnt(0)\n\t" \
  "v_and_b32 v10, v10, v11\n\t" \
  "v_and_b32 v12, v12, v13\n\t" \
  "v_and_b32 v10, v10, v12\n\t" \
  "s_nop 1\n\t" \
  "v_readfirstlane_b32 vcc_lo, v10\n\t" \
  "s_nop 1\n\t" \
  "s_cmp_eq_u32 vcc_lo, 0\n\t" \
  "s_cbranch_scc1 15b\n\t" \
  "s_branch 14b\n\t" \
  "9:\n\t" \
  "507:\n\t" \
  "s_cbranch_execz 26b\n\t" \
  "s_waitcnt lgkmcnt(14)\n\t" \
  "v_and_b32 v60, v60, v61\n\t" \
  "v_and_b32 v62, v62, v63\n\t" \
  "v_mov_b32 v12, %13\n\t" \
  "v_and_b32 v60, v60, v62\n\t" \
  "s_nop 0\n\t" \
  "v_readfirstlane_b32 vcc_lo, v60\n\t" \
  "s_nop 1\n\t" \
  "s_cmp_eq_u32 vcc_lo, 0\n\t" \
  "s_cbranch_scc1 7f\n\t" \
  "6:\n\t" \
  "s_waitcnt lgkmcnt(11)\n\t" \
  "v_mfma_f32_16x16x32_bf16 v[192:195], v[108:111], v[112:115], 0\n\t" \
  "ds_read_b64_tr_b16 v[146:147], v5 offset:2064\n\t" \
  "ds_read_b128 v[148:151], v6 offset:11264\n\t" \
  "s_waitcnt lgkmcnt(12)\n\t" \
  "v_mfma_f32_16x16x32_bf16 v[196:199], v[108:111], v[116:119], 0\n\t" \
  "ds_read_b64_tr_b16 v[64:65], v5 offset:2560\n\t" \
  "ds_read_b64_tr_b16 v[66:67], v5 offset:2576\n\t" \
  "s_waitcnt lgkmcnt(12)\n\t" \
  "v_mfma_f32_16x16x32_bf16 v[196:199], v[120:123], v[112:115], v[196:199]\n\t" \
  "ds_read_b128 v[68:71], v6 offset:10240\n\t" \
  "ds_read_b64_tr_b16 v[72:73], v5 offset:3072\n\t" \
  "s_waitcnt lgkmcnt(13)\n\t" \
  "v_mfma_f32_16x16x32_bf16 v[200:203], v[108:111], v[124:127], 0\n\t" \
  "ds_read_b64_tr_b16 v[74:75], v5 offset:3088\n\t" \
  "ds_read_b128 v[76:79], v6 offset:9216\n\t" \
  "v_mfma_f32_16x16x32_bf16 v[200:203], v[120:123], v[116:119], v[200:203]\n\t" \
  "ds_read_b64_tr_b16 v[80:81], v5 offset:3584\n\t" \
  "ds_read_b64_tr_b16 v[82:83], v5 offset:3600\n\t" \
  "s_waitcnt lgkmcnt(15)\n\t" \
  "v_mfma_f32_16x16x32_bf16 v[200:203], v[128:131], v[112:115], v[200:203]\n\t" \
  "ds_read_b128 v[84:87], v6 offset:0\n\t" \
  "ds_read_b128 v[88:91], v6 offset:1024\n\t" \
  "s_waitcnt lgkmcnt(15)\n\t" \
  "v_mfma_f32_16x16x32_bf16 v[204:207], v[108:111], v[132:135], 0\n\t" \
  "ds_read_b128 v[92:95], v6 offset:2048\n\t" \
  "ds_read_b128 v[96:99], v6 offset:3072\n\t" \
  "v_mfma_f32_16x16x32_bf16 v[204:207], v[120:123], v[124:127], v[204:207]\n\t" \
  "ds_read_b128 v[100:103], v6 offset:4096\n\t" \
  "ds_read_b128 v[104:107], v6 offset:5120\n\t" \
  "v_mfma_f32_16x16x32_bf16 v[204:207], v[128:131], v[116:119], v[204:207]\n\t" \
  "ds_read_b128 v[152:155], v6 offset:6144\n\t" \
  "ds_read_b128 v[156:159], v6 offset:7168\n\t" \
  "s_waitcnt lgkmcnt(14)\n\t" \
  "v_mfma_f32_16x16x32_bf16 v[204:207], v[136:139], v[112:115], v[204:207]\n\t" \
  "v_lshl_add_u32 v12, v12, 12, v4\n\t" \
  "s_lshl_b32 s88, %13, 5\n\t" \
  "v_mfma_f32_16x16x32_bf16 v[208:211], v[108:111], v[140:143], 0\n\t" \
  "v_mfma_f32_16x16x32_bf16 v[208:211], v[120:123], v[132:135], v[208:211]\n\t" \
  "v_mfma_f32_16x16x32_bf16 v[208:211], v[128:131], v[124:127], v[208:211]\n\t" \
  "s_and_b32 s88, s88, 64\n\t" \
  "v_xor_b32 v13, s88, v54\n\t" \
  "v_mfma_f32_16x16x32_bf16 v[208:211], v[136:139], v[116:119], v[208:211]\n\t" \
  "v_mfma_f32_16x16x32_bf16 v[208:211], v[144:147], v[112:115], v[208:211]\n\t" \
  "v_mfma_f32_16x16x32_bf16 v[212:215], v[108:111], v[148:151], 0\n\t" \
  "ds_read_b32 v55, v56 offset:104\n\t" \
  "v_mfma_f32_16x16x32_bf16 v[212:215], v[120:123], v[140:143], v[212:215]\n\t" \
  "v_mfma_f32_16x16x32_bf16 v[212:215], v[128:131], v[132:135], v[212:215]\n\t" \
  "v_bfe_u32 v11, v12, 4, 4\n\t" \
  "v_mfma_f32_16x16x32_bf16 v[212:215], v[136:139], v[124:127], v[212:215]\n\t" \
  "v_lshlrev_b32 v11, 7, v11\n\t" \
  "v_mfma_f32_16x16x32_bf16 v[212:215], v[144:147], v[116:119], v[212:215]\n\t" \
  "v_mov_b32 v10, 1\n\t" \
  "v_mfma_f32_16x16x32_bf16 v[212:215], v[64:67], v[112:115], v[212:215]\n\t" \
  "s_waitcnt lgkmcnt(1)\n\t" \
  "v_mfma_f32_16x16x32_bf16 v[216:219], v[108:111], v[68:71], 0\n\t" \
  "v_mfma_f32_16x16x32_bf16 v[216:219], v[120:123], v[148:151], v[216:219]\n\t" \
  "v_and_b32 v1, 0x180, v4\n\t" \
  "v_mfma_f32_16x16x32_bf16 v[216:219], v[128:131], v[140:143], v[216:219]\n\t" \
  "v_and_b32 v2, 0x70, v4\n\t" \
  "v_mfma_f32_16x16x32_bf16 v[216:219], v[136:139], v[132:135], v[216:219]\n\t" \
  "v_lshlrev_b32 v2, 3, v2\n\t" \
  "v_mfma_f32_16x16x32_bf16 v[216:219], v[144:147], v[124:127], v[216:219]\n\t" \
  "v_lshl_add_u32 v1, v1, 5, v2\n\t" \
  "v_mfma_f32_16x16x32_bf16 v[216:219], v[64:67], v[116:119], v[216:219]\n\t" \
  "v_and_b32 v2, 0x2000, v1\n\t" \
  "v_mfma_f32_16x16x32_bf16 v[216:219], v[72:75], v[112:115], v[216:219]\n\t" \
  "v_add_u32 v1, v1, v2\n\t" \
  "v_mfma_f32_16x16x32_bf16 v[220:223], v[108:111], v[76:79], 0\n\t" \
  "v_mfma_f32_16x16x32_bf16 v[220:223], v[120:123], v[68:71], v[220:223]\n\t" \
  "v_mfma_f32_16x16x32_bf16 v[220:223], v[128:131], v[148:151], v[220:223]\n\t" \
  "v_mfma_f32_16x16x32_bf16 v[220:223], v[136:139], v[140:143], v[220:223]\n\t" \
  "v_mfma_f32_16x16x32_bf16 v[220:223], v[144:147], v[132:135], v[220:223]\n\t" \
  "ds_read_b64_tr_b16 v[160:161], v5 offset:128\n\t" \
  "v_mfma_f32_16x16x32_bf16 v[220:223], v[64:67], v[124:127], v[220:223]\n\t" \
  "ds_read_b64_tr_b16 v[162:163], v5 offset:144\n\t" \
  "v_mfma_f32_16x16x32_bf16 v[220:223], v[72:75], v[116:119], v[220:223]\n\t" \
  "ds_read_b128 v[164:167], v7 offset:16384\n\t" \
  "v_mfma_f32_16x16x32_bf16 v[220:223], v[80:83], v[112:115], v[220:223]\n\t" \
  "ds_read_b128 v[168:171], v7 offset:15360\n\t" \
  "v_mfma_f32_16x16x32_bf16 v[252:255], v[108:111], v[84:87], 0\n\t" \
  "ds_read_b64_tr_b16 v[172:173], v5 offset:640\n\t" \
  "v_mfma_f32_16x16x32_bf16 v[252:255], v[120:123], v[88:91], v[252:255]\n\t" \
  "ds_read_b64_tr_b16 v[174:175], v5 offset:656\n\t" \
  "v_mfma_f32_16x16x32_bf16 v[252:255], v[128:131], v[92:95], v[252:255]\n\t" \
  "ds_read_b128 v[176:179], v7 offset:14336\n\t" \
  "v_mfma_f32_16x16x32_bf16 v[252:255], v[136:139], v[96:99], v[252:255]\n\t" \
  "ds_read_b64_tr_b16 v[180:181], v5 offset:1152\n\t" \
  "v_mfma_f32_16x16x32_bf16 v[252:255], v[144:147], v[100:103], v[252:255]\n\t" \
  "ds_read_b64_tr_b16 v[182:183], v5 offset:1168\n\t" \
  "v_mfma_f32_16x16x32_bf16 v[252:255], v[64:67], v[104:107], v[252:255]\n\t" \
  "ds_read_b128 v[184:187], v7 offset:13312\n\t" \
  "v_mfma_f32_16x16x32_bf16 v[252:255], v[72:75], v[152:155], v[252:255]\n\t" \
  "ds_read_b64_tr_b16 v[188:189], v5 offset:1664\n\t" \
  "v_mfma_f32_16x16x32_bf16 v[252:255], v[80:83], v[156:159], v[252:255]\n\t" \
  "ds_read_b64_tr_b16 v[190:191], v5 offset:1680\n\t" \
  "v_mfma_f32_16x16x32_bf16 v[248:251], v[108:111], v[88:91], 0\n\t" \
  "ds_read_b128 v[76:79], v7 offset:12288\n\t" \
  "v_mfma_f32_16x16x32_bf16 v[248:251], v[120:123], v[92:95], v[248:251]\n\t" \
  "v_mfma_f32_16x16x32_bf16 v[248:251], v[128:131], v[96:99], v[248:251]\n\t" \
  "ds_read_b64_tr_b16 v[68:69], v5 offset:2176\n\t" \
  "v_mfma_f32_16x16x32_bf16 v[248:251], v[136:139], v[100:103], v[248:251]\n\t" \
  "ds_read_b64_tr_b16 v[70:71], v5 offset:2192\n\t" \
  "v_mfma_f32_16x16x32_bf16 v[248:251], v[144:147], v[104:107], v[248:251]\n\t" \
  "ds_read_b128 v[148:151], v7 offset:11264\n\t" \
  "v_mfma_f32_16x16x32_bf16 v[248:251], v[64:67], v[152:155], v[248:251]\n\t" \
  "v_mfma_f32_16x16x32_bf16 v[248:251], v[72:75], v[156:159], v[248:251]\n\t" \
  "v_mfma_f32_16x16x32_bf16 v[244:247], v[108:111], v[92:95], 0\n\t" \
  "v_mfma_f32_16x16x32_bf16 v[244:247], v[120:123], v[96:99], v[244:247]\n\t" \
  "ds_read_b64_tr_b16 v[140:141], v5 offset:2688\n\t" \
  "v_mfma_f32_16x16x32_bf16 v[244:247], v[128:131], v[100:103], v[244:247]\n\t" \
  "ds_read_b64_tr_b16 v[142:143], v5 offset:2704\n\t" \
  "v_mfma_f32_16x16x32_bf16 v[244:247], v[136:139], v[104:107], v[244:247]\n\t" \
  "ds_read_b128 v[132:135], v7 offset:10240\n\t" \
  "v_mfma_f32_16x16x32_bf16 v[244:247], v[144:147], v[152:155], v[244:247]\n\t" \
  "v_mfma_f32_16x16x32_bf16 v[244:247], v[64:67], v[156:159], v[244:247]\n\t" \
  "v_mfma_f32_16x16x32_bf16 v[240:243], v[108:111], v[96:99], 0\n\t" \
  "v_mfma_f32_16x16x32_bf16 v[240:243], v[120:123], v[100:103], v[240:243]\n\t" \
  "v_mfma_f32_16x16x32_bf16 v[240:243], v[128:131], v[104:107], v[240:243]\n\t" \
  "ds_read_b64_tr_b16 v[124:125], v5 offset:3200\n\t" \
  "v_mfma_f32_16x16x32_bf16 v[240:243], v[136:139], v[152:155], v[240:243]\n\t" \
  "s_waitcnt vmcnt(0)\n\t" \
  "s_mov_b64 exec, 1\n\t" \
  "ds_add_rtn_u32 v10, v56, v10 offset:16\n\t" \
  "s_mov_b64 exec, -1\n\t" \
  "v_mfma_f32_16x16x32_bf16 v[240:243], v[144:147], v[156:159], v[240:243]\n\t" \
  "ds_read_b64_tr_b16 v[126:127], v5 offset:3216\n\t" \
  "v_mfma_f32_16x16x32_bf16 v[236:239], v[108:111], v[100:103], 0\n\t" \
  "ds_read_b128 v[116:119], v7 offset:9216\n\t" \
  "v_mfma_f32_16x16x32_bf16 v[236:239], v[120:123], v[104:107], v[236:239]\n\t" \
  "v_mfma_f32_16x16x32_bf16 v[236:239], v[128:131], v[152:155], v[236:239]\n\t" \
  "v_mfma_f32_16x16x32_bf16 v[236:239], v[136:139], v[156:159], v[236:239]\n\t" \
  "v_mfma_f32_16x16x32_bf16 v[232:235], v[108:111], v[104:107], 0\n\t" \
  "v_mfma_f32_16x16x32_bf16 v[232:235], v[120:123], v[152:155], v[232:235]\n\t" \
  "s_waitcnt lgkmcnt(2)\n\t" \
  "v_readfirstlane_b32 vcc_lo, v10\n\t" \
  "v_mfma_f32_16x16x32_bf16 v[232:235], v[128:131], v[156:159], v[232:235]\n\t" \
  "s_cmp_eq_u32 vcc_lo, 3\n\t" \
  "s_cbranch_scc1 23f\n\t" \
  "22:\n\t" \
  "v_mfma_f32_16x16x32_bf16 v[228:231], v[108:111], v[152:155], 0\n\t" \
  "ds_read_b64_tr_b16 v[64:65], v5 offset:3712\n\t" \
  "v_mfma_f32_16x16x32_bf16 v[228:231], v[120:123], v[156:159], v[228:231]\n\t" \
  "ds_read_b64_tr_b16 v[66:67], v5 offset:3728\n\t" \
  "v_mfma_f32_16x16x32_bf16 v[224:227], v[108:111], v[156:159], 0\n\t" \
  "ds_read_b128 v[72:75], v7 offset:0\n\t" \
  "v_mfma_f32_16x16x32_bf16 v[192:195], v[160:163], v[164:167], v[192:195]\n\t" \
  "ds_read_b128 v[80:83], v7 offset:1024\n\t" \
  "v_mfma_f32_16x16x32_bf16 v[196:199], v[160:163], v[168:171], v[196:199]\n\t" \
  "ds_read_b128 v[84:87], v7 offset:2048\n\t" \
  "v_mfma_f32_16x16x32_bf16 v[196:199], v[172:175], v[164:167], v[196:199]\n\t" \
  "ds_read_b128 v[88:91], v7 offset:3072\n\t" \
  "v_mfma_f32_16x16x32_bf16 v[200:203], v[160:163], v[176:179], v[200:203]\n\t" \
  "ds_read_b128 v[92:95], v7 offset:4096\n\t" \
  "v_mfma_f32_16x16x32_bf16 v[200:203], v[172:175], v[168:171], v[200:203]\n\t" \
  "v_and_b32 v53, 0x2000, v12\n\t" \
  "ds_read_b128 v[108:111], v7 offset:5120\n\t" \
  "v_mfma_f32_16x16x32_bf16 v[200:203], v[180:183], v[164:167], v[200:203]\n\t" \
  "ds_read_b128 v[112:115], v7 offset:6144\n\t" \
  "v_mfma_f32_16x16x32_bf16 v[204:207], v[160:163], v[184:187], v[204:207]\n\t" \
  "v_add_u32 v53, v53, v12\n\t" \
  "ds_read_b128 v[120:123], v7 offset:7168\n\t" \
  "v_mfma_f32_16x16x32_bf16 v[204:207], v[172:175], v[176:179], v[204:207]\n\t" \
  "v_mfma_f32_16x16x32_bf16 v[204:207], v[180:183], v[168:171], v[204:207]\n\t" \
  "v_lshrrev_b32 v14, 1, v4\n\t" \
  "v_mfma_f32_16x16x32_bf16 v[204:207], v[188:191], v[164:167], v[204:207]\n\t" \
  "v_mfma_f32_16x16x32_bf16 v[208:211], v[160:163], v[76:79], v[208:211]\n\t" \
  "v_lshrrev_b32 v15, 24, v3\n\t" \
  "v_mfma_f32_16x16x32_bf16 v[208:211], v[172:175], v[184:187], v[208:211]\n\t" \
  "v_mfma_f32_16x16x32_bf16 v[208:211], v[180:183], v[176:179], v[208:211]\n\t" \
  "v_add_u32 v14, %20, v14\n\t" \
  "v_mfma_f32_16x16x32_bf16 v[208:211], v[188:191], v[168:171], v[208:211]\n\t" \
  "v_mfma_f32_16x16x32_bf16 v[208:211], v[68:71], v[164:167], v[208:211]\n\t" \
  "v_lshrrev_b32 v18, 10, v12\n\t" \
  "v_mfma_f32_16x16x32_bf16 v[212:215], v[160:163], v[148:151], v[212:215]\n\t" \
  "v_mfma_f32_16x16x32_bf16 v[212:215], v[172:175], v[76:79], v[212:215]\n\t" \
  "v_and_b32 v18, 12, v18\n\t" \
  "v_mfma_f32_16x16x32_bf16 v[212:215], v[180:183], v[184:187], v[212:215]\n\t" \
  "v_mfma_f32_16x16x32_bf16 v[212:215], v[188:191], v[176:179], v[212:215]\n\t" \
  "v_mov_b32 v17, 16\n\t" \
  "v_mfma_f32_16x16x32_bf16 v[212:215], v[68:71], v[168:171], v[212:215]\n\t" \
  "v_mfma_f32_16x16x32_bf16 v[212:215], v[140:143], v[164:167], v[212:215]\n\t" \
  "v_add_u32 v18, v56, v18\n\t" \
  "v_mfma_f32_16x16x32_bf16 v[216:219], v[160:163], v[132:135], v[216:219]\n\t" \
  "v_mfma_f32_16x16x32_bf16 v[216:219], v[172:175], v[148:151], v[216:219]\n\t" \
  "s_mov_b32 s76, 0\n\t" \
  "v_mfma_f32_16x16x32_bf16 v[216:219], v[180:183], v[76:79], v[216:219]\n\t" \
  "v_mfma_f32_16x16x32_bf16 v[216:219], v[188:191], v[184:187], v[216:219]\n\t" \
  "s_brev_b32 s77, 1\n\t" \
  "v_mfma_f32_16x16x32_bf16 v[216:219], v[68:71], v[176:179], v[216:219]\n\t" \
  "v_mfma_f32_16x16x32_bf16 v[216:219], v[140:143], v[168:171], v[216:219]\n\t" \
  "v_add_u32 v28, 0, v13\n\t" \
  "s_waitcnt lgkmcnt(0)\n\t" \
  "v_mfma_f32_16x16x32_bf16 v[216:219], v[124:127], v[164:167], v[216:219]\n\t" \
  "v_mfma_f32_16x16x32_bf16 v[220:223], v[160:163], v[116:119], v[220:223]\n\t" \
  "v_xor_b32 v29, 32, v28\n\t" \
  "v_mfma_f32_16x16x32_bf16 v[220:223], v[172:175], v[132:135], v[220:223]\n\t" \
  "v_mfma_f32_16x16x32_bf16 v[220:223], v[180:183], v[148:151], v[220:223]\n\t" \
  "v_xor_b32 v30, 64, v28\n\t" \
  "v_mfma_f32_16x16x32_bf16 v[220:223], v[188:191], v[76:79], v[220:223]\n\t" \
  "v_mfma_f32_16x16x32_bf16 v[220:223], v[68:71], v[184:187], v[220:223]\n\t" \
  "v_xor_b32 v31, 96, v28\n\t" \
  "ds_read_b64_tr_b16 v[76:77], v5 offset:256\n\t" \
  "v_mfma_f32_16x16x32_bf16 v[220:223], v[140:143], v[176:179], v[220:223]\n\t" \
  "ds_read_b64_tr_b16 v[78:79], v5 offset:272\n\t" \
  "v_mfma_f32_16x16x32_bf16 v[220:223], v[124:127], v[168:171], v[220:223]\n\t" \
  "v_add_u32 v32, 65536, v13\n\t" \
  "ds_read_b128 v[128:131], v8 offset:16384\n\t" \
  "v_mfma_f32_16x16x32_bf16 v[220:223], v[64:67], v[164:167], v[220:223]\n\t" \
  "ds_read_b128 v[136:139], v8 offset:15360\n\t" \
  "v_mfma_f32_16x16x32_bf16 v[252:255], v[160:163], v[72:75], v[252:255]\n\t" \
  "v_xor_b32 v33, 32, v32\n\t" \
  "ds_read_b64_tr_b16 v[144:145], v5 offset:768\n\t" \
  "v_mfma_f32_16x16x32_bf16 v[252:255], v[172:175], v[80:83], v[252:255]\n\t" \
  "ds_read_b64_tr_b16 v[146:147], v5 offset:784\n\t" \
  "v_mfma_f32_16x16x32_bf16 v[252:255], v[180:183], v[84:87], v[252:255]\n\t" \
  "s_mov_b32 exec_hi, 0\n\t" \
  "global_load_dword v2, v1, %14 offset:-4096 sc1\n\t" \
  "s_mov_b32 exec_hi, -1\n\t" \
  "v_xor_b32 v34, 64, v32\n\t" \
  "v_mfma_f32_16x16x32_bf16 v[252:255], v[188:191], v[88:91], v[252:255]\n\t" \
  "ds_read_b128 v[96:99], v8 offset:14336\n\t" \
  "v_mfma_f32_16x16x32_bf16 v[252:255], v[68:71], v[92:95], v[252:255]\n\t" \
  "v_xor_b32 v35, 96, v32\n\t" \
  "ds_read_b64_tr_b16 v[72:73], v5 offset:1280\n\t" \
  "v_mfma_f32_16x16x32_bf16 v[252:255], v[140:143], v[108:111], v[252:255]\n\t" \
  "ds_read_b64_tr_b16 v[74:75], v5 offset:1296\n\t" \
  "v_mfma_f32_16x16x32_bf16 v[252:255], v[124:127], v[112:115], v[252:255]\n\t" \
  "v_add_u32 v36, 131072, v13\n\t" \
  "ds_read_b128 v[100:103], v8 offset:13312\n\t" \
  "v_mfma_f32_16x16x32_bf16 v[252:255], v[64:67], v[120:123], v[252:255]\n\t" \
  "ds_read_b64_tr_b16 v[104:105], v5 offset:1792\n\t" \
  "v_mfma_f32_16x16x32_bf16 v[248:251], v[160:163], v[80:83], v[248:251]\n\t" \
  "v_xor_b32 v37, 32, v36\n\t" \
  "ds_read_b64_tr_b16 v[106:107], v5 offset:1808\n\t" \
  "v_mfma_f32_16x16x32_bf16 v[248:251], v[172:175], v[84:87], v[248:251]\n\t" \
  "ds_read_b128 v[152:155], v8 offset:12288\n\t" \
  "v_mfma_f32_16x16x32_bf16 v[248:251], v[180:183], v[88:91], v[248:251]\n\t" \
  "v_xor_b32 v38, 64, v36\n\t" \
  "ds_read_b64_tr_b16 v[64:65], v5 offset:2304\n\t" \
  "v_mfma_f32_16x16x32_bf16 v[248:251], v[188:191], v[92:95], v[248:251]\n\t" \
  "ds_read_b64_tr_b16 v[66:67], v5 offset:2320\n\t" \
  "v_mfma_f32_16x16x32_bf16 v[248:251], v[68:71], v[108:111], v[248:251]\n\t" \
  "v_xor_b32 v39, 96, v36\n\t" \
  "ds_read_b128 v[80:83], v8 offset:11264\n\t" \
  "v_mfma_f32_16x16x32_bf16 v[248:251], v[140:143], v[112:115], v[248:251]\n\t" \
  "v_mfma_f32_16x16x32_bf16 v[248:251], v[124:127], v[120:123], v[248:251]\n\t" \
  "v_lshrrev_b32 v57, 5, v4\n\t" \
  "v_mfma_f32_16x16x32_bf16 v[244:247], v[160:163], v[84:87], v[244:247]\n\t" \
  "v_mfma_f32_16x16x32_bf16 v[244:247], v[172:175], v[88:91], v[244:247]\n\t" \
  "v_and_b32 v57, 0x18, v57\n\t" \
  "ds_read_b64_tr_b16 v[84:85], v5 offset:2816\n\t" \
  "v_mfma_f32_16x16x32_bf16 v[244:247], v[180:183], v[92:95], v[244:247]\n\t" \
  "ds_read_b64_tr_b16 v[86:87], v5 offset:2832\n\t" \
  "v_mfma_f32_16x16x32_bf16 v[244:247], v[188:191], v[108:111], v[244:247]\n\t" \
  "v_and_b32 v62, 0x70, v4\n\t" \
  "ds_read_b128 v[156:159], v8 offset:10240\n\t" \
  "v_mfma_f32_16x16x32_bf16 v[244:247], v[68:71], v[112:115], v[244:247]\n\t" \
  "v_mfma_f32_16x16x32_bf16 v[244:247], v[140:143], v[120:123], v[244:247]\n\t" \
  "v_xor_b32 v57, v57, v62\n\t" \
  "v_mfma_f32_16x16x32_bf16 v[240:243], v[160:163], v[88:91], v[240:243]\n\t" \
  "v_mfma_f32_16x16x32_bf16 v[240:243], v[172:175], v[92:95], v[240:243]\n\t" \
  "v_and_b32 v58, 0xf0, v4\n\t" \
  "v_mfma_f32_16x16x32_bf16 v[240:243], v[180:183], v[108:111], v[240:243]\n\t" \
  "ds_read_b64_tr_b16 v[88:89], v5 offset:3328\n\t" \
  "v_mfma_f32_16x16x32_bf16 v[240:243], v[188:191], v[112:115], v[240:243]\n\t" \
  "v_lshl_add_u32 v57, v58, 3, v57\n\t" \
  "ds_read_b64_tr_b16 v[90:91], v5 offset:3344\n\t" \
  "v_mfma_f32_16x16x32_bf16 v[240:243], v[68:71], v[120:123], v[240:243]\n\t" \
  "ds_read_b128 v[164:167], v8 offset:9216\n\t" \
  "v_mfma_f32_16x16x32_bf16 v[236:239], v[160:163], v[92:95], v[236:239]\n\t" \
  "v_add_u32 v57, %23, v57\n\t" \
  "v_mfma_f32_16x16x32_bf16 v[236:239], v[172:175], v[108:111], v[236:239]\n\t" \
  "v_mfma_f32_16x16x32_bf16 v[236:239], v[180:183], v[112:115], v[236:239]\n\t" \
  "v_xor_b32 v58, 32, v57\n\t" \
  "v_mfma_f32_16x16x32_bf16 v[236:239], v[188:191], v[120:123], v[236:239]\n\t" \
  "v_mfma_f32_16x16x32_bf16 v[232:235], v[160:163], v[108:111], v[232:235]\n\t" \
  "v_xor_b32 v59, 64, v57\n\t" \
  "v_mfma_f32_16x16x32_bf16 v[232:235], v[172:175], v[112:115], v[232:235]\n\t" \
  "ds_read_b64_tr_b16 v[68:69], v5 offset:3840\n\t" \
  "v_mfma_f32_16x16x32_bf16 v[232:235], v[180:183], v[120:123], v[232:235]\n\t" \
  "v_xor_b32 v60, 96, v57\n\t" \
  "ds_read_b64_tr_b16 v[70:71], v5 offset:3856\n\t" \
  "v_mfma_f32_16x16x32_bf16 v[228:231], v[160:163], v[112:115], v[228:231]\n\t" \
  "ds_read_b128 v[92:95], v8 offset:0\n\t" \
  "v_mfma_f32_16x16x32_bf16 v[228:231], v[172:175], v[120:123], v[228:231]\n\t" \
  "v_lshrrev_b32 v61, 3, v4\n\t" \
  "ds_read_b128 v[168:171], v8 offset:1024\n\t" \
  "v_mfma_f32_16x16x32_bf16 v[224:227], v[160:163], v[120:123], v[224:227]\n\t" \
  "ds_read_b128 v[172:175], v8 offset:2048\n\t" \
  "s_waitcnt lgkmcnt(5)\n\t" \
  "v_mfma_f32_16x16x32_bf16 v[192:195], v[76:79], v[128:131], v[192:195]\n\t" \
  "v_and_b32 v61, 0x70, v61\n\t" \
  "ds_read_b128 v[160:163], v8 offset:3072\n\t" \
  "v_mfma_f32_16x16x32_bf16 v[196:199], v[76:79], v[136:139], v[196:199]\n\t" \
  "ds_read_b128 v[176:179], v8 offset:4096\n\t" \
  "v_mfma_f32_16x16x32_bf16 v[196:199], v[144:147], v[128:131], v[196:199]\n\t" \
  "v_xor_b32 v61, v61, v4\n\t" \
  "ds_read_b128 v[180:183], v8 offset:5120\n\t" \
  "v_mfma_f32_16x16x32_bf16 v[200:203], v[76:79], v[96:99], v[200:203]\n\t" \
  "ds_read_b128 v[184:187], v8 offset:6144\n\t" \
  "v_mfma_f32_16x16x32_bf16 v[200:203], v[144:147], v[136:139], v[200:203]\n\t" \
  "v_add_u32 v61, %23, v61\n\t" \
  "ds_read_b128 v[188:191], v8 offset:7168\n\t" \
  "v_mfma_f32_16x16x32_bf16 v[200:203], v[72:75], v[128:131], v[200:203]\n\t" \
  "v_mfma_f32_16x16x32_bf16 v[204:207], v[76:79], v[100:103], v[204:207]\n\t" \
  "v_and_b32 v63, 0x380, v4\n\t" \
  "v_mfma_f32_16x16x32_bf16 v[204:207], v[144:147], v[96:99], v[204:207]\n\t" \
  "v_mfma_f32_16x16x32_bf16 v[204:207], v[72:75], v[136:139], v[204:207]\n\t" \
  "v_lshl_add_u32 v62, v63, 6, v62\n\t" \
  "v_mfma_f32_16x16x32_bf16 v[204:207], v[104:107], v[128:131], v[204:207]\n\t" \
  "v_mfma_f32_16x16x32_bf16 v[208:211], v[76:79], v[152:155], v[208:211]\n\t" \
  "v_add_u32 v63, 0x10000, v62\n\t" \
  "v_mfma_f32_16x16x32_bf16 v[208:211], v[144:147], v[100:103], v[208:211]\n\t" \
  "v_mfma_f32_16x16x32_bf16 v[208:211], v[72:75], v[96:99], v[208:211]\n\t" \
  "v_mfma_f32_16x16x32_bf16 v[208:211], v[104:107], v[136:139], v[208:211]\n\t" \
  "v_mfma_f32_16x16x32_bf16 v[208:211], v[64:67], v[128:131], v[208:211]\n\t" \
  "v_mfma_f32_16x16x32_bf16 v[212:215], v[76:79], v[80:83], v[212:215]\n\t" \
  "v_mfma_f32_16x16x32_bf16 v[212:215], v[144:147], v[152:155], v[212:215]\n\t" \
  "v_mfma_f32_16x16x32_bf16 v[212:215], v[72:75], v[100:103], v[212:215]\n\t" \
  "v_mfma_f32_16x16x32_bf16 v[212:215], v[104:107], v[96:99], v[212:215]\n\t" \
  "v_mfma_f32_16x16x32_bf16 v[212:215], v[64:67], v[136:139], v[212:215]\n\t" \
  "v_mfma_f32_16x16x32_bf16 v[212:215], v[84:87], v[128:131], v[212:215]\n\t" \
  "v_mfma_f32_16x16x32_bf16 v[216:219], v[76:79], v[156:159], v[216:219]\n\t" \
  "v_mfma_f32_16x16x32_bf16 v[216:219], v[144:147], v[80:83], v[216:219]\n\t" \
  "v_mfma_f32_16x16x32_bf16 v[216:219], v[72:75], v[152:155], v[216:219]\n\t" \
  "v_mfma_f32_16x16x32_bf16 v[216:219], v[104:107], v[100:103], v[216:219]\n\t" \
  "v_mfma_f32_16x16x32_bf16 v[216:219], v[64:67], v[96:99], v[216:219]\n\t" \
  "v_mfma_f32_16x16x32_bf16 v[216:219], v[84:87], v[136:139], v[216:219]\n\t" \
  "v_mfma_f32_16x16x32_bf16 v[216:219], v[88:91], v[128:131], v[216:219]\n\t" \
  "v_mfma_f32_16x16x32_bf16 v[220:223], v[76:79], v[164:167], v[220:223]\n\t" \
  "v_mfma_f32_16x16x32_bf16 v[220:223], v[144:147], v[156:159], v[220:223]\n\t" \
  "v_mfma_f32_16x16x32_bf16 v[220:223], v[72:75], v[80:83], v[220:223]\n\t" \
  "v_mfma_f32_16x16x32_bf16 v[220:223], v[104:107], v[152:155], v[220:223]\n\t" \
  "v_mfma_f32_16x16x32_bf16 v[220:223], v[64:67], v[100:103], v[220:223]\n\t" \
  "ds_read_b64_tr_b16 v[148:149], v5 offset:384\n\t" \
  "v_mfma_f32_16x16x32_bf16 v[220:223], v[84:87], v[96:99], v[220:223]\n\t" \
  "ds_read_b64_tr_b16 v[150:151], v5 offset:400\n\t" \
  "v_mfma_f32_16x16x32_bf16 v[220:223], v[88:91], v[136:139], v[220:223]\n\t" \
  "ds_read_b128 v[80:83], v9 offset:16384\n\t" \
  "s_waitcnt lgkmcnt(3)\n\t" \
  "v_mfma_f32_16x16x32_bf16 v[220:223], v[68:71], v[128:131], v[220:223]\n\t" \
  "ds_read_b128 v[140:143], v9 offset:15360\n\t" \
  "v_mfma_f32_16x16x32_bf16 v[252:255], v[76:79], v[92:95], v[252:255]\n\t" \
  "ds_read_b64_tr_b16 v[132:133], v5 offset:896\n\t" \
  "v_mfma_f32_16x16x32_bf16 v[252:255], v[144:147], v[168:171], v[252:255]\n\t" \
  "ds_read_b64_tr_b16 v[134:135], v5 offset:912\n\t" \
  "v_mfma_f32_16x16x32_bf16 v[252:255], v[72:75], v[172:175], v[252:255]\n\t" \
  "ds_read_b128 v[124:127], v9 offset:14336\n\t" \
  "v_mfma_f32_16x16x32_bf16 v[252:255], v[104:107], v[160:163], v[252:255]\n\t" \
  "ds_read_b64_tr_b16 v[116:117], v5 offset:1408\n\t" \
  "v_mfma_f32_16x16x32_bf16 v[252:255], v[64:67], v[176:179], v[252:255]\n\t" \
  "ds_read_b64_tr_b16 v[118:119], v5 offset:1424\n\t" \
  "v_mfma_f32_16x16x32_bf16 v[252:255], v[84:87], v[180:183], v[252:255]\n\t" \
  "ds_read_b128 v[92:95], v9 offset:13312\n\t" \
  "v_mfma_f32_16x16x32_bf16 v[252:255], v[88:91], v[184:187], v[252:255]\n\t" \
  "ds_read_b64_tr_b16 v[108:109], v5 offset:1920\n\t" \
  "v_mfma_f32_16x16x32_bf16 v[252:255], v[68:71], v[188:191], v[252:255]\n\t" \
  "ds_read_b64_tr_b16 v[110:111], v5 offset:1936\n\t" \
  "v_mfma_f32_16x16x32_bf16 v[248:251], v[76:79], v[168:171], v[248:251]\n\t" \
  "ds_read_b128 v[112:115], v9 offset:12288\n\t" \
  "v_mfma_f32_16x16x32_bf16 v[248:251], v[144:147], v[172:175], v[248:251]\n\t" \
  "v_mfma_f32_16x16x32_bf16 v[248:251], v[72:75], v[160:163], v[248:251]\n\t" \
  "ds_read_b64_tr_b16 v[120:121], v5 offset:2432\n\t" \
  "v_mfma_f32_16x16x32_bf16 v[248:251], v[104:107], v[176:179], v[248:251]\n\t" \
  "ds_read_b64_tr_b16 v[122:123], v5 offset:2448\n\t" \
  "v_mfma_f32_16x16x32_bf16 v[248:251], v[64:67], v[180:183], v[248:251]\n\t" \
  "ds_read_b128 v[68:71], v9 offset:11264\n\t" \
  "v_mfma_f32_16x16x32_bf16 v[248:251], v[84:87], v[184:187], v[248:251]\n\t" \
  "v_mfma_f32_16x16x32_bf16 v[248:251], v[88:91], v[188:191], v[248:251]\n\t" \
  "v_mfma_f32_16x16x32_bf16 v[244:247], v[76:79], v[172:175], v[244:247]\n\t" \
  "v_mfma_f32_16x16x32_bf16 v[244:247], v[144:147], v[160:163], v[244:247]\n\t" \
  "ds_read_b64_tr_b16 v[128:129], v5 offset:2944\n\t" \
  "v_mfma_f32_16x16x32_bf16 v[244:247], v[72:75], v[176:179], v[244:247]\n\t" \
  "ds_read_b64_tr_b16 v[130:131], v5 offset:2960\n\t" \
  "v_mfma_f32_16x16x32_bf16 v[244:247], v[104:107], v[180:183], v[244:247]\n\t" \
  "ds_read_b128 v[88:91], v9 offset:10240\n\t" \
  "v_mfma_f32_16x16x32_bf16 v[244:247], v[64:67], v[184:187], v[244:247]\n\t" \
  "v_mfma_f32_16x16x32_bf16 v[244:247], v[84:87], v[188:191], v[244:247]\n\t" \
  "v_mfma_f32_16x16x32_bf16 v[240:243], v[76:79], v[160:163], v[240:243]\n\t" \
  "v_mfma_f32_16x16x32_bf16 v[240:243], v[144:147], v[176:179], v[240:243]\n\t" \
  "v_mfma_f32_16x16x32_bf16 v[240:243], v[72:75], v[180:183], v[240:243]\n\t" \
  "ds_read_b64_tr_b16 v[136:137], v5 offset:3456\n\t" \
  "v_mfma_f32_16x16x32_bf16 v[240:243], v[104:107], v[184:187], v[240:243]\n\t" \
  "ds_read_b64_tr_b16 v[138:139], v5 offset:3472\n\t" \
  "v_mfma_f32_16x16x32_bf16 v[240:243], v[64:67], v[188:191], v[240:243]\n\t" \
  "ds_read_b128 v[84:87], v9 offset:9216\n\t" \
  "v_mfma_f32_16x16x32_bf16 v[236:239], v[76:79], v[176:179], v[236:239]\n\t" \
  "v_mfma_f32_16x16x32_bf16 v[236:239], v[144:147], v[180:183], v[236:239]\n\t" \
  "v_mfma_f32_16x16x32_bf16 v[236:239], v[72:75], v[184:187], v[236:239]\n\t" \
  "v_mfma_f32_16x16x32_bf16 v[236:239], v[104:107], v[188:191], v[236:239]\n\t" \
  "v_mfma_f32_16x16x32_bf16 v[232:235], v[76:79], v[180:183], v[232:235]\n\t" \
  "v_mfma_f32_16x16x32_bf16 v[232:235], v[144:147], v[184:187], v[232:235]\n\t" \
  "ds_read_b64_tr_b16 v[96:97], v5 offset:3968\n\t" \
  "v_mfma_f32_16x16x32_bf16 v[232:235], v[72:75], v[188:191], v[232:235]\n\t" \
  "ds_read_b64_tr_b16 v[98:99], v5 offset:3984\n\t" \
  "ds_write_b32 v18, v17 offset:176\n\t" \
  "v_mfma_f32_16x16x32_bf16 v[228:231], v[76:79], v[184:187], v[228:231]\n\t" \
  "ds_read_b128 v[100:103], v9 offset:7168\n\t" \
  "v_mfma_f32_16x16x32_bf16 v[228:231], v[144:147], v[188:191], v[228:231]\n\t" \
  "ds_read_b128 v[104:107], v9 offset:6144\n\t" \
  "v_mfma_f32_16x16x32_bf16 v[224:227], v[76:79], v[188:191], v[224:227]\n\t" \
  "ds_read_b128 v[144:147], v9 offset:5120\n\t" \
  "s_waitcnt lgkmcnt(6)\n\t" \
  "v_mfma_f32_16x16x32_bf16 v[192:195], v[148:151], v[80:83], v[192:195]\n\t" \
  "v_mfma_f32_16x16x32_bf16 v[196:199], v[148:151], v[140:143], v[196:199]\n\t" \
  "v_mfma_f32_16x16x32_bf16 v[196:199], v[132:135], v[80:83], v[196:199]\n\t" \
  "ds_read_b128 v[152:155], v9 offset:4096\n\t" \
  "v_mfma_f32_16x16x32_bf16 v[200:203], v[148:151], v[124:127], v[200:203]\n\t" \
  "v_mfma_f32_16x16x32_bf16 v[200:203], v[132:135], v[140:143], v[200:203]\n\t" \
  "v_mfma_f32_16x16x32_bf16 v[200:203], v[116:119], v[80:83], v[200:203]\n\t" \
  "v_mfma_f32_16x16x32_bf16 v[204:207], v[148:151], v[92:95], v[204:207]\n\t" \
  "ds_read_b128 v[156:159], v9 offset:3072\n\t" \
  "v_cndmask_b32 v20, v195, 0, s[76:77]\n\t" \
  "v_mfma_f32_16x16x32_bf16 v[204:207], v[132:135], v[124:127], v[204:207]\n\t" \
  "ds_bpermute_b32 v20, v15, v20\n\t" \
  "v_add_f32 v225, v225, v192\n\t" \
  "v_mfma_f32_16x16x32_bf16 v[204:207], v[116:119], v[140:143], v[204:207]\n\t" \
  "v_add_f32 v226, v226, v193\n\t" \
  "v_add_f32 v227, v227, v194\n\t" \
  "v_mfma_f32_16x16x32_bf16 v[204:207], v[108:111], v[80:83], v[204:207]\n\t" \
  "ds_read_b128 v[40:43], v56 offset:80\n\t" \
  "v_cndmask_b32 v21, v199, v195, s[76:77]\n\t" \
  "v_mfma_f32_16x16x32_bf16 v[208:211], v[148:151], v[112:115], v[208:211]\n\t" \
  "ds_bpermute_b32 v21, v15, v21\n\t" \
  "v_add_f32 v229, v229, v196\n\t" \
  "v_mfma_f32_16x16x32_bf16 v[208:211], v[132:135], v[92:95], v[208:211]\n\t" \
  "ds_read_b128 v[164:167], v9 offset:2048\n\t" \
  "v_add_f32 v230, v230, v197\n\t" \
  "v_mfma_f32_16x16x32_bf16 v[208:211], v[116:119], v[124:127], v[208:211]\n\t" \
  "v_add_f32 v231, v231, v198\n\t" \
  "v_cndmask_b32 v22, v203, v199, s[76:77]\n\t" \
  "v_mfma_f32_16x16x32_bf16 v[208:211], v[108:111], v[140:143], v[208:211]\n\t" \
  "ds_bpermute_b32 v22, v15, v22\n\t" \
  "v_add_f32 v233, v233, v200\n\t" \
  "v_mfma_f32_16x16x32_bf16 v[208:211], v[120:123], v[80:83], v[208:211]\n\t" \
  "v_add_f32 v234, v234, v201\n\t" \
  "v_add_f32 v235, v235, v202\n\t" \
  "v_mfma_f32_16x16x32_bf16 v[212:215], v[148:151], v[68:71], v[212:215]\n\t" \
  "s_waitcnt lgkmcnt(4)\n\t" \
  "v_add_f32 v224, v224, v20\n\t" \
  "v_cndmask_b32 v23, v207, v203, s[76:77]\n\t" \
  "v_mfma_f32_16x16x32_bf16 v[212:215], v[132:135], v[112:115], v[212:215]\n\t" \
  "s_waitcnt lgkmcnt(3)\n\t" \
  "v_min_u32 v40, v40, v41\n\t" \
  "v_min3_u32 v40, v40, v42, v43\n\t" \
  "v_mfma_f32_16x16x32_bf16 v[212:215], v[116:119], v[92:95], v[212:215]\n\t" \
  "v_readfirstlane_b32 vcc_lo, v40\n\t" \
  "ds_read_b128 v[168:171], v9 offset:1024\n\t" \
  "v_mfma_f32_16x16x32_bf16 v[212:215], v[108:111], v[124:127], v[212:215]\n\t" \
  "s_cmp_ge_u32 vcc_lo, 7\n\t" \
  "s_cbranch_scc0 30f\n\t" \
  "31:\n\t" \
  "v_mfma_f32_16x16x32_bf16 v[212:215], v[120:123], v[140:143], v[212:215]\n\t" \
  "ds_bpermute_b32 v23, v15, v23\n\t" \
  "v_add_f32 v237, v237, v204\n\t" \
  "v_mfma_f32_16x16x32_bf16 v[212:215], v[128:131], v[80:83], v[212:215]\n\t" \
  "v_add_f32 v238, v238, v205\n\t" \
  "v_add_f32 v239, v239, v206\n\t" \
  "v_mfma_f32_16x16x32_bf16 v[216:219], v[148:151], v[88:91], v[216:219]\n\t" \
  "s_waitcnt lgkmcnt(4)\n\t" \
  "v_add_f32 v228, v228, v21\n\t" \
  "s_waitcnt lgkmcnt(2)\n\t" \
  "v_add_f32 v232, v232, v22\n\t" \
  "v_mfma_f32_16x16x32_bf16 v[216:219], v[132:135], v[68:71], v[216:219]\n\t" \
  "v_cndmask_b32 v24, v211, v207, s[76:77]\n\t" \
  "ds_bpermute_b32 v24, v15, v24\n\t" \
  "v_mfma_f32_16x16x32_bf16 v[216:219], v[116:119], v[112:115], v[216:219]\n\t" \
  "v_add_f32 v241, v241, v208\n\t" \
  "v_add_f32 v242, v242, v209\n\t" \
  "v_mfma_f32_16x16x32_bf16 v[216:219], v[108:111], v[92:95], v[216:219]\n\t" \
  "ds_read_b128 v[172:175], v9 offset:0\n\t" \
  "ds_write_b32 v18, v17 offset:192\n\t" \
  "v_mfma_f32_16x16x32_bf16 v[216:219], v[120:123], v[124:127], v[216:219]\n\t" \
  "ds_read_b32 v16, v56 offset:96\n\t" \
  "v_add_f32 v243, v243, v210\n\t" \
  "v_mfma_f32_16x16x32_bf16 v[216:219], v[128:131], v[140:143], v[216:219]\n\t" \
  "v_cndmask_b32 v25, v215, v211, s[76:77]\n\t" \
  "ds_bpermute_b32 v25, v15, v25\n\t" \
  "v_mfma_f32_16x16x32_bf16 v[216:219], v[136:139], v[80:83], v[216:219]\n\t" \
  "s_waitcnt lgkmcnt(5)\n\t" \
  "v_add_f32 v236, v236, v23\n\t" \
  "v_add_f32 v245, v245, v212\n\t" \
  "v_mfma_f32_16x16x32_bf16 v[220:223], v[148:151], v[84:87], v[220:223]\n\t" \
  "v_add_f32 v246, v246, v213\n\t" \
  "v_add_f32 v247, v247, v214\n\t" \
  "v_mfma_f32_16x16x32_bf16 v[220:223], v[132:135], v[88:91], v[220:223]\n\t" \
  "ds_read_b64_tr_b16 v[192:193], v28 offset:22528\n\t" \
  "v_mfma_f32_16x16x32_bf16 v[220:223], v[116:119], v[68:71], v[220:223]\n\t" \
  "s_waitcnt lgkmcnt(5)\n\t" \
  "v_add_f32 v240, v240, v24\n\t" \
  "v_mfma_f32_16x16x32_bf16 v[220:223], v[108:111], v[112:115], v[220:223]\n\t" \
  "ds_read_b64_tr_b16 v[194:195], v28 offset:23040\n\t" \
  "v_mfma_f32_16x16x32_bf16 v[220:223], v[120:123], v[92:95], v[220:223]\n\t" \
  "s_waitcnt lgkmcnt(3)\n\t" \
  "v_readfirstlane_b32 s75, v16\n\t" \
  "v_mfma_f32_16x16x32_bf16 v[220:223], v[128:131], v[124:127], v[220:223]\n\t" \
  "s_cmp_lg_u32 s75, 0\n\t" \
  "s_cselect_b32 s69, -1, 0\n\t" \
  "v_mfma_f32_16x16x32_bf16 v[220:223], v[136:139], v[140:143], v[220:223]\n\t" \
  "s_lshr_b64 s[90:91], %18, 0\n\t" \
  "s_and_b32 s90, s90, 15\n\t" \
  "s_lshl_b32 s90, s90, 15\n\t" \
  "v_and_b32 v52, s69, v53\n\t" \
  "v_mfma_f32_16x16x32_bf16 v[220:223], v[96:99], v[80:83], v[220:223]\n\t" \
  "s_add_u32 s90, s90, s92\n\t" \
  "s_addc_u32 s91, s93, 0\n\t" \
  "s_cmp_lg_u32 s75, 0\n\t" \
  "v_mfma_f32_16x16x32_bf16 v[224:227], v[148:151], v[100:103], v[224:227]\n\t" \
  "s_cselect_b64 s[90:91], s[90:91], %11\n\t" \
  "v_cndmask_b32 v26, v219, v215, s[76:77]\n\t" \
  "v_mfma_f32_16x16x32_bf16 v[228:231], v[148:151], v[104:107], v[228:231]\n\t" \
  "global_load_dwordx4 v[64:67], v52, s[90:91] offset:0 sc1\n\t" \
  "ds_bpermute_b32 v26, v15, v26\n\t" \
  "v_mfma_f32_16x16x32_bf16 v[228:231], v[132:135], v[100:103], v[228:231]\n\t" \
  "v_add_f32 v249, v249, v216\n\t" \
  "v_add_f32 v250, v250, v217\n\t" \
  "v_mfma_f32_16x16x32_bf16 v[232:235], v[148:151], v[144:147], v[232:235]\n\t" \
  "v_add_f32 v251, v251, v218\n\t" \
  "s_waitcnt lgkmcnt(3)\n\t" \
  "v_add_f32 v244, v244, v25\n\t" \
  "v_mfma_f32_16x16x32_bf16 v[232:235], v[132:135], v[104:107], v[232:235]\n\t" \
  "global_load_dwordx4 v[68:71], v52, s[90:91] offset:1024 sc1\n\t" \
  "ds_read_b64_tr_b16 v[196:197], v28 offset:26624\n\t" \
  "v_mfma_f32_16x16x32_bf16 v[232:235], v[116:119], v[100:103], v[232:235]\n\t" \
  "v_cndmask_b32 v27, v223, v219, s[76:77]\n\t" \
  "ds_bpermute_b32 v27, v15, v27\n\t" \
  "508:\n\t" \
  "s_cbranch_execz 26b\n\t" \
  "v_mfma_f32_16x16x32_bf16 v[236:239], v[148:151], v[152:155], v[236:239]\n\t" \
  "v_add_f32 v253, v253, v220\n\t" \
  "v_add_f32 v254, v254, v221\n\t" \
  "v_mfma_f32_16x16x32_bf16 v[236:239], v[132:135], v[144:147], v[236:239]\n\t" \
  "global_load_dwordx4 v[72:75], v52, s[90:91] offset:2048 sc1\n\t" \
  "v_add_f32 v255, v255, v222\n\t" \
  "v_mfma_f32_16x16x32_bf16 v[236:239], v[116:119], v[104:107], v[236:239]\n\t" \
  "v_cvt_pk_bf16_f32 v45, v226, v227\n\t" \
  "v_cvt_pk_bf16_f32 v44, v224, v225\n\t" \
  "v_mfma_f32_16x16x32_bf16 v[236:239], v[108:111], v[100:103], v[236:239]\n\t" \
  "s_waitcnt lgkmcnt(2)\n\t" \
  "v_add_f32 v248, v248, v26\n\t" \
  "ds_write_b64 v14, v[44:45] offset:0\n\t" \
  "v_mfma_f32_16x16x32_bf16 v[240:243], v[148:151], v[156:159], v[240:243]\n\t" \
  "global_load_dwordx4 v[76:79], v52, s[90:91] offset:3072 sc1\n\t" \
  "v_cvt_pk_bf16_f32 v47, v230, v231\n\t" \
  "v_mfma_f32_16x16x32_bf16 v[240:243], v[132:135], v[152:155], v[240:243]\n\t" \
  "v_cvt_pk_bf16_f32 v46, v228, v229\n\t" \
  "v_cvt_pk_bf16_f32 v49, v234, v235\n\t" \
  "v_mfma_f32_16x16x32_bf16 v[240:243], v[116:119], v[144:147], v[240:243]\n\t" \
  "ds_write_b64 v14, v[46:47] offset:512\n\t" \
  "v_cvt_pk_bf16_f32 v48, v232, v233\n\t" \
  "v_mfma_f32_16x16x32_bf16 v[240:243], v[108:111], v[104:107], v[240:243]\n\t" \
  "ds_write_b64 v14, v[48:49] offset:1024\n\t" \
  "s_waitcnt lgkmcnt(3)\n\t" \
  "v_add_f32 v252, v252, v27\n\t" \
  "v_mfma_f32_16x16x32_bf16 v[240:243], v[120:123], v[100:103], v[240:243]\n\t" \
  "s_lshr_b64 s[90:91], %18, 4\n\t" \
  "s_and_b32 s90, s90, 15\n\t" \
  "s_lshl_b32 s90, s90, 15\n\t" \
  "v_mfma_f32_16x16x32_bf16 v[244:247], v[148:151], v[164:167], v[244:247]\n\t" \
  "s_add_u32 s90, s90, s92\n\t" \
  "s_addc_u32 s91, s93, 0\n\t" \
  "s_cmp_lg_u32 s75, 0\n\t" \
  "v_mfma_f32_16x16x32_bf16 v[244:247], v[132:135], v[156:159], v[244:247]\n\t" \
  "s_cselect_b64 s[90:91], s[90:91], %11\n\t" \
  "v_cvt_pk_bf16_f32 v51, v238, v239\n\t" \
  "v_mfma_f32_16x16x32_bf16 v[244:247], v[116:119], v[152:155], v[244:247]\n\t" \
  "global_load_dwordx4 v[80:83], v52, s[90:91] offset:0 sc1\n\t" \
  "v_cvt_pk_bf16_f32 v50, v236, v237\n\t" \
  "v_mfma_f32_16x16x32_bf16 v[244:247], v[108:111], v[144:147], v[244:247]\n\t" \
  "ds_write_b64 v14, v[50:51] offset:1536\n\t" \
  "ds_read_b64_tr_b16 v[198:199], v28 offset:27136\n\t" \
  "v_mfma_f32_16x16x32_bf16 v[244:247], v[120:123], v[104:107], v[244:247]\n\t" \
  "ds_read_b64_tr_b16 v[200:201], v29 offset:22528\n\t" \
  "v_mfma_f32_16x16x32_bf16 v[244:247], v[128:131], v[100:103], v[244:247]\n\t" \
  "global_load_dwordx4 v[84:87], v52, s[90:91] offset:1024 sc1\n\t" \
  "v_cvt_pk_bf16_f32 v45, v242, v243\n\t" \
  "v_mfma_f32_16x16x32_bf16 v[248:251], v[148:151], v[168:171], v[248:251]\n\t" \
  "v_cvt_pk_bf16_f32 v44, v240, v241\n\t" \
  "ds_read_b64_tr_b16 v[202:203], v29 offset:23040\n\t" \
  "v_mfma_f32_16x16x32_bf16 v[248:251], v[132:135], v[164:167], v[248:251]\n\t" \
  "ds_write_b64 v14, v[44:45] offset:2048\n\t" \
  "ds_read_b64_tr_b16 v[204:205], v29 offset:26624\n\t" \
  "v_mfma_f32_16x16x32_bf16 v[248:251], v[116:119], v[156:159], v[248:251]\n\t" \
  "global_load_dwordx4 v[88:91], v52, s[90:91] offset:2048 sc1\n\t" \
  "ds_read_b64_tr_b16 v[206:207], v29 offset:27136\n\t" \
  "v_mfma_f32_16x16x32_bf16 v[248:251], v[108:111], v[152:155], v[248:251]\n\t" \
  "ds_read_b64_tr_b16 v[208:209], v30 offset:22528\n\t" \
  "v_mfma_f32_16x16x32_bf16 v[248:251], v[120:123], v[144:147], v[248:251]\n\t" \
  "ds_read_b64_tr_b16 v[210:211], v30 offset:23040\n\t" \
  "v_mfma_f32_16x16x32_bf16 v[248:251], v[128:131], v[104:107], v[248:251]\n\t" \
  "global_load_dwordx4 v[92:95], v52, s[90:91] offset:3072 sc1\n\t" \
  "v_cvt_pk_bf16_f32 v47, v246, v247\n\t" \
  "v_mfma_f32_16x16x32_bf16 v[248:251], v[136:139], v[100:103], v[248:251]\n\t" \
  "v_cvt_pk_bf16_f32 v46, v244, v245\n\t" \
  "ds_read_b64_tr_b16 v[212:213], v30 offset:26624\n\t" \
  "v_mfma_f32_16x16x32_bf16 v[252:255], v[148:151], v[172:175], v[252:255]\n\t" \
  "ds_write_b64 v14, v[46:47] offset:2560\n\t" \
  "ds_read_b64_tr_b16 v[214:215], v30 offset:27136\n\t" \
  "v_mfma_f32_16x16x32_bf16 v[252:255], v[132:135], v[168:171], v[252:255]\n\t" \
  "ds_read_b64_tr_b16 v[216:217], v31 offset:22528\n\t" \
  "v_mfma_f32_16x16x32_bf16 v[252:255], v[116:119], v[164:167], v[252:255]\n\t" \
  "ds_read_b64_tr_b16 v[218:219], v31 offset:23040\n\t" \
  "v_mfma_f32_16x16x32_bf16 v[252:255], v[108:111], v[156:159], v[252:255]\n\t" \
  "ds_read_b64_tr_b16 v[220:221], v31 offset:26624\n\t" \
  "v_mfma_f32_16x16x32_bf16 v[252:255], v[120:123], v[152:155], v[252:255]\n\t" \
  "v_mov_b32 v19, 15\n\t" \
  "ds_read_b64_tr_b16 v[222:223], v31 offset:27136\n\t" \
  "v_mfma_f32_16x16x32_bf16 v[252:255], v[128:131], v[144:147], v[252:255]\n\t" \
  "v_cvt_pk_bf16_f32 v49, v250, v251\n\t" \
  "v_cvt_pk_bf16_f32 v48, v248, v249\n\t" \
  "v_mfma_f32_16x16x32_bf16 v[252:255], v[136:139], v[104:107], v[252:255]\n\t" \
  "ds_write_b64 v14, v[48:49] offset:3072\n\t" \
  "v_mfma_f32_16x16x32_bf16 v[252:255], v[96:99], v[100:103], v[252:255]\n\t" \
  "ds_read_b32 v16, v56 offset:96\n\t" \
  "s_nop 6\n\t" \
  "v_cvt_pk_bf16_f32 v51, v254, v255\n\t" \
  "v_cvt_pk_bf16_f32 v50, v252, v253\n\t" \
  "ds_write_b64 v14, v[50:51] offset:3584\n\t" \
  "ds_write_b32 v18, v19 offset:32\n\t" \
  "s_cmp_eq_u32 s75, 0\n\t" \
  "s_cbranch_scc1 38f\n\t" \
  "39:\n\t" \
  "s_waitcnt vmcnt(6)\n\t" \
  "v_mfma_f32_16x16x32_bf16 v[224:227], v[192:195], v[64:67], 0\n\t" \
  "s_waitcnt lgkmcnt(6)\n\t" \
  "v_mfma_f32_16x16x32_bf16 v[224:227], v[196:199], v[68:71], v[224:227]\n\t" \
  "s_lshr_b64 s[90:91], %18, 8\n\t" \
  "s_and_b32 s90, s90, 15\n\t" \
  "s_lshl_b32 s90, s90, 15\n\t" \
  "s_add_u32 s90, s90, s92\n\t" \
  "s_addc_u32 s91, s93, 0\n\t" \
  "global_load_dwordx4 v[96:99], v53, s[90:91] offset:0 sc1\n\t" \
  "s_waitcnt vmcnt(5)\n\t" \
  "v_mfma_f32_16x16x32_bf16 v[228:231], v[196:199], v[76:79], 0\n\t" \
  "v_mfma_f32_16x16x32_bf16 v[228:231], v[192:195], v[72:75], v[228:231]\n\t" \
  "global_load_dwordx4 v[100:103], v53, s[90:91] offset:1024 sc1\n\t" \
  "v_mfma_f32_16x16x32_bf16 v[232:235], v[200:203], v[64:67], 0\n\t" \
  "ds_read_b64_tr_b16 v[192:193], v28 offset:30720\n\t" \
  "v_mfma_f32_16x16x32_bf16 v[232:235], v[204:207], v[68:71], v[232:235]\n\t" \
  "ds_read_b64_tr_b16 v[194:195], v28 offset:31232\n\t" \
  "global_load_dwordx4 v[104:107], v53, s[90:91] offset:2048 sc1\n\t" \
  "v_mfma_f32_16x16x32_bf16 v[236:239], v[204:207], v[76:79], 0\n\t" \
  "ds_read_b64_tr_b16 v[196:197], v28 offset:34816\n\t" \
  "v_mfma_f32_16x16x32_bf16 v[236:239], v[200:203], v[72:75], v[236:239]\n\t" \
  "ds_read_b64_tr_b16 v[198:199], v28 offset:35328\n\t" \
  "global_load_dwordx4 v[108:111], v53, s[90:91] offset:3072 sc1\n\t" \
  "v_mfma_f32_16x16x32_bf16 v[240:243], v[208:211], v[64:67], 0\n\t" \
  "ds_read_b64_tr_b16 v[200:201], v29 offset:30720\n\t" \
  "v_mfma_f32_16x16x32_bf16 v[240:243], v[212:215], v[68:71], v[240:243]\n\t" \
  "ds_read_b64_tr_b16 v[202:203], v29 offset:31232\n\t" \
  "s_lshr_b64 s[90:91], %18, 12\n\t" \
  "s_and_b32 s90, s90, 15\n\t" \
  "s_lshl_b32 s90, s90, 15\n\t" \
  "s_add_u32 s90, s90, s92\n\t" \
  "s_addc_u32 s91, s93, 0\n\t" \
  "global_load_dwordx4 v[112:115], v53, s[90:91] offset:0 sc1\n\t" \
  "v_mfma_f32_16x16x32_bf16 v[244:247], v[212:215], v[76:79], 0\n\t" \
  "ds_read_b64_tr_b16 v[204:205], v29 offset:34816\n\t" \
  "v_mfma_f32_16x16x32_bf16 v[244:247], v[208:211], v[72:75], v[244:247]\n\t" \
  "ds_read_b64_tr_b16 v[206:207], v29 offset:35328\n\t" \
  "global_load_dwordx4 v[116:119], v53, s[90:91] offset:1024 sc1\n\t" \
  "v_mfma_f32_16x16x32_bf16 v[248:251], v[216:219], v[64:67], 0\n\t" \
  "ds_read_b64_tr_b16 v[208:209], v30 offset:30720\n\t" \
  "s_waitcnt lgkmcnt(6)\n\t" \
  "v_mfma_f32_16x16x32_bf16 v[248:251], v[220:223], v[68:71], v[248:251]\n\t" \
  "ds_read_b64_tr_b16 v[210:211], v30 offset:31232\n\t" \
  "global_load_dwordx4 v[120:123], v53, s[90:91] offset:2048 sc1\n\t" \
  "v_mfma_f32_16x16x32_bf16 v[252:255], v[220:223], v[76:79], 0\n\t" \
  "ds_read_b64_tr_b16 v[212:213], v30 offset:34816\n\t" \
  "v_mfma_f32_16x16x32_bf16 v[252:255], v[216:219], v[72:75], v[252:255]\n\t" \
  "ds_read_b64_tr_b16 v[214:215], v30 offset:35328\n\t" \
  "global_load_dwordx4 v[124:127], v53, s[90:91] offset:3072 sc1\n\t" \
  "s_waitcnt vmcnt(10)\n\t" \
  "v_mfma_f32_16x16x32_bf16 v[224:227], v[192:195], v[80:83], v[224:227]\n\t" \
  "ds_read_b64_tr_b16 v[216:217], v31 offset:30720\n\t" \
  "s_lshr_b64 s[90:91], %18, 16\n\t" \
  "s_and_b32 s90, s90, 15\n\t" \
  "s_lshl_b32 s90, s90, 15\n\t" \
  "s_waitcnt lgkmcnt(6)\n\t" \
  "v_mfma_f32_16x16x32_bf16 v[224:227], v[196:199], v[84:87], v[224:227]\n\t" \
  "ds_read_b64_tr_b16 v[218:219], v31 offset:31232\n\t" \
  "s_add_u32 s90, s90, s92\n\t" \
  "s_addc_u32 s91, s93, 0\n\t" \
  "s_waitcnt vmcnt(8)\n\t" \
  "v_mfma_f32_16x16x32_bf16 v[228:231], v[196:199], v[92:95], v[228:231]\n\t" \
  "ds_read_b64_tr_b16 v[220:221], v31 offset:34816\n\t" \
  "global_load_dwordx4 v[128:131], v53, s[90:91] offset:0 sc1\n\t" \
  "v_mfma_f32_16x16x32_bf16 v[228:231], v[192:195], v[88:91], v[228:231]\n\t" \
  "ds_read_b64_tr_b16 v[222:223], v31 offset:35328\n\t" \
  "v_mfma_f32_16x16x32_bf16 v[232:235], v[200:203], v[80:83], v[232:235]\n\t" \
  "ds_read_b64_tr_b16 v[192:193], v28 offset:38912\n\t" \
  "s_waitcnt lgkmcnt(6)\n\t" \
  "v_mfma_f32_16x16x32_bf16 v[232:235], v[204:207], v[84:87], v[232:235]\n\t" \
  "ds_read_b64_tr_b16 v[194:195], v28 offset:39424\n\t" \
  "ds_write_b32 v18, v17 offset:160\n\t" \
  "v_mfma_f32_16x16x32_bf16 v[236:239], v[204:207], v[92:95], v[236:239]\n\t" \
  "ds_read_b64_tr_b16 v[196:197], v28 offset:43008\n\t" \
  "global_load_dwordx4 v[132:135], v53, s[90:91] offset:1024 sc1\n\t" \
  "v_mfma_f32_16x16x32_bf16 v[236:239], v[200:203], v[88:91], v[236:239]\n\t" \
  "ds_read_b64_tr_b16 v[198:199], v28 offset:43520\n\t" \
  "v_mfma_f32_16x16x32_bf16 v[240:243], v[208:211], v[80:83], v[240:243]\n\t" \
  "ds_read_b64_tr_b16 v[200:201], v29 offset:38912\n\t" \
  "s_waitcnt lgkmcnt(7)\n\t" \
  "v_mfma_f32_16x16x32_bf16 v[240:243], v[212:215], v[84:87], v[240:243]\n\t" \
  "ds_read_b64_tr_b16 v[202:203], v29 offset:39424\n\t" \
  "v_mfma_f32_16x16x32_bf16 v[244:247], v[212:215], v[92:95], v[244:247]\n\t" \
  "ds_read_b64_tr_b16 v[204:205], v29 offset:43008\n\t" \
  "global_load_dwordx4 v[136:139], v53, s[90:91] offset:2048 sc1\n\t" \
  "v_mfma_f32_16x16x32_bf16 v[244:247], v[208:211], v[88:91], v[244:247]\n\t" \
  "ds_read_b64_tr_b16 v[206:207], v29 offset:43520\n\t" \
  "v_mfma_f32_16x16x32_bf16 v[248:251], v[216:219], v[80:83], v[248:251]\n\t" \
  "ds_read_b64_tr_b16 v[208:209], v30 offset:38912\n\t" \
  "s_waitcnt lgkmcnt(6)\n\t" \
  "v_mfma_f32_16x16x32_bf16 v[248:251], v[220:223], v[84:87], v[248:251]\n\t" \
  "ds_read_b64_tr_b16 v[210:211], v30 offset:39424\n\t" \
  "v_mfma_f32_16x16x32_bf16 v[252:255], v[220:223], v[92:95], v[252:255]\n\t" \
  "ds_read_b64_tr_b16 v[212:213], v30 offset:43008\n\t" \
  "global_load_dwordx4 v[140:143], v53, s[90:91] offset:3072 sc1\n\t" \
  "v_mfma_f32_16x16x32_bf16 v[252:255], v[216:219], v[88:91], v[252:255]\n\t" \
  "ds_read_b64_tr_b16 v[214:215], v30 offset:43520\n\t" \
  "s_waitcnt vmcnt(10)\n\t" \
  "v_mfma_f32_16x16x32_bf16 v[224:227], v[192:195], v[96:99], v[224:227]\n\t" \
  "ds_read_b64_tr_b16 v[216:217], v31 offset:38912\n\t" \
  "s_lshr_b64 s[90:91], %18, 20\n\t" \
  "s_and_b32 s90, s90, 15\n\t" \
  "s_lshl_b32 s90, s90, 15\n\t" \
  "s_waitcnt lgkmcnt(6)\n\t" \
  "v_mfma_f32_16x16x32_bf16 v[224:227], v[196:199], v[100:103], v[224:227]\n\t" \
  "ds_read_b64_tr_b16 v[218:219], v31 offset:39424\n\t" \
  "s_add_u32 s90, s90, s92\n\t" \
  "s_addc_u32 s91, s93, 0\n\t" \
  "s_waitcnt vmcnt(8)\n\t" \
  "v_mfma_f32_16x16x32_bf16 v[228:231], v[196:199], v[108:111], v[228:231]\n\t" \
  "ds_read_b64_tr_b16 v[220:221], v31 offset:43008\n\t" \
  "global_load_dwordx4 v[144:147], v53, s[90:91] offset:0 sc1\n\t" \
  "v_mfma_f32_16x16x32_bf16 v[228:231], v[192:195], v[104:107], v[228:231]\n\t" \
  "ds_read_b64_tr_b16 v[222:223], v31 offset:43520\n\t" \
  "v_mfma_f32_16x16x32_bf16 v[232:235], v[200:203], v[96:99], v[232:235]\n\t" \
  "ds_read_b64_tr_b16 v[192:193], v28 offset:47104\n\t" \
  "s_waitcnt lgkmcnt(6)\n\t" \
  "v_mfma_f32_16x16x32_bf16 v[232:235], v[204:207], v[100:103], v[232:235]\n\t" \
  "ds_read_b64_tr_b16 v[194:195], v28 offset:47616\n\t" \
  "v_mfma_f32_16x16x32_bf16 v[236:239], v[204:207], v[108:111], v[236:239]\n\t" \
  "ds_read_b64_tr_b16 v[196:197], v28 offset:51200\n\t" \
  "global_load_dwordx4 v[148:151], v53, s[90:91] offset:1024 sc1\n\t" \
  "v_mfma_f32_16x16x32_bf16 v[236:239], v[200:203], v[104:107], v[236:239]\n\t" \
  "ds_read_b64_tr_b16 v[198:199], v28 offset:51712\n\t" \
  "v_mfma_f32_16x16x32_bf16 v[240:243], v[208:211], v[96:99], v[240:243]\n\t" \
  "ds_read_b64_tr_b16 v[200:201], v29 offset:47104\n\t" \
  "s_waitcnt lgkmcnt(6)\n\t" \
  "v_mfma_f32_16x16x32_bf16 v[240:243], v[212:215], v[100:103], v[240:243]\n\t" \
  "ds_read_b64_tr_b16 v[202:203], v29 offset:47616\n\t" \
  "v_mfma_f32_16x16x32_bf16 v[244:247], v[212:215], v[108:111], v[244:247]\n\t" \
  "ds_read_b64_tr_b16 v[204:205], v29 offset:51200\n\t" \
  "global_load_dwordx4 v[152:155], v53, s[90:91] offset:2048 sc1\n\t" \
  "v_mfma_f32_16x16x32_bf16 v[244:247], v[208:211], v[104:107], v[244:247]\n\t" \
  "ds_read_b64_tr_b16 v[206:207], v29 offset:51712\n\t" \
  "v_mfma_f32_16x16x32_bf16 v[248:251], v[216:219], v[96:99], v[248:251]\n\t" \
  "ds_read_b64_tr_b16 v[208:209], v30 offset:47104\n\t" \
  "s_waitcnt lgkmcnt(6)\n\t" \
  "v_mfma_f32_16x16x32_bf16 v[248:251], v[220:223], v[100:103], v[248:251]\n\t" \
  "ds_read_b64_tr_b16 v[210:211], v30 offset:47616\n\t" \
  "v_mfma_f32_16x16x32_bf16 v[252:255], v[220:223], v[108:111], v[252:255]\n\t" \
  "ds_read_b64_tr_b16 v[212:213], v30 offset:51200\n\t" \
  "global_load_dwordx4 v[156:159], v53, s[90:91] offset:3072 sc1\n\t" \
  "v_mfma_f32_16x16x32_bf16 v[252:255], v[216:219], v[104:107], v[252:255]\n\t" \
  "ds_read_b64_tr_b16 v[214:215], v30 offset:51712\n\t" \
  "s_waitcnt vmcnt(10)\n\t" \
  "v_mfma_f32_16x16x32_bf16 v[224:227], v[192:195], v[112:115], v[224:227]\n\t" \
  "ds_read_b64_tr_b16 v[216:217], v31 offset:47104\n\t" \
  "s_lshr_b64 s[90:91], %18, 24\n\t" \
  "s_and_b32 s90, s90, 15\n\t" \
  "s_lshl_b32 s90, s90, 15\n\t" \
  "s_waitcnt lgkmcnt(6)\n\t" \
  "v_mfma_f32_16x16x32_bf16 v[224:227], v[196:199], v[116:119], v[224:227]\n\t" \
  "ds_read_b64_tr_b16 v[218:219], v31 offset:47616\n\t" \
  "s_add_u32 s90, s90, s92\n\t" \
  "s_addc_u32 s91, s93, 0\n\t" \
  "s_waitcnt vmcnt(8)\n\t" \
  "v_mfma_f32_16x16x32_bf16 v[228:231], v[196:199], v[124:127], v[228:231]\n\t" \
  "ds_read_b64_tr_b16 v[220:221], v31 offset:51200\n\t" \
  "global_load_dwordx4 v[160:163], v53, s[90:91] offset:0 sc1\n\t" \
  "v_mfma_f32_16x16x32_bf16 v[228:231], v[192:195], v[120:123], v[228:231]\n\t" \
  "ds_read_b64_tr_b16 v[222:223], v31 offset:51712\n\t" \
  "v_mfma_f32_16x16x32_bf16 v[232:235], v[200:203], v[112:115], v[232:235]\n\t" \
  "ds_read_b64_tr_b16 v[192:193], v28 offset:55296\n\t" \
  "s_waitcnt lgkmcnt(6)\n\t" \
  "v_mfma_f32_16x16x32_bf16 v[232:235], v[204:207], v[116:119], v[232:235]\n\t" \
  "ds_read_b64_tr_b16 v[194:195], v28 offset:55808\n\t" \
  "v_mfma_f32_16x16x32_bf16 v[236:239], v[204:207], v[124:127], v[236:239]\n\t" \
  "ds_read_b64_tr_b16 v[196:197], v28 offset:59392\n\t" \
  "global_load_dwordx4 v[164:167], v53, s[90:91] offset:1024 sc1\n\t" \
  "ds_read_b32 v16, v56 offset:96\n\t" \
  "v_mfma_f32_16x16x32_bf16 v[236:239], v[200:203], v[120:123], v[236:239]\n\t" \
  "ds_read_b64_tr_b16 v[198:199], v28 offset:59904\n\t" \
  "v_mfma_f32_16x16x32_bf16 v[240:243], v[208:211], v[112:115], v[240:243]\n\t" \
  "ds_read_b64_tr_b16 v[200:201], v29 offset:55296\n\t" \
  "s_waitcnt lgkmcnt(7)\n\t" \
  "v_mfma_f32_16x16x32_bf16 v[240:243], v[212:215], v[116:119], v[240:243]\n\t" \
  "ds_read_b64_tr_b16 v[202:203], v29 offset:55808\n\t" \
  "v_mfma_f32_16x16x32_bf16 v[244:247], v[212:215], v[124:127], v[244:247]\n\t" \
  "ds_read_b64_tr_b16 v[204:205], v29 offset:59392\n\t" \
  "global_load_dwordx4 v[168:171], v53, s[90:91] offset:2048 sc1\n\t" \
  "v_mfma_f32_16x16x32_bf16 v[244:247], v[208:211], v[120:123], v[244:247]\n\t" \
  "ds_read_b64_tr_b16 v[206:207], v29 offset:59904\n\t" \
  "v_mfma_f32_16x16x32_bf16 v[248:251], v[216:219], v[112:115], v[248:251]\n\t" \
  "ds_read_b64_tr_b16 v[208:209], v30 offset:55296\n\t" \
  "s_waitcnt lgkmcnt(6)\n\t" \
  "v_mfma_f32_16x16x32_bf16 v[248:251], v[220:223], v[116:119], v[248:251]\n\t" \
  "ds_read_b64_tr_b16 v[210:211], v30 offset:55808\n\t" \
  "v_mfma_f32_16x16x32_bf16 v[252:255], v[220:223], v[124:127], v[252:255]\n\t" \
  "ds_read_b64_tr_b16 v[212:213], v30 offset:59392\n\t" \
  "global_load_dwordx4 v[172:175], v53, s[90:91] offset:3072 sc1\n\t" \
  "v_readfirstlane_b32 vcc_lo, v16\n\t" \
  "v_mfma_f32_16x16x32_bf16 v[252:255], v[216:219], v[120:123], v[252:255]\n\t" \
  "ds_read_b64_tr_b16 v[214:215], v30 offset:59904\n\t" \
  "s_cmp_ge_u32 vcc_lo, 2\n\t" \
  "s_cbranch_scc0 43f\n\t" \
  "44:\n\t" \
  "s_waitcnt vmcnt(10)\n\t" \
  "v_mfma_f32_16x16x32_bf16 v[224:227], v[192:195], v[128:131], v[224:227]\n\t" \
  "ds_read_b64_tr_b16 v[216:217], v31 offset:55296\n\t" \
  "s_lshr_b64 s[90:91], %18, 28\n\t" \
  "s_and_b32 s90, s90, 15\n\t" \
  "s_lshl_b32 s90, s90, 15\n\t" \
  "s_waitcnt lgkmcnt(6)\n\t" \
  "v_mfma_f32_16x16x32_bf16 v[224:227], v[196:199], v[132:135], v[224:227]\n\t" \
  "ds_read_b64_tr_b16 v[218:219], v31 offset:55808\n\t" \
  "s_add_u32 s90, s90, s92\n\t" \
  "s_addc_u32 s91, s93, 0\n\t" \
  "s_waitcnt vmcnt(8)\n\t" \
  "v_mfma_f32_16x16x32_bf16 v[228:231], v[196:199], v[140:143], v[228:231]\n\t" \
  "ds_read_b64_tr_b16 v[220:221], v31 offset:59392\n\t" \
  "global_load_dwordx4 v[176:179], v53, s[90:91] offset:0 sc1\n\t" \
  "v_mfma_f32_16x16x32_bf16 v[228:231], v[192:195], v[136:139], v[228:231]\n\t" \
  "ds_read_b64_tr_b16 v[222:223], v31 offset:59904\n\t" \
  "v_mfma_f32_16x16x32_bf16 v[232:235], v[200:203], v[128:131], v[232:235]\n\t" \
  "ds_read_b64_tr_b16 v[192:193], v28 offset:63488\n\t" \
  "s_waitcnt lgkmcnt(6)\n\t" \
  "v_mfma_f32_16x16x32_bf16 v[232:235], v[204:207], v[132:135], v[232:235]\n\t" \
  "ds_read_b64_tr_b16 v[194:195], v28 offset:64000\n\t" \
  "v_mfma_f32_16x16x32_bf16 v[236:239], v[204:207], v[140:143], v[236:239]\n\t" \
  "ds_read_b64_tr_b16 v[196:197], v32 offset:2048\n\t" \
  "global_load_dwordx4 v[180:183], v53, s[90:91] offset:1024 sc1\n\t" \
  "v_mfma_f32_16x16x32_bf16 v[236:239], v[200:203], v[136:139], v[236:239]\n\t" \
  "ds_read_b64_tr_b16 v[198:199], v32 offset:2560\n\t" \
  "v_mfma_f32_16x16x32_bf16 v[240:243], v[208:211], v[128:131], v[240:243]\n\t" \
  "ds_read_b64_tr_b16 v[200:201], v29 offset:63488\n\t" \
  "s_waitcnt lgkmcnt(6)\n\t" \
  "v_mfma_f32_16x16x32_bf16 v[240:243], v[212:215], v[132:135], v[240:243]\n\t" \
  "ds_read_b64_tr_b16 v[202:203], v29 offset:64000\n\t" \
  "v_mfma_f32_16x16x32_bf16 v[244:247], v[212:215], v[140:143], v[244:247]\n\t" \
  "ds_read_b64_tr_b16 v[204:205], v33 offset:2048\n\t" \
  "global_load_dwordx4 v[184:187], v53, s[90:91] offset:2048 sc1\n\t" \
  "v_mfma_f32_16x16x32_bf16 v[244:247], v[208:211], v[136:139], v[244:247]\n\t" \
  "ds_read_b64_tr_b16 v[206:207], v33 offset:2560\n\t" \
  "v_mfma_f32_16x16x32_bf16 v[248:251], v[216:219], v[128:131], v[248:251]\n\t" \
  "ds_read_b64_tr_b16 v[208:209], v30 offset:63488\n\t" \
  "s_waitcnt lgkmcnt(6)\n\t" \
  "v_mfma_f32_16x16x32_bf16 v[248:251], v[220:223], v[132:135], v[248:251]\n\t" \
  "ds_read_b64_tr_b16 v[210:211], v30 offset:64000\n\t" \
  "v_mfma_f32_16x16x32_bf16 v[252:255], v[220:223], v[140:143], v[252:255]\n\t" \
  "ds_read_b64_tr_b16 v[212:213], v34 offset:2048\n\t" \
  "global_load_dwordx4 v[188:191], v53, s[90:91] offset:3072 sc1\n\t" \
  "v_mfma_f32_16x16x32_bf16 v[252:255], v[216:219], v[136:139], v[252:255]\n\t" \
  "ds_read_b64_tr_b16 v[214:215], v34 offset:2560\n\t" \
  "s_waitcnt vmcnt(10)\n\t" \
  "v_mfma_f32_16x16x32_bf16 v[224:227], v[192:195], v[144:147], v[224:227]\n\t" \
  "ds_read_b64_tr_b16 v[216:217], v31 offset:63488\n\t" \
  "s_lshr_b64 s[90:91], %18, 32\n\t" \
  "s_and_b32 s90, s90, 15\n\t" \
  "s_lshl_b32 s90, s90, 15\n\t" \
  "s_waitcnt lgkmcnt(6)\n\t" \
  "v_mfma_f32_16x16x32_bf16 v[224:227], v[196:199], v[148:151], v[224:227]\n\t" \
  "ds_read_b64_tr_b16 v[218:219], v31 offset:64000\n\t" \
  "s_add_u32 s90, s90, s92\n\t" \
  "s_addc_u32 s91, s93, 0\n\t" \
  "s_waitcnt vmcnt(8)\n\t" \
  "v_mfma_f32_16x16x32_bf16 v[228:231], v[196:199], v[156:159], v[228:231]\n\t" \
  "ds_read_b64_tr_b16 v[220:221], v35 offset:2048\n\t" \
  "global_load_dwordx4 v[64:67], v53, s[90:91] offset:0 sc1\n\t" \
  "v_mfma_f32_16x16x32_bf16 v[228:231], v[192:195], v[152:155], v[228:231]\n\t" \
  "ds_read_b64_tr_b16 v[222:223], v35 offset:2560\n\t" \
  "v_mfma_f32_16x16x32_bf16 v[232:235], v[200:203], v[144:147], v[232:235]\n\t" \
  "ds_read_b64_tr_b16 v[192:193], v32 offset:6144\n\t" \
  "s_waitcnt lgkmcnt(6)\n\t" \
  "v_mfma_f32_16x16x32_bf16 v[232:235], v[204:207], v[148:151], v[232:235]\n\t" \
  "ds_read_b64_tr_b16 v[194:195], v32 offset:6656\n\t" \
  "v_mfma_f32_16x16x32_bf16 v[236:239], v[204:207], v[156:159], v[236:239]\n\t" \
  "ds_read_b64_tr_b16 v[196:197], v32 offset:10240\n\t" \
  "global_load_dwordx4 v[68:71], v53, s[90:91] offset:1024 sc1\n\t" \
  "v_mfma_f32_16x16x32_bf16 v[236:239], v[200:203], v[152:155], v[236:239]\n\t" \
  "ds_read_b64_tr_b16 v[198:199], v32 offset:10752\n\t" \
  "v_mfma_f32_16x16x32_bf16 v[240:243], v[208:211], v[144:147], v[240:243]\n\t" \
  "ds_read_b64_tr_b16 v[200:201], v33 offset:6144\n\t" \
  "s_waitcnt lgkmcnt(6)\n\t" \
  "v_mfma_f32_16x16x32_bf16 v[240:243], v[212:215], v[148:151], v[240:243]\n\t" \
  "ds_read_b128 v[40:43], v56 offset:80\n\t" \
  "ds_read_b64_tr_b16 v[202:203], v33 offset:6656\n\t" \
  "v_mfma_f32_16x16x32_bf16 v[244:247], v[212:215], v[156:159], v[244:247]\n\t" \
  "ds_read_b64_tr_b16 v[204:205], v33 offset:10240\n\t" \
  "global_load_dwordx4 v[72:75], v53, s[90:91] offset:2048 sc1\n\t" \
  "v_mfma_f32_16x16x32_bf16 v[244:247], v[208:211], v[152:155], v[244:247]\n\t" \
  "ds_read_b64_tr_b16 v[206:207], v33 offset:10752\n\t" \
  "v_mfma_f32_16x16x32_bf16 v[248:251], v[216:219], v[144:147], v[248:251]\n\t" \
  "ds_read_b64_tr_b16 v[208:209], v34 offset:6144\n\t" \
  "s_waitcnt lgkmcnt(7)\n\t" \
  "v_mfma_f32_16x16x32_bf16 v[248:251], v[220:223], v[148:151], v[248:251]\n\t" \
  "ds_read_b64_tr_b16 v[210:211], v34 offset:6656\n\t" \
  "v_mfma_f32_16x16x32_bf16 v[252:255], v[220:223], v[156:159], v[252:255]\n\t" \
  "ds_read_b64_tr_b16 v[212:213], v34 offset:10240\n\t" \
  "global_load_dwordx4 v[76:79], v53, s[90:91] offset:3072 sc1\n\t" \
  "v_mfma_f32_16x16x32_bf16 v[252:255], v[216:219], v[152:155], v[252:255]\n\t" \
  "ds_read_b64_tr_b16 v[214:215], v34 offset:10752\n\t" \
  "s_waitcnt vmcnt(10)\n\t" \
  "v_mfma_f32_16x16x32_bf16 v[224:227], v[192:195], v[160:163], v[224:227]\n\t" \
  "s_waitcnt lgkmcnt(7)\n\t" \
  "v_min_u32 v40, v40, v41\n\t" \
  "v_min3_u32 v40, v40, v42, v43\n\t" \
  "ds_read_b64_tr_b16 v[216:217], v35 offset:6144\n\t" \
  "s_lshr_b64 s[90:91], %18, 36\n\t" \
  "s_and_b32 s90, s90, 15\n\t" \
  "s_lshl_b32 s90, s90, 15\n\t" \
  "v_mfma_f32_16x16x32_bf16 v[224:227], v[196:199], v[164:167], v[224:227]\n\t" \
  "v_readfirstlane_b32 vcc_lo, v40\n\t" \
  "ds_read_b64_tr_b16 v[218:219], v35 offset:6656\n\t" \
  "s_add_u32 s90, s90, s92\n\t" \
  "s_addc_u32 s91, s93, 0\n\t" \
  "s_waitcnt vmcnt(8)\n\t" \
  "v_mfma_f32_16x16x32_bf16 v[228:231], v[196:199], v[172:175], v[228:231]\n\t" \
  "s_cmp_ge_u32 vcc_lo, 13\n\t" \
  "s_cbranch_scc0 32f\n\t" \
  "33:\n\t" \
  "ds_read_b64_tr_b16 v[220:221], v35 offset:10240\n\t" \
  "global_load_dwordx4 v[80:83], v53, s[90:91] offset:0 sc1\n\t" \
  "v_mfma_f32_16x16x32_bf16 v[228:231], v[192:195], v[168:171], v[228:231]\n\t" \
  "ds_read_b64_tr_b16 v[222:223], v35 offset:10752\n\t" \
  "s_waitcnt lgkmcnt(6)\n\t" \
  "v_mfma_f32_16x16x32_bf16 v[232:235], v[200:203], v[160:163], v[232:235]\n\t" \
  "ds_read_b64_tr_b16 v[192:193], v32 offset:14336\n\t" \
  "v_mfma_f32_16x16x32_bf16 v[232:235], v[204:207], v[164:167], v[232:235]\n\t" \
  "ds_read_b64_tr_b16 v[194:195], v32 offset:14848\n\t" \
  "v_mfma_f32_16x16x32_bf16 v[236:239], v[204:207], v[172:175], v[236:239]\n\t" \
  "ds_read_b64_tr_b16 v[196:197], v32 offset:18432\n\t" \
  "global_load_dwordx4 v[84:87], v53, s[90:91] offset:1024 sc1\n\t" \
  "v_mfma_f32_16x16x32_bf16 v[236:239], v[200:203], v[168:171], v[236:239]\n\t" \
  "ds_read_b64_tr_b16 v[198:199], v32 offset:18944\n\t" \
  "v_mfma_f32_16x16x32_bf16 v[240:243], v[208:211], v[160:163], v[240:243]\n\t" \
  "ds_read_b64_tr_b16 v[200:201], v33 offset:14336\n\t" \
  "s_waitcnt lgkmcnt(6)\n\t" \
  "v_mfma_f32_16x16x32_bf16 v[240:243], v[212:215], v[164:167], v[240:243]\n\t" \
  "ds_read_b64_tr_b16 v[202:203], v33 offset:14848\n\t" \
  "v_mfma_f32_16x16x32_bf16 v[244:247], v[212:215], v[172:175], v[244:247]\n\t" \
  "ds_read_b64_tr_b16 v[204:205], v33 offset:18432\n\t" \
  "global_load_dwordx4 v[88:91], v53, s[90:91] offset:2048 sc1\n\t" \
  "v_mfma_f32_16x16x32_bf16 v[244:247], v[208:211], v[168:171], v[244:247]\n\t" \
  "ds_read_b64_tr_b16 v[206:207], v33 offset:18944\n\t" \
  "v_mfma_f32_16x16x32_bf16 v[248:251], v[216:219], v[160:163], v[248:251]\n\t" \
  "ds_read_b64_tr_b16 v[208:209], v34 offset:14336\n\t" \
  "s_waitcnt lgkmcnt(6)\n\t" \
  "v_mfma_f32_16x16x32_bf16 v[248:251], v[220:223], v[164:167], v[248:251]\n\t" \
  "ds_read_b64_tr_b16 v[210:211], v34 offset:14848\n\t" \
  "v_mfma_f32_16x16x32_bf16 v[252:255], v[220:223], v[172:175], v[252:255]\n\t" \
  "ds_read_b64_tr_b16 v[212:213], v34 offset:18432\n\t" \
  "global_load_dwordx4 v[92:95], v53, s[90:91] offset:3072 sc1\n\t" \
  "v_mfma_f32_16x16x32_bf16 v[252:255], v[216:219], v[168:171], v[252:255]\n\t" \
  "ds_read_b64_tr_b16 v[214:215], v34 offset:18944\n\t" \
  "s_waitcnt vmcnt(10)\n\t" \
  "v_mfma_f32_16x16x32_bf16 v[224:227], v[192:195], v[176:179], v[224:227]\n\t" \
  "ds_read_b64_tr_b16 v[216:217], v35 offset:14336\n\t" \
  "s_lshr_b64 s[90:91], %18, 40\n\t" \
  "s_and_b32 s90, s90, 15\n\t" \
  "s_lshl_b32 s90, s90, 15\n\t" \
  "s_waitcnt lgkmcnt(6)\n\t" \
  "v_mfma_f32_16x16x32_bf16 v[224:227], v[196:199], v[180:183], v[224:227]\n\t" \
  "ds_read_b64_tr_b16 v[218:219], v35 offset:14848\n\t" \
  "s_add_u32 s90, s90, s92\n\t" \
  "s_addc_u32 s91, s93, 0\n\t" \
  "s_waitcnt vmcnt(8)\n\t" \
  "v_mfma_f32_16x16x32_bf16 v[228:231], v[196:199], v[188:191], v[228:231]\n\t" \
  "ds_read_b64_tr_b16 v[220:221], v35 offset:18432\n\t" \
  "global_load_dwordx4 v[96:99], v53, s[90:91] offset:0 sc1\n\t" \
  "v_mfma_f32_16x16x32_bf16 v[228:231], v[192:195], v[184:187], v[228:231]\n\t" \
  "ds_read_b64_tr_b16 v[222:223], v35 offset:18944\n\t" \
  "v_mfma_f32_16x16x32_bf16 v[232:235], v[200:203], v[176:179], v[232:235]\n\t" \
  "ds_read_b64_tr_b16 v[192:193], v32 offset:22528\n\t" \
  "s_waitcnt lgkmcnt(6)\n\t" \
  "v_mfma_f32_16x16x32_bf16 v[232:235], v[204:207], v[180:183], v[232:235]\n\t" \
  "ds_read_b64_tr_b16 v[194:195], v32 offset:23040\n\t" \
  "v_mfma_f32_16x16x32_bf16 v[236:239], v[204:207], v[188:191], v[236:239]\n\t" \
  "ds_read_b64_tr_b16 v[196:197], v32 offset:26624\n\t" \
  "global_load_dwordx4 v[100:103], v53, s[90:91] offset:1024 sc1\n\t" \
  "v_mfma_f32_16x16x32_bf16 v[236:239], v[200:203], v[184:187], v[236:239]\n\t" \
  "ds_read_b64_tr_b16 v[198:199], v32 offset:27136\n\t" \
  "v_mfma_f32_16x16x32_bf16 v[240:243], v[208:211], v[176:179], v[240:243]\n\t" \
  "ds_read_b64_tr_b16 v[200:201], v33 offset:22528\n\t" \
  "s_waitcnt lgkmcnt(6)\n\t" \
  "v_mfma_f32_16x16x32_bf16 v[240:243], v[212:215], v[180:183], v[240:243]\n\t" \
  "ds_read_b64_tr_b16 v[202:203], v33 offset:23040\n\t" \
  "v_mfma_f32_16x16x32_bf16 v[244:247], v[212:215], v[188:191], v[244:247]\n\t" \
  "ds_read_b64_tr_b16 v[204:205], v33 offset:26624\n\t" \
  "global_load_dwordx4 v[104:107], v53, s[90:91] offset:2048 sc1\n\t" \
  "v_mfma_f32_16x16x32_bf16 v[244:247], v[208:211], v[184:187], v[244:247]\n\t" \
  "ds_read_b64_tr_b16 v[206:207], v33 offset:27136\n\t" \
  "v_mfma_f32_16x16x32_bf16 v[248:251], v[216:219], v[176:179], v[248:251]\n\t" \
  "ds_read_b64_tr_b16 v[208:209], v34 offset:22528\n\t" \
  "s_waitcnt lgkmcnt(6)\n\t" \
  "v_mfma_f32_16x16x32_bf16 v[248:251], v[220:223], v[180:183], v[248:251]\n\t" \
  "ds_read_b64_tr_b16 v[210:211], v34 offset:23040\n\t" \
  "v_mfma_f32_16x16x32_bf16 v[252:255], v[220:223], v[188:191], v[252:255]\n\t" \
  "ds_read_b64_tr_b16 v[212:213], v34 offset:26624\n\t" \
  "global_load_dwordx4 v[108:111], v53, s[90:91] offset:3072 sc1\n\t" \
  "ds_write_b32 v56, v56 offset:108\n\t" \
  "v_mfma_f32_16x16x32_bf16 v[252:255], v[216:219], v[184:187], v[252:255]\n\t" \
  "ds_read_b64_tr_b16 v[214:215], v34 offset:27136\n\t" \
  "s_waitcnt vmcnt(10)\n\t" \
  "v_mfma_f32_16x16x32_bf16 v[224:227], v[192:195], v[64:67], v[224:227]\n\t" \
  "ds_read_b64_tr_b16 v[216:217], v35 offset:22528\n\t" \
  "s_lshr_b64 s[90:91], %18, 44\n\t" \
  "s_and_b32 s90, s90, 15\n\t" \
  "s_lshl_b32 s90, s90, 15\n\t" \
  "s_waitcnt lgkmcnt(7)\n\t" \
  "v_mfma_f32_16x16x32_bf16 v[224:227], v[196:199], v[68:71], v[224:227]\n\t" \
  "ds_read_b64_tr_b16 v[218:219], v35 offset:23040\n\t" \
  "s_add_u32 s90, s90, s92\n\t" \
  "s_addc_u32 s91, s93, 0\n\t" \
  "s_waitcnt vmcnt(8)\n\t" \
  "v_mfma_f32_16x16x32_bf16 v[228:231], v[196:199], v[76:79], v[228:231]\n\t" \
  "ds_read_b64_tr_b16 v[220:221], v35 offset:26624\n\t" \
  "global_load_dwordx4 v[112:115], v53, s[90:91] offset:0 sc1\n\t" \
  "v_mfma_f32_16x16x32_bf16 v[228:231], v[192:195], v[72:75], v[228:231]\n\t" \
  "ds_read_b64_tr_b16 v[222:223], v35 offset:27136\n\t" \
  "v_mfma_f32_16x16x32_bf16 v[232:235], v[200:203], v[64:67], v[232:235]\n\t" \
  "ds_read_b64_tr_b16 v[192:193], v32 offset:30720\n\t" \
  "s_waitcnt lgkmcnt(6)\n\t" \
  "v_mfma_f32_16x16x32_bf16 v[232:235], v[204:207], v[68:71], v[232:235]\n\t" \
  "ds_read_b64_tr_b16 v[194:195], v32 offset:31232\n\t" \
  "v_mfma_f32_16x16x32_bf16 v[236:239], v[204:207], v[76:79], v[236:239]\n\t" \
  "ds_read_b64_tr_b16 v[196:197], v32 offset:34816\n\t" \
  "global_load_dwordx4 v[116:119], v53, s[90:91] offset:1024 sc1\n\t" \
  "v_mfma_f32_16x16x32_bf16 v[236:239], v[200:203], v[72:75], v[236:239]\n\t" \
  "ds_read_b64_tr_b16 v[198:199], v32 offset:35328\n\t" \
  "v_mfma_f32_16x16x32_bf16 v[240:243], v[208:211], v[64:67], v[240:243]\n\t" \
  "ds_read_b64_tr_b16 v[200:201], v33 offset:30720\n\t" \
  "s_waitcnt lgkmcnt(6)\n\t" \
  "v_mfma_f32_16x16x32_bf16 v[240:243], v[212:215], v[68:71], v[240:243]\n\t" \
  "ds_read_b64_tr_b16 v[202:203], v33 offset:31232\n\t" \
  "v_mfma_f32_16x16x32_bf16 v[244:247], v[212:215], v[76:79], v[244:247]\n\t" \
  "ds_read_b64_tr_b16 v[204:205], v33 offset:34816\n\t" \
  "global_load_dwordx4 v[120:123], v53, s[90:91] offset:2048 sc1\n\t" \
  "v_mfma_f32_16x16x32_bf16 v[244:247], v[208:211], v[72:75], v[244:247]\n\t" \
  "ds_read_b64_tr_b16 v[206:207], v33 offset:35328\n\t" \
  "v_mfma_f32_16x16x32_bf16 v[248:251], v[216:219], v[64:67], v[248:251]\n\t" \
  "ds_read_b64_tr_b16 v[208:209], v34 offset:30720\n\t" \
  "s_waitcnt lgkmcnt(6)\n\t" \
  "v_mfma_f32_16x16x32_bf16 v[248:251], v[220:223], v[68:71], v[248:251]\n\t" \
  "ds_read_b64_tr_b16 v[210:211], v34 offset:31232\n\t" \
  "v_mfma_f32_16x16x32_bf16 v[252:255], v[220:223], v[76:79], v[252:255]\n\t" \
  "ds_read_b64_tr_b16 v[212:213], v34 offset:34816\n\t" \
  "global_load_dwordx4 v[124:127], v53, s[90:91] offset:3072 sc1\n\t" \
  "v_mfma_f32_16x16x32_bf16 v[252:255], v[216:219], v[72:75], v[252:255]\n\t" \
  "ds_read_b64_tr_b16 v[214:215], v34 offset:35328\n\t" \
  "s_waitcnt vmcnt(10)\n\t" \
  "v_mfma_f32_16x16x32_bf16 v[224:227], v[192:195], v[80:83], v[224:227]\n\t" \
  "ds_read_b64_tr_b16 v[216:217], v35 offset:30720\n\t" \
  "s_lshr_b64 s[90:91], %18, 48\n\t" \
  "s_and_b32 s90, s90, 15\n\t" \
  "s_lshl_b32 s90, s90, 15\n\t" \
  "s_waitcnt lgkmcnt(6)\n\t" \
  "v_mfma_f32_16x16x32_bf16 v[224:227], v[196:199], v[84:87], v[224:227]\n\t" \
  "ds_read_b64_tr_b16 v[218:219], v35 offset:31232\n\t" \
  "s_add_u32 s90, s90, s92\n\t" \
  "s_addc_u32 s91, s93, 0\n\t" \
  "s_waitcnt vmcnt(8)\n\t" \
  "v_mfma_f32_16x16x32_bf16 v[228:231], v[196:199], v[92:95], v[228:231]\n\t" \
  "ds_read_b64_tr_b16 v[220:221], v35 offset:34816\n\t" \
  "global_load_dwordx4 v[128:131], v53, s[90:91] offset:0 sc1\n\t" \
  "v_mfma_f32_16x16x32_bf16 v[228:231], v[192:195], v[88:91], v[228:231]\n\t" \
  "ds_read_b64_tr_b16 v[222:223], v35 offset:35328\n\t" \
  "v_mfma_f32_16x16x32_bf16 v[232:235], v[200:203], v[80:83], v[232:235]\n\t" \
  "ds_read_b64_tr_b16 v[192:193], v32 offset:38912\n\t" \
  "s_waitcnt lgkmcnt(6)\n\t" \
  "v_mfma_f32_16x16x32_bf16 v[232:235], v[204:207], v[84:87], v[232:235]\n\t" \
  "ds_read_b64_tr_b16 v[194:195], v32 offset:39424\n\t" \
  "v_mfma_f32_16x16x32_bf16 v[236:239], v[204:207], v[92:95], v[236:239]\n\t" \
  "ds_read_b64_tr_b16 v[196:197], v32 offset:43008\n\t" \
  "global_load_dwordx4 v[132:135], v53, s[90:91] offset:1024 sc1\n\t" \
  "v_mfma_f32_16x16x32_bf16 v[236:239], v[200:203], v[88:91], v[236:239]\n\t" \
  "ds_read_b64_tr_b16 v[198:199], v32 offset:43520\n\t" \
  "v_mfma_f32_16x16x32_bf16 v[240:243], v[208:211], v[80:83], v[240:243]\n\t" \
  "ds_read_b64_tr_b16 v[200:201], v33 offset:38912\n\t" \
  "s_waitcnt lgkmcnt(6)\n\t" \
  "v_mfma_f32_16x16x32_bf16 v[240:243], v[212:215], v[84:87], v[240:243]\n\t" \
  "ds_read_b64_tr_b16 v[202:203], v33 offset:39424\n\t" \
  "v_mfma_f32_16x16x32_bf16 v[244:247], v[212:215], v[92:95], v[244:247]\n\t" \
  "ds_read_b64_tr_b16 v[204:205], v33 offset:43008\n\t" \
  "global_load_dwordx4 v[136:139], v53, s[90:91] offset:2048 sc1\n\t" \
  "v_mfma_f32_16x16x32_bf16 v[244:247], v[208:211], v[88:91], v[244:247]\n\t" \
  "ds_read_b64_tr_b16 v[206:207], v33 offset:43520\n\t" \
  "v_mfma_f32_16x16x32_bf16 v[248:251], v[216:219], v[80:83], v[248:251]\n\t" \
  "ds_read_b64_tr_b16 v[208:209], v34 offset:38912\n\t" \
  "s_waitcnt lgkmcnt(6)\n\t" \
  "v_mfma_f32_16x16x32_bf16 v[248:251], v[220:223], v[84:87], v[248:251]\n\t" \
  "ds_read_b64_tr_b16 v[210:211], v34 offset:39424\n\t" \
  "v_mfma_f32_16x16x32_bf16 v[252:255], v[220:223], v[92:95], v[252:255]\n\t" \
  "ds_read_b64_tr_b16 v[212:213], v34 offset:43008\n\t" \
  "global_load_dwordx4 v[140:143], v53, s[90:91] offset:3072 sc1\n\t" \
  "v_mfma_f32_16x16x32_bf16 v[252:255], v[216:219], v[88:91], v[252:255]\n\t" \
  "ds_read_b64_tr_b16 v[214:215], v34 offset:43520\n\t" \
  "s_waitcnt vmcnt(10)\n\t" \
  "v_mfma_f32_16x16x32_bf16 v[224:227], v[192:195], v[96:99], v[224:227]\n\t" \
  "ds_read_b64_tr_b16 v[216:217], v35 offset:38912\n\t" \
  "509:\n\t" \
  "s_cbranch_execz 26b\n\t" \
  "s_lshr_b64 s[90:91], %18, 52\n\t" \
  "s_and_b32 s90, s90, 15\n\t" \
  "s_lshl_b32 s90, s90, 15\n\t" \
  "s_waitcnt lgkmcnt(6)\n\t" \
  "v_mfma_f32_16x16x32_bf16 v[224:227], v[196:199], v[100:103], v[224:227]\n\t" \
  "ds_read_b64_tr_b16 v[218:219], v35 offset:39424\n\t" \
  "s_add_u32 s90, s90, s92\n\t" \
  "s_addc_u32 s91, s93, 0\n\t" \
  "s_waitcnt vmcnt(8)\n\t" \
  "v_mfma_f32_16x16x32_bf16 v[228:231], v[196:199], v[108:111], v[228:231]\n\t" \
  "ds_read_b64_tr_b16 v[220:221], v35 offset:43008\n\t" \
  "global_load_dwordx4 v[144:147], v53, s[90:91] offset:0 sc1\n\t" \
  "v_mfma_f32_16x16x32_bf16 v[228:231], v[192:195], v[104:107], v[228:231]\n\t" \
  "ds_read_b64_tr_b16 v[222:223], v35 offset:43520\n\t" \
  "v_mfma_f32_16x16x32_bf16 v[232:235], v[200:203], v[96:99], v[232:235]\n\t" \
  "ds_read_b64_tr_b16 v[192:193], v32 offset:47104\n\t" \
  "s_waitcnt lgkmcnt(6)\n\t" \
  "v_mfma_f32_16x16x32_bf16 v[232:235], v[204:207], v[100:103], v[232:235]\n\t" \
  "ds_read_b64_tr_b16 v[194:195], v32 offset:47616\n\t" \
  "v_mfma_f32_16x16x32_bf16 v[236:239], v[204:207], v[108:111], v[236:239]\n\t" \
  "ds_read_b64_tr_b16 v[196:197], v32 offset:51200\n\t" \
  "global_load_dwordx4 v[148:151], v53, s[90:91] offset:1024 sc1\n\t" \
  "v_mfma_f32_16x16x32_bf16 v[236:239], v[200:203], v[104:107], v[236:239]\n\t" \
  "ds_read_b64_tr_b16 v[198:199], v32 offset:51712\n\t" \
  "v_mfma_f32_16x16x32_bf16 v[240:243], v[208:211], v[96:99], v[240:243]\n\t" \
  "ds_read_b64_tr_b16 v[200:201], v33 offset:47104\n\t" \
  "s_waitcnt lgkmcnt(6)\n\t" \
  "v_mfma_f32_16x16x32_bf16 v[240:243], v[212:215], v[100:103], v[240:243]\n\t" \
  "ds_read_b64_tr_b16 v[202:203], v33 offset:47616\n\t" \
  "v_mfma_f32_16x16x32_bf16 v[244:247], v[212:215], v[108:111], v[244:247]\n\t" \
  "ds_read_b64_tr_b16 v[204:205], v33 offset:51200\n\t" \
  "global_load_dwordx4 v[152:155], v53, s[90:91] offset:2048 sc1\n\t" \
  "v_mfma_f32_16x16x32_bf16 v[244:247], v[208:211], v[104:107], v[244:247]\n\t" \
  "ds_read_b64_tr_b16 v[206:207], v33 offset:51712\n\t" \
  "v_mfma_f32_16x16x32_bf16 v[248:251], v[216:219], v[96:99], v[248:251]\n\t" \
  "ds_read_b64_tr_b16 v[208:209], v34 offset:47104\n\t" \
  "s_waitcnt lgkmcnt(6)\n\t" \
  "v_mfma_f32_16x16x32_bf16 v[248:251], v[220:223], v[100:103], v[248:251]\n\t" \
  "ds_read_b64_tr_b16 v[210:211], v34 offset:47616\n\t" \
  "v_mfma_f32_16x16x32_bf16 v[252:255], v[220:223], v[108:111], v[252:255]\n\t" \
  "ds_read_b64_tr_b16 v[212:213], v34 offset:51200\n\t" \
  "global_load_dwordx4 v[156:159], v53, s[90:91] offset:3072 sc1\n\t" \
  "v_mfma_f32_16x16x32_bf16 v[252:255], v[216:219], v[104:107], v[252:255]\n\t" \
  "ds_read_b64_tr_b16 v[214:215], v34 offset:51712\n\t" \
  "s_waitcnt vmcnt(10)\n\t" \
  "v_mfma_f32_16x16x32_bf16 v[224:227], v[192:195], v[112:115], v[224:227]\n\t" \
  "ds_read_b64_tr_b16 v[216:217], v35 offset:47104\n\t" \
  "s_lshr_b64 s[90:91], %18, 56\n\t" \
  "s_and_b32 s90, s90, 15\n\t" \
  "s_lshl_b32 s90, s90, 15\n\t" \
  "s_waitcnt lgkmcnt(6)\n\t" \
  "v_mfma_f32_16x16x32_bf16 v[224:227], v[196:199], v[116:119], v[224:227]\n\t" \
  "ds_read_b64_tr_b16 v[218:219], v35 offset:47616\n\t" \
  "s_add_u32 s90, s90, s92\n\t" \
  "s_addc_u32 s91, s93, 0\n\t" \
  "s_waitcnt vmcnt(8)\n\t" \
  "v_mfma_f32_16x16x32_bf16 v[228:231], v[196:199], v[124:127], v[228:231]\n\t" \
  "ds_read_b64_tr_b16 v[220:221], v35 offset:51200\n\t" \
  "global_load_dwordx4 v[160:163], v53, s[90:91] offset:0 sc1\n\t" \
  "v_mfma_f32_16x16x32_bf16 v[228:231], v[192:195], v[120:123], v[228:231]\n\t" \
  "ds_read_b64_tr_b16 v[222:223], v35 offset:51712\n\t" \
  "v_mfma_f32_16x16x32_bf16 v[232:235], v[200:203], v[112:115], v[232:235]\n\t" \
  "ds_read_b64_tr_b16 v[192:193], v32 offset:55296\n\t" \
  "s_waitcnt lgkmcnt(6)\n\t" \
  "v_mfma_f32_16x16x32_bf16 v[232:235], v[204:207], v[116:119], v[232:235]\n\t" \
  "ds_read_b64_tr_b16 v[194:195], v32 offset:55808\n\t" \
  "v_mfma_f32_16x16x32_bf16 v[236:239], v[204:207], v[124:127], v[236:239]\n\t" \
  "ds_read_b64_tr_b16 v[196:197], v32 offset:59392\n\t" \
  "global_load_dwordx4 v[164:167], v53, s[90:91] offset:1024 sc1\n\t" \
  "v_mfma_f32_16x16x32_bf16 v[236:239], v[200:203], v[120:123], v[236:239]\n\t" \
  "ds_read_b64_tr_b16 v[198:199], v32 offset:59904\n\t" \
  "v_mfma_f32_16x16x32_bf16 v[240:243], v[208:211], v[112:115], v[240:243]\n\t" \
  "ds_read_b64_tr_b16 v[200:201], v33 offset:55296\n\t" \
  "s_waitcnt lgkmcnt(6)\n\t" \
  "v_mfma_f32_16x16x32_bf16 v[240:243], v[212:215], v[116:119], v[240:243]\n\t" \
  "ds_read_b128 v[40:43], v56 offset:80\n\t" \
  "ds_read_b64_tr_b16 v[202:203], v33 offset:55808\n\t" \
  "v_mfma_f32_16x16x32_bf16 v[244:247], v[212:215], v[124:127], v[244:247]\n\t" \
  "ds_read_b64_tr_b16 v[204:205], v33 offset:59392\n\t" \
  "global_load_dwordx4 v[168:171], v53, s[90:91] offset:2048 sc1\n\t" \
  "v_mfma_f32_16x16x32_bf16 v[244:247], v[208:211], v[120:123], v[244:247]\n\t" \
  "ds_read_b64_tr_b16 v[206:207], v33 offset:59904\n\t" \
  "v_mfma_f32_16x16x32_bf16 v[248:251], v[216:219], v[112:115], v[248:251]\n\t" \
  "ds_read_b64_tr_b16 v[208:209], v34 offset:55296\n\t" \
  "s_waitcnt lgkmcnt(7)\n\t" \
  "v_mfma_f32_16x16x32_bf16 v[248:251], v[220:223], v[116:119], v[248:251]\n\t" \
  "ds_read_b64_tr_b16 v[210:211], v34 offset:55808\n\t" \
  "v_mfma_f32_16x16x32_bf16 v[252:255], v[220:223], v[124:127], v[252:255]\n\t" \
  "ds_read_b64_tr_b16 v[212:213], v34 offset:59392\n\t" \
  "global_load_dwordx4 v[172:175], v53, s[90:91] offset:3072 sc1\n\t" \
  "v_mfma_f32_16x16x32_bf16 v[252:255], v[216:219], v[120:123], v[252:255]\n\t" \
  "ds_read_b64_tr_b16 v[214:215], v34 offset:59904\n\t" \
  "s_waitcnt vmcnt(10)\n\t" \
  "v_mfma_f32_16x16x32_bf16 v[224:227], v[192:195], v[128:131], v[224:227]\n\t" \
  "s_waitcnt lgkmcnt(7)\n\t" \
  "v_min_u32 v40, v40, v41\n\t" \
  "v_min3_u32 v40, v40, v42, v43\n\t" \
  "ds_read_b64_tr_b16 v[216:217], v35 offset:55296\n\t" \
  "s_lshr_b64 s[90:91], %18, 60\n\t" \
  "s_and_b32 s90, s90, 15\n\t" \
  "s_lshl_b32 s90, s90, 15\n\t" \
  "v_mfma_f32_16x16x32_bf16 v[224:227], v[196:199], v[132:135], v[224:227]\n\t" \
  "v_readfirstlane_b32 vcc_lo, v40\n\t" \
  "ds_read_b64_tr_b16 v[218:219], v35 offset:55808\n\t" \
  "s_add_u32 s90, s90, s92\n\t" \
  "s_addc_u32 s91, s93, 0\n\t" \
  "s_waitcnt vmcnt(8)\n\t" \
  "v_mfma_f32_16x16x32_bf16 v[228:231], v[196:199], v[140:143], v[228:231]\n\t" \
  "s_cmp_ge_u32 vcc_lo, 16\n\t" \
  "s_cbranch_scc0 34f\n\t" \
  "35:\n\t" \
  "ds_read_b64_tr_b16 v[220:221], v35 offset:59392\n\t" \
  "global_load_dwordx4 v[176:179], v53, s[90:91] offset:0 sc1\n\t" \
  "v_mfma_f32_16x16x32_bf16 v[228:231], v[192:195], v[136:139], v[228:231]\n\t" \
  "ds_read_b64_tr_b16 v[222:223], v35 offset:59904\n\t" \
  "s_waitcnt lgkmcnt(6)\n\t" \
  "v_mfma_f32_16x16x32_bf16 v[232:235], v[200:203], v[128:131], v[232:235]\n\t" \
  "ds_read_b64_tr_b16 v[192:193], v36 offset:2304\n\t" \
  "v_mfma_f32_16x16x32_bf16 v[232:235], v[204:207], v[132:135], v[232:235]\n\t" \
  "ds_read_b64_tr_b16 v[194:195], v36 offset:2816\n\t" \
  "v_mfma_f32_16x16x32_bf16 v[236:239], v[204:207], v[140:143], v[236:239]\n\t" \
  "ds_read_b64_tr_b16 v[196:197], v36 offset:10752\n\t" \
  "global_load_dwordx4 v[180:183], v53, s[90:91] offset:1024 sc1\n\t" \
  "v_mfma_f32_16x16x32_bf16 v[236:239], v[200:203], v[136:139], v[236:239]\n\t" \
  "ds_read_b64_tr_b16 v[198:199], v36 offset:11264\n\t" \
  "v_mfma_f32_16x16x32_bf16 v[240:243], v[208:211], v[128:131], v[240:243]\n\t" \
  "ds_read_b64_tr_b16 v[200:201], v37 offset:2304\n\t" \
  "s_waitcnt lgkmcnt(6)\n\t" \
  "v_mfma_f32_16x16x32_bf16 v[240:243], v[212:215], v[132:135], v[240:243]\n\t" \
  "ds_read_b64_tr_b16 v[202:203], v37 offset:2816\n\t" \
  "v_mfma_f32_16x16x32_bf16 v[244:247], v[212:215], v[140:143], v[244:247]\n\t" \
  "ds_read_b64_tr_b16 v[204:205], v37 offset:10752\n\t" \
  "global_load_dwordx4 v[184:187], v53, s[90:91] offset:2048 sc1\n\t" \
  "v_mfma_f32_16x16x32_bf16 v[244:247], v[208:211], v[136:139], v[244:247]\n\t" \
  "ds_read_b64_tr_b16 v[206:207], v37 offset:11264\n\t" \
  "v_mfma_f32_16x16x32_bf16 v[248:251], v[216:219], v[128:131], v[248:251]\n\t" \
  "ds_read_b64_tr_b16 v[208:209], v38 offset:2304\n\t" \
  "s_waitcnt lgkmcnt(6)\n\t" \
  "v_mfma_f32_16x16x32_bf16 v[248:251], v[220:223], v[132:135], v[248:251]\n\t" \
  "ds_read_b64_tr_b16 v[210:211], v38 offset:2816\n\t" \
  "v_mfma_f32_16x16x32_bf16 v[252:255], v[220:223], v[140:143], v[252:255]\n\t" \
  "ds_read_b64_tr_b16 v[212:213], v38 offset:10752\n\t" \
  "global_load_dwordx4 v[188:191], v53, s[90:91] offset:3072 sc1\n\t" \
  "v_mfma_f32_16x16x32_bf16 v[252:255], v[216:219], v[136:139], v[252:255]\n\t" \
  "ds_read_b64_tr_b16 v[214:215], v38 offset:11264\n\t" \
  "s_waitcnt vmcnt(10)\n\t" \
  "v_mfma_f32_16x16x32_bf16 v[224:227], v[192:195], v[144:147], v[224:227]\n\t" \
  "ds_read_b64_tr_b16 v[216:217], v39 offset:2304\n\t" \
  "s_waitcnt lgkmcnt(6)\n\t" \
  "v_mfma_f32_16x16x32_bf16 v[224:227], v[196:199], v[148:151], v[224:227]\n\t" \
  "ds_read_b64_tr_b16 v[218:219], v39 offset:2816\n\t" \
  "s_waitcnt vmcnt(8)\n\t" \
  "v_mfma_f32_16x16x32_bf16 v[228:231], v[196:199], v[156:159], v[228:231]\n\t" \
  "ds_read_b64_tr_b16 v[220:221], v39 offset:10752\n\t" \
  "v_mfma_f32_16x16x32_bf16 v[228:231], v[192:195], v[152:155], v[228:231]\n\t" \
  "ds_read_b64_tr_b16 v[222:223], v39 offset:11264\n\t" \
  "v_mfma_f32_16x16x32_bf16 v[232:235], v[200:203], v[144:147], v[232:235]\n\t" \
  "ds_read_b64_tr_b16 v[192:193], v36 offset:19200\n\t" \
  "s_waitcnt lgkmcnt(6)\n\t" \
  "v_mfma_f32_16x16x32_bf16 v[232:235], v[204:207], v[148:151], v[232:235]\n\t" \
  "ds_read_b64_tr_b16 v[194:195], v36 offset:19712\n\t" \
  "v_mfma_f32_16x16x32_bf16 v[236:239], v[204:207], v[156:159], v[236:239]\n\t" \
  "ds_read_b64_tr_b16 v[196:197], v36 offset:27648\n\t" \
  "v_mfma_f32_16x16x32_bf16 v[236:239], v[200:203], v[152:155], v[236:239]\n\t" \
  "ds_read_b64_tr_b16 v[198:199], v36 offset:28160\n\t" \
  "v_mfma_f32_16x16x32_bf16 v[240:243], v[208:211], v[144:147], v[240:243]\n\t" \
  "ds_read_b64_tr_b16 v[200:201], v37 offset:19200\n\t" \
  "s_waitcnt lgkmcnt(6)\n\t" \
  "v_mfma_f32_16x16x32_bf16 v[240:243], v[212:215], v[148:151], v[240:243]\n\t" \
  "ds_read_b128 v[40:43], v56 offset:160\n\t" \
  "ds_read_b64_tr_b16 v[202:203], v37 offset:19712\n\t" \
  "v_mfma_f32_16x16x32_bf16 v[244:247], v[212:215], v[156:159], v[244:247]\n\t" \
  "ds_read_b64_tr_b16 v[204:205], v37 offset:27648\n\t" \
  "v_mfma_f32_16x16x32_bf16 v[244:247], v[208:211], v[152:155], v[244:247]\n\t" \
  "ds_read_b64_tr_b16 v[206:207], v37 offset:28160\n\t" \
  "v_mfma_f32_16x16x32_bf16 v[248:251], v[216:219], v[144:147], v[248:251]\n\t" \
  "ds_read_b64_tr_b16 v[208:209], v38 offset:19200\n\t" \
  "s_waitcnt lgkmcnt(7)\n\t" \
  "v_mfma_f32_16x16x32_bf16 v[248:251], v[220:223], v[148:151], v[248:251]\n\t" \
  "ds_read_b64_tr_b16 v[210:211], v38 offset:19712\n\t" \
  "v_mfma_f32_16x16x32_bf16 v[252:255], v[220:223], v[156:159], v[252:255]\n\t" \
  "ds_read_b64_tr_b16 v[212:213], v38 offset:27648\n\t" \
  "v_mfma_f32_16x16x32_bf16 v[252:255], v[216:219], v[152:155], v[252:255]\n\t" \
  "ds_read_b64_tr_b16 v[214:215], v38 offset:28160\n\t" \
  "s_waitcnt vmcnt(6)\n\t" \
  "v_mfma_f32_16x16x32_bf16 v[224:227], v[192:195], v[160:163], v[224:227]\n\t" \
  "s_waitcnt lgkmcnt(7)\n\t" \
  "v_min_u32 v40, v40, v41\n\t" \
  "v_min3_u32 v40, v40, v42, v43\n\t" \
  "ds_read_b64_tr_b16 v[216:217], v39 offset:19200\n\t" \
  "v_mfma_f32_16x16x32_bf16 v[224:227], v[196:199], v[164:167], v[224:227]\n\t" \
  "v_readfirstlane_b32 vcc_lo, v40\n\t" \
  "ds_read_b64_tr_b16 v[218:219], v39 offset:19712\n\t" \
  "s_waitcnt vmcnt(4)\n\t" \
  "v_mfma_f32_16x16x32_bf16 v[228:231], v[196:199], v[172:175], v[228:231]\n\t" \
  "s_cmp_ge_u32 vcc_lo, 16\n\t" \
  "s_cbranch_scc0 36f\n\t" \
  "37:\n\t" \
  "ds_read_b64_tr_b16 v[220:221], v39 offset:27648\n\t" \
  "v_mfma_f32_16x16x32_bf16 v[228:231], v[192:195], v[168:171], v[228:231]\n\t" \
  "ds_read_b64_tr_b16 v[222:223], v39 offset:28160\n\t" \
  "s_waitcnt lgkmcnt(6)\n\t" \
  "v_mfma_f32_16x16x32_bf16 v[232:235], v[200:203], v[160:163], v[232:235]\n\t" \
  "ds_read_b64_tr_b16 v[192:193], v28 offset:0\n\t" \
  "v_mfma_f32_16x16x32_bf16 v[232:235], v[204:207], v[164:167], v[232:235]\n\t" \
  "ds_read_b64_tr_b16 v[194:195], v28 offset:512\n\t" \
  "v_mfma_f32_16x16x32_bf16 v[236:239], v[204:207], v[172:175], v[236:239]\n\t" \
  "ds_read_b64_tr_b16 v[196:197], v28 offset:4096\n\t" \
  "v_mfma_f32_16x16x32_bf16 v[236:239], v[200:203], v[168:171], v[236:239]\n\t" \
  "ds_read_b64_tr_b16 v[198:199], v28 offset:4608\n\t" \
  "v_mfma_f32_16x16x32_bf16 v[240:243], v[208:211], v[160:163], v[240:243]\n\t" \
  "ds_read_b64_tr_b16 v[200:201], v29 offset:0\n\t" \
  "s_waitcnt lgkmcnt(6)\n\t" \
  "v_mfma_f32_16x16x32_bf16 v[240:243], v[212:215], v[164:167], v[240:243]\n\t" \
  "ds_read_b64_tr_b16 v[202:203], v29 offset:512\n\t" \
  "v_mfma_f32_16x16x32_bf16 v[244:247], v[212:215], v[172:175], v[244:247]\n\t" \
  "ds_read_b64_tr_b16 v[204:205], v29 offset:4096\n\t" \
  "v_mfma_f32_16x16x32_bf16 v[244:247], v[208:211], v[168:171], v[244:247]\n\t" \
  "ds_read_b64_tr_b16 v[206:207], v29 offset:4608\n\t" \
  "v_mfma_f32_16x16x32_bf16 v[248:251], v[216:219], v[160:163], v[248:251]\n\t" \
  "ds_read_b64_tr_b16 v[208:209], v30 offset:0\n\t" \
  "s_waitcnt lgkmcnt(6)\n\t" \
  "v_mfma_f32_16x16x32_bf16 v[248:251], v[220:223], v[164:167], v[248:251]\n\t" \
  "ds_read_b64_tr_b16 v[210:211], v30 offset:512\n\t" \
  "v_mfma_f32_16x16x32_bf16 v[252:255], v[220:223], v[172:175], v[252:255]\n\t" \
  "ds_read_b64_tr_b16 v[212:213], v30 offset:4096\n\t" \
  "v_mfma_f32_16x16x32_bf16 v[252:255], v[216:219], v[168:171], v[252:255]\n\t" \
  "ds_read_b64_tr_b16 v[214:215], v30 offset:4608\n\t" \
  "s_waitcnt vmcnt(2)\n\t" \
  "v_mfma_f32_16x16x32_bf16 v[224:227], v[192:195], v[176:179], v[224:227]\n\t" \
  "ds_read_b64_tr_b16 v[216:217], v31 offset:0\n\t" \
  "s_waitcnt lgkmcnt(6)\n\t" \
  "v_mfma_f32_16x16x32_bf16 v[224:227], v[196:199], v[180:183], v[224:227]\n\t" \
  "ds_read_b64_tr_b16 v[218:219], v31 offset:512\n\t" \
  "s_waitcnt vmcnt(0)\n\t" \
  "v_mfma_f32_16x16x32_bf16 v[228:231], v[196:199], v[188:191], v[228:231]\n\t" \
  "ds_read_b64_tr_b16 v[220:221], v31 offset:4096\n\t" \
  "v_mfma_f32_16x16x32_bf16 v[228:231], v[192:195], v[184:187], v[228:231]\n\t" \
  "ds_read_b64_tr_b16 v[222:223], v31 offset:4608\n\t" \
  "v_mfma_f32_16x16x32_bf16 v[232:235], v[200:203], v[176:179], v[232:235]\n\t" \
  "s_waitcnt lgkmcnt(5)\n\t" \
  "v_mfma_f32_16x16x32_bf16 v[232:235], v[204:207], v[180:183], v[232:235]\n\t" \
  "v_mfma_f32_16x16x32_bf16 v[236:239], v[204:207], v[188:191], v[236:239]\n\t" \
  "v_cvt_pk_bf16_f32 v224, v224, v225\n\t" \
  "v_cvt_pk_bf16_f32 v225, v226, v227\n\t" \
  "v_mfma_f32_16x16x32_bf16 v[236:239], v[200:203], v[184:187], v[236:239]\n\t" \
  "ds_write_b64 v57, v[224:225] offset:0\n\t" \
  "v_mfma_f32_16x16x32_bf16 v[240:243], v[208:211], v[176:179], v[240:243]\n\t" \
  "v_cvt_pk_bf16_f32 v228, v228, v229\n\t" \
  "v_cvt_pk_bf16_f32 v229, v230, v231\n\t" \
  "s_waitcnt lgkmcnt(2)\n\t" \
  "v_mfma_f32_16x16x32_bf16 v[240:243], v[212:215], v[180:183], v[240:243]\n\t" \
  "ds_write_b64 v57, v[228:229] offset:2048\n\t" \
  "v_mfma_f32_16x16x32_bf16 v[244:247], v[212:215], v[188:191], v[244:247]\n\t" \
  "v_cvt_pk_bf16_f32 v232, v232, v233\n\t" \
  "v_cvt_pk_bf16_f32 v233, v234, v235\n\t" \
  "v_mfma_f32_16x16x32_bf16 v[244:247], v[208:211], v[184:187], v[244:247]\n\t" \
  "ds_write_b64 v58, v[232:233] offset:0\n\t" \
  "v_mfma_f32_16x16x32_bf16 v[248:251], v[216:219], v[176:179], v[248:251]\n\t" \
  "v_cvt_pk_bf16_f32 v236, v236, v237\n\t" \
  "v_cvt_pk_bf16_f32 v237, v238, v239\n\t" \
  "s_waitcnt lgkmcnt(3)\n\t" \
  "v_mfma_f32_16x16x32_bf16 v[248:251], v[220:223], v[180:183], v[248:251]\n\t" \
  "ds_write_b64 v58, v[236:237] offset:2048\n\t" \
  "v_mfma_f32_16x16x32_bf16 v[252:255], v[220:223], v[188:191], v[252:255]\n\t" \
  "v_cvt_pk_bf16_f32 v240, v240, v241\n\t" \
  "v_cvt_pk_bf16_f32 v241, v242, v243\n\t" \
  "v_mfma_f32_16x16x32_bf16 v[252:255], v[216:219], v[184:187], v[252:255]\n\t" \
  "ds_write_b64 v59, v[240:241] offset:0\n\t" \
  "v_cvt_pk_bf16_f32 v244, v244, v245\n\t" \
  "v_cvt_pk_bf16_f32 v245, v246, v247\n\t" \
  "ds_write_b64 v59, v[244:245] offset:2048\n\t" \
  "v_cvt_pk_bf16_f32 v248, v248, v249\n\t" \
  "v_cvt_pk_bf16_f32 v249, v250, v251\n\t" \
  "ds_write_b64 v60, v[248:249] offset:0\n\t" \
  "s_nop 0\n\t" \
  "v_cvt_pk_bf16_f32 v252, v252, v253\n\t" \
  "v_cvt_pk_bf16_f32 v253, v254, v255\n\t" \
  "ds_write_b64 v60, v[252:253] offset:2048\n\t" \
  "ds_read_b128 v[64:67], v61 offset:0\n\t" \
  "ds_read_b128 v[68:71], v61 offset:1024\n\t" \
  "ds_read_b128 v[72:75], v61 offset:2048\n\t" \
  "ds_read_b128 v[76:79], v61 offset:3072\n\t" \
  "s_waitcnt lgkmcnt(3)\n\t" \
  "global_store_dwordx4 v62, v[64:67], %22 offset:-4096" OUTPOL "\n\t" \
  "s_waitcnt lgkmcnt(2)\n\t" \
  "global_store_dwordx4 v63, v[68:71], %22 offset:-4096" OUTPOL "\n\t" \
  "s_waitcnt lgkmcnt(1)\n\t" \
  "global_store_dwordx4 v62, v[72:75], %22 offset:0" OUTPOL "\n\t" \
  "s_waitcnt lgkmcnt(0)\n\t" \
  "global_store_dwordx4 v63, v[76:79], %22 offset:0" OUTPOL "\n\t" \
  "s_branch 19f\n\t" \
  "23:\n\t" \
  "510:\n\t" \
  "s_cbranch_execz 26b\n\t" \
  "s_mov_b64 exec, 0xffff\n\t" \
  "global_store_dword v11, v55, %19 sc1\n\t" \
  "s_mov_b64 exec, -1\n\t" \
  "s_branch 22b\n\t" \
  "7:\n\t" \
  "s_sleep 1\n\t" \
  "ds_read_b128 v[60:63], v56 offset:64\n\t" \
  "ds_read_b64_tr_b16 v[108:109], v5 offset:0\n\t" \
  "ds_read_b64_tr_b16 v[110:111], v5 offset:16\n\t" \
  "ds_read_b128 v[112:115], v6 offset:16384\n\t" \
  "ds_read_b128 v[116:119], v6 offset:15360\n\t" \
  "ds_read_b64_tr_b16 v[120:121], v5 offset:512\n\t" \
  "ds_read_b64_tr_b16 v[122:123], v5 offset:528\n\t" \
  "ds_read_b128 v[124:127], v6 offset:14336\n\t" \
  "ds_read_b64_tr_b16 v[128:129], v5 offset:1024\n\t" \
  "ds_read_b64_tr_b16 v[130:131], v5 offset:1040\n\t" \
  "ds_read_b128 v[132:135], v6 offset:13312\n\t" \
  "ds_read_b64_tr_b16 v[136:137], v5 offset:1536\n\t" \
  "ds_read_b64_tr_b16 v[138:139], v5 offset:1552\n\t" \
  "ds_read_b128 v[140:143], v6 offset:12288\n\t" \
  "ds_read_b64_tr_b16 v[144:145], v5 offset:2048\n\t" \
  "s_waitcnt lgkmcnt(0)\n\t" \
  "v_and_b32 v60, v60, v61\n\t" \
  "v_and_b32 v62, v62, v63\n\t" \
  "v_mov_b32 v12, %13\n\t" \
  "v_and_b32 v60, v60, v62\n\t" \
  "s_nop 0\n\t" \
  "v_readfirstlane_b32 vcc_lo, v60\n\t" \
  "s_nop 1\n\t" \
  "s_cmp_eq_u32 vcc_lo, 0\n\t" \
  "s_cbranch_scc1 7b\n\t" \
  "s_branch 6b\n\t" \
  "30:\n\t" \
  "ds_read_b128 v[40:43], v56 offset:80\n\t" \
  "s_waitcnt lgkmcnt(0)\n\t" \
  "v_min_u32 v40, v40, v41\n\t" \
  "v_min3_u32 v40, v40, v42, v43\n\t" \
  "s_nop 1\n\t" \
  "v_readfirstlane_b32 vcc_lo, v40\n\t" \
  "s_nop 1\n\t" \
  "s_cmp_ge_u32 vcc_lo, 7\n\t" \
  "s_cbranch_scc0 30b\n\t" \
  "s_branch 31b\n\t" \
  "32:\n\t" \
  "ds_read_b128 v[40:43], v56 offset:80\n\t" \
  "s_waitcnt lgkmcnt(0)\n\t" \
  "v_min_u32 v40, v40, v41\n\t" \
  "v_min3_u32 v40, v40, v42, v43\n\t" \
  "s_nop 1\n\t" \
  "v_readfirstlane_b32 vcc_lo, v40\n\t" \
  "s_nop 1\n\t" \
  "s_cmp_ge_u32 vcc_lo, 13\n\t" \
  "s_cbranch_scc0 32b\n\t" \
  "s_branch 33b\n\t" \
  "34:\n\t" \
  "ds_read_b128 v[40:43], v56 offset:80\n\t" \
  "s_waitcnt lgkmcnt(0)\n\t" \
  "v_min_u32 v40, v40, v41\n\t" \
  "v_min3_u32 v40, v40, v42, v43\n\t" \
  "s_nop 1\n\t" \
  "v_readfirstlane_b32 vcc_lo, v40\n\t" \
  "s_nop 1\n\t" \
  "s_cmp_ge_u32 vcc_lo, 16\n\t" \
  "s_cbranch_scc0 34b\n\t" \
  "s_branch 35b\n\t" \
  "36:\n\t" \
  "ds_read_b128 v[40:43], v56 offset:160\n\t" \
  "s_waitcnt lgkmcnt(0)\n\t" \
  "v_min_u32 v40, v40, v41\n\t" \
  "v_min3_u32 v40, v40, v42, v43\n\t" \
  "s_nop 1\n\t" \
  "v_readfirstlane_b32 vcc_lo, v40\n\t" \
  "s_nop 1\n\t" \
  "s_cmp_ge_u32 vcc_lo, 16\n\t" \
  "s_cbranch_scc0 36b\n\t" \
  "s_branch 37b\n\t" \
  "38:\n\t" \
  "s_waitcnt vmcnt(0) lgkmcnt(0)\n\t" \
  "v_readfirstlane_b32 vcc_lo, v16\n\t" \
  "s_nop 1\n\t" \
  "s_cmp_ge_u32 vcc_lo, 1\n\t" \
  "s_cbranch_scc0 40f\n\t" \
  "41:\n\t" \
  "s_lshr_b64 s[90:91], %18, 0\n\t" \
  "s_and_b32 s90, s90, 15\n\t" \
  "s_lshl_b32 s90, s90, 15\n\t" \
  "s_add_u32 s90, s90, s92\n\t" \
  "s_addc_u32 s91, s93, 0\n\t" \
  "global_load_dwordx4 v[64:67], v53, s[90:91] offset:0 sc1\n\t" \
  "global_load_dwordx4 v[68:71], v53, s[90:91] offset:1024 sc1\n\t" \
  "global_load_dwordx4 v[72:75], v53, s[90:91] offset:2048 sc1\n\t" \
  "global_load_dwordx4 v[76:79], v53, s[90:91] offset:3072 sc1\n\t" \
  "s_lshr_b64 s[90:91], %18, 4\n\t" \
  "s_and_b32 s90, s90, 15\n\t" \
  "s_lshl_b32 s90, s90, 15\n\t" \
  "s_add_u32 s90, s90, s92\n\t" \
  "s_addc_u32 s91, s93, 0\n\t" \
  "global_load_dwordx4 v[80:83], v53, s[90:91] offset:0 sc1\n\t" \
  "global_load_dwordx4 v[84:87], v53, s[90:91] offset:1024 sc1\n\t" \
  "global_load_dwordx4 v[88:91], v53, s[90:91] offset:2048 sc1\n\t" \
  "global_load_dwordx4 v[92:95], v53, s[90:91] offset:3072 sc1\n\t" \
  "s_branch 39b\n\t" \
  "40:\n\t" \
  "s_cmp_eq_u32 %13, 0\n\t" \
  "s_cbranch_scc1 42f\n\t" \
  "s_sleep 1\n\t" \
  "ds_read_b32 v16, v56 offset:96\n\t" \
  "s_waitcnt lgkmcnt(0)\n\t" \
  "v_readfirstlane_b32 vcc_lo, v16\n\t" \
  "s_nop 1\n\t" \
  "s_cmp_ge_u32 vcc_lo, 1\n\t" \
  "s_cbranch_scc0 40b\n\t" \
  "s_branch 41b\n\t" \
  "42:\n\t" \
  "v_bfe_u32 v19, v12, 4, 4\n\t" \
  "v_lshlrev_b32 v19, 2, v19\n\t" \
  "s_mov_b32 s90, 0\n\t" \
  "142:\n\t" \
  "global_load_dword v16, v19, %21 sc1\n\t" \
  "s_add_u32 s90, s90, 1\n\t" \
  "s_waitcnt vmcnt(0)\n\t" \
  "v_cmp_ne_u32 vcc, v55, v16\n\t" \
  "s_mov_b32 s88, %26\n\t" \
  "s_mov_b32 s89, s88\n\t" \
  "s_and_b64 vcc, vcc, s[88:89]\n\t" \
  "s_cbranch_scc0 242f\n\t" \
  "s_sleep 2\n\t" \
  "s_cmp_lt_u32 s90, 0x400000\n\t" \
  "s_cbranch_scc1 142b\n\t" \
  "242:\n\t" \
  "v_mov_b32 v16, 1\n\t" \
  "ds_max_u32 v56, v16 offset:96\n\t" \
  "s_branch 41b\n\t" \
  "43:\n\t" \
  "s_cmp_eq_u32 %13, 0\n\t" \
  "s_cbranch_scc1 45f\n\t" \
  "s_sleep 1\n\t" \
  "ds_read_b32 v16, v56 offset:96\n\t" \
  "s_waitcnt lgkmcnt(0)\n\t" \
  "v_readfirstlane_b32 vcc_lo, v16\n\t" \
  "s_nop 1\n\t" \
  "s_cmp_ge_u32 vcc_lo, 2\n\t" \
  "s_cbranch_scc0 43b\n\t" \
  "s_branch 44b\n\t" \
  "45:\n\t" \
  "v_bfe_u32 v19, v12, 4, 4\n\t" \
  "v_lshlrev_b32 v19, 2, v19\n\t" \
  "s_mov_b32 s90, 0\n\t" \
  "145:\n\t" \
  "global_load_dword v16, v19, %21 sc1\n\t" \
  "s_add_u32 s90, s90, 1\n\t" \
  "s_waitcnt vmcnt(0)\n\t" \
  "v_cmp_ne_u32 vcc, v55, v16\n\t" \
  "s_mov_b32 s88, %26\n\t" \
  "s_mov_b32 s89, s88\n\t" \
  "s_and_b64 vcc, vcc, -1\n\t" \
  "s_cbranch_scc0 245f\n\t" \
  "s_sleep 2\n\t" \
  "s_cmp_lt_u32 s90, 0x400000\n\t" \
  "s_cbranch_scc1 145b\n\t" \
  "245:\n\t" \
  "v_mov_b32 v16, 2\n\t" \
  "ds_max_u32 v56, v16 offset:96\n\t" \
  "s_branch 44b\n\t" \
  "19:\n\t"
#define C_CLOB "v10","v11","v12","v13","v18","v19","v20","v21","v22","v23","v24","v25","v26","v27","v28","v29","v30","v31","v32","v33","v34","v35","v36","v37","v38","v39","v40","v41","v42","v43","v44","v45","v46","v47","v48","v49","v50","v53","v55","v56","v57","v58","v59","v60","v61","v62","v63","v64","v65","v66","v67","v68","v69","v70","v71","v72","v73","v74","v75","v76","v77","v78","v79","v80","v81","v82","v83","v84","v85","v86","v87","v88","v89","v90","v91","v92","v93","v94","v95","v96","v97","v98","v99","v100","v101","v102","v103","v104","v105","v106","v107","v108","v109","v110","v111","v112","v113","v114","v115","v116","v117","v118","v119","v120","v121","v122","v123","v124","v125","v126","v127","v128","v129","v130","v131","v132","v133","v134","v135","v136","v137","v138","v139","v140","v141","v142","v143","v144","v145","v146","v147","v148","v149","v150","v151","v152","v153","v154","v155","v156","v157","v158","v159","v160","v161","v162","v163","v164","v165","v166","v167","v168","v169","v170","v171","v172","v173","v174","v175","v176","v177","v178","v179","v180","v181","v182","v183","v184","v185","v186","v187","v188","v189","v190","v191","v192","v193","v194","v195","v196","v197","v198","v199","v200","v201","v202","v203","v204","v205","v206","v207","v208","v209","v210","v211","v212","v213","v214","v215","v216","v217","v218","v219","v220","v221","v222","v223","v224","v225","v226","v227","v228","v229","v230","v231","v232","v233","v234","v235","v236","v237","v238","v239","v240","v241","v242","v243","v244","v245","v246","v247","v248","v249","v250","v251","v252","v253","v254","v255"

static_assert(KVP<13 && KVP>TGS+1, "");

#define TS(i)
#define TSDUMP()
#define TSB(i)
#define TSR(i)
#define TSH(i)
#define TSDECL()

#define TSF(i)
typedef unsigned long long u64;
typedef u64 u64x8 __attribute__((ext_vector_type(8)));
typedef u64 u64x4 __attribute__((ext_vector_type(4)));
typedef unsigned u32x8 __attribute__((ext_vector_type(8)));
typedef unsigned u32x16 __attribute__((ext_vector_type(16)));
template<class T> __device__ __forceinline__ T sg(T v){
  if constexpr(sizeof(T)==4) return (T)__builtin_amdgcn_readfirstlane((unsigned)v); else return v; }
#define GP(T,v) ((T*)(__attribute__((address_space(1))) T*)(v))
__global__ __launch_bounds__(512,2) __attribute__((aligned(4096))) void fused_kernel(const u16* x_, const u16* wqkv_, const u16* kin_, const u16* vin_, const u16* shk_, const u16* hk_,
    const u16* wo_, const u16* qin_, const u16* sD_, u16* out_, u16* ws_, u16* unused_)
{
#define ST0()
#define ST1()
#define ARGS0() u64x8 ka; u64x4 kb; asm volatile("s_load_dwordx16 %0, %2, 0\n\ts_load_dwordx8 %1, %2, 64" : "=&s"(ka), "=&s"(kb) : "s"(__builtin_amdgcn_kernarg_segment_ptr()) : "memory")
#define ARGS() asm volatile("s_waitcnt lgkmcnt(0)" : "+s"(ka), "+s"(kb) :: "memory"); ST1() \
  const u16 *x=GP(const u16,ka[0]), *wqkv=GP(const u16,ka[1]), *kin=GP(const u16,ka[2]), *vin=GP(const u16,ka[3]), *shk=GP(const u16,ka[4]), *hk=GP(const u16,ka[5]), *wo=GP(const u16,ka[6]), *qin=GP(const u16,ka[7]), *sD=GP(const u16,kb[0]); \
  u16 *out=GP(u16,kb[1]), *o=GP(u16,kb[2]); char* ws=(char*)o; unsigned cpar=xcd>>2; \
  auto loc=[&](unsigned off)->unsigned{ return off|(((cpar^(off>>21))&1)<<13); }; \
  (void)x; (void)wqkv; (void)kin; (void)vin; (void)shk; (void)hk; (void)wo; (void)qin; (void)sD; (void)out; (void)loc
  ST0()
  __shared__ __attribute__((aligned(1024))) char smem[ALL_LDS];
  const int tid=threadIdx.x;
  const int w8=__builtin_amdgcn_readfirstlane(tid>>6);
  asm volatile("s_sub_u32 vcc_lo, %0, 4\n\ts_cbranch_scc1 1f\n\ts_cmp_lg_u32 %0, 7\n\ts_cbranch_scc1 2f\n\t"
      "s_lshr_b32 vcc_lo, %2, 3\n\ts_mul_i32 vcc_lo, vcc_lo, %5\n\tv_and_b32 v254, 15, %1\n\tv_lshl_add_u32 v254, v254, 7, vcc_lo\n\ts_getpc_b64 vcc\n\tglobal_load_dword v255, v254, vcc\n\ts_branch 1f\n"
      "2:\n\ts_and_b32 vcc_hi, %2, 7\n\ts_cbranch_scc1 1f\n\ts_cmp_gt_u32 %2, 32\n\ts_cbranch_scc1 1f\n\ts_mul_i32 vcc_hi, %2, 3\n\ts_lshr_b32 vcc_hi, vcc_hi, 3\n\ts_add_u32 vcc_lo, vcc_lo, vcc_hi\n\t"
      "v_and_b32 v253, 3, %1\n\tv_and_b32 v254, 4, %1\n\tv_lshlrev_b32 v253, 7, v253\n\tv_lshl_add_u32 v253, v254, 11, v253\n\tv_lshl_add_u32 v253, vcc_lo, 9, v253\n\tv_add_u32 v253, %4, v253\n\t"
      "s_load_dwordx2 vcc, %3, 80\n\ts_mov_b64 exec, 255\n\ts_waitcnt lgkmcnt(0)\n\tglobal_store_dword v253, v253, vcc sc1\n\ts_mov_b64 exec, -1\n1:"
      :: "s"(w8), "v"(tid), "s"(blockIdx.x), "s"(__builtin_amdgcn_kernarg_segment_ptr()), "n"(WS_GEN+16384), "n"(CODE_ST) : "memory", "scc", "vcc", "v253", "v254", "v255");
  LDS_AS unsigned* const ct=(LDS_AS unsigned*)(LDS_AS char*)(smem+CT_OFF);
  volatile LDS_AS unsigned* const vct=ct;
  auto all4=[&](int i)->unsigned{ u32x4 v=*(volatile LDS_AS u32x4*)(ct+i); return __builtin_amdgcn_readfirstlane(v[0]&v[1]&v[2]&v[3]); };
#define ROTF(b) ((4+4*xr+4*xc+8*(b))&15)
#define BLOCK() int bid=blockIdx.x, w=w8&3; asm volatile("" : "+s"(bid), "+s"(w)); __builtin_assume(bid>=0 && bid<256 && w>=0 && w<4); DERIVE()
#define DERIVE() const int xcd=bid&7, idx=bid>>3, ra=idx&3, rbb=idx>>2, xr=xcd>>1, xc=xcd&1, rb=xr*4+ra, hp=xc*8+rbb, s0=rb*128, tm=rb, tn=hp^TNX, cb=(xcd>>2)*128+idx*4+(xcd&3), gi=cb^16; (void)s0; (void)gi; \
  auto korder=[&]()->unsigned long long{ const unsigned N=0x7654210u, sh=4*(KR), q=(((N>>sh)|(N<<(28-sh)))&0xfffffffu)|0x30000000u; \
    return ((unsigned long long)q<<32|q)^(xc? 0x88888888ull : 0x8888888800000000ull); }; \
  const unsigned l1m=0xf7u<<(8*xc); (void)l1m; (void)korder

  if(__builtin_expect(w8>=4,0)){
  BLOCK(); constexpr int WB=4; (void)WB;
  const int hd=w>>1, par=w&1;
  __builtin_amdgcn_s_setprio(CPRIO);
  {
    ARGS0(); LANES();
    if(w==0){ if(l<52) ct[l]=0u; if(idx==0) *(u32x4*)(smem+AUX+l*16)=u32x4{0,0,0,0}; }
    unsigned a0b=fr*128+((g^(fr&7))*16);
    const int r0=8*g+tq;
    unsigned bbb=r0*128+(((2*hd)^wswz(r0))*32)+8*tp;
    unsigned kA=AUX+(fr^3)*KVP_+w*64+(g&1)*16-(g>>1)*256, kB=AUX+(20+4*g-tq)*KVP_+w*64+8*tp;
    const int cch=127-4*idx-g;
    unsigned kvo=(8*w+tq)*32768+(cch>>6)*16384+((cch>>4)&3)*2048+(cch&15)*64+((tp^((cch>>2)&3))*16);
    unsigned sho=(xcd*128+32*w+fr)*8; const unsigned ctl=CT_OFF+4*w;
    u32x4 sel;
    { const unsigned b2=(g>>1)? 0x0302u : 0x0100u, sv=(fr&1)? (b2<<16|0x0c0cu) : (0x0c0c0000u|b2);
      #pragma unroll
      for(int i=0;i<4;i++) sel[i]= (fr>>1)==(g&1)*4+i ? sv : 0x0c0c0c0cu; }
    unsigned msk=0, lx=w*1024+l*16;
    #pragma unroll
    for(int e=0;e<4;e++){ const int d=4*g+e-fr; const unsigned pv=d<9, hv=par? pv : (unsigned)(d<1 || 4*g+e>7);
      msk|=((unsigned)(d+7)>7u?1u:0u)<<e | pv<<(4+e) | hv<<(8+e) | (d<=0?1u:0u)<<(12+e); }
    msk|=(unsigned)(g? l-16 : 48+((l-1)&15))<<26;
    const unsigned hb=__builtin_amdgcn_readfirstlane(rb==0 ? 0xcfa8965fu : 0u);
    unsigned ap=AUX+w*CV_W+(tq&1)*(CV_CP-4)+8+32*g+4*tq+8*tp;
    u32x4 bp;
    #pragma unroll
    for(int b=0;b<4;b++){ const int c=15+4*b-fr+g; bp[b]=c*64+((w^((c>>2)&3))*16); }
    asm volatile("" : "+v"(a0b), "+v"(bbb), "+v"(kA), "+v"(kB), "+v"(kvo), "+v"(sho), "+v"(msk), "+v"(lx), "+v"(ap), "+v"(bp), "+v"(sel));
    ARGS();
    const char* ow=ws+loc((rb*16+hp)*32768)+(par*2+hd)*1024+4096;
    asm volatile("" : "+s"(shk));
    asm volatile(C_ASM : "+{v52}"(a0b), "+{v54}"(bbb), "+{v0}"(kA), "+{v1}"(kB), "+{v2}"(kvo), "+{v51}"(sho), "+{v3}"(msk), "+{v4}"(lx), "+{v5}"(ap), "+{v[6:9]}"(bp), "+{v[14:17]}"(sel)
       : "s"(shk), "s"(ctl), "s"(w), "s"(ow), "s"(hb), "s"(0.17677669529663687f*1.44269504088896341f), "{s[92:93]}"(ws+loc(tm*16*32768)), "s"(korder()),
         "s"(ws+loc(flo(1,rb*16))+hp*4), "s"(AUX+w*(CV_W+64)), "s"(ws+loc(flo(1,tm*16+tn))),
         "s"(out+(size_t)(tm*128+w*32+1)*2048+1024+tn*64), "s"(8192+w*4096), "s"(ws+loc(WS_KV+xcd*32*32768)), "s"(ws+loc(flo(0,xcd*32))+idx*4), "s"(l1m), "s"(xcd==0 && idx<5 && w<3? (u64)(ws+WS_GEN+idx*3+w) : 0ull) : "memory", "scc", "vcc", "s68", "s69", "s70", "s71", "s74", "s75", "s76", "s77", "s88", "s89", "s90", "s91",
       C_CLOB);
    TSDECL();
    TS(20); TSDUMP();
  }
  return;
  }
  {
    ARGS0(); BLOCK(); constexpr int WB=0; (void)WB; LANES(); TSDECL();
    const unsigned vx=(l>>3)*4096+(((l&7)^(l>>3))*16), vxs=vx+(s0+32*w)*2048, vw=(l>>3)*6144+((((l&7)>>1)^((l>>4)&1))*32)+(l&1)*16+hp*128+w*98304, vw1=(vw^64)+49152-1024;
    u32x8 va; u32x2 vb; unsigned vsp;
    #pragma unroll
    for(int i=0;i<6;i++) va[i]=((i&1)? vw1 : vw)+(i>>1)*2048;
    va[6]=vxs; va[7]=vxs+32768-1024; vb[0]=vxs+2048; vb[1]=vxs+2048+32768-1024;
    vsp=vx+(rb? s0-16+w : 0)*2048;
    const unsigned sw1=w*1024, sw2=w*2048;
    unsigned rot=ROTF(rbb);
    asm volatile("" : "+v"(va), "+v"(vb), "+v"(vsp));
    asm volatile("" : "+s"(rot));
    unsigned vt=(w&1)? (xr*512+idx*16+(l>>1))*2048+(l&1)*1024 : (2*idx+(l&1))*6144+((l>>1)&3)*2048+xc*1024;
    const int kt0=idx*64; int kvs=KS0+KVSEL;
    unsigned kvl=(kt0+16*w+g)*2048+xcd*256+fr*16, l16=l*16;
    const bool hl= w==0 && kt0;
    unsigned c46=sg(w<2? 128u : w==2? 393216u : 0u), c49=sg(w<2? sw1 : (unsigned)ALL_LDS-96), c64=sg(hl? 8192u : 0u), c68=sg(hl? AUX : ALL_LDS-96);
    unsigned long long c50= w<2? ~0ull : w==2? 63ull : 1ull, c66= hl? ~0ull : 1ull;
    asm volatile("" : "+v"(vt), "+v"(kvl), "+v"(l16));
    asm volatile("" : "+s"(c46), "+s"(c49), "+s"(c64), "+s"(c68), "+s"(c50), "+s"(c66), "+s"(kvs));
    ARGS();
    TSB(16);
    asm volatile(H0_ASM : "+{s[40:41]}"(x), "+{s[42:43]}"(wqkv) : "{v[0:7]}"(va), "{v[8:9]}"(vb), "{v10}"(vsp), "{s52}"(rot), "{s53}"(sw2), "{s59}"(w)
      : "memory", "scc", "s76", "s77", "s78", "s79", "s82", "s83");
    const u16* spb= w==2? wqkv : x;
    const int hl3=(3-(int)((size_t)spb>>7))&7, roth=ROTF(hl3), xs=((hl3-ROTF(0))&7)-LEAD;
    unsigned i11=sg((w==2? roth+LEAD+1 : rot+1)&15), sbo, sst;
    if(w>=2){
      vsp=vt+hl3*128;
      touch(spb, vsp+((roth+(l>>3))&15)*c46, w==2? 0x3f3f3f3f3f3f3f3full>>(56-8*LEAD) : xs<=1? 0xffffffffull : 1ull);
    }
    TSB(17);
    unsigned gof=(((gi&15)*128+64*(w&1)+4*(w>>1)+16*((l>>5)&1)+((l>>3)&3))*DM+(gi>>4)*64)*2+(l16&127);
    unsigned wv[2];
    wv[0]=(((l>>5)*16+w*4+((l>>3)&3))*DM+tn*64)*2+((l16&127)^((l&16)*2)^((w&1)*64)); wv[1]=wv[0]+32*DM*2;
    unsigned zpa=(w>>1)*9216+(w&1)*512+l16, hda=AUX+w*CV_W+(l&1)*CV_CP+(l>>1)*16, yra=AUX+tq*(CV_W+64)+(g>>1)*128+(g&1)*32+tp*8+w*1024;
    const unsigned f0=loc(flo(0,cb)), f1=loc(flo(1,tm*16+tn)), f2=loc(flo(2,gi));
    unsigned vc=(w&1)? (g==0? WS_GEN : (g==1? f0 : g==2? f1 : f2)^((l&1)<<13)) : idx*CODE_ST+fr*128u;
    unsigned kfo=f0+(l&31)*4, ofo= w==0? f1+fr*4 : WS_GEN;
    asm volatile("" : "+v"(kfo), "+v"(ofo), "+v"(gof), "+v"(wv[0]), "+v"(wv[1]), "+v"(hda), "+v"(yra), "+v"(zpa), "+v"(vc));
    unsigned long long kt=korder();
    asm volatile("" : "+s"(kt));
#define HDBG_O
#define HDBG_I
#define H_IN "{v[0:7]}"(va), "{v[8:9]}"(vb), "{v10}"(vsp), "{v11}"(kvl), "{s[40:41]}"(x), "{s[42:43]}"(wqkv), "{s[44:45]}"(spb), "{s46}"(c46), "{s49}"(c49), "{s[50:51]}"(c50), "{s53}"(sw2), \
      "{s58}"(kvs), "{s59}"(w), "{s[60:61]}"(kin), "{s[62:63]}"(vin), "{s64}"(c64), "{s[66:67]}"(c66), "{s68}"(c68) HDBG_I
#define H_CLOB "memory", "scc", "vcc", "s76", "s77", "s78", "s79", "s80", "s81", "s82", "s83", "s84", "s85", "v20", "v21", "v22", "v253", "v254", "v255"
    asm volatile(H1_ASM : "+{s47}"(i11), "+{s52}"(rot), "={s54}"(sbo), "={s55}"(sst) HDBG_O : H_IN : H_CLOB);
    const u16* hrow=hk+(size_t)(cb*4+w)*S;
    u32x4 h0; unsigned dvu;
    asm volatile("s_load_dwordx4 %0, %2, 0\n\ts_load_dword %1, %3, %4" : "=&s"(h0), "=&s"(dvu) : "s"(hrow), "s"(sD), "s"(cb*8+(w&2)*2) : "memory");
    unsigned long long pc; asm volatile("s_getpc_b64 %0" : "=s"(pc)); pc&=~4095ull;
    asm volatile("" : "+s"(wo), "+s"(qin), "+s"(out), "+s"(ws));
    __builtin_amdgcn_s_setprio(HP1);
    unsigned kf2=kfo;
    asm volatile(H_ASM : "+{v16}"(kf2), "+{s47}"(i11), "+{s52}"(rot), "+{s54}"(sbo), "+{s55}"(sst) HDBG_O
      : H_IN, "{v14}"(vc), "{v13}"(WS_GEN+256+(l&31)*4), "{s56}"(sg(3u<<(kvs-1) | 1u<<15)),
        "{s57}"(sg(((w&1)? (1u<<TGS | 3u<<PLS | 1u<<14 | (xs>1? 8u : 0u)) : 1u<<CPS)&~(unsigned)(c50>>32))), "{s[70:71]}"(ws), "{s[72:73]}"(pc) : H_CLOB);
    asm volatile("s_waitcnt lgkmcnt(0)" : "+s"(h0), "+s"(dvu) :: "memory");
    asm volatile("" : "+s"(bid), "+s"(w)); __builtin_assume(bid>=0 && bid<256 && w>=0 && w<4);
    { DERIVE(); const unsigned sw1=w*1024, sw2=w*2048;
    u32x4 qv[4];
    BAR(); TSB(14);
    __builtin_amdgcn_s_setprio(HP2);
    { const unsigned fa=AUX+w*CV_W+32;
      asm volatile("s_mov_b64 exec, -2\n\ts_add_u32 m0, %3, %4\n\ts_nop 0\n\tglobal_load_lds_dwordx4 %0, %1" LDPOL "\n\ts_mov_b32 m0, %3\n\ts_nop 0\n\tglobal_load_lds_dwordx4 %0, %2" LDPOL "\n\ts_mov_b64 exec, -1"
        :: "v"(l16), "s"(hrow), "s"(hrow-2), "s"(fa), "n"(CV_CP) : "memory", "scc");
      #pragma unroll
      for(int i=1;i<4;i++){ gldsn(hrow+i*512, l16, fa, CV_CP+i*1024); gldsn(hrow-2+i*512, l16, fa, i*1024); } }
    WAITV(8);
    vct[32+w]=1u;
    TSH(6);
    const unsigned tag=__builtin_amdgcn_readfirstlane(vct[26]); const bool kvok=__builtin_amdgcn_readfirstlane(vct[5])!=0;
    { const unsigned pl=__builtin_amdgcn_readfirstlane(vct[6]); cpar=(xcd>>2)^(pl>>13); kfo^=pl; if(w==0) ofo^=pl; }
    u32x4 hv;
    { const unsigned d0=(h0[0]&0xffff0000u)|(pack2(bflo(h0[0])+((w&1)? bfhi(dvu) : bflo(dvu)),0.f)&0xffffu);
      hv= l==4? u32x4{0,d0,h0[1],h0[2]} : l==5? u32x4{d0,h0[1],h0[2],h0[3]} : u32x4{0,0,0,0}; }
    asm volatile("s_mov_b64 exec, 63\n\tds_write_b128 %0, %1\n\ts_mov_b64 exec, %3\n\tds_write_b128 %2, %1 offset:7680\n\ts_mov_b64 exec, -1"
      :: "v"(hda), "v"(hv), "v"(zpa), "s"(0xffffffff00000000ull) : "memory");
#define QLD() asm volatile("global_load_dwordx4 %0, %4, %5" LDPOL "\n\tglobal_load_dwordx4 %1, %4, %6" LDPOL "\n\tglobal_load_dwordx4 %2, %4, %7" LDPOL "\n\tglobal_load_dwordx4 %3, %4, %8" LDPOL \
        : "=&v"(qv[0]), "=&v"(qv[1]), "=&v"(qv[2]), "=&v"(qv[3]) : "v"(gof), "s"(qin), "s"(qin+8*DM), "s"(qin+32*DM), "s"(qin+40*DM) : "memory")
    auto kvt=[&](){
      #pragma unroll
      for(int i=0;i<4;i++) gldsc(ws+loc(WS_KV+cb*32768)+(w>>1)*16384+(w&1)*4096+i*1024, l16, sw2, (i&1)*9216+(i>>1)*1024); };
    if(!kvok) wait_flag((const unsigned*)(ws+kfo), tag);
    kvt();
    TSH(7);
    auto wos=[&](int st){ if(NOWO && st>1) return; const u16* b=wo+(size_t)((kt>>(4*st))&15)*64*DM; glds(b,wv[0],sw1,WOB+st*8192); glds(b,wv[1],sw1,WOB+st*8192+4096); };
    wos(0); wos(1);
    while(!all4(36)) __builtin_amdgcn_s_sleep(1);
    #pragma unroll
    for(int st=2;st<NE;st++) wos(st);
    WAITV(2*NE); WAITL();
    vct[16+w]=1u;
    TSH(8);
    __builtin_amdgcn_s_setprio(HP4);
    if(GATE==1) while(!all4(28)) __builtin_amdgcn_s_sleep(1);
    if(GATE==2) while(vct[4]<4u) __builtin_amdgcn_s_sleep(1);
    TSF(24);
    QLD();
    #pragma unroll
    for(int st=NE;st<13;st++){ if(GATE==0 && st==7) while(!all4(0)) __builtin_amdgcn_s_sleep(1);
      WAITV(W1); TSF(24+st);
      if(st==EP+W1/2+1 || st==EP2+W1/2+1)
        asm volatile("s_cmp_lg_u32 %0, 0\n\ts_cbranch_scc1 1f\n\tv_cmp_ne_u32 vcc, %1, v253\n\tv_mov_b32 v253, %3\n\ts_nop 1\n\ts_and_b32 vcc_lo, vcc_lo, %2\n\ts_cbranch_scc1 1f\n\tv_mov_b32 v254, 1\n\tds_max_u32 v253, v254\n1:"
          :: "s"(w), "s"(tag), "s"(l1m), "n"(CT_OFF+96) : "memory", "scc", "vcc", "v253", "v254");
      if(st==EP || st==EP2) asm volatile("global_load_dword v253, %0, %1 sc1" :: "v"(ofo), "s"(ws) : "memory", "v253");
      wos(st); if(st==7+W1/2) vct[20+w]=7u; }
    unsigned op;
    asm volatile("global_load_dword %0, %1, %2 sc1" : "=v"(op) : "v"(ofo), "s"(ws) : "memory");
    WAITV(1); vct[20+w]=13u;
    TSH(9);
    asm volatile("s_waitcnt vmcnt(0)" : "+v"(qv[0]), "+v"(qv[1]), "+v"(qv[2]), "+v"(qv[3]), "+v"(op) :: "memory");
    unsigned oseen=0; bool ptch=false;
    auto olevel=[&](unsigned v){
      const unsigned ne=(unsigned)__ballot(v!=tag), lv= ne==0 ? 2u : (ne&l1m)==0 ? 1u : 0u;
      if(lv>oseen){ if(oseen==0){ TSH(10); } oseen=lv; if(l==0) __hip_atomic_fetch_max(ct+24, lv, __ATOMIC_RELAXED, __HIP_MEMORY_SCOPE_WORKGROUP); }
      if(PTCH && !ptch && !((ne>>(hp^8))&1)){ ptch=true; const char* b=ws+loc((tm*16+(hp^8))*32768);
        asm volatile("global_load_dword v255, %0, %1 sc1\n\tglobal_load_dword v255, %0, %2 sc1" :: "v"(l16*8), "s"(b), "s"(b+16384) : "memory", "v255"); } };
    auto opoll=[&](){ olevel(__hip_atomic_load((const unsigned*)(ws+ofo), __ATOMIC_RELAXED, __HIP_MEMORY_SCOPE_AGENT)); };
    if(w==0) olevel(op);
    float qf[32];
    auto qun=[&](){
    #pragma unroll
    for(int i=0;i<16;i++){ qf[2*i]=bflo(qv[i>>2][i&3]); qf[2*i+1]=bfhi(qv[i>>2][i&3]); }
    #pragma unroll
    for(int i=0;i<32;i+=8) asm volatile("" : "+v"(qf[i]), "+v"(qf[i+1]), "+v"(qf[i+2]), "+v"(qf[i+3]), "+v"(qf[i+4]), "+v"(qf[i+5]), "+v"(qf[i+6]), "+v"(qf[i+7]));
    };
    qun();
    auto wol=[&](int i){ if(!NOWO) glds(wo+(size_t)((kt>>(52+4*(i>>1)))&15)*64*DM, wv[i&1], sw1, i<4? AUX+4352+i*CV_W : (i&1)*4096); };
    while(!all4(44)){ if(w==0 && oseen<1) opoll(); else __builtin_amdgcn_s_sleep(1); }
    wol(0); wol(1); wol(2); wol(3);
    while(!all4(48)){ if(w==0 && oseen<1) opoll(); else __builtin_amdgcn_s_sleep(1); }
    wol(4); wol(5);
    __builtin_amdgcn_s_setprio(HP3);
    bool gen= !(bid==0 && w==1);
    auto trygen=[&](){
      unsigned v[4];
      #pragma unroll
      for(int i=0;i<4;i++) v[i]=__hip_atomic_load((const unsigned*)(ws+(loc(flo(0,i*64))^((i>>1)<<13))+(l>>5)*4096+(l&31)*4), __ATOMIC_RELAXED, __HIP_MEMORY_SCOPE_AGENT);
      if(__all(v[0]==tag && v[1]==tag && v[2]==tag && v[3]==tag)){ if(l==0) st4c(ws+WS_GEN, tag); gen=true; } };
    if(!gen) trygen();
    while(!((all4(8)>>w)&1)){
      if(w==0 && oseen<2) opoll();
      __builtin_amdgcn_s_sleep(OPOLLS);
    }
    TSH(11);
    __builtin_amdgcn_s_setprio(HP5);
    {
      const char* yb=smem+yra;
      s16x4 ya[4], yc[4];
      #pragma unroll
      for(int i=0;i<4;i++){ ya[i]=trread(yb+(i>>1)*512+(i&1)*64); yc[i]=trread(yb+(i>>1)*512+(i&1)*64+256); }
      #pragma unroll
      for(int i=0;i<4;i++) if(NOY!=1) st16c(ws+loc(WS_Y+(cb&~15)*32768)+(w*4+i)*32768+((cb>>3)&1)*16384+(cb&7)*1024, l16, __builtin_bit_cast(u32x4,__builtin_shufflevector(ya[i],yc[i],0,1,2,3,4,5,6,7)));
    }
    TSH(12);
    if(NOY){ WAITV(0); vct[20+w]=16u; for(int sp_=0; !gen && sp_<(1<<22); sp_++) trygen(); TS(20); TSDUMP(); return; }
    unsigned po=gof+(gof&~2047u);
    const char* p0=ws+loc(WS_Y+gi*32768);
    unsigned y0=g*1024+w*256+((fr^(g&2))*16);
    asm volatile("" : "+v"(y0), "+v"(po));
    WAITV(0);
    vct[20+w]=16u;
    if(l<4) st4c(ws+loc(flo(2,cb&~15))+(w*4+l)*128+(cb&15)*4, tag);
    TSH(13);
    if(w==0){ int sp_=0; while(oseen<2 && vct[24]<2u && ++sp_<(1<<22)){ opoll(); __builtin_amdgcn_s_sleep(8); } }
    if(w==0){
      const unsigned* fp=(const unsigned*)(ws+loc(flo(2,gi)))+(l&15); int spins=0;
      while(vct[27]==0u) __builtin_amdgcn_s_sleep(2);
      while(true){
        unsigned v=__hip_atomic_load(fp, __ATOMIC_RELAXED, __HIP_MEMORY_SCOPE_AGENT);
        if(__all(v==tag) || ++spins>(1<<20)) break;
        __builtin_amdgcn_s_sleep(POLLS);
      }
      { u32x4 v; do{ v=*(volatile LDS_AS u32x4*)(ct+20); }while(__builtin_amdgcn_readfirstlane(v[0]&v[1]&v[2]&v[3]&16u)==0); }
      vct[25]=1u;
    } else { while(vct[25]==0u) __builtin_amdgcn_s_sleep(2); }
    TSH(14);
    {
      u32x4 yv[4];
      #pragma unroll
      for(int i=0;i<4;i++) gldsc(p0+(i>>1)*16384+(i&1)*4096, y0^(i*64), w*CV_W, AUX+i*1024);
      const char* ys=smem+AUX+w*CV_W+(l&7)*512;
      const int yi=((l>>3)^((l&7)*2))*16;
      WAITV(0);
      yv[0]=*(const u32x4*)(ys+yi); yv[1]=*(const u32x4*)(ys+yi+256); yv[2]=*(const u32x4*)(ys+(yi^128)); yv[3]=*(const u32x4*)(ys+(yi^128)+256);
      TSH(15);
      #pragma unroll
      for(int r=0;r<4;r++){
        u32x4 ov;
        #pragma unroll
        for(int e=0;e<4;e++){ const unsigned y2=yv[(r>>1)*2+(e>>1)][(r&1)*2+(e&1)]; ov[e]=pack2(qf[r*8+2*e]*bflo(y2), qf[r*8+2*e+1]*bfhi(y2)); }
        stos(out+((r&1)*8+(r>>1)*32)*2048, po, ov);
      }
    }
    for(int sp_=0; !gen && sp_<(1<<22); sp_++) trygen();
    TS(20); TSDUMP();
    }
  }

}

extern "C" void kernel_launch(void* const* d_in, const int* in_sizes, int n_in, void* d_out, int out_size, void* d_ws, size_t ws_size, hipStream_t stream){
  const u16* q=(const u16*)d_in[0]; const u16* k=(const u16*)d_in[1]; const u16* v=(const u16*)d_in[2];
  const u16* shk=(const u16*)d_in[3]; const u16* hk=(const u16*)d_in[4]; const u16* sD=(const u16*)d_in[5];
  const u16* x=(const u16*)d_in[6]; const u16* wqkv=(const u16*)d_in[7]; const u16* wo=(const u16*)d_in[8];
  u16* ws=(u16*)(((size_t)d_ws+16383)&~(size_t)16383);
  fused_kernel<<<256,512,0,stream>>>(x,wqkv,k,v,shk,hk,wo,q,sD,(u16*)d_out,ws,ws);
}
```

```cpp
#include <hip/hip_runtime.h>
#include <hip/hip_bf16.h>
#include <stdint.h>
#define DBG_OFF (28u<<20)
#define SHOWC

typedef unsigned short u16;
typedef short s16x4 __attribute__((ext_vector_type(4)));
typedef float f32x32 __attribute__((ext_vector_type(32)));
typedef unsigned u32x4 __attribute__((ext_vector_type(4)));
typedef unsigned u32x2 __attribute__((ext_vector_type(2)));
#define LDS_AS __attribute__((address_space(3)))

__device__ __forceinline__ float bflo(unsigned x){ return __uint_as_float(x<<16); }
__device__ __forceinline__ float bfhi(unsigned x){ return __uint_as_float(x&0xffff0000u); }
typedef float f32x2 __attribute__((ext_vector_type(2)));
typedef __bf16 bf16x2_t __attribute__((ext_vector_type(2)));
__device__ __forceinline__ unsigned pack2(float lo, float hi){ return __builtin_bit_cast(unsigned, __builtin_convertvector(f32x2{lo,hi}, bf16x2_t)); }

#define GLDS(name,pol) __device__ __forceinline__ void name(const void* sb, unsigned voff, unsigned ldsb, int imm){ \
  asm volatile("s_add_u32 m0, %2, %3\n\ts_nop 0\n\tglobal_load_lds_dwordx4 %0, %1" pol :: "v"(voff), "s"(sb), "s"(ldsb), "n"(imm) : "memory", "scc"); }
#define LDPOL " nt"
GLDS(glds,"") GLDS(gldsn,LDPOL) GLDS(gldsc," sc1")
#define PACE 63
__device__ __forceinline__ void gldsp(const void* sb, unsigned voff, unsigned ldsb, int imm){
  asm volatile("s_add_u32 m0, %2, %3\n\ts_waitcnt vmcnt(%4)\n\tglobal_load_lds_dwordx4 %0, %1" :: "v"(voff), "s"(sb), "s"(ldsb), "n"(imm), "n"(PACE) : "memory", "scc"); }
__device__ __forceinline__ void touch(const void* sb, unsigned voff, unsigned long long mask){
  asm volatile("s_mov_b64 exec, %2\n\tglobal_load_dword v255, %0, %1\n\ts_mov_b64 exec, -1" :: "v"(voff), "s"(sb), "s"(mask) : "memory", "v255"); }
__device__ __forceinline__ void st16c(void* sb, unsigned vo, u32x4 v){ asm volatile("global_store_dwordx4 %0, %1, %2 sc1\n\ts_nop 1" :: "v"(vo), "v"(v), "s"(sb) : "memory"); }
#define OUTPOL " sc1 nt"
__device__ __forceinline__ void stos(void* sb, unsigned vo, u32x4 v){ asm volatile("global_store_dwordx4 %0, %1, %2" OUTPOL "\n\ts_nop 1" :: "v"(vo), "v"(v), "s"(sb) : "memory"); }
__device__ __forceinline__ void stoa(void* p, u32x4 v){ asm volatile("global_store_dwordx4 %0, %1, off" OUTPOL "\n\ts_nop 1" :: "v"(p), "v"(v) : "memory"); }
__device__ __forceinline__ void st4c(void* p, unsigned v){ asm volatile("global_store_dword %0, %1, off sc1" :: "v"(p), "v"(v) : "memory"); }
__device__ __forceinline__ void wait_flag(const unsigned* p, unsigned tag){
  int spins=0;
  while(true){
    unsigned v=__hip_atomic_load(p, __ATOMIC_RELAXED, __HIP_MEMORY_SCOPE_AGENT);
    if(__all(v==tag)) break;
    __builtin_amdgcn_s_sleep(2);
    if(++spins>(1<<22)) break;
  }
}
#define WAITV(n) asm volatile("s_waitcnt vmcnt(%0)"::"n"(n):"memory")
#define WAITL() asm volatile("s_waitcnt lgkmcnt(0)":::"memory")
#define BAR() do{ asm volatile("s_waitcnt lgkmcnt(0)":::"memory"); __builtin_amdgcn_s_barrier(); asm volatile("":::"memory"); }while(0)
#define LANES() int l; asm volatile("v_mbcnt_lo_u32_b32 %0, -1, 0\n\tv_mbcnt_hi_u32_b32 %0, -1, %0" : "=v"(l)); __builtin_assume(l>=0 && l<64); const int g=l>>4, fr=l&15, tq=(l&15)>>2, tp=l&3; (void)g; (void)fr; (void)tq; (void)tp;

__device__ __forceinline__ s16x4 trread(const char* p){ return __builtin_amdgcn_ds_read_tr16_b64_v4i16((LDS_AS s16x4*)p); }
__device__ __forceinline__ int wswz(int row){ return ((row>>1)&1) | (((row>>3)&1)<<1); }
__device__ __forceinline__ u32x4 widen(u32x2 a, u32x2 b){
  auto r0=__builtin_amdgcn_permlane16_swap(a[0],b[0],false,false);
  auto r1=__builtin_amdgcn_permlane16_swap(a[1],b[1],false,false);
  return u32x4{r0[0],r1[0],r0[1],r1[1]};
}

constexpr int S=2048, DM=1024;
constexpr int K1_XB=144*128, K1_WB=3*64*128, K1_ST=K1_XB+K1_WB;
constexpr int AUX=3*K1_ST, KVP_=1040;
constexpr int WOB=22528, CV_W=8448, CV_CP=4224;
constexpr int CT_OFF=AUX+33*KVP_;
constexpr int ALL_LDS=CT_OFF+208+96;
constexpr unsigned WS_KV=8u<<20, WS_Y=16u<<20, WS_FL=24u<<20, WS_GEN=WS_FL+(1u<<20);
__device__ __forceinline__ unsigned flo(int kind, int c){ return WS_FL+kind*65536+(c>>6)*16384+(c&63)*128; }

#define CPRIO 2
#define CODE_ST 1600
#define LEAD 3
#define HP3 3
#define HP2 3
#define HP4 3
#define QP 1
#define TNX 1
#define HWS 99
#define HP1 0
#define HUNR 15
#define NE 2
#define WOC 0
#define NS 4
#define PTCH 0
#define GATE 0
#define WRD 2
#define EP 99
#define EP2 99
#define HP5 3
#define NOWO 0
#define W0 6
#define OPD 8
#define WT0 99
#define W1 2
#define W2 2
#define W3 4
#define OPOLLS 1
#define POLLS 3
#define HPRE 0
#define CPS2 99
#define KR ((rbb+2*ra)%7)
#define NOY 0
#define YLDS 1
#define OLDS 0
#define KVSEL (rbb==3? 0 : idx%KSN)
#define KS0 1
#define KSN 3
#define KST 10
#define KVP 5
#define TGS 2
#define CPS 13
#define PLS 4
#define H0_ASM \
  "s_mul_i32 s76, s52, 0x60000\n\t" \
  "s_add_u32 s76, s42, s76\n\t" \
  "s_addc_u32 s77, s43, 0\n\t" \
  "s_add_u32 s82, s53, 86016\n\t" \
  "s_add_u32 m0, s82, 18432\n\t" \
  "s_lshl_b32 s78, s52, 7\n\t" \
  "global_load_lds_dwordx4 v0, s[76:77]\n\t" \
  "global_load_lds_dwordx4 v1, s[76:77] offset:1024\n\t" \
  "s_add_u32 s78, s40, s78\n\t" \
  "s_addc_u32 s79, s41, 0\n\t" \
  "s_add_u32 m0, s82, 2048\n\t" \
  "s_nop 0\n\t" \
  "global_load_lds_dwordx4 v6, s[78:79]\n\t" \
  "global_load_lds_dwordx4 v7, s[78:79] offset:1024\n\t" \
  "s_add_u32 m0, s82, 26624\n\t" \
  "s_nop 0\n\t" \
  "global_load_lds_dwordx4 v2, s[76:77]\n\t" \
  "global_load_lds_dwordx4 v3, s[76:77] offset:1024\n\t" \
  "s_add_u32 m0, s82, 10240\n\t" \
  "s_nop 0\n\t" \
  "global_load_lds_dwordx4 v8, s[78:79]\n\t" \
  "global_load_lds_dwordx4 v9, s[78:79] offset:1024\n\t" \
  "s_add_u32 m0, s82, 34816\n\t" \
  "s_nop 0\n\t" \
  "global_load_lds_dwordx4 v4, s[76:77]\n\t" \
  "global_load_lds_dwordx4 v5, s[76:77] offset:1024\n\t" \
  "s_cmp_gt_u32 s59, 1\n\t" \
  "s_cbranch_scc1 1f\n\t" \
  "s_lshr_b32 s83, s53, 1\n\t" \
  "s_add_u32 m0, s83, 86016\n\t" \
  "s_nop 0\n\t" \
  "global_load_lds_dwordx4 v10, s[78:79]\n\t" \
  "1:\n\t"
#define H1_ASM \
  "s_mov_b32 s55, 0\n\t" \
  "s_mov_b32 s54, 0\n\t" \
  "s_add_u32 s52, s52, 1\n\t" \
  "s_and_b32 s52, s52, 15\n\t" \
  "s_cmp_eq_u32 s58, 1\n\t" \
  "s_cbranch_scc1 1f\n\t" \
  "s_mul_i32 s76, s52, 0x60000\n\t" \
  "s_add_u32 s76, s42, s76\n\t" \
  "s_addc_u32 s77, s43, 0\n\t" \
  "s_lshl_b32 s78, s52, 7\n\t" \
  "s_add_u32 s78, s40, s78\n\t" \
  "s_addc_u32 s79, s41, 0\n\t" \
  "s_mul_i32 s80, s47, s46\n\t" \
  "s_add_u32 s80, s44, s80\n\t" \
  "s_addc_u32 s81, s45, 0\n\t" \
  "s_mov_b32 s82, s53\n\t" \
  "s_add_u32 m0, s82, 18432\n\t" \
  "s_add_u32 s52, s52, 1\n\t" \
  "global_load_lds_dwordx4 v0, s[76:77]\n\t" \
  "global_load_lds_dwordx4 v1, s[76:77] offset:1024\n\t" \
  "s_add_u32 m0, s82, 2048\n\t" \
  "s_and_b32 s52, s52, 15\n\t" \
  "global_load_lds_dwordx4 v6, s[78:79]\n\t" \
  "global_load_lds_dwordx4 v7, s[78:79] offset:1024\n\t" \
  "s_add_u32 m0, s82, 26624\n\t" \
  "s_add_u32 s47, s47, 1\n\t" \
  "global_load_lds_dwordx4 v2, s[76:77]\n\t" \
  "global_load_lds_dwordx4 v3, s[76:77] offset:1024\n\t" \
  "s_add_u32 m0, s82, 10240\n\t" \
  "s_and_b32 s47, s47, 15\n\t" \
  "global_load_lds_dwordx4 v8, s[78:79]\n\t" \
  "global_load_lds_dwordx4 v9, s[78:79] offset:1024\n\t" \
  "s_add_u32 m0, s82, 34816\n\t" \
  "s_and_b32 s83, s54, s51\n\t" \
  "global_load_lds_dwordx4 v4, s[76:77]\n\t" \
  "global_load_lds_dwordx4 v5, s[76:77] offset:1024\n\t" \
  "s_add_u32 m0, s83, s49\n\t" \
  "s_mov_b64 exec, s[50:51]\n\t" \
  "global_load_lds_dwordx4 v10, s[80:81]\n\t" \
  "s_mov_b64 exec, -1\n\t" \
  "s_mov_b32 s55, 1\n\t" \
  "s_mov_b32 s54, 43008\n\t" \
  "s_waitcnt vmcnt(11)\n\t" \
  "s_branch 2f\n\t" \
  "1:\n\t" \
  "s_mul_i32 s83, s59, 4160\n\t" \
  "v_add_u32 v20, 7152, v11\n\t" \
  "v_add_u32 v21, 14304, v11\n\t" \
  "v_add_u32 v22, 21456, v11\n\t" \
  "s_add_u32 m0, s83, 130064\n\t" \
  "s_nop 0\n\t" \
  "global_load_lds_dwordx4 v11, s[60:61]" LDPOL "\n\t" \
  "global_load_lds_dwordx4 v20, s[60:61] offset:1040" LDPOL "\n\t" \
  "global_load_lds_dwordx4 v21, s[60:61] offset:2080" LDPOL "\n\t" \
  "global_load_lds_dwordx4 v22, s[60:61] offset:3120" LDPOL "\n\t" \
  "s_add_u32 m0, s83, 146704\n\t" \
  "s_nop 0\n\t" \
  "global_load_lds_dwordx4 v11, s[62:63]" LDPOL "\n\t" \
  "global_load_lds_dwordx4 v20, s[62:63] offset:1040" LDPOL "\n\t" \
  "global_load_lds_dwordx4 v21, s[62:63] offset:2080" LDPOL "\n\t" \
  "global_load_lds_dwordx4 v22, s[62:63] offset:3120" LDPOL "\n\t" \
  "v_subrev_u32 v20, s64, v11\n\t" \
  "s_mov_b32 m0, s68\n\t" \
  "s_mov_b64 exec, s[66:67]\n\t" \
  "global_load_lds_dwordx4 v20, s[60:61]" LDPOL "\n\t" \
  "s_mov_b64 exec, -1\n\t" \
  "s_waitcnt vmcnt(9)\n\t" \
  "2:\n\t" \
  "s_waitcnt lgkmcnt(0)\n\t" \
  "s_barrier\n\t"
#define H_ASM \
  "s_mul_i32 s76, s52, 0x60000\n\t" \
  "s_add_u32 s76, s42, s76\n\t" \
  "s_addc_u32 s77, s43, 0\n\t" \
  "s_lshl_b32 s78, s52, 7\n\t" \
  "s_add_u32 s78, s40, s78\n\t" \
  "s_addc_u32 s79, s41, 0\n\t" \
  "s_mul_i32 s80, s47, s46\n\t" \
  "s_add_u32 s80, s44, s80\n\t" \
  "s_addc_u32 s81, s45, 0\n\t" \
  "s_add_u32 s82, s54, s53\n\t" \
  "s_cmp_eq_u32 s58, 2\n\t" \
  "s_cbranch_scc1 5f\n\t" \
  "s_branch 3f\n\t" \
  "1:\n\t" \
  "s_waitcnt vmcnt(11)\n\t" \
  "2:\n\t" \
  "s_barrier\n\t" \
  "3:\n\t" \
  "s_add_u32 s55, s55, 1\n\t" \
  "s_add_u32 m0, s82, 18432\n\t" \
  "s_add_u32 s52, s52, 1\n\t" \
  "global_load_lds_dwordx4 v0, s[76:77]\n\t" \
  "global_load_lds_dwordx4 v1, s[76:77] offset:1024\n\t" \
  "s_add_u32 m0, s82, 2048\n\t" \
  "s_and_b32 s52, s52, 15\n\t" \
  "global_load_lds_dwordx4 v6, s[78:79]\n\t" \
  "global_load_lds_dwordx4 v7, s[78:79] offset:1024\n\t" \
  "s_add_u32 m0, s82, 26624\n\t" \
  "s_add_u32 s47, s47, 1\n\t" \
  "global_load_lds_dwordx4 v2, s[76:77]\n\t" \
  "global_load_lds_dwordx4 v3, s[76:77] offset:1024\n\t" \
  "s_add_u32 m0, s82, 10240\n\t" \
  "s_and_b32 s47, s47, 15\n\t" \
  "global_load_lds_dwordx4 v8, s[78:79]\n\t" \
  "global_load_lds_dwordx4 v9, s[78:79] offset:1024\n\t" \
  "s_add_u32 m0, s82, 34816\n\t" \
  "s_and_b32 s83, s54, s51\n\t" \
  "global_load_lds_dwordx4 v4, s[76:77]\n\t" \
  "global_load_lds_dwordx4 v5, s[76:77] offset:1024\n\t" \
  "s_bitcmp1_b32 s57, s55\n\t" \
  "s_cbranch_scc1 10f\n\t" \
  "8:\n\t" \
  "s_add_u32 m0, s83, s49\n\t" \
  "s_mov_b64 exec, s[50:51]\n\t" \
  "global_load_lds_dwordx4 v10, s[80:81]\n\t" \
  "s_mov_b64 exec, -1\n\t" \
  "4:\n\t" \
  "s_mul_i32 s76, s52, 0x60000\n\t" \
  "s_add_u32 s76, s42, s76\n\t" \
  "s_addc_u32 s77, s43, 0\n\t" \
  "s_lshl_b32 s78, s52, 7\n\t" \
  "s_add_u32 s78, s40, s78\n\t" \
  "s_addc_u32 s79, s41, 0\n\t" \
  "s_mul_i32 s80, s47, s46\n\t" \
  "s_add_u32 s80, s44, s80\n\t" \
  "s_addc_u32 s81, s45, 0\n\t" \
  "s_add_u32 s54, s54, 43008\n\t" \
  "s_cmp_eq_u32 s54, 129024\n\t" \
  "s_cselect_b32 s54, 0, s54\n\t" \
  "s_add_u32 s82, s54, s53\n\t" \
  "s_bitcmp0_b32 s56, s55\n\t" \
  "s_cbranch_scc1 1b\n\t" \
  "s_cmp_eq_u32 s55, 15\n\t" \
  "s_cbranch_scc1 19f\n\t" \
  "s_cmp_eq_u32 s55, s58\n\t" \
  "s_cbranch_scc1 3b\n\t" \
  "s_waitcnt vmcnt(11)\n\t" \
  "s_barrier\n\t" \
  "5:\n\t" \
  "s_mul_i32 s83, s59, 4160\n\t" \
  "v_add_u32 v20, 7152, v11\n\t" \
  "v_add_u32 v21, 14304, v11\n\t" \
  "v_add_u32 v22, 21456, v11\n\t" \
  "s_add_u32 m0, s83, 130064\n\t" \
  "s_nop 0\n\t" \
  "global_load_lds_dwordx4 v11, s[60:61]" LDPOL "\n\t" \
  "global_load_lds_dwordx4 v20, s[60:61] offset:1040" LDPOL "\n\t" \
  "global_load_lds_dwordx4 v21, s[60:61] offset:2080" LDPOL "\n\t" \
  "global_load_lds_dwordx4 v22, s[60:61] offset:3120" LDPOL "\n\t" \
  "s_add_u32 m0, s83, 146704\n\t" \
  "s_nop 0\n\t" \
  "global_load_lds_dwordx4 v11, s[62:63]" LDPOL "\n\t" \
  "global_load_lds_dwordx4 v20, s[62:63] offset:1040" LDPOL "\n\t" \
  "global_load_lds_dwordx4 v21, s[62:63] offset:2080" LDPOL "\n\t" \
  "global_load_lds_dwordx4 v22, s[62:63] offset:3120" LDPOL "\n\t" \
  "v_subrev_u32 v20, s64, v11\n\t" \
  "s_mov_b32 m0, s68\n\t" \
  "s_mov_b64 exec, s[66:67]\n\t" \
  "global_load_lds_dwordx4 v20, s[60:61]" LDPOL "\n\t" \
  "s_mov_b64 exec, -1\n\t" \
  "s_waitcnt vmcnt(9)\n\t" \
  "s_branch 2b\n\t" \
  "10:\n\t" \
  "s_cmp_eq_u32 s59, 2\n\t" \
  "s_cbranch_scc0 11f\n\t" \
  "s_mov_b64 exec, 0xffff\n\t" \
  "global_load_dword v255, v14, s[72:73]\n\t" \
  "s_mov_b64 exec, -1\n\t" \
  "s_branch 4b\n\t" \
  "11:\n\t" \
  "s_cmp_eq_u32 s55, 2\n\t" \
  "s_cbranch_scc0 12f\n\t" \
  "global_load_dword v254, v14, s[70:71] sc1\n\t" \
  "s_branch 4b\n\t" \
  "12:\n\t" \
  "s_cmp_eq_u32 s55, 4\n\t" \
  "s_cbranch_scc0 15f\n\t" \
  "global_load_dword v253, v13, s[70:71] sc1\n\t" \
  "s_branch 4b\n\t" \
  "15:\n\t" \
  "s_cmp_eq_u32 s55, 5\n\t" \
  "s_cbranch_scc0 13f\n\t" \
  "v_readfirstlane_b32 s84, v254\n\t" \
  "s_mov_b32 m0, 0x400000\n\t" \
  "s_waitcnt vmcnt(10)\n\t" \
  "s_add_u32 s84, s84, 1\n\t" \
  "v_mov_b32 v254, s84\n\t" \
  "16:\n\t" \
  "v_cmp_eq_u32 vcc, v253, v254\n\t" \
  "s_nop 1\n\t" \
  "s_and_b32 s84, vcc_lo, 0x3fffffff\n\t" \
  "s_bcnt1_i32_b32 s85, s84\n\t" \
  "s_cmp_ge_u32 s85, 15\n\t" \
  "s_cbranch_scc1 17f\n\t" \
  "s_sub_u32 m0, m0, 1\n\t" \
  "s_cbranch_scc1 17f\n\t" \
  "global_load_dword v253, v13, s[70:71] sc1\n\t" \
  "s_waitcnt vmcnt(0)\n\t" \
  "s_branch 16b\n\t" \
  "17:\n\t" \
  "s_and_b32 s84, s84, 0xaaaaaaaa\n\t" \
  "s_bcnt1_i32_b32 s84, s84\n\t" \
  "s_and_b32 s84, s84, 8\n\t" \
  "s_lshl_b32 s84, s84, 10\n\t" \
  "v_mov_b32 v20, s84\n\t" \
  "v_mov_b32 v253, 163344\n\t" \
  "ds_write_b32 v253, v254 offset:104\n\t" \
  "ds_write_b32 v253, v20 offset:24\n\t" \
  "s_waitcnt lgkmcnt(0)\n\t" \
  "s_branch 8b\n\t" \
  "13:\n\t" \
  "s_mov_b32 exec_hi, 0\n\t" \
  "global_load_dword v255, v10, s[44:45]\n\t" \
  "s_mov_b64 exec, -1\n\t" \
  "s_branch 4b\n\t" \
  "19:\n\t" \
  "s_waitcnt vmcnt(11) lgkmcnt(0)\n\t" \
  "s_barrier\n\t"
#define C_ASM \
  "s_branch 29f\n\t" \
  "21:\n\t" \
  "s_mov_b64 s[68:69], %27\n\t" \
  "s_and_b32 s74, s68, 15\n\t" \
  "s_andn2_b32 s68, s68, 15\n\t" \
  "s_load_dword vcc_lo, s[68:69], 0 glc\n\t" \
  "s_and_b32 s75, s74, 1\n\t" \
  "s_lshl_b32 s70, s74, 9\n\t" \
  "s_lshl_b32 s71, s75, 13\n\t" \
  "s_or_b32 s70, s70, s71\n\t" \
  "s_waitcnt lgkmcnt(0)\n\t" \
  "s_or_b32 s68, s68, s70\n\t" \
  "s_add_u32 vcc_lo, vcc_lo, 1\n\t" \
  "v_mov_b32 v18, vcc_lo\n\t" \
  "s_memtime s[70:71]\n\t" \
  "s_waitcnt lgkmcnt(0)\n\t" \
  "s_load_dword s76, s[68:69], 16384 glc\n\t" \
  "s_waitcnt lgkmcnt(0)\n\t" \
  "s_xor_b32 s68, s68, 0x2000\n\t" \
  "s_memtime s[90:91]\n\t" \
  "s_waitcnt lgkmcnt(0)\n\t" \
  "s_load_dword s76, s[68:69], 16384 glc\n\t" \
  "s_waitcnt lgkmcnt(0)\n\t" \
  "s_sub_u32 vcc_lo, s90, s70\n\t" \
  "s_memtime s[70:71]\n\t" \
  "s_waitcnt lgkmcnt(0)\n\t" \
  "s_sub_u32 s70, s70, s90\n\t" \
  "s_cmp_gt_u32 vcc_lo, s70\n\t" \
  "s_cselect_b32 s70, 1, 0\n\t" \
  "s_xor_b32 s70, s70, s75\n\t" \
  "s_lshl1_add_u32 s70, s74, s70\n\t" \
  "s_lshl_b32 s70, s70, 2\n\t" \
  "v_mov_b32 v19, s70\n\t" \
  "s_and_b32 s68, s68, 0xffffc000\n\t" \
  "s_mov_b64 exec, 1\n\t" \
  "global_store_dword v19, v18, s[68:69] offset:256 sc1\n\t" \
  "s_branch 26f\n\t" \
  "28:\n\t" \
  "s_cmp_lg_u64 %27, 0\n\t" \
  "s_cbranch_scc1 21b\n\t" \
  "s_mov_b64 exec, 0\n\t" \
  "s_cmp_eq_u32 %13, 0\n\t" \
  "s_cbranch_scc1 500f\n\t" \
  "s_cmp_eq_u32 %13, 1\n\t" \
  "s_cbranch_scc1 501f\n\t" \
  "s_cmp_eq_u32 %13, 2\n\t" \
  "s_cbranch_scc1 502f\n\t" \
  "s_branch 503f\n\t" \
  "26:\n\t" \
  "s_mov_b64 exec, -1\n\t" \
  "s_add_u32 s88, s88, 4\n\t" \
  "s_addc_u32 s89, s89, 0\n\t" \
  "s_setpc_b64 s[88:89]\n\t" \
  ".p2align 12\n\t" \
  "29:\n\t" \
  "s_bitcmp0_b32 %13, 0\n\t" \
  "s_cbranch_scc1 8f\n\t" \
  "global_load_dwordx2 v[10:11], v51, %11\n\t" \
  "global_load_dwordx2 v[12:13], v51, %11 offset:128\n\t" \
  "v_xor_b32 v53, 64, v52\n\t" \
  "v_xor_b32 v55, 32, v54\n\t" \
  "v_add_u32 v56, 43008, v52\n\t" \
  "v_add_u32 v58, 43008, v54\n\t" \
  "v_xor_b32 v57, 64, v56\n\t" \
  "v_xor_b32 v59, 32, v58\n\t" \
  "v_add_u32 v60, 86016, v52\n\t" \
  "v_add_u32 v62, 86016, v54\n\t" \
  "v_xor_b32 v61, 64, v60\n\t" \
  "v_xor_b32 v63, 32, v62\n\t" \
  "s_mov_b32 vcc_hi, 0x05040100\n\t" \
  "s_waitcnt vmcnt(2)\n\t" \
  "s_barrier\n\t" \
  "ds_read_b64_tr_b16 v[64:65], v62 offset:26624\n\t" \
  "ds_read_b64_tr_b16 v[66:67], v62 offset:27136\n\t" \
  "ds_read_b128 v[68:71], v60 offset:10240\n\t" \
  "ds_read_b64_tr_b16 v[72:73], v62 offset:30720\n\t" \
  "ds_read_b64_tr_b16 v[74:75], v62 offset:31232\n\t" \
  "ds_read_b128 v[76:79], v61 offset:10240\n\t" \
  "ds_read_b128 v[80:83], v61 offset:12288\n\t" \
  "ds_read_b128 v[84:87], v60 offset:12288\n\t" \
  "ds_read_b128 v[88:91], v60 offset:14336\n\t" \
  "ds_read_b128 v[92:95], v61 offset:14336\n\t" \
  "ds_read_b128 v[96:99], v61 offset:16384\n\t" \
  "ds_read_b128 v[100:103], v60 offset:16384\n\t" \
  "ds_read_b64_tr_b16 v[104:105], v63 offset:26624\n\t" \
  "ds_read_b64_tr_b16 v[106:107], v63 offset:27136\n\t" \
  "ds_read_b64_tr_b16 v[108:109], v63 offset:30720\n\t" \
  "ds_read_b64_tr_b16 v[110:111], v63 offset:31232\n\t" \
  "s_waitcnt lgkmcnt(13)\n\t" \
  "v_mfma_f32_16x16x32_bf16 v[184:187], v[64:67], v[68:71], 0\n\t" \
  "ds_read_b64_tr_b16 v[112:113], v62 offset:34816\n\t" \
  "ds_read_b64_tr_b16 v[114:115], v62 offset:35328\n\t" \
  "s_waitcnt lgkmcnt(12)\n\t" \
  "v_mfma_f32_16x16x32_bf16 v[184:187], v[72:75], v[76:79], v[184:187]\n\t" \
  "ds_read_b64_tr_b16 v[116:117], v62 offset:38912\n\t" \
  "ds_read_b64_tr_b16 v[118:119], v62 offset:39424\n\t" \
  "s_waitcnt lgkmcnt(13)\n\t" \
  "v_mfma_f32_16x16x32_bf16 v[192:195], v[72:75], v[80:83], 0\n\t" \
  "ds_read_b128 v[120:123], v60 offset:0\n\t" \
  "s_waitcnt lgkmcnt(13)\n\t" \
  "v_mfma_f32_16x16x32_bf16 v[192:195], v[64:67], v[84:87], v[192:195]\n\t" \
  "ds_read_b128 v[124:127], v61 offset:0\n\t" \
  "s_waitcnt lgkmcnt(13)\n\t" \
  "v_mfma_f32_16x16x32_bf16 v[200:203], v[64:67], v[88:91], 0\n\t" \
  "ds_read_b64_tr_b16 v[128:129], v63 offset:34816\n\t" \
  "s_waitcnt lgkmcnt(13)\n\t" \
  "v_mfma_f32_16x16x32_bf16 v[200:203], v[72:75], v[92:95], v[200:203]\n\t" \
  "ds_read_b64_tr_b16 v[130:131], v63 offset:35328\n\t" \
  "s_waitcnt lgkmcnt(13)\n\t" \
  "v_mfma_f32_16x16x32_bf16 v[208:211], v[72:75], v[96:99], 0\n\t" \
  "ds_read_b64_tr_b16 v[132:133], v63 offset:38912\n\t" \
  "s_waitcnt lgkmcnt(9)\n\t" \
  "v_mfma_f32_16x16x32_bf16 v[208:211], v[64:67], v[100:103], v[208:211]\n\t" \
  "ds_read_b64_tr_b16 v[134:135], v63 offset:39424\n\t" \
  "v_mfma_f32_16x16x32_bf16 v[188:191], v[104:107], v[68:71], 0\n\t" \
  "ds_read_b64_tr_b16 v[136:137], v62 offset:18432\n\t" \
  "v_mfma_f32_16x16x32_bf16 v[188:191], v[108:111], v[76:79], v[188:191]\n\t" \
  "ds_read_b64_tr_b16 v[138:139], v62 offset:18944\n\t" \
  "v_mfma_f32_16x16x32_bf16 v[196:199], v[108:111], v[80:83], 0\n\t" \
  "ds_read_b64_tr_b16 v[140:141], v62 offset:22528\n\t" \
  "v_mfma_f32_16x16x32_bf16 v[196:199], v[104:107], v[84:87], v[196:199]\n\t" \
  "ds_read_b64_tr_b16 v[142:143], v62 offset:23040\n\t" \
  "v_mfma_f32_16x16x32_bf16 v[204:207], v[104:107], v[88:91], 0\n\t" \
  "ds_read_b64_tr_b16 v[144:145], v63 offset:18432\n\t" \
  "v_mfma_f32_16x16x32_bf16 v[204:207], v[108:111], v[92:95], v[204:207]\n\t" \
  "ds_read_b64_tr_b16 v[146:147], v63 offset:18944\n\t" \
  "v_mfma_f32_16x16x32_bf16 v[212:215], v[108:111], v[96:99], 0\n\t" \
  "ds_read_b64_tr_b16 v[148:149], v63 offset:22528\n\t" \
  "v_mfma_f32_16x16x32_bf16 v[212:215], v[104:107], v[100:103], v[212:215]\n\t" \
  "ds_read_b64_tr_b16 v[150:151], v63 offset:23040\n\t" \
  "s_waitcnt lgkmcnt(7)\n\t" \
  "v_mfma_f32_16x16x32_bf16 v[216:219], v[68:71], v[112:115], 0\n\t" \
  "v_mfma_f32_16x16x32_bf16 v[216:219], v[76:79], v[116:119], v[216:219]\n\t" \
  "v_mfma_f32_16x16x32_bf16 v[224:227], v[80:83], v[116:119], 0\n\t" \
  "v_mfma_f32_16x16x32_bf16 v[224:227], v[84:87], v[112:115], v[224:227]\n\t" \
  "v_mfma_f32_16x16x32_bf16 v[232:235], v[88:91], v[112:115], 0\n\t" \
  "v_mfma_f32_16x16x32_bf16 v[232:235], v[92:95], v[116:119], v[232:235]\n\t" \
  "v_mfma_f32_16x16x32_bf16 v[240:243], v[96:99], v[116:119], 0\n\t" \
  "v_mfma_f32_16x16x32_bf16 v[240:243], v[100:103], v[112:115], v[240:243]\n\t" \
  "v_mfma_f32_16x16x32_bf16 v[248:251], v[120:123], v[112:115], 0\n\t" \
  "v_mfma_f32_16x16x32_bf16 v[248:251], v[124:127], v[116:119], v[248:251]\n\t" \
  "v_mfma_f32_16x16x32_bf16 v[252:255], v[120:123], v[128:131], 0\n\t" \
  "v_mfma_f32_16x16x32_bf16 v[252:255], v[124:127], v[132:135], v[252:255]\n\t" \
  "v_mfma_f32_16x16x32_bf16 v[220:223], v[76:79], v[132:135], 0\n\t" \
  "v_mfma_f32_16x16x32_bf16 v[220:223], v[68:71], v[128:131], v[220:223]\n\t" \
  "s_waitcnt lgkmcnt(0)\n\t" \
  "s_getpc_b64 s[88:89]\n\t" \
  "s_branch 28b\n\t" \
  "s_barrier\n\t" \
  "v_mfma_f32_16x16x32_bf16 v[228:231], v[84:87], v[128:131], 0\n\t" \
  "ds_read_b64_tr_b16 v[72:73], v54 offset:26624\n\t" \
  "v_mfma_f32_16x16x32_bf16 v[228:231], v[80:83], v[132:135], v[228:231]\n\t" \
  "ds_read_b64_tr_b16 v[74:75], v54 offset:27136\n\t" \
  "v_mfma_f32_16x16x32_bf16 v[236:239], v[92:95], v[132:135], 0\n\t" \
  "ds_read_b128 v[64:67], v52 offset:10240\n\t" \
  "v_mfma_f32_16x16x32_bf16 v[236:239], v[88:91], v[128:131], v[236:239]\n\t" \
  "ds_read_b64_tr_b16 v[108:109], v54 offset:30720\n\t" \
  "v_mfma_f32_16x16x32_bf16 v[244:247], v[100:103], v[128:131], 0\n\t" \
  "ds_read_b64_tr_b16 v[110:111], v54 offset:31232\n\t" \
  "v_mfma_f32_16x16x32_bf16 v[244:247], v[96:99], v[132:135], v[244:247]\n\t" \
  "ds_read_b128 v[104:107], v53 offset:10240\n\t" \
  "v_mfma_f32_16x16x32_bf16 v[152:155], v[136:139], v[68:71], 0\n\t" \
  "ds_read_b128 v[112:115], v53 offset:12288\n\t" \
  "v_mfma_f32_16x16x32_bf16 v[152:155], v[140:143], v[76:79], v[152:155]\n\t" \
  "ds_read_b128 v[116:119], v52 offset:12288\n\t" \
  "v_mfma_f32_16x16x32_bf16 v[160:163], v[140:143], v[80:83], 0\n\t" \
  "ds_read_b128 v[120:123], v52 offset:14336\n\t" \
  "v_mfma_f32_16x16x32_bf16 v[160:163], v[136:139], v[84:87], v[160:163]\n\t" \
  "ds_read_b128 v[124:127], v53 offset:14336\n\t" \
  "v_mfma_f32_16x16x32_bf16 v[168:171], v[136:139], v[88:91], 0\n\t" \
  "ds_read_b128 v[128:131], v53 offset:16384\n\t" \
  "v_mfma_f32_16x16x32_bf16 v[168:171], v[140:143], v[92:95], v[168:171]\n\t" \
  "ds_read_b128 v[132:135], v52 offset:16384\n\t" \
  "v_mfma_f32_16x16x32_bf16 v[176:179], v[140:143], v[96:99], 0\n\t" \
  "ds_read_b64_tr_b16 v[140:141], v55 offset:26624\n\t" \
  "v_mfma_f32_16x16x32_bf16 v[176:179], v[136:139], v[100:103], v[176:179]\n\t" \
  "ds_read_b64_tr_b16 v[142:143], v55 offset:27136\n\t" \
  "v_mfma_f32_16x16x32_bf16 v[156:159], v[144:147], v[68:71], 0\n\t" \
  "ds_read_b64_tr_b16 v[136:137], v55 offset:30720\n\t" \
  "v_mfma_f32_16x16x32_bf16 v[156:159], v[148:151], v[76:79], v[156:159]\n\t" \
  "ds_read_b64_tr_b16 v[138:139], v55 offset:31232\n\t" \
  "v_mfma_f32_16x16x32_bf16 v[164:167], v[148:151], v[80:83], 0\n\t" \
  "ds_read_b64_tr_b16 v[68:69], v54 offset:34816\n\t" \
  "v_mfma_f32_16x16x32_bf16 v[164:167], v[144:147], v[84:87], v[164:167]\n\t" \
  "ds_read_b64_tr_b16 v[70:71], v54 offset:35328\n\t" \
  "v_mfma_f32_16x16x32_bf16 v[172:175], v[144:147], v[88:91], 0\n\t" \
  "ds_read_b64_tr_b16 v[76:77], v54 offset:38912\n\t" \
  "v_mfma_f32_16x16x32_bf16 v[172:175], v[148:151], v[92:95], v[172:175]\n\t" \
  "ds_read_b64_tr_b16 v[78:79], v54 offset:39424\n\t" \
  "v_mfma_f32_16x16x32_bf16 v[180:183], v[148:151], v[96:99], 0\n\t" \
  "ds_read_b128 v[80:83], v52 offset:0\n\t" \
  "v_mfma_f32_16x16x32_bf16 v[180:183], v[144:147], v[100:103], v[180:183]\n\t" \
  "ds_read_b128 v[84:87], v53 offset:0\n\t" \
  "s_waitcnt lgkmcnt(7)\n\t" \
  "v_mfma_f32_16x16x32_bf16 v[184:187], v[72:75], v[64:67], v[184:187]\n\t" \
  "ds_read_b64_tr_b16 v[88:89], v55 offset:34816\n\t" \
  "v_mfma_f32_16x16x32_bf16 v[184:187], v[108:111], v[104:107], v[184:187]\n\t" \
  "ds_read_b64_tr_b16 v[90:91], v55 offset:35328\n\t" \
  "v_mfma_f32_16x16x32_bf16 v[192:195], v[108:111], v[112:115], v[192:195]\n\t" \
  "ds_read_b64_tr_b16 v[92:93], v55 offset:38912\n\t" \
  "v_mfma_f32_16x16x32_bf16 v[192:195], v[72:75], v[116:119], v[192:195]\n\t" \
  "ds_read_b64_tr_b16 v[94:95], v55 offset:39424\n\t" \
  "v_mfma_f32_16x16x32_bf16 v[200:203], v[72:75], v[120:123], v[200:203]\n\t" \
  "ds_read_b64_tr_b16 v[96:97], v54 offset:18432\n\t" \
  "v_mfma_f32_16x16x32_bf16 v[200:203], v[108:111], v[124:127], v[200:203]\n\t" \
  "ds_read_b64_tr_b16 v[98:99], v54 offset:18944\n\t" \
  "v_mfma_f32_16x16x32_bf16 v[208:211], v[108:111], v[128:131], v[208:211]\n\t" \
  "ds_read_b64_tr_b16 v[148:149], v54 offset:22528\n\t" \
  "v_mfma_f32_16x16x32_bf16 v[208:211], v[72:75], v[132:135], v[208:211]\n\t" \
  "ds_read_b64_tr_b16 v[150:151], v54 offset:23040\n\t" \
  "v_mfma_f32_16x16x32_bf16 v[188:191], v[140:143], v[64:67], v[188:191]\n\t" \
  "ds_read_b64_tr_b16 v[100:101], v55 offset:18432\n\t" \
  "s_waitcnt lgkmcnt(7)\n\t" \
  "v_mfma_f32_16x16x32_bf16 v[188:191], v[136:139], v[104:107], v[188:191]\n\t" \
  "ds_read_b64_tr_b16 v[102:103], v55 offset:18944\n\t" \
  "v_mfma_f32_16x16x32_bf16 v[196:199], v[136:139], v[112:115], v[196:199]\n\t" \
  "ds_read_b64_tr_b16 v[144:145], v55 offset:22528\n\t" \
  "v_mfma_f32_16x16x32_bf16 v[196:199], v[140:143], v[116:119], v[196:199]\n\t" \
  "ds_read_b64_tr_b16 v[146:147], v55 offset:23040\n\t" \
  "v_mfma_f32_16x16x32_bf16 v[204:207], v[140:143], v[120:123], v[204:207]\n\t" \
  "v_mfma_f32_16x16x32_bf16 v[204:207], v[136:139], v[124:127], v[204:207]\n\t" \
  "v_mfma_f32_16x16x32_bf16 v[212:215], v[136:139], v[128:131], v[212:215]\n\t" \
  "v_mfma_f32_16x16x32_bf16 v[212:215], v[140:143], v[132:135], v[212:215]\n\t" \
  "v_mfma_f32_16x16x32_bf16 v[216:219], v[64:67], v[68:71], v[216:219]\n\t" \
  "v_mfma_f32_16x16x32_bf16 v[216:219], v[104:107], v[76:79], v[216:219]\n\t" \
  "v_mfma_f32_16x16x32_bf16 v[224:227], v[112:115], v[76:79], v[224:227]\n\t" \
  "v_mfma_f32_16x16x32_bf16 v[224:227], v[116:119], v[68:71], v[224:227]\n\t" \
  "v_mfma_f32_16x16x32_bf16 v[232:235], v[120:123], v[68:71], v[232:235]\n\t" \
  "v_mfma_f32_16x16x32_bf16 v[232:235], v[124:127], v[76:79], v[232:235]\n\t" \
  "v_mfma_f32_16x16x32_bf16 v[240:243], v[128:131], v[76:79], v[240:243]\n\t" \
  "v_mfma_f32_16x16x32_bf16 v[240:243], v[132:135], v[68:71], v[240:243]\n\t" \
  "v_mfma_f32_16x16x32_bf16 v[248:251], v[80:83], v[68:71], v[248:251]\n\t" \
  "v_mfma_f32_16x16x32_bf16 v[248:251], v[84:87], v[76:79], v[248:251]\n\t" \
  "v_mfma_f32_16x16x32_bf16 v[252:255], v[80:83], v[88:91], v[252:255]\n\t" \
  "s_waitcnt lgkmcnt(0)\n\t" \
  "v_mfma_f32_16x16x32_bf16 v[252:255], v[84:87], v[92:95], v[252:255]\n\t" \
  "v_mfma_f32_16x16x32_bf16 v[220:223], v[104:107], v[92:95], v[220:223]\n\t" \
  "v_mfma_f32_16x16x32_bf16 v[220:223], v[64:67], v[88:91], v[220:223]\n\t" \
  "s_barrier\n\t" \
  "v_mfma_f32_16x16x32_bf16 v[228:231], v[116:119], v[88:91], v[228:231]\n\t" \
  "ds_read_b64_tr_b16 v[108:109], v58 offset:26624\n\t" \
  "v_mfma_f32_16x16x32_bf16 v[228:231], v[112:115], v[92:95], v[228:231]\n\t" \
  "ds_read_b64_tr_b16 v[110:111], v58 offset:27136\n\t" \
  "v_mfma_f32_16x16x32_bf16 v[236:239], v[124:127], v[92:95], v[236:239]\n\t" \
  "ds_read_b128 v[72:75], v56 offset:10240\n\t" \
  "v_mfma_f32_16x16x32_bf16 v[236:239], v[120:123], v[88:91], v[236:239]\n\t" \
  "ds_read_b64_tr_b16 v[136:137], v58 offset:30720\n\t" \
  "v_mfma_f32_16x16x32_bf16 v[244:247], v[132:135], v[88:91], v[244:247]\n\t" \
  "ds_read_b64_tr_b16 v[138:139], v58 offset:31232\n\t" \
  "v_mfma_f32_16x16x32_bf16 v[244:247], v[128:131], v[92:95], v[244:247]\n\t" \
  "ds_read_b128 v[140:143], v57 offset:10240\n\t" \
  "v_mfma_f32_16x16x32_bf16 v[152:155], v[96:99], v[64:67], v[152:155]\n\t" \
  "ds_read_b128 v[68:71], v57 offset:12288\n\t" \
  "v_mfma_f32_16x16x32_bf16 v[152:155], v[148:151], v[104:107], v[152:155]\n\t" \
  "ds_read_b128 v[76:79], v56 offset:12288\n\t" \
  "v_mfma_f32_16x16x32_bf16 v[160:163], v[148:151], v[112:115], v[160:163]\n\t" \
  "ds_read_b128 v[80:83], v56 offset:14336\n\t" \
  "v_mfma_f32_16x16x32_bf16 v[160:163], v[96:99], v[116:119], v[160:163]\n\t" \
  "ds_read_b128 v[84:87], v57 offset:14336\n\t" \
  "v_mfma_f32_16x16x32_bf16 v[168:171], v[96:99], v[120:123], v[168:171]\n\t" \
  "ds_read_b128 v[88:91], v57 offset:16384\n\t" \
  "v_mfma_f32_16x16x32_bf16 v[168:171], v[148:151], v[124:127], v[168:171]\n\t" \
  "ds_read_b128 v[92:95], v56 offset:16384\n\t" \
  "v_mfma_f32_16x16x32_bf16 v[176:179], v[148:151], v[128:131], v[176:179]\n\t" \
  "ds_read_b64_tr_b16 v[148:149], v59 offset:26624\n\t" \
  "v_mfma_f32_16x16x32_bf16 v[176:179], v[96:99], v[132:135], v[176:179]\n\t" \
  "ds_read_b64_tr_b16 v[150:151], v59 offset:27136\n\t" \
  "v_mfma_f32_16x16x32_bf16 v[156:159], v[100:103], v[64:67], v[156:159]\n\t" \
  "ds_read_b64_tr_b16 v[96:97], v59 offset:30720\n\t" \
  "v_mfma_f32_16x16x32_bf16 v[156:159], v[144:147], v[104:107], v[156:159]\n\t" \
  "ds_read_b64_tr_b16 v[98:99], v59 offset:31232\n\t" \
  "v_mfma_f32_16x16x32_bf16 v[164:167], v[144:147], v[112:115], v[164:167]\n\t" \
  "ds_read_b64_tr_b16 v[64:65], v58 offset:34816\n\t" \
  "v_mfma_f32_16x16x32_bf16 v[164:167], v[100:103], v[116:119], v[164:167]\n\t" \
  "ds_read_b64_tr_b16 v[66:67], v58 offset:35328\n\t" \
  "v_mfma_f32_16x16x32_bf16 v[172:175], v[100:103], v[120:123], v[172:175]\n\t" \
  "ds_read_b64_tr_b16 v[104:105], v58 offset:38912\n\t" \
  "v_mfma_f32_16x16x32_bf16 v[172:175], v[144:147], v[124:127], v[172:175]\n\t" \
  "ds_read_b64_tr_b16 v[106:107], v58 offset:39424\n\t" \
  "v_mfma_f32_16x16x32_bf16 v[180:183], v[144:147], v[128:131], v[180:183]\n\t" \
  "ds_read_b128 v[112:115], v56 offset:0\n\t" \
  "v_mfma_f32_16x16x32_bf16 v[180:183], v[100:103], v[132:135], v[180:183]\n\t" \
  "ds_read_b128 v[116:119], v57 offset:0\n\t" \
  "s_waitcnt lgkmcnt(7)\n\t" \
  "v_mfma_f32_16x16x32_bf16 v[184:187], v[108:111], v[72:75], v[184:187]\n\t" \
  "ds_read_b64_tr_b16 v[120:121], v59 offset:34816\n\t" \
  "v_mfma_f32_16x16x32_bf16 v[184:187], v[136:139], v[140:143], v[184:187]\n\t" \
  "ds_read_b64_tr_b16 v[122:123], v59 offset:35328\n\t" \
  "v_mfma_f32_16x16x32_bf16 v[192:195], v[136:139], v[68:71], v[192:195]\n\t" \
  "ds_read_b64_tr_b16 v[124:125], v59 offset:38912\n\t" \
  "v_mfma_f32_16x16x32_bf16 v[192:195], v[108:111], v[76:79], v[192:195]\n\t" \
  "ds_read_b64_tr_b16 v[126:127], v59 offset:39424\n\t" \
  "v_mfma_f32_16x16x32_bf16 v[200:203], v[108:111], v[80:83], v[200:203]\n\t" \
  "ds_read_b64_tr_b16 v[128:129], v58 offset:18432\n\t" \
  "v_mfma_f32_16x16x32_bf16 v[200:203], v[136:139], v[84:87], v[200:203]\n\t" \
  "ds_read_b64_tr_b16 v[130:131], v58 offset:18944\n\t" \
  "v_mfma_f32_16x16x32_bf16 v[208:211], v[136:139], v[88:91], v[208:211]\n\t" \
  "ds_read_b64_tr_b16 v[144:145], v58 offset:22528\n\t" \
  "v_mfma_f32_16x16x32_bf16 v[208:211], v[108:111], v[92:95], v[208:211]\n\t" \
  "ds_read_b64_tr_b16 v[146:147], v58 offset:23040\n\t" \
  "v_mfma_f32_16x16x32_bf16 v[188:191], v[148:151], v[72:75], v[188:191]\n\t" \
  "ds_read_b64_tr_b16 v[100:101], v59 offset:18432\n\t" \
  "s_waitcnt lgkmcnt(7)\n\t" \
  "v_mfma_f32_16x16x32_bf16 v[188:191], v[96:99], v[140:143], v[188:191]\n\t" \
  "ds_read_b64_tr_b16 v[102:103], v59 offset:18944\n\t" \
  "v_mfma_f32_16x16x32_bf16 v[196:199], v[96:99], v[68:71], v[196:199]\n\t" \
  "ds_read_b64_tr_b16 v[132:133], v59 offset:22528\n\t" \
  "v_mfma_f32_16x16x32_bf16 v[196:199], v[148:151], v[76:79], v[196:199]\n\t" \
  "ds_read_b64_tr_b16 v[134:135], v59 offset:23040\n\t" \
  "v_mfma_f32_16x16x32_bf16 v[204:207], v[148:151], v[80:83], v[204:207]\n\t" \
  "v_mfma_f32_16x16x32_bf16 v[204:207], v[96:99], v[84:87], v[204:207]\n\t" \
  "v_mfma_f32_16x16x32_bf16 v[212:215], v[96:99], v[88:91], v[212:215]\n\t" \
  "v_mfma_f32_16x16x32_bf16 v[212:215], v[148:151], v[92:95], v[212:215]\n\t" \
  "v_mfma_f32_16x16x32_bf16 v[216:219], v[72:75], v[64:67], v[216:219]\n\t" \
  "v_mfma_f32_16x16x32_bf16 v[216:219], v[140:143], v[104:107], v[216:219]\n\t" \
  "v_mfma_f32_16x16x32_bf16 v[224:227], v[68:71], v[104:107], v[224:227]\n\t" \
  "v_mfma_f32_16x16x32_bf16 v[224:227], v[76:79], v[64:67], v[224:227]\n\t" \
  "v_mfma_f32_16x16x32_bf16 v[232:235], v[80:83], v[64:67], v[232:235]\n\t" \
  "v_mfma_f32_16x16x32_bf16 v[232:235], v[84:87], v[104:107], v[232:235]\n\t" \
  "v_mfma_f32_16x16x32_bf16 v[240:243], v[88:91], v[104:107], v[240:243]\n\t" \
  "v_mfma_f32_16x16x32_bf16 v[240:243], v[92:95], v[64:67], v[240:243]\n\t" \
  "v_mfma_f32_16x16x32_bf16 v[248:251], v[112:115], v[64:67], v[248:251]\n\t" \
  "v_mfma_f32_16x16x32_bf16 v[248:251], v[116:119], v[104:107], v[248:251]\n\t" \
  "v_mfma_f32_16x16x32_bf16 v[252:255], v[112:115], v[120:123], v[252:255]\n\t" \
  "s_waitcnt lgkmcnt(0)\n\t" \
  "v_mfma_f32_16x16x32_bf16 v[252:255], v[116:119], v[124:127], v[252:255]\n\t" \
  "v_mfma_f32_16x16x32_bf16 v[220:223], v[140:143], v[124:127], v[220:223]\n\t" \
  "v_mfma_f32_16x16x32_bf16 v[220:223], v[72:75], v[120:123], v[220:223]\n\t" \
  "s_barrier\n\t" \
  "v_mfma_f32_16x16x32_bf16 v[228:231], v[76:79], v[120:123], v[228:231]\n\t" \
  "ds_read_b64_tr_b16 v[136:137], v62 offset:26624\n\t" \
  "v_bfe_u32 v30, v4, 9, 1\n\t" \
  "v_mfma_f32_16x16x32_bf16 v[228:231], v[68:71], v[124:127], v[228:231]\n\t" \
  "ds_read_b64_tr_b16 v[138:139], v62 offset:27136\n\t" \
  "v_mad_i32_i24 v30, v30, -16, v0\n\t" \
  "v_mfma_f32_16x16x32_bf16 v[236:239], v[84:87], v[124:127], v[236:239]\n\t" \
  "ds_read_b128 v[108:111], v60 offset:10240\n\t" \
  "ds_read_b128 v[26:29], v0 offset:1552\n\t" \
  "v_mfma_f32_16x16x32_bf16 v[236:239], v[80:83], v[120:123], v[236:239]\n\t" \
  "ds_read_b64_tr_b16 v[96:97], v62 offset:30720\n\t" \
  "ds_read_b128 v[30:33], v30 offset:1040\n\t" \
  "v_mfma_f32_16x16x32_bf16 v[244:247], v[92:95], v[120:123], v[244:247]\n\t" \
  "ds_read_b64_tr_b16 v[98:99], v62 offset:31232\n\t" \
  "ds_read_b128 v[34:37], v0 offset:512\n\t" \
  "v_mfma_f32_16x16x32_bf16 v[244:247], v[88:91], v[124:127], v[244:247]\n\t" \
  "ds_read_b128 v[148:151], v61 offset:10240\n\t" \
  "ds_read_b64_tr_b16 v[38:39], v1 offset:512\n\t" \
  "v_mfma_f32_16x16x32_bf16 v[152:155], v[128:131], v[72:75], v[152:155]\n\t" \
  "ds_read_b128 v[64:67], v61 offset:12288\n\t" \
  "ds_read_b64_tr_b16 v[40:41], v1 offset:0\n\t" \
  "v_mfma_f32_16x16x32_bf16 v[152:155], v[144:147], v[140:143], v[152:155]\n\t" \
  "ds_read_b128 v[104:107], v60 offset:12288\n\t" \
  "s_waitcnt vmcnt(0)\n\t" \
  "v_mfma_f32_16x16x32_bf16 v[160:163], v[144:147], v[68:71], v[160:163]\n\t" \
  "ds_read_b128 v[112:115], v60 offset:14336\n\t" \
  "v_perm_b32 v18, v10, v10, v14\n\t" \
  "v_mfma_f32_16x16x32_bf16 v[160:163], v[128:131], v[76:79], v[160:163]\n\t" \
  "ds_read_b128 v[116:119], v61 offset:14336\n\t" \
  "v_perm_b32 v19, v10, v10, v15\n\t" \
  "v_mfma_f32_16x16x32_bf16 v[168:171], v[128:131], v[80:83], v[168:171]\n\t" \
  "ds_read_b128 v[120:123], v61 offset:16384\n\t" \
  "v_perm_b32 v20, v10, v10, v16\n\t" \
  "v_mfma_f32_16x16x32_bf16 v[168:171], v[144:147], v[84:87], v[168:171]\n\t" \
  "ds_read_b128 v[124:127], v60 offset:16384\n\t" \
  "v_perm_b32 v21, v10, v10, v17\n\t" \
  "v_mfma_f32_16x16x32_bf16 v[176:179], v[144:147], v[88:91], v[176:179]\n\t" \
  "ds_read_b64_tr_b16 v[144:145], v63 offset:26624\n\t" \
  "v_perm_b32 v22, v11, v11, v14\n\t" \
  "v_mfma_f32_16x16x32_bf16 v[176:179], v[128:131], v[92:95], v[176:179]\n\t" \
  "ds_read_b64_tr_b16 v[146:147], v63 offset:27136\n\t" \
  "v_perm_b32 v23, v11, v11, v15\n\t" \
  "v_mfma_f32_16x16x32_bf16 v[156:159], v[100:103], v[72:75], v[156:159]\n\t" \
  "ds_read_b64_tr_b16 v[128:129], v63 offset:30720\n\t" \
  "v_perm_b32 v24, v11, v11, v16\n\t" \
  "v_mfma_f32_16x16x32_bf16 v[156:159], v[132:135], v[140:143], v[156:159]\n\t" \
  "ds_read_b64_tr_b16 v[130:131], v63 offset:31232\n\t" \
  "v_perm_b32 v25, v11, v11, v17\n\t" \
  "v_mfma_f32_16x16x32_bf16 v[164:167], v[132:135], v[68:71], v[164:167]\n\t" \
  "ds_read_b64_tr_b16 v[72:73], v62 offset:34816\n\t" \
  "v_mfma_f32_16x16x32_bf16 v[164:167], v[100:103], v[76:79], v[164:167]\n\t" \
  "ds_read_b64_tr_b16 v[74:75], v62 offset:35328\n\t" \
  "v_mfma_f32_16x16x32_bf16 v[172:175], v[100:103], v[80:83], v[172:175]\n\t" \
  "ds_read_b64_tr_b16 v[140:141], v62 offset:38912\n\t" \
  "s_waitcnt lgkmcnt(6)\n\t" \
  "v_mfma_f32_16x16x32_bf16 v[42:45], v[26:29], v[18:21], 0\n\t" \
  "v_mfma_f32_16x16x32_bf16 v[42:45], v[30:33], v[22:25], v[42:45]\n\t" \
  "v_mfma_f32_16x16x32_bf16 v[46:49], v[30:33], v[18:21], 0\n\t" \
  "v_mfma_f32_16x16x32_bf16 v[46:49], v[34:37], v[22:25], v[46:49]\n\t" \
  "ds_read_b128 v[26:29], v0 offset:1808\n\t" \
  "ds_read_b128 v[30:33], v0 offset:1296\n\t" \
  "v_mfma_f32_16x16x32_bf16 v[172:175], v[132:135], v[84:87], v[172:175]\n\t" \
  "ds_read_b64_tr_b16 v[142:143], v62 offset:39424\n\t" \
  "ds_read_b128 v[34:37], v0 offset:768\n\t" \
  "v_mfma_f32_16x16x32_bf16 v[180:183], v[132:135], v[88:91], v[180:183]\n\t" \
  "ds_read_b128 v[68:71], v60 offset:0\n\t" \
  "v_mfma_f32_16x16x32_bf16 v[180:183], v[100:103], v[92:95], v[180:183]\n\t" \
  "ds_read_b128 v[76:79], v61 offset:0\n\t" \
  "v_mfma_f32_16x16x32_bf16 v[184:187], v[136:139], v[108:111], v[184:187]\n\t" \
  "ds_read_b64_tr_b16 v[80:81], v63 offset:34816\n\t" \
  "v_lshlrev_b32 v50, 16, v38\n\t" \
  "v_mfma_f32_16x16x32_bf16 v[184:187], v[96:99], v[148:151], v[184:187]\n\t" \
  "ds_read_b64_tr_b16 v[82:83], v63 offset:35328\n\t" \
  "v_lshlrev_b32 v51, 16, v40\n\t" \
  "v_mfma_f32_16x16x32_bf16 v[192:195], v[96:99], v[64:67], v[192:195]\n\t" \
  "ds_read_b64_tr_b16 v[84:85], v63 offset:38912\n\t" \
  "v_mul_f32 v42, v42, v50\n\t" \
  "v_mfma_f32_16x16x32_bf16 v[192:195], v[136:139], v[104:107], v[192:195]\n\t" \
  "ds_read_b64_tr_b16 v[86:87], v63 offset:39424\n\t" \
  "v_mul_f32 v46, v46, v51\n\t" \
  "v_mfma_f32_16x16x32_bf16 v[200:203], v[136:139], v[112:115], v[200:203]\n\t" \
  "ds_read_b64_tr_b16 v[88:89], v62 offset:18432\n\t" \
  "v_cvt_pk_bf16_f32 v42, v42, v46\n\t" \
  "v_mfma_f32_16x16x32_bf16 v[200:203], v[96:99], v[116:119], v[200:203]\n\t" \
  "ds_read_b64_tr_b16 v[90:91], v62 offset:18944\n\t" \
  "v_and_b32 v50, 0xffff0000, v38\n\t" \
  "v_mfma_f32_16x16x32_bf16 v[208:211], v[96:99], v[120:123], v[208:211]\n\t" \
  "ds_read_b64_tr_b16 v[132:133], v62 offset:22528\n\t" \
  "v_and_b32 v51, 0xffff0000, v40\n\t" \
  "v_mfma_f32_16x16x32_bf16 v[208:211], v[136:139], v[124:127], v[208:211]\n\t" \
  "ds_read_b64_tr_b16 v[134:135], v62 offset:23040\n\t" \
  "v_mul_f32 v43, v43, v50\n\t" \
  "s_waitcnt lgkmcnt(7)\n\t" \
  "v_mfma_f32_16x16x32_bf16 v[188:191], v[144:147], v[108:111], v[188:191]\n\t" \
  "ds_read_b64_tr_b16 v[92:93], v63 offset:18432\n\t" \
  "v_mul_f32 v47, v47, v51\n\t" \
  "v_mfma_f32_16x16x32_bf16 v[188:191], v[128:131], v[148:151], v[188:191]\n\t" \
  "ds_read_b64_tr_b16 v[94:95], v63 offset:18944\n\t" \
  "v_cvt_pk_bf16_f32 v43, v43, v47\n\t" \
  "v_mfma_f32_16x16x32_bf16 v[196:199], v[128:131], v[64:67], v[196:199]\n\t" \
  "ds_read_b64_tr_b16 v[100:101], v63 offset:22528\n\t" \
  "v_lshlrev_b32 v50, 16, v39\n\t" \
  "v_mfma_f32_16x16x32_bf16 v[196:199], v[144:147], v[104:107], v[196:199]\n\t" \
  "ds_read_b64_tr_b16 v[102:103], v63 offset:23040\n\t" \
  "v_lshlrev_b32 v51, 16, v41\n\t" \
  "v_mfma_f32_16x16x32_bf16 v[204:207], v[144:147], v[112:115], v[204:207]\n\t" \
  "v_mul_f32 v44, v44, v50\n\t" \
  "v_mul_f32 v48, v48, v51\n\t" \
  "v_mfma_f32_16x16x32_bf16 v[204:207], v[128:131], v[116:119], v[204:207]\n\t" \
  "v_cvt_pk_bf16_f32 v44, v44, v48\n\t" \
  "v_and_b32 v50, 0xffff0000, v39\n\t" \
  "v_mfma_f32_16x16x32_bf16 v[212:215], v[128:131], v[120:123], v[212:215]\n\t" \
  "v_and_b32 v51, 0xffff0000, v41\n\t" \
  "v_mul_f32 v45, v45, v50\n\t" \
  "v_mfma_f32_16x16x32_bf16 v[212:215], v[144:147], v[124:127], v[212:215]\n\t" \
  "v_mul_f32 v49, v49, v51\n\t" \
  "ds_read_b64_tr_b16 v[38:39], v1 offset:768\n\t" \
  "v_mfma_f32_16x16x32_bf16 v[216:219], v[108:111], v[72:75], v[216:219]\n\t" \
  "ds_read_b64_tr_b16 v[40:41], v1 offset:256\n\t" \
  "v_cvt_pk_bf16_f32 v45, v45, v49\n\t" \
  "v_mfma_f32_16x16x32_bf16 v[216:219], v[148:151], v[140:143], v[216:219]\n\t" \
  "v_mfma_f32_16x16x32_bf16 v[224:227], v[64:67], v[140:143], v[224:227]\n\t" \
  "v_mfma_f32_16x16x32_bf16 v[224:227], v[104:107], v[72:75], v[224:227]\n\t" \
  "v_mfma_f32_16x16x32_bf16 v[232:235], v[112:115], v[72:75], v[232:235]\n\t" \
  "v_mfma_f32_16x16x32_bf16 v[232:235], v[116:119], v[140:143], v[232:235]\n\t" \
  "v_mfma_f32_16x16x32_bf16 v[240:243], v[120:123], v[140:143], v[240:243]\n\t" \
  "v_mfma_f32_16x16x32_bf16 v[240:243], v[124:127], v[72:75], v[240:243]\n\t" \
  "v_mfma_f32_16x16x32_bf16 v[248:251], v[68:71], v[72:75], v[248:251]\n\t" \
  "v_mfma_f32_16x16x32_bf16 v[248:251], v[76:79], v[140:143], v[248:251]\n\t" \
  "s_waitcnt lgkmcnt(0)\n\t" \
  "v_mfma_f32_16x16x32_bf16 v[252:255], v[68:71], v[80:83], v[252:255]\n\t" \
  "v_mfma_f32_16x16x32_bf16 v[252:255], v[76:79], v[84:87], v[252:255]\n\t" \
  "v_mfma_f32_16x16x32_bf16 v[220:223], v[148:151], v[84:87], v[220:223]\n\t" \
  "v_mfma_f32_16x16x32_bf16 v[220:223], v[108:111], v[80:83], v[220:223]\n\t" \
  "s_barrier\n\t" \
  "v_mfma_f32_16x16x32_bf16 v[228:231], v[104:107], v[80:83], v[228:231]\n\t" \
  "ds_read_b64_tr_b16 v[96:97], v54 offset:26624\n\t" \
  "v_mfma_f32_16x16x32_bf16 v[228:231], v[64:67], v[84:87], v[228:231]\n\t" \
  "v_mov_b32 v50, 163344\n\t" \
  "ds_read_b64_tr_b16 v[98:99], v54 offset:27136\n\t" \
  "v_mfma_f32_16x16x32_bf16 v[236:239], v[116:119], v[84:87], v[236:239]\n\t" \
  "ds_read_b32 v50, v50 offset:24\n\t" \
  "ds_read_b128 v[136:139], v52 offset:10240\n\t" \
  "v_mfma_f32_16x16x32_bf16 v[236:239], v[112:115], v[80:83], v[236:239]\n\t" \
  "ds_read_b64_tr_b16 v[128:129], v54 offset:30720\n\t" \
  "v_mfma_f32_16x16x32_bf16 v[244:247], v[124:127], v[80:83], v[244:247]\n\t" \
  "ds_read_b64_tr_b16 v[130:131], v54 offset:31232\n\t" \
  "v_mfma_f32_16x16x32_bf16 v[244:247], v[120:123], v[84:87], v[244:247]\n\t" \
  "ds_read_b128 v[144:147], v53 offset:10240\n\t" \
  "v_mfma_f32_16x16x32_bf16 v[152:155], v[88:91], v[108:111], v[152:155]\n\t" \
  "ds_read_b128 v[72:75], v53 offset:12288\n\t" \
  "v_mfma_f32_16x16x32_bf16 v[152:155], v[132:135], v[148:151], v[152:155]\n\t" \
  "ds_read_b128 v[140:143], v52 offset:12288\n\t" \
  "v_mfma_f32_16x16x32_bf16 v[160:163], v[132:135], v[64:67], v[160:163]\n\t" \
  "ds_read_b128 v[68:71], v52 offset:14336\n\t" \
  "v_mfma_f32_16x16x32_bf16 v[160:163], v[88:91], v[104:107], v[160:163]\n\t" \
  "ds_read_b128 v[76:79], v53 offset:14336\n\t" \
  "v_mfma_f32_16x16x32_bf16 v[168:171], v[88:91], v[112:115], v[168:171]\n\t" \
  "ds_read_b128 v[80:83], v53 offset:16384\n\t" \
  "v_mfma_f32_16x16x32_bf16 v[168:171], v[132:135], v[116:119], v[168:171]\n\t" \
  "ds_read_b128 v[84:87], v52 offset:16384\n\t" \
  "v_mfma_f32_16x16x32_bf16 v[176:179], v[132:135], v[120:123], v[176:179]\n\t" \
  "s_waitcnt lgkmcnt(10)\n\t" \
  "v_readfirstlane_b32 s88, v50\n\t" \
  "s_mov_b32 s89, 0\n\t" \
  "ds_read_b64_tr_b16 v[132:133], v55 offset:26624\n\t" \
  "v_mfma_f32_16x16x32_bf16 v[176:179], v[88:91], v[124:127], v[176:179]\n\t" \
  "ds_read_b64_tr_b16 v[134:135], v55 offset:27136\n\t" \
  "v_mfma_f32_16x16x32_bf16 v[156:159], v[92:95], v[108:111], v[156:159]\n\t" \
  "s_xor_b64 %24, %24, s[88:89]\n\t" \
  "ds_read_b64_tr_b16 v[88:89], v55 offset:30720\n\t" \
  "v_mfma_f32_16x16x32_bf16 v[156:159], v[100:103], v[148:151], v[156:159]\n\t" \
  "s_xor_b64 %25, %25, s[88:89]\n\t" \
  "ds_read_b64_tr_b16 v[90:91], v55 offset:31232\n\t" \
  "v_mfma_f32_16x16x32_bf16 v[164:167], v[100:103], v[64:67], v[164:167]\n\t" \
  "s_xor_b64 %14, %14, s[88:89]\n\t" \
  "ds_read_b64_tr_b16 v[108:109], v54 offset:34816\n\t" \
  "v_mfma_f32_16x16x32_bf16 v[164:167], v[92:95], v[104:107], v[164:167]\n\t" \
  "s_xor_b64 %19, %19, s[88:89]\n\t" \
  "ds_read_b64_tr_b16 v[110:111], v54 offset:35328\n\t" \
  "v_mfma_f32_16x16x32_bf16 v[172:175], v[92:95], v[112:115], v[172:175]\n\t" \
  "s_xor_b64 %21, %21, s[88:89]\n\t" \
  "ds_read_b64_tr_b16 v[148:149], v54 offset:38912\n\t" \
  "v_mfma_f32_16x16x32_bf16 v[172:175], v[100:103], v[116:119], v[172:175]\n\t" \
  "s_xor_b64 s[92:93], s[92:93], s[88:89]\n\t" \
  "ds_read_b64_tr_b16 v[150:151], v54 offset:39424\n\t" \
  "v_mfma_f32_16x16x32_bf16 v[180:183], v[100:103], v[120:123], v[180:183]\n\t" \
  "ds_read_b128 v[64:67], v52 offset:0\n\t" \
  "v_mfma_f32_16x16x32_bf16 v[180:183], v[92:95], v[124:127], v[180:183]\n\t" \
  "ds_read_b128 v[104:107], v53 offset:0\n\t" \
  "s_waitcnt lgkmcnt(7)\n\t" \
  "v_mfma_f32_16x16x32_bf16 v[184:187], v[96:99], v[136:139], v[184:187]\n\t" \
  "ds_read_b64_tr_b16 v[112:113], v55 offset:34816\n\t" \
  "v_mfma_f32_16x16x32_bf16 v[184:187], v[128:131], v[144:147], v[184:187]\n\t" \
  "ds_read_b64_tr_b16 v[114:115], v55 offset:35328\n\t" \
  "v_mfma_f32_16x16x32_bf16 v[192:195], v[128:131], v[72:75], v[192:195]\n\t" \
  "ds_read_b64_tr_b16 v[116:117], v55 offset:38912\n\t" \
  "v_mfma_f32_16x16x32_bf16 v[192:195], v[96:99], v[140:143], v[192:195]\n\t" \
  "ds_read_b64_tr_b16 v[118:119], v55 offset:39424\n\t" \
  "v_mfma_f32_16x16x32_bf16 v[200:203], v[96:99], v[68:71], v[200:203]\n\t" \
  "ds_read_b64_tr_b16 v[100:101], v54 offset:18432\n\t" \
  "v_mfma_f32_16x16x32_bf16 v[200:203], v[128:131], v[76:79], v[200:203]\n\t" \
  "ds_read_b64_tr_b16 v[102:103], v54 offset:18944\n\t" \
  "v_mfma_f32_16x16x32_bf16 v[208:211], v[128:131], v[80:83], v[208:211]\n\t" \
  "ds_read_b64_tr_b16 v[120:121], v54 offset:22528\n\t" \
  "v_mfma_f32_16x16x32_bf16 v[208:211], v[96:99], v[84:87], v[208:211]\n\t" \
  "ds_read_b64_tr_b16 v[122:123], v54 offset:23040\n\t" \
  "v_mfma_f32_16x16x32_bf16 v[188:191], v[132:135], v[136:139], v[188:191]\n\t" \
  "ds_read_b64_tr_b16 v[92:93], v55 offset:18432\n\t" \
  "s_waitcnt lgkmcnt(7)\n\t" \
  "v_mfma_f32_16x16x32_bf16 v[188:191], v[88:91], v[144:147], v[188:191]\n\t" \
  "ds_read_b64_tr_b16 v[94:95], v55 offset:18944\n\t" \
  "v_mfma_f32_16x16x32_bf16 v[196:199], v[88:91], v[72:75], v[196:199]\n\t" \
  "ds_read_b64_tr_b16 v[124:125], v55 offset:22528\n\t" \
  "v_mfma_f32_16x16x32_bf16 v[196:199], v[132:135], v[140:143], v[196:199]\n\t" \
  "ds_read_b64_tr_b16 v[126:127], v55 offset:23040\n\t" \
  "v_mfma_f32_16x16x32_bf16 v[204:207], v[132:135], v[68:71], v[204:207]\n\t" \
  "v_mfma_f32_16x16x32_bf16 v[204:207], v[88:91], v[76:79], v[204:207]\n\t" \
  "v_mfma_f32_16x16x32_bf16 v[212:215], v[88:91], v[80:83], v[212:215]\n\t" \
  "v_mfma_f32_16x16x32_bf16 v[212:215], v[132:135], v[84:87], v[212:215]\n\t" \
  "v_mfma_f32_16x16x32_bf16 v[216:219], v[136:139], v[108:111], v[216:219]\n\t" \
  "v_mfma_f32_16x16x32_bf16 v[216:219], v[144:147], v[148:151], v[216:219]\n\t" \
  "v_mfma_f32_16x16x32_bf16 v[224:227], v[72:75], v[148:151], v[224:227]\n\t" \
  "v_mfma_f32_16x16x32_bf16 v[224:227], v[140:143], v[108:111], v[224:227]\n\t" \
  "v_mfma_f32_16x16x32_bf16 v[232:235], v[68:71], v[108:111], v[232:235]\n\t" \
  "global_store_dwordx4 v2, v[42:45], %24 offset:0 sc1\n\t" \
  "v_mfma_f32_16x16x32_bf16 v[232:235], v[76:79], v[148:151], v[232:235]\n\t" \
  "s_nop 0\n\t" \
  "v_mfma_f32_16x16x32_bf16 v[42:45], v[26:29], v[18:21], 0\n\t" \
  "v_mfma_f32_16x16x32_bf16 v[42:45], v[30:33], v[22:25], v[42:45]\n\t" \
  "v_mfma_f32_16x16x32_bf16 v[46:49], v[30:33], v[18:21], 0\n\t" \
  "v_mfma_f32_16x16x32_bf16 v[46:49], v[34:37], v[22:25], v[46:49]\n\t" \
  "v_bfe_u32 v30, v4, 9, 1\n\t" \
  "v_mad_i32_i24 v30, v30, -16, v0\n\t" \
  "v_mfma_f32_16x16x32_bf16 v[240:243], v[80:83], v[148:151], v[240:243]\n\t" \
  "ds_read_b128 v[26:29], v0 offset:1584\n\t" \
  "ds_read_b128 v[30:33], v30 offset:1072\n\t" \
  "v_mfma_f32_16x16x32_bf16 v[240:243], v[84:87], v[108:111], v[240:243]\n\t" \
  "ds_read_b128 v[34:37], v0 offset:544\n\t" \
  "v_perm_b32 v18, v12, v12, v14\n\t" \
  "v_mfma_f32_16x16x32_bf16 v[248:251], v[64:67], v[108:111], v[248:251]\n\t" \
  "v_perm_b32 v19, v12, v12, v15\n\t" \
  "v_perm_b32 v20, v12, v12, v16\n\t" \
  "v_mfma_f32_16x16x32_bf16 v[248:251], v[104:107], v[148:151], v[248:251]\n\t" \
  "v_perm_b32 v21, v12, v12, v17\n\t" \
  "v_perm_b32 v22, v13, v13, v14\n\t" \
  "v_mfma_f32_16x16x32_bf16 v[252:255], v[64:67], v[112:115], v[252:255]\n\t" \
  "v_perm_b32 v23, v13, v13, v15\n\t" \
  "v_perm_b32 v24, v13, v13, v16\n\t" \
  "s_waitcnt lgkmcnt(3)\n\t" \
  "v_mfma_f32_16x16x32_bf16 v[252:255], v[104:107], v[116:119], v[252:255]\n\t" \
  "v_perm_b32 v25, v13, v13, v17\n\t" \
  "v_lshlrev_b32 v50, 16, v38\n\t" \
  "v_mfma_f32_16x16x32_bf16 v[220:223], v[144:147], v[116:119], v[220:223]\n\t" \
  "v_lshlrev_b32 v51, 16, v40\n\t" \
  "v_mul_f32 v42, v42, v50\n\t" \
  "v_mfma_f32_16x16x32_bf16 v[220:223], v[136:139], v[112:115], v[220:223]\n\t" \
  "v_mul_f32 v46, v46, v51\n\t" \
  "v_cvt_pk_bf16_f32 v42, v42, v46\n\t" \
  "s_waitcnt lgkmcnt(0)\n\t" \
  "s_barrier\n\t" \
  "v_mfma_f32_16x16x32_bf16 v[228:231], v[140:143], v[112:115], v[228:231]\n\t" \
  "ds_read_b64_tr_b16 v[128:129], v58 offset:26624\n\t" \
  "v_and_b32 v50, 0xffff0000, v38\n\t" \
  "v_mfma_f32_16x16x32_bf16 v[228:231], v[72:75], v[116:119], v[228:231]\n\t" \
  "ds_read_b64_tr_b16 v[130:131], v58 offset:27136\n\t" \
  "v_and_b32 v51, 0xffff0000, v40\n\t" \
  "v_mfma_f32_16x16x32_bf16 v[236:239], v[76:79], v[116:119], v[236:239]\n\t" \
  "ds_read_b128 v[96:99], v56 offset:10240\n\t" \
  "v_mul_f32 v43, v43, v50\n\t" \
  "v_mfma_f32_16x16x32_bf16 v[236:239], v[68:71], v[112:115], v[236:239]\n\t" \
  "ds_read_b64_tr_b16 v[88:89], v58 offset:30720\n\t" \
  "500:\n\t" \
  "s_cbranch_execz 504f\n\t" \
  "v_mul_f32 v47, v47, v51\n\t" \
  "v_mfma_f32_16x16x32_bf16 v[244:247], v[84:87], v[112:115], v[244:247]\n\t" \
  "ds_read_b64_tr_b16 v[90:91], v58 offset:31232\n\t" \
  "v_cvt_pk_bf16_f32 v43, v43, v47\n\t" \
  "v_mfma_f32_16x16x32_bf16 v[244:247], v[80:83], v[116:119], v[244:247]\n\t" \
  "ds_read_b128 v[132:135], v57 offset:10240\n\t" \
  "v_lshlrev_b32 v50, 16, v39\n\t" \
  "v_mfma_f32_16x16x32_bf16 v[152:155], v[100:103], v[136:139], v[152:155]\n\t" \
  "ds_read_b128 v[108:111], v57 offset:12288\n\t" \
  "v_lshlrev_b32 v51, 16, v41\n\t" \
  "v_mfma_f32_16x16x32_bf16 v[152:155], v[120:123], v[144:147], v[152:155]\n\t" \
  "ds_read_b128 v[148:151], v56 offset:12288\n\t" \
  "v_mul_f32 v44, v44, v50\n\t" \
  "v_mfma_f32_16x16x32_bf16 v[160:163], v[120:123], v[72:75], v[160:163]\n\t" \
  "ds_read_b128 v[64:67], v56 offset:14336\n\t" \
  "v_mul_f32 v48, v48, v51\n\t" \
  "v_mfma_f32_16x16x32_bf16 v[160:163], v[100:103], v[140:143], v[160:163]\n\t" \
  "ds_read_b128 v[104:107], v57 offset:14336\n\t" \
  "v_cvt_pk_bf16_f32 v44, v44, v48\n\t" \
  "v_mfma_f32_16x16x32_bf16 v[168:171], v[100:103], v[68:71], v[168:171]\n\t" \
  "ds_read_b128 v[112:115], v57 offset:16384\n\t" \
  "v_and_b32 v50, 0xffff0000, v39\n\t" \
  "v_mfma_f32_16x16x32_bf16 v[168:171], v[120:123], v[76:79], v[168:171]\n\t" \
  "ds_read_b128 v[116:119], v56 offset:16384\n\t" \
  "v_and_b32 v51, 0xffff0000, v41\n\t" \
  "v_mfma_f32_16x16x32_bf16 v[176:179], v[120:123], v[80:83], v[176:179]\n\t" \
  "ds_read_b64_tr_b16 v[120:121], v59 offset:26624\n\t" \
  "v_mul_f32 v45, v45, v50\n\t" \
  "v_mfma_f32_16x16x32_bf16 v[176:179], v[100:103], v[84:87], v[176:179]\n\t" \
  "ds_read_b64_tr_b16 v[122:123], v59 offset:27136\n\t" \
  "v_mul_f32 v49, v49, v51\n\t" \
  "v_mfma_f32_16x16x32_bf16 v[156:159], v[92:95], v[136:139], v[156:159]\n\t" \
  "ds_read_b64_tr_b16 v[100:101], v59 offset:30720\n\t" \
  "ds_read_b64_tr_b16 v[38:39], v1 offset:544\n\t" \
  "v_mfma_f32_16x16x32_bf16 v[156:159], v[124:127], v[144:147], v[156:159]\n\t" \
  "ds_read_b64_tr_b16 v[102:103], v59 offset:31232\n\t" \
  "ds_read_b64_tr_b16 v[40:41], v1 offset:32\n\t" \
  "v_mfma_f32_16x16x32_bf16 v[164:167], v[124:127], v[72:75], v[164:167]\n\t" \
  "ds_read_b64_tr_b16 v[136:137], v58 offset:34816\n\t" \
  "v_cvt_pk_bf16_f32 v45, v45, v49\n\t" \
  "v_mfma_f32_16x16x32_bf16 v[164:167], v[92:95], v[140:143], v[164:167]\n\t" \
  "ds_read_b64_tr_b16 v[138:139], v58 offset:35328\n\t" \
  "v_mfma_f32_16x16x32_bf16 v[172:175], v[92:95], v[68:71], v[172:175]\n\t" \
  "ds_read_b64_tr_b16 v[144:145], v58 offset:38912\n\t" \
  "v_mfma_f32_16x16x32_bf16 v[172:175], v[124:127], v[76:79], v[172:175]\n\t" \
  "ds_read_b64_tr_b16 v[146:147], v58 offset:39424\n\t" \
  "v_mfma_f32_16x16x32_bf16 v[180:183], v[124:127], v[80:83], v[180:183]\n\t" \
  "ds_read_b128 v[72:75], v56 offset:0\n\t" \
  "v_mfma_f32_16x16x32_bf16 v[180:183], v[92:95], v[84:87], v[180:183]\n\t" \
  "ds_read_b128 v[140:143], v57 offset:0\n\t" \
  "s_waitcnt lgkmcnt(8)\n\t" \
  "v_mfma_f32_16x16x32_bf16 v[184:187], v[128:131], v[96:99], v[184:187]\n\t" \
  "ds_read_b64_tr_b16 v[68:69], v59 offset:34816\n\t" \
  "v_mfma_f32_16x16x32_bf16 v[184:187], v[88:91], v[132:135], v[184:187]\n\t" \
  "ds_read_b64_tr_b16 v[70:71], v59 offset:35328\n\t" \
  "v_mfma_f32_16x16x32_bf16 v[192:195], v[88:91], v[108:111], v[192:195]\n\t" \
  "ds_read_b64_tr_b16 v[76:77], v59 offset:38912\n\t" \
  "v_mfma_f32_16x16x32_bf16 v[192:195], v[128:131], v[148:151], v[192:195]\n\t" \
  "ds_read_b64_tr_b16 v[78:79], v59 offset:39424\n\t" \
  "v_mfma_f32_16x16x32_bf16 v[200:203], v[128:131], v[64:67], v[200:203]\n\t" \
  "ds_read_b64_tr_b16 v[80:81], v58 offset:18432\n\t" \
  "v_mfma_f32_16x16x32_bf16 v[200:203], v[88:91], v[104:107], v[200:203]\n\t" \
  "ds_read_b64_tr_b16 v[82:83], v58 offset:18944\n\t" \
  "v_mfma_f32_16x16x32_bf16 v[208:211], v[88:91], v[112:115], v[208:211]\n\t" \
  "ds_read_b64_tr_b16 v[124:125], v58 offset:22528\n\t" \
  "v_mfma_f32_16x16x32_bf16 v[208:211], v[128:131], v[116:119], v[208:211]\n\t" \
  "ds_read_b64_tr_b16 v[126:127], v58 offset:23040\n\t" \
  "v_mfma_f32_16x16x32_bf16 v[188:191], v[120:123], v[96:99], v[188:191]\n\t" \
  "ds_read_b64_tr_b16 v[84:85], v59 offset:18432\n\t" \
  "s_waitcnt lgkmcnt(7)\n\t" \
  "v_mfma_f32_16x16x32_bf16 v[188:191], v[100:103], v[132:135], v[188:191]\n\t" \
  "ds_read_b64_tr_b16 v[86:87], v59 offset:18944\n\t" \
  "v_mfma_f32_16x16x32_bf16 v[196:199], v[100:103], v[108:111], v[196:199]\n\t" \
  "ds_read_b64_tr_b16 v[92:93], v59 offset:22528\n\t" \
  "v_mfma_f32_16x16x32_bf16 v[196:199], v[120:123], v[148:151], v[196:199]\n\t" \
  "ds_read_b64_tr_b16 v[94:95], v59 offset:23040\n\t" \
  "v_mfma_f32_16x16x32_bf16 v[204:207], v[120:123], v[64:67], v[204:207]\n\t" \
  "v_mfma_f32_16x16x32_bf16 v[204:207], v[100:103], v[104:107], v[204:207]\n\t" \
  "v_mfma_f32_16x16x32_bf16 v[212:215], v[100:103], v[112:115], v[212:215]\n\t" \
  "v_mfma_f32_16x16x32_bf16 v[212:215], v[120:123], v[116:119], v[212:215]\n\t" \
  "v_mfma_f32_16x16x32_bf16 v[216:219], v[96:99], v[136:139], v[216:219]\n\t" \
  "v_mfma_f32_16x16x32_bf16 v[216:219], v[132:135], v[144:147], v[216:219]\n\t" \
  "v_mfma_f32_16x16x32_bf16 v[224:227], v[108:111], v[144:147], v[224:227]\n\t" \
  "v_mfma_f32_16x16x32_bf16 v[224:227], v[148:151], v[136:139], v[224:227]\n\t" \
  "v_mfma_f32_16x16x32_bf16 v[232:235], v[64:67], v[136:139], v[232:235]\n\t" \
  "global_store_dwordx4 v2, v[42:45], %24 offset:1024 sc1\n\t" \
  "v_add_u32 v2, 0x20000, v2\n\t" \
  "v_mfma_f32_16x16x32_bf16 v[232:235], v[104:107], v[144:147], v[232:235]\n\t" \
  "v_mfma_f32_16x16x32_bf16 v[240:243], v[112:115], v[144:147], v[240:243]\n\t" \
  "v_mfma_f32_16x16x32_bf16 v[42:45], v[26:29], v[18:21], 0\n\t" \
  "v_mfma_f32_16x16x32_bf16 v[42:45], v[30:33], v[22:25], v[42:45]\n\t" \
  "v_mfma_f32_16x16x32_bf16 v[46:49], v[30:33], v[18:21], 0\n\t" \
  "v_mfma_f32_16x16x32_bf16 v[46:49], v[34:37], v[22:25], v[46:49]\n\t" \
  "ds_read_b128 v[26:29], v0 offset:1840\n\t" \
  "ds_read_b128 v[30:33], v0 offset:1328\n\t" \
  "v_mfma_f32_16x16x32_bf16 v[240:243], v[116:119], v[136:139], v[240:243]\n\t" \
  "ds_read_b128 v[34:37], v0 offset:800\n\t" \
  "v_mfma_f32_16x16x32_bf16 v[248:251], v[72:75], v[136:139], v[248:251]\n\t" \
  "v_mfma_f32_16x16x32_bf16 v[248:251], v[140:143], v[144:147], v[248:251]\n\t" \
  "v_lshlrev_b32 v50, 16, v38\n\t" \
  "v_lshlrev_b32 v51, 16, v40\n\t" \
  "v_mfma_f32_16x16x32_bf16 v[252:255], v[72:75], v[68:71], v[252:255]\n\t" \
  "v_mul_f32 v42, v42, v50\n\t" \
  "v_mul_f32 v46, v46, v51\n\t" \
  "s_waitcnt lgkmcnt(3)\n\t" \
  "v_mfma_f32_16x16x32_bf16 v[252:255], v[140:143], v[76:79], v[252:255]\n\t" \
  "v_cvt_pk_bf16_f32 v42, v42, v46\n\t" \
  "v_and_b32 v50, 0xffff0000, v38\n\t" \
  "v_mfma_f32_16x16x32_bf16 v[220:223], v[132:135], v[76:79], v[220:223]\n\t" \
  "v_and_b32 v51, 0xffff0000, v40\n\t" \
  "v_mul_f32 v43, v43, v50\n\t" \
  "v_mfma_f32_16x16x32_bf16 v[220:223], v[96:99], v[68:71], v[220:223]\n\t" \
  "v_mul_f32 v47, v47, v51\n\t" \
  "v_cvt_pk_bf16_f32 v43, v43, v47\n\t" \
  "s_waitcnt lgkmcnt(0)\n\t" \
  "s_barrier\n\t" \
  "v_mfma_f32_16x16x32_bf16 v[228:231], v[148:151], v[68:71], v[228:231]\n\t" \
  "ds_read_b64_tr_b16 v[88:89], v62 offset:26624\n\t" \
  "v_lshlrev_b32 v50, 16, v39\n\t" \
  "v_mfma_f32_16x16x32_bf16 v[228:231], v[108:111], v[76:79], v[228:231]\n\t" \
  "ds_read_b64_tr_b16 v[90:91], v62 offset:27136\n\t" \
  "v_lshlrev_b32 v51, 16, v41\n\t" \
  "v_mfma_f32_16x16x32_bf16 v[236:239], v[104:107], v[76:79], v[236:239]\n\t" \
  "ds_read_b128 v[128:131], v60 offset:10240\n\t" \
  "v_mul_f32 v44, v44, v50\n\t" \
  "v_mfma_f32_16x16x32_bf16 v[236:239], v[64:67], v[68:71], v[236:239]\n\t" \
  "ds_read_b64_tr_b16 v[100:101], v62 offset:30720\n\t" \
  "v_mul_f32 v48, v48, v51\n\t" \
  "v_mfma_f32_16x16x32_bf16 v[244:247], v[116:119], v[68:71], v[244:247]\n\t" \
  "ds_read_b64_tr_b16 v[102:103], v62 offset:31232\n\t" \
  "v_cvt_pk_bf16_f32 v44, v44, v48\n\t" \
  "v_mfma_f32_16x16x32_bf16 v[244:247], v[112:115], v[76:79], v[244:247]\n\t" \
  "ds_read_b128 v[120:123], v61 offset:10240\n\t" \
  "v_and_b32 v50, 0xffff0000, v39\n\t" \
  "v_mfma_f32_16x16x32_bf16 v[152:155], v[80:83], v[96:99], v[152:155]\n\t" \
  "ds_read_b128 v[136:139], v61 offset:12288\n\t" \
  "v_and_b32 v51, 0xffff0000, v41\n\t" \
  "v_mfma_f32_16x16x32_bf16 v[152:155], v[124:127], v[132:135], v[152:155]\n\t" \
  "ds_read_b128 v[144:147], v60 offset:12288\n\t" \
  "v_mul_f32 v45, v45, v50\n\t" \
  "v_mfma_f32_16x16x32_bf16 v[160:163], v[124:127], v[108:111], v[160:163]\n\t" \
  "ds_read_b128 v[72:75], v60 offset:14336\n\t" \
  "v_mul_f32 v49, v49, v51\n\t" \
  "v_mfma_f32_16x16x32_bf16 v[160:163], v[80:83], v[148:151], v[160:163]\n\t" \
  "ds_read_b128 v[140:143], v61 offset:14336\n\t" \
  "ds_read_b64_tr_b16 v[38:39], v1 offset:800\n\t" \
  "v_mfma_f32_16x16x32_bf16 v[168:171], v[80:83], v[64:67], v[168:171]\n\t" \
  "ds_read_b128 v[68:71], v61 offset:16384\n\t" \
  "ds_read_b64_tr_b16 v[40:41], v1 offset:288\n\t" \
  "v_mfma_f32_16x16x32_bf16 v[168:171], v[124:127], v[104:107], v[168:171]\n\t" \
  "ds_read_b128 v[76:79], v60 offset:16384\n\t" \
  "v_cvt_pk_bf16_f32 v45, v45, v49\n\t" \
  "v_mfma_f32_16x16x32_bf16 v[176:179], v[124:127], v[112:115], v[176:179]\n\t" \
  "ds_read_b64_tr_b16 v[124:125], v63 offset:26624\n\t" \
  "v_mfma_f32_16x16x32_bf16 v[176:179], v[80:83], v[116:119], v[176:179]\n\t" \
  "ds_read_b64_tr_b16 v[126:127], v63 offset:27136\n\t" \
  "v_mfma_f32_16x16x32_bf16 v[156:159], v[84:87], v[96:99], v[156:159]\n\t" \
  "ds_read_b64_tr_b16 v[80:81], v63 offset:30720\n\t" \
  "v_mfma_f32_16x16x32_bf16 v[156:159], v[92:95], v[132:135], v[156:159]\n\t" \
  "ds_read_b64_tr_b16 v[82:83], v63 offset:31232\n\t" \
  "v_mfma_f32_16x16x32_bf16 v[164:167], v[92:95], v[108:111], v[164:167]\n\t" \
  "ds_read_b64_tr_b16 v[96:97], v62 offset:34816\n\t" \
  "v_mfma_f32_16x16x32_bf16 v[164:167], v[84:87], v[148:151], v[164:167]\n\t" \
  "ds_read_b64_tr_b16 v[98:99], v62 offset:35328\n\t" \
  "v_mfma_f32_16x16x32_bf16 v[172:175], v[84:87], v[64:67], v[172:175]\n\t" \
  "ds_read_b64_tr_b16 v[132:133], v62 offset:38912\n\t" \
  "v_mfma_f32_16x16x32_bf16 v[172:175], v[92:95], v[104:107], v[172:175]\n\t" \
  "ds_read_b64_tr_b16 v[134:135], v62 offset:39424\n\t" \
  "v_mfma_f32_16x16x32_bf16 v[180:183], v[92:95], v[112:115], v[180:183]\n\t" \
  "ds_read_b128 v[108:111], v60 offset:0\n\t" \
  "v_mfma_f32_16x16x32_bf16 v[180:183], v[84:87], v[116:119], v[180:183]\n\t" \
  "ds_read_b128 v[148:151], v61 offset:0\n\t" \
  "s_waitcnt lgkmcnt(7)\n\t" \
  "v_mfma_f32_16x16x32_bf16 v[184:187], v[88:91], v[128:131], v[184:187]\n\t" \
  "ds_read_b64_tr_b16 v[64:65], v63 offset:34816\n\t" \
  "v_mfma_f32_16x16x32_bf16 v[184:187], v[100:103], v[120:123], v[184:187]\n\t" \
  "ds_read_b64_tr_b16 v[66:67], v63 offset:35328\n\t" \
  "v_mfma_f32_16x16x32_bf16 v[192:195], v[100:103], v[136:139], v[192:195]\n\t" \
  "ds_read_b64_tr_b16 v[104:105], v63 offset:38912\n\t" \
  "v_mfma_f32_16x16x32_bf16 v[192:195], v[88:91], v[144:147], v[192:195]\n\t" \
  "ds_read_b64_tr_b16 v[106:107], v63 offset:39424\n\t" \
  "v_mfma_f32_16x16x32_bf16 v[200:203], v[88:91], v[72:75], v[200:203]\n\t" \
  "ds_read_b64_tr_b16 v[92:93], v62 offset:18432\n\t" \
  "v_mfma_f32_16x16x32_bf16 v[200:203], v[100:103], v[140:143], v[200:203]\n\t" \
  "ds_read_b64_tr_b16 v[94:95], v62 offset:18944\n\t" \
  "v_mfma_f32_16x16x32_bf16 v[208:211], v[100:103], v[68:71], v[208:211]\n\t" \
  "ds_read_b64_tr_b16 v[112:113], v62 offset:22528\n\t" \
  "v_mfma_f32_16x16x32_bf16 v[208:211], v[88:91], v[76:79], v[208:211]\n\t" \
  "ds_read_b64_tr_b16 v[114:115], v62 offset:23040\n\t" \
  "v_mfma_f32_16x16x32_bf16 v[188:191], v[124:127], v[128:131], v[188:191]\n\t" \
  "ds_read_b64_tr_b16 v[84:85], v63 offset:18432\n\t" \
  "s_waitcnt lgkmcnt(7)\n\t" \
  "v_mfma_f32_16x16x32_bf16 v[188:191], v[80:83], v[120:123], v[188:191]\n\t" \
  "ds_read_b64_tr_b16 v[86:87], v63 offset:18944\n\t" \
  "v_mfma_f32_16x16x32_bf16 v[196:199], v[80:83], v[136:139], v[196:199]\n\t" \
  "ds_read_b64_tr_b16 v[116:117], v63 offset:22528\n\t" \
  "v_mfma_f32_16x16x32_bf16 v[196:199], v[124:127], v[144:147], v[196:199]\n\t" \
  "ds_read_b64_tr_b16 v[118:119], v63 offset:23040\n\t" \
  "v_mfma_f32_16x16x32_bf16 v[204:207], v[124:127], v[72:75], v[204:207]\n\t" \
  "v_mfma_f32_16x16x32_bf16 v[204:207], v[80:83], v[140:143], v[204:207]\n\t" \
  "v_mfma_f32_16x16x32_bf16 v[212:215], v[80:83], v[68:71], v[212:215]\n\t" \
  "v_mfma_f32_16x16x32_bf16 v[212:215], v[124:127], v[76:79], v[212:215]\n\t" \
  "v_mfma_f32_16x16x32_bf16 v[216:219], v[128:131], v[96:99], v[216:219]\n\t" \
  "v_mfma_f32_16x16x32_bf16 v[216:219], v[120:123], v[132:135], v[216:219]\n\t" \
  "v_mfma_f32_16x16x32_bf16 v[224:227], v[136:139], v[132:135], v[224:227]\n\t" \
  "v_mfma_f32_16x16x32_bf16 v[224:227], v[144:147], v[96:99], v[224:227]\n\t" \
  "v_mfma_f32_16x16x32_bf16 v[232:235], v[72:75], v[96:99], v[232:235]\n\t" \
  "global_store_dwordx4 v2, v[42:45], %24 offset:0 sc1\n\t" \
  "v_mfma_f32_16x16x32_bf16 v[232:235], v[140:143], v[132:135], v[232:235]\n\t" \
  "s_nop 0\n\t" \
  "v_mfma_f32_16x16x32_bf16 v[42:45], v[26:29], v[18:21], 0\n\t" \
  "v_mfma_f32_16x16x32_bf16 v[42:45], v[30:33], v[22:25], v[42:45]\n\t" \
  "v_mfma_f32_16x16x32_bf16 v[46:49], v[30:33], v[18:21], 0\n\t" \
  "v_mfma_f32_16x16x32_bf16 v[46:49], v[34:37], v[22:25], v[46:49]\n\t" \
  "v_mfma_f32_16x16x32_bf16 v[240:243], v[68:71], v[132:135], v[240:243]\n\t" \
  "v_mfma_f32_16x16x32_bf16 v[240:243], v[76:79], v[96:99], v[240:243]\n\t" \
  "v_lshlrev_b32 v50, 16, v38\n\t" \
  "v_lshlrev_b32 v51, 16, v40\n\t" \
  "v_mfma_f32_16x16x32_bf16 v[248:251], v[108:111], v[96:99], v[248:251]\n\t" \
  "s_nop 0\n\t" \
  "v_mul_f32 v42, v42, v50\n\t" \
  "s_nop 0\n\t" \
  "v_mul_f32 v46, v46, v51\n\t" \
  "v_mfma_f32_16x16x32_bf16 v[248:251], v[148:151], v[132:135], v[248:251]\n\t" \
  "v_cvt_pk_bf16_f32 v42, v42, v46\n\t" \
  "v_and_b32 v50, 0xffff0000, v38\n\t" \
  "v_mfma_f32_16x16x32_bf16 v[252:255], v[108:111], v[64:67], v[252:255]\n\t" \
  "v_and_b32 v51, 0xffff0000, v40\n\t" \
  "v_mul_f32 v43, v43, v50\n\t" \
  "s_waitcnt lgkmcnt(0)\n\t" \
  "v_mfma_f32_16x16x32_bf16 v[252:255], v[148:151], v[104:107], v[252:255]\n\t" \
  "v_mul_f32 v47, v47, v51\n\t" \
  "v_cvt_pk_bf16_f32 v43, v43, v47\n\t" \
  "v_mfma_f32_16x16x32_bf16 v[220:223], v[120:123], v[104:107], v[220:223]\n\t" \
  "v_lshlrev_b32 v50, 16, v39\n\t" \
  "v_lshlrev_b32 v51, 16, v41\n\t" \
  "v_mfma_f32_16x16x32_bf16 v[220:223], v[128:131], v[64:67], v[220:223]\n\t" \
  "v_mul_f32 v44, v44, v50\n\t" \
  "v_mul_f32 v48, v48, v51\n\t" \
  "s_barrier\n\t" \
  "v_mfma_f32_16x16x32_bf16 v[228:231], v[144:147], v[64:67], v[228:231]\n\t" \
  "ds_read_b64_tr_b16 v[100:101], v54 offset:26624\n\t" \
  "v_cvt_pk_bf16_f32 v44, v44, v48\n\t" \
  "v_mfma_f32_16x16x32_bf16 v[228:231], v[136:139], v[104:107], v[228:231]\n\t" \
  "ds_read_b64_tr_b16 v[102:103], v54 offset:27136\n\t" \
  "v_and_b32 v50, 0xffff0000, v39\n\t" \
  "v_mfma_f32_16x16x32_bf16 v[236:239], v[140:143], v[104:107], v[236:239]\n\t" \
  "ds_read_b128 v[88:91], v52 offset:10240\n\t" \
  "v_and_b32 v51, 0xffff0000, v41\n\t" \
  "v_mfma_f32_16x16x32_bf16 v[236:239], v[72:75], v[64:67], v[236:239]\n\t" \
  "ds_read_b64_tr_b16 v[80:81], v54 offset:30720\n\t" \
  "v_mul_f32 v45, v45, v50\n\t" \
  "v_mfma_f32_16x16x32_bf16 v[244:247], v[76:79], v[64:67], v[244:247]\n\t" \
  "ds_read_b64_tr_b16 v[82:83], v54 offset:31232\n\t" \
  "v_mul_f32 v49, v49, v51\n\t" \
  "v_mfma_f32_16x16x32_bf16 v[244:247], v[68:71], v[104:107], v[244:247]\n\t" \
  "ds_read_b128 v[124:127], v53 offset:10240\n\t" \
  "v_cvt_pk_bf16_f32 v45, v45, v49\n\t" \
  "v_mfma_f32_16x16x32_bf16 v[152:155], v[92:95], v[128:131], v[152:155]\n\t" \
  "ds_read_b128 v[96:99], v53 offset:12288\n\t" \
  "v_mfma_f32_16x16x32_bf16 v[152:155], v[112:115], v[120:123], v[152:155]\n\t" \
  "ds_read_b128 v[132:135], v52 offset:12288\n\t" \
  "v_mfma_f32_16x16x32_bf16 v[160:163], v[112:115], v[136:139], v[160:163]\n\t" \
  "ds_read_b128 v[108:111], v52 offset:14336\n\t" \
  "v_mfma_f32_16x16x32_bf16 v[160:163], v[92:95], v[144:147], v[160:163]\n\t" \
  "ds_read_b128 v[148:151], v53 offset:14336\n\t" \
  "v_mfma_f32_16x16x32_bf16 v[168:171], v[92:95], v[72:75], v[168:171]\n\t" \
  "ds_read_b128 v[64:67], v53 offset:16384\n\t" \
  "v_mfma_f32_16x16x32_bf16 v[168:171], v[112:115], v[140:143], v[168:171]\n\t" \
  "ds_read_b128 v[104:107], v52 offset:16384\n\t" \
  "v_mfma_f32_16x16x32_bf16 v[176:179], v[112:115], v[68:71], v[176:179]\n\t" \
  "ds_read_b64_tr_b16 v[112:113], v55 offset:26624\n\t" \
  "v_mfma_f32_16x16x32_bf16 v[176:179], v[92:95], v[76:79], v[176:179]\n\t" \
  "ds_read_b64_tr_b16 v[114:115], v55 offset:27136\n\t" \
  "v_mfma_f32_16x16x32_bf16 v[156:159], v[84:87], v[128:131], v[156:159]\n\t" \
  "ds_read_b64_tr_b16 v[92:93], v55 offset:30720\n\t" \
  "v_mfma_f32_16x16x32_bf16 v[156:159], v[116:119], v[120:123], v[156:159]\n\t" \
  "ds_read_b64_tr_b16 v[94:95], v55 offset:31232\n\t" \
  "v_mfma_f32_16x16x32_bf16 v[164:167], v[116:119], v[136:139], v[164:167]\n\t" \
  "ds_read_b64_tr_b16 v[128:129], v54 offset:34816\n\t" \
  "v_mfma_f32_16x16x32_bf16 v[164:167], v[84:87], v[144:147], v[164:167]\n\t" \
  "ds_read_b64_tr_b16 v[130:131], v54 offset:35328\n\t" \
  "v_mfma_f32_16x16x32_bf16 v[172:175], v[84:87], v[72:75], v[172:175]\n\t" \
  "ds_read_b64_tr_b16 v[120:121], v54 offset:38912\n\t" \
  "v_mfma_f32_16x16x32_bf16 v[172:175], v[116:119], v[140:143], v[172:175]\n\t" \
  "ds_read_b64_tr_b16 v[122:123], v54 offset:39424\n\t" \
  "v_mfma_f32_16x16x32_bf16 v[180:183], v[116:119], v[68:71], v[180:183]\n\t" \
  "ds_read_b128 v[136:139], v52 offset:0\n\t" \
  "v_mfma_f32_16x16x32_bf16 v[180:183], v[84:87], v[76:79], v[180:183]\n\t" \
  "ds_read_b128 v[144:147], v53 offset:0\n\t" \
  "s_waitcnt lgkmcnt(7)\n\t" \
  "v_mfma_f32_16x16x32_bf16 v[184:187], v[100:103], v[88:91], v[184:187]\n\t" \
  "ds_read_b64_tr_b16 v[72:73], v55 offset:34816\n\t" \
  "v_mfma_f32_16x16x32_bf16 v[184:187], v[80:83], v[124:127], v[184:187]\n\t" \
  "ds_read_b64_tr_b16 v[74:75], v55 offset:35328\n\t" \
  "v_mfma_f32_16x16x32_bf16 v[192:195], v[80:83], v[96:99], v[192:195]\n\t" \
  "ds_read_b64_tr_b16 v[140:141], v55 offset:38912\n\t" \
  "v_mfma_f32_16x16x32_bf16 v[192:195], v[100:103], v[132:135], v[192:195]\n\t" \
  "ds_read_b64_tr_b16 v[142:143], v55 offset:39424\n\t" \
  "v_mfma_f32_16x16x32_bf16 v[200:203], v[100:103], v[108:111], v[200:203]\n\t" \
  "ds_read_b64_tr_b16 v[68:69], v54 offset:18432\n\t" \
  "v_mfma_f32_16x16x32_bf16 v[200:203], v[80:83], v[148:151], v[200:203]\n\t" \
  "ds_read_b64_tr_b16 v[70:71], v54 offset:18944\n\t" \
  "v_mfma_f32_16x16x32_bf16 v[208:211], v[80:83], v[64:67], v[208:211]\n\t" \
  "ds_read_b64_tr_b16 v[116:117], v54 offset:22528\n\t" \
  "v_mfma_f32_16x16x32_bf16 v[208:211], v[100:103], v[104:107], v[208:211]\n\t" \
  "ds_read_b64_tr_b16 v[118:119], v54 offset:23040\n\t" \
  "v_mfma_f32_16x16x32_bf16 v[188:191], v[112:115], v[88:91], v[188:191]\n\t" \
  "ds_read_b64_tr_b16 v[76:77], v55 offset:18432\n\t" \
  "s_waitcnt lgkmcnt(7)\n\t" \
  "v_mfma_f32_16x16x32_bf16 v[188:191], v[92:95], v[124:127], v[188:191]\n\t" \
  "ds_read_b64_tr_b16 v[78:79], v55 offset:18944\n\t" \
  "v_mfma_f32_16x16x32_bf16 v[196:199], v[92:95], v[96:99], v[196:199]\n\t" \
  "ds_read_b64_tr_b16 v[84:85], v55 offset:22528\n\t" \
  "v_mfma_f32_16x16x32_bf16 v[196:199], v[112:115], v[132:135], v[196:199]\n\t" \
  "ds_read_b64_tr_b16 v[86:87], v55 offset:23040\n\t" \
  "v_mfma_f32_16x16x32_bf16 v[204:207], v[112:115], v[108:111], v[204:207]\n\t" \
  "v_mfma_f32_16x16x32_bf16 v[204:207], v[92:95], v[148:151], v[204:207]\n\t" \
  "v_mfma_f32_16x16x32_bf16 v[212:215], v[92:95], v[64:67], v[212:215]\n\t" \
  "v_mfma_f32_16x16x32_bf16 v[212:215], v[112:115], v[104:107], v[212:215]\n\t" \
  "v_mfma_f32_16x16x32_bf16 v[216:219], v[88:91], v[128:131], v[216:219]\n\t" \
  "v_mfma_f32_16x16x32_bf16 v[216:219], v[124:127], v[120:123], v[216:219]\n\t" \
  "v_mfma_f32_16x16x32_bf16 v[224:227], v[96:99], v[120:123], v[224:227]\n\t" \
  "v_mfma_f32_16x16x32_bf16 v[224:227], v[132:135], v[128:131], v[224:227]\n\t" \
  "v_mfma_f32_16x16x32_bf16 v[232:235], v[108:111], v[128:131], v[232:235]\n\t" \
  "global_store_dwordx4 v2, v[42:45], %24 offset:1024 sc1\n\t" \
  "v_mfma_f32_16x16x32_bf16 v[232:235], v[148:151], v[120:123], v[232:235]\n\t" \
  "v_mfma_f32_16x16x32_bf16 v[240:243], v[64:67], v[120:123], v[240:243]\n\t" \
  "v_mfma_f32_16x16x32_bf16 v[240:243], v[104:107], v[128:131], v[240:243]\n\t" \
  "v_mov_b32 v30, 0xff61b1e6\n\t" \
  "v_bfe_i32 v34, v3, 0, 1\n\t" \
  "v_mfma_f32_16x16x32_bf16 v[248:251], v[136:139], v[128:131], v[248:251]\n\t" \
  "v_and_b32 v34, v30, v34\n\t" \
  "v_bfe_i32 v38, v3, 4, 1\n\t" \
  "v_mfma_f32_16x16x32_bf16 v[248:251], v[144:147], v[120:123], v[248:251]\n\t" \
  "v_and_b32 v38, v30, v38\n\t" \
  "v_bfe_i32 v42, v3, 8, 1\n\t" \
  "v_mfma_f32_16x16x32_bf16 v[252:255], v[136:139], v[72:75], v[252:255]\n\t" \
  "v_bfi_b32 v42, v42, v30, %15\n\t" \
  "v_mov_b32 v48, 0x3f803f80\n\t" \
  "s_waitcnt lgkmcnt(0)\n\t" \
  "v_mfma_f32_16x16x32_bf16 v[252:255], v[144:147], v[140:143], v[252:255]\n\t" \
  "v_bfe_i32 v35, v3, 1, 1\n\t" \
  "v_and_b32 v35, v30, v35\n\t" \
  "v_mfma_f32_16x16x32_bf16 v[220:223], v[124:127], v[140:143], v[220:223]\n\t" \
  "v_bfe_i32 v39, v3, 5, 1\n\t" \
  "v_and_b32 v39, v30, v39\n\t" \
  "v_mfma_f32_16x16x32_bf16 v[220:223], v[88:91], v[72:75], v[220:223]\n\t" \
  "v_bfe_i32 v43, v3, 9, 1\n\t" \
  "v_bfi_b32 v43, v43, v30, %15\n\t" \
  "s_barrier\n\t" \
  "v_mfma_f32_16x16x32_bf16 v[228:231], v[132:135], v[72:75], v[228:231]\n\t" \
  "ds_read_b64_tr_b16 v[80:81], v58 offset:26624\n\t" \
  "v_mov_b32 v49, 0x3f803f80\n\t" \
  "v_mfma_f32_16x16x32_bf16 v[228:231], v[96:99], v[140:143], v[228:231]\n\t" \
  "ds_read_b64_tr_b16 v[82:83], v58 offset:27136\n\t" \
  "v_bfe_i32 v36, v3, 2, 1\n\t" \
  "v_mfma_f32_16x16x32_bf16 v[236:239], v[148:151], v[140:143], v[236:239]\n\t" \
  "ds_read_b128 v[100:103], v56 offset:10240\n\t" \
  "v_and_b32 v36, v30, v36\n\t" \
  "v_mfma_f32_16x16x32_bf16 v[236:239], v[108:111], v[72:75], v[236:239]\n\t" \
  "ds_read_b64_tr_b16 v[92:93], v58 offset:30720\n\t" \
  "v_bfe_i32 v40, v3, 6, 1\n\t" \
  "v_mfma_f32_16x16x32_bf16 v[244:247], v[104:107], v[72:75], v[244:247]\n\t" \
  "ds_read_b64_tr_b16 v[94:95], v58 offset:31232\n\t" \
  "v_and_b32 v40, v30, v40\n\t" \
  "v_mfma_f32_16x16x32_bf16 v[244:247], v[64:67], v[140:143], v[244:247]\n\t" \
  "ds_read_b128 v[112:115], v57 offset:10240\n\t" \
  "v_bfe_i32 v44, v3, 10, 1\n\t" \
  "v_mfma_f32_16x16x32_bf16 v[152:155], v[68:71], v[88:91], v[152:155]\n\t" \
  "ds_read_b128 v[128:131], v57 offset:12288\n\t" \
  "v_bfi_b32 v44, v44, v30, %15\n\t" \
  "v_mfma_f32_16x16x32_bf16 v[152:155], v[116:119], v[124:127], v[152:155]\n\t" \
  "ds_read_b128 v[120:123], v56 offset:12288\n\t" \
  "v_mov_b32 v50, 0x3f803f80\n\t" \
  "v_mfma_f32_16x16x32_bf16 v[160:163], v[116:119], v[96:99], v[160:163]\n\t" \
  "ds_read_b128 v[136:139], v56 offset:14336\n\t" \
  "v_bfe_i32 v37, v3, 3, 1\n\t" \
  "v_mfma_f32_16x16x32_bf16 v[160:163], v[68:71], v[132:135], v[160:163]\n\t" \
  "ds_read_b128 v[144:147], v57 offset:14336\n\t" \
  "v_and_b32 v37, v30, v37\n\t" \
  "v_mfma_f32_16x16x32_bf16 v[168:171], v[68:71], v[108:111], v[168:171]\n\t" \
  "ds_read_b128 v[72:75], v57 offset:16384\n\t" \
  "v_bfe_i32 v41, v3, 7, 1\n\t" \
  "v_mfma_f32_16x16x32_bf16 v[168:171], v[116:119], v[148:151], v[168:171]\n\t" \
  "ds_read_b128 v[140:143], v56 offset:16384\n\t" \
  "v_and_b32 v41, v30, v41\n\t" \
  "v_mfma_f32_16x16x32_bf16 v[176:179], v[116:119], v[64:67], v[176:179]\n\t" \
  "ds_read_b64_tr_b16 v[116:117], v59 offset:26624\n\t" \
  "v_bfe_i32 v45, v3, 11, 1\n\t" \
  "v_mfma_f32_16x16x32_bf16 v[176:179], v[68:71], v[104:107], v[176:179]\n\t" \
  "ds_read_b64_tr_b16 v[118:119], v59 offset:27136\n\t" \
  "v_bfi_b32 v45, v45, v30, %15\n\t" \
  "v_mfma_f32_16x16x32_bf16 v[156:159], v[76:79], v[88:91], v[156:159]\n\t" \
  "ds_read_b64_tr_b16 v[68:69], v59 offset:30720\n\t" \
  "v_mov_b32 v51, 0x3f803f80\n\t" \
  "v_mfma_f32_16x16x32_bf16 v[156:159], v[84:87], v[124:127], v[156:159]\n\t" \
  "ds_read_b64_tr_b16 v[70:71], v59 offset:31232\n\t" \
  "v_bfe_i32 v46, v3, 12, 1\n\t" \
  "v_mfma_f32_16x16x32_bf16 v[164:167], v[84:87], v[96:99], v[164:167]\n\t" \
  "ds_read_b64_tr_b16 v[88:89], v58 offset:34816\n\t" \
  "v_bfe_i32 v31, v3, 13, 1\n\t" \
  "v_mfma_f32_16x16x32_bf16 v[164:167], v[76:79], v[132:135], v[164:167]\n\t" \
  "ds_read_b64_tr_b16 v[90:91], v58 offset:35328\n\t" \
  "v_perm_b32 v46, v31, v46, vcc_hi\n\t" \
  "v_mfma_f32_16x16x32_bf16 v[172:175], v[76:79], v[108:111], v[172:175]\n\t" \
  "ds_read_b64_tr_b16 v[124:125], v58 offset:38912\n\t" \
  "v_bfe_i32 v47, v3, 14, 1\n\t" \
  "v_mfma_f32_16x16x32_bf16 v[172:175], v[84:87], v[148:151], v[172:175]\n\t" \
  "ds_read_b64_tr_b16 v[126:127], v58 offset:39424\n\t" \
  "v_bfe_i32 v31, v3, 15, 1\n\t" \
  "v_mfma_f32_16x16x32_bf16 v[180:183], v[84:87], v[64:67], v[180:183]\n\t" \
  "ds_read_b128 v[96:99], v56 offset:0\n\t" \
  "v_perm_b32 v47, v31, v47, vcc_hi\n\t" \
  "v_mfma_f32_16x16x32_bf16 v[180:183], v[76:79], v[104:107], v[180:183]\n\t" \
  "ds_read_b128 v[132:135], v57 offset:0\n\t" \
  "v_add_u32 v30, 18432, v4\n\t" \
  "s_waitcnt lgkmcnt(7)\n\t" \
  "v_mfma_f32_16x16x32_bf16 v[184:187], v[80:83], v[100:103], v[184:187]\n\t" \
  "ds_read_b64_tr_b16 v[108:109], v59 offset:34816\n\t" \
  "v_xor_b32 v31, 1024, v30\n\t" \
  "v_mfma_f32_16x16x32_bf16 v[184:187], v[92:95], v[112:115], v[184:187]\n\t" \
  "ds_read_b64_tr_b16 v[110:111], v59 offset:35328\n\t" \
  "v_mov_b32 v32, %12\n\t" \
  "v_mfma_f32_16x16x32_bf16 v[192:195], v[92:95], v[128:131], v[192:195]\n\t" \
  "ds_read_b64_tr_b16 v[148:149], v59 offset:38912\n\t" \
  "v_xor_b32 v33, 4, v32\n\t" \
  "v_mfma_f32_16x16x32_bf16 v[192:195], v[80:83], v[120:123], v[192:195]\n\t" \
  "ds_read_b64_tr_b16 v[150:151], v59 offset:39424\n\t" \
  "v_mov_b32 v0, 1\n\t" \
  "v_mfma_f32_16x16x32_bf16 v[200:203], v[80:83], v[136:139], v[200:203]\n\t" \
  "ds_read_b64_tr_b16 v[64:65], v58 offset:18432\n\t" \
  "v_and_b32 v4, 1023, v4\n\t" \
  "v_mfma_f32_16x16x32_bf16 v[200:203], v[92:95], v[144:147], v[200:203]\n\t" \
  "ds_read_b64_tr_b16 v[66:67], v58 offset:18944\n\t" \
  "v_add_u32 v1, 16384, v4\n\t" \
  "v_mfma_f32_16x16x32_bf16 v[208:211], v[92:95], v[72:75], v[208:211]\n\t" \
  "ds_read_b64_tr_b16 v[84:85], v58 offset:22528\n\t" \
  "v_mfma_f32_16x16x32_bf16 v[208:211], v[80:83], v[140:143], v[208:211]\n\t" \
  "ds_read_b64_tr_b16 v[86:87], v58 offset:23040\n\t" \
  "v_mfma_f32_16x16x32_bf16 v[188:191], v[116:119], v[100:103], v[188:191]\n\t" \
  "ds_read_b64_tr_b16 v[76:77], v59 offset:18432\n\t" \
  "s_waitcnt lgkmcnt(7)\n\t" \
  "v_mfma_f32_16x16x32_bf16 v[188:191], v[68:71], v[112:115], v[188:191]\n\t" \
  "ds_read_b64_tr_b16 v[78:79], v59 offset:18944\n\t" \
  "v_mfma_f32_16x16x32_bf16 v[196:199], v[68:71], v[128:131], v[196:199]\n\t" \
  "ds_read_b64_tr_b16 v[104:105], v59 offset:22528\n\t" \
  "v_mfma_f32_16x16x32_bf16 v[196:199], v[116:119], v[120:123], v[196:199]\n\t" \
  "ds_read_b64_tr_b16 v[106:107], v59 offset:23040\n\t" \
  "v_mfma_f32_16x16x32_bf16 v[204:207], v[116:119], v[136:139], v[204:207]\n\t" \
  "v_mfma_f32_16x16x32_bf16 v[204:207], v[68:71], v[144:147], v[204:207]\n\t" \
  "v_mfma_f32_16x16x32_bf16 v[212:215], v[68:71], v[72:75], v[212:215]\n\t" \
  "v_mfma_f32_16x16x32_bf16 v[212:215], v[116:119], v[140:143], v[212:215]\n\t" \
  "v_mfma_f32_16x16x32_bf16 v[216:219], v[100:103], v[88:91], v[216:219]\n\t" \
  "v_mfma_f32_16x16x32_bf16 v[216:219], v[112:115], v[124:127], v[216:219]\n\t" \
  "v_mfma_f32_16x16x32_bf16 v[224:227], v[128:131], v[124:127], v[224:227]\n\t" \
  "v_mfma_f32_16x16x32_bf16 v[224:227], v[120:123], v[88:91], v[224:227]\n\t" \
  "v_mfma_f32_16x16x32_bf16 v[232:235], v[136:139], v[88:91], v[232:235]\n\t" \
  "v_mfma_f32_16x16x32_bf16 v[232:235], v[144:147], v[124:127], v[232:235]\n\t" \
  "v_mfma_f32_16x16x32_bf16 v[240:243], v[72:75], v[124:127], v[240:243]\n\t" \
  "v_mfma_f32_16x16x32_bf16 v[240:243], v[140:143], v[88:91], v[240:243]\n\t" \
  "v_mfma_f32_16x16x32_bf16 v[248:251], v[96:99], v[88:91], v[248:251]\n\t" \
  "v_mfma_f32_16x16x32_bf16 v[248:251], v[132:135], v[124:127], v[248:251]\n\t" \
  "v_mfma_f32_16x16x32_bf16 v[252:255], v[96:99], v[108:111], v[252:255]\n\t" \
  "s_waitcnt lgkmcnt(0)\n\t" \
  "v_mfma_f32_16x16x32_bf16 v[252:255], v[132:135], v[148:151], v[252:255]\n\t" \
  "v_mfma_f32_16x16x32_bf16 v[220:223], v[112:115], v[148:151], v[220:223]\n\t" \
  "v_mfma_f32_16x16x32_bf16 v[220:223], v[100:103], v[108:111], v[220:223]\n\t" \
  "s_barrier\n\t" \
  "v_mfma_f32_16x16x32_bf16 v[228:231], v[120:123], v[108:111], v[228:231]\n\t" \
  "ds_read_b64_tr_b16 v[92:93], v62 offset:26624\n\t" \
  "v_mfma_f32_16x16x32_bf16 v[228:231], v[128:131], v[148:151], v[228:231]\n\t" \
  "ds_read_b64_tr_b16 v[94:95], v62 offset:27136\n\t" \
  "v_mfma_f32_16x16x32_bf16 v[236:239], v[144:147], v[148:151], v[236:239]\n\t" \
  "ds_read_b128 v[80:83], v60 offset:10240\n\t" \
  "v_mfma_f32_16x16x32_bf16 v[236:239], v[136:139], v[108:111], v[236:239]\n\t" \
  "ds_read_b64_tr_b16 v[68:69], v62 offset:30720\n\t" \
  "v_mfma_f32_16x16x32_bf16 v[244:247], v[140:143], v[108:111], v[244:247]\n\t" \
  "ds_read_b64_tr_b16 v[70:71], v62 offset:31232\n\t" \
  "v_mfma_f32_16x16x32_bf16 v[244:247], v[72:75], v[148:151], v[244:247]\n\t" \
  "ds_read_b128 v[116:119], v61 offset:10240\n\t" \
  "v_mfma_f32_16x16x32_bf16 v[152:155], v[64:67], v[100:103], v[152:155]\n\t" \
  "ds_read_b128 v[88:91], v61 offset:12288\n\t" \
  "v_mfma_f32_16x16x32_bf16 v[152:155], v[84:87], v[112:115], v[152:155]\n\t" \
  "ds_read_b128 v[124:127], v60 offset:12288\n\t" \
  "v_mfma_f32_16x16x32_bf16 v[160:163], v[84:87], v[128:131], v[160:163]\n\t" \
  "ds_read_b128 v[96:99], v60 offset:14336\n\t" \
  "v_mfma_f32_16x16x32_bf16 v[160:163], v[64:67], v[120:123], v[160:163]\n\t" \
  "ds_read_b128 v[132:135], v61 offset:14336\n\t" \
  "v_mfma_f32_16x16x32_bf16 v[168:171], v[64:67], v[136:139], v[168:171]\n\t" \
  "ds_read_b128 v[108:111], v61 offset:16384\n\t" \
  "v_mfma_f32_16x16x32_bf16 v[168:171], v[84:87], v[144:147], v[168:171]\n\t" \
  "ds_read_b128 v[148:151], v60 offset:16384\n\t" \
  "v_mfma_f32_16x16x32_bf16 v[176:179], v[84:87], v[72:75], v[176:179]\n\t" \
  "ds_read_b64_tr_b16 v[84:85], v63 offset:26624\n\t" \
  "v_mfma_f32_16x16x32_bf16 v[176:179], v[64:67], v[140:143], v[176:179]\n\t" \
  "ds_read_b64_tr_b16 v[86:87], v63 offset:27136\n\t" \
  "v_mfma_f32_16x16x32_bf16 v[156:159], v[76:79], v[100:103], v[156:159]\n\t" \
  "ds_read_b64_tr_b16 v[64:65], v63 offset:30720\n\t" \
  "v_mfma_f32_16x16x32_bf16 v[156:159], v[104:107], v[112:115], v[156:159]\n\t" \
  "ds_read_b64_tr_b16 v[66:67], v63 offset:31232\n\t" \
  "v_mfma_f32_16x16x32_bf16 v[164:167], v[104:107], v[128:131], v[164:167]\n\t" \
  "ds_read_b64_tr_b16 v[100:101], v62 offset:34816\n\t" \
  "v_mfma_f32_16x16x32_bf16 v[164:167], v[76:79], v[120:123], v[164:167]\n\t" \
  "ds_read_b64_tr_b16 v[102:103], v62 offset:35328\n\t" \
  "v_mfma_f32_16x16x32_bf16 v[172:175], v[76:79], v[136:139], v[172:175]\n\t" \
  "ds_read_b64_tr_b16 v[112:113], v62 offset:38912\n\t" \
  "v_mfma_f32_16x16x32_bf16 v[172:175], v[104:107], v[144:147], v[172:175]\n\t" \
  "ds_read_b64_tr_b16 v[114:115], v62 offset:39424\n\t" \
  "v_mfma_f32_16x16x32_bf16 v[180:183], v[104:107], v[72:75], v[180:183]\n\t" \
  "ds_read_b128 v[128:131], v60 offset:0\n\t" \
  "v_mfma_f32_16x16x32_bf16 v[180:183], v[76:79], v[140:143], v[180:183]\n\t" \
  "ds_read_b128 v[120:123], v61 offset:0\n\t" \
  "s_waitcnt lgkmcnt(7)\n\t" \
  "v_mfma_f32_16x16x32_bf16 v[184:187], v[92:95], v[80:83], v[184:187]\n\t" \
  "ds_read_b64_tr_b16 v[136:137], v63 offset:34816\n\t" \
  "v_mfma_f32_16x16x32_bf16 v[184:187], v[68:71], v[116:119], v[184:187]\n\t" \
  "ds_read_b64_tr_b16 v[138:139], v63 offset:35328\n\t" \
  "v_mfma_f32_16x16x32_bf16 v[192:195], v[68:71], v[88:91], v[192:195]\n\t" \
  "ds_read_b64_tr_b16 v[144:145], v63 offset:38912\n\t" \
  "v_mfma_f32_16x16x32_bf16 v[192:195], v[92:95], v[124:127], v[192:195]\n\t" \
  "ds_read_b64_tr_b16 v[146:147], v63 offset:39424\n\t" \
  "v_mfma_f32_16x16x32_bf16 v[200:203], v[92:95], v[96:99], v[200:203]\n\t" \
  "ds_read_b64_tr_b16 v[72:73], v62 offset:18432\n\t" \
  "v_mfma_f32_16x16x32_bf16 v[200:203], v[68:71], v[132:135], v[200:203]\n\t" \
  "ds_read_b64_tr_b16 v[74:75], v62 offset:18944\n\t" \
  "v_mfma_f32_16x16x32_bf16 v[208:211], v[68:71], v[108:111], v[208:211]\n\t" \
  "ds_read_b64_tr_b16 v[104:105], v62 offset:22528\n\t" \
  "v_mfma_f32_16x16x32_bf16 v[208:211], v[92:95], v[148:151], v[208:211]\n\t" \
  "ds_read_b64_tr_b16 v[106:107], v62 offset:23040\n\t" \
  "v_mfma_f32_16x16x32_bf16 v[188:191], v[84:87], v[80:83], v[188:191]\n\t" \
  "ds_read_b64_tr_b16 v[76:77], v63 offset:18432\n\t" \
  "s_waitcnt lgkmcnt(7)\n\t" \
  "v_mfma_f32_16x16x32_bf16 v[188:191], v[64:67], v[116:119], v[188:191]\n\t" \
  "ds_read_b64_tr_b16 v[78:79], v63 offset:18944\n\t" \
  "v_mfma_f32_16x16x32_bf16 v[196:199], v[64:67], v[88:91], v[196:199]\n\t" \
  "ds_read_b64_tr_b16 v[140:141], v63 offset:22528\n\t" \
  "v_mfma_f32_16x16x32_bf16 v[196:199], v[84:87], v[124:127], v[196:199]\n\t" \
  "ds_read_b64_tr_b16 v[142:143], v63 offset:23040\n\t" \
  "v_mfma_f32_16x16x32_bf16 v[204:207], v[84:87], v[96:99], v[204:207]\n\t" \
  "v_mfma_f32_16x16x32_bf16 v[204:207], v[64:67], v[132:135], v[204:207]\n\t" \
  "v_mfma_f32_16x16x32_bf16 v[212:215], v[64:67], v[108:111], v[212:215]\n\t" \
  "v_mfma_f32_16x16x32_bf16 v[212:215], v[84:87], v[148:151], v[212:215]\n\t" \
  "v_mfma_f32_16x16x32_bf16 v[216:219], v[80:83], v[100:103], v[216:219]\n\t" \
  "501:\n\t" \
  "s_cbranch_execz 505f\n\t" \
  "v_mfma_f32_16x16x32_bf16 v[216:219], v[116:119], v[112:115], v[216:219]\n\t" \
  "v_mfma_f32_16x16x32_bf16 v[224:227], v[88:91], v[112:115], v[224:227]\n\t" \
  "v_mfma_f32_16x16x32_bf16 v[224:227], v[124:127], v[100:103], v[224:227]\n\t" \
  "v_mfma_f32_16x16x32_bf16 v[232:235], v[96:99], v[100:103], v[232:235]\n\t" \
  "v_mfma_f32_16x16x32_bf16 v[232:235], v[132:135], v[112:115], v[232:235]\n\t" \
  "v_mfma_f32_16x16x32_bf16 v[240:243], v[108:111], v[112:115], v[240:243]\n\t" \
  "v_mfma_f32_16x16x32_bf16 v[240:243], v[148:151], v[100:103], v[240:243]\n\t" \
  "v_mfma_f32_16x16x32_bf16 v[248:251], v[128:131], v[100:103], v[248:251]\n\t" \
  "v_mfma_f32_16x16x32_bf16 v[248:251], v[120:123], v[112:115], v[248:251]\n\t" \
  "v_mfma_f32_16x16x32_bf16 v[252:255], v[128:131], v[136:139], v[252:255]\n\t" \
  "s_waitcnt lgkmcnt(0)\n\t" \
  "v_mfma_f32_16x16x32_bf16 v[252:255], v[120:123], v[144:147], v[252:255]\n\t" \
  "v_mfma_f32_16x16x32_bf16 v[220:223], v[116:119], v[144:147], v[220:223]\n\t" \
  "v_mfma_f32_16x16x32_bf16 v[220:223], v[80:83], v[136:139], v[220:223]\n\t" \
  "s_waitcnt vmcnt(0)\n\t" \
  "s_barrier\n\t" \
  "v_mfma_f32_16x16x32_bf16 v[228:231], v[124:127], v[136:139], v[228:231]\n\t" \
  "ds_read_b64_tr_b16 v[68:69], v54 offset:26624\n\t" \
  "v_mfma_f32_16x16x32_bf16 v[228:231], v[88:91], v[144:147], v[228:231]\n\t" \
  "ds_read_b64_tr_b16 v[70:71], v54 offset:27136\n\t" \
  "v_mfma_f32_16x16x32_bf16 v[236:239], v[132:135], v[144:147], v[236:239]\n\t" \
  "ds_read_b128 v[92:95], v52 offset:10240\n\t" \
  "v_mfma_f32_16x16x32_bf16 v[236:239], v[96:99], v[136:139], v[236:239]\n\t" \
  "ds_read_b64_tr_b16 v[64:65], v54 offset:30720\n\t" \
  "v_mfma_f32_16x16x32_bf16 v[244:247], v[148:151], v[136:139], v[244:247]\n\t" \
  "ds_read_b64_tr_b16 v[66:67], v54 offset:31232\n\t" \
  "v_mfma_f32_16x16x32_bf16 v[244:247], v[108:111], v[144:147], v[244:247]\n\t" \
  "ds_read_b128 v[84:87], v53 offset:10240\n\t" \
  "v_mfma_f32_16x16x32_bf16 v[152:155], v[72:75], v[80:83], v[152:155]\n\t" \
  "ds_read_b128 v[100:103], v53 offset:12288\n\t" \
  "v_mfma_f32_16x16x32_bf16 v[152:155], v[104:107], v[116:119], v[152:155]\n\t" \
  "ds_read_b128 v[112:115], v52 offset:12288\n\t" \
  "v_mfma_f32_16x16x32_bf16 v[160:163], v[104:107], v[88:91], v[160:163]\n\t" \
  "ds_read_b128 v[128:131], v52 offset:14336\n\t" \
  "v_mfma_f32_16x16x32_bf16 v[160:163], v[72:75], v[124:127], v[160:163]\n\t" \
  "ds_read_b128 v[120:123], v53 offset:14336\n\t" \
  "v_mfma_f32_16x16x32_bf16 v[168:171], v[72:75], v[96:99], v[168:171]\n\t" \
  "ds_read_b128 v[136:139], v53 offset:16384\n\t" \
  "v_mfma_f32_16x16x32_bf16 v[168:171], v[104:107], v[132:135], v[168:171]\n\t" \
  "ds_read_b128 v[144:147], v52 offset:16384\n\t" \
  "v_mfma_f32_16x16x32_bf16 v[176:179], v[104:107], v[108:111], v[176:179]\n\t" \
  "ds_read_b64_tr_b16 v[104:105], v55 offset:26624\n\t" \
  "v_mfma_f32_16x16x32_bf16 v[176:179], v[72:75], v[148:151], v[176:179]\n\t" \
  "ds_read_b64_tr_b16 v[106:107], v55 offset:27136\n\t" \
  "v_mfma_f32_16x16x32_bf16 v[156:159], v[76:79], v[80:83], v[156:159]\n\t" \
  "ds_read_b64_tr_b16 v[72:73], v55 offset:30720\n\t" \
  "v_mfma_f32_16x16x32_bf16 v[156:159], v[140:143], v[116:119], v[156:159]\n\t" \
  "ds_read_b64_tr_b16 v[74:75], v55 offset:31232\n\t" \
  "v_mfma_f32_16x16x32_bf16 v[164:167], v[140:143], v[88:91], v[164:167]\n\t" \
  "ds_read_b64_tr_b16 v[80:81], v54 offset:34816\n\t" \
  "v_mfma_f32_16x16x32_bf16 v[164:167], v[76:79], v[124:127], v[164:167]\n\t" \
  "ds_read_b64_tr_b16 v[82:83], v54 offset:35328\n\t" \
  "v_mfma_f32_16x16x32_bf16 v[172:175], v[76:79], v[96:99], v[172:175]\n\t" \
  "ds_read_b64_tr_b16 v[116:117], v54 offset:38912\n\t" \
  "v_mfma_f32_16x16x32_bf16 v[172:175], v[140:143], v[132:135], v[172:175]\n\t" \
  "ds_read_b64_tr_b16 v[118:119], v54 offset:39424\n\t" \
  "v_mfma_f32_16x16x32_bf16 v[180:183], v[140:143], v[108:111], v[180:183]\n\t" \
  "ds_read_b128 v[88:91], v52 offset:0\n\t" \
  "v_mfma_f32_16x16x32_bf16 v[180:183], v[76:79], v[148:151], v[180:183]\n\t" \
  "ds_read_b128 v[124:127], v53 offset:0\n\t" \
  "s_waitcnt lgkmcnt(7)\n\t" \
  "v_mfma_f32_16x16x32_bf16 v[184:187], v[68:71], v[92:95], v[184:187]\n\t" \
  "ds_read_b64_tr_b16 v[96:97], v55 offset:34816\n\t" \
  "v_mfma_f32_16x16x32_bf16 v[184:187], v[64:67], v[84:87], v[184:187]\n\t" \
  "ds_read_b64_tr_b16 v[98:99], v55 offset:35328\n\t" \
  "v_mfma_f32_16x16x32_bf16 v[192:195], v[64:67], v[100:103], v[192:195]\n\t" \
  "ds_read_b64_tr_b16 v[132:133], v55 offset:38912\n\t" \
  "v_mfma_f32_16x16x32_bf16 v[192:195], v[68:71], v[112:115], v[192:195]\n\t" \
  "ds_read_b64_tr_b16 v[134:135], v55 offset:39424\n\t" \
  "v_mfma_f32_16x16x32_bf16 v[200:203], v[68:71], v[128:131], v[200:203]\n\t" \
  "ds_read_b64_tr_b16 v[108:109], v54 offset:18432\n\t" \
  "v_mfma_f32_16x16x32_bf16 v[200:203], v[64:67], v[120:123], v[200:203]\n\t" \
  "ds_read_b64_tr_b16 v[110:111], v54 offset:18944\n\t" \
  "v_mfma_f32_16x16x32_bf16 v[208:211], v[64:67], v[136:139], v[208:211]\n\t" \
  "ds_read_b64_tr_b16 v[140:141], v54 offset:22528\n\t" \
  "v_mfma_f32_16x16x32_bf16 v[208:211], v[68:71], v[144:147], v[208:211]\n\t" \
  "ds_read_b64_tr_b16 v[142:143], v54 offset:23040\n\t" \
  "v_mfma_f32_16x16x32_bf16 v[188:191], v[104:107], v[92:95], v[188:191]\n\t" \
  "ds_read_b64_tr_b16 v[76:77], v55 offset:18432\n\t" \
  "s_waitcnt lgkmcnt(7)\n\t" \
  "v_mfma_f32_16x16x32_bf16 v[188:191], v[72:75], v[84:87], v[188:191]\n\t" \
  "ds_read_b64_tr_b16 v[78:79], v55 offset:18944\n\t" \
  "v_mfma_f32_16x16x32_bf16 v[196:199], v[72:75], v[100:103], v[196:199]\n\t" \
  "ds_read_b64_tr_b16 v[148:149], v55 offset:22528\n\t" \
  "v_mfma_f32_16x16x32_bf16 v[196:199], v[104:107], v[112:115], v[196:199]\n\t" \
  "ds_read_b64_tr_b16 v[150:151], v55 offset:23040\n\t" \
  "v_mfma_f32_16x16x32_bf16 v[204:207], v[104:107], v[128:131], v[204:207]\n\t" \
  "v_mfma_f32_16x16x32_bf16 v[204:207], v[72:75], v[120:123], v[204:207]\n\t" \
  "v_mfma_f32_16x16x32_bf16 v[212:215], v[72:75], v[136:139], v[212:215]\n\t" \
  "v_mfma_f32_16x16x32_bf16 v[212:215], v[104:107], v[144:147], v[212:215]\n\t" \
  "v_mfma_f32_16x16x32_bf16 v[216:219], v[92:95], v[80:83], v[216:219]\n\t" \
  "v_mfma_f32_16x16x32_bf16 v[216:219], v[84:87], v[116:119], v[216:219]\n\t" \
  "v_mfma_f32_16x16x32_bf16 v[224:227], v[100:103], v[116:119], v[224:227]\n\t" \
  "v_mfma_f32_16x16x32_bf16 v[224:227], v[112:115], v[80:83], v[224:227]\n\t" \
  "v_mfma_f32_16x16x32_bf16 v[232:235], v[128:131], v[80:83], v[232:235]\n\t" \
  "v_mfma_f32_16x16x32_bf16 v[232:235], v[120:123], v[116:119], v[232:235]\n\t" \
  "v_mfma_f32_16x16x32_bf16 v[240:243], v[136:139], v[116:119], v[240:243]\n\t" \
  "v_mfma_f32_16x16x32_bf16 v[240:243], v[144:147], v[80:83], v[240:243]\n\t" \
  "v_mfma_f32_16x16x32_bf16 v[248:251], v[88:91], v[80:83], v[248:251]\n\t" \
  "v_mfma_f32_16x16x32_bf16 v[248:251], v[124:127], v[116:119], v[248:251]\n\t" \
  "v_mfma_f32_16x16x32_bf16 v[252:255], v[88:91], v[96:99], v[252:255]\n\t" \
  "s_waitcnt lgkmcnt(0)\n\t" \
  "v_mfma_f32_16x16x32_bf16 v[252:255], v[124:127], v[132:135], v[252:255]\n\t" \
  "v_mfma_f32_16x16x32_bf16 v[220:223], v[84:87], v[132:135], v[220:223]\n\t" \
  "v_mfma_f32_16x16x32_bf16 v[220:223], v[92:95], v[96:99], v[220:223]\n\t" \
  "s_barrier\n\t" \
  "v_mfma_f32_16x16x32_bf16 v[228:231], v[112:115], v[96:99], v[228:231]\n\t" \
  "ds_read_b64_tr_b16 v[64:65], v58 offset:26624\n\t" \
  "v_mfma_f32_16x16x32_bf16 v[228:231], v[100:103], v[132:135], v[228:231]\n\t" \
  "ds_read_b64_tr_b16 v[66:67], v58 offset:27136\n\t" \
  "v_mfma_f32_16x16x32_bf16 v[236:239], v[120:123], v[132:135], v[236:239]\n\t" \
  "ds_read_b128 v[68:71], v56 offset:10240\n\t" \
  "v_mfma_f32_16x16x32_bf16 v[236:239], v[128:131], v[96:99], v[236:239]\n\t" \
  "ds_read_b64_tr_b16 v[72:73], v58 offset:30720\n\t" \
  "v_mfma_f32_16x16x32_bf16 v[244:247], v[144:147], v[96:99], v[244:247]\n\t" \
  "ds_read_b64_tr_b16 v[74:75], v58 offset:31232\n\t" \
  "v_mfma_f32_16x16x32_bf16 v[244:247], v[136:139], v[132:135], v[244:247]\n\t" \
  "ds_read_b128 v[104:107], v57 offset:10240\n\t" \
  "v_mfma_f32_16x16x32_bf16 v[152:155], v[108:111], v[92:95], v[152:155]\n\t" \
  "ds_read_b128 v[80:83], v57 offset:12288\n\t" \
  "v_mfma_f32_16x16x32_bf16 v[152:155], v[140:143], v[84:87], v[152:155]\n\t" \
  "ds_read_b128 v[116:119], v56 offset:12288\n\t" \
  "v_mfma_f32_16x16x32_bf16 v[160:163], v[140:143], v[100:103], v[160:163]\n\t" \
  "ds_read_b128 v[88:91], v56 offset:14336\n\t" \
  "v_mfma_f32_16x16x32_bf16 v[160:163], v[108:111], v[112:115], v[160:163]\n\t" \
  "ds_read_b128 v[124:127], v57 offset:14336\n\t" \
  "v_mfma_f32_16x16x32_bf16 v[168:171], v[108:111], v[128:131], v[168:171]\n\t" \
  "ds_read_b128 v[96:99], v57 offset:16384\n\t" \
  "v_mfma_f32_16x16x32_bf16 v[168:171], v[140:143], v[120:123], v[168:171]\n\t" \
  "ds_read_b128 v[132:135], v56 offset:16384\n\t" \
  "v_mfma_f32_16x16x32_bf16 v[176:179], v[140:143], v[136:139], v[176:179]\n\t" \
  "ds_read_b64_tr_b16 v[140:141], v59 offset:26624\n\t" \
  "v_mfma_f32_16x16x32_bf16 v[176:179], v[108:111], v[144:147], v[176:179]\n\t" \
  "ds_read_b64_tr_b16 v[142:143], v59 offset:27136\n\t" \
  "v_mfma_f32_16x16x32_bf16 v[156:159], v[76:79], v[92:95], v[156:159]\n\t" \
  "ds_read_b64_tr_b16 v[108:109], v59 offset:30720\n\t" \
  "v_mfma_f32_16x16x32_bf16 v[156:159], v[148:151], v[84:87], v[156:159]\n\t" \
  "ds_read_b64_tr_b16 v[110:111], v59 offset:31232\n\t" \
  "v_mfma_f32_16x16x32_bf16 v[164:167], v[148:151], v[100:103], v[164:167]\n\t" \
  "ds_read_b64_tr_b16 v[92:93], v58 offset:34816\n\t" \
  "v_mfma_f32_16x16x32_bf16 v[164:167], v[76:79], v[112:115], v[164:167]\n\t" \
  "ds_read_b64_tr_b16 v[94:95], v58 offset:35328\n\t" \
  "v_mfma_f32_16x16x32_bf16 v[172:175], v[76:79], v[128:131], v[172:175]\n\t" \
  "ds_read_b64_tr_b16 v[84:85], v58 offset:38912\n\t" \
  "v_mfma_f32_16x16x32_bf16 v[172:175], v[148:151], v[120:123], v[172:175]\n\t" \
  "ds_read_b64_tr_b16 v[86:87], v58 offset:39424\n\t" \
  "v_mfma_f32_16x16x32_bf16 v[180:183], v[148:151], v[136:139], v[180:183]\n\t" \
  "ds_read_b128 v[100:103], v56 offset:0\n\t" \
  "v_mfma_f32_16x16x32_bf16 v[180:183], v[76:79], v[144:147], v[180:183]\n\t" \
  "ds_read_b128 v[112:115], v57 offset:0\n\t" \
  "s_waitcnt lgkmcnt(7)\n\t" \
  "v_mfma_f32_16x16x32_bf16 v[184:187], v[64:67], v[68:71], v[184:187]\n\t" \
  "ds_read_b64_tr_b16 v[128:129], v59 offset:34816\n\t" \
  "v_mfma_f32_16x16x32_bf16 v[184:187], v[72:75], v[104:107], v[184:187]\n\t" \
  "ds_read_b64_tr_b16 v[130:131], v59 offset:35328\n\t" \
  "v_mfma_f32_16x16x32_bf16 v[192:195], v[72:75], v[80:83], v[192:195]\n\t" \
  "ds_read_b64_tr_b16 v[120:121], v59 offset:38912\n\t" \
  "v_mfma_f32_16x16x32_bf16 v[192:195], v[64:67], v[116:119], v[192:195]\n\t" \
  "ds_read_b64_tr_b16 v[122:123], v59 offset:39424\n\t" \
  "v_mfma_f32_16x16x32_bf16 v[200:203], v[64:67], v[88:91], v[200:203]\n\t" \
  "ds_read_b64_tr_b16 v[136:137], v58 offset:18432\n\t" \
  "v_mfma_f32_16x16x32_bf16 v[200:203], v[72:75], v[124:127], v[200:203]\n\t" \
  "ds_read_b64_tr_b16 v[138:139], v58 offset:18944\n\t" \
  "v_mfma_f32_16x16x32_bf16 v[208:211], v[72:75], v[96:99], v[208:211]\n\t" \
  "ds_read_b64_tr_b16 v[148:149], v58 offset:22528\n\t" \
  "v_mfma_f32_16x16x32_bf16 v[208:211], v[64:67], v[132:135], v[208:211]\n\t" \
  "ds_read_b64_tr_b16 v[150:151], v58 offset:23040\n\t" \
  "v_mfma_f32_16x16x32_bf16 v[188:191], v[140:143], v[68:71], v[188:191]\n\t" \
  "ds_read_b64_tr_b16 v[76:77], v59 offset:18432\n\t" \
  "s_waitcnt lgkmcnt(7)\n\t" \
  "v_mfma_f32_16x16x32_bf16 v[188:191], v[108:111], v[104:107], v[188:191]\n\t" \
  "ds_read_b64_tr_b16 v[78:79], v59 offset:18944\n\t" \
  "v_mfma_f32_16x16x32_bf16 v[196:199], v[108:111], v[80:83], v[196:199]\n\t" \
  "ds_read_b64_tr_b16 v[144:145], v59 offset:22528\n\t" \
  "v_mfma_f32_16x16x32_bf16 v[196:199], v[140:143], v[116:119], v[196:199]\n\t" \
  "ds_read_b64_tr_b16 v[146:147], v59 offset:23040\n\t" \
  "v_mfma_f32_16x16x32_bf16 v[204:207], v[140:143], v[88:91], v[204:207]\n\t" \
  "v_mfma_f32_16x16x32_bf16 v[204:207], v[108:111], v[124:127], v[204:207]\n\t" \
  "v_mfma_f32_16x16x32_bf16 v[212:215], v[108:111], v[96:99], v[212:215]\n\t" \
  "v_mfma_f32_16x16x32_bf16 v[212:215], v[140:143], v[132:135], v[212:215]\n\t" \
  "v_mfma_f32_16x16x32_bf16 v[216:219], v[68:71], v[92:95], v[216:219]\n\t" \
  "v_mfma_f32_16x16x32_bf16 v[216:219], v[104:107], v[84:87], v[216:219]\n\t" \
  "v_mfma_f32_16x16x32_bf16 v[224:227], v[80:83], v[84:87], v[224:227]\n\t" \
  "v_mfma_f32_16x16x32_bf16 v[224:227], v[116:119], v[92:95], v[224:227]\n\t" \
  "v_mfma_f32_16x16x32_bf16 v[232:235], v[88:91], v[92:95], v[232:235]\n\t" \
  "v_mfma_f32_16x16x32_bf16 v[232:235], v[124:127], v[84:87], v[232:235]\n\t" \
  "v_mfma_f32_16x16x32_bf16 v[240:243], v[96:99], v[84:87], v[240:243]\n\t" \
  "v_mfma_f32_16x16x32_bf16 v[240:243], v[132:135], v[92:95], v[240:243]\n\t" \
  "v_mfma_f32_16x16x32_bf16 v[248:251], v[100:103], v[92:95], v[248:251]\n\t" \
  "v_mfma_f32_16x16x32_bf16 v[248:251], v[112:115], v[84:87], v[248:251]\n\t" \
  "v_mfma_f32_16x16x32_bf16 v[252:255], v[100:103], v[128:131], v[252:255]\n\t" \
  "s_waitcnt lgkmcnt(0)\n\t" \
  "v_mfma_f32_16x16x32_bf16 v[252:255], v[112:115], v[120:123], v[252:255]\n\t" \
  "v_mfma_f32_16x16x32_bf16 v[220:223], v[104:107], v[120:123], v[220:223]\n\t" \
  "v_mfma_f32_16x16x32_bf16 v[220:223], v[68:71], v[128:131], v[220:223]\n\t" \
  "s_barrier\n\t" \
  "v_mfma_f32_16x16x32_bf16 v[228:231], v[116:119], v[128:131], v[228:231]\n\t" \
  "ds_read_b64_tr_b16 v[72:73], v62 offset:26624\n\t" \
  "v_mfma_f32_16x16x32_bf16 v[228:231], v[80:83], v[120:123], v[228:231]\n\t" \
  "ds_read_b64_tr_b16 v[74:75], v62 offset:27136\n\t" \
  "v_mfma_f32_16x16x32_bf16 v[236:239], v[124:127], v[120:123], v[236:239]\n\t" \
  "ds_read_b128 v[64:67], v60 offset:10240\n\t" \
  "v_mfma_f32_16x16x32_bf16 v[236:239], v[88:91], v[128:131], v[236:239]\n\t" \
  "ds_read_b64_tr_b16 v[108:109], v62 offset:30720\n\t" \
  "v_mfma_f32_16x16x32_bf16 v[244:247], v[132:135], v[128:131], v[244:247]\n\t" \
  "ds_read_b64_tr_b16 v[110:111], v62 offset:31232\n\t" \
  "v_mfma_f32_16x16x32_bf16 v[244:247], v[96:99], v[120:123], v[244:247]\n\t" \
  "ds_read_b128 v[140:143], v61 offset:10240\n\t" \
  "v_mfma_f32_16x16x32_bf16 v[152:155], v[136:139], v[68:71], v[152:155]\n\t" \
  "ds_read_b128 v[92:95], v61 offset:12288\n\t" \
  "v_mfma_f32_16x16x32_bf16 v[152:155], v[148:151], v[104:107], v[152:155]\n\t" \
  "ds_read_b128 v[84:87], v60 offset:12288\n\t" \
  "v_mfma_f32_16x16x32_bf16 v[160:163], v[148:151], v[80:83], v[160:163]\n\t" \
  "ds_read_b128 v[100:103], v60 offset:14336\n\t" \
  "v_mfma_f32_16x16x32_bf16 v[160:163], v[136:139], v[116:119], v[160:163]\n\t" \
  "ds_read_b128 v[112:115], v61 offset:14336\n\t" \
  "v_mfma_f32_16x16x32_bf16 v[168:171], v[136:139], v[88:91], v[168:171]\n\t" \
  "ds_read_b128 v[128:131], v61 offset:16384\n\t" \
  "v_mfma_f32_16x16x32_bf16 v[168:171], v[148:151], v[124:127], v[168:171]\n\t" \
  "ds_read_b128 v[120:123], v60 offset:16384\n\t" \
  "v_mfma_f32_16x16x32_bf16 v[176:179], v[148:151], v[96:99], v[176:179]\n\t" \
  "ds_read_b64_tr_b16 v[148:149], v63 offset:26624\n\t" \
  "v_mfma_f32_16x16x32_bf16 v[176:179], v[136:139], v[132:135], v[176:179]\n\t" \
  "ds_read_b64_tr_b16 v[150:151], v63 offset:27136\n\t" \
  "v_mfma_f32_16x16x32_bf16 v[156:159], v[76:79], v[68:71], v[156:159]\n\t" \
  "ds_read_b64_tr_b16 v[136:137], v63 offset:30720\n\t" \
  "v_mfma_f32_16x16x32_bf16 v[156:159], v[144:147], v[104:107], v[156:159]\n\t" \
  "ds_read_b64_tr_b16 v[138:139], v63 offset:31232\n\t" \
  "v_mfma_f32_16x16x32_bf16 v[164:167], v[144:147], v[80:83], v[164:167]\n\t" \
  "ds_read_b64_tr_b16 v[68:69], v62 offset:34816\n\t" \
  "v_mfma_f32_16x16x32_bf16 v[164:167], v[76:79], v[116:119], v[164:167]\n\t" \
  "ds_read_b64_tr_b16 v[70:71], v62 offset:35328\n\t" \
  "v_mfma_f32_16x16x32_bf16 v[172:175], v[76:79], v[88:91], v[172:175]\n\t" \
  "ds_read_b64_tr_b16 v[104:105], v62 offset:38912\n\t" \
  "v_mfma_f32_16x16x32_bf16 v[172:175], v[144:147], v[124:127], v[172:175]\n\t" \
  "ds_read_b64_tr_b16 v[106:107], v62 offset:39424\n\t" \
  "v_mfma_f32_16x16x32_bf16 v[180:183], v[144:147], v[96:99], v[180:183]\n\t" \
  "ds_read_b128 v[80:83], v60 offset:0\n\t" \
  "v_mfma_f32_16x16x32_bf16 v[180:183], v[76:79], v[132:135], v[180:183]\n\t" \
  "ds_read_b128 v[116:119], v61 offset:0\n\t" \
  "s_waitcnt lgkmcnt(7)\n\t" \
  "v_mfma_f32_16x16x32_bf16 v[184:187], v[72:75], v[64:67], v[184:187]\n\t" \
  "ds_read_b64_tr_b16 v[88:89], v63 offset:34816\n\t" \
  "v_mfma_f32_16x16x32_bf16 v[184:187], v[108:111], v[140:143], v[184:187]\n\t" \
  "ds_read_b64_tr_b16 v[90:91], v63 offset:35328\n\t" \
  "v_mfma_f32_16x16x32_bf16 v[192:195], v[108:111], v[92:95], v[192:195]\n\t" \
  "ds_read_b64_tr_b16 v[124:125], v63 offset:38912\n\t" \
  "v_mfma_f32_16x16x32_bf16 v[192:195], v[72:75], v[84:87], v[192:195]\n\t" \
  "ds_read_b64_tr_b16 v[126:127], v63 offset:39424\n\t" \
  "v_mfma_f32_16x16x32_bf16 v[200:203], v[72:75], v[100:103], v[200:203]\n\t" \
  "ds_read_b64_tr_b16 v[96:97], v62 offset:18432\n\t" \
  "v_mfma_f32_16x16x32_bf16 v[200:203], v[108:111], v[112:115], v[200:203]\n\t" \
  "ds_read_b64_tr_b16 v[98:99], v62 offset:18944\n\t" \
  "v_mfma_f32_16x16x32_bf16 v[208:211], v[108:111], v[128:131], v[208:211]\n\t" \
  "ds_read_b64_tr_b16 v[144:145], v62 offset:22528\n\t" \
  "v_mfma_f32_16x16x32_bf16 v[208:211], v[72:75], v[120:123], v[208:211]\n\t" \
  "ds_read_b64_tr_b16 v[146:147], v62 offset:23040\n\t" \
  "v_mfma_f32_16x16x32_bf16 v[188:191], v[148:151], v[64:67], v[188:191]\n\t" \
  "ds_read_b64_tr_b16 v[76:77], v63 offset:18432\n\t" \
  "s_waitcnt lgkmcnt(7)\n\t" \
  "v_mfma_f32_16x16x32_bf16 v[188:191], v[136:139], v[140:143], v[188:191]\n\t" \
  "ds_read_b64_tr_b16 v[78:79], v63 offset:18944\n\t" \
  "v_mfma_f32_16x16x32_bf16 v[196:199], v[136:139], v[92:95], v[196:199]\n\t" \
  "ds_read_b64_tr_b16 v[132:133], v63 offset:22528\n\t" \
  "v_mfma_f32_16x16x32_bf16 v[196:199], v[148:151], v[84:87], v[196:199]\n\t" \
  "ds_read_b64_tr_b16 v[134:135], v63 offset:23040\n\t" \
  "v_mfma_f32_16x16x32_bf16 v[204:207], v[148:151], v[100:103], v[204:207]\n\t" \
  "v_mfma_f32_16x16x32_bf16 v[204:207], v[136:139], v[112:115], v[204:207]\n\t" \
  "v_mfma_f32_16x16x32_bf16 v[212:215], v[136:139], v[128:131], v[212:215]\n\t" \
  "v_mfma_f32_16x16x32_bf16 v[212:215], v[148:151], v[120:123], v[212:215]\n\t" \
  "v_mfma_f32_16x16x32_bf16 v[216:219], v[64:67], v[68:71], v[216:219]\n\t" \
  "v_mfma_f32_16x16x32_bf16 v[216:219], v[140:143], v[104:107], v[216:219]\n\t" \
  "v_mfma_f32_16x16x32_bf16 v[224:227], v[92:95], v[104:107], v[224:227]\n\t" \
  "v_mfma_f32_16x16x32_bf16 v[224:227], v[84:87], v[68:71], v[224:227]\n\t" \
  "v_mfma_f32_16x16x32_bf16 v[232:235], v[100:103], v[68:71], v[232:235]\n\t" \
  "v_mfma_f32_16x16x32_bf16 v[232:235], v[112:115], v[104:107], v[232:235]\n\t" \
  "v_mfma_f32_16x16x32_bf16 v[240:243], v[128:131], v[104:107], v[240:243]\n\t" \
  "v_mfma_f32_16x16x32_bf16 v[240:243], v[120:123], v[68:71], v[240:243]\n\t" \
  "v_mfma_f32_16x16x32_bf16 v[248:251], v[80:83], v[68:71], v[248:251]\n\t" \
  "v_mfma_f32_16x16x32_bf16 v[248:251], v[116:119], v[104:107], v[248:251]\n\t" \
  "v_mfma_f32_16x16x32_bf16 v[252:255], v[80:83], v[88:91], v[252:255]\n\t" \
  "s_waitcnt lgkmcnt(0)\n\t" \
  "v_mfma_f32_16x16x32_bf16 v[252:255], v[116:119], v[124:127], v[252:255]\n\t" \
  "v_mfma_f32_16x16x32_bf16 v[220:223], v[140:143], v[124:127], v[220:223]\n\t" \
  "v_mfma_f32_16x16x32_bf16 v[220:223], v[64:67], v[88:91], v[220:223]\n\t" \
  "s_barrier\n\t" \
  "v_mfma_f32_16x16x32_bf16 v[228:231], v[84:87], v[88:91], v[228:231]\n\t" \
  "ds_read_b64_tr_b16 v[108:109], v54 offset:26624\n\t" \
  "v_mfma_f32_16x16x32_bf16 v[228:231], v[92:95], v[124:127], v[228:231]\n\t" \
  "ds_read_b64_tr_b16 v[110:111], v54 offset:27136\n\t" \
  "v_mfma_f32_16x16x32_bf16 v[236:239], v[112:115], v[124:127], v[236:239]\n\t" \
  "ds_read_b128 v[72:75], v52 offset:10240\n\t" \
  "v_mfma_f32_16x16x32_bf16 v[236:239], v[100:103], v[88:91], v[236:239]\n\t" \
  "ds_read_b64_tr_b16 v[136:137], v54 offset:30720\n\t" \
  "v_mfma_f32_16x16x32_bf16 v[244:247], v[120:123], v[88:91], v[244:247]\n\t" \
  "ds_read_b64_tr_b16 v[138:139], v54 offset:31232\n\t" \
  "v_mfma_f32_16x16x32_bf16 v[244:247], v[128:131], v[124:127], v[244:247]\n\t" \
  "ds_read_b128 v[148:151], v53 offset:10240\n\t" \
  "v_mfma_f32_16x16x32_bf16 v[152:155], v[96:99], v[64:67], v[152:155]\n\t" \
  "ds_read_b128 v[68:71], v53 offset:12288\n\t" \
  "v_mfma_f32_16x16x32_bf16 v[152:155], v[144:147], v[140:143], v[152:155]\n\t" \
  "ds_read_b128 v[104:107], v52 offset:12288\n\t" \
  "v_mfma_f32_16x16x32_bf16 v[160:163], v[144:147], v[92:95], v[160:163]\n\t" \
  "ds_read_b128 v[80:83], v52 offset:14336\n\t" \
  "v_mfma_f32_16x16x32_bf16 v[160:163], v[96:99], v[84:87], v[160:163]\n\t" \
  "ds_read_b128 v[116:119], v53 offset:14336\n\t" \
  "v_mfma_f32_16x16x32_bf16 v[168:171], v[96:99], v[100:103], v[168:171]\n\t" \
  "ds_read_b128 v[88:91], v53 offset:16384\n\t" \
  "v_mfma_f32_16x16x32_bf16 v[168:171], v[144:147], v[112:115], v[168:171]\n\t" \
  "ds_read_b128 v[124:127], v52 offset:16384\n\t" \
  "v_mfma_f32_16x16x32_bf16 v[176:179], v[144:147], v[128:131], v[176:179]\n\t" \
  "ds_read_b64_tr_b16 v[144:145], v55 offset:26624\n\t" \
  "v_mfma_f32_16x16x32_bf16 v[176:179], v[96:99], v[120:123], v[176:179]\n\t" \
  "ds_read_b64_tr_b16 v[146:147], v55 offset:27136\n\t" \
  "v_mfma_f32_16x16x32_bf16 v[156:159], v[76:79], v[64:67], v[156:159]\n\t" \
  "ds_read_b64_tr_b16 v[96:97], v55 offset:30720\n\t" \
  "v_mfma_f32_16x16x32_bf16 v[156:159], v[132:135], v[140:143], v[156:159]\n\t" \
  "ds_read_b64_tr_b16 v[98:99], v55 offset:31232\n\t" \
  "v_mfma_f32_16x16x32_bf16 v[164:167], v[132:135], v[92:95], v[164:167]\n\t" \
  "ds_read_b64_tr_b16 v[64:65], v54 offset:34816\n\t" \
  "v_mfma_f32_16x16x32_bf16 v[164:167], v[76:79], v[84:87], v[164:167]\n\t" \
  "ds_read_b64_tr_b16 v[66:67], v54 offset:35328\n\t" \
  "v_mfma_f32_16x16x32_bf16 v[172:175], v[76:79], v[100:103], v[172:175]\n\t" \
  "ds_read_b64_tr_b16 v[140:141], v54 offset:38912\n\t" \
  "v_mfma_f32_16x16x32_bf16 v[172:175], v[132:135], v[112:115], v[172:175]\n\t" \
  "ds_read_b64_tr_b16 v[142:143], v54 offset:39424\n\t" \
  "v_mfma_f32_16x16x32_bf16 v[180:183], v[132:135], v[128:131], v[180:183]\n\t" \
  "ds_read_b128 v[92:95], v52 offset:0\n\t" \
  "v_mfma_f32_16x16x32_bf16 v[180:183], v[76:79], v[120:123], v[180:183]\n\t" \
  "ds_read_b128 v[84:87], v53 offset:0\n\t" \
  "s_waitcnt lgkmcnt(7)\n\t" \
  "v_mfma_f32_16x16x32_bf16 v[184:187], v[108:111], v[72:75], v[184:187]\n\t" \
  "ds_read_b64_tr_b16 v[100:101], v55 offset:34816\n\t" \
  "v_mfma_f32_16x16x32_bf16 v[184:187], v[136:139], v[148:151], v[184:187]\n\t" \
  "ds_read_b64_tr_b16 v[102:103], v55 offset:35328\n\t" \
  "v_mfma_f32_16x16x32_bf16 v[192:195], v[136:139], v[68:71], v[192:195]\n\t" \
  "ds_read_b64_tr_b16 v[112:113], v55 offset:38912\n\t" \
  "v_mfma_f32_16x16x32_bf16 v[192:195], v[108:111], v[104:107], v[192:195]\n\t" \
  "ds_read_b64_tr_b16 v[114:115], v55 offset:39424\n\t" \
  "v_mfma_f32_16x16x32_bf16 v[200:203], v[108:111], v[80:83], v[200:203]\n\t" \
  "ds_read_b64_tr_b16 v[128:129], v54 offset:18432\n\t" \
  "v_mfma_f32_16x16x32_bf16 v[200:203], v[136:139], v[116:119], v[200:203]\n\t" \
  "ds_read_b64_tr_b16 v[130:131], v54 offset:18944\n\t" \
  "v_mfma_f32_16x16x32_bf16 v[208:211], v[136:139], v[88:91], v[208:211]\n\t" \
  "ds_read_b64_tr_b16 v[132:133], v54 offset:22528\n\t" \
  "v_mfma_f32_16x16x32_bf16 v[208:211], v[108:111], v[124:127], v[208:211]\n\t" \
  "ds_read_b64_tr_b16 v[134:135], v54 offset:23040\n\t" \
  "v_mfma_f32_16x16x32_bf16 v[188:191], v[144:147], v[72:75], v[188:191]\n\t" \
  "ds_read_b64_tr_b16 v[76:77], v55 offset:18432\n\t" \
  "s_waitcnt lgkmcnt(7)\n\t" \
  "v_mfma_f32_16x16x32_bf16 v[188:191], v[96:99], v[148:151], v[188:191]\n\t" \
  "ds_read_b64_tr_b16 v[78:79], v55 offset:18944\n\t" \
  "v_mfma_f32_16x16x32_bf16 v[196:199], v[96:99], v[68:71], v[196:199]\n\t" \
  "ds_read_b64_tr_b16 v[120:121], v55 offset:22528\n\t" \
  "v_mfma_f32_16x16x32_bf16 v[196:199], v[144:147], v[104:107], v[196:199]\n\t" \
  "ds_read_b64_tr_b16 v[122:123], v55 offset:23040\n\t" \
  "v_mfma_f32_16x16x32_bf16 v[204:207], v[144:147], v[80:83], v[204:207]\n\t" \
  "v_mfma_f32_16x16x32_bf16 v[204:207], v[96:99], v[116:119], v[204:207]\n\t" \
  "v_mfma_f32_16x16x32_bf16 v[212:215], v[96:99], v[88:91], v[212:215]\n\t" \
  "v_mfma_f32_16x16x32_bf16 v[212:215], v[144:147], v[124:127], v[212:215]\n\t" \
  "v_mfma_f32_16x16x32_bf16 v[216:219], v[72:75], v[64:67], v[216:219]\n\t" \
  "v_mfma_f32_16x16x32_bf16 v[216:219], v[148:151], v[140:143], v[216:219]\n\t" \
  "v_mfma_f32_16x16x32_bf16 v[224:227], v[68:71], v[140:143], v[224:227]\n\t" \
  "v_mfma_f32_16x16x32_bf16 v[224:227], v[104:107], v[64:67], v[224:227]\n\t" \
  "v_mfma_f32_16x16x32_bf16 v[232:235], v[80:83], v[64:67], v[232:235]\n\t" \
  "v_mfma_f32_16x16x32_bf16 v[232:235], v[116:119], v[140:143], v[232:235]\n\t" \
  "v_mfma_f32_16x16x32_bf16 v[240:243], v[88:91], v[140:143], v[240:243]\n\t" \
  "v_mfma_f32_16x16x32_bf16 v[240:243], v[124:127], v[64:67], v[240:243]\n\t" \
  "v_mfma_f32_16x16x32_bf16 v[248:251], v[92:95], v[64:67], v[248:251]\n\t" \
  "v_mfma_f32_16x16x32_bf16 v[248:251], v[84:87], v[140:143], v[248:251]\n\t" \
  "v_mfma_f32_16x16x32_bf16 v[252:255], v[92:95], v[100:103], v[252:255]\n\t" \
  "s_waitcnt lgkmcnt(0)\n\t" \
  "v_mfma_f32_16x16x32_bf16 v[252:255], v[84:87], v[112:115], v[252:255]\n\t" \
  "v_mfma_f32_16x16x32_bf16 v[220:223], v[148:151], v[112:115], v[220:223]\n\t" \
  "v_mfma_f32_16x16x32_bf16 v[220:223], v[72:75], v[100:103], v[220:223]\n\t" \
  "s_barrier\n\t" \
  "v_mfma_f32_16x16x32_bf16 v[228:231], v[104:107], v[100:103], v[228:231]\n\t" \
  "ds_read_b64_tr_b16 v[96:97], v58 offset:26624\n\t" \
  "v_mfma_f32_16x16x32_bf16 v[228:231], v[68:71], v[112:115], v[228:231]\n\t" \
  "ds_read_b64_tr_b16 v[98:99], v58 offset:27136\n\t" \
  "v_mfma_f32_16x16x32_bf16 v[236:239], v[116:119], v[112:115], v[236:239]\n\t" \
  "ds_read_b128 v[136:139], v56 offset:10240\n\t" \
  "v_mfma_f32_16x16x32_bf16 v[236:239], v[80:83], v[100:103], v[236:239]\n\t" \
  "ds_read_b64_tr_b16 v[64:65], v58 offset:30720\n\t" \
  "v_mfma_f32_16x16x32_bf16 v[244:247], v[124:127], v[100:103], v[244:247]\n\t" \
  "ds_read_b64_tr_b16 v[66:67], v58 offset:31232\n\t" \
  "v_mfma_f32_16x16x32_bf16 v[244:247], v[88:91], v[112:115], v[244:247]\n\t" \
  "ds_read_b128 v[108:111], v57 offset:10240\n\t" \
  "v_mfma_f32_16x16x32_bf16 v[152:155], v[128:131], v[72:75], v[152:155]\n\t" \
  "ds_read_b128 v[144:147], v57 offset:12288\n\t" \
  "v_mfma_f32_16x16x32_bf16 v[152:155], v[132:135], v[148:151], v[152:155]\n\t" \
  "ds_read_b128 v[140:143], v56 offset:12288\n\t" \
  "v_mfma_f32_16x16x32_bf16 v[160:163], v[132:135], v[68:71], v[160:163]\n\t" \
  "ds_read_b128 v[112:115], v56 offset:14336\n\t" \
  "v_mfma_f32_16x16x32_bf16 v[160:163], v[128:131], v[104:107], v[160:163]\n\t" \
  "ds_read_b64_tr_b16 v[92:93], v59 offset:26624\n\t" \
  "v_mfma_f32_16x16x32_bf16 v[168:171], v[128:131], v[80:83], v[168:171]\n\t" \
  "ds_read_b64_tr_b16 v[94:95], v59 offset:27136\n\t" \
  "v_mfma_f32_16x16x32_bf16 v[168:171], v[132:135], v[116:119], v[168:171]\n\t" \
  "ds_read_b64_tr_b16 v[84:85], v59 offset:30720\n\t" \
  "v_mfma_f32_16x16x32_bf16 v[176:179], v[132:135], v[88:91], v[176:179]\n\t" \
  "ds_read_b128 v[132:135], v57 offset:14336\n\t" \
  "v_mfma_f32_16x16x32_bf16 v[176:179], v[128:131], v[124:127], v[176:179]\n\t" \
  "ds_read_b128 v[128:131], v57 offset:16384\n\t" \
  "v_mfma_f32_16x16x32_bf16 v[156:159], v[76:79], v[72:75], v[156:159]\n\t" \
  "ds_read_b64_tr_b16 v[86:87], v59 offset:31232\n\t" \
  "v_mfma_f32_16x16x32_bf16 v[156:159], v[120:123], v[148:151], v[156:159]\n\t" \
  "ds_read_b128 v[148:151], v56 offset:16384\n\t" \
  "v_mfma_f32_16x16x32_bf16 v[164:167], v[120:123], v[68:71], v[164:167]\n\t" \
  "ds_read_b64_tr_b16 v[100:101], v58 offset:34816\n\t" \
  "v_mfma_f32_16x16x32_bf16 v[164:167], v[76:79], v[104:107], v[164:167]\n\t" \
  "ds_read_b64_tr_b16 v[102:103], v58 offset:35328\n\t" \
  "v_mfma_f32_16x16x32_bf16 v[172:175], v[76:79], v[80:83], v[172:175]\n\t" \
  "ds_read_b64_tr_b16 v[72:73], v58 offset:38912\n\t" \
  "v_mfma_f32_16x16x32_bf16 v[172:175], v[120:123], v[116:119], v[172:175]\n\t" \
  "ds_read_b64_tr_b16 v[74:75], v58 offset:39424\n\t" \
  "v_mfma_f32_16x16x32_bf16 v[180:183], v[120:123], v[88:91], v[180:183]\n\t" \
  "ds_read_b128 v[68:71], v56 offset:0\n\t" \
  "v_mfma_f32_16x16x32_bf16 v[180:183], v[76:79], v[124:127], v[180:183]\n\t" \
  "ds_read_b128 v[104:107], v57 offset:0\n\t" \
  "s_waitcnt lgkmcnt(7)\n\t" \
  "v_mfma_f32_16x16x32_bf16 v[184:187], v[96:99], v[136:139], v[184:187]\n\t" \
  "ds_read_b64_tr_b16 v[80:81], v59 offset:34816\n\t" \
  "v_mfma_f32_16x16x32_bf16 v[184:187], v[64:67], v[108:111], v[184:187]\n\t" \
  "ds_read_b64_tr_b16 v[82:83], v59 offset:35328\n\t" \
  "v_mfma_f32_16x16x32_bf16 v[192:195], v[64:67], v[144:147], v[192:195]\n\t" \
  "ds_read_b64_tr_b16 v[88:89], v59 offset:38912\n\t" \
  "v_mfma_f32_16x16x32_bf16 v[192:195], v[96:99], v[140:143], v[192:195]\n\t" \
  "ds_read_b64_tr_b16 v[90:91], v59 offset:39424\n\t" \
  "v_mfma_f32_16x16x32_bf16 v[200:203], v[96:99], v[112:115], v[200:203]\n\t" \
  "ds_read_b64_tr_b16 v[116:117], v58 offset:18432\n\t" \
  "v_mfma_f32_16x16x32_bf16 v[200:203], v[64:67], v[132:135], v[200:203]\n\t" \
  "ds_read_b64_tr_b16 v[118:119], v58 offset:18944\n\t" \
  "v_mfma_f32_16x16x32_bf16 v[208:211], v[64:67], v[128:131], v[208:211]\n\t" \
  "ds_read_b64_tr_b16 v[120:121], v58 offset:22528\n\t" \
  "s_waitcnt lgkmcnt(7)\n\t" \
  "v_mfma_f32_16x16x32_bf16 v[208:211], v[96:99], v[148:151], v[208:211]\n\t" \
  "ds_read_b64_tr_b16 v[122:123], v58 offset:23040\n\t" \
  "v_mfma_f32_16x16x32_bf16 v[188:191], v[92:95], v[136:139], v[188:191]\n\t" \
  "ds_read_b64_tr_b16 v[124:125], v59 offset:18432\n\t" \
  "v_mfma_f32_16x16x32_bf16 v[188:191], v[84:87], v[108:111], v[188:191]\n\t" \
  "ds_read_b64_tr_b16 v[126:127], v59 offset:18944\n\t" \
  "v_mfma_f32_16x16x32_bf16 v[196:199], v[84:87], v[144:147], v[196:199]\n\t" \
  "ds_read_b64_tr_b16 v[76:77], v59 offset:22528\n\t" \
  "v_mfma_f32_16x16x32_bf16 v[196:199], v[92:95], v[140:143], v[196:199]\n\t" \
  "ds_read_b64_tr_b16 v[78:79], v59 offset:23040\n\t" \
  "v_mfma_f32_16x16x32_bf16 v[204:207], v[92:95], v[112:115], v[204:207]\n\t" \
  "v_mfma_f32_16x16x32_bf16 v[204:207], v[84:87], v[132:135], v[204:207]\n\t" \
  "v_mfma_f32_16x16x32_bf16 v[212:215], v[84:87], v[128:131], v[212:215]\n\t" \
  "v_mfma_f32_16x16x32_bf16 v[212:215], v[92:95], v[148:151], v[212:215]\n\t" \
  "v_mfma_f32_16x16x32_bf16 v[216:219], v[136:139], v[100:103], v[216:219]\n\t" \
  "v_mfma_f32_16x16x32_bf16 v[216:219], v[108:111], v[72:75], v[216:219]\n\t" \
  "v_mfma_f32_16x16x32_bf16 v[224:227], v[144:147], v[72:75], v[224:227]\n\t" \
  "v_and_b32 v14, -16, v32\n\t" \
  "v_mfma_f32_16x16x32_bf16 v[224:227], v[140:143], v[100:103], v[224:227]\n\t" \
  "v_mfma_f32_16x16x32_bf16 v[232:235], v[112:115], v[100:103], v[232:235]\n\t" \
  "ds_read_b128 v[10:13], v14 offset:128\n\t" \
  "v_mfma_f32_16x16x32_bf16 v[232:235], v[132:135], v[72:75], v[232:235]\n\t" \
  "v_mfma_f32_16x16x32_bf16 v[240:243], v[128:131], v[72:75], v[240:243]\n\t" \
  "v_mfma_f32_16x16x32_bf16 v[240:243], v[148:151], v[100:103], v[240:243]\n\t" \
  "v_mfma_f32_16x16x32_bf16 v[248:251], v[68:71], v[100:103], v[248:251]\n\t" \
  "v_mfma_f32_16x16x32_bf16 v[248:251], v[104:107], v[72:75], v[248:251]\n\t" \
  "s_waitcnt lgkmcnt(1)\n\t" \
  "v_mfma_f32_16x16x32_bf16 v[252:255], v[68:71], v[80:83], v[252:255]\n\t" \
  "s_waitcnt lgkmcnt(0)\n\t" \
  "v_and_b32 v10, v10, v11\n\t" \
  "v_and_b32 v12, v12, v13\n\t" \
  "v_mfma_f32_16x16x32_bf16 v[252:255], v[104:107], v[88:91], v[252:255]\n\t" \
  "v_and_b32 v10, v10, v12\n\t" \
  "v_mfma_f32_16x16x32_bf16 v[220:223], v[108:111], v[88:91], v[220:223]\n\t" \
  "v_readfirstlane_b32 vcc_lo, v10\n\t" \
  "v_mfma_f32_16x16x32_bf16 v[220:223], v[136:139], v[80:83], v[220:223]\n\t" \
  "ds_write_b32 v32, v0 offset:144\n\t" \
  "s_cmp_eq_u32 vcc_lo, 0\n\t" \
  "s_cbranch_scc1 15f\n\t" \
  "14:\n\t" \
  "v_mfma_f32_16x16x32_bf16 v[228:231], v[140:143], v[80:83], v[228:231]\n\t" \
  "ds_read_b64_tr_b16 v[64:65], v62 offset:26624\n\t" \
  "v_mfma_f32_16x16x32_bf16 v[228:231], v[144:147], v[88:91], v[228:231]\n\t" \
  "ds_read_b64_tr_b16 v[66:67], v62 offset:27136\n\t" \
  "v_mfma_f32_16x16x32_bf16 v[236:239], v[132:135], v[88:91], v[236:239]\n\t" \
  "ds_read_b128 v[96:99], v60 offset:10240\n\t" \
  "v_mfma_f32_16x16x32_bf16 v[236:239], v[112:115], v[80:83], v[236:239]\n\t" \
  "ds_read_b128 v[84:87], v60 offset:12288\n\t" \
  "v_mfma_f32_16x16x32_bf16 v[244:247], v[148:151], v[80:83], v[244:247]\n\t" \
  "ds_read_b128 v[92:95], v60 offset:14336\n\t" \
  "v_mfma_f32_16x16x32_bf16 v[244:247], v[128:131], v[88:91], v[244:247]\n\t" \
  "ds_read_b128 v[100:103], v60 offset:16384\n\t" \
  "v_mfma_f32_16x16x32_bf16 v[152:155], v[116:119], v[136:139], v[152:155]\n\t" \
  "ds_read_b64_tr_b16 v[72:73], v63 offset:26624\n\t" \
  "v_mfma_f32_16x16x32_bf16 v[152:155], v[120:123], v[108:111], v[152:155]\n\t" \
  "ds_read_b64_tr_b16 v[74:75], v63 offset:27136\n\t" \
  "v_mfma_f32_16x16x32_bf16 v[160:163], v[120:123], v[144:147], v[160:163]\n\t" \
  "ds_read_b64_tr_b16 v[68:69], v62 offset:34816\n\t" \
  "v_mfma_f32_16x16x32_bf16 v[160:163], v[116:119], v[140:143], v[160:163]\n\t" \
  "ds_read_b64_tr_b16 v[70:71], v62 offset:35328\n\t" \
  "v_mfma_f32_16x16x32_bf16 v[168:171], v[116:119], v[112:115], v[168:171]\n\t" \
  "ds_read_b128 v[104:107], v60 offset:0\n\t" \
  "v_mfma_f32_16x16x32_bf16 v[168:171], v[120:123], v[132:135], v[168:171]\n\t" \
  "ds_read_b64_tr_b16 v[80:81], v63 offset:34816\n\t" \
  "v_mfma_f32_16x16x32_bf16 v[176:179], v[120:123], v[128:131], v[176:179]\n\t" \
  "ds_read_b64_tr_b16 v[82:83], v63 offset:35328\n\t" \
  "v_mfma_f32_16x16x32_bf16 v[176:179], v[116:119], v[148:151], v[176:179]\n\t" \
  "ds_read_b64_tr_b16 v[88:89], v62 offset:18432\n\t" \
  "v_mfma_f32_16x16x32_bf16 v[156:159], v[124:127], v[136:139], v[156:159]\n\t" \
  "ds_read_b64_tr_b16 v[90:91], v62 offset:18944\n\t" \
  "v_mfma_f32_16x16x32_bf16 v[156:159], v[76:79], v[108:111], v[156:159]\n\t" \
  "ds_read_b64_tr_b16 v[120:121], v62 offset:30720\n\t" \
  "v_mfma_f32_16x16x32_bf16 v[164:167], v[76:79], v[144:147], v[164:167]\n\t" \
  "ds_read_b64_tr_b16 v[122:123], v62 offset:31232\n\t" \
  "v_mfma_f32_16x16x32_bf16 v[164:167], v[124:127], v[140:143], v[164:167]\n\t" \
  "ds_read_b128 v[116:119], v61 offset:10240\n\t" \
  "v_mfma_f32_16x16x32_bf16 v[172:175], v[124:127], v[112:115], v[172:175]\n\t" \
  "ds_read_b128 v[136:139], v61 offset:12288\n\t" \
  "v_mfma_f32_16x16x32_bf16 v[172:175], v[76:79], v[132:135], v[172:175]\n\t" \
  "ds_read_b128 v[108:111], v61 offset:14336\n\t" \
  "v_mfma_f32_16x16x32_bf16 v[180:183], v[76:79], v[128:131], v[180:183]\n\t" \
  "ds_read_b64_tr_b16 v[76:77], v63 offset:18432\n\t" \
  "v_mfma_f32_16x16x32_bf16 v[180:183], v[124:127], v[148:151], v[180:183]\n\t" \
  "ds_read_b64_tr_b16 v[78:79], v63 offset:18944\n\t" \
  "s_waitcnt lgkmcnt(7)\n\t" \
  "v_mfma_f32_16x16x32_bf16 v[184:187], v[64:67], v[96:99], v[184:187]\n\t" \
  "ds_read_b128 v[144:147], v61 offset:16384\n\t" \
  "v_mfma_f32_16x16x32_bf16 v[192:195], v[64:67], v[84:87], v[192:195]\n\t" \
  "ds_read_b64_tr_b16 v[140:141], v63 offset:30720\n\t" \
  "v_mfma_f32_16x16x32_bf16 v[200:203], v[64:67], v[92:95], v[200:203]\n\t" \
  "ds_read_b64_tr_b16 v[142:143], v63 offset:31232\n\t" \
  "v_mfma_f32_16x16x32_bf16 v[208:211], v[64:67], v[100:103], v[208:211]\n\t" \
  "502:\n\t" \
  "s_cbranch_execz 506f\n\t" \
  "ds_read_b64_tr_b16 v[112:113], v62 offset:22528\n\t" \
  "v_mfma_f32_16x16x32_bf16 v[188:191], v[72:75], v[96:99], v[188:191]\n\t" \
  "ds_read_b64_tr_b16 v[114:115], v62 offset:23040\n\t" \
  "v_mfma_f32_16x16x32_bf16 v[196:199], v[72:75], v[84:87], v[196:199]\n\t" \
  "ds_read_b64_tr_b16 v[132:133], v63 offset:22528\n\t" \
  "v_mfma_f32_16x16x32_bf16 v[204:207], v[72:75], v[92:95], v[204:207]\n\t" \
  "ds_read_b64_tr_b16 v[134:135], v63 offset:23040\n\t" \
  "v_mfma_f32_16x16x32_bf16 v[212:215], v[72:75], v[100:103], v[212:215]\n\t" \
  "ds_read_b64_tr_b16 v[128:129], v62 offset:38912\n\t" \
  "v_mfma_f32_16x16x32_bf16 v[248:251], v[104:107], v[68:71], v[248:251]\n\t" \
  "ds_read_b64_tr_b16 v[130:131], v62 offset:39424\n\t" \
  "v_mfma_f32_16x16x32_bf16 v[216:219], v[96:99], v[68:71], v[216:219]\n\t" \
  "ds_read_b128 v[124:127], v61 offset:0\n\t" \
  "v_mfma_f32_16x16x32_bf16 v[224:227], v[84:87], v[68:71], v[224:227]\n\t" \
  "ds_read_b64_tr_b16 v[148:149], v63 offset:38912\n\t" \
  "v_mfma_f32_16x16x32_bf16 v[232:235], v[92:95], v[68:71], v[232:235]\n\t" \
  "ds_read_b64_tr_b16 v[150:151], v63 offset:39424\n\t" \
  "v_mfma_f32_16x16x32_bf16 v[240:243], v[100:103], v[68:71], v[240:243]\n\t" \
  "v_mfma_f32_16x16x32_bf16 v[252:255], v[104:107], v[80:83], v[252:255]\n\t" \
  "v_mfma_f32_16x16x32_bf16 v[220:223], v[96:99], v[80:83], v[220:223]\n\t" \
  "v_mfma_f32_16x16x32_bf16 v[228:231], v[84:87], v[80:83], v[228:231]\n\t" \
  "v_mfma_f32_16x16x32_bf16 v[236:239], v[92:95], v[80:83], v[236:239]\n\t" \
  "v_mfma_f32_16x16x32_bf16 v[244:247], v[100:103], v[80:83], v[244:247]\n\t" \
  "v_mfma_f32_16x16x32_bf16 v[152:155], v[88:91], v[96:99], v[152:155]\n\t" \
  "v_mfma_f32_16x16x32_bf16 v[160:163], v[88:91], v[84:87], v[160:163]\n\t" \
  "v_mfma_f32_16x16x32_bf16 v[168:171], v[88:91], v[92:95], v[168:171]\n\t" \
  "v_mfma_f32_16x16x32_bf16 v[176:179], v[88:91], v[100:103], v[176:179]\n\t" \
  "s_waitcnt lgkmcnt(0)\n\t" \
  "v_mfma_f32_16x16x32_bf16 v[156:159], v[76:79], v[96:99], v[156:159]\n\t" \
  "v_mfma_f32_16x16x32_bf16 v[164:167], v[76:79], v[84:87], v[164:167]\n\t" \
  "ds_write_b32 v32, v0\n\t" \
  "v_mfma_f32_16x16x32_bf16 v[172:175], v[76:79], v[92:95], v[172:175]\n\t" \
  "v_mfma_f32_16x16x32_bf16 v[180:183], v[76:79], v[100:103], v[180:183]\n\t" \
  "v_mfma_f32_16x16x32_bf16 v[184:187], v[120:123], v[116:119], v[184:187]\n\t" \
  "v_and_b32 v56, -16, v32\n\t" \
  "v_mfma_f32_16x16x32_bf16 v[192:195], v[120:123], v[136:139], v[192:195]\n\t" \
  "v_mfma_f32_16x16x32_bf16 v[200:203], v[120:123], v[108:111], v[200:203]\n\t" \
  "v_mfma_f32_16x16x32_bf16 v[208:211], v[120:123], v[144:147], v[208:211]\n\t" \
  "v_mfma_f32_16x16x32_bf16 v[188:191], v[140:143], v[116:119], v[188:191]\n\t" \
  "v_mfma_f32_16x16x32_bf16 v[196:199], v[140:143], v[136:139], v[196:199]\n\t" \
  "v_mfma_f32_16x16x32_bf16 v[204:207], v[140:143], v[108:111], v[204:207]\n\t" \
  "v_mfma_f32_16x16x32_bf16 v[212:215], v[140:143], v[144:147], v[212:215]\n\t" \
  "v_cvt_pk_bf16_f32 v14, v184, v185\n\t" \
  "v_cvt_pk_bf16_f32 v15, v186, v187\n\t" \
  "v_mfma_f32_16x16x32_bf16 v[152:155], v[112:115], v[116:119], v[152:155]\n\t" \
  "v_cvt_pk_bf16_f32 v18, v192, v193\n\t" \
  "v_cvt_pk_bf16_f32 v19, v194, v195\n\t" \
  "v_mfma_f32_16x16x32_bf16 v[160:163], v[112:115], v[136:139], v[160:163]\n\t" \
  "v_cvt_pk_bf16_f32 v16, v188, v189\n\t" \
  "v_cvt_pk_bf16_f32 v17, v190, v191\n\t" \
  "v_mfma_f32_16x16x32_bf16 v[168:171], v[112:115], v[108:111], v[168:171]\n\t" \
  "v_cvt_pk_bf16_f32 v20, v196, v197\n\t" \
  "v_cvt_pk_bf16_f32 v21, v198, v199\n\t" \
  "v_mfma_f32_16x16x32_bf16 v[176:179], v[112:115], v[144:147], v[176:179]\n\t" \
  "v_cvt_pk_bf16_f32 v22, v200, v201\n\t" \
  "v_cvt_pk_bf16_f32 v23, v202, v203\n\t" \
  "v_mfma_f32_16x16x32_bf16 v[156:159], v[132:135], v[116:119], v[156:159]\n\t" \
  "v_cvt_pk_bf16_f32 v24, v204, v205\n\t" \
  "v_cvt_pk_bf16_f32 v25, v206, v207\n\t" \
  "v_mfma_f32_16x16x32_bf16 v[164:167], v[132:135], v[136:139], v[164:167]\n\t" \
  "v_cvt_pk_bf16_f32 v26, v208, v209\n\t" \
  "v_cvt_pk_bf16_f32 v27, v210, v211\n\t" \
  "v_mfma_f32_16x16x32_bf16 v[172:175], v[132:135], v[108:111], v[172:175]\n\t" \
  "v_cvt_pk_bf16_f32 v28, v212, v213\n\t" \
  "v_cvt_pk_bf16_f32 v29, v214, v215\n\t" \
  "v_mfma_f32_16x16x32_bf16 v[180:183], v[132:135], v[144:147], v[180:183]\n\t" \
  "v_cvt_pk_bf16_f32 v84, v152, v153\n\t" \
  "v_cvt_pk_bf16_f32 v85, v154, v155\n\t" \
  "v_mfma_f32_16x16x32_bf16 v[248:251], v[124:127], v[128:131], v[248:251]\n\t" \
  "v_cvt_pk_bf16_f32 v86, v156, v157\n\t" \
  "v_cvt_pk_bf16_f32 v87, v158, v159\n\t" \
  "v_mfma_f32_16x16x32_bf16 v[216:219], v[116:119], v[128:131], v[216:219]\n\t" \
  "v_cvt_pk_bf16_f32 v88, v160, v161\n\t" \
  "v_cvt_pk_bf16_f32 v89, v162, v163\n\t" \
  "v_mfma_f32_16x16x32_bf16 v[224:227], v[136:139], v[128:131], v[224:227]\n\t" \
  "v_cvt_pk_bf16_f32 v90, v164, v165\n\t" \
  "v_cvt_pk_bf16_f32 v91, v166, v167\n\t" \
  "v_mfma_f32_16x16x32_bf16 v[232:235], v[108:111], v[128:131], v[232:235]\n\t" \
  "v_cvt_pk_bf16_f32 v72, v248, v249\n\t" \
  "v_cvt_pk_bf16_f32 v73, v250, v251\n\t" \
  "v_mfma_f32_16x16x32_bf16 v[240:243], v[144:147], v[128:131], v[240:243]\n\t" \
  "ds_read_b32 v52, v33 offset:48\n\t" \
  "ds_read_b128 v[10:13], v31\n\t" \
  "v_cvt_pk_bf16_f32 v92, v168, v169\n\t" \
  "v_mfma_f32_16x16x32_bf16 v[252:255], v[124:127], v[148:151], v[252:255]\n\t" \
  "v_cvt_pk_bf16_f32 v93, v170, v171\n\t" \
  "v_cvt_pk_bf16_f32 v94, v172, v173\n\t" \
  "v_mfma_f32_16x16x32_bf16 v[220:223], v[116:119], v[148:151], v[220:223]\n\t" \
  "v_cvt_pk_bf16_f32 v95, v174, v175\n\t" \
  "v_cvt_pk_bf16_f32 v96, v176, v177\n\t" \
  "v_mfma_f32_16x16x32_bf16 v[228:231], v[136:139], v[148:151], v[228:231]\n\t" \
  "v_cvt_pk_bf16_f32 v97, v178, v179\n\t" \
  "v_cvt_pk_bf16_f32 v98, v180, v181\n\t" \
  "v_mfma_f32_16x16x32_bf16 v[236:239], v[108:111], v[148:151], v[236:239]\n\t" \
  "v_cvt_pk_bf16_f32 v82, v252, v253\n\t" \
  "v_cvt_pk_bf16_f32 v83, v254, v255\n\t" \
  "v_mfma_f32_16x16x32_bf16 v[244:247], v[144:147], v[148:151], v[244:247]\n\t" \
  "ds_write_b64 v30, v[72:73]\n\t" \
  "ds_write_b64 v30, v[82:83] offset:8\n\t" \
  "ds_write_b32 v32, v0 offset:48\n\t" \
  "v_mfma_f32_16x16x32_bf16 v[200:203], v[22:25], v[92:95], v[34:37]\n\t" \
  "v_cvt_pk_bf16_f32 v99, v182, v183\n\t" \
  "s_waitcnt lgkmcnt(0)\n\t" \
  "v_readfirstlane_b32 vcc_lo, v52\n\t" \
  "s_nop 1\n\t" \
  "s_cmp_eq_u32 vcc_lo, 0\n\t" \
  "s_cbranch_scc1 5f\n\t" \
  "4:\n\t" \
  "v_mfma_f32_16x16x32_bf16 v[204:207], v[18:21], v[92:95], v[38:41]\n\t" \
  "v_cvt_pk_bf16_f32 v66, v232, v233\n\t" \
  "v_cvt_pk_bf16_f32 v67, v234, v235\n\t" \
  "v_mfma_f32_16x16x32_bf16 v[208:211], v[26:29], v[96:99], v[34:37]\n\t" \
  "v_cvt_pk_bf16_f32 v76, v236, v237\n\t" \
  "v_cvt_pk_bf16_f32 v77, v238, v239\n\t" \
  "v_mfma_f32_16x16x32_bf16 v[212:215], v[22:25], v[96:99], v[38:41]\n\t" \
  "v_cvt_pk_bf16_f32 v64, v240, v241\n\t" \
  "v_cvt_pk_bf16_f32 v65, v242, v243\n\t" \
  "v_mfma_f32_16x16x32_bf16 v[192:195], v[18:21], v[88:91], v[34:37]\n\t" \
  "v_max_f32 v200, v200, v204\n\t" \
  "v_max_f32 v201, v201, v205\n\t" \
  "v_mfma_f32_16x16x32_bf16 v[196:199], v[14:17], v[88:91], v[38:41]\n\t" \
  "v_max_f32 v202, v202, v206\n\t" \
  "v_max_f32 v203, v203, v207\n\t" \
  "v_mfma_f32_16x16x32_bf16 v[184:187], v[14:17], v[84:87], v[34:37]\n\t" \
  "v_max_f32 v208, v208, v212\n\t" \
  "v_max_f32 v209, v209, v213\n\t" \
  "v_mfma_f32_16x16x32_bf16 v[188:191], v[10:13], v[84:87], v[42:45]\n\t" \
  "v_max_f32 v210, v210, v214\n\t" \
  "v_max_f32 v211, v211, v215\n\t" \
  "v_max_f32 v192, v192, v196\n\t" \
  "v_max_f32 v193, v193, v197\n\t" \
  "v_max_f32 v194, v194, v198\n\t" \
  "v_max3_f32 v106, v208, v209, v210\n\t" \
  "v_max3_f32 v104, v200, v201, v202\n\t" \
  "v_max_f32 v195, v195, v199\n\t" \
  "v_max_f32 v184, v184, v188\n\t" \
  "v_max_f32 v185, v185, v189\n\t" \
  "v_max_f32 v186, v186, v190\n\t" \
  "v_max3_f32 v102, v192, v193, v194\n\t" \
  "v_max_f32 v187, v187, v191\n\t" \
  "v_max3_f32 v100, v184, v185, v186\n\t" \
  "v_max_f32 v106, v106, v211\n\t" \
  "v_max_f32 v104, v104, v203\n\t" \
  "v_max_f32 v102, v102, v195\n\t" \
  "v_max_f32 v100, v100, v187\n\t" \
  "v_permlane16_swap_b32 v104, v106\n\t" \
  "v_cvt_pk_bf16_f32 v74, v244, v245\n\t" \
  "v_permlane16_swap_b32 v100, v102\n\t" \
  "v_max_f32 v104, v104, v106\n\t" \
  "v_max_f32 v100, v100, v102\n\t" \
  "v_cvt_pk_bf16_f32 v75, v246, v247\n\t" \
  "v_cvt_pk_bf16_f32 v68, v224, v225\n\t" \
  "v_permlane32_swap_b32 v100, v104\n\t" \
  "v_cvt_pk_bf16_f32 v69, v226, v227\n\t" \
  "v_max_f32 v100, v100, v104\n\t" \
  "v_mul_f32 v100, 0xbe8293ee, v100\n\t" \
  "v_mov_b32 v104, v100\n\t" \
  "v_cvt_pk_bf16_f32 v78, v228, v229\n\t" \
  "v_cvt_pk_bf16_f32 v79, v230, v231\n\t" \
  "v_permlane32_swap_b32 v100, v104\n\t" \
  "v_cvt_pk_bf16_f32 v70, v216, v217\n\t" \
  "v_mov_b32 v106, v104\n\t" \
  "v_mov_b32 v102, v100\n\t" \
  "v_cvt_pk_bf16_f32 v71, v218, v219\n\t" \
  "v_permlane16_swap_b32 v104, v106\n\t" \
  "v_permlane16_swap_b32 v100, v102\n\t" \
  "v_fma_f32 v208, v208, %16, v106\n\t" \
  "v_fma_f32 v209, v209, %16, v106\n\t" \
  "v_fma_f32 v210, v210, %16, v106\n\t" \
  "v_fma_f32 v211, v211, %16, v106\n\t" \
  "v_exp_f32 v208, v208\n\t" \
  "v_exp_f32 v209, v209\n\t" \
  "v_exp_f32 v210, v210\n\t" \
  "v_exp_f32 v211, v211\n\t" \
  "v_cvt_pk_bf16_f32 v212, v208, v209\n\t" \
  "v_cvt_pk_bf16_f32 v213, v210, v211\n\t" \
  "v_bfi_b32 v214, v46, 0, v212\n\t" \
  "v_bfi_b32 v215, v47, 0, v213\n\t" \
  "v_and_b32 v212, v46, v212\n\t" \
  "v_and_b32 v213, v47, v213\n\t" \
  "v_fma_f32 v200, v200, %16, v104\n\t" \
  "v_fma_f32 v201, v201, %16, v104\n\t" \
  "v_mfma_f32_16x16x32_bf16 v[208:211], v[48:51], v[212:215], 0\n\t" \
  "v_fma_f32 v202, v202, %16, v104\n\t" \
  "v_fma_f32 v203, v203, %16, v104\n\t" \
  "v_mfma_f32_16x16x32_bf16 v[176:179], v[64:67], v[212:215], 0\n\t" \
  "v_exp_f32 v200, v200\n\t" \
  "v_mfma_f32_16x16x32_bf16 v[180:183], v[74:77], v[212:215], 0\n\t" \
  "v_exp_f32 v201, v201\n\t" \
  "v_exp_f32 v202, v202\n\t" \
  "v_exp_f32 v203, v203\n\t" \
  "v_cvt_pk_bf16_f32 v204, v200, v201\n\t" \
  "v_cvt_pk_bf16_f32 v205, v202, v203\n\t" \
  "v_bfi_b32 v206, v46, 0, v204\n\t" \
  "v_bfi_b32 v207, v47, 0, v205\n\t" \
  "v_rcp_f32 v106, v208\n\t" \
  "v_and_b32 v204, v46, v204\n\t" \
  "v_and_b32 v205, v47, v205\n\t" \
  "v_pk_mul_f32 v[176:177], v[176:177], v[106:107] op_sel_hi:[1,0]\n\t" \
  "v_pk_mul_f32 v[178:179], v[178:179], v[106:107] op_sel_hi:[1,0]\n\t" \
  "v_mfma_f32_16x16x32_bf16 v[200:203], v[48:51], v[204:207], 0\n\t" \
  "v_pk_mul_f32 v[180:181], v[180:181], v[106:107] op_sel_hi:[1,0]\n\t" \
  "v_pk_mul_f32 v[182:183], v[182:183], v[106:107] op_sel_hi:[1,0]\n\t" \
  "v_mfma_f32_16x16x32_bf16 v[168:171], v[66:69], v[204:207], 0\n\t" \
  "v_cvt_pk_bf16_f32 v240, v176, v177\n\t" \
  "v_cvt_pk_bf16_f32 v241, v178, v179\n\t" \
  "v_mfma_f32_16x16x32_bf16 v[172:175], v[76:79], v[204:207], 0\n\t" \
  "v_cvt_pk_bf16_f32 v242, v180, v181\n\t" \
  "v_cvt_pk_bf16_f32 v243, v182, v183\n\t" \
  "global_store_dwordx4 v1, v[240:243], %14 offset:0 sc1\n\t" \
  "v_fma_f32 v192, v192, %16, v102\n\t" \
  "v_fma_f32 v193, v193, %16, v102\n\t" \
  "v_fma_f32 v194, v194, %16, v102\n\t" \
  "v_fma_f32 v195, v195, %16, v102\n\t" \
  "v_exp_f32 v192, v192\n\t" \
  "v_exp_f32 v193, v193\n\t" \
  "v_exp_f32 v194, v194\n\t" \
  "v_exp_f32 v195, v195\n\t" \
  "v_cvt_pk_bf16_f32 v196, v192, v193\n\t" \
  "v_cvt_pk_bf16_f32 v197, v194, v195\n\t" \
  "v_bfi_b32 v198, v46, 0, v196\n\t" \
  "v_bfi_b32 v199, v47, 0, v197\n\t" \
  "v_rcp_f32 v104, v200\n\t" \
  "v_and_b32 v196, v46, v196\n\t" \
  "v_and_b32 v197, v47, v197\n\t" \
  "v_cvt_pk_bf16_f32 v80, v220, v221\n\t" \
  "v_cvt_pk_bf16_f32 v81, v222, v223\n\t" \
  "v_mfma_f32_16x16x32_bf16 v[192:195], v[48:51], v[196:199], 0\n\t" \
  "v_pk_mul_f32 v[168:169], v[168:169], v[104:105] op_sel_hi:[1,0]\n\t" \
  "v_pk_mul_f32 v[170:171], v[170:171], v[104:105] op_sel_hi:[1,0]\n\t" \
  "v_mfma_f32_16x16x32_bf16 v[160:163], v[68:71], v[196:199], 0\n\t" \
  "v_pk_mul_f32 v[172:173], v[172:173], v[104:105] op_sel_hi:[1,0]\n\t" \
  "v_pk_mul_f32 v[174:175], v[174:175], v[104:105] op_sel_hi:[1,0]\n\t" \
  "v_mfma_f32_16x16x32_bf16 v[164:167], v[78:81], v[196:199], 0\n\t" \
  "v_cvt_pk_bf16_f32 v232, v168, v169\n\t" \
  "v_cvt_pk_bf16_f32 v233, v170, v171\n\t" \
  "v_cvt_pk_bf16_f32 v234, v172, v173\n\t" \
  "v_cvt_pk_bf16_f32 v235, v174, v175\n\t" \
  "global_store_dwordx4 v1, v[232:235], %14 offset:-4096 sc1\n\t" \
  "v_fma_f32 v184, v184, %16, v100\n\t" \
  "v_fma_f32 v185, v185, %16, v100\n\t" \
  "v_fma_f32 v186, v186, %16, v100\n\t" \
  "v_fma_f32 v187, v187, %16, v100\n\t" \
  "v_exp_f32 v184, v184\n\t" \
  "v_exp_f32 v185, v185\n\t" \
  "v_exp_f32 v186, v186\n\t" \
  "v_exp_f32 v187, v187\n\t" \
  "v_cvt_pk_bf16_f32 v188, v184, v185\n\t" \
  "v_cvt_pk_bf16_f32 v189, v186, v187\n\t" \
  "v_bfi_b32 v190, v46, 0, v188\n\t" \
  "v_bfi_b32 v191, v47, 0, v189\n\t" \
  "v_rcp_f32 v102, v192\n\t" \
  "v_and_b32 v188, v46, v188\n\t" \
  "v_and_b32 v189, v47, v189\n\t" \
  "v_pk_mul_f32 v[160:161], v[160:161], v[102:103] op_sel_hi:[1,0]\n\t" \
  "v_pk_mul_f32 v[162:163], v[162:163], v[102:103] op_sel_hi:[1,0]\n\t" \
  "v_mfma_f32_16x16x32_bf16 v[184:187], v[48:51], v[188:191], 0\n\t" \
  "v_pk_mul_f32 v[164:165], v[164:165], v[102:103] op_sel_hi:[1,0]\n\t" \
  "v_pk_mul_f32 v[166:167], v[166:167], v[102:103] op_sel_hi:[1,0]\n\t" \
  "v_mfma_f32_16x16x32_bf16 v[152:155], v[70:73], v[188:191], 0\n\t" \
  "v_cvt_pk_bf16_f32 v224, v160, v161\n\t" \
  "v_cvt_pk_bf16_f32 v225, v162, v163\n\t" \
  "v_mfma_f32_16x16x32_bf16 v[156:159], v[80:83], v[188:191], 0\n\t" \
  "v_cvt_pk_bf16_f32 v226, v164, v165\n\t" \
  "v_cvt_pk_bf16_f32 v227, v166, v167\n\t" \
  "global_store_dwordx4 v4, v[224:227], %14 offset:0 sc1\n\t" \
  "v_rcp_f32 v100, v184\n\t" \
  "ds_read_b128 v[60:63], v56 offset:64\n\t" \
  "ds_read_b64_tr_b16 v[108:109], v5 offset:0\n\t" \
  "ds_read_b64_tr_b16 v[110:111], v5 offset:16\n\t" \
  "ds_read_b128 v[112:115], v6 offset:16384\n\t" \
  "ds_read_b128 v[116:119], v6 offset:15360\n\t" \
  "ds_read_b64_tr_b16 v[120:121], v5 offset:512\n\t" \
  "ds_read_b64_tr_b16 v[122:123], v5 offset:528\n\t" \
  "ds_read_b128 v[124:127], v6 offset:14336\n\t" \
  "ds_read_b64_tr_b16 v[128:129], v5 offset:1024\n\t" \
  "ds_read_b64_tr_b16 v[130:131], v5 offset:1040\n\t" \
  "ds_read_b128 v[132:135], v6 offset:13312\n\t" \
  "ds_read_b64_tr_b16 v[136:137], v5 offset:1536\n\t" \
  "ds_read_b64_tr_b16 v[138:139], v5 offset:1552\n\t" \
  "ds_read_b128 v[140:143], v6 offset:12288\n\t" \
  "ds_read_b64_tr_b16 v[144:145], v5 offset:2048\n\t" \
  "v_pk_mul_f32 v[152:153], v[152:153], v[100:101] op_sel_hi:[1,0]\n\t" \
  "v_pk_mul_f32 v[154:155], v[154:155], v[100:101] op_sel_hi:[1,0]\n\t" \
  "v_cvt_pk_bf16_f32 v216, v152, v153\n\t" \
  "v_pk_mul_f32 v[156:157], v[156:157], v[100:101] op_sel_hi:[1,0]\n\t" \
  "v_pk_mul_f32 v[158:159], v[158:159], v[100:101] op_sel_hi:[1,0]\n\t" \
  "v_cvt_pk_bf16_f32 v217, v154, v155\n\t" \
  "v_cvt_pk_bf16_f32 v218, v156, v157\n\t" \
  "v_cvt_pk_bf16_f32 v219, v158, v159\n\t" \
  "global_store_dwordx4 v4, v[216:219], %14 offset:-4096 sc1\n\t" \
  "ds_write_b32 v32, v0 offset:112\n\t" \
  "s_branch 9f\n\t" \
  "5:\n\t" \
  "ds_read_b32 v52, v33 offset:48\n\t" \
  "ds_read_b128 v[10:13], v31\n\t" \
  "s_waitcnt lgkmcnt(0)\n\t" \
  "v_readfirstlane_b32 vcc_lo, v52\n\t" \
  "s_nop 1\n\t" \
  "s_cmp_eq_u32 vcc_lo, 0\n\t" \
  "s_cbranch_scc1 5b\n\t" \
  "s_branch 4b\n\t" \
  "15:\n\t" \
  "ds_read_b128 v[10:13], v14 offset:128\n\t" \
  "s_waitcnt lgkmcnt(0)\n\t" \
  "v_and_b32 v10, v10, v11\n\t" \
  "v_and_b32 v12, v12, v13\n\t" \
  "v_and_b32 v10, v10, v12\n\t" \
  "s_nop 1\n\t" \
  "v_readfirstlane_b32 vcc_lo, v10\n\t" \
  "s_nop 1\n\t" \
  "s_cmp_eq_u32 vcc_lo, 0\n\t" \
  "s_cbranch_scc1 15b\n\t" \
  "s_branch 14b\n\t" \
  "8:\n\t" \
  "global_load_dwordx2 v[10:11], v51, %11\n\t" \
  "global_load_dwordx2 v[12:13], v51, %11 offset:128\n\t" \
  "v_xor_b32 v53, 64, v52\n\t" \
  "v_xor_b32 v55, 32, v54\n\t" \
  "v_add_u32 v56, 43008, v52\n\t" \
  "v_add_u32 v58, 43008, v54\n\t" \
  "v_xor_b32 v57, 64, v56\n\t" \
  "v_xor_b32 v59, 32, v58\n\t" \
  "v_add_u32 v60, 86016, v52\n\t" \
  "v_add_u32 v62, 86016, v54\n\t" \
  "v_xor_b32 v61, 64, v60\n\t" \
  "v_xor_b32 v63, 32, v62\n\t" \
  "s_mov_b32 vcc_hi, 0x05040100\n\t" \
  "s_waitcnt vmcnt(2)\n\t" \
  "s_barrier\n\t" \
  "ds_read_b64_tr_b16 v[64:65], v62 offset:34816\n\t" \
  "ds_read_b64_tr_b16 v[66:67], v62 offset:35328\n\t" \
  "ds_read_b128 v[68:71], v60 offset:2048\n\t" \
  "ds_read_b64_tr_b16 v[72:73], v62 offset:38912\n\t" \
  "ds_read_b64_tr_b16 v[74:75], v62 offset:39424\n\t" \
  "ds_read_b128 v[76:79], v61 offset:2048\n\t" \
  "ds_read_b128 v[80:83], v61 offset:4096\n\t" \
  "ds_read_b128 v[84:87], v60 offset:4096\n\t" \
  "ds_read_b128 v[88:91], v60 offset:6144\n\t" \
  "ds_read_b128 v[92:95], v61 offset:6144\n\t" \
  "ds_read_b128 v[96:99], v61 offset:8192\n\t" \
  "ds_read_b128 v[100:103], v60 offset:8192\n\t" \
  "ds_read_b64_tr_b16 v[104:105], v63 offset:34816\n\t" \
  "ds_read_b64_tr_b16 v[106:107], v63 offset:35328\n\t" \
  "ds_read_b64_tr_b16 v[108:109], v63 offset:38912\n\t" \
  "ds_read_b64_tr_b16 v[110:111], v63 offset:39424\n\t" \
  "s_waitcnt lgkmcnt(13)\n\t" \
  "v_mfma_f32_16x16x32_bf16 v[216:219], v[68:71], v[64:67], 0\n\t" \
  "ds_read_b64_tr_b16 v[112:113], v62 offset:26624\n\t" \
  "ds_read_b64_tr_b16 v[114:115], v62 offset:27136\n\t" \
  "s_waitcnt lgkmcnt(12)\n\t" \
  "v_mfma_f32_16x16x32_bf16 v[216:219], v[76:79], v[72:75], v[216:219]\n\t" \
  "ds_read_b64_tr_b16 v[116:117], v62 offset:30720\n\t" \
  "ds_read_b64_tr_b16 v[118:119], v62 offset:31232\n\t" \
  "s_waitcnt lgkmcnt(13)\n\t" \
  "v_mfma_f32_16x16x32_bf16 v[224:227], v[80:83], v[72:75], 0\n\t" \
  "ds_read_b128 v[120:123], v60 offset:0\n\t" \
  "s_waitcnt lgkmcnt(13)\n\t" \
  "v_mfma_f32_16x16x32_bf16 v[224:227], v[84:87], v[64:67], v[224:227]\n\t" \
  "ds_read_b128 v[124:127], v61 offset:0\n\t" \
  "s_waitcnt lgkmcnt(13)\n\t" \
  "v_mfma_f32_16x16x32_bf16 v[232:235], v[88:91], v[64:67], 0\n\t" \
  "ds_read_b64_tr_b16 v[128:129], v63 offset:26624\n\t" \
  "s_waitcnt lgkmcnt(13)\n\t" \
  "v_mfma_f32_16x16x32_bf16 v[232:235], v[92:95], v[72:75], v[232:235]\n\t" \
  "ds_read_b64_tr_b16 v[130:131], v63 offset:27136\n\t" \
  "s_waitcnt lgkmcnt(13)\n\t" \
  "v_mfma_f32_16x16x32_bf16 v[240:243], v[96:99], v[72:75], 0\n\t" \
  "ds_read_b64_tr_b16 v[132:133], v63 offset:30720\n\t" \
  "s_waitcnt lgkmcnt(9)\n\t" \
  "v_mfma_f32_16x16x32_bf16 v[240:243], v[100:103], v[64:67], v[240:243]\n\t" \
  "ds_read_b64_tr_b16 v[134:135], v63 offset:31232\n\t" \
  "v_mfma_f32_16x16x32_bf16 v[220:223], v[68:71], v[104:107], 0\n\t" \
  "ds_read_b64_tr_b16 v[136:137], v62 offset:18432\n\t" \
  "v_mfma_f32_16x16x32_bf16 v[220:223], v[76:79], v[108:111], v[220:223]\n\t" \
  "ds_read_b64_tr_b16 v[138:139], v62 offset:18944\n\t" \
  "v_mfma_f32_16x16x32_bf16 v[228:231], v[80:83], v[108:111], 0\n\t" \
  "ds_read_b64_tr_b16 v[140:141], v62 offset:22528\n\t" \
  "v_mfma_f32_16x16x32_bf16 v[228:231], v[84:87], v[104:107], v[228:231]\n\t" \
  "ds_read_b64_tr_b16 v[142:143], v62 offset:23040\n\t" \
  "v_mfma_f32_16x16x32_bf16 v[236:239], v[88:91], v[104:107], 0\n\t" \
  "ds_read_b64_tr_b16 v[144:145], v63 offset:18432\n\t" \
  "v_mfma_f32_16x16x32_bf16 v[236:239], v[92:95], v[108:111], v[236:239]\n\t" \
  "ds_read_b64_tr_b16 v[146:147], v63 offset:18944\n\t" \
  "v_mfma_f32_16x16x32_bf16 v[244:247], v[96:99], v[108:111], 0\n\t" \
  "ds_read_b64_tr_b16 v[148:149], v63 offset:22528\n\t" \
  "v_mfma_f32_16x16x32_bf16 v[244:247], v[100:103], v[104:107], v[244:247]\n\t" \
  "ds_read_b64_tr_b16 v[150:151], v63 offset:23040\n\t" \
  "s_waitcnt lgkmcnt(7)\n\t" \
  "v_mfma_f32_16x16x32_bf16 v[184:187], v[112:115], v[68:71], 0\n\t" \
  "v_mfma_f32_16x16x32_bf16 v[184:187], v[116:119], v[76:79], v[184:187]\n\t" \
  "v_mfma_f32_16x16x32_bf16 v[192:195], v[116:119], v[80:83], 0\n\t" \
  "v_mfma_f32_16x16x32_bf16 v[192:195], v[112:115], v[84:87], v[192:195]\n\t" \
  "v_mfma_f32_16x16x32_bf16 v[200:203], v[112:115], v[88:91], 0\n\t" \
  "v_mfma_f32_16x16x32_bf16 v[200:203], v[116:119], v[92:95], v[200:203]\n\t" \
  "v_mfma_f32_16x16x32_bf16 v[208:211], v[116:119], v[96:99], 0\n\t" \
  "v_mfma_f32_16x16x32_bf16 v[208:211], v[112:115], v[100:103], v[208:211]\n\t" \
  "v_mfma_f32_16x16x32_bf16 v[248:251], v[112:115], v[120:123], 0\n\t" \
  "v_mfma_f32_16x16x32_bf16 v[248:251], v[116:119], v[124:127], v[248:251]\n\t" \
  "v_mfma_f32_16x16x32_bf16 v[252:255], v[128:131], v[120:123], 0\n\t" \
  "v_mfma_f32_16x16x32_bf16 v[252:255], v[132:135], v[124:127], v[252:255]\n\t" \
  "v_mfma_f32_16x16x32_bf16 v[188:191], v[132:135], v[76:79], 0\n\t" \
  "v_mfma_f32_16x16x32_bf16 v[188:191], v[128:131], v[68:71], v[188:191]\n\t" \
  "s_waitcnt lgkmcnt(0)\n\t" \
  "s_getpc_b64 s[88:89]\n\t" \
  "s_branch 28b\n\t" \
  "s_barrier\n\t" \
  "v_mfma_f32_16x16x32_bf16 v[196:199], v[128:131], v[84:87], 0\n\t" \
  "ds_read_b64_tr_b16 v[72:73], v54 offset:34816\n\t" \
  "v_mfma_f32_16x16x32_bf16 v[196:199], v[132:135], v[80:83], v[196:199]\n\t" \
  "ds_read_b64_tr_b16 v[74:75], v54 offset:35328\n\t" \
  "v_mfma_f32_16x16x32_bf16 v[204:207], v[132:135], v[92:95], 0\n\t" \
  "ds_read_b128 v[64:67], v52 offset:2048\n\t" \
  "v_mfma_f32_16x16x32_bf16 v[204:207], v[128:131], v[88:91], v[204:207]\n\t" \
  "ds_read_b64_tr_b16 v[108:109], v54 offset:38912\n\t" \
  "v_mfma_f32_16x16x32_bf16 v[212:215], v[128:131], v[100:103], 0\n\t" \
  "ds_read_b64_tr_b16 v[110:111], v54 offset:39424\n\t" \
  "v_mfma_f32_16x16x32_bf16 v[212:215], v[132:135], v[96:99], v[212:215]\n\t" \
  "ds_read_b128 v[104:107], v53 offset:2048\n\t" \
  "v_mfma_f32_16x16x32_bf16 v[152:155], v[136:139], v[68:71], 0\n\t" \
  "ds_read_b128 v[112:115], v53 offset:4096\n\t" \
  "v_mfma_f32_16x16x32_bf16 v[152:155], v[140:143], v[76:79], v[152:155]\n\t" \
  "ds_read_b128 v[116:119], v52 offset:4096\n\t" \
  "v_mfma_f32_16x16x32_bf16 v[160:163], v[140:143], v[80:83], 0\n\t" \
  "ds_read_b128 v[120:123], v52 offset:6144\n\t" \
  "v_mfma_f32_16x16x32_bf16 v[160:163], v[136:139], v[84:87], v[160:163]\n\t" \
  "ds_read_b128 v[124:127], v53 offset:6144\n\t" \
  "v_mfma_f32_16x16x32_bf16 v[168:171], v[136:139], v[88:91], 0\n\t" \
  "ds_read_b128 v[128:131], v53 offset:8192\n\t" \
  "v_mfma_f32_16x16x32_bf16 v[168:171], v[140:143], v[92:95], v[168:171]\n\t" \
  "ds_read_b128 v[132:135], v52 offset:8192\n\t" \
  "v_mfma_f32_16x16x32_bf16 v[176:179], v[140:143], v[96:99], 0\n\t" \
  "ds_read_b64_tr_b16 v[140:141], v55 offset:34816\n\t" \
  "v_mfma_f32_16x16x32_bf16 v[176:179], v[136:139], v[100:103], v[176:179]\n\t" \
  "ds_read_b64_tr_b16 v[142:143], v55 offset:35328\n\t" \
  "v_mfma_f32_16x16x32_bf16 v[156:159], v[144:147], v[68:71], 0\n\t" \
  "ds_read_b64_tr_b16 v[136:137], v55 offset:38912\n\t" \
  "v_mfma_f32_16x16x32_bf16 v[156:159], v[148:151], v[76:79], v[156:159]\n\t" \
  "ds_read_b64_tr_b16 v[138:139], v55 offset:39424\n\t" \
  "v_mfma_f32_16x16x32_bf16 v[164:167], v[148:151], v[80:83], 0\n\t" \
  "ds_read_b64_tr_b16 v[68:69], v54 offset:26624\n\t" \
  "v_mfma_f32_16x16x32_bf16 v[164:167], v[144:147], v[84:87], v[164:167]\n\t" \
  "ds_read_b64_tr_b16 v[70:71], v54 offset:27136\n\t" \
  "v_mfma_f32_16x16x32_bf16 v[172:175], v[144:147], v[88:91], 0\n\t" \
  "ds_read_b64_tr_b16 v[76:77], v54 offset:30720\n\t" \
  "v_mfma_f32_16x16x32_bf16 v[172:175], v[148:151], v[92:95], v[172:175]\n\t" \
  "ds_read_b64_tr_b16 v[78:79], v54 offset:31232\n\t" \
  "v_mfma_f32_16x16x32_bf16 v[180:183], v[148:151], v[96:99], 0\n\t" \
  "ds_read_b128 v[80:83], v52 offset:0\n\t" \
  "v_mfma_f32_16x16x32_bf16 v[180:183], v[144:147], v[100:103], v[180:183]\n\t" \
  "ds_read_b128 v[84:87], v53 offset:0\n\t" \
  "s_waitcnt lgkmcnt(7)\n\t" \
  "v_mfma_f32_16x16x32_bf16 v[216:219], v[64:67], v[72:75], v[216:219]\n\t" \
  "ds_read_b64_tr_b16 v[88:89], v55 offset:26624\n\t" \
  "v_mfma_f32_16x16x32_bf16 v[216:219], v[104:107], v[108:111], v[216:219]\n\t" \
  "ds_read_b64_tr_b16 v[90:91], v55 offset:27136\n\t" \
  "v_mfma_f32_16x16x32_bf16 v[224:227], v[112:115], v[108:111], v[224:227]\n\t" \
  "ds_read_b64_tr_b16 v[92:93], v55 offset:30720\n\t" \
  "v_mfma_f32_16x16x32_bf16 v[224:227], v[116:119], v[72:75], v[224:227]\n\t" \
  "ds_read_b64_tr_b16 v[94:95], v55 offset:31232\n\t" \
  "v_mfma_f32_16x16x32_bf16 v[232:235], v[120:123], v[72:75], v[232:235]\n\t" \
  "ds_read_b64_tr_b16 v[96:97], v54 offset:18432\n\t" \
  "v_mfma_f32_16x16x32_bf16 v[232:235], v[124:127], v[108:111], v[232:235]\n\t" \
  "ds_read_b64_tr_b16 v[98:99], v54 offset:18944\n\t" \
  "v_mfma_f32_16x16x32_bf16 v[240:243], v[128:131], v[108:111], v[240:243]\n\t" \
  "ds_read_b64_tr_b16 v[148:149], v54 offset:22528\n\t" \
  "v_mfma_f32_16x16x32_bf16 v[240:243], v[132:135], v[72:75], v[240:243]\n\t" \
  "ds_read_b64_tr_b16 v[150:151], v54 offset:23040\n\t" \
  "v_mfma_f32_16x16x32_bf16 v[220:223], v[64:67], v[140:143], v[220:223]\n\t" \
  "ds_read_b64_tr_b16 v[100:101], v55 offset:18432\n\t" \
  "s_waitcnt lgkmcnt(7)\n\t" \
  "v_mfma_f32_16x16x32_bf16 v[220:223], v[104:107], v[136:139], v[220:223]\n\t" \
  "ds_read_b64_tr_b16 v[102:103], v55 offset:18944\n\t" \
  "v_mfma_f32_16x16x32_bf16 v[228:231], v[112:115], v[136:139], v[228:231]\n\t" \
  "ds_read_b64_tr_b16 v[144:145], v55 offset:22528\n\t" \
  "v_mfma_f32_16x16x32_bf16 v[228:231], v[116:119], v[140:143], v[228:231]\n\t" \
  "ds_read_b64_tr_b16 v[146:147], v55 offset:23040\n\t" \
  "v_mfma_f32_16x16x32_bf16 v[236:239], v[120:123], v[140:143], v[236:239]\n\t" \
  "v_mfma_f32_16x16x32_bf16 v[236:239], v[124:127], v[136:139], v[236:239]\n\t" \
  "v_mfma_f32_16x16x32_bf16 v[244:247], v[128:131], v[136:139], v[244:247]\n\t" \
  "v_mfma_f32_16x16x32_bf16 v[244:247], v[132:135], v[140:143], v[244:247]\n\t" \
  "v_mfma_f32_16x16x32_bf16 v[184:187], v[68:71], v[64:67], v[184:187]\n\t" \
  "v_mfma_f32_16x16x32_bf16 v[184:187], v[76:79], v[104:107], v[184:187]\n\t" \
  "v_mfma_f32_16x16x32_bf16 v[192:195], v[76:79], v[112:115], v[192:195]\n\t" \
  "v_mfma_f32_16x16x32_bf16 v[192:195], v[68:71], v[116:119], v[192:195]\n\t" \
  "v_mfma_f32_16x16x32_bf16 v[200:203], v[68:71], v[120:123], v[200:203]\n\t" \
  "v_mfma_f32_16x16x32_bf16 v[200:203], v[76:79], v[124:127], v[200:203]\n\t" \
  "v_mfma_f32_16x16x32_bf16 v[208:211], v[76:79], v[128:131], v[208:211]\n\t" \
  "v_mfma_f32_16x16x32_bf16 v[208:211], v[68:71], v[132:135], v[208:211]\n\t" \
  "v_mfma_f32_16x16x32_bf16 v[248:251], v[68:71], v[80:83], v[248:251]\n\t" \
  "v_mfma_f32_16x16x32_bf16 v[248:251], v[76:79], v[84:87], v[248:251]\n\t" \
  "v_mfma_f32_16x16x32_bf16 v[252:255], v[88:91], v[80:83], v[252:255]\n\t" \
  "s_waitcnt lgkmcnt(0)\n\t" \
  "v_mfma_f32_16x16x32_bf16 v[252:255], v[92:95], v[84:87], v[252:255]\n\t" \
  "v_mfma_f32_16x16x32_bf16 v[188:191], v[92:95], v[104:107], v[188:191]\n\t" \
  "v_mfma_f32_16x16x32_bf16 v[188:191], v[88:91], v[64:67], v[188:191]\n\t" \
  "s_barrier\n\t" \
  "v_mfma_f32_16x16x32_bf16 v[196:199], v[88:91], v[116:119], v[196:199]\n\t" \
  "ds_read_b64_tr_b16 v[108:109], v58 offset:34816\n\t" \
  "v_mfma_f32_16x16x32_bf16 v[196:199], v[92:95], v[112:115], v[196:199]\n\t" \
  "ds_read_b64_tr_b16 v[110:111], v58 offset:35328\n\t" \
  "v_mfma_f32_16x16x32_bf16 v[204:207], v[92:95], v[124:127], v[204:207]\n\t" \
  "ds_read_b128 v[72:75], v56 offset:2048\n\t" \
  "v_mfma_f32_16x16x32_bf16 v[204:207], v[88:91], v[120:123], v[204:207]\n\t" \
  "ds_read_b64_tr_b16 v[136:137], v58 offset:38912\n\t" \
  "v_mfma_f32_16x16x32_bf16 v[212:215], v[88:91], v[132:135], v[212:215]\n\t" \
  "ds_read_b64_tr_b16 v[138:139], v58 offset:39424\n\t" \
  "v_mfma_f32_16x16x32_bf16 v[212:215], v[92:95], v[128:131], v[212:215]\n\t" \
  "ds_read_b128 v[140:143], v57 offset:2048\n\t" \
  "v_mfma_f32_16x16x32_bf16 v[152:155], v[96:99], v[64:67], v[152:155]\n\t" \
  "ds_read_b128 v[68:71], v57 offset:4096\n\t" \
  "v_mfma_f32_16x16x32_bf16 v[152:155], v[148:151], v[104:107], v[152:155]\n\t" \
  "ds_read_b128 v[76:79], v56 offset:4096\n\t" \
  "v_mfma_f32_16x16x32_bf16 v[160:163], v[148:151], v[112:115], v[160:163]\n\t" \
  "ds_read_b128 v[80:83], v56 offset:6144\n\t" \
  "v_mfma_f32_16x16x32_bf16 v[160:163], v[96:99], v[116:119], v[160:163]\n\t" \
  "ds_read_b128 v[84:87], v57 offset:6144\n\t" \
  "v_mfma_f32_16x16x32_bf16 v[168:171], v[96:99], v[120:123], v[168:171]\n\t" \
  "ds_read_b128 v[88:91], v57 offset:8192\n\t" \
  "v_mfma_f32_16x16x32_bf16 v[168:171], v[148:151], v[124:127], v[168:171]\n\t" \
  "ds_read_b128 v[92:95], v56 offset:8192\n\t" \
  "v_mfma_f32_16x16x32_bf16 v[176:179], v[148:151], v[128:131], v[176:179]\n\t" \
  "ds_read_b64_tr_b16 v[148:149], v59 offset:34816\n\t" \
  "v_mfma_f32_16x16x32_bf16 v[176:179], v[96:99], v[132:135], v[176:179]\n\t" \
  "ds_read_b64_tr_b16 v[150:151], v59 offset:35328\n\t" \
  "v_mfma_f32_16x16x32_bf16 v[156:159], v[100:103], v[64:67], v[156:159]\n\t" \
  "ds_read_b64_tr_b16 v[96:97], v59 offset:38912\n\t" \
  "v_mfma_f32_16x16x32_bf16 v[156:159], v[144:147], v[104:107], v[156:159]\n\t" \
  "ds_read_b64_tr_b16 v[98:99], v59 offset:39424\n\t" \
  "v_mfma_f32_16x16x32_bf16 v[164:167], v[144:147], v[112:115], v[164:167]\n\t" \
  "ds_read_b64_tr_b16 v[64:65], v58 offset:26624\n\t" \
  "v_mfma_f32_16x16x32_bf16 v[164:167], v[100:103], v[116:119], v[164:167]\n\t" \
  "ds_read_b64_tr_b16 v[66:67], v58 offset:27136\n\t" \
  "v_mfma_f32_16x16x32_bf16 v[172:175], v[100:103], v[120:123], v[172:175]\n\t" \
  "ds_read_b64_tr_b16 v[104:105], v58 offset:30720\n\t" \
  "v_mfma_f32_16x16x32_bf16 v[172:175], v[144:147], v[124:127], v[172:175]\n\t" \
  "ds_read_b64_tr_b16 v[106:107], v58 offset:31232\n\t" \
  "v_mfma_f32_16x16x32_bf16 v[180:183], v[144:147], v[128:131], v[180:183]\n\t" \
  "ds_read_b128 v[112:115], v56 offset:0\n\t" \
  "v_mfma_f32_16x16x32_bf16 v[180:183], v[100:103], v[132:135], v[180:183]\n\t" \
  "ds_read_b128 v[116:119], v57 offset:0\n\t" \
  "s_waitcnt lgkmcnt(7)\n\t" \
  "v_mfma_f32_16x16x32_bf16 v[216:219], v[72:75], v[108:111], v[216:219]\n\t" \
  "ds_read_b64_tr_b16 v[120:121], v59 offset:26624\n\t" \
  "v_mfma_f32_16x16x32_bf16 v[216:219], v[140:143], v[136:139], v[216:219]\n\t" \
  "ds_read_b64_tr_b16 v[122:123], v59 offset:27136\n\t" \
  "v_mfma_f32_16x16x32_bf16 v[224:227], v[68:71], v[136:139], v[224:227]\n\t" \
  "ds_read_b64_tr_b16 v[124:125], v59 offset:30720\n\t" \
  "v_mfma_f32_16x16x32_bf16 v[224:227], v[76:79], v[108:111], v[224:227]\n\t" \
  "ds_read_b64_tr_b16 v[126:127], v59 offset:31232\n\t" \
  "v_mfma_f32_16x16x32_bf16 v[232:235], v[80:83], v[108:111], v[232:235]\n\t" \
  "ds_read_b64_tr_b16 v[128:129], v58 offset:18432\n\t" \
  "v_mfma_f32_16x16x32_bf16 v[232:235], v[84:87], v[136:139], v[232:235]\n\t" \
  "ds_read_b64_tr_b16 v[130:131], v58 offset:18944\n\t" \
  "v_mfma_f32_16x16x32_bf16 v[240:243], v[88:91], v[136:139], v[240:243]\n\t" \
  "ds_read_b64_tr_b16 v[144:145], v58 offset:22528\n\t" \
  "v_mfma_f32_16x16x32_bf16 v[240:243], v[92:95], v[108:111], v[240:243]\n\t" \
  "ds_read_b64_tr_b16 v[146:147], v58 offset:23040\n\t" \
  "v_mfma_f32_16x16x32_bf16 v[220:223], v[72:75], v[148:151], v[220:223]\n\t" \
  "ds_read_b64_tr_b16 v[100:101], v59 offset:18432\n\t" \
  "s_waitcnt lgkmcnt(7)\n\t" \
  "v_mfma_f32_16x16x32_bf16 v[220:223], v[140:143], v[96:99], v[220:223]\n\t" \
  "ds_read_b64_tr_b16 v[102:103], v59 offset:18944\n\t" \
  "v_mfma_f32_16x16x32_bf16 v[228:231], v[68:71], v[96:99], v[228:231]\n\t" \
  "ds_read_b64_tr_b16 v[132:133], v59 offset:22528\n\t" \
  "v_mfma_f32_16x16x32_bf16 v[228:231], v[76:79], v[148:151], v[228:231]\n\t" \
  "ds_read_b64_tr_b16 v[134:135], v59 offset:23040\n\t" \
  "v_mfma_f32_16x16x32_bf16 v[236:239], v[80:83], v[148:151], v[236:239]\n\t" \
  "v_mfma_f32_16x16x32_bf16 v[236:239], v[84:87], v[96:99], v[236:239]\n\t" \
  "v_mfma_f32_16x16x32_bf16 v[244:247], v[88:91], v[96:99], v[244:247]\n\t" \
  "v_mfma_f32_16x16x32_bf16 v[244:247], v[92:95], v[148:151], v[244:247]\n\t" \
  "503:\n\t" \
  "s_cbranch_execz 507f\n\t" \
  "v_mfma_f32_16x16x32_bf16 v[184:187], v[64:67], v[72:75], v[184:187]\n\t" \
  "v_mfma_f32_16x16x32_bf16 v[184:187], v[104:107], v[140:143], v[184:187]\n\t" \
  "v_mfma_f32_16x16x32_bf16 v[192:195], v[104:107], v[68:71], v[192:195]\n\t" \
  "v_mfma_f32_16x16x32_bf16 v[192:195], v[64:67], v[76:79], v[192:195]\n\t" \
  "v_mfma_f32_16x16x32_bf16 v[200:203], v[64:67], v[80:83], v[200:203]\n\t" \
  "v_mfma_f32_16x16x32_bf16 v[200:203], v[104:107], v[84:87], v[200:203]\n\t" \
  "v_mfma_f32_16x16x32_bf16 v[208:211], v[104:107], v[88:91], v[208:211]\n\t" \
  "v_mfma_f32_16x16x32_bf16 v[208:211], v[64:67], v[92:95], v[208:211]\n\t" \
  "v_mfma_f32_16x16x32_bf16 v[248:251], v[64:67], v[112:115], v[248:251]\n\t" \
  "v_mfma_f32_16x16x32_bf16 v[248:251], v[104:107], v[116:119], v[248:251]\n\t" \
  "v_mfma_f32_16x16x32_bf16 v[252:255], v[120:123], v[112:115], v[252:255]\n\t" \
  "s_waitcnt lgkmcnt(0)\n\t" \
  "v_mfma_f32_16x16x32_bf16 v[252:255], v[124:127], v[116:119], v[252:255]\n\t" \
  "v_mfma_f32_16x16x32_bf16 v[188:191], v[124:127], v[140:143], v[188:191]\n\t" \
  "v_mfma_f32_16x16x32_bf16 v[188:191], v[120:123], v[72:75], v[188:191]\n\t" \
  "s_barrier\n\t" \
  "v_mfma_f32_16x16x32_bf16 v[196:199], v[120:123], v[76:79], v[196:199]\n\t" \
  "ds_read_b64_tr_b16 v[136:137], v62 offset:34816\n\t" \
  "v_bfe_u32 v30, v4, 9, 1\n\t" \
  "v_mfma_f32_16x16x32_bf16 v[196:199], v[124:127], v[68:71], v[196:199]\n\t" \
  "ds_read_b64_tr_b16 v[138:139], v62 offset:35328\n\t" \
  "v_mad_i32_i24 v30, v30, -16, v0\n\t" \
  "v_mfma_f32_16x16x32_bf16 v[204:207], v[124:127], v[84:87], v[204:207]\n\t" \
  "ds_read_b128 v[108:111], v60 offset:2048\n\t" \
  "ds_read_b128 v[26:29], v0 offset:1552\n\t" \
  "v_mfma_f32_16x16x32_bf16 v[204:207], v[120:123], v[80:83], v[204:207]\n\t" \
  "ds_read_b64_tr_b16 v[96:97], v62 offset:38912\n\t" \
  "ds_read_b128 v[30:33], v30 offset:1040\n\t" \
  "v_mfma_f32_16x16x32_bf16 v[212:215], v[120:123], v[92:95], v[212:215]\n\t" \
  "ds_read_b64_tr_b16 v[98:99], v62 offset:39424\n\t" \
  "ds_read_b128 v[34:37], v0 offset:512\n\t" \
  "v_mfma_f32_16x16x32_bf16 v[212:215], v[124:127], v[88:91], v[212:215]\n\t" \
  "ds_read_b128 v[148:151], v61 offset:2048\n\t" \
  "ds_read_b64_tr_b16 v[38:39], v1 offset:512\n\t" \
  "v_mfma_f32_16x16x32_bf16 v[152:155], v[128:131], v[72:75], v[152:155]\n\t" \
  "ds_read_b128 v[64:67], v61 offset:4096\n\t" \
  "ds_read_b64_tr_b16 v[40:41], v1 offset:0\n\t" \
  "v_mfma_f32_16x16x32_bf16 v[152:155], v[144:147], v[140:143], v[152:155]\n\t" \
  "ds_read_b128 v[104:107], v60 offset:4096\n\t" \
  "s_waitcnt vmcnt(0)\n\t" \
  "v_mfma_f32_16x16x32_bf16 v[160:163], v[144:147], v[68:71], v[160:163]\n\t" \
  "ds_read_b128 v[112:115], v60 offset:6144\n\t" \
  "v_perm_b32 v18, v10, v10, v14\n\t" \
  "v_mfma_f32_16x16x32_bf16 v[160:163], v[128:131], v[76:79], v[160:163]\n\t" \
  "ds_read_b128 v[116:119], v61 offset:6144\n\t" \
  "v_perm_b32 v19, v10, v10, v15\n\t" \
  "v_mfma_f32_16x16x32_bf16 v[168:171], v[128:131], v[80:83], v[168:171]\n\t" \
  "ds_read_b128 v[120:123], v61 offset:8192\n\t" \
  "v_perm_b32 v20, v10, v10, v16\n\t" \
  "v_mfma_f32_16x16x32_bf16 v[168:171], v[144:147], v[84:87], v[168:171]\n\t" \
  "ds_read_b128 v[124:127], v60 offset:8192\n\t" \
  "v_perm_b32 v21, v10, v10, v17\n\t" \
  "v_mfma_f32_16x16x32_bf16 v[176:179], v[144:147], v[88:91], v[176:179]\n\t" \
  "ds_read_b64_tr_b16 v[144:145], v63 offset:34816\n\t" \
  "v_perm_b32 v22, v11, v11, v14\n\t" \
  "v_mfma_f32_16x16x32_bf16 v[176:179], v[128:131], v[92:95], v[176:179]\n\t" \
  "ds_read_b64_tr_b16 v[146:147], v63 offset:35328\n\t" \
  "v_perm_b32 v23, v11, v11, v15\n\t" \
  "v_mfma_f32_16x16x32_bf16 v[156:159], v[100:103], v[72:75], v[156:159]\n\t" \
  "ds_read_b64_tr_b16 v[128:129], v63 offset:38912\n\t" \
  "v_perm_b32 v24, v11, v11, v16\n\t" \
  "v_mfma_f32_16x16x32_bf16 v[156:159], v[132:135], v[140:143], v[156:159]\n\t" \
  "ds_read_b64_tr_b16 v[130:131], v63 offset:39424\n\t" \
  "v_perm_b32 v25, v11, v11, v17\n\t" \
  "v_mfma_f32_16x16x32_bf16 v[164:167], v[132:135], v[68:71], v[164:167]\n\t" \
  "ds_read_b64_tr_b16 v[72:73], v62 offset:26624\n\t" \
  "v_mfma_f32_16x16x32_bf16 v[164:167], v[100:103], v[76:79], v[164:167]\n\t" \
  "ds_read_b64_tr_b16 v[74:75], v62 offset:27136\n\t" \
  "v_mfma_f32_16x16x32_bf16 v[172:175], v[100:103], v[80:83], v[172:175]\n\t" \
  "ds_read_b64_tr_b16 v[140:141], v62 offset:30720\n\t" \
  "s_waitcnt lgkmcnt(6)\n\t" \
  "v_mfma_f32_16x16x32_bf16 v[42:45], v[26:29], v[18:21], 0\n\t" \
  "v_mfma_f32_16x16x32_bf16 v[42:45], v[30:33], v[22:25], v[42:45]\n\t" \
  "v_mfma_f32_16x16x32_bf16 v[46:49], v[30:33], v[18:21], 0\n\t" \
  "v_mfma_f32_16x16x32_bf16 v[46:49], v[34:37], v[22:25], v[46:49]\n\t" \
  "ds_read_b128 v[26:29], v0 offset:1808\n\t" \
  "ds_read_b128 v[30:33], v0 offset:1296\n\t" \
  "v_mfma_f32_16x16x32_bf16 v[172:175], v[132:135], v[84:87], v[172:175]\n\t" \
  "ds_read_b64_tr_b16 v[142:143], v62 offset:31232\n\t" \
  "ds_read_b128 v[34:37], v0 offset:768\n\t" \
  "v_mfma_f32_16x16x32_bf16 v[180:183], v[132:135], v[88:91], v[180:183]\n\t" \
  "ds_read_b128 v[68:71], v60 offset:0\n\t" \
  "v_mfma_f32_16x16x32_bf16 v[180:183], v[100:103], v[92:95], v[180:183]\n\t" \
  "ds_read_b128 v[76:79], v61 offset:0\n\t" \
  "v_mfma_f32_16x16x32_bf16 v[216:219], v[108:111], v[136:139], v[216:219]\n\t" \
  "ds_read_b64_tr_b16 v[80:81], v63 offset:26624\n\t" \
  "v_lshlrev_b32 v50, 16, v38\n\t" \
  "v_mfma_f32_16x16x32_bf16 v[216:219], v[148:151], v[96:99], v[216:219]\n\t" \
  "ds_read_b64_tr_b16 v[82:83], v63 offset:27136\n\t" \
  "v_lshlrev_b32 v51, 16, v40\n\t" \
  "v_mfma_f32_16x16x32_bf16 v[224:227], v[64:67], v[96:99], v[224:227]\n\t" \
  "ds_read_b64_tr_b16 v[84:85], v63 offset:30720\n\t" \
  "v_mul_f32 v42, v42, v50\n\t" \
  "v_mfma_f32_16x16x32_bf16 v[224:227], v[104:107], v[136:139], v[224:227]\n\t" \
  "ds_read_b64_tr_b16 v[86:87], v63 offset:31232\n\t" \
  "v_mul_f32 v46, v46, v51\n\t" \
  "v_mfma_f32_16x16x32_bf16 v[232:235], v[112:115], v[136:139], v[232:235]\n\t" \
  "ds_read_b64_tr_b16 v[88:89], v62 offset:18432\n\t" \
  "v_cvt_pk_bf16_f32 v42, v42, v46\n\t" \
  "v_mfma_f32_16x16x32_bf16 v[232:235], v[116:119], v[96:99], v[232:235]\n\t" \
  "ds_read_b64_tr_b16 v[90:91], v62 offset:18944\n\t" \
  "v_and_b32 v50, 0xffff0000, v38\n\t" \
  "v_mfma_f32_16x16x32_bf16 v[240:243], v[120:123], v[96:99], v[240:243]\n\t" \
  "ds_read_b64_tr_b16 v[132:133], v62 offset:22528\n\t" \
  "v_and_b32 v51, 0xffff0000, v40\n\t" \
  "v_mfma_f32_16x16x32_bf16 v[240:243], v[124:127], v[136:139], v[240:243]\n\t" \
  "ds_read_b64_tr_b16 v[134:135], v62 offset:23040\n\t" \
  "v_mul_f32 v43, v43, v50\n\t" \
  "s_waitcnt lgkmcnt(7)\n\t" \
  "v_mfma_f32_16x16x32_bf16 v[220:223], v[108:111], v[144:147], v[220:223]\n\t" \
  "ds_read_b64_tr_b16 v[92:93], v63 offset:18432\n\t" \
  "v_mul_f32 v47, v47, v51\n\t" \
  "v_mfma_f32_16x16x32_bf16 v[220:223], v[148:151], v[128:131], v[220:223]\n\t" \
  "ds_read_b64_tr_b16 v[94:95], v63 offset:18944\n\t" \
  "v_cvt_pk_bf16_f32 v43, v43, v47\n\t" \
  "v_mfma_f32_16x16x32_bf16 v[228:231], v[64:67], v[128:131], v[228:231]\n\t" \
  "ds_read_b64_tr_b16 v[100:101], v63 offset:22528\n\t" \
  "v_lshlrev_b32 v50, 16, v39\n\t" \
  "v_mfma_f32_16x16x32_bf16 v[228:231], v[104:107], v[144:147], v[228:231]\n\t" \
  "ds_read_b64_tr_b16 v[102:103], v63 offset:23040\n\t" \
  "v_lshlrev_b32 v51, 16, v41\n\t" \
  "v_mfma_f32_16x16x32_bf16 v[236:239], v[112:115], v[144:147], v[236:239]\n\t" \
  "v_mul_f32 v44, v44, v50\n\t" \
  "v_mul_f32 v48, v48, v51\n\t" \
  "v_mfma_f32_16x16x32_bf16 v[236:239], v[116:119], v[128:131], v[236:239]\n\t" \
  "v_cvt_pk_bf16_f32 v44, v44, v48\n\t" \
  "v_and_b32 v50, 0xffff0000, v39\n\t" \
  "v_mfma_f32_16x16x32_bf16 v[244:247], v[120:123], v[128:131], v[244:247]\n\t" \
  "v_and_b32 v51, 0xffff0000, v41\n\t" \
  "v_mul_f32 v45, v45, v50\n\t" \
  "v_mfma_f32_16x16x32_bf16 v[244:247], v[124:127], v[144:147], v[244:247]\n\t" \
  "v_mul_f32 v49, v49, v51\n\t" \
  "ds_read_b64_tr_b16 v[38:39], v1 offset:768\n\t" \
  "v_mfma_f32_16x16x32_bf16 v[184:187], v[72:75], v[108:111], v[184:187]\n\t" \
  "ds_read_b64_tr_b16 v[40:41], v1 offset:256\n\t" \
  "v_cvt_pk_bf16_f32 v45, v45, v49\n\t" \
  "v_mfma_f32_16x16x32_bf16 v[184:187], v[140:143], v[148:151], v[184:187]\n\t" \
  "v_mfma_f32_16x16x32_bf16 v[192:195], v[140:143], v[64:67], v[192:195]\n\t" \
  "v_mfma_f32_16x16x32_bf16 v[192:195], v[72:75], v[104:107], v[192:195]\n\t" \
  "v_mfma_f32_16x16x32_bf16 v[200:203], v[72:75], v[112:115], v[200:203]\n\t" \
  "v_mfma_f32_16x16x32_bf16 v[200:203], v[140:143], v[116:119], v[200:203]\n\t" \
  "v_mfma_f32_16x16x32_bf16 v[208:211], v[140:143], v[120:123], v[208:211]\n\t" \
  "v_mfma_f32_16x16x32_bf16 v[208:211], v[72:75], v[124:127], v[208:211]\n\t" \
  "v_mfma_f32_16x16x32_bf16 v[248:251], v[72:75], v[68:71], v[248:251]\n\t" \
  "v_mfma_f32_16x16x32_bf16 v[248:251], v[140:143], v[76:79], v[248:251]\n\t" \
  "s_waitcnt lgkmcnt(0)\n\t" \
  "v_mfma_f32_16x16x32_bf16 v[252:255], v[80:83], v[68:71], v[252:255]\n\t" \
  "v_mfma_f32_16x16x32_bf16 v[252:255], v[84:87], v[76:79], v[252:255]\n\t" \
  "v_mfma_f32_16x16x32_bf16 v[188:191], v[84:87], v[148:151], v[188:191]\n\t" \
  "v_mfma_f32_16x16x32_bf16 v[188:191], v[80:83], v[108:111], v[188:191]\n\t" \
  "s_barrier\n\t" \
  "v_mfma_f32_16x16x32_bf16 v[196:199], v[80:83], v[104:107], v[196:199]\n\t" \
  "ds_read_b64_tr_b16 v[96:97], v54 offset:34816\n\t" \
  "v_mfma_f32_16x16x32_bf16 v[196:199], v[84:87], v[64:67], v[196:199]\n\t" \
  "v_mov_b32 v50, 163344\n\t" \
  "ds_read_b64_tr_b16 v[98:99], v54 offset:35328\n\t" \
  "v_mfma_f32_16x16x32_bf16 v[204:207], v[84:87], v[116:119], v[204:207]\n\t" \
  "ds_read_b32 v50, v50 offset:24\n\t" \
  "ds_read_b128 v[136:139], v52 offset:2048\n\t" \
  "v_mfma_f32_16x16x32_bf16 v[204:207], v[80:83], v[112:115], v[204:207]\n\t" \
  "ds_read_b64_tr_b16 v[128:129], v54 offset:38912\n\t" \
  "v_mfma_f32_16x16x32_bf16 v[212:215], v[80:83], v[124:127], v[212:215]\n\t" \
  "ds_read_b64_tr_b16 v[130:131], v54 offset:39424\n\t" \
  "v_mfma_f32_16x16x32_bf16 v[212:215], v[84:87], v[120:123], v[212:215]\n\t" \
  "ds_read_b128 v[144:147], v53 offset:2048\n\t" \
  "v_mfma_f32_16x16x32_bf16 v[152:155], v[88:91], v[108:111], v[152:155]\n\t" \
  "ds_read_b128 v[72:75], v53 offset:4096\n\t" \
  "v_mfma_f32_16x16x32_bf16 v[152:155], v[132:135], v[148:151], v[152:155]\n\t" \
  "ds_read_b128 v[140:143], v52 offset:4096\n\t" \
  "v_mfma_f32_16x16x32_bf16 v[160:163], v[132:135], v[64:67], v[160:163]\n\t" \
  "ds_read_b128 v[68:71], v52 offset:6144\n\t" \
  "v_mfma_f32_16x16x32_bf16 v[160:163], v[88:91], v[104:107], v[160:163]\n\t" \
  "ds_read_b128 v[76:79], v53 offset:6144\n\t" \
  "v_mfma_f32_16x16x32_bf16 v[168:171], v[88:91], v[112:115], v[168:171]\n\t" \
  "ds_read_b128 v[80:83], v53 offset:8192\n\t" \
  "v_mfma_f32_16x16x32_bf16 v[168:171], v[132:135], v[116:119], v[168:171]\n\t" \
  "ds_read_b128 v[84:87], v52 offset:8192\n\t" \
  "v_mfma_f32_16x16x32_bf16 v[176:179], v[132:135], v[120:123], v[176:179]\n\t" \
  "s_waitcnt lgkmcnt(10)\n\t" \
  "v_readfirstlane_b32 s88, v50\n\t" \
  "s_mov_b32 s89, 0\n\t" \
  "ds_read_b64_tr_b16 v[132:133], v55 offset:34816\n\t" \
  "v_mfma_f32_16x16x32_bf16 v[176:179], v[88:91], v[124:127], v[176:179]\n\t" \
  "ds_read_b64_tr_b16 v[134:135], v55 offset:35328\n\t" \
  "v_mfma_f32_16x16x32_bf16 v[156:159], v[92:95], v[108:111], v[156:159]\n\t" \
  "s_xor_b64 %24, %24, s[88:89]\n\t" \
  "ds_read_b64_tr_b16 v[88:89], v55 offset:38912\n\t" \
  "v_mfma_f32_16x16x32_bf16 v[156:159], v[100:103], v[148:151], v[156:159]\n\t" \
  "s_xor_b64 %25, %25, s[88:89]\n\t" \
  "ds_read_b64_tr_b16 v[90:91], v55 offset:39424\n\t" \
  "v_mfma_f32_16x16x32_bf16 v[164:167], v[100:103], v[64:67], v[164:167]\n\t" \
  "s_xor_b64 %14, %14, s[88:89]\n\t" \
  "ds_read_b64_tr_b16 v[108:109], v54 offset:26624\n\t" \
  "v_mfma_f32_16x16x32_bf16 v[164:167], v[92:95], v[104:107], v[164:167]\n\t" \
  "s_xor_b64 %19, %19, s[88:89]\n\t" \
  "ds_read_b64_tr_b16 v[110:111], v54 offset:27136\n\t" \
  "v_mfma_f32_16x16x32_bf16 v[172:175], v[92:95], v[112:115], v[172:175]\n\t" \
  "s_xor_b64 %21, %21, s[88:89]\n\t" \
  "ds_read_b64_tr_b16 v[148:149], v54 offset:30720\n\t" \
  "v_mfma_f32_16x16x32_bf16 v[172:175], v[100:103], v[116:119], v[172:175]\n\t" \
  "s_xor_b64 s[92:93], s[92:93], s[88:89]\n\t" \
  "ds_read_b64_tr_b16 v[150:151], v54 offset:31232\n\t" \
  "v_mfma_f32_16x16x32_bf16 v[180:183], v[100:103], v[120:123], v[180:183]\n\t" \
  "ds_read_b128 v[64:67], v52 offset:0\n\t" \
  "v_mfma_f32_16x16x32_bf16 v[180:183], v[92:95], v[124:127], v[180:183]\n\t" \
  "ds_read_b128 v[104:107], v53 offset:0\n\t" \
  "s_waitcnt lgkmcnt(7)\n\t" \
  "v_mfma_f32_16x16x32_bf16 v[216:219], v[136:139], v[96:99], v[216:219]\n\t" \
  "ds_read_b64_tr_b16 v[112:113], v55 offset:26624\n\t" \
  "v_mfma_f32_16x16x32_bf16 v[216:219], v[144:147], v[128:131], v[216:219]\n\t" \
  "ds_read_b64_tr_b16 v[114:115], v55 offset:27136\n\t" \
  "v_mfma_f32_16x16x32_bf16 v[224:227], v[72:75], v[128:131], v[224:227]\n\t" \
  "ds_read_b64_tr_b16 v[116:117], v55 offset:30720\n\t" \
  "v_mfma_f32_16x16x32_bf16 v[224:227], v[140:143], v[96:99], v[224:227]\n\t" \
  "ds_read_b64_tr_b16 v[118:119], v55 offset:31232\n\t" \
  "v_mfma_f32_16x16x32_bf16 v[232:235], v[68:71], v[96:99], v[232:235]\n\t" \
  "ds_read_b64_tr_b16 v[100:101], v54 offset:18432\n\t" \
  "v_mfma_f32_16x16x32_bf16 v[232:235], v[76:79], v[128:131], v[232:235]\n\t" \
  "ds_read_b64_tr_b16 v[102:103], v54 offset:18944\n\t" \
  "v_mfma_f32_16x16x32_bf16 v[240:243], v[80:83], v[128:131], v[240:243]\n\t" \
  "ds_read_b64_tr_b16 v[120:121], v54 offset:22528\n\t" \
  "v_mfma_f32_16x16x32_bf16 v[240:243], v[84:87], v[96:99], v[240:243]\n\t" \
  "ds_read_b64_tr_b16 v[122:123], v54 offset:23040\n\t" \
  "v_mfma_f32_16x16x32_bf16 v[220:223], v[136:139], v[132:135], v[220:223]\n\t" \
  "ds_read_b64_tr_b16 v[92:93], v55 offset:18432\n\t" \
  "s_waitcnt lgkmcnt(7)\n\t" \
  "v_mfma_f32_16x16x32_bf16 v[220:223], v[144:147], v[88:91], v[220:223]\n\t" \
  "ds_read_b64_tr_b16 v[94:95], v55 offset:18944\n\t" \
  "v_mfma_f32_16x16x32_bf16 v[228:231], v[72:75], v[88:91], v[228:231]\n\t" \
  "ds_read_b64_tr_b16 v[124:125], v55 offset:22528\n\t" \
  "v_mfma_f32_16x16x32_bf16 v[228:231], v[140:143], v[132:135], v[228:231]\n\t" \
  "ds_read_b64_tr_b16 v[126:127], v55 offset:23040\n\t" \
  "v_mfma_f32_16x16x32_bf16 v[236:239], v[68:71], v[132:135], v[236:239]\n\t" \
  "v_mfma_f32_16x16x32_bf16 v[236:239], v[76:79], v[88:91], v[236:239]\n\t" \
  "v_mfma_f32_16x16x32_bf16 v[244:247], v[80:83], v[88:91], v[244:247]\n\t" \
  "v_mfma_f32_16x16x32_bf16 v[244:247], v[84:87], v[132:135], v[244:247]\n\t" \
  "v_mfma_f32_16x16x32_bf16 v[184:187], v[108:111], v[136:139], v[184:187]\n\t" \
  "v_mfma_f32_16x16x32_bf16 v[184:187], v[148:151], v[144:147], v[184:187]\n\t" \
  "v_mfma_f32_16x16x32_bf16 v[192:195], v[148:151], v[72:75], v[192:195]\n\t" \
  "v_mfma_f32_16x16x32_bf16 v[192:195], v[108:111], v[140:143], v[192:195]\n\t" \
  "v_mfma_f32_16x16x32_bf16 v[200:203], v[108:111], v[68:71], v[200:203]\n\t" \
  "global_store_dwordx4 v2, v[42:45], %24 offset:0 sc1\n\t" \
  "v_mfma_f32_16x16x32_bf16 v[200:203], v[148:151], v[76:79], v[200:203]\n\t" \
  "s_nop 0\n\t" \
  "v_mfma_f32_16x16x32_bf16 v[42:45], v[26:29], v[18:21], 0\n\t" \
  "v_mfma_f32_16x16x32_bf16 v[42:45], v[30:33], v[22:25], v[42:45]\n\t" \
  "v_mfma_f32_16x16x32_bf16 v[46:49], v[30:33], v[18:21], 0\n\t" \
  "v_mfma_f32_16x16x32_bf16 v[46:49], v[34:37], v[22:25], v[46:49]\n\t" \
  "v_bfe_u32 v30, v4, 9, 1\n\t" \
  "v_mad_i32_i24 v30, v30, -16, v0\n\t" \
  "v_mfma_f32_16x16x32_bf16 v[208:211], v[148:151], v[80:83], v[208:211]\n\t" \
  "ds_read_b128 v[26:29], v0 offset:1584\n\t" \
  "ds_read_b128 v[30:33], v30 offset:1072\n\t" \
  "v_mfma_f32_16x16x32_bf16 v[208:211], v[108:111], v[84:87], v[208:211]\n\t" \
  "ds_read_b128 v[34:37], v0 offset:544\n\t" \
  "v_perm_b32 v18, v12, v12, v14\n\t" \
  "v_mfma_f32_16x16x32_bf16 v[248:251], v[108:111], v[64:67], v[248:251]\n\t" \
  "v_perm_b32 v19, v12, v12, v15\n\t" \
  "v_perm_b32 v20, v12, v12, v16\n\t" \
  "v_mfma_f32_16x16x32_bf16 v[248:251], v[148:151], v[104:107], v[248:251]\n\t" \
  "v_perm_b32 v21, v12, v12, v17\n\t" \
  "v_perm_b32 v22, v13, v13, v14\n\t" \
  "v_mfma_f32_16x16x32_bf16 v[252:255], v[112:115], v[64:67], v[252:255]\n\t" \
  "v_perm_b32 v23, v13, v13, v15\n\t" \
  "v_perm_b32 v24, v13, v13, v16\n\t" \
  "s_waitcnt lgkmcnt(3)\n\t" \
  "v_mfma_f32_16x16x32_bf16 v[252:255], v[116:119], v[104:107], v[252:255]\n\t" \
  "v_perm_b32 v25, v13, v13, v17\n\t" \
  "v_lshlrev_b32 v50, 16, v38\n\t" \
  "v_mfma_f32_16x16x32_bf16 v[188:191], v[116:119], v[144:147], v[188:191]\n\t" \
  "v_lshlrev_b32 v51, 16, v40\n\t" \
  "v_mul_f32 v42, v42, v50\n\t" \
  "v_mfma_f32_16x16x32_bf16 v[188:191], v[112:115], v[136:139], v[188:191]\n\t" \
  "v_mul_f32 v46, v46, v51\n\t" \
  "v_cvt_pk_bf16_f32 v42, v42, v46\n\t" \
  "s_waitcnt lgkmcnt(0)\n\t" \
  "s_barrier\n\t" \
  "v_mfma_f32_16x16x32_bf16 v[196:199], v[112:115], v[140:143], v[196:199]\n\t" \
  "ds_read_b64_tr_b16 v[128:129], v58 offset:34816\n\t" \
  "v_and_b32 v50, 0xffff0000, v38\n\t" \
  "v_mfma_f32_16x16x32_bf16 v[196:199], v[116:119], v[72:75], v[196:199]\n\t" \
  "ds_read_b64_tr_b16 v[130:131], v58 offset:35328\n\t" \
  "v_and_b32 v51, 0xffff0000, v40\n\t" \
  "v_mfma_f32_16x16x32_bf16 v[204:207], v[116:119], v[76:79], v[204:207]\n\t" \
  "ds_read_b128 v[96:99], v56 offset:2048\n\t" \
  "v_mul_f32 v43, v43, v50\n\t" \
  "v_mfma_f32_16x16x32_bf16 v[204:207], v[112:115], v[68:71], v[204:207]\n\t" \
  "ds_read_b64_tr_b16 v[88:89], v58 offset:38912\n\t" \
  "v_mul_f32 v47, v47, v51\n\t" \
  "v_mfma_f32_16x16x32_bf16 v[212:215], v[112:115], v[84:87], v[212:215]\n\t" \
  "ds_read_b64_tr_b16 v[90:91], v58 offset:39424\n\t" \
  "v_cvt_pk_bf16_f32 v43, v43, v47\n\t" \
  "v_mfma_f32_16x16x32_bf16 v[212:215], v[116:119], v[80:83], v[212:215]\n\t" \
  "ds_read_b128 v[132:135], v57 offset:2048\n\t" \
  "v_lshlrev_b32 v50, 16, v39\n\t" \
  "v_mfma_f32_16x16x32_bf16 v[152:155], v[100:103], v[136:139], v[152:155]\n\t" \
  "ds_read_b128 v[108:111], v57 offset:4096\n\t" \
  "v_lshlrev_b32 v51, 16, v41\n\t" \
  "v_mfma_f32_16x16x32_bf16 v[152:155], v[120:123], v[144:147], v[152:155]\n\t" \
  "ds_read_b128 v[148:151], v56 offset:4096\n\t" \
  "v_mul_f32 v44, v44, v50\n\t" \
  "v_mfma_f32_16x16x32_bf16 v[160:163], v[120:123], v[72:75], v[160:163]\n\t" \
  "ds_read_b128 v[64:67], v56 offset:6144\n\t" \
  "v_mul_f32 v48, v48, v51\n\t" \
  "v_mfma_f32_16x16x32_bf16 v[160:163], v[100:103], v[140:143], v[160:163]\n\t" \
  "ds_read_b128 v[104:107], v57 offset:6144\n\t" \
  "v_cvt_pk_bf16_f32 v44, v44, v48\n\t" \
  "v_mfma_f32_16x16x32_bf16 v[168:171], v[100:103], v[68:71], v[168:171]\n\t" \
  "ds_read_b128 v[112:115], v57 offset:8192\n\t" \
  "v_and_b32 v50, 0xffff0000, v39\n\t" \
  "v_mfma_f32_16x16x32_bf16 v[168:171], v[120:123], v[76:79], v[168:171]\n\t" \
  "ds_read_b128 v[116:119], v56 offset:8192\n\t" \
  "v_and_b32 v51, 0xffff0000, v41\n\t" \
  "v_mfma_f32_16x16x32_bf16 v[176:179], v[120:123], v[80:83], v[176:179]\n\t" \
  "ds_read_b64_tr_b16 v[120:121], v59 offset:34816\n\t" \
  "v_mul_f32 v45, v45, v50\n\t" \
  "v_mfma_f32_16x16x32_bf16 v[176:179], v[100:103], v[84:87], v[176:179]\n\t" \
  "ds_read_b64_tr_b16 v[122:123], v59 offset:35328\n\t" \
  "v_mul_f32 v49, v49, v51\n\t" \
  "v_mfma_f32_16x16x32_bf16 v[156:159], v[92:95], v[136:139], v[156:159]\n\t" \
  "ds_read_b64_tr_b16 v[100:101], v59 offset:38912\n\t" \
  "ds_read_b64_tr_b16 v[38:39], v1 offset:544\n\t" \
  "v_mfma_f32_16x16x32_bf16 v[156:159], v[124:127], v[144:147], v[156:159]\n\t" \
  "ds_read_b64_tr_b16 v[102:103], v59 offset:39424\n\t" \
  "ds_read_b64_tr_b16 v[40:41], v1 offset:32\n\t" \
  "v_mfma_f32_16x16x32_bf16 v[164:167], v[124:127], v[72:75], v[164:167]\n\t" \
  "ds_read_b64_tr_b16 v[136:137], v58 offset:26624\n\t" \
  "v_cvt_pk_bf16_f32 v45, v45, v49\n\t" \
  "v_mfma_f32_16x16x32_bf16 v[164:167], v[92:95], v[140:143], v[164:167]\n\t" \
  "ds_read_b64_tr_b16 v[138:139], v58 offset:27136\n\t" \
  "v_mfma_f32_16x16x32_bf16 v[172:175], v[92:95], v[68:71], v[172:175]\n\t" \
  "ds_read_b64_tr_b16 v[144:145], v58 offset:30720\n\t" \
  "v_mfma_f32_16x16x32_bf16 v[172:175], v[124:127], v[76:79], v[172:175]\n\t" \
  "ds_read_b64_tr_b16 v[146:147], v58 offset:31232\n\t" \
  "v_mfma_f32_16x16x32_bf16 v[180:183], v[124:127], v[80:83], v[180:183]\n\t" \
  "ds_read_b128 v[72:75], v56 offset:0\n\t" \
  "v_mfma_f32_16x16x32_bf16 v[180:183], v[92:95], v[84:87], v[180:183]\n\t" \
  "ds_read_b128 v[140:143], v57 offset:0\n\t" \
  "s_waitcnt lgkmcnt(8)\n\t" \
  "v_mfma_f32_16x16x32_bf16 v[216:219], v[96:99], v[128:131], v[216:219]\n\t" \
  "ds_read_b64_tr_b16 v[68:69], v59 offset:26624\n\t" \
  "v_mfma_f32_16x16x32_bf16 v[216:219], v[132:135], v[88:91], v[216:219]\n\t" \
  "ds_read_b64_tr_b16 v[70:71], v59 offset:27136\n\t" \
  "v_mfma_f32_16x16x32_bf16 v[224:227], v[108:111], v[88:91], v[224:227]\n\t" \
  "ds_read_b64_tr_b16 v[76:77], v59 offset:30720\n\t" \
  "v_mfma_f32_16x16x32_bf16 v[224:227], v[148:151], v[128:131], v[224:227]\n\t" \
  "ds_read_b64_tr_b16 v[78:79], v59 offset:31232\n\t" \
  "v_mfma_f32_16x16x32_bf16 v[232:235], v[64:67], v[128:131], v[232:235]\n\t" \
  "ds_read_b64_tr_b16 v[80:81], v58 offset:18432\n\t" \
  "v_mfma_f32_16x16x32_bf16 v[232:235], v[104:107], v[88:91], v[232:235]\n\t" \
  "ds_read_b64_tr_b16 v[82:83], v58 offset:18944\n\t" \
  "v_mfma_f32_16x16x32_bf16 v[240:243], v[112:115], v[88:91], v[240:243]\n\t" \
  "ds_read_b64_tr_b16 v[124:125], v58 offset:22528\n\t" \
  "v_mfma_f32_16x16x32_bf16 v[240:243], v[116:119], v[128:131], v[240:243]\n\t" \
  "ds_read_b64_tr_b16 v[126:127], v58 offset:23040\n\t" \
  "v_mfma_f32_16x16x32_bf16 v[220:223], v[96:99], v[120:123], v[220:223]\n\t" \
  "ds_read_b64_tr_b16 v[84:85], v59 offset:18432\n\t" \
  "s_waitcnt lgkmcnt(7)\n\t" \
  "v_mfma_f32_16x16x32_bf16 v[220:223], v[132:135], v[100:103], v[220:223]\n\t" \
  "ds_read_b64_tr_b16 v[86:87], v59 offset:18944\n\t" \
  "v_mfma_f32_16x16x32_bf16 v[228:231], v[108:111], v[100:103], v[228:231]\n\t" \
  "ds_read_b64_tr_b16 v[92:93], v59 offset:22528\n\t" \
  "v_mfma_f32_16x16x32_bf16 v[228:231], v[148:151], v[120:123], v[228:231]\n\t" \
  "ds_read_b64_tr_b16 v[94:95], v59 offset:23040\n\t" \
  "v_mfma_f32_16x16x32_bf16 v[236:239], v[64:67], v[120:123], v[236:239]\n\t" \
  "v_mfma_f32_16x16x32_bf16 v[236:239], v[104:107], v[100:103], v[236:239]\n\t" \
  "v_mfma_f32_16x16x32_bf16 v[244:247], v[112:115], v[100:103], v[244:247]\n\t" \
  "v_mfma_f32_16x16x32_bf16 v[244:247], v[116:119], v[120:123], v[244:247]\n\t" \
  "v_mfma_f32_16x16x32_bf16 v[184:187], v[136:139], v[96:99], v[184:187]\n\t" \
  "v_mfma_f32_16x16x32_bf16 v[184:187], v[144:147], v[132:135], v[184:187]\n\t" \
  "v_mfma_f32_16x16x32_bf16 v[192:195], v[144:147], v[108:111], v[192:195]\n\t" \
  "v_mfma_f32_16x16x32_bf16 v[192:195], v[136:139], v[148:151], v[192:195]\n\t" \
  "v_mfma_f32_16x16x32_bf16 v[200:203], v[136:139], v[64:67], v[200:203]\n\t" \
  "global_store_dwordx4 v2, v[42:45], %24 offset:1024 sc1\n\t" \
  "v_add_u32 v2, 0x20000, v2\n\t" \
  "v_mfma_f32_16x16x32_bf16 v[200:203], v[144:147], v[104:107], v[200:203]\n\t" \
  "v_mfma_f32_16x16x32_bf16 v[208:211], v[144:147], v[112:115], v[208:211]\n\t" \
  "v_mfma_f32_16x16x32_bf16 v[42:45], v[26:29], v[18:21], 0\n\t" \
  "v_mfma_f32_16x16x32_bf16 v[42:45], v[30:33], v[22:25], v[42:45]\n\t" \
  "v_mfma_f32_16x16x32_bf16 v[46:49], v[30:33], v[18:21], 0\n\t" \
  "v_mfma_f32_16x16x32_bf16 v[46:49], v[34:37], v[22:25], v[46:49]\n\t" \
  "ds_read_b128 v[26:29], v0 offset:1840\n\t" \
  "ds_read_b128 v[30:33], v0 offset:1328\n\t" \
  "v_mfma_f32_16x16x32_bf16 v[208:211], v[136:139], v[116:119], v[208:211]\n\t" \
  "ds_read_b128 v[34:37], v0 offset:800\n\t" \
  "v_mfma_f32_16x16x32_bf16 v[248:251], v[136:139], v[72:75], v[248:251]\n\t" \
  "v_mfma_f32_16x16x32_bf16 v[248:251], v[144:147], v[140:143], v[248:251]\n\t" \
  "v_lshlrev_b32 v50, 16, v38\n\t" \
  "v_lshlrev_b32 v51, 16, v40\n\t" \
  "v_mfma_f32_16x16x32_bf16 v[252:255], v[68:71], v[72:75], v[252:255]\n\t" \
  "v_mul_f32 v42, v42, v50\n\t" \
  "v_mul_f32 v46, v46, v51\n\t" \
  "s_waitcnt lgkmcnt(3)\n\t" \
  "v_mfma_f32_16x16x32_bf16 v[252:255], v[76:79], v[140:143], v[252:255]\n\t" \
  "v_cvt_pk_bf16_f32 v42, v42, v46\n\t" \
  "v_and_b32 v50, 0xffff0000, v38\n\t" \
  "v_mfma_f32_16x16x32_bf16 v[188:191], v[76:79], v[132:135], v[188:191]\n\t" \
  "v_and_b32 v51, 0xffff0000, v40\n\t" \
  "v_mul_f32 v43, v43, v50\n\t" \
  "v_mfma_f32_16x16x32_bf16 v[188:191], v[68:71], v[96:99], v[188:191]\n\t" \
  "v_mul_f32 v47, v47, v51\n\t" \
  "v_cvt_pk_bf16_f32 v43, v43, v47\n\t" \
  "s_waitcnt lgkmcnt(0)\n\t" \
  "s_barrier\n\t" \
  "v_mfma_f32_16x16x32_bf16 v[196:199], v[68:71], v[148:151], v[196:199]\n\t" \
  "ds_read_b64_tr_b16 v[88:89], v62 offset:34816\n\t" \
  "v_lshlrev_b32 v50, 16, v39\n\t" \
  "v_mfma_f32_16x16x32_bf16 v[196:199], v[76:79], v[108:111], v[196:199]\n\t" \
  "ds_read_b64_tr_b16 v[90:91], v62 offset:35328\n\t" \
  "v_lshlrev_b32 v51, 16, v41\n\t" \
  "v_mfma_f32_16x16x32_bf16 v[204:207], v[76:79], v[104:107], v[204:207]\n\t" \
  "ds_read_b128 v[128:131], v60 offset:2048\n\t" \
  "v_mul_f32 v44, v44, v50\n\t" \
  "v_mfma_f32_16x16x32_bf16 v[204:207], v[68:71], v[64:67], v[204:207]\n\t" \
  "ds_read_b64_tr_b16 v[100:101], v62 offset:38912\n\t" \
  "v_mul_f32 v48, v48, v51\n\t" \
  "v_mfma_f32_16x16x32_bf16 v[212:215], v[68:71], v[116:119], v[212:215]\n\t" \
  "ds_read_b64_tr_b16 v[102:103], v62 offset:39424\n\t" \
  "v_cvt_pk_bf16_f32 v44, v44, v48\n\t" \
  "v_mfma_f32_16x16x32_bf16 v[212:215], v[76:79], v[112:115], v[212:215]\n\t" \
  "ds_read_b128 v[120:123], v61 offset:2048\n\t" \
  "v_and_b32 v50, 0xffff0000, v39\n\t" \
  "v_mfma_f32_16x16x32_bf16 v[152:155], v[80:83], v[96:99], v[152:155]\n\t" \
  "ds_read_b128 v[136:139], v61 offset:4096\n\t" \
  "v_and_b32 v51, 0xffff0000, v41\n\t" \
  "v_mfma_f32_16x16x32_bf16 v[152:155], v[124:127], v[132:135], v[152:155]\n\t" \
  "ds_read_b128 v[144:147], v60 offset:4096\n\t" \
  "v_mul_f32 v45, v45, v50\n\t" \
  "v_mfma_f32_16x16x32_bf16 v[160:163], v[124:127], v[108:111], v[160:163]\n\t" \
  "ds_read_b128 v[72:75], v60 offset:6144\n\t" \
  "v_mul_f32 v49, v49, v51\n\t" \
  "v_mfma_f32_16x16x32_bf16 v[160:163], v[80:83], v[148:151], v[160:163]\n\t" \
  "ds_read_b128 v[140:143], v61 offset:6144\n\t" \
  "ds_read_b64_tr_b16 v[38:39], v1 offset:800\n\t" \
  "v_mfma_f32_16x16x32_bf16 v[168:171], v[80:83], v[64:67], v[168:171]\n\t" \
  "ds_read_b128 v[68:71], v61 offset:8192\n\t" \
  "ds_read_b64_tr_b16 v[40:41], v1 offset:288\n\t" \
  "v_mfma_f32_16x16x32_bf16 v[168:171], v[124:127], v[104:107], v[168:171]\n\t" \
  "ds_read_b128 v[76:79], v60 offset:8192\n\t" \
  "v_cvt_pk_bf16_f32 v45, v45, v49\n\t" \
  "v_mfma_f32_16x16x32_bf16 v[176:179], v[124:127], v[112:115], v[176:179]\n\t" \
  "ds_read_b64_tr_b16 v[124:125], v63 offset:34816\n\t" \
  "v_mfma_f32_16x16x32_bf16 v[176:179], v[80:83], v[116:119], v[176:179]\n\t" \
  "ds_read_b64_tr_b16 v[126:127], v63 offset:35328\n\t" \
  "v_mfma_f32_16x16x32_bf16 v[156:159], v[84:87], v[96:99], v[156:159]\n\t" \
  "ds_read_b64_tr_b16 v[80:81], v63 offset:38912\n\t" \
  "v_mfma_f32_16x16x32_bf16 v[156:159], v[92:95], v[132:135], v[156:159]\n\t" \
  "ds_read_b64_tr_b16 v[82:83], v63 offset:39424\n\t" \
  "v_mfma_f32_16x16x32_bf16 v[164:167], v[92:95], v[108:111], v[164:167]\n\t" \
  "ds_read_b64_tr_b16 v[96:97], v62 offset:26624\n\t" \
  "v_mfma_f32_16x16x32_bf16 v[164:167], v[84:87], v[148:151], v[164:167]\n\t" \
  "ds_read_b64_tr_b16 v[98:99], v62 offset:27136\n\t" \
  "v_mfma_f32_16x16x32_bf16 v[172:175], v[84:87], v[64:67], v[172:175]\n\t" \
  "ds_read_b64_tr_b16 v[132:133], v62 offset:30720\n\t" \
  "v_mfma_f32_16x16x32_bf16 v[172:175], v[92:95], v[104:107], v[172:175]\n\t" \
  "ds_read_b64_tr_b16 v[134:135], v62 offset:31232\n\t" \
  "v_mfma_f32_16x16x32_bf16 v[180:183], v[92:95], v[112:115], v[180:183]\n\t" \
  "ds_read_b128 v[108:111], v60 offset:0\n\t" \
  "v_mfma_f32_16x16x32_bf16 v[180:183], v[84:87], v[116:119], v[180:183]\n\t" \
  "ds_read_b128 v[148:151], v61 offset:0\n\t" \
  "s_waitcnt lgkmcnt(7)\n\t" \
  "v_mfma_f32_16x16x32_bf16 v[216:219], v[128:131], v[88:91], v[216:219]\n\t" \
  "ds_read_b64_tr_b16 v[64:65], v63 offset:26624\n\t" \
  "v_mfma_f32_16x16x32_bf16 v[216:219], v[120:123], v[100:103], v[216:219]\n\t" \
  "ds_read_b64_tr_b16 v[66:67], v63 offset:27136\n\t" \
  "v_mfma_f32_16x16x32_bf16 v[224:227], v[136:139], v[100:103], v[224:227]\n\t" \
  "ds_read_b64_tr_b16 v[104:105], v63 offset:30720\n\t" \
  "v_mfma_f32_16x16x32_bf16 v[224:227], v[144:147], v[88:91], v[224:227]\n\t" \
  "ds_read_b64_tr_b16 v[106:107], v63 offset:31232\n\t" \
  "v_mfma_f32_16x16x32_bf16 v[232:235], v[72:75], v[88:91], v[232:235]\n\t" \
  "ds_read_b64_tr_b16 v[92:93], v62 offset:18432\n\t" \
  "v_mfma_f32_16x16x32_bf16 v[232:235], v[140:143], v[100:103], v[232:235]\n\t" \
  "ds_read_b64_tr_b16 v[94:95], v62 offset:18944\n\t" \
  "v_mfma_f32_16x16x32_bf16 v[240:243], v[68:71], v[100:103], v[240:243]\n\t" \
  "ds_read_b64_tr_b16 v[112:113], v62 offset:22528\n\t" \
  "v_mfma_f32_16x16x32_bf16 v[240:243], v[76:79], v[88:91], v[240:243]\n\t" \
  "ds_read_b64_tr_b16 v[114:115], v62 offset:23040\n\t" \
  "v_mfma_f32_16x16x32_bf16 v[220:223], v[128:131], v[124:127], v[220:223]\n\t" \
  "ds_read_b64_tr_b16 v[84:85], v63 offset:18432\n\t" \
  "s_waitcnt lgkmcnt(7)\n\t" \
  "v_mfma_f32_16x16x32_bf16 v[220:223], v[120:123], v[80:83], v[220:223]\n\t" \
  "ds_read_b64_tr_b16 v[86:87], v63 offset:18944\n\t" \
  "v_mfma_f32_16x16x32_bf16 v[228:231], v[136:139], v[80:83], v[228:231]\n\t" \
  "ds_read_b64_tr_b16 v[116:117], v63 offset:22528\n\t" \
  "v_mfma_f32_16x16x32_bf16 v[228:231], v[144:147], v[124:127], v[228:231]\n\t" \
  "ds_read_b64_tr_b16 v[118:119], v63 offset:23040\n\t" \
  "v_mfma_f32_16x16x32_bf16 v[236:239], v[72:75], v[124:127], v[236:239]\n\t" \
  "v_mfma_f32_16x16x32_bf16 v[236:239], v[140:143], v[80:83], v[236:239]\n\t" \
  "v_mfma_f32_16x16x32_bf16 v[244:247], v[68:71], v[80:83], v[244:247]\n\t" \
  "v_mfma_f32_16x16x32_bf16 v[244:247], v[76:79], v[124:127], v[244:247]\n\t" \
  "v_mfma_f32_16x16x32_bf16 v[184:187], v[96:99], v[128:131], v[184:187]\n\t" \
  "v_mfma_f32_16x16x32_bf16 v[184:187], v[132:135], v[120:123], v[184:187]\n\t" \
  "v_mfma_f32_16x16x32_bf16 v[192:195], v[132:135], v[136:139], v[192:195]\n\t" \
  "v_mfma_f32_16x16x32_bf16 v[192:195], v[96:99], v[144:147], v[192:195]\n\t" \
  "v_mfma_f32_16x16x32_bf16 v[200:203], v[96:99], v[72:75], v[200:203]\n\t" \
  "global_store_dwordx4 v2, v[42:45], %24 offset:0 sc1\n\t" \
  "v_mfma_f32_16x16x32_bf16 v[200:203], v[132:135], v[140:143], v[200:203]\n\t" \
  "s_nop 0\n\t" \
  "v_mfma_f32_16x16x32_bf16 v[42:45], v[26:29], v[18:21], 0\n\t" \
  "v_mfma_f32_16x16x32_bf16 v[42:45], v[30:33], v[22:25], v[42:45]\n\t" \
  "v_mfma_f32_16x16x32_bf16 v[46:49], v[30:33], v[18:21], 0\n\t" \
  "v_mfma_f32_16x16x32_bf16 v[46:49], v[34:37], v[22:25], v[46:49]\n\t" \
  "v_mfma_f32_16x16x32_bf16 v[208:211], v[132:135], v[68:71], v[208:211]\n\t" \
  "v_mfma_f32_16x16x32_bf16 v[208:211], v[96:99], v[76:79], v[208:211]\n\t" \
  "v_lshlrev_b32 v50, 16, v38\n\t" \
  "v_lshlrev_b32 v51, 16, v40\n\t" \
  "v_mfma_f32_16x16x32_bf16 v[248:251], v[96:99], v[108:111], v[248:251]\n\t" \
  "s_nop 0\n\t" \
  "v_mul_f32 v42, v42, v50\n\t" \
  "s_nop 0\n\t" \
  "v_mul_f32 v46, v46, v51\n\t" \
  "v_mfma_f32_16x16x32_bf16 v[248:251], v[132:135], v[148:151], v[248:251]\n\t" \
  "v_cvt_pk_bf16_f32 v42, v42, v46\n\t" \
  "v_and_b32 v50, 0xffff0000, v38\n\t" \
  "v_mfma_f32_16x16x32_bf16 v[252:255], v[64:67], v[108:111], v[252:255]\n\t" \
  "v_and_b32 v51, 0xffff0000, v40\n\t" \
  "v_mul_f32 v43, v43, v50\n\t" \
  "s_waitcnt lgkmcnt(0)\n\t" \
  "v_mfma_f32_16x16x32_bf16 v[252:255], v[104:107], v[148:151], v[252:255]\n\t" \
  "v_mul_f32 v47, v47, v51\n\t" \
  "v_cvt_pk_bf16_f32 v43, v43, v47\n\t" \
  "v_mfma_f32_16x16x32_bf16 v[188:191], v[104:107], v[120:123], v[188:191]\n\t" \
  "v_lshlrev_b32 v50, 16, v39\n\t" \
  "v_lshlrev_b32 v51, 16, v41\n\t" \
  "v_mfma_f32_16x16x32_bf16 v[188:191], v[64:67], v[128:131], v[188:191]\n\t" \
  "v_mul_f32 v44, v44, v50\n\t" \
  "v_mul_f32 v48, v48, v51\n\t" \
  "s_barrier\n\t" \
  "v_mfma_f32_16x16x32_bf16 v[196:199], v[64:67], v[144:147], v[196:199]\n\t" \
  "ds_read_b64_tr_b16 v[100:101], v54 offset:34816\n\t" \
  "v_cvt_pk_bf16_f32 v44, v44, v48\n\t" \
  "v_mfma_f32_16x16x32_bf16 v[196:199], v[104:107], v[136:139], v[196:199]\n\t" \
  "ds_read_b64_tr_b16 v[102:103], v54 offset:35328\n\t" \
  "v_and_b32 v50, 0xffff0000, v39\n\t" \
  "v_mfma_f32_16x16x32_bf16 v[204:207], v[104:107], v[140:143], v[204:207]\n\t" \
  "ds_read_b128 v[88:91], v52 offset:2048\n\t" \
  "v_and_b32 v51, 0xffff0000, v41\n\t" \
  "v_mfma_f32_16x16x32_bf16 v[204:207], v[64:67], v[72:75], v[204:207]\n\t" \
  "ds_read_b64_tr_b16 v[80:81], v54 offset:38912\n\t" \
  "v_mul_f32 v45, v45, v50\n\t" \
  "v_mfma_f32_16x16x32_bf16 v[212:215], v[64:67], v[76:79], v[212:215]\n\t" \
  "ds_read_b64_tr_b16 v[82:83], v54 offset:39424\n\t" \
  "v_mul_f32 v49, v49, v51\n\t" \
  "v_mfma_f32_16x16x32_bf16 v[212:215], v[104:107], v[68:71], v[212:215]\n\t" \
  "ds_read_b128 v[124:127], v53 offset:2048\n\t" \
  "v_cvt_pk_bf16_f32 v45, v45, v49\n\t" \
  "v_mfma_f32_16x16x32_bf16 v[152:155], v[92:95], v[128:131], v[152:155]\n\t" \
  "ds_read_b128 v[96:99], v53 offset:4096\n\t" \
  "v_mfma_f32_16x16x32_bf16 v[152:155], v[112:115], v[120:123], v[152:155]\n\t" \
  "ds_read_b128 v[132:135], v52 offset:4096\n\t" \
  "v_mfma_f32_16x16x32_bf16 v[160:163], v[112:115], v[136:139], v[160:163]\n\t" \
  "ds_read_b128 v[108:111], v52 offset:6144\n\t" \
  "504:\n\t" \
  "s_cbranch_execz 508f\n\t" \
  "v_mfma_f32_16x16x32_bf16 v[160:163], v[92:95], v[144:147], v[160:163]\n\t" \
  "ds_read_b128 v[148:151], v53 offset:6144\n\t" \
  "v_mfma_f32_16x16x32_bf16 v[168:171], v[92:95], v[72:75], v[168:171]\n\t" \
  "ds_read_b128 v[64:67], v53 offset:8192\n\t" \
  "v_mfma_f32_16x16x32_bf16 v[168:171], v[112:115], v[140:143], v[168:171]\n\t" \
  "ds_read_b128 v[104:107], v52 offset:8192\n\t" \
  "v_mfma_f32_16x16x32_bf16 v[176:179], v[112:115], v[68:71], v[176:179]\n\t" \
  "ds_read_b64_tr_b16 v[112:113], v55 offset:34816\n\t" \
  "v_mfma_f32_16x16x32_bf16 v[176:179], v[92:95], v[76:79], v[176:179]\n\t" \
  "ds_read_b64_tr_b16 v[114:115], v55 offset:35328\n\t" \
  "v_mfma_f32_16x16x32_bf16 v[156:159], v[84:87], v[128:131], v[156:159]\n\t" \
  "ds_read_b64_tr_b16 v[92:93], v55 offset:38912\n\t" \
  "v_mfma_f32_16x16x32_bf16 v[156:159], v[116:119], v[120:123], v[156:159]\n\t" \
  "ds_read_b64_tr_b16 v[94:95], v55 offset:39424\n\t" \
  "v_mfma_f32_16x16x32_bf16 v[164:167], v[116:119], v[136:139], v[164:167]\n\t" \
  "ds_read_b64_tr_b16 v[128:129], v54 offset:26624\n\t" \
  "v_mfma_f32_16x16x32_bf16 v[164:167], v[84:87], v[144:147], v[164:167]\n\t" \
  "ds_read_b64_tr_b16 v[130:131], v54 offset:27136\n\t" \
  "v_mfma_f32_16x16x32_bf16 v[172:175], v[84:87], v[72:75], v[172:175]\n\t" \
  "ds_read_b64_tr_b16 v[120:121], v54 offset:30720\n\t" \
  "v_mfma_f32_16x16x32_bf16 v[172:175], v[116:119], v[140:143], v[172:175]\n\t" \
  "ds_read_b64_tr_b16 v[122:123], v54 offset:31232\n\t" \
  "v_mfma_f32_16x16x32_bf16 v[180:183], v[116:119], v[68:71], v[180:183]\n\t" \
  "ds_read_b128 v[136:139], v52 offset:0\n\t" \
  "v_mfma_f32_16x16x32_bf16 v[180:183], v[84:87], v[76:79], v[180:183]\n\t" \
  "ds_read_b128 v[144:147], v53 offset:0\n\t" \
  "s_waitcnt lgkmcnt(7)\n\t" \
  "v_mfma_f32_16x16x32_bf16 v[216:219], v[88:91], v[100:103], v[216:219]\n\t" \
  "ds_read_b64_tr_b16 v[72:73], v55 offset:26624\n\t" \
  "v_mfma_f32_16x16x32_bf16 v[216:219], v[124:127], v[80:83], v[216:219]\n\t" \
  "ds_read_b64_tr_b16 v[74:75], v55 offset:27136\n\t" \
  "v_mfma_f32_16x16x32_bf16 v[224:227], v[96:99], v[80:83], v[224:227]\n\t" \
  "ds_read_b64_tr_b16 v[140:141], v55 offset:30720\n\t" \
  "v_mfma_f32_16x16x32_bf16 v[224:227], v[132:135], v[100:103], v[224:227]\n\t" \
  "ds_read_b64_tr_b16 v[142:143], v55 offset:31232\n\t" \
  "v_mfma_f32_16x16x32_bf16 v[232:235], v[108:111], v[100:103], v[232:235]\n\t" \
  "ds_read_b64_tr_b16 v[68:69], v54 offset:18432\n\t" \
  "v_mfma_f32_16x16x32_bf16 v[232:235], v[148:151], v[80:83], v[232:235]\n\t" \
  "ds_read_b64_tr_b16 v[70:71], v54 offset:18944\n\t" \
  "v_mfma_f32_16x16x32_bf16 v[240:243], v[64:67], v[80:83], v[240:243]\n\t" \
  "ds_read_b64_tr_b16 v[116:117], v54 offset:22528\n\t" \
  "v_mfma_f32_16x16x32_bf16 v[240:243], v[104:107], v[100:103], v[240:243]\n\t" \
  "ds_read_b64_tr_b16 v[118:119], v54 offset:23040\n\t" \
  "v_mfma_f32_16x16x32_bf16 v[220:223], v[88:91], v[112:115], v[220:223]\n\t" \
  "ds_read_b64_tr_b16 v[76:77], v55 offset:18432\n\t" \
  "s_waitcnt lgkmcnt(7)\n\t" \
  "v_mfma_f32_16x16x32_bf16 v[220:223], v[124:127], v[92:95], v[220:223]\n\t" \
  "ds_read_b64_tr_b16 v[78:79], v55 offset:18944\n\t" \
  "v_mfma_f32_16x16x32_bf16 v[228:231], v[96:99], v[92:95], v[228:231]\n\t" \
  "ds_read_b64_tr_b16 v[84:85], v55 offset:22528\n\t" \
  "v_mfma_f32_16x16x32_bf16 v[228:231], v[132:135], v[112:115], v[228:231]\n\t" \
  "ds_read_b64_tr_b16 v[86:87], v55 offset:23040\n\t" \
  "v_mfma_f32_16x16x32_bf16 v[236:239], v[108:111], v[112:115], v[236:239]\n\t" \
  "v_mfma_f32_16x16x32_bf16 v[236:239], v[148:151], v[92:95], v[236:239]\n\t" \
  "v_mfma_f32_16x16x32_bf16 v[244:247], v[64:67], v[92:95], v[244:247]\n\t" \
  "v_mfma_f32_16x16x32_bf16 v[244:247], v[104:107], v[112:115], v[244:247]\n\t" \
  "v_mfma_f32_16x16x32_bf16 v[184:187], v[128:131], v[88:91], v[184:187]\n\t" \
  "v_mfma_f32_16x16x32_bf16 v[184:187], v[120:123], v[124:127], v[184:187]\n\t" \
  "v_mfma_f32_16x16x32_bf16 v[192:195], v[120:123], v[96:99], v[192:195]\n\t" \
  "v_mfma_f32_16x16x32_bf16 v[192:195], v[128:131], v[132:135], v[192:195]\n\t" \
  "v_mfma_f32_16x16x32_bf16 v[200:203], v[128:131], v[108:111], v[200:203]\n\t" \
  "global_store_dwordx4 v2, v[42:45], %24 offset:1024 sc1\n\t" \
  "v_mfma_f32_16x16x32_bf16 v[200:203], v[120:123], v[148:151], v[200:203]\n\t" \
  "v_mfma_f32_16x16x32_bf16 v[208:211], v[120:123], v[64:67], v[208:211]\n\t" \
  "v_mfma_f32_16x16x32_bf16 v[208:211], v[128:131], v[104:107], v[208:211]\n\t" \
  "v_mov_b32 v30, 0xff61b1e6\n\t" \
  "v_bfe_i32 v34, v3, 0, 1\n\t" \
  "v_mfma_f32_16x16x32_bf16 v[248:251], v[128:131], v[136:139], v[248:251]\n\t" \
  "v_and_b32 v34, v30, v34\n\t" \
  "v_bfe_i32 v38, v3, 4, 1\n\t" \
  "v_mfma_f32_16x16x32_bf16 v[248:251], v[120:123], v[144:147], v[248:251]\n\t" \
  "v_and_b32 v38, v30, v38\n\t" \
  "v_bfe_i32 v42, v3, 8, 1\n\t" \
  "v_mfma_f32_16x16x32_bf16 v[252:255], v[72:75], v[136:139], v[252:255]\n\t" \
  "v_bfi_b32 v42, v42, v30, %15\n\t" \
  "v_mov_b32 v48, 0x3f803f80\n\t" \
  "s_waitcnt lgkmcnt(0)\n\t" \
  "v_mfma_f32_16x16x32_bf16 v[252:255], v[140:143], v[144:147], v[252:255]\n\t" \
  "v_bfe_i32 v35, v3, 1, 1\n\t" \
  "v_and_b32 v35, v30, v35\n\t" \
  "v_mfma_f32_16x16x32_bf16 v[188:191], v[140:143], v[124:127], v[188:191]\n\t" \
  "v_bfe_i32 v39, v3, 5, 1\n\t" \
  "v_and_b32 v39, v30, v39\n\t" \
  "v_mfma_f32_16x16x32_bf16 v[188:191], v[72:75], v[88:91], v[188:191]\n\t" \
  "v_bfe_i32 v43, v3, 9, 1\n\t" \
  "v_bfi_b32 v43, v43, v30, %15\n\t" \
  "s_barrier\n\t" \
  "v_mfma_f32_16x16x32_bf16 v[196:199], v[72:75], v[132:135], v[196:199]\n\t" \
  "ds_read_b64_tr_b16 v[80:81], v58 offset:34816\n\t" \
  "v_mov_b32 v49, 0x3f803f80\n\t" \
  "v_mfma_f32_16x16x32_bf16 v[196:199], v[140:143], v[96:99], v[196:199]\n\t" \
  "ds_read_b64_tr_b16 v[82:83], v58 offset:35328\n\t" \
  "v_bfe_i32 v36, v3, 2, 1\n\t" \
  "v_mfma_f32_16x16x32_bf16 v[204:207], v[140:143], v[148:151], v[204:207]\n\t" \
  "ds_read_b128 v[100:103], v56 offset:2048\n\t" \
  "v_and_b32 v36, v30, v36\n\t" \
  "v_mfma_f32_16x16x32_bf16 v[204:207], v[72:75], v[108:111], v[204:207]\n\t" \
  "ds_read_b64_tr_b16 v[92:93], v58 offset:38912\n\t" \
  "v_bfe_i32 v40, v3, 6, 1\n\t" \
  "v_mfma_f32_16x16x32_bf16 v[212:215], v[72:75], v[104:107], v[212:215]\n\t" \
  "ds_read_b64_tr_b16 v[94:95], v58 offset:39424\n\t" \
  "v_and_b32 v40, v30, v40\n\t" \
  "v_mfma_f32_16x16x32_bf16 v[212:215], v[140:143], v[64:67], v[212:215]\n\t" \
  "ds_read_b128 v[112:115], v57 offset:2048\n\t" \
  "v_bfe_i32 v44, v3, 10, 1\n\t" \
  "v_mfma_f32_16x16x32_bf16 v[152:155], v[68:71], v[88:91], v[152:155]\n\t" \
  "ds_read_b128 v[128:131], v57 offset:4096\n\t" \
  "v_bfi_b32 v44, v44, v30, %15\n\t" \
  "v_mfma_f32_16x16x32_bf16 v[152:155], v[116:119], v[124:127], v[152:155]\n\t" \
  "ds_read_b128 v[120:123], v56 offset:4096\n\t" \
  "v_mov_b32 v50, 0x3f803f80\n\t" \
  "v_mfma_f32_16x16x32_bf16 v[160:163], v[116:119], v[96:99], v[160:163]\n\t" \
  "ds_read_b128 v[136:139], v56 offset:6144\n\t" \
  "v_bfe_i32 v37, v3, 3, 1\n\t" \
  "v_mfma_f32_16x16x32_bf16 v[160:163], v[68:71], v[132:135], v[160:163]\n\t" \
  "ds_read_b128 v[144:147], v57 offset:6144\n\t" \
  "v_and_b32 v37, v30, v37\n\t" \
  "v_mfma_f32_16x16x32_bf16 v[168:171], v[68:71], v[108:111], v[168:171]\n\t" \
  "ds_read_b128 v[72:75], v57 offset:8192\n\t" \
  "v_bfe_i32 v41, v3, 7, 1\n\t" \
  "v_mfma_f32_16x16x32_bf16 v[168:171], v[116:119], v[148:151], v[168:171]\n\t" \
  "ds_read_b128 v[140:143], v56 offset:8192\n\t" \
  "v_and_b32 v41, v30, v41\n\t" \
  "v_mfma_f32_16x16x32_bf16 v[176:179], v[116:119], v[64:67], v[176:179]\n\t" \
  "ds_read_b64_tr_b16 v[116:117], v59 offset:34816\n\t" \
  "v_bfe_i32 v45, v3, 11, 1\n\t" \
  "v_mfma_f32_16x16x32_bf16 v[176:179], v[68:71], v[104:107], v[176:179]\n\t" \
  "ds_read_b64_tr_b16 v[118:119], v59 offset:35328\n\t" \
  "v_bfi_b32 v45, v45, v30, %15\n\t" \
  "v_mfma_f32_16x16x32_bf16 v[156:159], v[76:79], v[88:91], v[156:159]\n\t" \
  "ds_read_b64_tr_b16 v[68:69], v59 offset:38912\n\t" \
  "v_mov_b32 v51, 0x3f803f80\n\t" \
  "v_mfma_f32_16x16x32_bf16 v[156:159], v[84:87], v[124:127], v[156:159]\n\t" \
  "ds_read_b64_tr_b16 v[70:71], v59 offset:39424\n\t" \
  "v_bfe_i32 v46, v3, 12, 1\n\t" \
  "v_mfma_f32_16x16x32_bf16 v[164:167], v[84:87], v[96:99], v[164:167]\n\t" \
  "ds_read_b64_tr_b16 v[88:89], v58 offset:26624\n\t" \
  "v_bfe_i32 v31, v3, 13, 1\n\t" \
  "v_mfma_f32_16x16x32_bf16 v[164:167], v[76:79], v[132:135], v[164:167]\n\t" \
  "ds_read_b64_tr_b16 v[90:91], v58 offset:27136\n\t" \
  "v_perm_b32 v46, v31, v46, vcc_hi\n\t" \
  "v_mfma_f32_16x16x32_bf16 v[172:175], v[76:79], v[108:111], v[172:175]\n\t" \
  "ds_read_b64_tr_b16 v[124:125], v58 offset:30720\n\t" \
  "v_bfe_i32 v47, v3, 14, 1\n\t" \
  "v_mfma_f32_16x16x32_bf16 v[172:175], v[84:87], v[148:151], v[172:175]\n\t" \
  "ds_read_b64_tr_b16 v[126:127], v58 offset:31232\n\t" \
  "v_bfe_i32 v31, v3, 15, 1\n\t" \
  "v_mfma_f32_16x16x32_bf16 v[180:183], v[84:87], v[64:67], v[180:183]\n\t" \
  "ds_read_b128 v[96:99], v56 offset:0\n\t" \
  "v_perm_b32 v47, v31, v47, vcc_hi\n\t" \
  "v_mfma_f32_16x16x32_bf16 v[180:183], v[76:79], v[104:107], v[180:183]\n\t" \
  "ds_read_b128 v[132:135], v57 offset:0\n\t" \
  "v_add_u32 v30, 18432, v4\n\t" \
  "s_waitcnt lgkmcnt(7)\n\t" \
  "v_mfma_f32_16x16x32_bf16 v[216:219], v[100:103], v[80:83], v[216:219]\n\t" \
  "ds_read_b64_tr_b16 v[108:109], v59 offset:26624\n\t" \
  "v_xor_b32 v31, 1024, v30\n\t" \
  "v_mfma_f32_16x16x32_bf16 v[216:219], v[112:115], v[92:95], v[216:219]\n\t" \
  "ds_read_b64_tr_b16 v[110:111], v59 offset:27136\n\t" \
  "v_mov_b32 v32, %12\n\t" \
  "v_mfma_f32_16x16x32_bf16 v[224:227], v[128:131], v[92:95], v[224:227]\n\t" \
  "ds_read_b64_tr_b16 v[148:149], v59 offset:30720\n\t" \
  "v_xor_b32 v33, 4, v32\n\t" \
  "v_mfma_f32_16x16x32_bf16 v[224:227], v[120:123], v[80:83], v[224:227]\n\t" \
  "ds_read_b64_tr_b16 v[150:151], v59 offset:31232\n\t" \
  "v_mov_b32 v0, 1\n\t" \
  "v_mfma_f32_16x16x32_bf16 v[232:235], v[136:139], v[80:83], v[232:235]\n\t" \
  "ds_read_b64_tr_b16 v[64:65], v58 offset:18432\n\t" \
  "v_and_b32 v4, 1023, v4\n\t" \
  "v_mfma_f32_16x16x32_bf16 v[232:235], v[144:147], v[92:95], v[232:235]\n\t" \
  "ds_read_b64_tr_b16 v[66:67], v58 offset:18944\n\t" \
  "v_add_u32 v1, 16384, v4\n\t" \
  "v_mfma_f32_16x16x32_bf16 v[240:243], v[72:75], v[92:95], v[240:243]\n\t" \
  "ds_read_b64_tr_b16 v[84:85], v58 offset:22528\n\t" \
  "v_mfma_f32_16x16x32_bf16 v[240:243], v[140:143], v[80:83], v[240:243]\n\t" \
  "ds_read_b64_tr_b16 v[86:87], v58 offset:23040\n\t" \
  "v_mfma_f32_16x16x32_bf16 v[220:223], v[100:103], v[116:119], v[220:223]\n\t" \
  "ds_read_b64_tr_b16 v[76:77], v59 offset:18432\n\t" \
  "s_waitcnt lgkmcnt(7)\n\t" \
  "v_mfma_f32_16x16x32_bf16 v[220:223], v[112:115], v[68:71], v[220:223]\n\t" \
  "ds_read_b64_tr_b16 v[78:79], v59 offset:18944\n\t" \
  "v_mfma_f32_16x16x32_bf16 v[228:231], v[128:131], v[68:71], v[228:231]\n\t" \
  "ds_read_b64_tr_b16 v[104:105], v59 offset:22528\n\t" \
  "v_mfma_f32_16x16x32_bf16 v[228:231], v[120:123], v[116:119], v[228:231]\n\t" \
  "ds_read_b64_tr_b16 v[106:107], v59 offset:23040\n\t" \
  "v_mfma_f32_16x16x32_bf16 v[236:239], v[136:139], v[116:119], v[236:239]\n\t" \
  "v_mfma_f32_16x16x32_bf16 v[236:239], v[144:147], v[68:71], v[236:239]\n\t" \
  "v_mfma_f32_16x16x32_bf16 v[244:247], v[72:75], v[68:71], v[244:247]\n\t" \
  "v_mfma_f32_16x16x32_bf16 v[244:247], v[140:143], v[116:119], v[244:247]\n\t" \
  "v_mfma_f32_16x16x32_bf16 v[184:187], v[88:91], v[100:103], v[184:187]\n\t" \
  "v_mfma_f32_16x16x32_bf16 v[184:187], v[124:127], v[112:115], v[184:187]\n\t" \
  "v_mfma_f32_16x16x32_bf16 v[192:195], v[124:127], v[128:131], v[192:195]\n\t" \
  "v_mfma_f32_16x16x32_bf16 v[192:195], v[88:91], v[120:123], v[192:195]\n\t" \
  "v_mfma_f32_16x16x32_bf16 v[200:203], v[88:91], v[136:139], v[200:203]\n\t" \
  "v_mfma_f32_16x16x32_bf16 v[200:203], v[124:127], v[144:147], v[200:203]\n\t" \
  "v_mfma_f32_16x16x32_bf16 v[208:211], v[124:127], v[72:75], v[208:211]\n\t" \
  "v_mfma_f32_16x16x32_bf16 v[208:211], v[88:91], v[140:143], v[208:211]\n\t" \
  "v_mfma_f32_16x16x32_bf16 v[248:251], v[88:91], v[96:99], v[248:251]\n\t" \
  "v_mfma_f32_16x16x32_bf16 v[248:251], v[124:127], v[132:135], v[248:251]\n\t" \
  "v_mfma_f32_16x16x32_bf16 v[252:255], v[108:111], v[96:99], v[252:255]\n\t" \
  "s_waitcnt lgkmcnt(0)\n\t" \
  "v_mfma_f32_16x16x32_bf16 v[252:255], v[148:151], v[132:135], v[252:255]\n\t" \
  "v_mfma_f32_16x16x32_bf16 v[188:191], v[148:151], v[112:115], v[188:191]\n\t" \
  "v_mfma_f32_16x16x32_bf16 v[188:191], v[108:111], v[100:103], v[188:191]\n\t" \
  "s_barrier\n\t" \
  "v_mfma_f32_16x16x32_bf16 v[196:199], v[108:111], v[120:123], v[196:199]\n\t" \
  "ds_read_b64_tr_b16 v[92:93], v62 offset:34816\n\t" \
  "v_mfma_f32_16x16x32_bf16 v[196:199], v[148:151], v[128:131], v[196:199]\n\t" \
  "ds_read_b64_tr_b16 v[94:95], v62 offset:35328\n\t" \
  "v_mfma_f32_16x16x32_bf16 v[204:207], v[148:151], v[144:147], v[204:207]\n\t" \
  "ds_read_b128 v[80:83], v60 offset:2048\n\t" \
  "v_mfma_f32_16x16x32_bf16 v[204:207], v[108:111], v[136:139], v[204:207]\n\t" \
  "ds_read_b64_tr_b16 v[68:69], v62 offset:38912\n\t" \
  "v_mfma_f32_16x16x32_bf16 v[212:215], v[108:111], v[140:143], v[212:215]\n\t" \
  "ds_read_b64_tr_b16 v[70:71], v62 offset:39424\n\t" \
  "v_mfma_f32_16x16x32_bf16 v[212:215], v[148:151], v[72:75], v[212:215]\n\t" \
  "ds_read_b128 v[116:119], v61 offset:2048\n\t" \
  "v_mfma_f32_16x16x32_bf16 v[152:155], v[64:67], v[100:103], v[152:155]\n\t" \
  "ds_read_b128 v[88:91], v61 offset:4096\n\t" \
  "v_mfma_f32_16x16x32_bf16 v[152:155], v[84:87], v[112:115], v[152:155]\n\t" \
  "ds_read_b128 v[124:127], v60 offset:4096\n\t" \
  "v_mfma_f32_16x16x32_bf16 v[160:163], v[84:87], v[128:131], v[160:163]\n\t" \
  "ds_read_b128 v[96:99], v60 offset:6144\n\t" \
  "v_mfma_f32_16x16x32_bf16 v[160:163], v[64:67], v[120:123], v[160:163]\n\t" \
  "ds_read_b128 v[132:135], v61 offset:6144\n\t" \
  "v_mfma_f32_16x16x32_bf16 v[168:171], v[64:67], v[136:139], v[168:171]\n\t" \
  "ds_read_b128 v[108:111], v61 offset:8192\n\t" \
  "v_mfma_f32_16x16x32_bf16 v[168:171], v[84:87], v[144:147], v[168:171]\n\t" \
  "ds_read_b128 v[148:151], v60 offset:8192\n\t" \
  "v_mfma_f32_16x16x32_bf16 v[176:179], v[84:87], v[72:75], v[176:179]\n\t" \
  "ds_read_b64_tr_b16 v[84:85], v63 offset:34816\n\t" \
  "v_mfma_f32_16x16x32_bf16 v[176:179], v[64:67], v[140:143], v[176:179]\n\t" \
  "ds_read_b64_tr_b16 v[86:87], v63 offset:35328\n\t" \
  "v_mfma_f32_16x16x32_bf16 v[156:159], v[76:79], v[100:103], v[156:159]\n\t" \
  "ds_read_b64_tr_b16 v[64:65], v63 offset:38912\n\t" \
  "v_mfma_f32_16x16x32_bf16 v[156:159], v[104:107], v[112:115], v[156:159]\n\t" \
  "ds_read_b64_tr_b16 v[66:67], v63 offset:39424\n\t" \
  "v_mfma_f32_16x16x32_bf16 v[164:167], v[104:107], v[128:131], v[164:167]\n\t" \
  "ds_read_b64_tr_b16 v[100:101], v62 offset:26624\n\t" \
  "v_mfma_f32_16x16x32_bf16 v[164:167], v[76:79], v[120:123], v[164:167]\n\t" \
  "ds_read_b64_tr_b16 v[102:103], v62 offset:27136\n\t" \
  "v_mfma_f32_16x16x32_bf16 v[172:175], v[76:79], v[136:139], v[172:175]\n\t" \
  "ds_read_b64_tr_b16 v[112:113], v62 offset:30720\n\t" \
  "v_mfma_f32_16x16x32_bf16 v[172:175], v[104:107], v[144:147], v[172:175]\n\t" \
  "ds_read_b64_tr_b16 v[114:115], v62 offset:31232\n\t" \
  "v_mfma_f32_16x16x32_bf16 v[180:183], v[104:107], v[72:75], v[180:183]\n\t" \
  "ds_read_b128 v[128:131], v60 offset:0\n\t" \
  "v_mfma_f32_16x16x32_bf16 v[180:183], v[76:79], v[140:143], v[180:183]\n\t" \
  "ds_read_b128 v[120:123], v61 offset:0\n\t" \
  "s_waitcnt lgkmcnt(7)\n\t" \
  "v_mfma_f32_16x16x32_bf16 v[216:219], v[80:83], v[92:95], v[216:219]\n\t" \
  "ds_read_b64_tr_b16 v[136:137], v63 offset:26624\n\t" \
  "v_mfma_f32_16x16x32_bf16 v[216:219], v[116:119], v[68:71], v[216:219]\n\t" \
  "ds_read_b64_tr_b16 v[138:139], v63 offset:27136\n\t" \
  "v_mfma_f32_16x16x32_bf16 v[224:227], v[88:91], v[68:71], v[224:227]\n\t" \
  "ds_read_b64_tr_b16 v[144:145], v63 offset:30720\n\t" \
  "v_mfma_f32_16x16x32_bf16 v[224:227], v[124:127], v[92:95], v[224:227]\n\t" \
  "ds_read_b64_tr_b16 v[146:147], v63 offset:31232\n\t" \
  "v_mfma_f32_16x16x32_bf16 v[232:235], v[96:99], v[92:95], v[232:235]\n\t" \
  "ds_read_b64_tr_b16 v[72:73], v62 offset:18432\n\t" \
  "v_mfma_f32_16x16x32_bf16 v[232:235], v[132:135], v[68:71], v[232:235]\n\t" \
  "ds_read_b64_tr_b16 v[74:75], v62 offset:18944\n\t" \
  "v_mfma_f32_16x16x32_bf16 v[240:243], v[108:111], v[68:71], v[240:243]\n\t" \
  "ds_read_b64_tr_b16 v[104:105], v62 offset:22528\n\t" \
  "v_mfma_f32_16x16x32_bf16 v[240:243], v[148:151], v[92:95], v[240:243]\n\t" \
  "ds_read_b64_tr_b16 v[106:107], v62 offset:23040\n\t" \
  "v_mfma_f32_16x16x32_bf16 v[220:223], v[80:83], v[84:87], v[220:223]\n\t" \
  "ds_read_b64_tr_b16 v[76:77], v63 offset:18432\n\t" \
  "s_waitcnt lgkmcnt(7)\n\t" \
  "v_mfma_f32_16x16x32_bf16 v[220:223], v[116:119], v[64:67], v[220:223]\n\t" \
  "ds_read_b64_tr_b16 v[78:79], v63 offset:18944\n\t" \
  "v_mfma_f32_16x16x32_bf16 v[228:231], v[88:91], v[64:67], v[228:231]\n\t" \
  "ds_read_b64_tr_b16 v[140:141], v63 offset:22528\n\t" \
  "v_mfma_f32_16x16x32_bf16 v[228:231], v[124:127], v[84:87], v[228:231]\n\t" \
  "ds_read_b64_tr_b16 v[142:143], v63 offset:23040\n\t" \
  "v_mfma_f32_16x16x32_bf16 v[236:239], v[96:99], v[84:87], v[236:239]\n\t" \
  "v_mfma_f32_16x16x32_bf16 v[236:239], v[132:135], v[64:67], v[236:239]\n\t" \
  "v_mfma_f32_16x16x32_bf16 v[244:247], v[108:111], v[64:67], v[244:247]\n\t" \
  "v_mfma_f32_16x16x32_bf16 v[244:247], v[148:151], v[84:87], v[244:247]\n\t" \
  "v_mfma_f32_16x16x32_bf16 v[184:187], v[100:103], v[80:83], v[184:187]\n\t" \
  "v_mfma_f32_16x16x32_bf16 v[184:187], v[112:115], v[116:119], v[184:187]\n\t" \
  "v_mfma_f32_16x16x32_bf16 v[192:195], v[112:115], v[88:91], v[192:195]\n\t" \
  "v_mfma_f32_16x16x32_bf16 v[192:195], v[100:103], v[124:127], v[192:195]\n\t" \
  "v_mfma_f32_16x16x32_bf16 v[200:203], v[100:103], v[96:99], v[200:203]\n\t" \
  "v_mfma_f32_16x16x32_bf16 v[200:203], v[112:115], v[132:135], v[200:203]\n\t" \
  "v_mfma_f32_16x16x32_bf16 v[208:211], v[112:115], v[108:111], v[208:211]\n\t" \
  "v_mfma_f32_16x16x32_bf16 v[208:211], v[100:103], v[148:151], v[208:211]\n\t" \
  "v_mfma_f32_16x16x32_bf16 v[248:251], v[100:103], v[128:131], v[248:251]\n\t" \
  "v_mfma_f32_16x16x32_bf16 v[248:251], v[112:115], v[120:123], v[248:251]\n\t" \
  "v_mfma_f32_16x16x32_bf16 v[252:255], v[136:139], v[128:131], v[252:255]\n\t" \
  "s_waitcnt lgkmcnt(0)\n\t" \
  "v_mfma_f32_16x16x32_bf16 v[252:255], v[144:147], v[120:123], v[252:255]\n\t" \
  "v_mfma_f32_16x16x32_bf16 v[188:191], v[144:147], v[116:119], v[188:191]\n\t" \
  "v_mfma_f32_16x16x32_bf16 v[188:191], v[136:139], v[80:83], v[188:191]\n\t" \
  "s_waitcnt vmcnt(0)\n\t" \
  "s_barrier\n\t" \
  "v_mfma_f32_16x16x32_bf16 v[196:199], v[136:139], v[124:127], v[196:199]\n\t" \
  "ds_read_b64_tr_b16 v[68:69], v54 offset:34816\n\t" \
  "v_mfma_f32_16x16x32_bf16 v[196:199], v[144:147], v[88:91], v[196:199]\n\t" \
  "ds_read_b64_tr_b16 v[70:71], v54 offset:35328\n\t" \
  "v_mfma_f32_16x16x32_bf16 v[204:207], v[144:147], v[132:135], v[204:207]\n\t" \
  "ds_read_b128 v[92:95], v52 offset:2048\n\t" \
  "v_mfma_f32_16x16x32_bf16 v[204:207], v[136:139], v[96:99], v[204:207]\n\t" \
  "ds_read_b64_tr_b16 v[64:65], v54 offset:38912\n\t" \
  "v_mfma_f32_16x16x32_bf16 v[212:215], v[136:139], v[148:151], v[212:215]\n\t" \
  "ds_read_b64_tr_b16 v[66:67], v54 offset:39424\n\t" \
  "v_mfma_f32_16x16x32_bf16 v[212:215], v[144:147], v[108:111], v[212:215]\n\t" \
  "ds_read_b128 v[84:87], v53 offset:2048\n\t" \
  "v_mfma_f32_16x16x32_bf16 v[152:155], v[72:75], v[80:83], v[152:155]\n\t" \
  "ds_read_b128 v[100:103], v53 offset:4096\n\t" \
  "v_mfma_f32_16x16x32_bf16 v[152:155], v[104:107], v[116:119], v[152:155]\n\t" \
  "ds_read_b128 v[112:115], v52 offset:4096\n\t" \
  "v_mfma_f32_16x16x32_bf16 v[160:163], v[104:107], v[88:91], v[160:163]\n\t" \
  "ds_read_b128 v[128:131], v52 offset:6144\n\t" \
  "v_mfma_f32_16x16x32_bf16 v[160:163], v[72:75], v[124:127], v[160:163]\n\t" \
  "ds_read_b128 v[120:123], v53 offset:6144\n\t" \
  "v_mfma_f32_16x16x32_bf16 v[168:171], v[72:75], v[96:99], v[168:171]\n\t" \
  "ds_read_b128 v[136:139], v53 offset:8192\n\t" \
  "v_mfma_f32_16x16x32_bf16 v[168:171], v[104:107], v[132:135], v[168:171]\n\t" \
  "ds_read_b128 v[144:147], v52 offset:8192\n\t" \
  "v_mfma_f32_16x16x32_bf16 v[176:179], v[104:107], v[108:111], v[176:179]\n\t" \
  "ds_read_b64_tr_b16 v[104:105], v55 offset:34816\n\t" \
  "v_mfma_f32_16x16x32_bf16 v[176:179], v[72:75], v[148:151], v[176:179]\n\t" \
  "ds_read_b64_tr_b16 v[106:107], v55 offset:35328\n\t" \
  "v_mfma_f32_16x16x32_bf16 v[156:159], v[76:79], v[80:83], v[156:159]\n\t" \
  "ds_read_b64_tr_b16 v[72:73], v55 offset:38912\n\t" \
  "v_mfma_f32_16x16x32_bf16 v[156:159], v[140:143], v[116:119], v[156:159]\n\t" \
  "ds_read_b64_tr_b16 v[74:75], v55 offset:39424\n\t" \
  "v_mfma_f32_16x16x32_bf16 v[164:167], v[140:143], v[88:91], v[164:167]\n\t" \
  "ds_read_b64_tr_b16 v[80:81], v54 offset:26624\n\t" \
  "v_mfma_f32_16x16x32_bf16 v[164:167], v[76:79], v[124:127], v[164:167]\n\t" \
  "ds_read_b64_tr_b16 v[82:83], v54 offset:27136\n\t" \
  "v_mfma_f32_16x16x32_bf16 v[172:175], v[76:79], v[96:99], v[172:175]\n\t" \
  "ds_read_b64_tr_b16 v[116:117], v54 offset:30720\n\t" \
  "v_mfma_f32_16x16x32_bf16 v[172:175], v[140:143], v[132:135], v[172:175]\n\t" \
  "ds_read_b64_tr_b16 v[118:119], v54 offset:31232\n\t" \
  "v_mfma_f32_16x16x32_bf16 v[180:183], v[140:143], v[108:111], v[180:183]\n\t" \
  "ds_read_b128 v[88:91], v52 offset:0\n\t" \
  "v_mfma_f32_16x16x32_bf16 v[180:183], v[76:79], v[148:151], v[180:183]\n\t" \
  "ds_read_b128 v[124:127], v53 offset:0\n\t" \
  "s_waitcnt lgkmcnt(7)\n\t" \
  "v_mfma_f32_16x16x32_bf16 v[216:219], v[92:95], v[68:71], v[216:219]\n\t" \
  "ds_read_b64_tr_b16 v[96:97], v55 offset:26624\n\t" \
  "v_mfma_f32_16x16x32_bf16 v[216:219], v[84:87], v[64:67], v[216:219]\n\t" \
  "ds_read_b64_tr_b16 v[98:99], v55 offset:27136\n\t" \
  "v_mfma_f32_16x16x32_bf16 v[224:227], v[100:103], v[64:67], v[224:227]\n\t" \
  "ds_read_b64_tr_b16 v[132:133], v55 offset:30720\n\t" \
  "v_mfma_f32_16x16x32_bf16 v[224:227], v[112:115], v[68:71], v[224:227]\n\t" \
  "ds_read_b64_tr_b16 v[134:135], v55 offset:31232\n\t" \
  "v_mfma_f32_16x16x32_bf16 v[232:235], v[128:131], v[68:71], v[232:235]\n\t" \
  "ds_read_b64_tr_b16 v[108:109], v54 offset:18432\n\t" \
  "v_mfma_f32_16x16x32_bf16 v[232:235], v[120:123], v[64:67], v[232:235]\n\t" \
  "ds_read_b64_tr_b16 v[110:111], v54 offset:18944\n\t" \
  "v_mfma_f32_16x16x32_bf16 v[240:243], v[136:139], v[64:67], v[240:243]\n\t" \
  "ds_read_b64_tr_b16 v[140:141], v54 offset:22528\n\t" \
  "v_mfma_f32_16x16x32_bf16 v[240:243], v[144:147], v[68:71], v[240:243]\n\t" \
  "ds_read_b64_tr_b16 v[142:143], v54 offset:23040\n\t" \
  "v_mfma_f32_16x16x32_bf16 v[220:223], v[92:95], v[104:107], v[220:223]\n\t" \
  "ds_read_b64_tr_b16 v[76:77], v55 offset:18432\n\t" \
  "s_waitcnt lgkmcnt(7)\n\t" \
  "v_mfma_f32_16x16x32_bf16 v[220:223], v[84:87], v[72:75], v[220:223]\n\t" \
  "ds_read_b64_tr_b16 v[78:79], v55 offset:18944\n\t" \
  "v_mfma_f32_16x16x32_bf16 v[228:231], v[100:103], v[72:75], v[228:231]\n\t" \
  "ds_read_b64_tr_b16 v[148:149], v55 offset:22528\n\t" \
  "v_mfma_f32_16x16x32_bf16 v[228:231], v[112:115], v[104:107], v[228:231]\n\t" \
  "ds_read_b64_tr_b16 v[150:151], v55 offset:23040\n\t" \
  "v_mfma_f32_16x16x32_bf16 v[236:239], v[128:131], v[104:107], v[236:239]\n\t" \
  "v_mfma_f32_16x16x32_bf16 v[236:239], v[120:123], v[72:75], v[236:239]\n\t" \
  "v_mfma_f32_16x16x32_bf16 v[244:247], v[136:139], v[72:75], v[244:247]\n\t" \
  "v_mfma_f32_16x16x32_bf16 v[244:247], v[144:147], v[104:107], v[244:247]\n\t" \
  "v_mfma_f32_16x16x32_bf16 v[184:187], v[80:83], v[92:95], v[184:187]\n\t" \
  "ds_read_b32 v10, v32 offset:104\n\t" \
  "v_mfma_f32_16x16x32_bf16 v[184:187], v[116:119], v[84:87], v[184:187]\n\t" \
  "v_lshlrev_b32 v11, 3, v4\n\t" \
  "v_mfma_f32_16x16x32_bf16 v[192:195], v[116:119], v[100:103], v[192:195]\n\t" \
  "v_mfma_f32_16x16x32_bf16 v[192:195], v[80:83], v[112:115], v[192:195]\n\t" \
  "v_mfma_f32_16x16x32_bf16 v[200:203], v[80:83], v[128:131], v[200:203]\n\t" \
  "s_waitcnt lgkmcnt(0)\n\t" \
  "s_cmp_lg_u32 %13, 0\n\t" \
  "s_cbranch_scc1 27f\n\t" \
  "s_mov_b32 exec_hi, 0\n\t" \
  "global_store_dword v11, v10, %25 sc1\n\t" \
  "s_mov_b32 exec_hi, -1\n\t" \
  "27:\n\t" \
  "v_mfma_f32_16x16x32_bf16 v[200:203], v[116:119], v[120:123], v[200:203]\n\t" \
  "v_mfma_f32_16x16x32_bf16 v[208:211], v[116:119], v[136:139], v[208:211]\n\t" \
  "v_mfma_f32_16x16x32_bf16 v[208:211], v[80:83], v[144:147], v[208:211]\n\t" \
  "v_mfma_f32_16x16x32_bf16 v[248:251], v[80:83], v[88:91], v[248:251]\n\t" \
  "v_mfma_f32_16x16x32_bf16 v[248:251], v[116:119], v[124:127], v[248:251]\n\t" \
  "v_mfma_f32_16x16x32_bf16 v[252:255], v[96:99], v[88:91], v[252:255]\n\t" \
  "v_mfma_f32_16x16x32_bf16 v[252:255], v[132:135], v[124:127], v[252:255]\n\t" \
  "v_mfma_f32_16x16x32_bf16 v[188:191], v[132:135], v[84:87], v[188:191]\n\t" \
  "v_mfma_f32_16x16x32_bf16 v[188:191], v[96:99], v[92:95], v[188:191]\n\t" \
  "s_barrier\n\t" \
  "v_mfma_f32_16x16x32_bf16 v[196:199], v[96:99], v[112:115], v[196:199]\n\t" \
  "ds_read_b64_tr_b16 v[64:65], v58 offset:34816\n\t" \
  "v_mfma_f32_16x16x32_bf16 v[196:199], v[132:135], v[100:103], v[196:199]\n\t" \
  "ds_read_b64_tr_b16 v[66:67], v58 offset:35328\n\t" \
  "v_mfma_f32_16x16x32_bf16 v[204:207], v[132:135], v[120:123], v[204:207]\n\t" \
  "ds_read_b128 v[68:71], v56 offset:2048\n\t" \
  "v_mfma_f32_16x16x32_bf16 v[204:207], v[96:99], v[128:131], v[204:207]\n\t" \
  "ds_read_b64_tr_b16 v[72:73], v58 offset:38912\n\t" \
  "v_mfma_f32_16x16x32_bf16 v[212:215], v[96:99], v[144:147], v[212:215]\n\t" \
  "ds_read_b64_tr_b16 v[74:75], v58 offset:39424\n\t" \
  "v_mfma_f32_16x16x32_bf16 v[212:215], v[132:135], v[136:139], v[212:215]\n\t" \
  "ds_read_b128 v[104:107], v57 offset:2048\n\t" \
  "v_mfma_f32_16x16x32_bf16 v[152:155], v[108:111], v[92:95], v[152:155]\n\t" \
  "ds_read_b128 v[80:83], v57 offset:4096\n\t" \
  "v_mfma_f32_16x16x32_bf16 v[152:155], v[140:143], v[84:87], v[152:155]\n\t" \
  "ds_read_b128 v[116:119], v56 offset:4096\n\t" \
  "v_mfma_f32_16x16x32_bf16 v[160:163], v[140:143], v[100:103], v[160:163]\n\t" \
  "ds_read_b128 v[88:91], v56 offset:6144\n\t" \
  "v_mfma_f32_16x16x32_bf16 v[160:163], v[108:111], v[112:115], v[160:163]\n\t" \
  "ds_read_b128 v[124:127], v57 offset:6144\n\t" \
  "v_mfma_f32_16x16x32_bf16 v[168:171], v[108:111], v[128:131], v[168:171]\n\t" \
  "ds_read_b128 v[96:99], v57 offset:8192\n\t" \
  "v_mfma_f32_16x16x32_bf16 v[168:171], v[140:143], v[120:123], v[168:171]\n\t" \
  "ds_read_b128 v[132:135], v56 offset:8192\n\t" \
  "v_mfma_f32_16x16x32_bf16 v[176:179], v[140:143], v[136:139], v[176:179]\n\t" \
  "ds_read_b64_tr_b16 v[140:141], v59 offset:34816\n\t" \
  "v_mfma_f32_16x16x32_bf16 v[176:179], v[108:111], v[144:147], v[176:179]\n\t" \
  "ds_read_b64_tr_b16 v[142:143], v59 offset:35328\n\t" \
  "v_mfma_f32_16x16x32_bf16 v[156:159], v[76:79], v[92:95], v[156:159]\n\t" \
  "ds_read_b64_tr_b16 v[108:109], v59 offset:38912\n\t" \
  "v_mfma_f32_16x16x32_bf16 v[156:159], v[148:151], v[84:87], v[156:159]\n\t" \
  "ds_read_b64_tr_b16 v[110:111], v59 offset:39424\n\t" \
  "v_mfma_f32_16x16x32_bf16 v[164:167], v[148:151], v[100:103], v[164:167]\n\t" \
  "ds_read_b64_tr_b16 v[92:93], v58 offset:26624\n\t" \
  "v_mfma_f32_16x16x32_bf16 v[164:167], v[76:79], v[112:115], v[164:167]\n\t" \
  "ds_read_b64_tr_b16 v[94:95], v58 offset:27136\n\t" \
  "v_mfma_f32_16x16x32_bf16 v[172:175], v[76:79], v[128:131], v[172:175]\n\t" \
  "ds_read_b64_tr_b16 v[84:85], v58 offset:30720\n\t" \
  "v_mfma_f32_16x16x32_bf16 v[172:175], v[148:151], v[120:123], v[172:175]\n\t" \
  "ds_read_b64_tr_b16 v[86:87], v58 offset:31232\n\t" \
  "v_mfma_f32_16x16x32_bf16 v[180:183], v[148:151], v[136:139], v[180:183]\n\t" \
  "ds_read_b128 v[100:103], v56 offset:0\n\t" \
  "v_mfma_f32_16x16x32_bf16 v[180:183], v[76:79], v[144:147], v[180:183]\n\t" \
  "ds_read_b128 v[112:115], v57 offset:0\n\t" \
  "s_waitcnt lgkmcnt(7)\n\t" \
  "v_mfma_f32_16x16x32_bf16 v[216:219], v[68:71], v[64:67], v[216:219]\n\t" \
  "ds_read_b64_tr_b16 v[128:129], v59 offset:26624\n\t" \
  "v_mfma_f32_16x16x32_bf16 v[216:219], v[104:107], v[72:75], v[216:219]\n\t" \
  "ds_read_b64_tr_b16 v[130:131], v59 offset:27136\n\t" \
  "v_mfma_f32_16x16x32_bf16 v[224:227], v[80:83], v[72:75], v[224:227]\n\t" \
  "ds_read_b64_tr_b16 v[120:121], v59 offset:30720\n\t" \
  "v_mfma_f32_16x16x32_bf16 v[224:227], v[116:119], v[64:67], v[224:227]\n\t" \
  "ds_read_b64_tr_b16 v[122:123], v59 offset:31232\n\t" \
  "v_mfma_f32_16x16x32_bf16 v[232:235], v[88:91], v[64:67], v[232:235]\n\t" \
  "ds_read_b64_tr_b16 v[136:137], v58 offset:18432\n\t" \
  "v_mfma_f32_16x16x32_bf16 v[232:235], v[124:127], v[72:75], v[232:235]\n\t" \
  "ds_read_b64_tr_b16 v[138:139], v58 offset:18944\n\t" \
  "v_mfma_f32_16x16x32_bf16 v[240:243], v[96:99], v[72:75], v[240:243]\n\t" \
  "ds_read_b64_tr_b16 v[148:149], v58 offset:22528\n\t" \
  "v_mfma_f32_16x16x32_bf16 v[240:243], v[132:135], v[64:67], v[240:243]\n\t" \
  "ds_read_b64_tr_b16 v[150:151], v58 offset:23040\n\t" \
  "v_mfma_f32_16x16x32_bf16 v[220:223], v[68:71], v[140:143], v[220:223]\n\t" \
  "ds_read_b64_tr_b16 v[76:77], v59 offset:18432\n\t" \
  "s_waitcnt lgkmcnt(7)\n\t" \
  "v_mfma_f32_16x16x32_bf16 v[220:223], v[104:107], v[108:111], v[220:223]\n\t" \
  "ds_read_b64_tr_b16 v[78:79], v59 offset:18944\n\t" \
  "v_mfma_f32_16x16x32_bf16 v[228:231], v[80:83], v[108:111], v[228:231]\n\t" \
  "ds_read_b64_tr_b16 v[144:145], v59 offset:22528\n\t" \
  "v_mfma_f32_16x16x32_bf16 v[228:231], v[116:119], v[140:143], v[228:231]\n\t" \
  "ds_read_b64_tr_b16 v[146:147], v59 offset:23040\n\t" \
  "v_mfma_f32_16x16x32_bf16 v[236:239], v[88:91], v[140:143], v[236:239]\n\t" \
  "v_mfma_f32_16x16x32_bf16 v[236:239], v[124:127], v[108:111], v[236:239]\n\t" \
  "v_mfma_f32_16x16x32_bf16 v[244:247], v[96:99], v[108:111], v[244:247]\n\t" \
  "v_mfma_f32_16x16x32_bf16 v[244:247], v[132:135], v[140:143], v[244:247]\n\t" \
  "v_mfma_f32_16x16x32_bf16 v[184:187], v[92:95], v[68:71], v[184:187]\n\t" \
  "v_mfma_f32_16x16x32_bf16 v[184:187], v[84:87], v[104:107], v[184:187]\n\t" \
  "v_mfma_f32_16x16x32_bf16 v[192:195], v[84:87], v[80:83], v[192:195]\n\t" \
  "v_mfma_f32_16x16x32_bf16 v[192:195], v[92:95], v[116:119], v[192:195]\n\t" \
  "v_mfma_f32_16x16x32_bf16 v[200:203], v[92:95], v[88:91], v[200:203]\n\t" \
  "v_mfma_f32_16x16x32_bf16 v[200:203], v[84:87], v[124:127], v[200:203]\n\t" \
  "v_mfma_f32_16x16x32_bf16 v[208:211], v[84:87], v[96:99], v[208:211]\n\t" \
  "v_mfma_f32_16x16x32_bf16 v[208:211], v[92:95], v[132:135], v[208:211]\n\t" \
  "v_mfma_f32_16x16x32_bf16 v[248:251], v[92:95], v[100:103], v[248:251]\n\t" \
  "v_mfma_f32_16x16x32_bf16 v[248:251], v[84:87], v[112:115], v[248:251]\n\t" \
  "v_mfma_f32_16x16x32_bf16 v[252:255], v[128:131], v[100:103], v[252:255]\n\t" \
  "s_waitcnt lgkmcnt(0)\n\t" \
  "v_mfma_f32_16x16x32_bf16 v[252:255], v[120:123], v[112:115], v[252:255]\n\t" \
  "v_mfma_f32_16x16x32_bf16 v[188:191], v[120:123], v[104:107], v[188:191]\n\t" \
  "v_mfma_f32_16x16x32_bf16 v[188:191], v[128:131], v[68:71], v[188:191]\n\t" \
  "s_barrier\n\t" \
  "v_mfma_f32_16x16x32_bf16 v[196:199], v[128:131], v[116:119], v[196:199]\n\t" \
  "ds_read_b64_tr_b16 v[72:73], v62 offset:34816\n\t" \
  "v_mfma_f32_16x16x32_bf16 v[196:199], v[120:123], v[80:83], v[196:199]\n\t" \
  "ds_read_b64_tr_b16 v[74:75], v62 offset:35328\n\t" \
  "v_mfma_f32_16x16x32_bf16 v[204:207], v[120:123], v[124:127], v[204:207]\n\t" \
  "ds_read_b128 v[64:67], v60 offset:2048\n\t" \
  "v_mfma_f32_16x16x32_bf16 v[204:207], v[128:131], v[88:91], v[204:207]\n\t" \
  "ds_read_b64_tr_b16 v[108:109], v62 offset:38912\n\t" \
  "v_mfma_f32_16x16x32_bf16 v[212:215], v[128:131], v[132:135], v[212:215]\n\t" \
  "ds_read_b64_tr_b16 v[110:111], v62 offset:39424\n\t" \
  "v_mfma_f32_16x16x32_bf16 v[212:215], v[120:123], v[96:99], v[212:215]\n\t" \
  "ds_read_b128 v[140:143], v61 offset:2048\n\t" \
  "v_mfma_f32_16x16x32_bf16 v[152:155], v[136:139], v[68:71], v[152:155]\n\t" \
  "ds_read_b128 v[92:95], v61 offset:4096\n\t" \
  "v_mfma_f32_16x16x32_bf16 v[152:155], v[148:151], v[104:107], v[152:155]\n\t" \
  "ds_read_b128 v[84:87], v60 offset:4096\n\t" \
  "v_mfma_f32_16x16x32_bf16 v[160:163], v[148:151], v[80:83], v[160:163]\n\t" \
  "ds_read_b128 v[100:103], v60 offset:6144\n\t" \
  "v_mfma_f32_16x16x32_bf16 v[160:163], v[136:139], v[116:119], v[160:163]\n\t" \
  "ds_read_b128 v[112:115], v61 offset:6144\n\t" \
  "v_mfma_f32_16x16x32_bf16 v[168:171], v[136:139], v[88:91], v[168:171]\n\t" \
  "ds_read_b128 v[128:131], v61 offset:8192\n\t" \
  "v_mfma_f32_16x16x32_bf16 v[168:171], v[148:151], v[124:127], v[168:171]\n\t" \
  "ds_read_b128 v[120:123], v60 offset:8192\n\t" \
  "v_mfma_f32_16x16x32_bf16 v[176:179], v[148:151], v[96:99], v[176:179]\n\t" \
  "ds_read_b64_tr_b16 v[148:149], v63 offset:34816\n\t" \
  "v_mfma_f32_16x16x32_bf16 v[176:179], v[136:139], v[132:135], v[176:179]\n\t" \
  "ds_read_b64_tr_b16 v[150:151], v63 offset:35328\n\t" \
  "v_mfma_f32_16x16x32_bf16 v[156:159], v[76:79], v[68:71], v[156:159]\n\t" \
  "ds_read_b64_tr_b16 v[136:137], v63 offset:38912\n\t" \
  "v_mfma_f32_16x16x32_bf16 v[156:159], v[144:147], v[104:107], v[156:159]\n\t" \
  "ds_read_b64_tr_b16 v[138:139], v63 offset:39424\n\t" \
  "v_mfma_f32_16x16x32_bf16 v[164:167], v[144:147], v[80:83], v[164:167]\n\t" \
  "ds_read_b64_tr_b16 v[68:69], v62 offset:26624\n\t" \
  "v_mfma_f32_16x16x32_bf16 v[164:167], v[76:79], v[116:119], v[164:167]\n\t" \
  "ds_read_b64_tr_b16 v[70:71], v62 offset:27136\n\t" \
  "v_mfma_f32_16x16x32_bf16 v[172:175], v[76:79], v[88:91], v[172:175]\n\t" \
  "ds_read_b64_tr_b16 v[104:105], v62 offset:30720\n\t" \
  "v_mfma_f32_16x16x32_bf16 v[172:175], v[144:147], v[124:127], v[172:175]\n\t" \
  "ds_read_b64_tr_b16 v[106:107], v62 offset:31232\n\t" \
  "v_mfma_f32_16x16x32_bf16 v[180:183], v[144:147], v[96:99], v[180:183]\n\t" \
  "ds_read_b128 v[80:83], v60 offset:0\n\t" \
  "v_mfma_f32_16x16x32_bf16 v[180:183], v[76:79], v[132:135], v[180:183]\n\t" \
  "ds_read_b128 v[116:119], v61 offset:0\n\t" \
  "s_waitcnt lgkmcnt(7)\n\t" \
  "v_mfma_f32_16x16x32_bf16 v[216:219], v[64:67], v[72:75], v[216:219]\n\t" \
  "ds_read_b64_tr_b16 v[88:89], v63 offset:26624\n\t" \
  "v_mfma_f32_16x16x32_bf16 v[216:219], v[140:143], v[108:111], v[216:219]\n\t" \
  "505:\n\t" \
  "s_cbranch_execz 509f\n\t" \
  "ds_read_b64_tr_b16 v[90:91], v63 offset:27136\n\t" \
  "v_mfma_f32_16x16x32_bf16 v[224:227], v[92:95], v[108:111], v[224:227]\n\t" \
  "ds_read_b64_tr_b16 v[124:125], v63 offset:30720\n\t" \
  "v_mfma_f32_16x16x32_bf16 v[224:227], v[84:87], v[72:75], v[224:227]\n\t" \
  "ds_read_b64_tr_b16 v[126:127], v63 offset:31232\n\t" \
  "v_mfma_f32_16x16x32_bf16 v[232:235], v[100:103], v[72:75], v[232:235]\n\t" \
  "ds_read_b64_tr_b16 v[96:97], v62 offset:18432\n\t" \
  "v_mfma_f32_16x16x32_bf16 v[232:235], v[112:115], v[108:111], v[232:235]\n\t" \
  "ds_read_b64_tr_b16 v[98:99], v62 offset:18944\n\t" \
  "v_mfma_f32_16x16x32_bf16 v[240:243], v[128:131], v[108:111], v[240:243]\n\t" \
  "ds_read_b64_tr_b16 v[144:145], v62 offset:22528\n\t" \
  "v_mfma_f32_16x16x32_bf16 v[240:243], v[120:123], v[72:75], v[240:243]\n\t" \
  "ds_read_b64_tr_b16 v[146:147], v62 offset:23040\n\t" \
  "v_mfma_f32_16x16x32_bf16 v[220:223], v[64:67], v[148:151], v[220:223]\n\t" \
  "ds_read_b64_tr_b16 v[76:77], v63 offset:18432\n\t" \
  "s_waitcnt lgkmcnt(7)\n\t" \
  "v_mfma_f32_16x16x32_bf16 v[220:223], v[140:143], v[136:139], v[220:223]\n\t" \
  "ds_read_b64_tr_b16 v[78:79], v63 offset:18944\n\t" \
  "v_mfma_f32_16x16x32_bf16 v[228:231], v[92:95], v[136:139], v[228:231]\n\t" \
  "ds_read_b64_tr_b16 v[132:133], v63 offset:22528\n\t" \
  "v_mfma_f32_16x16x32_bf16 v[228:231], v[84:87], v[148:151], v[228:231]\n\t" \
  "ds_read_b64_tr_b16 v[134:135], v63 offset:23040\n\t" \
  "v_mfma_f32_16x16x32_bf16 v[236:239], v[100:103], v[148:151], v[236:239]\n\t" \
  "v_mfma_f32_16x16x32_bf16 v[236:239], v[112:115], v[136:139], v[236:239]\n\t" \
  "v_mfma_f32_16x16x32_bf16 v[244:247], v[128:131], v[136:139], v[244:247]\n\t" \
  "v_mfma_f32_16x16x32_bf16 v[244:247], v[120:123], v[148:151], v[244:247]\n\t" \
  "v_mfma_f32_16x16x32_bf16 v[184:187], v[68:71], v[64:67], v[184:187]\n\t" \
  "v_mfma_f32_16x16x32_bf16 v[184:187], v[104:107], v[140:143], v[184:187]\n\t" \
  "v_mfma_f32_16x16x32_bf16 v[192:195], v[104:107], v[92:95], v[192:195]\n\t" \
  "v_mfma_f32_16x16x32_bf16 v[192:195], v[68:71], v[84:87], v[192:195]\n\t" \
  "v_mfma_f32_16x16x32_bf16 v[200:203], v[68:71], v[100:103], v[200:203]\n\t" \
  "v_mfma_f32_16x16x32_bf16 v[200:203], v[104:107], v[112:115], v[200:203]\n\t" \
  "v_mfma_f32_16x16x32_bf16 v[208:211], v[104:107], v[128:131], v[208:211]\n\t" \
  "v_mfma_f32_16x16x32_bf16 v[208:211], v[68:71], v[120:123], v[208:211]\n\t" \
  "v_mfma_f32_16x16x32_bf16 v[248:251], v[68:71], v[80:83], v[248:251]\n\t" \
  "v_mfma_f32_16x16x32_bf16 v[248:251], v[104:107], v[116:119], v[248:251]\n\t" \
  "v_mfma_f32_16x16x32_bf16 v[252:255], v[88:91], v[80:83], v[252:255]\n\t" \
  "s_waitcnt lgkmcnt(0)\n\t" \
  "v_mfma_f32_16x16x32_bf16 v[252:255], v[124:127], v[116:119], v[252:255]\n\t" \
  "v_mfma_f32_16x16x32_bf16 v[188:191], v[124:127], v[140:143], v[188:191]\n\t" \
  "v_mfma_f32_16x16x32_bf16 v[188:191], v[88:91], v[64:67], v[188:191]\n\t" \
  "s_barrier\n\t" \
  "v_mfma_f32_16x16x32_bf16 v[196:199], v[88:91], v[84:87], v[196:199]\n\t" \
  "ds_read_b64_tr_b16 v[108:109], v54 offset:34816\n\t" \
  "v_mfma_f32_16x16x32_bf16 v[196:199], v[124:127], v[92:95], v[196:199]\n\t" \
  "ds_read_b64_tr_b16 v[110:111], v54 offset:35328\n\t" \
  "v_mfma_f32_16x16x32_bf16 v[204:207], v[124:127], v[112:115], v[204:207]\n\t" \
  "ds_read_b128 v[72:75], v52 offset:2048\n\t" \
  "v_mfma_f32_16x16x32_bf16 v[204:207], v[88:91], v[100:103], v[204:207]\n\t" \
  "ds_read_b64_tr_b16 v[136:137], v54 offset:38912\n\t" \
  "v_mfma_f32_16x16x32_bf16 v[212:215], v[88:91], v[120:123], v[212:215]\n\t" \
  "ds_read_b64_tr_b16 v[138:139], v54 offset:39424\n\t" \
  "v_mfma_f32_16x16x32_bf16 v[212:215], v[124:127], v[128:131], v[212:215]\n\t" \
  "ds_read_b128 v[148:151], v53 offset:2048\n\t" \
  "v_mfma_f32_16x16x32_bf16 v[152:155], v[96:99], v[64:67], v[152:155]\n\t" \
  "ds_read_b128 v[68:71], v53 offset:4096\n\t" \
  "v_mfma_f32_16x16x32_bf16 v[152:155], v[144:147], v[140:143], v[152:155]\n\t" \
  "ds_read_b128 v[104:107], v52 offset:4096\n\t" \
  "v_mfma_f32_16x16x32_bf16 v[160:163], v[144:147], v[92:95], v[160:163]\n\t" \
  "ds_read_b128 v[80:83], v52 offset:6144\n\t" \
  "v_mfma_f32_16x16x32_bf16 v[160:163], v[96:99], v[84:87], v[160:163]\n\t" \
  "ds_read_b128 v[116:119], v53 offset:6144\n\t" \
  "v_mfma_f32_16x16x32_bf16 v[168:171], v[96:99], v[100:103], v[168:171]\n\t" \
  "ds_read_b128 v[88:91], v53 offset:8192\n\t" \
  "v_mfma_f32_16x16x32_bf16 v[168:171], v[144:147], v[112:115], v[168:171]\n\t" \
  "ds_read_b128 v[124:127], v52 offset:8192\n\t" \
  "v_mfma_f32_16x16x32_bf16 v[176:179], v[144:147], v[128:131], v[176:179]\n\t" \
  "ds_read_b64_tr_b16 v[144:145], v55 offset:34816\n\t" \
  "v_mfma_f32_16x16x32_bf16 v[176:179], v[96:99], v[120:123], v[176:179]\n\t" \
  "ds_read_b64_tr_b16 v[146:147], v55 offset:35328\n\t" \
  "v_mfma_f32_16x16x32_bf16 v[156:159], v[76:79], v[64:67], v[156:159]\n\t" \
  "ds_read_b64_tr_b16 v[96:97], v55 offset:38912\n\t" \
  "v_mfma_f32_16x16x32_bf16 v[156:159], v[132:135], v[140:143], v[156:159]\n\t" \
  "ds_read_b64_tr_b16 v[98:99], v55 offset:39424\n\t" \
  "v_mfma_f32_16x16x32_bf16 v[164:167], v[132:135], v[92:95], v[164:167]\n\t" \
  "ds_read_b64_tr_b16 v[64:65], v54 offset:26624\n\t" \
  "v_mfma_f32_16x16x32_bf16 v[164:167], v[76:79], v[84:87], v[164:167]\n\t" \
  "ds_read_b64_tr_b16 v[66:67], v54 offset:27136\n\t" \
  "v_mfma_f32_16x16x32_bf16 v[172:175], v[76:79], v[100:103], v[172:175]\n\t" \
  "ds_read_b64_tr_b16 v[140:141], v54 offset:30720\n\t" \
  "v_mfma_f32_16x16x32_bf16 v[172:175], v[132:135], v[112:115], v[172:175]\n\t" \
  "ds_read_b64_tr_b16 v[142:143], v54 offset:31232\n\t" \
  "v_mfma_f32_16x16x32_bf16 v[180:183], v[132:135], v[128:131], v[180:183]\n\t" \
  "ds_read_b128 v[92:95], v52 offset:0\n\t" \
  "v_mfma_f32_16x16x32_bf16 v[180:183], v[76:79], v[120:123], v[180:183]\n\t" \
  "ds_read_b128 v[84:87], v53 offset:0\n\t" \
  "s_waitcnt lgkmcnt(7)\n\t" \
  "v_mfma_f32_16x16x32_bf16 v[216:219], v[72:75], v[108:111], v[216:219]\n\t" \
  "ds_read_b64_tr_b16 v[100:101], v55 offset:26624\n\t" \
  "v_mfma_f32_16x16x32_bf16 v[216:219], v[148:151], v[136:139], v[216:219]\n\t" \
  "ds_read_b64_tr_b16 v[102:103], v55 offset:27136\n\t" \
  "v_mfma_f32_16x16x32_bf16 v[224:227], v[68:71], v[136:139], v[224:227]\n\t" \
  "ds_read_b64_tr_b16 v[112:113], v55 offset:30720\n\t" \
  "v_mfma_f32_16x16x32_bf16 v[224:227], v[104:107], v[108:111], v[224:227]\n\t" \
  "ds_read_b64_tr_b16 v[114:115], v55 offset:31232\n\t" \
  "v_mfma_f32_16x16x32_bf16 v[232:235], v[80:83], v[108:111], v[232:235]\n\t" \
  "ds_read_b64_tr_b16 v[128:129], v54 offset:18432\n\t" \
  "v_mfma_f32_16x16x32_bf16 v[232:235], v[116:119], v[136:139], v[232:235]\n\t" \
  "ds_read_b64_tr_b16 v[130:131], v54 offset:18944\n\t" \
  "v_mfma_f32_16x16x32_bf16 v[240:243], v[88:91], v[136:139], v[240:243]\n\t" \
  "ds_read_b64_tr_b16 v[132:133], v54 offset:22528\n\t" \
  "v_mfma_f32_16x16x32_bf16 v[240:243], v[124:127], v[108:111], v[240:243]\n\t" \
  "ds_read_b64_tr_b16 v[134:135], v54 offset:23040\n\t" \
  "v_mfma_f32_16x16x32_bf16 v[220:223], v[72:75], v[144:147], v[220:223]\n\t" \
  "ds_read_b64_tr_b16 v[76:77], v55 offset:18432\n\t" \
  "s_waitcnt lgkmcnt(7)\n\t" \
  "v_mfma_f32_16x16x32_bf16 v[220:223], v[148:151], v[96:99], v[220:223]\n\t" \
  "ds_read_b64_tr_b16 v[78:79], v55 offset:18944\n\t" \
  "v_mfma_f32_16x16x32_bf16 v[228:231], v[68:71], v[96:99], v[228:231]\n\t" \
  "ds_read_b64_tr_b16 v[120:121], v55 offset:22528\n\t" \
  "v_mfma_f32_16x16x32_bf16 v[228:231], v[104:107], v[144:147], v[228:231]\n\t" \
  "ds_read_b64_tr_b16 v[122:123], v55 offset:23040\n\t" \
  "v_mfma_f32_16x16x32_bf16 v[236:239], v[80:83], v[144:147], v[236:239]\n\t" \
  "v_mfma_f32_16x16x32_bf16 v[236:239], v[116:119], v[96:99], v[236:239]\n\t" \
  "v_mfma_f32_16x16x32_bf16 v[244:247], v[88:91], v[96:99], v[244:247]\n\t" \
  "v_mfma_f32_16x16x32_bf16 v[244:247], v[124:127], v[144:147], v[244:247]\n\t" \
  "v_mfma_f32_16x16x32_bf16 v[184:187], v[64:67], v[72:75], v[184:187]\n\t" \
  "v_mfma_f32_16x16x32_bf16 v[184:187], v[140:143], v[148:151], v[184:187]\n\t" \
  "v_mfma_f32_16x16x32_bf16 v[192:195], v[140:143], v[68:71], v[192:195]\n\t" \
  "v_mfma_f32_16x16x32_bf16 v[192:195], v[64:67], v[104:107], v[192:195]\n\t" \
  "v_mfma_f32_16x16x32_bf16 v[200:203], v[64:67], v[80:83], v[200:203]\n\t" \
  "v_mfma_f32_16x16x32_bf16 v[200:203], v[140:143], v[116:119], v[200:203]\n\t" \
  "v_mfma_f32_16x16x32_bf16 v[208:211], v[140:143], v[88:91], v[208:211]\n\t" \
  "v_mfma_f32_16x16x32_bf16 v[208:211], v[64:67], v[124:127], v[208:211]\n\t" \
  "v_mfma_f32_16x16x32_bf16 v[248:251], v[64:67], v[92:95], v[248:251]\n\t" \
  "v_mfma_f32_16x16x32_bf16 v[248:251], v[140:143], v[84:87], v[248:251]\n\t" \
  "v_mfma_f32_16x16x32_bf16 v[252:255], v[100:103], v[92:95], v[252:255]\n\t" \
  "s_waitcnt lgkmcnt(0)\n\t" \
  "v_mfma_f32_16x16x32_bf16 v[252:255], v[112:115], v[84:87], v[252:255]\n\t" \
  "v_mfma_f32_16x16x32_bf16 v[188:191], v[112:115], v[148:151], v[188:191]\n\t" \
  "v_mfma_f32_16x16x32_bf16 v[188:191], v[100:103], v[72:75], v[188:191]\n\t" \
  "s_barrier\n\t" \
  "v_mfma_f32_16x16x32_bf16 v[196:199], v[100:103], v[104:107], v[196:199]\n\t" \
  "ds_read_b64_tr_b16 v[96:97], v58 offset:34816\n\t" \
  "v_mfma_f32_16x16x32_bf16 v[196:199], v[112:115], v[68:71], v[196:199]\n\t" \
  "ds_read_b64_tr_b16 v[98:99], v58 offset:35328\n\t" \
  "v_mfma_f32_16x16x32_bf16 v[204:207], v[112:115], v[116:119], v[204:207]\n\t" \
  "ds_read_b128 v[136:139], v56 offset:2048\n\t" \
  "v_mfma_f32_16x16x32_bf16 v[204:207], v[100:103], v[80:83], v[204:207]\n\t" \
  "ds_read_b64_tr_b16 v[64:65], v58 offset:38912\n\t" \
  "v_mfma_f32_16x16x32_bf16 v[212:215], v[100:103], v[124:127], v[212:215]\n\t" \
  "ds_read_b64_tr_b16 v[66:67], v58 offset:39424\n\t" \
  "v_mfma_f32_16x16x32_bf16 v[212:215], v[112:115], v[88:91], v[212:215]\n\t" \
  "ds_read_b128 v[108:111], v57 offset:2048\n\t" \
  "v_mfma_f32_16x16x32_bf16 v[152:155], v[128:131], v[72:75], v[152:155]\n\t" \
  "ds_read_b128 v[144:147], v57 offset:4096\n\t" \
  "v_mfma_f32_16x16x32_bf16 v[152:155], v[132:135], v[148:151], v[152:155]\n\t" \
  "ds_read_b128 v[140:143], v56 offset:4096\n\t" \
  "v_mfma_f32_16x16x32_bf16 v[160:163], v[132:135], v[68:71], v[160:163]\n\t" \
  "ds_read_b128 v[112:115], v56 offset:6144\n\t" \
  "v_mfma_f32_16x16x32_bf16 v[160:163], v[128:131], v[104:107], v[160:163]\n\t" \
  "ds_read_b64_tr_b16 v[92:93], v59 offset:34816\n\t" \
  "v_mfma_f32_16x16x32_bf16 v[168:171], v[128:131], v[80:83], v[168:171]\n\t" \
  "ds_read_b64_tr_b16 v[94:95], v59 offset:35328\n\t" \
  "v_mfma_f32_16x16x32_bf16 v[168:171], v[132:135], v[116:119], v[168:171]\n\t" \
  "ds_read_b64_tr_b16 v[84:85], v59 offset:38912\n\t" \
  "v_mfma_f32_16x16x32_bf16 v[176:179], v[132:135], v[88:91], v[176:179]\n\t" \
  "ds_read_b128 v[132:135], v57 offset:6144\n\t" \
  "v_mfma_f32_16x16x32_bf16 v[176:179], v[128:131], v[124:127], v[176:179]\n\t" \
  "ds_read_b128 v[128:131], v57 offset:8192\n\t" \
  "v_mfma_f32_16x16x32_bf16 v[156:159], v[76:79], v[72:75], v[156:159]\n\t" \
  "ds_read_b64_tr_b16 v[86:87], v59 offset:39424\n\t" \
  "v_mfma_f32_16x16x32_bf16 v[156:159], v[120:123], v[148:151], v[156:159]\n\t" \
  "ds_read_b128 v[148:151], v56 offset:8192\n\t" \
  "v_mfma_f32_16x16x32_bf16 v[164:167], v[120:123], v[68:71], v[164:167]\n\t" \
  "ds_read_b64_tr_b16 v[100:101], v58 offset:26624\n\t" \
  "v_mfma_f32_16x16x32_bf16 v[164:167], v[76:79], v[104:107], v[164:167]\n\t" \
  "ds_read_b64_tr_b16 v[102:103], v58 offset:27136\n\t" \
  "v_mfma_f32_16x16x32_bf16 v[172:175], v[76:79], v[80:83], v[172:175]\n\t" \
  "ds_read_b64_tr_b16 v[72:73], v58 offset:30720\n\t" \
  "v_mfma_f32_16x16x32_bf16 v[172:175], v[120:123], v[116:119], v[172:175]\n\t" \
  "ds_read_b64_tr_b16 v[74:75], v58 offset:31232\n\t" \
  "v_mfma_f32_16x16x32_bf16 v[180:183], v[120:123], v[88:91], v[180:183]\n\t" \
  "ds_read_b128 v[68:71], v56 offset:0\n\t" \
  "v_mfma_f32_16x16x32_bf16 v[180:183], v[76:79], v[124:127], v[180:183]\n\t" \
  "ds_read_b128 v[104:107], v57 offset:0\n\t" \
  "s_waitcnt lgkmcnt(7)\n\t" \
  "v_mfma_f32_16x16x32_bf16 v[216:219], v[136:139], v[96:99], v[216:219]\n\t" \
  "ds_read_b64_tr_b16 v[80:81], v59 offset:26624\n\t" \
  "v_mfma_f32_16x16x32_bf16 v[216:219], v[108:111], v[64:67], v[216:219]\n\t" \
  "ds_read_b64_tr_b16 v[82:83], v59 offset:27136\n\t" \
  "v_mfma_f32_16x16x32_bf16 v[224:227], v[144:147], v[64:67], v[224:227]\n\t" \
  "ds_read_b64_tr_b16 v[88:89], v59 offset:30720\n\t" \
  "v_mfma_f32_16x16x32_bf16 v[224:227], v[140:143], v[96:99], v[224:227]\n\t" \
  "ds_read_b64_tr_b16 v[90:91], v59 offset:31232\n\t" \
  "v_mfma_f32_16x16x32_bf16 v[232:235], v[112:115], v[96:99], v[232:235]\n\t" \
  "ds_read_b64_tr_b16 v[116:117], v58 offset:18432\n\t" \
  "v_mfma_f32_16x16x32_bf16 v[232:235], v[132:135], v[64:67], v[232:235]\n\t" \
  "ds_read_b64_tr_b16 v[118:119], v58 offset:18944\n\t" \
  "v_mfma_f32_16x16x32_bf16 v[240:243], v[128:131], v[64:67], v[240:243]\n\t" \
  "ds_read_b64_tr_b16 v[120:121], v58 offset:22528\n\t" \
  "s_waitcnt lgkmcnt(7)\n\t" \
  "v_mfma_f32_16x16x32_bf16 v[240:243], v[148:151], v[96:99], v[240:243]\n\t" \
  "ds_read_b64_tr_b16 v[122:123], v58 offset:23040\n\t" \
  "v_mfma_f32_16x16x32_bf16 v[220:223], v[136:139], v[92:95], v[220:223]\n\t" \
  "ds_read_b64_tr_b16 v[124:125], v59 offset:18432\n\t" \
  "v_mfma_f32_16x16x32_bf16 v[220:223], v[108:111], v[84:87], v[220:223]\n\t" \
  "ds_read_b64_tr_b16 v[126:127], v59 offset:18944\n\t" \
  "v_mfma_f32_16x16x32_bf16 v[228:231], v[144:147], v[84:87], v[228:231]\n\t" \
  "ds_read_b64_tr_b16 v[76:77], v59 offset:22528\n\t" \
  "v_mfma_f32_16x16x32_bf16 v[228:231], v[140:143], v[92:95], v[228:231]\n\t" \
  "ds_read_b64_tr_b16 v[78:79], v59 offset:23040\n\t" \
  "v_mfma_f32_16x16x32_bf16 v[236:239], v[112:115], v[92:95], v[236:239]\n\t" \
  "v_mfma_f32_16x16x32_bf16 v[236:239], v[132:135], v[84:87], v[236:239]\n\t" \
  "v_mfma_f32_16x16x32_bf16 v[244:247], v[128:131], v[84:87], v[244:247]\n\t" \
  "v_mfma_f32_16x16x32_bf16 v[244:247], v[148:151], v[92:95], v[244:247]\n\t" \
  "v_mfma_f32_16x16x32_bf16 v[184:187], v[100:103], v[136:139], v[184:187]\n\t" \
  "v_mfma_f32_16x16x32_bf16 v[184:187], v[72:75], v[108:111], v[184:187]\n\t" \
  "v_mfma_f32_16x16x32_bf16 v[192:195], v[72:75], v[144:147], v[192:195]\n\t" \
  "v_and_b32 v14, -16, v32\n\t" \
  "v_mfma_f32_16x16x32_bf16 v[192:195], v[100:103], v[140:143], v[192:195]\n\t" \
  "v_mfma_f32_16x16x32_bf16 v[200:203], v[100:103], v[112:115], v[200:203]\n\t" \
  "ds_read_b128 v[10:13], v14 offset:128\n\t" \
  "v_mfma_f32_16x16x32_bf16 v[200:203], v[72:75], v[132:135], v[200:203]\n\t" \
  "v_mfma_f32_16x16x32_bf16 v[208:211], v[72:75], v[128:131], v[208:211]\n\t" \
  "v_mfma_f32_16x16x32_bf16 v[208:211], v[100:103], v[148:151], v[208:211]\n\t" \
  "v_mfma_f32_16x16x32_bf16 v[248:251], v[100:103], v[68:71], v[248:251]\n\t" \
  "v_mfma_f32_16x16x32_bf16 v[248:251], v[72:75], v[104:107], v[248:251]\n\t" \
  "s_waitcnt lgkmcnt(1)\n\t" \
  "v_mfma_f32_16x16x32_bf16 v[252:255], v[80:83], v[68:71], v[252:255]\n\t" \
  "s_waitcnt lgkmcnt(0)\n\t" \
  "v_and_b32 v10, v10, v11\n\t" \
  "v_and_b32 v12, v12, v13\n\t" \
  "v_mfma_f32_16x16x32_bf16 v[252:255], v[88:91], v[104:107], v[252:255]\n\t" \
  "v_and_b32 v10, v10, v12\n\t" \
  "v_mfma_f32_16x16x32_bf16 v[188:191], v[88:91], v[108:111], v[188:191]\n\t" \
  "v_readfirstlane_b32 vcc_lo, v10\n\t" \
  "v_mfma_f32_16x16x32_bf16 v[188:191], v[80:83], v[136:139], v[188:191]\n\t" \
  "ds_write_b32 v32, v0 offset:144\n\t" \
  "s_cmp_eq_u32 vcc_lo, 0\n\t" \
  "s_cbranch_scc1 15f\n\t" \
  "14:\n\t" \
  "v_mfma_f32_16x16x32_bf16 v[196:199], v[80:83], v[140:143], v[196:199]\n\t" \
  "ds_read_b64_tr_b16 v[64:65], v62 offset:26624\n\t" \
  "v_mfma_f32_16x16x32_bf16 v[196:199], v[88:91], v[144:147], v[196:199]\n\t" \
  "ds_read_b64_tr_b16 v[66:67], v62 offset:27136\n\t" \
  "v_mfma_f32_16x16x32_bf16 v[204:207], v[88:91], v[132:135], v[204:207]\n\t" \
  "ds_read_b128 v[96:99], v60 offset:0\n\t" \
  "v_mfma_f32_16x16x32_bf16 v[204:207], v[80:83], v[112:115], v[204:207]\n\t" \
  "ds_read_b128 v[84:87], v60 offset:2048\n\t" \
  "v_mfma_f32_16x16x32_bf16 v[212:215], v[80:83], v[148:151], v[212:215]\n\t" \
  "ds_read_b128 v[92:95], v60 offset:4096\n\t" \
  "v_mfma_f32_16x16x32_bf16 v[212:215], v[88:91], v[128:131], v[212:215]\n\t" \
  "ds_read_b128 v[100:103], v60 offset:6144\n\t" \
  "v_mfma_f32_16x16x32_bf16 v[152:155], v[116:119], v[136:139], v[152:155]\n\t" \
  "ds_read_b128 v[72:75], v60 offset:8192\n\t" \
  "v_mfma_f32_16x16x32_bf16 v[152:155], v[120:123], v[108:111], v[152:155]\n\t" \
  "ds_read_b64_tr_b16 v[68:69], v63 offset:26624\n\t" \
  "v_mfma_f32_16x16x32_bf16 v[160:163], v[120:123], v[144:147], v[160:163]\n\t" \
  "ds_read_b64_tr_b16 v[70:71], v63 offset:27136\n\t" \
  "v_mfma_f32_16x16x32_bf16 v[160:163], v[116:119], v[140:143], v[160:163]\n\t" \
  "ds_read_b64_tr_b16 v[104:105], v62 offset:34816\n\t" \
  "v_mfma_f32_16x16x32_bf16 v[168:171], v[116:119], v[112:115], v[168:171]\n\t" \
  "ds_read_b64_tr_b16 v[106:107], v62 offset:35328\n\t" \
  "v_mfma_f32_16x16x32_bf16 v[168:171], v[120:123], v[132:135], v[168:171]\n\t" \
  "ds_read_b64_tr_b16 v[80:81], v63 offset:34816\n\t" \
  "v_mfma_f32_16x16x32_bf16 v[176:179], v[120:123], v[128:131], v[176:179]\n\t" \
  "ds_read_b64_tr_b16 v[82:83], v63 offset:35328\n\t" \
  "v_mfma_f32_16x16x32_bf16 v[176:179], v[116:119], v[148:151], v[176:179]\n\t" \
  "ds_read_b64_tr_b16 v[88:89], v62 offset:18432\n\t" \
  "v_mfma_f32_16x16x32_bf16 v[156:159], v[124:127], v[136:139], v[156:159]\n\t" \
  "ds_read_b64_tr_b16 v[90:91], v62 offset:18944\n\t" \
  "v_mfma_f32_16x16x32_bf16 v[156:159], v[76:79], v[108:111], v[156:159]\n\t" \
  "ds_read_b64_tr_b16 v[120:121], v62 offset:30720\n\t" \
  "v_mfma_f32_16x16x32_bf16 v[164:167], v[76:79], v[144:147], v[164:167]\n\t" \
  "ds_read_b64_tr_b16 v[122:123], v62 offset:31232\n\t" \
  "v_mfma_f32_16x16x32_bf16 v[164:167], v[124:127], v[140:143], v[164:167]\n\t" \
  "ds_read_b128 v[116:119], v61 offset:0\n\t" \
  "v_mfma_f32_16x16x32_bf16 v[172:175], v[124:127], v[112:115], v[172:175]\n\t" \
  "ds_read_b128 v[136:139], v61 offset:2048\n\t" \
  "v_mfma_f32_16x16x32_bf16 v[172:175], v[76:79], v[132:135], v[172:175]\n\t" \
  "ds_read_b128 v[108:111], v61 offset:4096\n\t" \
  "v_mfma_f32_16x16x32_bf16 v[180:183], v[76:79], v[128:131], v[180:183]\n\t" \
  "ds_read_b64_tr_b16 v[76:77], v63 offset:18432\n\t" \
  "v_mfma_f32_16x16x32_bf16 v[180:183], v[124:127], v[148:151], v[180:183]\n\t" \
  "ds_read_b64_tr_b16 v[78:79], v63 offset:18944\n\t" \
  "s_waitcnt lgkmcnt(7)\n\t" \
  "v_mfma_f32_16x16x32_bf16 v[248:251], v[64:67], v[96:99], v[248:251]\n\t" \
  "ds_read_b128 v[144:147], v61 offset:6144\n\t" \
  "v_mfma_f32_16x16x32_bf16 v[184:187], v[64:67], v[84:87], v[184:187]\n\t" \
  "ds_read_b128 v[140:143], v61 offset:8192\n\t" \
  "v_mfma_f32_16x16x32_bf16 v[192:195], v[64:67], v[92:95], v[192:195]\n\t" \
  "ds_read_b64_tr_b16 v[112:113], v63 offset:30720\n\t" \
  "v_mfma_f32_16x16x32_bf16 v[200:203], v[64:67], v[100:103], v[200:203]\n\t" \
  "ds_read_b64_tr_b16 v[114:115], v63 offset:31232\n\t" \
  "v_mfma_f32_16x16x32_bf16 v[208:211], v[64:67], v[72:75], v[208:211]\n\t" \
  "ds_read_b64_tr_b16 v[132:133], v62 offset:22528\n\t" \
  "v_mfma_f32_16x16x32_bf16 v[252:255], v[68:71], v[96:99], v[252:255]\n\t" \
  "ds_read_b64_tr_b16 v[134:135], v62 offset:23040\n\t" \
  "v_mfma_f32_16x16x32_bf16 v[188:191], v[68:71], v[84:87], v[188:191]\n\t" \
  "ds_read_b64_tr_b16 v[128:129], v63 offset:22528\n\t" \
  "v_mfma_f32_16x16x32_bf16 v[196:199], v[68:71], v[92:95], v[196:199]\n\t" \
  "ds_read_b64_tr_b16 v[130:131], v63 offset:23040\n\t" \
  "v_mfma_f32_16x16x32_bf16 v[204:207], v[68:71], v[100:103], v[204:207]\n\t" \
  "ds_read_b64_tr_b16 v[124:125], v62 offset:38912\n\t" \
  "v_mfma_f32_16x16x32_bf16 v[212:215], v[68:71], v[72:75], v[212:215]\n\t" \
  "ds_read_b64_tr_b16 v[126:127], v62 offset:39424\n\t" \
  "v_mfma_f32_16x16x32_bf16 v[216:219], v[84:87], v[104:107], v[216:219]\n\t" \
  "ds_read_b64_tr_b16 v[148:149], v63 offset:38912\n\t" \
  "v_mfma_f32_16x16x32_bf16 v[224:227], v[92:95], v[104:107], v[224:227]\n\t" \
  "ds_read_b64_tr_b16 v[150:151], v63 offset:39424\n\t" \
  "v_mfma_f32_16x16x32_bf16 v[232:235], v[100:103], v[104:107], v[232:235]\n\t" \
  "v_mfma_f32_16x16x32_bf16 v[240:243], v[72:75], v[104:107], v[240:243]\n\t" \
  "v_mfma_f32_16x16x32_bf16 v[220:223], v[84:87], v[80:83], v[220:223]\n\t" \
  "v_mfma_f32_16x16x32_bf16 v[228:231], v[92:95], v[80:83], v[228:231]\n\t" \
  "v_mfma_f32_16x16x32_bf16 v[236:239], v[100:103], v[80:83], v[236:239]\n\t" \
  "v_mfma_f32_16x16x32_bf16 v[244:247], v[72:75], v[80:83], v[244:247]\n\t" \
  "v_mfma_f32_16x16x32_bf16 v[152:155], v[88:91], v[84:87], v[152:155]\n\t" \
  "v_mfma_f32_16x16x32_bf16 v[160:163], v[88:91], v[92:95], v[160:163]\n\t" \
  "v_mfma_f32_16x16x32_bf16 v[168:171], v[88:91], v[100:103], v[168:171]\n\t" \
  "v_mfma_f32_16x16x32_bf16 v[176:179], v[88:91], v[72:75], v[176:179]\n\t" \
  "s_waitcnt lgkmcnt(0)\n\t" \
  "v_mfma_f32_16x16x32_bf16 v[156:159], v[76:79], v[84:87], v[156:159]\n\t" \
  "v_mfma_f32_16x16x32_bf16 v[164:167], v[76:79], v[92:95], v[164:167]\n\t" \
  "ds_write_b32 v32, v0\n\t" \
  "v_mfma_f32_16x16x32_bf16 v[172:175], v[76:79], v[100:103], v[172:175]\n\t" \
  "v_mfma_f32_16x16x32_bf16 v[180:183], v[76:79], v[72:75], v[180:183]\n\t" \
  "v_mfma_f32_16x16x32_bf16 v[248:251], v[120:123], v[116:119], v[248:251]\n\t" \
  "v_and_b32 v56, -16, v32\n\t" \
  "v_mfma_f32_16x16x32_bf16 v[184:187], v[120:123], v[136:139], v[184:187]\n\t" \
  "v_mfma_f32_16x16x32_bf16 v[192:195], v[120:123], v[108:111], v[192:195]\n\t" \
  "v_mfma_f32_16x16x32_bf16 v[200:203], v[120:123], v[144:147], v[200:203]\n\t" \
  "v_mfma_f32_16x16x32_bf16 v[208:211], v[120:123], v[140:143], v[208:211]\n\t" \
  "v_mfma_f32_16x16x32_bf16 v[252:255], v[112:115], v[116:119], v[252:255]\n\t" \
  "v_mfma_f32_16x16x32_bf16 v[188:191], v[112:115], v[136:139], v[188:191]\n\t" \
  "v_mfma_f32_16x16x32_bf16 v[196:199], v[112:115], v[108:111], v[196:199]\n\t" \
  "v_cvt_pk_bf16_f32 v10, v248, v249\n\t" \
  "v_cvt_pk_bf16_f32 v11, v250, v251\n\t" \
  "v_mfma_f32_16x16x32_bf16 v[204:207], v[112:115], v[144:147], v[204:207]\n\t" \
  "v_cvt_pk_bf16_f32 v14, v184, v185\n\t" \
  "v_cvt_pk_bf16_f32 v15, v186, v187\n\t" \
  "v_mfma_f32_16x16x32_bf16 v[212:215], v[112:115], v[140:143], v[212:215]\n\t" \
  "v_cvt_pk_bf16_f32 v12, v252, v253\n\t" \
  "v_cvt_pk_bf16_f32 v13, v254, v255\n\t" \
  "v_mfma_f32_16x16x32_bf16 v[152:155], v[132:135], v[136:139], v[152:155]\n\t" \
  "ds_write_b128 v30, v[10:13]\n\t" \
  "ds_write_b32 v32, v0 offset:48\n\t" \
  "v_mfma_f32_16x16x32_bf16 v[160:163], v[132:135], v[108:111], v[160:163]\n\t" \
  "v_cvt_pk_bf16_f32 v16, v188, v189\n\t" \
  "v_cvt_pk_bf16_f32 v17, v190, v191\n\t" \
  "v_mfma_f32_16x16x32_bf16 v[168:171], v[132:135], v[144:147], v[168:171]\n\t" \
  "v_cvt_pk_bf16_f32 v18, v192, v193\n\t" \
  "v_cvt_pk_bf16_f32 v19, v194, v195\n\t" \
  "v_mfma_f32_16x16x32_bf16 v[176:179], v[132:135], v[140:143], v[176:179]\n\t" \
  "v_cvt_pk_bf16_f32 v20, v196, v197\n\t" \
  "v_cvt_pk_bf16_f32 v21, v198, v199\n\t" \
  "v_mfma_f32_16x16x32_bf16 v[156:159], v[128:131], v[136:139], v[156:159]\n\t" \
  "v_cvt_pk_bf16_f32 v22, v200, v201\n\t" \
  "v_cvt_pk_bf16_f32 v23, v202, v203\n\t" \
  "v_mfma_f32_16x16x32_bf16 v[164:167], v[128:131], v[108:111], v[164:167]\n\t" \
  "v_cvt_pk_bf16_f32 v24, v204, v205\n\t" \
  "v_cvt_pk_bf16_f32 v25, v206, v207\n\t" \
  "v_mfma_f32_16x16x32_bf16 v[172:175], v[128:131], v[144:147], v[172:175]\n\t" \
  "v_cvt_pk_bf16_f32 v26, v208, v209\n\t" \
  "v_cvt_pk_bf16_f32 v27, v210, v211\n\t" \
  "v_mfma_f32_16x16x32_bf16 v[180:183], v[128:131], v[140:143], v[180:183]\n\t" \
  "v_cvt_pk_bf16_f32 v28, v212, v213\n\t" \
  "v_cvt_pk_bf16_f32 v29, v214, v215\n\t" \
  "v_mfma_f32_16x16x32_bf16 v[216:219], v[136:139], v[124:127], v[216:219]\n\t" \
  "v_cvt_pk_bf16_f32 v84, v152, v153\n\t" \
  "v_cvt_pk_bf16_f32 v85, v154, v155\n\t" \
  "v_mfma_f32_16x16x32_bf16 v[224:227], v[108:111], v[124:127], v[224:227]\n\t" \
  "v_cvt_pk_bf16_f32 v86, v156, v157\n\t" \
  "v_cvt_pk_bf16_f32 v87, v158, v159\n\t" \
  "v_mfma_f32_16x16x32_bf16 v[232:235], v[144:147], v[124:127], v[232:235]\n\t" \
  "v_cvt_pk_bf16_f32 v88, v160, v161\n\t" \
  "v_cvt_pk_bf16_f32 v89, v162, v163\n\t" \
  "v_mfma_f32_16x16x32_bf16 v[240:243], v[140:143], v[124:127], v[240:243]\n\t" \
  "v_cvt_pk_bf16_f32 v90, v164, v165\n\t" \
  "v_cvt_pk_bf16_f32 v91, v166, v167\n\t" \
  "v_mfma_f32_16x16x32_bf16 v[220:223], v[136:139], v[148:151], v[220:223]\n\t" \
  "v_cvt_pk_bf16_f32 v92, v168, v169\n\t" \
  "v_cvt_pk_bf16_f32 v93, v170, v171\n\t" \
  "v_mfma_f32_16x16x32_bf16 v[228:231], v[108:111], v[148:151], v[228:231]\n\t" \
  "v_cvt_pk_bf16_f32 v94, v172, v173\n\t" \
  "v_cvt_pk_bf16_f32 v95, v174, v175\n\t" \
  "v_mfma_f32_16x16x32_bf16 v[236:239], v[144:147], v[148:151], v[236:239]\n\t" \
  "v_cvt_pk_bf16_f32 v96, v176, v177\n\t" \
  "v_cvt_pk_bf16_f32 v97, v178, v179\n\t" \
  "v_mfma_f32_16x16x32_bf16 v[244:247], v[140:143], v[148:151], v[244:247]\n\t" \
  "v_cvt_pk_bf16_f32 v98, v180, v181\n\t" \
  "v_cvt_pk_bf16_f32 v99, v182, v183\n\t" \
  "v_mfma_f32_16x16x32_bf16 v[200:203], v[22:25], v[92:95], v[34:37]\n\t" \
  "v_cvt_pk_bf16_f32 v66, v232, v233\n\t" \
  "v_cvt_pk_bf16_f32 v67, v234, v235\n\t" \
  "v_mfma_f32_16x16x32_bf16 v[208:211], v[26:29], v[96:99], v[34:37]\n\t" \
  "v_cvt_pk_bf16_f32 v76, v236, v237\n\t" \
  "v_cvt_pk_bf16_f32 v77, v238, v239\n\t" \
  "v_mfma_f32_16x16x32_bf16 v[212:215], v[22:25], v[96:99], v[38:41]\n\t" \
  "v_cvt_pk_bf16_f32 v64, v240, v241\n\t" \
  "v_cvt_pk_bf16_f32 v65, v242, v243\n\t" \
  "v_mfma_f32_16x16x32_bf16 v[204:207], v[18:21], v[92:95], v[38:41]\n\t" \
  "v_cvt_pk_bf16_f32 v74, v244, v245\n\t" \
  "v_cvt_pk_bf16_f32 v75, v246, v247\n\t" \
  "v_mfma_f32_16x16x32_bf16 v[192:195], v[18:21], v[88:91], v[34:37]\n\t" \
  "v_cvt_pk_bf16_f32 v68, v224, v225\n\t" \
  "v_cvt_pk_bf16_f32 v69, v226, v227\n\t" \
  "v_mfma_f32_16x16x32_bf16 v[196:199], v[14:17], v[88:91], v[38:41]\n\t" \
  "v_max_f32 v208, v208, v212\n\t" \
  "v_max_f32 v209, v209, v213\n\t" \
  "v_mfma_f32_16x16x32_bf16 v[184:187], v[14:17], v[84:87], v[34:37]\n\t" \
  "v_max_f32 v210, v210, v214\n\t" \
  "v_max_f32 v200, v200, v204\n\t" \
  "v_mfma_f32_16x16x32_bf16 v[188:191], v[10:13], v[84:87], v[42:45]\n\t" \
  "v_max_f32 v201, v201, v205\n\t" \
  "v_max_f32 v202, v202, v206\n\t" \
  "v_max_f32 v192, v192, v196\n\t" \
  "v_max_f32 v193, v193, v197\n\t" \
  "v_max_f32 v194, v194, v198\n\t" \
  "v_max_f32 v211, v211, v215\n\t" \
  "ds_read_b32 v52, v33 offset:48\n\t" \
  "ds_read_b64 v[72:73], v31\n\t" \
  "ds_read_b64 v[82:83], v31 offset:8\n\t" \
  "v_max3_f32 v106, v208, v209, v210\n\t" \
  "v_max_f32 v184, v184, v188\n\t" \
  "v_max_f32 v185, v185, v189\n\t" \
  "v_max_f32 v186, v186, v190\n\t" \
  "v_max_f32 v203, v203, v207\n\t" \
  "v_max3_f32 v104, v200, v201, v202\n\t" \
  "v_max_f32 v195, v195, v199\n\t" \
  "v_max3_f32 v102, v192, v193, v194\n\t" \
  "v_max_f32 v187, v187, v191\n\t" \
  "v_max3_f32 v100, v184, v185, v186\n\t" \
  "v_max_f32 v106, v106, v211\n\t" \
  "v_max_f32 v104, v104, v203\n\t" \
  "v_max_f32 v102, v102, v195\n\t" \
  "v_max_f32 v100, v100, v187\n\t" \
  "v_permlane16_swap_b32 v104, v106\n\t" \
  "v_cvt_pk_bf16_f32 v78, v228, v229\n\t" \
  "v_permlane16_swap_b32 v100, v102\n\t" \
  "v_max_f32 v104, v104, v106\n\t" \
  "v_max_f32 v100, v100, v102\n\t" \
  "v_cvt_pk_bf16_f32 v79, v230, v231\n\t" \
  "v_cvt_pk_bf16_f32 v70, v216, v217\n\t" \
  "v_permlane32_swap_b32 v100, v104\n\t" \
  "v_cvt_pk_bf16_f32 v71, v218, v219\n\t" \
  "v_max_f32 v100, v100, v104\n\t" \
  "v_mul_f32 v100, 0xbe8293ee, v100\n\t" \
  "v_mov_b32 v104, v100\n\t" \
  "v_cvt_pk_bf16_f32 v80, v220, v221\n\t" \
  "v_cvt_pk_bf16_f32 v81, v222, v223\n\t" \
  "v_permlane32_swap_b32 v100, v104\n\t" \
  "s_waitcnt lgkmcnt(0)\n\t" \
  "v_readfirstlane_b32 vcc_lo, v52\n\t" \
  "s_nop 1\n\t" \
  "s_cmp_eq_u32 vcc_lo, 0\n\t" \
  "s_cbranch_scc1 5f\n\t" \
  "4:\n\t" \
  "v_mov_b32 v106, v104\n\t" \
  "v_mov_b32 v102, v100\n\t" \
  "s_nop 0\n\t" \
  "v_permlane16_swap_b32 v104, v106\n\t" \
  "v_permlane16_swap_b32 v100, v102\n\t" \
  "v_fma_f32 v208, v208, %16, v106\n\t" \
  "v_fma_f32 v209, v209, %16, v106\n\t" \
  "v_fma_f32 v210, v210, %16, v106\n\t" \
  "v_fma_f32 v211, v211, %16, v106\n\t" \
  "v_exp_f32 v208, v208\n\t" \
  "v_exp_f32 v209, v209\n\t" \
  "v_exp_f32 v210, v210\n\t" \
  "v_exp_f32 v211, v211\n\t" \
  "v_cvt_pk_bf16_f32 v212, v208, v209\n\t" \
  "v_cvt_pk_bf16_f32 v213, v210, v211\n\t" \
  "v_bfi_b32 v214, v46, 0, v212\n\t" \
  "v_bfi_b32 v215, v47, 0, v213\n\t" \
  "v_and_b32 v212, v46, v212\n\t" \
  "v_and_b32 v213, v47, v213\n\t" \
  "v_fma_f32 v200, v200, %16, v104\n\t" \
  "v_fma_f32 v201, v201, %16, v104\n\t" \
  "v_mfma_f32_16x16x32_bf16 v[208:211], v[48:51], v[212:215], 0\n\t" \
  "v_fma_f32 v202, v202, %16, v104\n\t" \
  "v_fma_f32 v203, v203, %16, v104\n\t" \
  "v_mfma_f32_16x16x32_bf16 v[176:179], v[64:67], v[212:215], 0\n\t" \
  "v_exp_f32 v200, v200\n\t" \
  "v_mfma_f32_16x16x32_bf16 v[180:183], v[74:77], v[212:215], 0\n\t" \
  "v_exp_f32 v201, v201\n\t" \
  "v_exp_f32 v202, v202\n\t" \
  "v_exp_f32 v203, v203\n\t" \
  "v_cvt_pk_bf16_f32 v204, v200, v201\n\t" \
  "v_cvt_pk_bf16_f32 v205, v202, v203\n\t" \
  "v_bfi_b32 v206, v46, 0, v204\n\t" \
  "v_bfi_b32 v207, v47, 0, v205\n\t" \
  "v_rcp_f32 v106, v208\n\t" \
  "v_and_b32 v204, v46, v204\n\t" \
  "v_and_b32 v205, v47, v205\n\t" \
  "v_pk_mul_f32 v[176:177], v[176:177], v[106:107] op_sel_hi:[1,0]\n\t" \
  "v_pk_mul_f32 v[178:179], v[178:179], v[106:107] op_sel_hi:[1,0]\n\t" \
  "v_mfma_f32_16x16x32_bf16 v[200:203], v[48:51], v[204:207], 0\n\t" \
  "v_pk_mul_f32 v[180:181], v[180:181], v[106:107] op_sel_hi:[1,0]\n\t" \
  "v_pk_mul_f32 v[182:183], v[182:183], v[106:107] op_sel_hi:[1,0]\n\t" \
  "v_mfma_f32_16x16x32_bf16 v[168:171], v[66:69], v[204:207], 0\n\t" \
  "v_cvt_pk_bf16_f32 v240, v176, v177\n\t" \
  "v_cvt_pk_bf16_f32 v241, v178, v179\n\t" \
  "v_mfma_f32_16x16x32_bf16 v[172:175], v[76:79], v[204:207], 0\n\t" \
  "v_cvt_pk_bf16_f32 v242, v180, v181\n\t" \
  "v_cvt_pk_bf16_f32 v243, v182, v183\n\t" \
  "global_store_dwordx4 v1, v[240:243], %14 offset:0 sc1\n\t" \
  "v_fma_f32 v192, v192, %16, v102\n\t" \
  "v_fma_f32 v193, v193, %16, v102\n\t" \
  "v_fma_f32 v194, v194, %16, v102\n\t" \
  "v_fma_f32 v195, v195, %16, v102\n\t" \
  "v_exp_f32 v192, v192\n\t" \
  "v_exp_f32 v193, v193\n\t" \
  "v_exp_f32 v194, v194\n\t" \
  "v_exp_f32 v195, v195\n\t" \
  "v_cvt_pk_bf16_f32 v196, v192, v193\n\t" \
  "v_cvt_pk_bf16_f32 v197, v194, v195\n\t" \
  "v_bfi_b32 v198, v46, 0, v196\n\t" \
  "v_bfi_b32 v199, v47, 0, v197\n\t" \
  "v_rcp_f32 v104, v200\n\t" \
  "v_and_b32 v196, v46, v196\n\t" \
  "v_and_b32 v197, v47, v197\n\t" \
  "v_pk_mul_f32 v[168:169], v[168:169], v[104:105] op_sel_hi:[1,0]\n\t" \
  "v_pk_mul_f32 v[170:171], v[170:171], v[104:105] op_sel_hi:[1,0]\n\t" \
  "v_mfma_f32_16x16x32_bf16 v[192:195], v[48:51], v[196:199], 0\n\t" \
  "v_pk_mul_f32 v[172:173], v[172:173], v[104:105] op_sel_hi:[1,0]\n\t" \
  "v_pk_mul_f32 v[174:175], v[174:175], v[104:105] op_sel_hi:[1,0]\n\t" \
  "v_mfma_f32_16x16x32_bf16 v[160:163], v[68:71], v[196:199], 0\n\t" \
  "v_cvt_pk_bf16_f32 v232, v168, v169\n\t" \
  "v_cvt_pk_bf16_f32 v233, v170, v171\n\t" \
  "v_mfma_f32_16x16x32_bf16 v[164:167], v[78:81], v[196:199], 0\n\t" \
  "v_cvt_pk_bf16_f32 v234, v172, v173\n\t" \
  "v_cvt_pk_bf16_f32 v235, v174, v175\n\t" \
  "global_store_dwordx4 v1, v[232:235], %14 offset:-4096 sc1\n\t" \
  "v_fma_f32 v184, v184, %16, v100\n\t" \
  "v_fma_f32 v185, v185, %16, v100\n\t" \
  "v_fma_f32 v186, v186, %16, v100\n\t" \
  "v_fma_f32 v187, v187, %16, v100\n\t" \
  "v_exp_f32 v184, v184\n\t" \
  "v_exp_f32 v185, v185\n\t" \
  "v_exp_f32 v186, v186\n\t" \
  "v_exp_f32 v187, v187\n\t" \
  "v_cvt_pk_bf16_f32 v188, v184, v185\n\t" \
  "v_cvt_pk_bf16_f32 v189, v186, v187\n\t" \
  "v_bfi_b32 v190, v46, 0, v188\n\t" \
  "v_bfi_b32 v191, v47, 0, v189\n\t" \
  "v_rcp_f32 v102, v192\n\t" \
  "v_and_b32 v188, v46, v188\n\t" \
  "v_and_b32 v189, v47, v189\n\t" \
  "v_pk_mul_f32 v[160:161], v[160:161], v[102:103] op_sel_hi:[1,0]\n\t" \
  "v_pk_mul_f32 v[162:163], v[162:163], v[102:103] op_sel_hi:[1,0]\n\t" \
  "v_mfma_f32_16x16x32_bf16 v[184:187], v[48:51], v[188:191], 0\n\t" \
  "v_pk_mul_f32 v[164:165], v[164:165], v[102:103] op_sel_hi:[1,0]\n\t" \
  "v_pk_mul_f32 v[166:167], v[166:167], v[102:103] op_sel_hi:[1,0]\n\t" \
  "v_mfma_f32_16x16x32_bf16 v[152:155], v[70:73], v[188:191], 0\n\t" \
  "v_cvt_pk_bf16_f32 v224, v160, v161\n\t" \
  "v_cvt_pk_bf16_f32 v225, v162, v163\n\t" \
  "v_mfma_f32_16x16x32_bf16 v[156:159], v[80:83], v[188:191], 0\n\t" \
  "v_cvt_pk_bf16_f32 v226, v164, v165\n\t" \
  "v_cvt_pk_bf16_f32 v227, v166, v167\n\t" \
  "global_store_dwordx4 v4, v[224:227], %14 offset:0 sc1\n\t" \
  "v_rcp_f32 v100, v184\n\t" \
  "ds_read_b128 v[60:63], v56 offset:64\n\t" \
  "ds_read_b64_tr_b16 v[108:109], v5 offset:0\n\t" \
  "ds_read_b64_tr_b16 v[110:111], v5 offset:16\n\t" \
  "ds_read_b128 v[112:115], v6 offset:16384\n\t" \
  "ds_read_b128 v[116:119], v6 offset:15360\n\t" \
  "506:\n\t" \
  "s_cbranch_execz 510f\n\t" \
  "ds_read_b64_tr_b16 v[120:121], v5 offset:512\n\t" \
  "ds_read_b64_tr_b16 v[122:123], v5 offset:528\n\t" \
  "ds_read_b128 v[124:127], v6 offset:14336\n\t" \
  "ds_read_b64_tr_b16 v[128:129], v5 offset:1024\n\t" \
  "ds_read_b64_tr_b16 v[130:131], v5 offset:1040\n\t" \
  "ds_read_b128 v[132:135], v6 offset:13312\n\t" \
  "ds_read_b64_tr_b16 v[136:137], v5 offset:1536\n\t" \
  "ds_read_b64_tr_b16 v[138:139], v5 offset:1552\n\t" \
  "ds_read_b128 v[140:143], v6 offset:12288\n\t" \
  "ds_read_b64_tr_b16 v[144:145], v5 offset:2048\n\t" \
  "v_pk_mul_f32 v[152:153], v[152:153], v[100:101] op_sel_hi:[1,0]\n\t" \
  "v_pk_mul_f32 v[154:155], v[154:155], v[100:101] op_sel_hi:[1,0]\n\t" \
  "v_cvt_pk_bf16_f32 v216, v152, v153\n\t" \
  "v_pk_mul_f32 v[156:157], v[156:157], v[100:101] op_sel_hi:[1,0]\n\t" \
  "v_pk_mul_f32 v[158:159], v[158:159], v[100:101] op_sel_hi:[1,0]\n\t" \
  "v_cvt_pk_bf16_f32 v217, v154, v155\n\t" \
  "v_cvt_pk_bf16_f32 v218, v156, v157\n\t" \
  "v_cvt_pk_bf16_f32 v219, v158, v159\n\t" \
  "global_store_dwordx4 v4, v[216:219], %14 offset:-4096 sc1\n\t" \
  "ds_write_b32 v32, v0 offset:112\n\t" \
  "s_branch 9f\n\t" \
  "5:\n\t" \
  "ds_read_b32 v52, v33 offset:48\n\t" \
  "ds_read_b64 v[72:73], v31\n\t" \
  "ds_read_b64 v[82:83], v31 offset:8\n\t" \
  "s_waitcnt lgkmcnt(0)\n\t" \
  "v_readfirstlane_b32 vcc_lo, v52\n\t" \
  "s_nop 1\n\t" \
  "s_cmp_eq_u32 vcc_lo, 0\n\t" \
  "s_cbranch_scc1 5b\n\t" \
  "s_branch 4b\n\t" \
  "15:\n\t" \
  "ds_read_b128 v[10:13], v14 offset:128\n\t" \
  "s_waitcnt lgkmcnt(0)\n\t" \
  "v_and_b32 v10, v10, v11\n\t" \
  "v_and_b32 v12, v12, v13\n\t" \
  "v_and_b32 v10, v10, v12\n\t" \
  "s_nop 1\n\t" \
  "v_readfirstlane_b32 vcc_lo, v10\n\t" \
  "s_nop 1\n\t" \
  "s_cmp_eq_u32 vcc_lo, 0\n\t" \
  "s_cbranch_scc1 15b\n\t" \
  "s_branch 14b\n\t" \
  "9:\n\t" \
  "507:\n\t" \
  "s_cbranch_execz 26b\n\t" \
  "s_waitcnt lgkmcnt(14)\n\t" \
  "v_and_b32 v60, v60, v61\n\t" \
  "v_and_b32 v62, v62, v63\n\t" \
  "v_mov_b32 v12, %13\n\t" \
  "v_and_b32 v60, v60, v62\n\t" \
  "s_nop 0\n\t" \
  "v_readfirstlane_b32 vcc_lo, v60\n\t" \
  "s_nop 1\n\t" \
  "s_cmp_eq_u32 vcc_lo, 0\n\t" \
  "s_cbranch_scc1 7f\n\t" \
  "6:\n\t" \
  "s_waitcnt lgkmcnt(11)\n\t" \
  "v_mfma_f32_16x16x32_bf16 v[192:195], v[108:111], v[112:115], 0\n\t" \
  "ds_read_b64_tr_b16 v[146:147], v5 offset:2064\n\t" \
  "ds_read_b128 v[148:151], v6 offset:11264\n\t" \
  "s_waitcnt lgkmcnt(12)\n\t" \
  "v_mfma_f32_16x16x32_bf16 v[196:199], v[108:111], v[116:119], 0\n\t" \
  "ds_read_b64_tr_b16 v[64:65], v5 offset:2560\n\t" \
  "ds_read_b64_tr_b16 v[66:67], v5 offset:2576\n\t" \
  "s_waitcnt lgkmcnt(12)\n\t" \
  "v_mfma_f32_16x16x32_bf16 v[196:199], v[120:123], v[112:115], v[196:199]\n\t" \
  "ds_read_b128 v[68:71], v6 offset:10240\n\t" \
  "ds_read_b64_tr_b16 v[72:73], v5 offset:3072\n\t" \
  "s_waitcnt lgkmcnt(13)\n\t" \
  "v_mfma_f32_16x16x32_bf16 v[200:203], v[108:111], v[124:127], 0\n\t" \
  "ds_read_b64_tr_b16 v[74:75], v5 offset:3088\n\t" \
  "ds_read_b128 v[76:79], v6 offset:9216\n\t" \
  "v_mfma_f32_16x16x32_bf16 v[200:203], v[120:123], v[116:119], v[200:203]\n\t" \
  "ds_read_b64_tr_b16 v[80:81], v5 offset:3584\n\t" \
  "ds_read_b64_tr_b16 v[82:83], v5 offset:3600\n\t" \
  "s_waitcnt lgkmcnt(15)\n\t" \
  "v_mfma_f32_16x16x32_bf16 v[200:203], v[128:131], v[112:115], v[200:203]\n\t" \
  "ds_read_b128 v[84:87], v6 offset:0\n\t" \
  "ds_read_b128 v[88:91], v6 offset:1024\n\t" \
  "s_waitcnt lgkmcnt(15)\n\t" \
  "v_mfma_f32_16x16x32_bf16 v[204:207], v[108:111], v[132:135], 0\n\t" \
  "ds_read_b128 v[92:95], v6 offset:2048\n\t" \
  "ds_read_b128 v[96:99], v6 offset:3072\n\t" \
  "v_mfma_f32_16x16x32_bf16 v[204:207], v[120:123], v[124:127], v[204:207]\n\t" \
  "ds_read_b128 v[100:103], v6 offset:4096\n\t" \
  "ds_read_b128 v[104:107], v6 offset:5120\n\t" \
  "v_mfma_f32_16x16x32_bf16 v[204:207], v[128:131], v[116:119], v[204:207]\n\t" \
  "ds_read_b128 v[152:155], v6 offset:6144\n\t" \
  "ds_read_b128 v[156:159], v6 offset:7168\n\t" \
  "s_waitcnt lgkmcnt(14)\n\t" \
  "v_mfma_f32_16x16x32_bf16 v[204:207], v[136:139], v[112:115], v[204:207]\n\t" \
  "v_lshl_add_u32 v12, v12, 12, v4\n\t" \
  "s_lshl_b32 s88, %13, 5\n\t" \
  "v_mfma_f32_16x16x32_bf16 v[208:211], v[108:111], v[140:143], 0\n\t" \
  "v_mfma_f32_16x16x32_bf16 v[208:211], v[120:123], v[132:135], v[208:211]\n\t" \
  "v_mfma_f32_16x16x32_bf16 v[208:211], v[128:131], v[124:127], v[208:211]\n\t" \
  "s_and_b32 s88, s88, 64\n\t" \
  "v_xor_b32 v13, s88, v54\n\t" \
  "v_mfma_f32_16x16x32_bf16 v[208:211], v[136:139], v[116:119], v[208:211]\n\t" \
  "v_mfma_f32_16x16x32_bf16 v[208:211], v[144:147], v[112:115], v[208:211]\n\t" \
  "v_mfma_f32_16x16x32_bf16 v[212:215], v[108:111], v[148:151], 0\n\t" \
  "ds_read_b32 v55, v56 offset:104\n\t" \
  "v_mfma_f32_16x16x32_bf16 v[212:215], v[120:123], v[140:143], v[212:215]\n\t" \
  "v_mfma_f32_16x16x32_bf16 v[212:215], v[128:131], v[132:135], v[212:215]\n\t" \
  "v_bfe_u32 v11, v12, 4, 4\n\t" \
  "v_mfma_f32_16x16x32_bf16 v[212:215], v[136:139], v[124:127], v[212:215]\n\t" \
  "v_lshlrev_b32 v11, 7, v11\n\t" \
  "v_mfma_f32_16x16x32_bf16 v[212:215], v[144:147], v[116:119], v[212:215]\n\t" \
  "v_mov_b32 v10, 1\n\t" \
  "v_mfma_f32_16x16x32_bf16 v[212:215], v[64:67], v[112:115], v[212:215]\n\t" \
  "s_waitcnt lgkmcnt(1)\n\t" \
  "v_mfma_f32_16x16x32_bf16 v[216:219], v[108:111], v[68:71], 0\n\t" \
  "v_mfma_f32_16x16x32_bf16 v[216:219], v[120:123], v[148:151], v[216:219]\n\t" \
  "v_and_b32 v1, 0x180, v4\n\t" \
  "v_mfma_f32_16x16x32_bf16 v[216:219], v[128:131], v[140:143], v[216:219]\n\t" \
  "v_and_b32 v2, 0x70, v4\n\t" \
  "v_mfma_f32_16x16x32_bf16 v[216:219], v[136:139], v[132:135], v[216:219]\n\t" \
  "v_lshlrev_b32 v2, 3, v2\n\t" \
  "v_mfma_f32_16x16x32_bf16 v[216:219], v[144:147], v[124:127], v[216:219]\n\t" \
  "v_lshl_add_u32 v1, v1, 5, v2\n\t" \
  "v_mfma_f32_16x16x32_bf16 v[216:219], v[64:67], v[116:119], v[216:219]\n\t" \
  "v_and_b32 v2, 0x2000, v1\n\t" \
  "v_mfma_f32_16x16x32_bf16 v[216:219], v[72:75], v[112:115], v[216:219]\n\t" \
  "v_add_u32 v1, v1, v2\n\t" \
  "v_mfma_f32_16x16x32_bf16 v[220:223], v[108:111], v[76:79], 0\n\t" \
  "v_mfma_f32_16x16x32_bf16 v[220:223], v[120:123], v[68:71], v[220:223]\n\t" \
  "v_mfma_f32_16x16x32_bf16 v[220:223], v[128:131], v[148:151], v[220:223]\n\t" \
  "v_mfma_f32_16x16x32_bf16 v[220:223], v[136:139], v[140:143], v[220:223]\n\t" \
  "v_mfma_f32_16x16x32_bf16 v[220:223], v[144:147], v[132:135], v[220:223]\n\t" \
  "ds_read_b64_tr_b16 v[160:161], v5 offset:128\n\t" \
  "v_mfma_f32_16x16x32_bf16 v[220:223], v[64:67], v[124:127], v[220:223]\n\t" \
  "ds_read_b64_tr_b16 v[162:163], v5 offset:144\n\t" \
  "v_mfma_f32_16x16x32_bf16 v[220:223], v[72:75], v[116:119], v[220:223]\n\t" \
  "ds_read_b128 v[164:167], v7 offset:16384\n\t" \
  "v_mfma_f32_16x16x32_bf16 v[220:223], v[80:83], v[112:115], v[220:223]\n\t" \
  "ds_read_b128 v[168:171], v7 offset:15360\n\t" \
  "v_mfma_f32_16x16x32_bf16 v[252:255], v[108:111], v[84:87], 0\n\t" \
  "ds_read_b64_tr_b16 v[172:173], v5 offset:640\n\t" \
  "v_mfma_f32_16x16x32_bf16 v[252:255], v[120:123], v[88:91], v[252:255]\n\t" \
  "ds_read_b64_tr_b16 v[174:175], v5 offset:656\n\t" \
  "v_mfma_f32_16x16x32_bf16 v[252:255], v[128:131], v[92:95], v[252:255]\n\t" \
  "ds_read_b128 v[176:179], v7 offset:14336\n\t" \
  "v_mfma_f32_16x16x32_bf16 v[252:255], v[136:139], v[96:99], v[252:255]\n\t" \
  "ds_read_b64_tr_b16 v[180:181], v5 offset:1152\n\t" \
  "v_mfma_f32_16x16x32_bf16 v[252:255], v[144:147], v[100:103], v[252:255]\n\t" \
  "ds_read_b64_tr_b16 v[182:183], v5 offset:1168\n\t" \
  "v_mfma_f32_16x16x32_bf16 v[252:255], v[64:67], v[104:107], v[252:255]\n\t" \
  "ds_read_b128 v[184:187], v7 offset:13312\n\t" \
  "v_mfma_f32_16x16x32_bf16 v[252:255], v[72:75], v[152:155], v[252:255]\n\t" \
  "ds_read_b64_tr_b16 v[188:189], v5 offset:1664\n\t" \
  "v_mfma_f32_16x16x32_bf16 v[252:255], v[80:83], v[156:159], v[252:255]\n\t" \
  "ds_read_b64_tr_b16 v[190:191], v5 offset:1680\n\t" \
  "v_mfma_f32_16x16x32_bf16 v[248:251], v[108:111], v[88:91], 0\n\t" \
  "ds_read_b128 v[76:79], v7 offset:12288\n\t" \
  "v_mfma_f32_16x16x32_bf16 v[248:251], v[120:123], v[92:95], v[248:251]\n\t" \
  "v_mfma_f32_16x16x32_bf16 v[248:251], v[128:131], v[96:99], v[248:251]\n\t" \
  "ds_read_b64_tr_b16 v[68:69], v5 offset:2176\n\t" \
  "v_mfma_f32_16x16x32_bf16 v[248:251], v[136:139], v[100:103], v[248:251]\n\t" \
  "ds_read_b64_tr_b16 v[70:71], v5 offset:2192\n\t" \
  "v_mfma_f32_16x16x32_bf16 v[248:251], v[144:147], v[104:107], v[248:251]\n\t" \
  "ds_read_b128 v[148:151], v7 offset:11264\n\t" \
  "v_mfma_f32_16x16x32_bf16 v[248:251], v[64:67], v[152:155], v[248:251]\n\t" \
  "v_mfma_f32_16x16x32_bf16 v[248:251], v[72:75], v[156:159], v[248:251]\n\t" \
  "v_mfma_f32_16x16x32_bf16 v[244:247], v[108:111], v[92:95], 0\n\t" \
  "v_mfma_f32_16x16x32_bf16 v[244:247], v[120:123], v[96:99], v[244:247]\n\t" \
  "ds_read_b64_tr_b16 v[140:141], v5 offset:2688\n\t" \
  "v_mfma_f32_16x16x32_bf16 v[244:247], v[128:131], v[100:103], v[244:247]\n\t" \
  "ds_read_b64_tr_b16 v[142:143], v5 offset:2704\n\t" \
  "v_mfma_f32_16x16x32_bf16 v[244:247], v[136:139], v[104:107], v[244:247]\n\t" \
  "ds_read_b128 v[132:135], v7 offset:10240\n\t" \
  "v_mfma_f32_16x16x32_bf16 v[244:247], v[144:147], v[152:155], v[244:247]\n\t" \
  "v_mfma_f32_16x16x32_bf16 v[244:247], v[64:67], v[156:159], v[244:247]\n\t" \
  "v_mfma_f32_16x16x32_bf16 v[240:243], v[108:111], v[96:99], 0\n\t" \
  "v_mfma_f32_16x16x32_bf16 v[240:243], v[120:123], v[100:103], v[240:243]\n\t" \
  "v_mfma_f32_16x16x32_bf16 v[240:243], v[128:131], v[104:107], v[240:243]\n\t" \
  "ds_read_b64_tr_b16 v[124:125], v5 offset:3200\n\t" \
  "v_mfma_f32_16x16x32_bf16 v[240:243], v[136:139], v[152:155], v[240:243]\n\t" \
  "s_waitcnt vmcnt(0)\n\t" \
  "s_mov_b64 exec, 1\n\t" \
  "ds_add_rtn_u32 v10, v56, v10 offset:16\n\t" \
  "s_mov_b64 exec, -1\n\t" \
  "v_mfma_f32_16x16x32_bf16 v[240:243], v[144:147], v[156:159], v[240:243]\n\t" \
  "ds_read_b64_tr_b16 v[126:127], v5 offset:3216\n\t" \
  "v_mfma_f32_16x16x32_bf16 v[236:239], v[108:111], v[100:103], 0\n\t" \
  "ds_read_b128 v[116:119], v7 offset:9216\n\t" \
  "v_mfma_f32_16x16x32_bf16 v[236:239], v[120:123], v[104:107], v[236:239]\n\t" \
  "v_mfma_f32_16x16x32_bf16 v[236:239], v[128:131], v[152:155], v[236:239]\n\t" \
  "v_mfma_f32_16x16x32_bf16 v[236:239], v[136:139], v[156:159], v[236:239]\n\t" \
  "v_mfma_f32_16x16x32_bf16 v[232:235], v[108:111], v[104:107], 0\n\t" \
  "v_mfma_f32_16x16x32_bf16 v[232:235], v[120:123], v[152:155], v[232:235]\n\t" \
  "s_waitcnt lgkmcnt(2)\n\t" \
  "v_readfirstlane_b32 vcc_lo, v10\n\t" \
  "v_mfma_f32_16x16x32_bf16 v[232:235], v[128:131], v[156:159], v[232:235]\n\t" \
  "s_cmp_eq_u32 vcc_lo, 3\n\t" \
  "s_cbranch_scc1 23f\n\t" \
  "22:\n\t" \
  "v_mfma_f32_16x16x32_bf16 v[228:231], v[108:111], v[152:155], 0\n\t" \
  "ds_read_b64_tr_b16 v[64:65], v5 offset:3712\n\t" \
  "v_mfma_f32_16x16x32_bf16 v[228:231], v[120:123], v[156:159], v[228:231]\n\t" \
  "ds_read_b64_tr_b16 v[66:67], v5 offset:3728\n\t" \
  "v_mfma_f32_16x16x32_bf16 v[224:227], v[108:111], v[156:159], 0\n\t" \
  "ds_read_b128 v[72:75], v7 offset:0\n\t" \
  "v_mfma_f32_16x16x32_bf16 v[192:195], v[160:163], v[164:167], v[192:195]\n\t" \
  "ds_read_b128 v[80:83], v7 offset:1024\n\t" \
  "v_mfma_f32_16x16x32_bf16 v[196:199], v[160:163], v[168:171], v[196:199]\n\t" \
  "ds_read_b128 v[84:87], v7 offset:2048\n\t" \
  "v_mfma_f32_16x16x32_bf16 v[196:199], v[172:175], v[164:167], v[196:199]\n\t" \
  "ds_read_b128 v[88:91], v7 offset:3072\n\t" \
  "v_mfma_f32_16x16x32_bf16 v[200:203], v[160:163], v[176:179], v[200:203]\n\t" \
  "ds_read_b128 v[92:95], v7 offset:4096\n\t" \
  "v_mfma_f32_16x16x32_bf16 v[200:203], v[172:175], v[168:171], v[200:203]\n\t" \
  "v_and_b32 v53, 0x2000, v12\n\t" \
  "ds_read_b128 v[108:111], v7 offset:5120\n\t" \
  "v_mfma_f32_16x16x32_bf16 v[200:203], v[180:183], v[164:167], v[200:203]\n\t" \
  "ds_read_b128 v[112:115], v7 offset:6144\n\t" \
  "v_mfma_f32_16x16x32_bf16 v[204:207], v[160:163], v[184:187], v[204:207]\n\t" \
  "v_add_u32 v53, v53, v12\n\t" \
  "ds_read_b128 v[120:123], v7 offset:7168\n\t" \
  "v_mfma_f32_16x16x32_bf16 v[204:207], v[172:175], v[176:179], v[204:207]\n\t" \
  "v_mfma_f32_16x16x32_bf16 v[204:207], v[180:183], v[168:171], v[204:207]\n\t" \
  "v_lshrrev_b32 v14, 1, v4\n\t" \
  "v_mfma_f32_16x16x32_bf16 v[204:207], v[188:191], v[164:167], v[204:207]\n\t" \
  "v_mfma_f32_16x16x32_bf16 v[208:211], v[160:163], v[76:79], v[208:211]\n\t" \
  "v_lshrrev_b32 v15, 24, v3\n\t" \
  "v_mfma_f32_16x16x32_bf16 v[208:211], v[172:175], v[184:187], v[208:211]\n\t" \
  "v_mfma_f32_16x16x32_bf16 v[208:211], v[180:183], v[176:179], v[208:211]\n\t" \
  "v_add_u32 v14, %20, v14\n\t" \
  "v_mfma_f32_16x16x32_bf16 v[208:211], v[188:191], v[168:171], v[208:211]\n\t" \
  "v_mfma_f32_16x16x32_bf16 v[208:211], v[68:71], v[164:167], v[208:211]\n\t" \
  "v_lshrrev_b32 v18, 10, v12\n\t" \
  "v_mfma_f32_16x16x32_bf16 v[212:215], v[160:163], v[148:151], v[212:215]\n\t" \
  "v_mfma_f32_16x16x32_bf16 v[212:215], v[172:175], v[76:79], v[212:215]\n\t" \
  "v_and_b32 v18, 12, v18\n\t" \
  "v_mfma_f32_16x16x32_bf16 v[212:215], v[180:183], v[184:187], v[212:215]\n\t" \
  "v_mfma_f32_16x16x32_bf16 v[212:215], v[188:191], v[176:179], v[212:215]\n\t" \
  "v_mov_b32 v17, 16\n\t" \
  "v_mfma_f32_16x16x32_bf16 v[212:215], v[68:71], v[168:171], v[212:215]\n\t" \
  "v_mfma_f32_16x16x32_bf16 v[212:215], v[140:143], v[164:167], v[212:215]\n\t" \
  "v_add_u32 v18, v56, v18\n\t" \
  "v_mfma_f32_16x16x32_bf16 v[216:219], v[160:163], v[132:135], v[216:219]\n\t" \
  "v_mfma_f32_16x16x32_bf16 v[216:219], v[172:175], v[148:151], v[216:219]\n\t" \
  "s_mov_b32 s76, 0\n\t" \
  "v_mfma_f32_16x16x32_bf16 v[216:219], v[180:183], v[76:79], v[216:219]\n\t" \
  "v_mfma_f32_16x16x32_bf16 v[216:219], v[188:191], v[184:187], v[216:219]\n\t" \
  "s_brev_b32 s77, 1\n\t" \
  "v_mfma_f32_16x16x32_bf16 v[216:219], v[68:71], v[176:179], v[216:219]\n\t" \
  "v_mfma_f32_16x16x32_bf16 v[216:219], v[140:143], v[168:171], v[216:219]\n\t" \
  "v_add_u32 v28, 0, v13\n\t" \
  "s_waitcnt lgkmcnt(0)\n\t" \
  "v_mfma_f32_16x16x32_bf16 v[216:219], v[124:127], v[164:167], v[216:219]\n\t" \
  "v_mfma_f32_16x16x32_bf16 v[220:223], v[160:163], v[116:119], v[220:223]\n\t" \
  "v_xor_b32 v29, 32, v28\n\t" \
  "v_mfma_f32_16x16x32_bf16 v[220:223], v[172:175], v[132:135], v[220:223]\n\t" \
  "v_mfma_f32_16x16x32_bf16 v[220:223], v[180:183], v[148:151], v[220:223]\n\t" \
  "v_xor_b32 v30, 64, v28\n\t" \
  "v_mfma_f32_16x16x32_bf16 v[220:223], v[188:191], v[76:79], v[220:223]\n\t" \
  "v_mfma_f32_16x16x32_bf16 v[220:223], v[68:71], v[184:187], v[220:223]\n\t" \
  "v_xor_b32 v31, 96, v28\n\t" \
  "ds_read_b64_tr_b16 v[76:77], v5 offset:256\n\t" \
  "v_mfma_f32_16x16x32_bf16 v[220:223], v[140:143], v[176:179], v[220:223]\n\t" \
  "ds_read_b64_tr_b16 v[78:79], v5 offset:272\n\t" \
  "v_mfma_f32_16x16x32_bf16 v[220:223], v[124:127], v[168:171], v[220:223]\n\t" \
  "v_add_u32 v32, 65536, v13\n\t" \
  "ds_read_b128 v[128:131], v8 offset:16384\n\t" \
  "v_mfma_f32_16x16x32_bf16 v[220:223], v[64:67], v[164:167], v[220:223]\n\t" \
  "ds_read_b128 v[136:139], v8 offset:15360\n\t" \
  "v_mfma_f32_16x16x32_bf16 v[252:255], v[160:163], v[72:75], v[252:255]\n\t" \
  "v_xor_b32 v33, 32, v32\n\t" \
  "ds_read_b64_tr_b16 v[144:145], v5 offset:768\n\t" \
  "v_mfma_f32_16x16x32_bf16 v[252:255], v[172:175], v[80:83], v[252:255]\n\t" \
  "ds_read_b64_tr_b16 v[146:147], v5 offset:784\n\t" \
  "v_mfma_f32_16x16x32_bf16 v[252:255], v[180:183], v[84:87], v[252:255]\n\t" \
  "s_mov_b32 exec_hi, 0\n\t" \
  "global_load_dword v2, v1, %14 offset:-4096 sc1\n\t" \
  "s_mov_b32 exec_hi, -1\n\t" \
  "v_xor_b32 v34, 64, v32\n\t" \
  "v_mfma_f32_16x16x32_bf16 v[252:255], v[188:191], v[88:91], v[252:255]\n\t" \
  "ds_read_b128 v[96:99], v8 offset:14336\n\t" \
  "v_mfma_f32_16x16x32_bf16 v[252:255], v[68:71], v[92:95], v[252:255]\n\t" \
  "v_xor_b32 v35, 96, v32\n\t" \
  "ds_read_b64_tr_b16 v[72:73], v5 offset:1280\n\t" \
  "v_mfma_f32_16x16x32_bf16 v[252:255], v[140:143], v[108:111], v[252:255]\n\t" \
  "ds_read_b64_tr_b16 v[74:75], v5 offset:1296\n\t" \
  "v_mfma_f32_16x16x32_bf16 v[252:255], v[124:127], v[112:115], v[252:255]\n\t" \
  "v_add_u32 v36, 131072, v13\n\t" \
  "ds_read_b128 v[100:103], v8 offset:13312\n\t" \
  "v_mfma_f32_16x16x32_bf16 v[252:255], v[64:67], v[120:123], v[252:255]\n\t" \
  "ds_read_b64_tr_b16 v[104:105], v5 offset:1792\n\t" \
  "v_mfma_f32_16x16x32_bf16 v[248:251], v[160:163], v[80:83], v[248:251]\n\t" \
  "v_xor_b32 v37, 32, v36\n\t" \
  "ds_read_b64_tr_b16 v[106:107], v5 offset:1808\n\t" \
  "v_mfma_f32_16x16x32_bf16 v[248:251], v[172:175], v[84:87], v[248:251]\n\t" \
  "ds_read_b128 v[152:155], v8 offset:12288\n\t" \
  "v_mfma_f32_16x16x32_bf16 v[248:251], v[180:183], v[88:91], v[248:251]\n\t" \
  "v_xor_b32 v38, 64, v36\n\t" \
  "ds_read_b64_tr_b16 v[64:65], v5 offset:2304\n\t" \
  "v_mfma_f32_16x16x32_bf16 v[248:251], v[188:191], v[92:95], v[248:251]\n\t" \
  "ds_read_b64_tr_b16 v[66:67], v5 offset:2320\n\t" \
  "v_mfma_f32_16x16x32_bf16 v[248:251], v[68:71], v[108:111], v[248:251]\n\t" \
  "v_xor_b32 v39, 96, v36\n\t" \
  "ds_read_b128 v[80:83], v8 offset:11264\n\t" \
  "v_mfma_f32_16x16x32_bf16 v[248:251], v[140:143], v[112:115], v[248:251]\n\t" \
  "v_mfma_f32_16x16x32_bf16 v[248:251], v[124:127], v[120:123], v[248:251]\n\t" \
  "v_lshrrev_b32 v57, 5, v4\n\t" \
  "v_mfma_f32_16x16x32_bf16 v[244:247], v[160:163], v[84:87], v[244:247]\n\t" \
  "v_mfma_f32_16x16x32_bf16 v[244:247], v[172:175], v[88:91], v[244:247]\n\t" \
  "v_and_b32 v57, 0x18, v57\n\t" \
  "ds_read_b64_tr_b16 v[84:85], v5 offset:2816\n\t" \
  "v_mfma_f32_16x16x32_bf16 v[244:247], v[180:183], v[92:95], v[244:247]\n\t" \
  "ds_read_b64_tr_b16 v[86:87], v5 offset:2832\n\t" \
  "v_mfma_f32_16x16x32_bf16 v[244:247], v[188:191], v[108:111], v[244:247]\n\t" \
  "v_and_b32 v62, 0x70, v4\n\t" \
  "ds_read_b128 v[156:159], v8 offset:10240\n\t" \
  "v_mfma_f32_16x16x32_bf16 v[244:247], v[68:71], v[112:115], v[244:247]\n\t" \
  "v_mfma_f32_16x16x32_bf16 v[244:247], v[140:143], v[120:123], v[244:247]\n\t" \
  "v_xor_b32 v57, v57, v62\n\t" \
  "v_mfma_f32_16x16x32_bf16 v[240:243], v[160:163], v[88:91], v[240:243]\n\t" \
  "v_mfma_f32_16x16x32_bf16 v[240:243], v[172:175], v[92:95], v[240:243]\n\t" \
  "v_and_b32 v58, 0xf0, v4\n\t" \
  "v_mfma_f32_16x16x32_bf16 v[240:243], v[180:183], v[108:111], v[240:243]\n\t" \
  "ds_read_b64_tr_b16 v[88:89], v5 offset:3328\n\t" \
  "v_mfma_f32_16x16x32_bf16 v[240:243], v[188:191], v[112:115], v[240:243]\n\t" \
  "v_lshl_add_u32 v57, v58, 3, v57\n\t" \
  "ds_read_b64_tr_b16 v[90:91], v5 offset:3344\n\t" \
  "v_mfma_f32_16x16x32_bf16 v[240:243], v[68:71], v[120:123], v[240:243]\n\t" \
  "ds_read_b128 v[164:167], v8 offset:9216\n\t" \
  "v_mfma_f32_16x16x32_bf16 v[236:239], v[160:163], v[92:95], v[236:239]\n\t" \
  "v_add_u32 v57, %23, v57\n\t" \
  "v_mfma_f32_16x16x32_bf16 v[236:239], v[172:175], v[108:111], v[236:239]\n\t" \
  "v_mfma_f32_16x16x32_bf16 v[236:239], v[180:183], v[112:115], v[236:239]\n\t" \
  "v_xor_b32 v58, 32, v57\n\t" \
  "v_mfma_f32_16x16x32_bf16 v[236:239], v[188:191], v[120:123], v[236:239]\n\t" \
  "v_mfma_f32_16x16x32_bf16 v[232:235], v[160:163], v[108:111], v[232:235]\n\t" \
  "v_xor_b32 v59, 64, v57\n\t" \
  "v_mfma_f32_16x16x32_bf16 v[232:235], v[172:175], v[112:115], v[232:235]\n\t" \
  "ds_read_b64_tr_b16 v[68:69], v5 offset:3840\n\t" \
  "v_mfma_f32_16x16x32_bf16 v[232:235], v[180:183], v[120:123], v[232:235]\n\t" \
  "v_xor_b32 v60, 96, v57\n\t" \
  "ds_read_b64_tr_b16 v[70:71], v5 offset:3856\n\t" \
  "v_mfma_f32_16x16x32_bf16 v[228:231], v[160:163], v[112:115], v[228:231]\n\t" \
  "ds_read_b128 v[92:95], v8 offset:0\n\t" \
  "v_mfma_f32_16x16x32_bf16 v[228:231], v[172:175], v[120:123], v[228:231]\n\t" \
  "v_lshrrev_b32 v61, 3, v4\n\t" \
  "ds_read_b128 v[168:171], v8 offset:1024\n\t" \
  "v_mfma_f32_16x16x32_bf16 v[224:227], v[160:163], v[120:123], v[224:227]\n\t" \
  "ds_read_b128 v[172:175], v8 offset:2048\n\t" \
  "s_waitcnt lgkmcnt(5)\n\t" \
  "v_mfma_f32_16x16x32_bf16 v[192:195], v[76:79], v[128:131], v[192:195]\n\t" \
  "v_and_b32 v61, 0x70, v61\n\t" \
  "ds_read_b128 v[160:163], v8 offset:3072\n\t" \
  "v_mfma_f32_16x16x32_bf16 v[196:199], v[76:79], v[136:139], v[196:199]\n\t" \
  "ds_read_b128 v[176:179], v8 offset:4096\n\t" \
  "v_mfma_f32_16x16x32_bf16 v[196:199], v[144:147], v[128:131], v[196:199]\n\t" \
  "v_xor_b32 v61, v61, v4\n\t" \
  "ds_read_b128 v[180:183], v8 offset:5120\n\t" \
  "v_mfma_f32_16x16x32_bf16 v[200:203], v[76:79], v[96:99], v[200:203]\n\t" \
  "ds_read_b128 v[184:187], v8 offset:6144\n\t" \
  "v_mfma_f32_16x16x32_bf16 v[200:203], v[144:147], v[136:139], v[200:203]\n\t" \
  "v_add_u32 v61, %23, v61\n\t" \
  "ds_read_b128 v[188:191], v8 offset:7168\n\t" \
  "v_mfma_f32_16x16x32_bf16 v[200:203], v[72:75], v[128:131], v[200:203]\n\t" \
  "v_mfma_f32_16x16x32_bf16 v[204:207], v[76:79], v[100:103], v[204:207]\n\t" \
  "v_and_b32 v63, 0x380, v4\n\t" \
  "v_mfma_f32_16x16x32_bf16 v[204:207], v[144:147], v[96:99], v[204:207]\n\t" \
  "v_mfma_f32_16x16x32_bf16 v[204:207], v[72:75], v[136:139], v[204:207]\n\t" \
  "v_lshl_add_u32 v62, v63, 6, v62\n\t" \
  "v_mfma_f32_16x16x32_bf16 v[204:207], v[104:107], v[128:131], v[204:207]\n\t" \
  "v_mfma_f32_16x16x32_bf16 v[208:211], v[76:79], v[152:155], v[208:211]\n\t" \
  "v_add_u32 v63, 0x10000, v62\n\t" \
  "v_mfma_f32_16x16x32_bf16 v[208:211], v[144:147], v[100:103], v[208:211]\n\t" \
  "v_mfma_f32_16x16x32_bf16 v[208:211], v[72:75], v[96:99], v[208:211]\n\t" \
  "v_mfma_f32_16x16x32_bf16 v[208:211], v[104:107], v[136:139], v[208:211]\n\t" \
  "v_mfma_f32_16x16x32_bf16 v[208:211], v[64:67], v[128:131], v[208:211]\n\t" \
  "v_mfma_f32_16x16x32_bf16 v[212:215], v[76:79], v[80:83], v[212:215]\n\t" \
  "v_mfma_f32_16x16x32_bf16 v[212:215], v[144:147], v[152:155], v[212:215]\n\t" \
  "v_mfma_f32_16x16x32_bf16 v[212:215], v[72:75], v[100:103], v[212:215]\n\t" \
  "v_mfma_f32_16x16x32_bf16 v[212:215], v[104:107], v[96:99], v[212:215]\n\t" \
  "v_mfma_f32_16x16x32_bf16 v[212:215], v[64:67], v[136:139], v[212:215]\n\t" \
  "v_mfma_f32_16x16x32_bf16 v[212:215], v[84:87], v[128:131], v[212:215]\n\t" \
  "v_mfma_f32_16x16x32_bf16 v[216:219], v[76:79], v[156:159], v[216:219]\n\t" \
  "v_mfma_f32_16x16x32_bf16 v[216:219], v[144:147], v[80:83], v[216:219]\n\t" \
  "v_mfma_f32_16x16x32_bf16 v[216:219], v[72:75], v[152:155], v[216:219]\n\t" \
  "v_mfma_f32_16x16x32_bf16 v[216:219], v[104:107], v[100:103], v[216:219]\n\t" \
  "v_mfma_f32_16x16x32_bf16 v[216:219], v[64:67], v[96:99], v[216:219]\n\t" \
  "v_mfma_f32_16x16x32_bf16 v[216:219], v[84:87], v[136:139], v[216:219]\n\t" \
  "v_mfma_f32_16x16x32_bf16 v[216:219], v[88:91], v[128:131], v[216:219]\n\t" \
  "v_mfma_f32_16x16x32_bf16 v[220:223], v[76:79], v[164:167], v[220:223]\n\t" \
  "v_mfma_f32_16x16x32_bf16 v[220:223], v[144:147], v[156:159], v[220:223]\n\t" \
  "v_mfma_f32_16x16x32_bf16 v[220:223], v[72:75], v[80:83], v[220:223]\n\t" \
  "v_mfma_f32_16x16x32_bf16 v[220:223], v[104:107], v[152:155], v[220:223]\n\t" \
  "v_mfma_f32_16x16x32_bf16 v[220:223], v[64:67], v[100:103], v[220:223]\n\t" \
  "ds_read_b64_tr_b16 v[148:149], v5 offset:384\n\t" \
  "v_mfma_f32_16x16x32_bf16 v[220:223], v[84:87], v[96:99], v[220:223]\n\t" \
  "ds_read_b64_tr_b16 v[150:151], v5 offset:400\n\t" \
  "v_mfma_f32_16x16x32_bf16 v[220:223], v[88:91], v[136:139], v[220:223]\n\t" \
  "ds_read_b128 v[80:83], v9 offset:16384\n\t" \
  "s_waitcnt lgkmcnt(3)\n\t" \
  "v_mfma_f32_16x16x32_bf16 v[220:223], v[68:71], v[128:131], v[220:223]\n\t" \
  "ds_read_b128 v[140:143], v9 offset:15360\n\t" \
  "v_mfma_f32_16x16x32_bf16 v[252:255], v[76:79], v[92:95], v[252:255]\n\t" \
  "ds_read_b64_tr_b16 v[132:133], v5 offset:896\n\t" \
  "v_mfma_f32_16x16x32_bf16 v[252:255], v[144:147], v[168:171], v[252:255]\n\t" \
  "ds_read_b64_tr_b16 v[134:135], v5 offset:912\n\t" \
  "v_mfma_f32_16x16x32_bf16 v[252:255], v[72:75], v[172:175], v[252:255]\n\t" \
  "ds_read_b128 v[124:127], v9 offset:14336\n\t" \
  "v_mfma_f32_16x16x32_bf16 v[252:255], v[104:107], v[160:163], v[252:255]\n\t" \
  "ds_read_b64_tr_b16 v[116:117], v5 offset:1408\n\t" \
  "v_mfma_f32_16x16x32_bf16 v[252:255], v[64:67], v[176:179], v[252:255]\n\t" \
  "ds_read_b64_tr_b16 v[118:119], v5 offset:1424\n\t" \
  "v_mfma_f32_16x16x32_bf16 v[252:255], v[84:87], v[180:183], v[252:255]\n\t" \
  "ds_read_b128 v[92:95], v9 offset:13312\n\t" \
  "v_mfma_f32_16x16x32_bf16 v[252:255], v[88:91], v[184:187], v[252:255]\n\t" \
  "ds_read_b64_tr_b16 v[108:109], v5 offset:1920\n\t" \
  "v_mfma_f32_16x16x32_bf16 v[252:255], v[68:71], v[188:191], v[252:255]\n\t" \
  "ds_read_b64_tr_b16 v[110:111], v5 offset:1936\n\t" \
  "v_mfma_f32_16x16x32_bf16 v[248:251], v[76:79], v[168:171], v[248:251]\n\t" \
  "ds_read_b128 v[112:115], v9 offset:12288\n\t" \
  "v_mfma_f32_16x16x32_bf16 v[248:251], v[144:147], v[172:175], v[248:251]\n\t" \
  "v_mfma_f32_16x16x32_bf16 v[248:251], v[72:75], v[160:163], v[248:251]\n\t" \
  "ds_read_b64_tr_b16 v[120:121], v5 offset:2432\n\t" \
  "v_mfma_f32_16x16x32_bf16 v[248:251], v[104:107], v[176:179], v[248:251]\n\t" \
  "ds_read_b64_tr_b16 v[122:123], v5 offset:2448\n\t" \
  "v_mfma_f32_16x16x32_bf16 v[248:251], v[64:67], v[180:183], v[248:251]\n\t" \
  "ds_read_b128 v[68:71], v9 offset:11264\n\t" \
  "v_mfma_f32_16x16x32_bf16 v[248:251], v[84:87], v[184:187], v[248:251]\n\t" \
  "v_mfma_f32_16x16x32_bf16 v[248:251], v[88:91], v[188:191], v[248:251]\n\t" \
  "v_mfma_f32_16x16x32_bf16 v[244:247], v[76:79], v[172:175], v[244:247]\n\t" \
  "v_mfma_f32_16x16x32_bf16 v[244:247], v[144:147], v[160:163], v[244:247]\n\t" \
  "ds_read_b64_tr_b16 v[128:129], v5 offset:2944\n\t" \
  "v_mfma_f32_16x16x32_bf16 v[244:247], v[72:75], v[176:179], v[244:247]\n\t" \
  "ds_read_b64_tr_b16 v[130:131], v5 offset:2960\n\t" \
  "v_mfma_f32_16x16x32_bf16 v[244:247], v[104:107], v[180:183], v[244:247]\n\t" \
  "ds_read_b128 v[88:91], v9 offset:10240\n\t" \
  "v_mfma_f32_16x16x32_bf16 v[244:247], v[64:67], v[184:187], v[244:247]\n\t" \
  "v_mfma_f32_16x16x32_bf16 v[244:247], v[84:87], v[188:191], v[244:247]\n\t" \
  "v_mfma_f32_16x16x32_bf16 v[240:243], v[76:79], v[160:163], v[240:243]\n\t" \
  "v_mfma_f32_16x16x32_bf16 v[240:243], v[144:147], v[176:179], v[240:243]\n\t" \
  "v_mfma_f32_16x16x32_bf16 v[240:243], v[72:75], v[180:183], v[240:243]\n\t" \
  "ds_read_b64_tr_b16 v[136:137], v5 offset:3456\n\t" \
  "v_mfma_f32_16x16x32_bf16 v[240:243], v[104:107], v[184:187], v[240:243]\n\t" \
  "ds_read_b64_tr_b16 v[138:139], v5 offset:3472\n\t" \
  "v_mfma_f32_16x16x32_bf16 v[240:243], v[64:67], v[188:191], v[240:243]\n\t" \
  "ds_read_b128 v[84:87], v9 offset:9216\n\t" \
  "v_mfma_f32_16x16x32_bf16 v[236:239], v[76:79], v[176:179], v[236:239]\n\t" \
  "v_mfma_f32_16x16x32_bf16 v[236:239], v[144:147], v[180:183], v[236:239]\n\t" \
  "v_mfma_f32_16x16x32_bf16 v[236:239], v[72:75], v[184:187], v[236:239]\n\t" \
  "v_mfma_f32_16x16x32_bf16 v[236:239], v[104:107], v[188:191], v[236:239]\n\t" \
  "v_mfma_f32_16x16x32_bf16 v[232:235], v[76:79], v[180:183], v[232:235]\n\t" \
  "v_mfma_f32_16x16x32_bf16 v[232:235], v[144:147], v[184:187], v[232:235]\n\t" \
  "ds_read_b64_tr_b16 v[96:97], v5 offset:3968\n\t" \
  "v_mfma_f32_16x16x32_bf16 v[232:235], v[72:75], v[188:191], v[232:235]\n\t" \
  "ds_read_b64_tr_b16 v[98:99], v5 offset:3984\n\t" \
  "ds_write_b32 v18, v17 offset:176\n\t" \
  "v_mfma_f32_16x16x32_bf16 v[228:231], v[76:79], v[184:187], v[228:231]\n\t" \
  "ds_read_b128 v[100:103], v9 offset:7168\n\t" \
  "v_mfma_f32_16x16x32_bf16 v[228:231], v[144:147], v[188:191], v[228:231]\n\t" \
  "ds_read_b128 v[104:107], v9 offset:6144\n\t" \
  "v_mfma_f32_16x16x32_bf16 v[224:227], v[76:79], v[188:191], v[224:227]\n\t" \
  "ds_read_b128 v[144:147], v9 offset:5120\n\t" \
  "s_waitcnt lgkmcnt(6)\n\t" \
  "v_mfma_f32_16x16x32_bf16 v[192:195], v[148:151], v[80:83], v[192:195]\n\t" \
  "v_mfma_f32_16x16x32_bf16 v[196:199], v[148:151], v[140:143], v[196:199]\n\t" \
  "v_mfma_f32_16x16x32_bf16 v[196:199], v[132:135], v[80:83], v[196:199]\n\t" \
  "ds_read_b128 v[152:155], v9 offset:4096\n\t" \
  "v_mfma_f32_16x16x32_bf16 v[200:203], v[148:151], v[124:127], v[200:203]\n\t" \
  "v_mfma_f32_16x16x32_bf16 v[200:203], v[132:135], v[140:143], v[200:203]\n\t" \
  "v_mfma_f32_16x16x32_bf16 v[200:203], v[116:119], v[80:83], v[200:203]\n\t" \
  "v_mfma_f32_16x16x32_bf16 v[204:207], v[148:151], v[92:95], v[204:207]\n\t" \
  "ds_read_b128 v[156:159], v9 offset:3072\n\t" \
  "v_cndmask_b32 v20, v195, 0, s[76:77]\n\t" \
  "v_mfma_f32_16x16x32_bf16 v[204:207], v[132:135], v[124:127], v[204:207]\n\t" \
  "ds_bpermute_b32 v20, v15, v20\n\t" \
  "v_add_f32 v225, v225, v192\n\t" \
  "v_mfma_f32_16x16x32_bf16 v[204:207], v[116:119], v[140:143], v[204:207]\n\t" \
  "v_add_f32 v226, v226, v193\n\t" \
  "v_add_f32 v227, v227, v194\n\t" \
  "v_mfma_f32_16x16x32_bf16 v[204:207], v[108:111], v[80:83], v[204:207]\n\t" \
  "ds_read_b128 v[40:43], v56 offset:80\n\t" \
  "v_cndmask_b32 v21, v199, v195, s[76:77]\n\t" \
  "v_mfma_f32_16x16x32_bf16 v[208:211], v[148:151], v[112:115], v[208:211]\n\t" \
  "ds_bpermute_b32 v21, v15, v21\n\t" \
  "v_add_f32 v229, v229, v196\n\t" \
  "v_mfma_f32_16x16x32_bf16 v[208:211], v[132:135], v[92:95], v[208:211]\n\t" \
  "ds_read_b128 v[164:167], v9 offset:2048\n\t" \
  "v_add_f32 v230, v230, v197\n\t" \
  "v_mfma_f32_16x16x32_bf16 v[208:211], v[116:119], v[124:127], v[208:211]\n\t" \
  "v_add_f32 v231, v231, v198\n\t" \
  "v_cndmask_b32 v22, v203, v199, s[76:77]\n\t" \
  "v_mfma_f32_16x16x32_bf16 v[208:211], v[108:111], v[140:143], v[208:211]\n\t" \
  "ds_bpermute_b32 v22, v15, v22\n\t" \
  "v_add_f32 v233, v233, v200\n\t" \
  "v_mfma_f32_16x16x32_bf16 v[208:211], v[120:123], v[80:83], v[208:211]\n\t" \
  "v_add_f32 v234, v234, v201\n\t" \
  "v_add_f32 v235, v235, v202\n\t" \
  "v_mfma_f32_16x16x32_bf16 v[212:215], v[148:151], v[68:71], v[212:215]\n\t" \
  "s_waitcnt lgkmcnt(4)\n\t" \
  "v_add_f32 v224, v224, v20\n\t" \
  "v_cndmask_b32 v23, v207, v203, s[76:77]\n\t" \
  "v_mfma_f32_16x16x32_bf16 v[212:215], v[132:135], v[112:115], v[212:215]\n\t" \
  "s_waitcnt lgkmcnt(3)\n\t" \
  "v_min_u32 v40, v40, v41\n\t" \
  "v_min3_u32 v40, v40, v42, v43\n\t" \
  "v_mfma_f32_16x16x32_bf16 v[212:215], v[116:119], v[92:95], v[212:215]\n\t" \
  "v_readfirstlane_b32 vcc_lo, v40\n\t" \
  "ds_read_b128 v[168:171], v9 offset:1024\n\t" \
  "v_mfma_f32_16x16x32_bf16 v[212:215], v[108:111], v[124:127], v[212:215]\n\t" \
  "s_cmp_ge_u32 vcc_lo, 7\n\t" \
  "s_cbranch_scc0 30f\n\t" \
  "31:\n\t" \
  "v_mfma_f32_16x16x32_bf16 v[212:215], v[120:123], v[140:143], v[212:215]\n\t" \
  "ds_bpermute_b32 v23, v15, v23\n\t" \
  "v_add_f32 v237, v237, v204\n\t" \
  "v_mfma_f32_16x16x32_bf16 v[212:215], v[128:131], v[80:83], v[212:215]\n\t" \
  "v_add_f32 v238, v238, v205\n\t" \
  "v_add_f32 v239, v239, v206\n\t" \
  "v_mfma_f32_16x16x32_bf16 v[216:219], v[148:151], v[88:91], v[216:219]\n\t" \
  "s_waitcnt lgkmcnt(4)\n\t" \
  "v_add_f32 v228, v228, v21\n\t" \
  "s_waitcnt lgkmcnt(2)\n\t" \
  "v_add_f32 v232, v232, v22\n\t" \
  "v_mfma_f32_16x16x32_bf16 v[216:219], v[132:135], v[68:71], v[216:219]\n\t" \
  "v_cndmask_b32 v24, v211, v207, s[76:77]\n\t" \
  "ds_bpermute_b32 v24, v15, v24\n\t" \
  "v_mfma_f32_16x16x32_bf16 v[216:219], v[116:119], v[112:115], v[216:219]\n\t" \
  "v_add_f32 v241, v241, v208\n\t" \
  "v_add_f32 v242, v242, v209\n\t" \
  "v_mfma_f32_16x16x32_bf16 v[216:219], v[108:111], v[92:95], v[216:219]\n\t" \
  "ds_read_b128 v[172:175], v9 offset:0\n\t" \
  "ds_write_b32 v18, v17 offset:192\n\t" \
  "v_mfma_f32_16x16x32_bf16 v[216:219], v[120:123], v[124:127], v[216:219]\n\t" \
  "ds_read_b32 v16, v56 offset:96\n\t" \
  "v_add_f32 v243, v243, v210\n\t" \
  "v_mfma_f32_16x16x32_bf16 v[216:219], v[128:131], v[140:143], v[216:219]\n\t" \
  "v_cndmask_b32 v25, v215, v211, s[76:77]\n\t" \
  "ds_bpermute_b32 v25, v15, v25\n\t" \
  "v_mfma_f32_16x16x32_bf16 v[216:219], v[136:139], v[80:83], v[216:219]\n\t" \
  "s_waitcnt lgkmcnt(5)\n\t" \
  "v_add_f32 v236, v236, v23\n\t" \
  "v_add_f32 v245, v245, v212\n\t" \
  "v_mfma_f32_16x16x32_bf16 v[220:223], v[148:151], v[84:87], v[220:223]\n\t" \
  "v_add_f32 v246, v246, v213\n\t" \
  "v_add_f32 v247, v247, v214\n\t" \
  "v_mfma_f32_16x16x32_bf16 v[220:223], v[132:135], v[88:91], v[220:223]\n\t" \
  "ds_read_b64_tr_b16 v[192:193], v28 offset:22528\n\t" \
  "v_mfma_f32_16x16x32_bf16 v[220:223], v[116:119], v[68:71], v[220:223]\n\t" \
  "s_waitcnt lgkmcnt(5)\n\t" \
  "v_add_f32 v240, v240, v24\n\t" \
  "v_mfma_f32_16x16x32_bf16 v[220:223], v[108:111], v[112:115], v[220:223]\n\t" \
  "ds_read_b64_tr_b16 v[194:195], v28 offset:23040\n\t" \
  "v_mfma_f32_16x16x32_bf16 v[220:223], v[120:123], v[92:95], v[220:223]\n\t" \
  "s_waitcnt lgkmcnt(3)\n\t" \
  "v_readfirstlane_b32 s75, v16\n\t" \
  "v_mfma_f32_16x16x32_bf16 v[220:223], v[128:131], v[124:127], v[220:223]\n\t" \
  "s_cmp_lg_u32 s75, 0\n\t" \
  "s_cselect_b32 s69, -1, 0\n\t" \
  "v_mfma_f32_16x16x32_bf16 v[220:223], v[136:139], v[140:143], v[220:223]\n\t" \
  "s_lshr_b64 s[90:91], %18, 0\n\t" \
  "s_and_b32 s90, s90, 15\n\t" \
  "s_lshl_b32 s90, s90, 15\n\t" \
  "v_and_b32 v52, s69, v53\n\t" \
  "v_mfma_f32_16x16x32_bf16 v[220:223], v[96:99], v[80:83], v[220:223]\n\t" \
  "s_add_u32 s90, s90, s92\n\t" \
  "s_addc_u32 s91, s93, 0\n\t" \
  "s_cmp_lg_u32 s75, 0\n\t" \
  "v_mfma_f32_16x16x32_bf16 v[224:227], v[148:151], v[100:103], v[224:227]\n\t" \
  "s_cselect_b64 s[90:91], s[90:91], %11\n\t" \
  "v_cndmask_b32 v26, v219, v215, s[76:77]\n\t" \
  "v_mfma_f32_16x16x32_bf16 v[228:231], v[148:151], v[104:107], v[228:231]\n\t" \
  "global_load_dwordx4 v[64:67], v52, s[90:91] offset:0 sc1\n\t" \
  "ds_bpermute_b32 v26, v15, v26\n\t" \
  "v_mfma_f32_16x16x32_bf16 v[228:231], v[132:135], v[100:103], v[228:231]\n\t" \
  "v_add_f32 v249, v249, v216\n\t" \
  "v_add_f32 v250, v250, v217\n\t" \
  "v_mfma_f32_16x16x32_bf16 v[232:235], v[148:151], v[144:147], v[232:235]\n\t" \
  "v_add_f32 v251, v251, v218\n\t" \
  "s_waitcnt lgkmcnt(3)\n\t" \
  "v_add_f32 v244, v244, v25\n\t" \
  "v_mfma_f32_16x16x32_bf16 v[232:235], v[132:135], v[104:107], v[232:235]\n\t" \
  "global_load_dwordx4 v[68:71], v52, s[90:91] offset:1024 sc1\n\t" \
  "ds_read_b64_tr_b16 v[196:197], v28 offset:26624\n\t" \
  "v_mfma_f32_16x16x32_bf16 v[232:235], v[116:119], v[100:103], v[232:235]\n\t" \
  "v_cndmask_b32 v27, v223, v219, s[76:77]\n\t" \
  "ds_bpermute_b32 v27, v15, v27\n\t" \
  "508:\n\t" \
  "s_cbranch_execz 26b\n\t" \
  "v_mfma_f32_16x16x32_bf16 v[236:239], v[148:151], v[152:155], v[236:239]\n\t" \
  "v_add_f32 v253, v253, v220\n\t" \
  "v_add_f32 v254, v254, v221\n\t" \
  "v_mfma_f32_16x16x32_bf16 v[236:239], v[132:135], v[144:147], v[236:239]\n\t" \
  "global_load_dwordx4 v[72:75], v52, s[90:91] offset:2048 sc1\n\t" \
  "v_add_f32 v255, v255, v222\n\t" \
  "v_mfma_f32_16x16x32_bf16 v[236:239], v[116:119], v[104:107], v[236:239]\n\t" \
  "v_cvt_pk_bf16_f32 v45, v226, v227\n\t" \
  "v_cvt_pk_bf16_f32 v44, v224, v225\n\t" \
  "v_mfma_f32_16x16x32_bf16 v[236:239], v[108:111], v[100:103], v[236:239]\n\t" \
  "s_waitcnt lgkmcnt(2)\n\t" \
  "v_add_f32 v248, v248, v26\n\t" \
  "ds_write_b64 v14, v[44:45] offset:0\n\t" \
  "v_mfma_f32_16x16x32_bf16 v[240:243], v[148:151], v[156:159], v[240:243]\n\t" \
  "global_load_dwordx4 v[76:79], v52, s[90:91] offset:3072 sc1\n\t" \
  "v_cvt_pk_bf16_f32 v47, v230, v231\n\t" \
  "v_mfma_f32_16x16x32_bf16 v[240:243], v[132:135], v[152:155], v[240:243]\n\t" \
  "v_cvt_pk_bf16_f32 v46, v228, v229\n\t" \
  "v_cvt_pk_bf16_f32 v49, v234, v235\n\t" \
  "v_mfma_f32_16x16x32_bf16 v[240:243], v[116:119], v[144:147], v[240:243]\n\t" \
  "ds_write_b64 v14, v[46:47] offset:512\n\t" \
  "v_cvt_pk_bf16_f32 v48, v232, v233\n\t" \
  "v_mfma_f32_16x16x32_bf16 v[240:243], v[108:111], v[104:107], v[240:243]\n\t" \
  "ds_write_b64 v14, v[48:49] offset:1024\n\t" \
  "s_waitcnt lgkmcnt(3)\n\t" \
  "v_add_f32 v252, v252, v27\n\t" \
  "v_mfma_f32_16x16x32_bf16 v[240:243], v[120:123], v[100:103], v[240:243]\n\t" \
  "s_lshr_b64 s[90:91], %18, 4\n\t" \
  "s_and_b32 s90, s90, 15\n\t" \
  "s_lshl_b32 s90, s90, 15\n\t" \
  "v_mfma_f32_16x16x32_bf16 v[244:247], v[148:151], v[164:167], v[244:247]\n\t" \
  "s_add_u32 s90, s90, s92\n\t" \
  "s_addc_u32 s91, s93, 0\n\t" \
  "s_cmp_lg_u32 s75, 0\n\t" \
  "v_mfma_f32_16x16x32_bf16 v[244:247], v[132:135], v[156:159], v[244:247]\n\t" \
  "s_cselect_b64 s[90:91], s[90:91], %11\n\t" \
  "v_cvt_pk_bf16_f32 v51, v238, v239\n\t" \
  "v_mfma_f32_16x16x32_bf16 v[244:247], v[116:119], v[152:155], v[244:247]\n\t" \
  "global_load_dwordx4 v[80:83], v52, s[90:91] offset:0 sc1\n\t" \
  "v_cvt_pk_bf16_f32 v50, v236, v237\n\t" \
  "v_mfma_f32_16x16x32_bf16 v[244:247], v[108:111], v[144:147], v[244:247]\n\t" \
  "ds_write_b64 v14, v[50:51] offset:1536\n\t" \
  "ds_read_b64_tr_b16 v[198:199], v28 offset:27136\n\t" \
  "v_mfma_f32_16x16x32_bf16 v[244:247], v[120:123], v[104:107], v[244:247]\n\t" \
  "ds_read_b64_tr_b16 v[200:201], v29 offset:22528\n\t" \
  "v_mfma_f32_16x16x32_bf16 v[244:247], v[128:131], v[100:103], v[244:247]\n\t" \
  "global_load_dwordx4 v[84:87], v52, s[90:91] offset:1024 sc1\n\t" \
  "v_cvt_pk_bf16_f32 v45, v242, v243\n\t" \
  "v_mfma_f32_16x16x32_bf16 v[248:251], v[148:151], v[168:171], v[248:251]\n\t" \
  "v_cvt_pk_bf16_f32 v44, v240, v241\n\t" \
  "ds_read_b64_tr_b16 v[202:203], v29 offset:23040\n\t" \
  "v_mfma_f32_16x16x32_bf16 v[248:251], v[132:135], v[164:167], v[248:251]\n\t" \
  "ds_write_b64 v14, v[44:45] offset:2048\n\t" \
  "ds_read_b64_tr_b16 v[204:205], v29 offset:26624\n\t" \
  "v_mfma_f32_16x16x32_bf16 v[248:251], v[116:119], v[156:159], v[248:251]\n\t" \
  "global_load_dwordx4 v[88:91], v52, s[90:91] offset:2048 sc1\n\t" \
  "ds_read_b64_tr_b16 v[206:207], v29 offset:27136\n\t" \
  "v_mfma_f32_16x16x32_bf16 v[248:251], v[108:111], v[152:155], v[248:251]\n\t" \
  "ds_read_b64_tr_b16 v[208:209], v30 offset:22528\n\t" \
  "v_mfma_f32_16x16x32_bf16 v[248:251], v[120:123], v[144:147], v[248:251]\n\t" \
  "ds_read_b64_tr_b16 v[210:211], v30 offset:23040\n\t" \
  "v_mfma_f32_16x16x32_bf16 v[248:251], v[128:131], v[104:107], v[248:251]\n\t" \
  "global_load_dwordx4 v[92:95], v52, s[90:91] offset:3072 sc1\n\t" \
  "v_cvt_pk_bf16_f32 v47, v246, v247\n\t" \
  "v_mfma_f32_16x16x32_bf16 v[248:251], v[136:139], v[100:103], v[248:251]\n\t" \
  "v_cvt_pk_bf16_f32 v46, v244, v245\n\t" \
  "ds_read_b64_tr_b16 v[212:213], v30 offset:26624\n\t" \
  "v_mfma_f32_16x16x32_bf16 v[252:255], v[148:151], v[172:175], v[252:255]\n\t" \
  "ds_write_b64 v14, v[46:47] offset:2560\n\t" \
  "ds_read_b64_tr_b16 v[214:215], v30 offset:27136\n\t" \
  "v_mfma_f32_16x16x32_bf16 v[252:255], v[132:135], v[168:171], v[252:255]\n\t" \
  "ds_read_b64_tr_b16 v[216:217], v31 offset:22528\n\t" \
  "v_mfma_f32_16x16x32_bf16 v[252:255], v[116:119], v[164:167], v[252:255]\n\t" \
  "ds_read_b64_tr_b16 v[218:219], v31 offset:23040\n\t" \
  "v_mfma_f32_16x16x32_bf16 v[252:255], v[108:111], v[156:159], v[252:255]\n\t" \
  "ds_read_b64_tr_b16 v[220:221], v31 offset:26624\n\t" \
  "v_mfma_f32_16x16x32_bf16 v[252:255], v[120:123], v[152:155], v[252:255]\n\t" \
  "v_mov_b32 v19, 15\n\t" \
  "ds_read_b64_tr_b16 v[222:223], v31 offset:27136\n\t" \
  "v_mfma_f32_16x16x32_bf16 v[252:255], v[128:131], v[144:147], v[252:255]\n\t" \
  "v_cvt_pk_bf16_f32 v49, v250, v251\n\t" \
  "v_cvt_pk_bf16_f32 v48, v248, v249\n\t" \
  "v_mfma_f32_16x16x32_bf16 v[252:255], v[136:139], v[104:107], v[252:255]\n\t" \
  "ds_write_b64 v14, v[48:49] offset:3072\n\t" \
  "v_mfma_f32_16x16x32_bf16 v[252:255], v[96:99], v[100:103], v[252:255]\n\t" \
  "ds_read_b32 v16, v56 offset:96\n\t" \
  "s_nop 6\n\t" \
  "v_cvt_pk_bf16_f32 v51, v254, v255\n\t" \
  "v_cvt_pk_bf16_f32 v50, v252, v253\n\t" \
  "ds_write_b64 v14, v[50:51] offset:3584\n\t" \
  "ds_write_b32 v18, v19 offset:32\n\t" \
  "s_cmp_eq_u32 s75, 0\n\t" \
  "s_cbranch_scc1 38f\n\t" \
  "39:\n\t" \
  "s_waitcnt vmcnt(6)\n\t" \
  "v_mfma_f32_16x16x32_bf16 v[224:227], v[192:195], v[64:67], 0\n\t" \
  "s_waitcnt lgkmcnt(6)\n\t" \
  "v_mfma_f32_16x16x32_bf16 v[224:227], v[196:199], v[68:71], v[224:227]\n\t" \
  "s_lshr_b64 s[90:91], %18, 8\n\t" \
  "s_and_b32 s90, s90, 15\n\t" \
  "s_lshl_b32 s90, s90, 15\n\t" \
  "s_add_u32 s90, s90, s92\n\t" \
  "s_addc_u32 s91, s93, 0\n\t" \
  "global_load_dwordx4 v[96:99], v53, s[90:91] offset:0 sc1\n\t" \
  "s_waitcnt vmcnt(5)\n\t" \
  "v_mfma_f32_16x16x32_bf16 v[228:231], v[196:199], v[76:79], 0\n\t" \
  "v_mfma_f32_16x16x32_bf16 v[228:231], v[192:195], v[72:75], v[228:231]\n\t" \
  "global_load_dwordx4 v[100:103], v53, s[90:91] offset:1024 sc1\n\t" \
  "v_mfma_f32_16x16x32_bf16 v[232:235], v[200:203], v[64:67], 0\n\t" \
  "ds_read_b64_tr_b16 v[192:193], v28 offset:30720\n\t" \
  "v_mfma_f32_16x16x32_bf16 v[232:235], v[204:207], v[68:71], v[232:235]\n\t" \
  "ds_read_b64_tr_b16 v[194:195], v28 offset:31232\n\t" \
  "global_load_dwordx4 v[104:107], v53, s[90:91] offset:2048 sc1\n\t" \
  "v_mfma_f32_16x16x32_bf16 v[236:239], v[204:207], v[76:79], 0\n\t" \
  "ds_read_b64_tr_b16 v[196:197], v28 offset:34816\n\t" \
  "v_mfma_f32_16x16x32_bf16 v[236:239], v[200:203], v[72:75], v[236:239]\n\t" \
  "ds_read_b64_tr_b16 v[198:199], v28 offset:35328\n\t" \
  "global_load_dwordx4 v[108:111], v53, s[90:91] offset:3072 sc1\n\t" \
  "v_mfma_f32_16x16x32_bf16 v[240:243], v[208:211], v[64:67], 0\n\t" \
  "ds_read_b64_tr_b16 v[200:201], v29 offset:30720\n\t" \
  "v_mfma_f32_16x16x32_bf16 v[240:243], v[212:215], v[68:71], v[240:243]\n\t" \
  "ds_read_b64_tr_b16 v[202:203], v29 offset:31232\n\t" \
  "s_lshr_b64 s[90:91], %18, 12\n\t" \
  "s_and_b32 s90, s90, 15\n\t" \
  "s_lshl_b32 s90, s90, 15\n\t" \
  "s_add_u32 s90, s90, s92\n\t" \
  "s_addc_u32 s91, s93, 0\n\t" \
  "global_load_dwordx4 v[112:115], v53, s[90:91] offset:0 sc1\n\t" \
  "v_mfma_f32_16x16x32_bf16 v[244:247], v[212:215], v[76:79], 0\n\t" \
  "ds_read_b64_tr_b16 v[204:205], v29 offset:34816\n\t" \
  "v_mfma_f32_16x16x32_bf16 v[244:247], v[208:211], v[72:75], v[244:247]\n\t" \
  "ds_read_b64_tr_b16 v[206:207], v29 offset:35328\n\t" \
  "global_load_dwordx4 v[116:119], v53, s[90:91] offset:1024 sc1\n\t" \
  "v_mfma_f32_16x16x32_bf16 v[248:251], v[216:219], v[64:67], 0\n\t" \
  "ds_read_b64_tr_b16 v[208:209], v30 offset:30720\n\t" \
  "s_waitcnt lgkmcnt(6)\n\t" \
  "v_mfma_f32_16x16x32_bf16 v[248:251], v[220:223], v[68:71], v[248:251]\n\t" \
  "ds_read_b64_tr_b16 v[210:211], v30 offset:31232\n\t" \
  "global_load_dwordx4 v[120:123], v53, s[90:91] offset:2048 sc1\n\t" \
  "v_mfma_f32_16x16x32_bf16 v[252:255], v[220:223], v[76:79], 0\n\t" \
  "ds_read_b64_tr_b16 v[212:213], v30 offset:34816\n\t" \
  "v_mfma_f32_16x16x32_bf16 v[252:255], v[216:219], v[72:75], v[252:255]\n\t" \
  "ds_read_b64_tr_b16 v[214:215], v30 offset:35328\n\t" \
  "global_load_dwordx4 v[124:127], v53, s[90:91] offset:3072 sc1\n\t" \
  "s_waitcnt vmcnt(10)\n\t" \
  "v_mfma_f32_16x16x32_bf16 v[224:227], v[192:195], v[80:83], v[224:227]\n\t" \
  "ds_read_b64_tr_b16 v[216:217], v31 offset:30720\n\t" \
  "s_lshr_b64 s[90:91], %18, 16\n\t" \
  "s_and_b32 s90, s90, 15\n\t" \
  "s_lshl_b32 s90, s90, 15\n\t" \
  "s_waitcnt lgkmcnt(6)\n\t" \
  "v_mfma_f32_16x16x32_bf16 v[224:227], v[196:199], v[84:87], v[224:227]\n\t" \
  "ds_read_b64_tr_b16 v[218:219], v31 offset:31232\n\t" \
  "s_add_u32 s90, s90, s92\n\t" \
  "s_addc_u32 s91, s93, 0\n\t" \
  "s_waitcnt vmcnt(8)\n\t" \
  "v_mfma_f32_16x16x32_bf16 v[228:231], v[196:199], v[92:95], v[228:231]\n\t" \
  "ds_read_b64_tr_b16 v[220:221], v31 offset:34816\n\t" \
  "global_load_dwordx4 v[128:131], v53, s[90:91] offset:0 sc1\n\t" \
  "v_mfma_f32_16x16x32_bf16 v[228:231], v[192:195], v[88:91], v[228:231]\n\t" \
  "ds_read_b64_tr_b16 v[222:223], v31 offset:35328\n\t" \
  "v_mfma_f32_16x16x32_bf16 v[232:235], v[200:203], v[80:83], v[232:235]\n\t" \
  "ds_read_b64_tr_b16 v[192:193], v28 offset:38912\n\t" \
  "s_waitcnt lgkmcnt(6)\n\t" \
  "v_mfma_f32_16x16x32_bf16 v[232:235], v[204:207], v[84:87], v[232:235]\n\t" \
  "ds_read_b64_tr_b16 v[194:195], v28 offset:39424\n\t" \
  "ds_write_b32 v18, v17 offset:160\n\t" \
  "v_mfma_f32_16x16x32_bf16 v[236:239], v[204:207], v[92:95], v[236:239]\n\t" \
  "ds_read_b64_tr_b16 v[196:197], v28 offset:43008\n\t" \
  "global_load_dwordx4 v[132:135], v53, s[90:91] offset:1024 sc1\n\t" \
  "v_mfma_f32_16x16x32_bf16 v[236:239], v[200:203], v[88:91], v[236:239]\n\t" \
  "ds_read_b64_tr_b16 v[198:199], v28 offset:43520\n\t" \
  "v_mfma_f32_16x16x32_bf16 v[240:243], v[208:211], v[80:83], v[240:243]\n\t" \
  "ds_read_b64_tr_b16 v[200:201], v29 offset:38912\n\t" \
  "s_waitcnt lgkmcnt(7)\n\t" \
  "v_mfma_f32_16x16x32_bf16 v[240:243], v[212:215], v[84:87], v[240:243]\n\t" \
  "ds_read_b64_tr_b16 v[202:203], v29 offset:39424\n\t" \
  "v_mfma_f32_16x16x32_bf16 v[244:247], v[212:215], v[92:95], v[244:247]\n\t" \
  "ds_read_b64_tr_b16 v[204:205], v29 offset:43008\n\t" \
  "global_load_dwordx4 v[136:139], v53, s[90:91] offset:2048 sc1\n\t" \
  "v_mfma_f32_16x16x32_bf16 v[244:247], v[208:211], v[88:91], v[244:247]\n\t" \
  "ds_read_b64_tr_b16 v[206:207], v29 offset:43520\n\t" \
  "v_mfma_f32_16x16x32_bf16 v[248:251], v[216:219], v[80:83], v[248:251]\n\t" \
  "ds_read_b64_tr_b16 v[208:209], v30 offset:38912\n\t" \
  "s_waitcnt lgkmcnt(6)\n\t" \
  "v_mfma_f32_16x16x32_bf16 v[248:251], v[220:223], v[84:87], v[248:251]\n\t" \
  "ds_read_b64_tr_b16 v[210:211], v30 offset:39424\n\t" \
  "v_mfma_f32_16x16x32_bf16 v[252:255], v[220:223], v[92:95], v[252:255]\n\t" \
  "ds_read_b64_tr_b16 v[212:213], v30 offset:43008\n\t" \
  "global_load_dwordx4 v[140:143], v53, s[90:91] offset:3072 sc1\n\t" \
  "v_mfma_f32_16x16x32_bf16 v[252:255], v[216:219], v[88:91], v[252:255]\n\t" \
  "ds_read_b64_tr_b16 v[214:215], v30 offset:43520\n\t" \
  "s_waitcnt vmcnt(10)\n\t" \
  "v_mfma_f32_16x16x32_bf16 v[224:227], v[192:195], v[96:99], v[224:227]\n\t" \
  "ds_read_b64_tr_b16 v[216:217], v31 offset:38912\n\t" \
  "s_lshr_b64 s[90:91], %18, 20\n\t" \
  "s_and_b32 s90, s90, 15\n\t" \
  "s_lshl_b32 s90, s90, 15\n\t" \
  "s_waitcnt lgkmcnt(6)\n\t" \
  "v_mfma_f32_16x16x32_bf16 v[224:227], v[196:199], v[100:103], v[224:227]\n\t" \
  "ds_read_b64_tr_b16 v[218:219], v31 offset:39424\n\t" \
  "s_add_u32 s90, s90, s92\n\t" \
  "s_addc_u32 s91, s93, 0\n\t" \
  "s_waitcnt vmcnt(8)\n\t" \
  "v_mfma_f32_16x16x32_bf16 v[228:231], v[196:199], v[108:111], v[228:231]\n\t" \
  "ds_read_b64_tr_b16 v[220:221], v31 offset:43008\n\t" \
  "global_load_dwordx4 v[144:147], v53, s[90:91] offset:0 sc1\n\t" \
  "v_mfma_f32_16x16x32_bf16 v[228:231], v[192:195], v[104:107], v[228:231]\n\t" \
  "ds_read_b64_tr_b16 v[222:223], v31 offset:43520\n\t" \
  "v_mfma_f32_16x16x32_bf16 v[232:235], v[200:203], v[96:99], v[232:235]\n\t" \
  "ds_read_b64_tr_b16 v[192:193], v28 offset:47104\n\t" \
  "s_waitcnt lgkmcnt(6)\n\t" \
  "v_mfma_f32_16x16x32_bf16 v[232:235], v[204:207], v[100:103], v[232:235]\n\t" \
  "ds_read_b64_tr_b16 v[194:195], v28 offset:47616\n\t" \
  "v_mfma_f32_16x16x32_bf16 v[236:239], v[204:207], v[108:111], v[236:239]\n\t" \
  "ds_read_b64_tr_b16 v[196:197], v28 offset:51200\n\t" \
  "global_load_dwordx4 v[148:151], v53, s[90:91] offset:1024 sc1\n\t" \
  "v_mfma_f32_16x16x32_bf16 v[236:239], v[200:203], v[104:107], v[236:239]\n\t" \
  "ds_read_b64_tr_b16 v[198:199], v28 offset:51712\n\t" \
  "v_mfma_f32_16x16x32_bf16 v[240:243], v[208:211], v[96:99], v[240:243]\n\t" \
  "ds_read_b64_tr_b16 v[200:201], v29 offset:47104\n\t" \
  "s_waitcnt lgkmcnt(6)\n\t" \
  "v_mfma_f32_16x16x32_bf16 v[240:243], v[212:215], v[100:103], v[240:243]\n\t" \
  "ds_read_b64_tr_b16 v[202:203], v29 offset:47616\n\t" \
  "v_mfma_f32_16x16x32_bf16 v[244:247], v[212:215], v[108:111], v[244:247]\n\t" \
  "ds_read_b64_tr_b16 v[204:205], v29 offset:51200\n\t" \
  "global_load_dwordx4 v[152:155], v53, s[90:91] offset:2048 sc1\n\t" \
  "v_mfma_f32_16x16x32_bf16 v[244:247], v[208:211], v[104:107], v[244:247]\n\t" \
  "ds_read_b64_tr_b16 v[206:207], v29 offset:51712\n\t" \
  "v_mfma_f32_16x16x32_bf16 v[248:251], v[216:219], v[96:99], v[248:251]\n\t" \
  "ds_read_b64_tr_b16 v[208:209], v30 offset:47104\n\t" \
  "s_waitcnt lgkmcnt(6)\n\t" \
  "v_mfma_f32_16x16x32_bf16 v[248:251], v[220:223], v[100:103], v[248:251]\n\t" \
  "ds_read_b64_tr_b16 v[210:211], v30 offset:47616\n\t" \
  "v_mfma_f32_16x16x32_bf16 v[252:255], v[220:223], v[108:111], v[252:255]\n\t" \
  "ds_read_b64_tr_b16 v[212:213], v30 offset:51200\n\t" \
  "global_load_dwordx4 v[156:159], v53, s[90:91] offset:3072 sc1\n\t" \
  "v_mfma_f32_16x16x32_bf16 v[252:255], v[216:219], v[104:107], v[252:255]\n\t" \
  "ds_read_b64_tr_b16 v[214:215], v30 offset:51712\n\t" \
  "s_waitcnt vmcnt(10)\n\t" \
  "v_mfma_f32_16x16x32_bf16 v[224:227], v[192:195], v[112:115], v[224:227]\n\t" \
  "ds_read_b64_tr_b16 v[216:217], v31 offset:47104\n\t" \
  "s_lshr_b64 s[90:91], %18, 24\n\t" \
  "s_and_b32 s90, s90, 15\n\t" \
  "s_lshl_b32 s90, s90, 15\n\t" \
  "s_waitcnt lgkmcnt(6)\n\t" \
  "v_mfma_f32_16x16x32_bf16 v[224:227], v[196:199], v[116:119], v[224:227]\n\t" \
  "ds_read_b64_tr_b16 v[218:219], v31 offset:47616\n\t" \
  "s_add_u32 s90, s90, s92\n\t" \
  "s_addc_u32 s91, s93, 0\n\t" \
  "s_waitcnt vmcnt(8)\n\t" \
  "v_mfma_f32_16x16x32_bf16 v[228:231], v[196:199], v[124:127], v[228:231]\n\t" \
  "ds_read_b64_tr_b16 v[220:221], v31 offset:51200\n\t" \
  "global_load_dwordx4 v[160:163], v53, s[90:91] offset:0 sc1\n\t" \
  "v_mfma_f32_16x16x32_bf16 v[228:231], v[192:195], v[120:123], v[228:231]\n\t" \
  "ds_read_b64_tr_b16 v[222:223], v31 offset:51712\n\t" \
  "v_mfma_f32_16x16x32_bf16 v[232:235], v[200:203], v[112:115], v[232:235]\n\t" \
  "ds_read_b64_tr_b16 v[192:193], v28 offset:55296\n\t" \
  "s_waitcnt lgkmcnt(6)\n\t" \
  "v_mfma_f32_16x16x32_bf16 v[232:235], v[204:207], v[116:119], v[232:235]\n\t" \
  "ds_read_b64_tr_b16 v[194:195], v28 offset:55808\n\t" \
  "v_mfma_f32_16x16x32_bf16 v[236:239], v[204:207], v[124:127], v[236:239]\n\t" \
  "ds_read_b64_tr_b16 v[196:197], v28 offset:59392\n\t" \
  "global_load_dwordx4 v[164:167], v53, s[90:91] offset:1024 sc1\n\t" \
  "ds_read_b32 v16, v56 offset:96\n\t" \
  "v_mfma_f32_16x16x32_bf16 v[236:239], v[200:203], v[120:123], v[236:239]\n\t" \
  "ds_read_b64_tr_b16 v[198:199], v28 offset:59904\n\t" \
  "v_mfma_f32_16x16x32_bf16 v[240:243], v[208:211], v[112:115], v[240:243]\n\t" \
  "ds_read_b64_tr_b16 v[200:201], v29 offset:55296\n\t" \
  "s_waitcnt lgkmcnt(7)\n\t" \
  "v_mfma_f32_16x16x32_bf16 v[240:243], v[212:215], v[116:119], v[240:243]\n\t" \
  "ds_read_b64_tr_b16 v[202:203], v29 offset:55808\n\t" \
  "v_mfma_f32_16x16x32_bf16 v[244:247], v[212:215], v[124:127], v[244:247]\n\t" \
  "ds_read_b64_tr_b16 v[204:205], v29 offset:59392\n\t" \
  "global_load_dwordx4 v[168:171], v53, s[90:91] offset:2048 sc1\n\t" \
  "v_mfma_f32_16x16x32_bf16 v[244:247], v[208:211], v[120:123], v[244:247]\n\t" \
  "ds_read_b64_tr_b16 v[206:207], v29 offset:59904\n\t" \
  "v_mfma_f32_16x16x32_bf16 v[248:251], v[216:219], v[112:115], v[248:251]\n\t" \
  "ds_read_b64_tr_b16 v[208:209], v30 offset:55296\n\t" \
  "s_waitcnt lgkmcnt(6)\n\t" \
  "v_mfma_f32_16x16x32_bf16 v[248:251], v[220:223], v[116:119], v[248:251]\n\t" \
  "ds_read_b64_tr_b16 v[210:211], v30 offset:55808\n\t" \
  "v_mfma_f32_16x16x32_bf16 v[252:255], v[220:223], v[124:127], v[252:255]\n\t" \
  "ds_read_b64_tr_b16 v[212:213], v30 offset:59392\n\t" \
  "global_load_dwordx4 v[172:175], v53, s[90:91] offset:3072 sc1\n\t" \
  "v_readfirstlane_b32 vcc_lo, v16\n\t" \
  "v_mfma_f32_16x16x32_bf16 v[252:255], v[216:219], v[120:123], v[252:255]\n\t" \
  "ds_read_b64_tr_b16 v[214:215], v30 offset:59904\n\t" \
  "s_cmp_ge_u32 vcc_lo, 2\n\t" \
  "s_cbranch_scc0 43f\n\t" \
  "44:\n\t" \
  "s_waitcnt vmcnt(10)\n\t" \
  "v_mfma_f32_16x16x32_bf16 v[224:227], v[192:195], v[128:131], v[224:227]\n\t" \
  "ds_read_b64_tr_b16 v[216:217], v31 offset:55296\n\t" \
  "s_lshr_b64 s[90:91], %18, 28\n\t" \
  "s_and_b32 s90, s90, 15\n\t" \
  "s_lshl_b32 s90, s90, 15\n\t" \
  "s_waitcnt lgkmcnt(6)\n\t" \
  "v_mfma_f32_16x16x32_bf16 v[224:227], v[196:199], v[132:135], v[224:227]\n\t" \
  "ds_read_b64_tr_b16 v[218:219], v31 offset:55808\n\t" \
  "s_add_u32 s90, s90, s92\n\t" \
  "s_addc_u32 s91, s93, 0\n\t" \
  "s_waitcnt vmcnt(8)\n\t" \
  "v_mfma_f32_16x16x32_bf16 v[228:231], v[196:199], v[140:143], v[228:231]\n\t" \
  "ds_read_b64_tr_b16 v[220:221], v31 offset:59392\n\t" \
  "global_load_dwordx4 v[176:179], v53, s[90:91] offset:0 sc1\n\t" \
  "v_mfma_f32_16x16x32_bf16 v[228:231], v[192:195], v[136:139], v[228:231]\n\t" \
  "ds_read_b64_tr_b16 v[222:223], v31 offset:59904\n\t" \
  "v_mfma_f32_16x16x32_bf16 v[232:235], v[200:203], v[128:131], v[232:235]\n\t" \
  "ds_read_b64_tr_b16 v[192:193], v28 offset:63488\n\t" \
  "s_waitcnt lgkmcnt(6)\n\t" \
  "v_mfma_f32_16x16x32_bf16 v[232:235], v[204:207], v[132:135], v[232:235]\n\t" \
  "ds_read_b64_tr_b16 v[194:195], v28 offset:64000\n\t" \
  "v_mfma_f32_16x16x32_bf16 v[236:239], v[204:207], v[140:143], v[236:239]\n\t" \
  "ds_read_b64_tr_b16 v[196:197], v32 offset:2048\n\t" \
  "global_load_dwordx4 v[180:183], v53, s[90:91] offset:1024 sc1\n\t" \
  "v_mfma_f32_16x16x32_bf16 v[236:239], v[200:203], v[136:139], v[236:239]\n\t" \
  "ds_read_b64_tr_b16 v[198:199], v32 offset:2560\n\t" \
  "v_mfma_f32_16x16x32_bf16 v[240:243], v[208:211], v[128:131], v[240:243]\n\t" \
  "ds_read_b64_tr_b16 v[200:201], v29 offset:63488\n\t" \
  "s_waitcnt lgkmcnt(6)\n\t" \
  "v_mfma_f32_16x16x32_bf16 v[240:243], v[212:215], v[132:135], v[240:243]\n\t" \
  "ds_read_b64_tr_b16 v[202:203], v29 offset:64000\n\t" \
  "v_mfma_f32_16x16x32_bf16 v[244:247], v[212:215], v[140:143], v[244:247]\n\t" \
  "ds_read_b64_tr_b16 v[204:205], v33 offset:2048\n\t" \
  "global_load_dwordx4 v[184:187], v53, s[90:91] offset:2048 sc1\n\t" \
  "v_mfma_f32_16x16x32_bf16 v[244:247], v[208:211], v[136:139], v[244:247]\n\t" \
  "ds_read_b64_tr_b16 v[206:207], v33 offset:2560\n\t" \
  "v_mfma_f32_16x16x32_bf16 v[248:251], v[216:219], v[128:131], v[248:251]\n\t" \
  "ds_read_b64_tr_b16 v[208:209], v30 offset:63488\n\t" \
  "s_waitcnt lgkmcnt(6)\n\t" \
  "v_mfma_f32_16x16x32_bf16 v[248:251], v[220:223], v[132:135], v[248:251]\n\t" \
  "ds_read_b64_tr_b16 v[210:211], v30 offset:64000\n\t" \
  "v_mfma_f32_16x16x32_bf16 v[252:255], v[220:223], v[140:143], v[252:255]\n\t" \
  "ds_read_b64_tr_b16 v[212:213], v34 offset:2048\n\t" \
  "global_load_dwordx4 v[188:191], v53, s[90:91] offset:3072 sc1\n\t" \
  "v_mfma_f32_16x16x32_bf16 v[252:255], v[216:219], v[136:139], v[252:255]\n\t" \
  "ds_read_b64_tr_b16 v[214:215], v34 offset:2560\n\t" \
  "s_waitcnt vmcnt(10)\n\t" \
  "v_mfma_f32_16x16x32_bf16 v[224:227], v[192:195], v[144:147], v[224:227]\n\t" \
  "ds_read_b64_tr_b16 v[216:217], v31 offset:63488\n\t" \
  "s_lshr_b64 s[90:91], %18, 32\n\t" \
  "s_and_b32 s90, s90, 15\n\t" \
  "s_lshl_b32 s90, s90, 15\n\t" \
  "s_waitcnt lgkmcnt(6)\n\t" \
  "v_mfma_f32_16x16x32_bf16 v[224:227], v[196:199], v[148:151], v[224:227]\n\t" \
  "ds_read_b64_tr_b16 v[218:219], v31 offset:64000\n\t" \
  "s_add_u32 s90, s90, s92\n\t" \
  "s_addc_u32 s91, s93, 0\n\t" \
  "s_waitcnt vmcnt(8)\n\t" \
  "v_mfma_f32_16x16x32_bf16 v[228:231], v[196:199], v[156:159], v[228:231]\n\t" \
  "ds_read_b64_tr_b16 v[220:221], v35 offset:2048\n\t" \
  "global_load_dwordx4 v[64:67], v53, s[90:91] offset:0 sc1\n\t" \
  "v_mfma_f32_16x16x32_bf16 v[228:231], v[192:195], v[152:155], v[228:231]\n\t" \
  "ds_read_b64_tr_b16 v[222:223], v35 offset:2560\n\t" \
  "v_mfma_f32_16x16x32_bf16 v[232:235], v[200:203], v[144:147], v[232:235]\n\t" \
  "ds_read_b64_tr_b16 v[192:193], v32 offset:6144\n\t" \
  "s_waitcnt lgkmcnt(6)\n\t" \
  "v_mfma_f32_16x16x32_bf16 v[232:235], v[204:207], v[148:151], v[232:235]\n\t" \
  "ds_read_b64_tr_b16 v[194:195], v32 offset:6656\n\t" \
  "v_mfma_f32_16x16x32_bf16 v[236:239], v[204:207], v[156:159], v[236:239]\n\t" \
  "ds_read_b64_tr_b16 v[196:197], v32 offset:10240\n\t" \
  "global_load_dwordx4 v[68:71], v53, s[90:91] offset:1024 sc1\n\t" \
  "v_mfma_f32_16x16x32_bf16 v[236:239], v[200:203], v[152:155], v[236:239]\n\t" \
  "ds_read_b64_tr_b16 v[198:199], v32 offset:10752\n\t" \
  "v_mfma_f32_16x16x32_bf16 v[240:243], v[208:211], v[144:147], v[240:243]\n\t" \
  "ds_read_b64_tr_b16 v[200:201], v33 offset:6144\n\t" \
  "s_waitcnt lgkmcnt(6)\n\t" \
  "v_mfma_f32_16x16x32_bf16 v[240:243], v[212:215], v[148:151], v[240:243]\n\t" \
  "ds_read_b128 v[40:43], v56 offset:80\n\t" \
  "ds_read_b64_tr_b16 v[202:203], v33 offset:6656\n\t" \
  "v_mfma_f32_16x16x32_bf16 v[244:247], v[212:215], v[156:159], v[244:247]\n\t" \
  "ds_read_b64_tr_b16 v[204:205], v33 offset:10240\n\t" \
  "global_load_dwordx4 v[72:75], v53, s[90:91] offset:2048 sc1\n\t" \
  "v_mfma_f32_16x16x32_bf16 v[244:247], v[208:211], v[152:155], v[244:247]\n\t" \
  "ds_read_b64_tr_b16 v[206:207], v33 offset:10752\n\t" \
  "v_mfma_f32_16x16x32_bf16 v[248:251], v[216:219], v[144:147], v[248:251]\n\t" \
  "ds_read_b64_tr_b16 v[208:209], v34 offset:6144\n\t" \
  "s_waitcnt lgkmcnt(7)\n\t" \
  "v_mfma_f32_16x16x32_bf16 v[248:251], v[220:223], v[148:151], v[248:251]\n\t" \
  "ds_read_b64_tr_b16 v[210:211], v34 offset:6656\n\t" \
  "v_mfma_f32_16x16x32_bf16 v[252:255], v[220:223], v[156:159], v[252:255]\n\t" \
  "ds_read_b64_tr_b16 v[212:213], v34 offset:10240\n\t" \
  "global_load_dwordx4 v[76:79], v53, s[90:91] offset:3072 sc1\n\t" \
  "v_mfma_f32_16x16x32_bf16 v[252:255], v[216:219], v[152:155], v[252:255]\n\t" \
  "ds_read_b64_tr_b16 v[214:215], v34 offset:10752\n\t" \
  "s_waitcnt vmcnt(10)\n\t" \
  "v_mfma_f32_16x16x32_bf16 v[224:227], v[192:195], v[160:163], v[224:227]\n\t" \
  "s_waitcnt lgkmcnt(7)\n\t" \
  "v_min_u32 v40, v40, v41\n\t" \
  "v_min3_u32 v40, v40, v42, v43\n\t" \
  "ds_read_b64_tr_b16 v[216:217], v35 offset:6144\n\t" \
  "s_lshr_b64 s[90:91], %18, 36\n\t" \
  "s_and_b32 s90, s90, 15\n\t" \
  "s_lshl_b32 s90, s90, 15\n\t" \
  "v_mfma_f32_16x16x32_bf16 v[224:227], v[196:199], v[164:167], v[224:227]\n\t" \
  "v_readfirstlane_b32 vcc_lo, v40\n\t" \
  "ds_read_b64_tr_b16 v[218:219], v35 offset:6656\n\t" \
  "s_add_u32 s90, s90, s92\n\t" \
  "s_addc_u32 s91, s93, 0\n\t" \
  "s_waitcnt vmcnt(8)\n\t" \
  "v_mfma_f32_16x16x32_bf16 v[228:231], v[196:199], v[172:175], v[228:231]\n\t" \
  "s_cmp_ge_u32 vcc_lo, 13\n\t" \
  "s_cbranch_scc0 32f\n\t" \
  "33:\n\t" \
  "ds_read_b64_tr_b16 v[220:221], v35 offset:10240\n\t" \
  "global_load_dwordx4 v[80:83], v53, s[90:91] offset:0 sc1\n\t" \
  "v_mfma_f32_16x16x32_bf16 v[228:231], v[192:195], v[168:171], v[228:231]\n\t" \
  "ds_read_b64_tr_b16 v[222:223], v35 offset:10752\n\t" \
  "s_waitcnt lgkmcnt(6)\n\t" \
  "v_mfma_f32_16x16x32_bf16 v[232:235], v[200:203], v[160:163], v[232:235]\n\t" \
  "ds_read_b64_tr_b16 v[192:193], v32 offset:14336\n\t" \
  "v_mfma_f32_16x16x32_bf16 v[232:235], v[204:207], v[164:167], v[232:235]\n\t" \
  "ds_read_b64_tr_b16 v[194:195], v32 offset:14848\n\t" \
  "v_mfma_f32_16x16x32_bf16 v[236:239], v[204:207], v[172:175], v[236:239]\n\t" \
  "ds_read_b64_tr_b16 v[196:197], v32 offset:18432\n\t" \
  "global_load_dwordx4 v[84:87], v53, s[90:91] offset:1024 sc1\n\t" \
  "v_mfma_f32_16x16x32_bf16 v[236:239], v[200:203], v[168:171], v[236:239]\n\t" \
  "ds_read_b64_tr_b16 v[198:199], v32 offset:18944\n\t" \
  "v_mfma_f32_16x16x32_bf16 v[240:243], v[208:211], v[160:163], v[240:243]\n\t" \
  "ds_read_b64_tr_b16 v[200:201], v33 offset:14336\n\t" \
  "s_waitcnt lgkmcnt(6)\n\t" \
  "v_mfma_f32_16x16x32_bf16 v[240:243], v[212:215], v[164:167], v[240:243]\n\t" \
  "ds_read_b64_tr_b16 v[202:203], v33 offset:14848\n\t" \
  "v_mfma_f32_16x16x32_bf16 v[244:247], v[212:215], v[172:175], v[244:247]\n\t" \
  "ds_read_b64_tr_b16 v[204:205], v33 offset:18432\n\t" \
  "global_load_dwordx4 v[88:91], v53, s[90:91] offset:2048 sc1\n\t" \
  "v_mfma_f32_16x16x32_bf16 v[244:247], v[208:211], v[168:171], v[244:247]\n\t" \
  "ds_read_b64_tr_b16 v[206:207], v33 offset:18944\n\t" \
  "v_mfma_f32_16x16x32_bf16 v[248:251], v[216:219], v[160:163], v[248:251]\n\t" \
  "ds_read_b64_tr_b16 v[208:209], v34 offset:14336\n\t" \
  "s_waitcnt lgkmcnt(6)\n\t" \
  "v_mfma_f32_16x16x32_bf16 v[248:251], v[220:223], v[164:167], v[248:251]\n\t" \
  "ds_read_b64_tr_b16 v[210:211], v34 offset:14848\n\t" \
  "v_mfma_f32_16x16x32_bf16 v[252:255], v[220:223], v[172:175], v[252:255]\n\t" \
  "ds_read_b64_tr_b16 v[212:213], v34 offset:18432\n\t" \
  "global_load_dwordx4 v[92:95], v53, s[90:91] offset:3072 sc1\n\t" \
  "v_mfma_f32_16x16x32_bf16 v[252:255], v[216:219], v[168:171], v[252:255]\n\t" \
  "ds_read_b64_tr_b16 v[214:215], v34 offset:18944\n\t" \
  "s_waitcnt vmcnt(10)\n\t" \
  "v_mfma_f32_16x16x32_bf16 v[224:227], v[192:195], v[176:179], v[224:227]\n\t" \
  "ds_read_b64_tr_b16 v[216:217], v35 offset:14336\n\t" \
  "s_lshr_b64 s[90:91], %18, 40\n\t" \
  "s_and_b32 s90, s90, 15\n\t" \
  "s_lshl_b32 s90, s90, 15\n\t" \
  "s_waitcnt lgkmcnt(6)\n\t" \
  "v_mfma_f32_16x16x32_bf16 v[224:227], v[196:199], v[180:183], v[224:227]\n\t" \
  "ds_read_b64_tr_b16 v[218:219], v35 offset:14848\n\t" \
  "s_add_u32 s90, s90, s92\n\t" \
  "s_addc_u32 s91, s93, 0\n\t" \
  "s_waitcnt vmcnt(8)\n\t" \
  "v_mfma_f32_16x16x32_bf16 v[228:231], v[196:199], v[188:191], v[228:231]\n\t" \
  "ds_read_b64_tr_b16 v[220:221], v35 offset:18432\n\t" \
  "global_load_dwordx4 v[96:99], v53, s[90:91] offset:0 sc1\n\t" \
  "v_mfma_f32_16x16x32_bf16 v[228:231], v[192:195], v[184:187], v[228:231]\n\t" \
  "ds_read_b64_tr_b16 v[222:223], v35 offset:18944\n\t" \
  "v_mfma_f32_16x16x32_bf16 v[232:235], v[200:203], v[176:179], v[232:235]\n\t" \
  "ds_read_b64_tr_b16 v[192:193], v32 offset:22528\n\t" \
  "s_waitcnt lgkmcnt(6)\n\t" \
  "v_mfma_f32_16x16x32_bf16 v[232:235], v[204:207], v[180:183], v[232:235]\n\t" \
  "ds_read_b64_tr_b16 v[194:195], v32 offset:23040\n\t" \
  "v_mfma_f32_16x16x32_bf16 v[236:239], v[204:207], v[188:191], v[236:239]\n\t" \
  "ds_read_b64_tr_b16 v[196:197], v32 offset:26624\n\t" \
  "global_load_dwordx4 v[100:103], v53, s[90:91] offset:1024 sc1\n\t" \
  "v_mfma_f32_16x16x32_bf16 v[236:239], v[200:203], v[184:187], v[236:239]\n\t" \
  "ds_read_b64_tr_b16 v[198:199], v32 offset:27136\n\t" \
  "v_mfma_f32_16x16x32_bf16 v[240:243], v[208:211], v[176:179], v[240:243]\n\t" \
  "ds_read_b64_tr_b16 v[200:201], v33 offset:22528\n\t" \
  "s_waitcnt lgkmcnt(6)\n\t" \
  "v_mfma_f32_16x16x32_bf16 v[240:243], v[212:215], v[180:183], v[240:243]\n\t" \
  "ds_read_b64_tr_b16 v[202:203], v33 offset:23040\n\t" \
  "v_mfma_f32_16x16x32_bf16 v[244:247], v[212:215], v[188:191], v[244:247]\n\t" \
  "ds_read_b64_tr_b16 v[204:205], v33 offset:26624\n\t" \
  "global_load_dwordx4 v[104:107], v53, s[90:91] offset:2048 sc1\n\t" \
  "v_mfma_f32_16x16x32_bf16 v[244:247], v[208:211], v[184:187], v[244:247]\n\t" \
  "ds_read_b64_tr_b16 v[206:207], v33 offset:27136\n\t" \
  "v_mfma_f32_16x16x32_bf16 v[248:251], v[216:219], v[176:179], v[248:251]\n\t" \
  "ds_read_b64_tr_b16 v[208:209], v34 offset:22528\n\t" \
  "s_waitcnt lgkmcnt(6)\n\t" \
  "v_mfma_f32_16x16x32_bf16 v[248:251], v[220:223], v[180:183], v[248:251]\n\t" \
  "ds_read_b64_tr_b16 v[210:211], v34 offset:23040\n\t" \
  "v_mfma_f32_16x16x32_bf16 v[252:255], v[220:223], v[188:191], v[252:255]\n\t" \
  "ds_read_b64_tr_b16 v[212:213], v34 offset:26624\n\t" \
  "global_load_dwordx4 v[108:111], v53, s[90:91] offset:3072 sc1\n\t" \
  "ds_write_b32 v56, v56 offset:108\n\t" \
  "v_mfma_f32_16x16x32_bf16 v[252:255], v[216:219], v[184:187], v[252:255]\n\t" \
  "ds_read_b64_tr_b16 v[214:215], v34 offset:27136\n\t" \
  "s_waitcnt vmcnt(10)\n\t" \
  "v_mfma_f32_16x16x32_bf16 v[224:227], v[192:195], v[64:67], v[224:227]\n\t" \
  "ds_read_b64_tr_b16 v[216:217], v35 offset:22528\n\t" \
  "s_lshr_b64 s[90:91], %18, 44\n\t" \
  "s_and_b32 s90, s90, 15\n\t" \
  "s_lshl_b32 s90, s90, 15\n\t" \
  "s_waitcnt lgkmcnt(7)\n\t" \
  "v_mfma_f32_16x16x32_bf16 v[224:227], v[196:199], v[68:71], v[224:227]\n\t" \
  "ds_read_b64_tr_b16 v[218:219], v35 offset:23040\n\t" \
  "s_add_u32 s90, s90, s92\n\t" \
  "s_addc_u32 s91, s93, 0\n\t" \
  "s_waitcnt vmcnt(8)\n\t" \
  "v_mfma_f32_16x16x32_bf16 v[228:231], v[196:199], v[76:79], v[228:231]\n\t" \
  "ds_read_b64_tr_b16 v[220:221], v35 offset:26624\n\t" \
  "global_load_dwordx4 v[112:115], v53, s[90:91] offset:0 sc1\n\t" \
  "v_mfma_f32_16x16x32_bf16 v[228:231], v[192:195], v[72:75], v[228:231]\n\t" \
  "ds_read_b64_tr_b16 v[222:223], v35 offset:27136\n\t" \
  "v_mfma_f32_16x16x32_bf16 v[232:235], v[200:203], v[64:67], v[232:235]\n\t" \
  "ds_read_b64_tr_b16 v[192:193], v32 offset:30720\n\t" \
  "s_waitcnt lgkmcnt(6)\n\t" \
  "v_mfma_f32_16x16x32_bf16 v[232:235], v[204:207], v[68:71], v[232:235]\n\t" \
  "ds_read_b64_tr_b16 v[194:195], v32 offset:31232\n\t" \
  "v_mfma_f32_16x16x32_bf16 v[236:239], v[204:207], v[76:79], v[236:239]\n\t" \
  "ds_read_b64_tr_b16 v[196:197], v32 offset:34816\n\t" \
  "global_load_dwordx4 v[116:119], v53, s[90:91] offset:1024 sc1\n\t" \
  "v_mfma_f32_16x16x32_bf16 v[236:239], v[200:203], v[72:75], v[236:239]\n\t" \
  "ds_read_b64_tr_b16 v[198:199], v32 offset:35328\n\t" \
  "v_mfma_f32_16x16x32_bf16 v[240:243], v[208:211], v[64:67], v[240:243]\n\t" \
  "ds_read_b64_tr_b16 v[200:201], v33 offset:30720\n\t" \
  "s_waitcnt lgkmcnt(6)\n\t" \
  "v_mfma_f32_16x16x32_bf16 v[240:243], v[212:215], v[68:71], v[240:243]\n\t" \
  "ds_read_b64_tr_b16 v[202:203], v33 offset:31232\n\t" \
  "v_mfma_f32_16x16x32_bf16 v[244:247], v[212:215], v[76:79], v[244:247]\n\t" \
  "ds_read_b64_tr_b16 v[204:205], v33 offset:34816\n\t" \
  "global_load_dwordx4 v[120:123], v53, s[90:91] offset:2048 sc1\n\t" \
  "v_mfma_f32_16x16x32_bf16 v[244:247], v[208:211], v[72:75], v[244:247]\n\t" \
  "ds_read_b64_tr_b16 v[206:207], v33 offset:35328\n\t" \
  "v_mfma_f32_16x16x32_bf16 v[248:251], v[216:219], v[64:67], v[248:251]\n\t" \
  "ds_read_b64_tr_b16 v[208:209], v34 offset:30720\n\t" \
  "s_waitcnt lgkmcnt(6)\n\t" \
  "v_mfma_f32_16x16x32_bf16 v[248:251], v[220:223], v[68:71], v[248:251]\n\t" \
  "ds_read_b64_tr_b16 v[210:211], v34 offset:31232\n\t" \
  "v_mfma_f32_16x16x32_bf16 v[252:255], v[220:223], v[76:79], v[252:255]\n\t" \
  "ds_read_b64_tr_b16 v[212:213], v34 offset:34816\n\t" \
  "global_load_dwordx4 v[124:127], v53, s[90:91] offset:3072 sc1\n\t" \
  "v_mfma_f32_16x16x32_bf16 v[252:255], v[216:219], v[72:75], v[252:255]\n\t" \
  "ds_read_b64_tr_b16 v[214:215], v34 offset:35328\n\t" \
  "s_waitcnt vmcnt(10)\n\t" \
  "v_mfma_f32_16x16x32_bf16 v[224:227], v[192:195], v[80:83], v[224:227]\n\t" \
  "ds_read_b64_tr_b16 v[216:217], v35 offset:30720\n\t" \
  "s_lshr_b64 s[90:91], %18, 48\n\t" \
  "s_and_b32 s90, s90, 15\n\t" \
  "s_lshl_b32 s90, s90, 15\n\t" \
  "s_waitcnt lgkmcnt(6)\n\t" \
  "v_mfma_f32_16x16x32_bf16 v[224:227], v[196:199], v[84:87], v[224:227]\n\t" \
  "ds_read_b64_tr_b16 v[218:219], v35 offset:31232\n\t" \
  "s_add_u32 s90, s90, s92\n\t" \
  "s_addc_u32 s91, s93, 0\n\t" \
  "s_waitcnt vmcnt(8)\n\t" \
  "v_mfma_f32_16x16x32_bf16 v[228:231], v[196:199], v[92:95], v[228:231]\n\t" \
  "ds_read_b64_tr_b16 v[220:221], v35 offset:34816\n\t" \
  "global_load_dwordx4 v[128:131], v53, s[90:91] offset:0 sc1\n\t" \
  "v_mfma_f32_16x16x32_bf16 v[228:231], v[192:195], v[88:91], v[228:231]\n\t" \
  "ds_read_b64_tr_b16 v[222:223], v35 offset:35328\n\t" \
  "v_mfma_f32_16x16x32_bf16 v[232:235], v[200:203], v[80:83], v[232:235]\n\t" \
  "ds_read_b64_tr_b16 v[192:193], v32 offset:38912\n\t" \
  "s_waitcnt lgkmcnt(6)\n\t" \
  "v_mfma_f32_16x16x32_bf16 v[232:235], v[204:207], v[84:87], v[232:235]\n\t" \
  "ds_read_b64_tr_b16 v[194:195], v32 offset:39424\n\t" \
  "v_mfma_f32_16x16x32_bf16 v[236:239], v[204:207], v[92:95], v[236:239]\n\t" \
  "ds_read_b64_tr_b16 v[196:197], v32 offset:43008\n\t" \
  "global_load_dwordx4 v[132:135], v53, s[90:91] offset:1024 sc1\n\t" \
  "v_mfma_f32_16x16x32_bf16 v[236:239], v[200:203], v[88:91], v[236:239]\n\t" \
  "ds_read_b64_tr_b16 v[198:199], v32 offset:43520\n\t" \
  "v_mfma_f32_16x16x32_bf16 v[240:243], v[208:211], v[80:83], v[240:243]\n\t" \
  "ds_read_b64_tr_b16 v[200:201], v33 offset:38912\n\t" \
  "s_waitcnt lgkmcnt(6)\n\t" \
  "v_mfma_f32_16x16x32_bf16 v[240:243], v[212:215], v[84:87], v[240:243]\n\t" \
  "ds_read_b64_tr_b16 v[202:203], v33 offset:39424\n\t" \
  "v_mfma_f32_16x16x32_bf16 v[244:247], v[212:215], v[92:95], v[244:247]\n\t" \
  "ds_read_b64_tr_b16 v[204:205], v33 offset:43008\n\t" \
  "global_load_dwordx4 v[136:139], v53, s[90:91] offset:2048 sc1\n\t" \
  "v_mfma_f32_16x16x32_bf16 v[244:247], v[208:211], v[88:91], v[244:247]\n\t" \
  "ds_read_b64_tr_b16 v[206:207], v33 offset:43520\n\t" \
  "v_mfma_f32_16x16x32_bf16 v[248:251], v[216:219], v[80:83], v[248:251]\n\t" \
  "ds_read_b64_tr_b16 v[208:209], v34 offset:38912\n\t" \
  "s_waitcnt lgkmcnt(6)\n\t" \
  "v_mfma_f32_16x16x32_bf16 v[248:251], v[220:223], v[84:87], v[248:251]\n\t" \
  "ds_read_b64_tr_b16 v[210:211], v34 offset:39424\n\t" \
  "v_mfma_f32_16x16x32_bf16 v[252:255], v[220:223], v[92:95], v[252:255]\n\t" \
  "ds_read_b64_tr_b16 v[212:213], v34 offset:43008\n\t" \
  "global_load_dwordx4 v[140:143], v53, s[90:91] offset:3072 sc1\n\t" \
  "v_mfma_f32_16x16x32_bf16 v[252:255], v[216:219], v[88:91], v[252:255]\n\t" \
  "ds_read_b64_tr_b16 v[214:215], v34 offset:43520\n\t" \
  "s_waitcnt vmcnt(10)\n\t" \
  "v_mfma_f32_16x16x32_bf16 v[224:227], v[192:195], v[96:99], v[224:227]\n\t" \
  "ds_read_b64_tr_b16 v[216:217], v35 offset:38912\n\t" \
  "509:\n\t" \
  "s_cbranch_execz 26b\n\t" \
  "s_lshr_b64 s[90:91], %18, 52\n\t" \
  "s_and_b32 s90, s90, 15\n\t" \
  "s_lshl_b32 s90, s90, 15\n\t" \
  "s_waitcnt lgkmcnt(6)\n\t" \
  "v_mfma_f32_16x16x32_bf16 v[224:227], v[196:199], v[100:103], v[224:227]\n\t" \
  "ds_read_b64_tr_b16 v[218:219], v35 offset:39424\n\t" \
  "s_add_u32 s90, s90, s92\n\t" \
  "s_addc_u32 s91, s93, 0\n\t" \
  "s_waitcnt vmcnt(8)\n\t" \
  "v_mfma_f32_16x16x32_bf16 v[228:231], v[196:199], v[108:111], v[228:231]\n\t" \
  "ds_read_b64_tr_b16 v[220:221], v35 offset:43008\n\t" \
  "global_load_dwordx4 v[144:147], v53, s[90:91] offset:0 sc1\n\t" \
  "v_mfma_f32_16x16x32_bf16 v[228:231], v[192:195], v[104:107], v[228:231]\n\t" \
  "ds_read_b64_tr_b16 v[222:223], v35 offset:43520\n\t" \
  "v_mfma_f32_16x16x32_bf16 v[232:235], v[200:203], v[96:99], v[232:235]\n\t" \
  "ds_read_b64_tr_b16 v[192:193], v32 offset:47104\n\t" \
  "s_waitcnt lgkmcnt(6)\n\t" \
  "v_mfma_f32_16x16x32_bf16 v[232:235], v[204:207], v[100:103], v[232:235]\n\t" \
  "ds_read_b64_tr_b16 v[194:195], v32 offset:47616\n\t" \
  "v_mfma_f32_16x16x32_bf16 v[236:239], v[204:207], v[108:111], v[236:239]\n\t" \
  "ds_read_b64_tr_b16 v[196:197], v32 offset:51200\n\t" \
  "global_load_dwordx4 v[148:151], v53, s[90:91] offset:1024 sc1\n\t" \
  "v_mfma_f32_16x16x32_bf16 v[236:239], v[200:203], v[104:107], v[236:239]\n\t" \
  "ds_read_b64_tr_b16 v[198:199], v32 offset:51712\n\t" \
  "v_mfma_f32_16x16x32_bf16 v[240:243], v[208:211], v[96:99], v[240:243]\n\t" \
  "ds_read_b64_tr_b16 v[200:201], v33 offset:47104\n\t" \
  "s_waitcnt lgkmcnt(6)\n\t" \
  "v_mfma_f32_16x16x32_bf16 v[240:243], v[212:215], v[100:103], v[240:243]\n\t" \
  "ds_read_b64_tr_b16 v[202:203], v33 offset:47616\n\t" \
  "v_mfma_f32_16x16x32_bf16 v[244:247], v[212:215], v[108:111], v[244:247]\n\t" \
  "ds_read_b64_tr_b16 v[204:205], v33 offset:51200\n\t" \
  "global_load_dwordx4 v[152:155], v53, s[90:91] offset:2048 sc1\n\t" \
  "v_mfma_f32_16x16x32_bf16 v[244:247], v[208:211], v[104:107], v[244:247]\n\t" \
  "ds_read_b64_tr_b16 v[206:207], v33 offset:51712\n\t" \
  "v_mfma_f32_16x16x32_bf16 v[248:251], v[216:219], v[96:99], v[248:251]\n\t" \
  "ds_read_b64_tr_b16 v[208:209], v34 offset:47104\n\t" \
  "s_waitcnt lgkmcnt(6)\n\t" \
  "v_mfma_f32_16x16x32_bf16 v[248:251], v[220:223], v[100:103], v[248:251]\n\t" \
  "ds_read_b64_tr_b16 v[210:211], v34 offset:47616\n\t" \
  "v_mfma_f32_16x16x32_bf16 v[252:255], v[220:223], v[108:111], v[252:255]\n\t" \
  "ds_read_b64_tr_b16 v[212:213], v34 offset:51200\n\t" \
  "global_load_dwordx4 v[156:159], v53, s[90:91] offset:3072 sc1\n\t" \
  "v_mfma_f32_16x16x32_bf16 v[252:255], v[216:219], v[104:107], v[252:255]\n\t" \
  "ds_read_b64_tr_b16 v[214:215], v34 offset:51712\n\t" \
  "s_waitcnt vmcnt(10)\n\t" \
  "v_mfma_f32_16x16x32_bf16 v[224:227], v[192:195], v[112:115], v[224:227]\n\t" \
  "ds_read_b64_tr_b16 v[216:217], v35 offset:47104\n\t" \
  "s_lshr_b64 s[90:91], %18, 56\n\t" \
  "s_and_b32 s90, s90, 15\n\t" \
  "s_lshl_b32 s90, s90, 15\n\t" \
  "s_waitcnt lgkmcnt(6)\n\t" \
  "v_mfma_f32_16x16x32_bf16 v[224:227], v[196:199], v[116:119], v[224:227]\n\t" \
  "ds_read_b64_tr_b16 v[218:219], v35 offset:47616\n\t" \
  "s_add_u32 s90, s90, s92\n\t" \
  "s_addc_u32 s91, s93, 0\n\t" \
  "s_waitcnt vmcnt(8)\n\t" \
  "v_mfma_f32_16x16x32_bf16 v[228:231], v[196:199], v[124:127], v[228:231]\n\t" \
  "ds_read_b64_tr_b16 v[220:221], v35 offset:51200\n\t" \
  "global_load_dwordx4 v[160:163], v53, s[90:91] offset:0 sc1\n\t" \
  "v_mfma_f32_16x16x32_bf16 v[228:231], v[192:195], v[120:123], v[228:231]\n\t" \
  "ds_read_b64_tr_b16 v[222:223], v35 offset:51712\n\t" \
  "v_mfma_f32_16x16x32_bf16 v[232:235], v[200:203], v[112:115], v[232:235]\n\t" \
  "ds_read_b64_tr_b16 v[192:193], v32 offset:55296\n\t" \
  "s_waitcnt lgkmcnt(6)\n\t" \
  "v_mfma_f32_16x16x32_bf16 v[232:235], v[204:207], v[116:119], v[232:235]\n\t" \
  "ds_read_b64_tr_b16 v[194:195], v32 offset:55808\n\t" \
  "v_mfma_f32_16x16x32_bf16 v[236:239], v[204:207], v[124:127], v[236:239]\n\t" \
  "ds_read_b64_tr_b16 v[196:197], v32 offset:59392\n\t" \
  "global_load_dwordx4 v[164:167], v53, s[90:91] offset:1024 sc1\n\t" \
  "v_mfma_f32_16x16x32_bf16 v[236:239], v[200:203], v[120:123], v[236:239]\n\t" \
  "ds_read_b64_tr_b16 v[198:199], v32 offset:59904\n\t" \
  "v_mfma_f32_16x16x32_bf16 v[240:243], v[208:211], v[112:115], v[240:243]\n\t" \
  "ds_read_b64_tr_b16 v[200:201], v33 offset:55296\n\t" \
  "s_waitcnt lgkmcnt(6)\n\t" \
  "v_mfma_f32_16x16x32_bf16 v[240:243], v[212:215], v[116:119], v[240:243]\n\t" \
  "ds_read_b128 v[40:43], v56 offset:80\n\t" \
  "ds_read_b64_tr_b16 v[202:203], v33 offset:55808\n\t" \
  "v_mfma_f32_16x16x32_bf16 v[244:247], v[212:215], v[124:127], v[244:247]\n\t" \
  "ds_read_b64_tr_b16 v[204:205], v33 offset:59392\n\t" \
  "global_load_dwordx4 v[168:171], v53, s[90:91] offset:2048 sc1\n\t" \
  "v_mfma_f32_16x16x32_bf16 v[244:247], v[208:211], v[120:123], v[244:247]\n\t" \
  "ds_read_b64_tr_b16 v[206:207], v33 offset:59904\n\t" \
  "v_mfma_f32_16x16x32_bf16 v[248:251], v[216:219], v[112:115], v[248:251]\n\t" \
  "ds_read_b64_tr_b16 v[208:209], v34 offset:55296\n\t" \
  "s_waitcnt lgkmcnt(7)\n\t" \
  "v_mfma_f32_16x16x32_bf16 v[248:251], v[220:223], v[116:119], v[248:251]\n\t" \
  "ds_read_b64_tr_b16 v[210:211], v34 offset:55808\n\t" \
  "v_mfma_f32_16x16x32_bf16 v[252:255], v[220:223], v[124:127], v[252:255]\n\t" \
  "ds_read_b64_tr_b16 v[212:213], v34 offset:59392\n\t" \
  "global_load_dwordx4 v[172:175], v53, s[90:91] offset:3072 sc1\n\t" \
  "v_mfma_f32_16x16x32_bf16 v[252:255], v[216:219], v[120:123], v[252:255]\n\t" \
  "ds_read_b64_tr_b16 v[214:215], v34 offset:59904\n\t" \
  "s_waitcnt vmcnt(10)\n\t" \
  "v_mfma_f32_16x16x32_bf16 v[224:227], v[192:195], v[128:131], v[224:227]\n\t" \
  "s_waitcnt lgkmcnt(7)\n\t" \
  "v_min_u32 v40, v40, v41\n\t" \
  "v_min3_u32 v40, v40, v42, v43\n\t" \
  "ds_read_b64_tr_b16 v[216:217], v35 offset:55296\n\t" \
  "s_lshr_b64 s[90:91], %18, 60\n\t" \
  "s_and_b32 s90, s90, 15\n\t" \
  "s_lshl_b32 s90, s90, 15\n\t" \
  "v_mfma_f32_16x16x32_bf16 v[224:227], v[196:199], v[132:135], v[224:227]\n\t" \
  "v_readfirstlane_b32 vcc_lo, v40\n\t" \
  "ds_read_b64_tr_b16 v[218:219], v35 offset:55808\n\t" \
  "s_add_u32 s90, s90, s92\n\t" \
  "s_addc_u32 s91, s93, 0\n\t" \
  "s_waitcnt vmcnt(8)\n\t" \
  "v_mfma_f32_16x16x32_bf16 v[228:231], v[196:199], v[140:143], v[228:231]\n\t" \
  "s_cmp_ge_u32 vcc_lo, 16\n\t" \
  "s_cbranch_scc0 34f\n\t" \
  "35:\n\t" \
  "ds_read_b64_tr_b16 v[220:221], v35 offset:59392\n\t" \
  "global_load_dwordx4 v[176:179], v53, s[90:91] offset:0 sc1\n\t" \
  "v_mfma_f32_16x16x32_bf16 v[228:231], v[192:195], v[136:139], v[228:231]\n\t" \
  "ds_read_b64_tr_b16 v[222:223], v35 offset:59904\n\t" \
  "s_waitcnt lgkmcnt(6)\n\t" \
  "v_mfma_f32_16x16x32_bf16 v[232:235], v[200:203], v[128:131], v[232:235]\n\t" \
  "ds_read_b64_tr_b16 v[192:193], v36 offset:2304\n\t" \
  "v_mfma_f32_16x16x32_bf16 v[232:235], v[204:207], v[132:135], v[232:235]\n\t" \
  "ds_read_b64_tr_b16 v[194:195], v36 offset:2816\n\t" \
  "v_mfma_f32_16x16x32_bf16 v[236:239], v[204:207], v[140:143], v[236:239]\n\t" \
  "ds_read_b64_tr_b16 v[196:197], v36 offset:10752\n\t" \
  "global_load_dwordx4 v[180:183], v53, s[90:91] offset:1024 sc1\n\t" \
  "v_mfma_f32_16x16x32_bf16 v[236:239], v[200:203], v[136:139], v[236:239]\n\t" \
  "ds_read_b64_tr_b16 v[198:199], v36 offset:11264\n\t" \
  "v_mfma_f32_16x16x32_bf16 v[240:243], v[208:211], v[128:131], v[240:243]\n\t" \
  "ds_read_b64_tr_b16 v[200:201], v37 offset:2304\n\t" \
  "s_waitcnt lgkmcnt(6)\n\t" \
  "v_mfma_f32_16x16x32_bf16 v[240:243], v[212:215], v[132:135], v[240:243]\n\t" \
  "ds_read_b64_tr_b16 v[202:203], v37 offset:2816\n\t" \
  "v_mfma_f32_16x16x32_bf16 v[244:247], v[212:215], v[140:143], v[244:247]\n\t" \
  "ds_read_b64_tr_b16 v[204:205], v37 offset:10752\n\t" \
  "global_load_dwordx4 v[184:187], v53, s[90:91] offset:2048 sc1\n\t" \
  "v_mfma_f32_16x16x32_bf16 v[244:247], v[208:211], v[136:139], v[244:247]\n\t" \
  "ds_read_b64_tr_b16 v[206:207], v37 offset:11264\n\t" \
  "v_mfma_f32_16x16x32_bf16 v[248:251], v[216:219], v[128:131], v[248:251]\n\t" \
  "ds_read_b64_tr_b16 v[208:209], v38 offset:2304\n\t" \
  "s_waitcnt lgkmcnt(6)\n\t" \
  "v_mfma_f32_16x16x32_bf16 v[248:251], v[220:223], v[132:135], v[248:251]\n\t" \
  "ds_read_b64_tr_b16 v[210:211], v38 offset:2816\n\t" \
  "v_mfma_f32_16x16x32_bf16 v[252:255], v[220:223], v[140:143], v[252:255]\n\t" \
  "ds_read_b64_tr_b16 v[212:213], v38 offset:10752\n\t" \
  "global_load_dwordx4 v[188:191], v53, s[90:91] offset:3072 sc1\n\t" \
  "v_mfma_f32_16x16x32_bf16 v[252:255], v[216:219], v[136:139], v[252:255]\n\t" \
  "ds_read_b64_tr_b16 v[214:215], v38 offset:11264\n\t" \
  "s_waitcnt vmcnt(10)\n\t" \
  "v_mfma_f32_16x16x32_bf16 v[224:227], v[192:195], v[144:147], v[224:227]\n\t" \
  "ds_read_b64_tr_b16 v[216:217], v39 offset:2304\n\t" \
  "s_waitcnt lgkmcnt(6)\n\t" \
  "v_mfma_f32_16x16x32_bf16 v[224:227], v[196:199], v[148:151], v[224:227]\n\t" \
  "ds_read_b64_tr_b16 v[218:219], v39 offset:2816\n\t" \
  "s_waitcnt vmcnt(8)\n\t" \
  "v_mfma_f32_16x16x32_bf16 v[228:231], v[196:199], v[156:159], v[228:231]\n\t" \
  "ds_read_b64_tr_b16 v[220:221], v39 offset:10752\n\t" \
  "v_mfma_f32_16x16x32_bf16 v[228:231], v[192:195], v[152:155], v[228:231]\n\t" \
  "ds_read_b64_tr_b16 v[222:223], v39 offset:11264\n\t" \
  "v_mfma_f32_16x16x32_bf16 v[232:235], v[200:203], v[144:147], v[232:235]\n\t" \
  "ds_read_b64_tr_b16 v[192:193], v36 offset:19200\n\t" \
  "s_waitcnt lgkmcnt(6)\n\t" \
  "v_mfma_f32_16x16x32_bf16 v[232:235], v[204:207], v[148:151], v[232:235]\n\t" \
  "ds_read_b64_tr_b16 v[194:195], v36 offset:19712\n\t" \
  "v_mfma_f32_16x16x32_bf16 v[236:239], v[204:207], v[156:159], v[236:239]\n\t" \
  "ds_read_b64_tr_b16 v[196:197], v36 offset:27648\n\t" \
  "v_mfma_f32_16x16x32_bf16 v[236:239], v[200:203], v[152:155], v[236:239]\n\t" \
  "ds_read_b64_tr_b16 v[198:199], v36 offset:28160\n\t" \
  "v_mfma_f32_16x16x32_bf16 v[240:243], v[208:211], v[144:147], v[240:243]\n\t" \
  "ds_read_b64_tr_b16 v[200:201], v37 offset:19200\n\t" \
  "s_waitcnt lgkmcnt(6)\n\t" \
  "v_mfma_f32_16x16x32_bf16 v[240:243], v[212:215], v[148:151], v[240:243]\n\t" \
  "ds_read_b128 v[40:43], v56 offset:160\n\t" \
  "ds_read_b64_tr_b16 v[202:203], v37 offset:19712\n\t" \
  "v_mfma_f32_16x16x32_bf16 v[244:247], v[212:215], v[156:159], v[244:247]\n\t" \
  "ds_read_b64_tr_b16 v[204:205], v37 offset:27648\n\t" \
  "v_mfma_f32_16x16x32_bf16 v[244:247], v[208:211], v[152:155], v[244:247]\n\t" \
  "ds_read_b64_tr_b16 v[206:207], v37 offset:28160\n\t" \
  "v_mfma_f32_16x16x32_bf16 v[248:251], v[216:219], v[144:147], v[248:251]\n\t" \
  "ds_read_b64_tr_b16 v[208:209], v38 offset:19200\n\t" \
  "s_waitcnt lgkmcnt(7)\n\t" \
  "v_mfma_f32_16x16x32_bf16 v[248:251], v[220:223], v[148:151], v[248:251]\n\t" \
  "ds_read_b64_tr_b16 v[210:211], v38 offset:19712\n\t" \
  "v_mfma_f32_16x16x32_bf16 v[252:255], v[220:223], v[156:159], v[252:255]\n\t" \
  "ds_read_b64_tr_b16 v[212:213], v38 offset:27648\n\t" \
  "v_mfma_f32_16x16x32_bf16 v[252:255], v[216:219], v[152:155], v[252:255]\n\t" \
  "ds_read_b64_tr_b16 v[214:215], v38 offset:28160\n\t" \
  "s_waitcnt vmcnt(6)\n\t" \
  "v_mfma_f32_16x16x32_bf16 v[224:227], v[192:195], v[160:163], v[224:227]\n\t" \
  "s_waitcnt lgkmcnt(7)\n\t" \
  "v_min_u32 v40, v40, v41\n\t" \
  "v_min3_u32 v40, v40, v42, v43\n\t" \
  "ds_read_b64_tr_b16 v[216:217], v39 offset:19200\n\t" \
  "v_mfma_f32_16x16x32_bf16 v[224:227], v[196:199], v[164:167], v[224:227]\n\t" \
  "v_readfirstlane_b32 vcc_lo, v40\n\t" \
  "ds_read_b64_tr_b16 v[218:219], v39 offset:19712\n\t" \
  "s_waitcnt vmcnt(4)\n\t" \
  "v_mfma_f32_16x16x32_bf16 v[228:231], v[196:199], v[172:175], v[228:231]\n\t" \
  "s_cmp_ge_u32 vcc_lo, 16\n\t" \
  "s_cbranch_scc0 36f\n\t" \
  "37:\n\t" \
  "ds_read_b64_tr_b16 v[220:221], v39 offset:27648\n\t" \
  "v_mfma_f32_16x16x32_bf16 v[228:231], v[192:195], v[168:171], v[228:231]\n\t" \
  "ds_read_b64_tr_b16 v[222:223], v39 offset:28160\n\t" \
  "s_waitcnt lgkmcnt(6)\n\t" \
  "v_mfma_f32_16x16x32_bf16 v[232:235], v[200:203], v[160:163], v[232:235]\n\t" \
  "ds_read_b64_tr_b16 v[192:193], v28 offset:0\n\t" \
  "v_mfma_f32_16x16x32_bf16 v[232:235], v[204:207], v[164:167], v[232:235]\n\t" \
  "ds_read_b64_tr_b16 v[194:195], v28 offset:512\n\t" \
  "v_mfma_f32_16x16x32_bf16 v[236:239], v[204:207], v[172:175], v[236:239]\n\t" \
  "ds_read_b64_tr_b16 v[196:197], v28 offset:4096\n\t" \
  "v_mfma_f32_16x16x32_bf16 v[236:239], v[200:203], v[168:171], v[236:239]\n\t" \
  "ds_read_b64_tr_b16 v[198:199], v28 offset:4608\n\t" \
  "v_mfma_f32_16x16x32_bf16 v[240:243], v[208:211], v[160:163], v[240:243]\n\t" \
  "ds_read_b64_tr_b16 v[200:201], v29 offset:0\n\t" \
  "s_waitcnt lgkmcnt(6)\n\t" \
  "v_mfma_f32_16x16x32_bf16 v[240:243], v[212:215], v[164:167], v[240:243]\n\t" \
  "ds_read_b64_tr_b16 v[202:203], v29 offset:512\n\t" \
  "v_mfma_f32_16x16x32_bf16 v[244:247], v[212:215], v[172:175], v[244:247]\n\t" \
  "ds_read_b64_tr_b16 v[204:205], v29 offset:4096\n\t" \
  "v_mfma_f32_16x16x32_bf16 v[244:247], v[208:211], v[168:171], v[244:247]\n\t" \
  "ds_read_b64_tr_b16 v[206:207], v29 offset:4608\n\t" \
  "v_mfma_f32_16x16x32_bf16 v[248:251], v[216:219], v[160:163], v[248:251]\n\t" \
  "ds_read_b64_tr_b16 v[208:209], v30 offset:0\n\t" \
  "s_waitcnt lgkmcnt(6)\n\t" \
  "v_mfma_f32_16x16x32_bf16 v[248:251], v[220:223], v[164:167], v[248:251]\n\t" \
  "ds_read_b64_tr_b16 v[210:211], v30 offset:512\n\t" \
  "v_mfma_f32_16x16x32_bf16 v[252:255], v[220:223], v[172:175], v[252:255]\n\t" \
  "ds_read_b64_tr_b16 v[212:213], v30 offset:4096\n\t" \
  "v_mfma_f32_16x16x32_bf16 v[252:255], v[216:219], v[168:171], v[252:255]\n\t" \
  "ds_read_b64_tr_b16 v[214:215], v30 offset:4608\n\t" \
  "s_waitcnt vmcnt(2)\n\t" \
  "v_mfma_f32_16x16x32_bf16 v[224:227], v[192:195], v[176:179], v[224:227]\n\t" \
  "ds_read_b64_tr_b16 v[216:217], v31 offset:0\n\t" \
  "s_waitcnt lgkmcnt(6)\n\t" \
  "v_mfma_f32_16x16x32_bf16 v[224:227], v[196:199], v[180:183], v[224:227]\n\t" \
  "ds_read_b64_tr_b16 v[218:219], v31 offset:512\n\t" \
  "s_waitcnt vmcnt(0)\n\t" \
  "v_mfma_f32_16x16x32_bf16 v[228:231], v[196:199], v[188:191], v[228:231]\n\t" \
  "ds_read_b64_tr_b16 v[220:221], v31 offset:4096\n\t" \
  "v_mfma_f32_16x16x32_bf16 v[228:231], v[192:195], v[184:187], v[228:231]\n\t" \
  "ds_read_b64_tr_b16 v[222:223], v31 offset:4608\n\t" \
  "v_mfma_f32_16x16x32_bf16 v[232:235], v[200:203], v[176:179], v[232:235]\n\t" \
  "s_nop 0\n\t" \
  "v_cvt_pk_bf16_f32 v224, v224, v225\n\t" \
  "v_cvt_pk_bf16_f32 v225, v226, v227\n\t" \
  "s_waitcnt lgkmcnt(5)\n\t" \
  "v_mfma_f32_16x16x32_bf16 v[232:235], v[204:207], v[180:183], v[232:235]\n\t" \
  "ds_write_b64 v57, v[224:225] offset:0\n\t" \
  "v_mfma_f32_16x16x32_bf16 v[236:239], v[204:207], v[188:191], v[236:239]\n\t" \
  "v_cvt_pk_bf16_f32 v228, v228, v229\n\t" \
  "v_cvt_pk_bf16_f32 v229, v230, v231\n\t" \
  "v_mfma_f32_16x16x32_bf16 v[236:239], v[200:203], v[184:187], v[236:239]\n\t" \
  "ds_write_b64 v57, v[228:229] offset:2048\n\t" \
  "v_mfma_f32_16x16x32_bf16 v[240:243], v[208:211], v[176:179], v[240:243]\n\t" \
  "s_nop 0\n\t" \
  "v_cvt_pk_bf16_f32 v232, v232, v233\n\t" \
  "v_cvt_pk_bf16_f32 v233, v234, v235\n\t" \
  "s_waitcnt lgkmcnt(3)\n\t" \
  "v_mfma_f32_16x16x32_bf16 v[240:243], v[212:215], v[180:183], v[240:243]\n\t" \
  "ds_write_b64 v58, v[232:233] offset:0\n\t" \
  "s_waitcnt lgkmcnt(3)\n\t" \
  "v_mfma_f32_16x16x32_bf16 v[248:251], v[220:223], v[180:183], v[248:251]\n\t" \
  "v_cvt_pk_bf16_f32 v236, v236, v237\n\t" \
  "v_cvt_pk_bf16_f32 v237, v238, v239\n\t" \
  "v_mfma_f32_16x16x32_bf16 v[248:251], v[216:219], v[176:179], v[248:251]\n\t" \
  "ds_write_b64 v58, v[236:237] offset:2048\n\t" \
  "v_mfma_f32_16x16x32_bf16 v[244:247], v[208:211], v[184:187], v[244:247]\n\t" \
  "v_cvt_pk_bf16_f32 v240, v240, v241\n\t" \
  "v_cvt_pk_bf16_f32 v241, v242, v243\n\t" \
  "v_mfma_f32_16x16x32_bf16 v[244:247], v[212:215], v[188:191], v[244:247]\n\t" \
  "ds_write_b64 v59, v[240:241] offset:0\n\t" \
  "v_mfma_f32_16x16x32_bf16 v[252:255], v[220:223], v[188:191], v[252:255]\n\t" \
  "s_nop 0\n\t" \
  "v_cvt_pk_bf16_f32 v248, v248, v249\n\t" \
  "v_cvt_pk_bf16_f32 v249, v250, v251\n\t" \
  "v_mfma_f32_16x16x32_bf16 v[252:255], v[216:219], v[184:187], v[252:255]\n\t" \
  "ds_write_b64 v60, v[248:249] offset:0\n\t" \
  "ds_read_b128 v[64:67], v61 offset:0\n\t" \
  "ds_read_b128 v[68:71], v61 offset:1024\n\t" \
  "v_cvt_pk_bf16_f32 v244, v244, v245\n\t" \
  "v_cvt_pk_bf16_f32 v245, v246, v247\n\t" \
  "s_nop 2\n\t" \
  "v_cvt_pk_bf16_f32 v252, v252, v253\n\t" \
  "v_cvt_pk_bf16_f32 v253, v254, v255\n\t" \
  "ds_write_b64 v59, v[244:245] offset:2048\n\t" \
  "ds_write_b64 v60, v[252:253] offset:2048\n\t" \
  "ds_read_b128 v[72:75], v61 offset:2048\n\t" \
  "ds_read_b128 v[76:79], v61 offset:3072\n\t" \
  "s_waitcnt lgkmcnt(5)\n\t" \
  "global_store_dwordx4 v62, v[64:67], %22 offset:-4096" OUTPOL "\n\t" \
  "s_waitcnt lgkmcnt(4)\n\t" \
  "global_store_dwordx4 v63, v[68:71], %22 offset:-4096" OUTPOL "\n\t" \
  "s_waitcnt lgkmcnt(1)\n\t" \
  "global_store_dwordx4 v62, v[72:75], %22 offset:0" OUTPOL "\n\t" \
  "s_waitcnt lgkmcnt(0)\n\t" \
  "global_store_dwordx4 v63, v[76:79], %22 offset:0" OUTPOL "\n\t" \
  "s_branch 19f\n\t" \
  "23:\n\t" \
  "510:\n\t" \
  "s_cbranch_execz 26b\n\t" \
  "s_mov_b64 exec, 0xffff\n\t" \
  "global_store_dword v11, v55, %19 sc1\n\t" \
  "s_mov_b64 exec, -1\n\t" \
  "s_branch 22b\n\t" \
  "7:\n\t" \
  "s_sleep 1\n\t" \
  "ds_read_b128 v[60:63], v56 offset:64\n\t" \
  "ds_read_b64_tr_b16 v[108:109], v5 offset:0\n\t" \
  "ds_read_b64_tr_b16 v[110:111], v5 offset:16\n\t" \
  "ds_read_b128 v[112:115], v6 offset:16384\n\t" \
  "ds_read_b128 v[116:119], v6 offset:15360\n\t" \
  "ds_read_b64_tr_b16 v[120:121], v5 offset:512\n\t" \
  "ds_read_b64_tr_b16 v[122:123], v5 offset:528\n\t" \
  "ds_read_b128 v[124:127], v6 offset:14336\n\t" \
  "ds_read_b64_tr_b16 v[128:129], v5 offset:1024\n\t" \
  "ds_read_b64_tr_b16 v[130:131], v5 offset:1040\n\t" \
  "ds_read_b128 v[132:135], v6 offset:13312\n\t" \
  "ds_read_b64_tr_b16 v[136:137], v5 offset:1536\n\t" \
  "ds_read_b64_tr_b16 v[138:139], v5 offset:1552\n\t" \
  "ds_read_b128 v[140:143], v6 offset:12288\n\t" \
  "ds_read_b64_tr_b16 v[144:145], v5 offset:2048\n\t" \
  "s_waitcnt lgkmcnt(0)\n\t" \
  "v_and_b32 v60, v60, v61\n\t" \
  "v_and_b32 v62, v62, v63\n\t" \
  "v_mov_b32 v12, %13\n\t" \
  "v_and_b32 v60, v60, v62\n\t" \
  "s_nop 0\n\t" \
  "v_readfirstlane_b32 vcc_lo, v60\n\t" \
  "s_nop 1\n\t" \
  "s_cmp_eq_u32 vcc_lo, 0\n\t" \
  "s_cbranch_scc1 7b\n\t" \
  "s_branch 6b\n\t" \
  "30:\n\t" \
  "ds_read_b128 v[40:43], v56 offset:80\n\t" \
  "s_waitcnt lgkmcnt(0)\n\t" \
  "v_min_u32 v40, v40, v41\n\t" \
  "v_min3_u32 v40, v40, v42, v43\n\t" \
  "s_nop 1\n\t" \
  "v_readfirstlane_b32 vcc_lo, v40\n\t" \
  "s_nop 1\n\t" \
  "s_cmp_ge_u32 vcc_lo, 7\n\t" \
  "s_cbranch_scc0 30b\n\t" \
  "s_branch 31b\n\t" \
  "32:\n\t" \
  "ds_read_b128 v[40:43], v56 offset:80\n\t" \
  "s_waitcnt lgkmcnt(0)\n\t" \
  "v_min_u32 v40, v40, v41\n\t" \
  "v_min3_u32 v40, v40, v42, v43\n\t" \
  "s_nop 1\n\t" \
  "v_readfirstlane_b32 vcc_lo, v40\n\t" \
  "s_nop 1\n\t" \
  "s_cmp_ge_u32 vcc_lo, 13\n\t" \
  "s_cbranch_scc0 32b\n\t" \
  "s_branch 33b\n\t" \
  "34:\n\t" \
  "ds_read_b128 v[40:43], v56 offset:80\n\t" \
  "s_waitcnt lgkmcnt(0)\n\t" \
  "v_min_u32 v40, v40, v41\n\t" \
  "v_min3_u32 v40, v40, v42, v43\n\t" \
  "s_nop 1\n\t" \
  "v_readfirstlane_b32 vcc_lo, v40\n\t" \
  "s_nop 1\n\t" \
  "s_cmp_ge_u32 vcc_lo, 16\n\t" \
  "s_cbranch_scc0 34b\n\t" \
  "s_branch 35b\n\t" \
  "36:\n\t" \
  "ds_read_b128 v[40:43], v56 offset:160\n\t" \
  "s_waitcnt lgkmcnt(0)\n\t" \
  "v_min_u32 v40, v40, v41\n\t" \
  "v_min3_u32 v40, v40, v42, v43\n\t" \
  "s_nop 1\n\t" \
  "v_readfirstlane_b32 vcc_lo, v40\n\t" \
  "s_nop 1\n\t" \
  "s_cmp_ge_u32 vcc_lo, 16\n\t" \
  "s_cbranch_scc0 36b\n\t" \
  "s_branch 37b\n\t" \
  "38:\n\t" \
  "s_waitcnt vmcnt(0) lgkmcnt(0)\n\t" \
  "v_readfirstlane_b32 vcc_lo, v16\n\t" \
  "s_nop 1\n\t" \
  "s_cmp_ge_u32 vcc_lo, 1\n\t" \
  "s_cbranch_scc0 40f\n\t" \
  "41:\n\t" \
  "s_lshr_b64 s[90:91], %18, 0\n\t" \
  "s_and_b32 s90, s90, 15\n\t" \
  "s_lshl_b32 s90, s90, 15\n\t" \
  "s_add_u32 s90, s90, s92\n\t" \
  "s_addc_u32 s91, s93, 0\n\t" \
  "global_load_dwordx4 v[64:67], v53, s[90:91] offset:0 sc1\n\t" \
  "global_load_dwordx4 v[68:71], v53, s[90:91] offset:1024 sc1\n\t" \
  "global_load_dwordx4 v[72:75], v53, s[90:91] offset:2048 sc1\n\t" \
  "global_load_dwordx4 v[76:79], v53, s[90:91] offset:3072 sc1\n\t" \
  "s_lshr_b64 s[90:91], %18, 4\n\t" \
  "s_and_b32 s90, s90, 15\n\t" \
  "s_lshl_b32 s90, s90, 15\n\t" \
  "s_add_u32 s90, s90, s92\n\t" \
  "s_addc_u32 s91, s93, 0\n\t" \
  "global_load_dwordx4 v[80:83], v53, s[90:91] offset:0 sc1\n\t" \
  "global_load_dwordx4 v[84:87], v53, s[90:91] offset:1024 sc1\n\t" \
  "global_load_dwordx4 v[88:91], v53, s[90:91] offset:2048 sc1\n\t" \
  "global_load_dwordx4 v[92:95], v53, s[90:91] offset:3072 sc1\n\t" \
  "s_branch 39b\n\t" \
  "40:\n\t" \
  "s_cmp_eq_u32 %13, 0\n\t" \
  "s_cbranch_scc1 42f\n\t" \
  "s_sleep 1\n\t" \
  "ds_read_b32 v16, v56 offset:96\n\t" \
  "s_waitcnt lgkmcnt(0)\n\t" \
  "v_readfirstlane_b32 vcc_lo, v16\n\t" \
  "s_nop 1\n\t" \
  "s_cmp_ge_u32 vcc_lo, 1\n\t" \
  "s_cbranch_scc0 40b\n\t" \
  "s_branch 41b\n\t" \
  "42:\n\t" \
  "v_bfe_u32 v19, v12, 4, 4\n\t" \
  "v_lshlrev_b32 v19, 2, v19\n\t" \
  "s_mov_b32 s90, 0\n\t" \
  "142:\n\t" \
  "global_load_dword v16, v19, %21 sc1\n\t" \
  "s_add_u32 s90, s90, 1\n\t" \
  "s_waitcnt vmcnt(0)\n\t" \
  "v_cmp_ne_u32 vcc, v55, v16\n\t" \
  "s_mov_b32 s88, %26\n\t" \
  "s_mov_b32 s89, s88\n\t" \
  "s_and_b64 vcc, vcc, s[88:89]\n\t" \
  "s_cbranch_scc0 242f\n\t" \
  "s_sleep 2\n\t" \
  "s_cmp_lt_u32 s90, 0x400000\n\t" \
  "s_cbranch_scc1 142b\n\t" \
  "242:\n\t" \
  "v_mov_b32 v16, 1\n\t" \
  "ds_max_u32 v56, v16 offset:96\n\t" \
  "s_branch 41b\n\t" \
  "43:\n\t" \
  "s_cmp_eq_u32 %13, 0\n\t" \
  "s_cbranch_scc1 45f\n\t" \
  "s_sleep 1\n\t" \
  "ds_read_b32 v16, v56 offset:96\n\t" \
  "s_waitcnt lgkmcnt(0)\n\t" \
  "v_readfirstlane_b32 vcc_lo, v16\n\t" \
  "s_nop 1\n\t" \
  "s_cmp_ge_u32 vcc_lo, 2\n\t" \
  "s_cbranch_scc0 43b\n\t" \
  "s_branch 44b\n\t" \
  "45:\n\t" \
  "v_bfe_u32 v19, v12, 4, 4\n\t" \
  "v_lshlrev_b32 v19, 2, v19\n\t" \
  "s_mov_b32 s90, 0\n\t" \
  "145:\n\t" \
  "global_load_dword v16, v19, %21 sc1\n\t" \
  "s_add_u32 s90, s90, 1\n\t" \
  "s_waitcnt vmcnt(0)\n\t" \
  "v_cmp_ne_u32 vcc, v55, v16\n\t" \
  "s_mov_b32 s88, %26\n\t" \
  "s_mov_b32 s89, s88\n\t" \
  "s_and_b64 vcc, vcc, -1\n\t" \
  "s_cbranch_scc0 245f\n\t" \
  "s_sleep 2\n\t" \
  "s_cmp_lt_u32 s90, 0x400000\n\t" \
  "s_cbranch_scc1 145b\n\t" \
  "245:\n\t" \
  "v_mov_b32 v16, 2\n\t" \
  "ds_max_u32 v56, v16 offset:96\n\t" \
  "s_branch 44b\n\t" \
  "19:\n\t"
#define C_CLOB "v10","v11","v12","v13","v18","v19","v20","v21","v22","v23","v24","v25","v26","v27","v28","v29","v30","v31","v32","v33","v34","v35","v36","v37","v38","v39","v40","v41","v42","v43","v44","v45","v46","v47","v48","v49","v50","v53","v55","v56","v57","v58","v59","v60","v61","v62","v63","v64","v65","v66","v67","v68","v69","v70","v71","v72","v73","v74","v75","v76","v77","v78","v79","v80","v81","v82","v83","v84","v85","v86","v87","v88","v89","v90","v91","v92","v93","v94","v95","v96","v97","v98","v99","v100","v101","v102","v103","v104","v105","v106","v107","v108","v109","v110","v111","v112","v113","v114","v115","v116","v117","v118","v119","v120","v121","v122","v123","v124","v125","v126","v127","v128","v129","v130","v131","v132","v133","v134","v135","v136","v137","v138","v139","v140","v141","v142","v143","v144","v145","v146","v147","v148","v149","v150","v151","v152","v153","v154","v155","v156","v157","v158","v159","v160","v161","v162","v163","v164","v165","v166","v167","v168","v169","v170","v171","v172","v173","v174","v175","v176","v177","v178","v179","v180","v181","v182","v183","v184","v185","v186","v187","v188","v189","v190","v191","v192","v193","v194","v195","v196","v197","v198","v199","v200","v201","v202","v203","v204","v205","v206","v207","v208","v209","v210","v211","v212","v213","v214","v215","v216","v217","v218","v219","v220","v221","v222","v223","v224","v225","v226","v227","v228","v229","v230","v231","v232","v233","v234","v235","v236","v237","v238","v239","v240","v241","v242","v243","v244","v245","v246","v247","v248","v249","v250","v251","v252","v253","v254","v255"

static_assert(KVP<13 && KVP>TGS+1, "");

#define TS(i)
#define TSDUMP()
#define TSB(i)
#define TSR(i)
#define TSH(i)
#define TSDECL()

#define TSF(i)
typedef unsigned long long u64;
typedef u64 u64x8 __attribute__((ext_vector_type(8)));
typedef u64 u64x4 __attribute__((ext_vector_type(4)));
typedef unsigned u32x8 __attribute__((ext_vector_type(8)));
typedef unsigned u32x16 __attribute__((ext_vector_type(16)));
template<class T> __device__ __forceinline__ T sg(T v){
  if constexpr(sizeof(T)==4) return (T)__builtin_amdgcn_readfirstlane((unsigned)v); else return v; }
#define GP(T,v) ((T*)(__attribute__((address_space(1))) T*)(v))
__global__ __launch_bounds__(512,2) __attribute__((aligned(4096))) void fused_kernel(const u16* x_, const u16* wqkv_, const u16* kin_, const u16* vin_, const u16* shk_, const u16* hk_,
    const u16* wo_, const u16* qin_, const u16* sD_, u16* out_, u16* ws_, u16* unused_)
{
#define ST0()
#define ST1()
#define ARGS0() u64x8 ka; u64x4 kb; asm volatile("s_load_dwordx16 %0, %2, 0\n\ts_load_dwordx8 %1, %2, 64" : "=&s"(ka), "=&s"(kb) : "s"(__builtin_amdgcn_kernarg_segment_ptr()) : "memory")
#define ARGS() asm volatile("s_waitcnt lgkmcnt(0)" : "+s"(ka), "+s"(kb) :: "memory"); ST1() \
  const u16 *x=GP(const u16,ka[0]), *wqkv=GP(const u16,ka[1]), *kin=GP(const u16,ka[2]), *vin=GP(const u16,ka[3]), *shk=GP(const u16,ka[4]), *hk=GP(const u16,ka[5]), *wo=GP(const u16,ka[6]), *qin=GP(const u16,ka[7]), *sD=GP(const u16,kb[0]); \
  u16 *out=GP(u16,kb[1]), *o=GP(u16,kb[2]); char* ws=(char*)o; unsigned cpar=xcd>>2; \
  auto loc=[&](unsigned off)->unsigned{ return off|(((cpar^(off>>21))&1)<<13); }; \
  (void)x; (void)wqkv; (void)kin; (void)vin; (void)shk; (void)hk; (void)wo; (void)qin; (void)sD; (void)out; (void)loc
  ST0()
  __shared__ __attribute__((aligned(1024))) char smem[ALL_LDS];
  const int tid=threadIdx.x;
  const int w8=__builtin_amdgcn_readfirstlane(tid>>6);
  asm volatile("s_sub_u32 vcc_lo, %0, 4\n\ts_cbranch_scc1 1f\n\ts_cmp_lg_u32 %0, 7\n\ts_cbranch_scc1 2f\n\t"
      "s_lshr_b32 vcc_lo, %2, 3\n\ts_mul_i32 vcc_lo, vcc_lo, %5\n\tv_and_b32 v254, 15, %1\n\tv_lshl_add_u32 v254, v254, 7, vcc_lo\n\ts_getpc_b64 vcc\n\tglobal_load_dword v255, v254, vcc\n\ts_branch 1f\n"
      "2:\n\ts_and_b32 vcc_hi, %2, 7\n\ts_cbranch_scc1 1f\n\ts_cmp_gt_u32 %2, 32\n\ts_cbranch_scc1 1f\n\ts_mul_i32 vcc_hi, %2, 3\n\ts_lshr_b32 vcc_hi, vcc_hi, 3\n\ts_add_u32 vcc_lo, vcc_lo, vcc_hi\n\t"
      "v_and_b32 v253, 3, %1\n\tv_and_b32 v254, 4, %1\n\tv_lshlrev_b32 v253, 7, v253\n\tv_lshl_add_u32 v253, v254, 11, v253\n\tv_lshl_add_u32 v253, vcc_lo, 9, v253\n\tv_add_u32 v253, %4, v253\n\t"
      "s_load_dwordx2 vcc, %3, 80\n\ts_mov_b64 exec, 255\n\ts_waitcnt lgkmcnt(0)\n\tglobal_store_dword v253, v253, vcc sc1\n\ts_mov_b64 exec, -1\n1:"
      :: "s"(w8), "v"(tid), "s"(blockIdx.x), "s"(__builtin_amdgcn_kernarg_segment_ptr()), "n"(WS_GEN+16384), "n"(CODE_ST) : "memory", "scc", "vcc", "v253", "v254", "v255");
  LDS_AS unsigned* const ct=(LDS_AS unsigned*)(LDS_AS char*)(smem+CT_OFF);
  volatile LDS_AS unsigned* const vct=ct;
  auto all4=[&](int i)->unsigned{ u32x4 v=*(volatile LDS_AS u32x4*)(ct+i); return __builtin_amdgcn_readfirstlane(v[0]&v[1]&v[2]&v[3]); };
#define ROTF(b) ((4+4*xr+4*xc+8*(b))&15)
#define BLOCK() int bid=blockIdx.x, w=w8&3; asm volatile("" : "+s"(bid), "+s"(w)); __builtin_assume(bid>=0 && bid<256 && w>=0 && w<4); DERIVE()
#define DERIVE() const int xcd=bid&7, idx=bid>>3, ra=idx&3, rbb=idx>>2, xr=xcd>>1, xc=xcd&1, rb=xr*4+ra, hp=xc*8+rbb, s0=rb*128, tm=rb, tn=hp^TNX, cb=(xcd>>2)*128+idx*4+(xcd&3), gi=cb^16; (void)s0; (void)gi; \
  auto korder=[&]()->unsigned long long{ const unsigned N=0x7654210u, sh=4*(KR), q=(((N>>sh)|(N<<(28-sh)))&0xfffffffu)|0x30000000u; \
    return ((unsigned long long)q<<32|q)^(xc? 0x88888888ull : 0x8888888800000000ull); }; \
  const unsigned l1m=0xf7u<<(8*xc); (void)l1m; (void)korder

  if(__builtin_expect(w8>=4,0)){
  BLOCK(); constexpr int WB=4; (void)WB;
  const int hd=w>>1, par=w&1;
  __builtin_amdgcn_s_setprio(CPRIO);
  {
    ARGS0(); LANES();
    if(w==0){ if(l<52) ct[l]=0u; if(idx==0) *(u32x4*)(smem+AUX+l*16)=u32x4{0,0,0,0}; }
    unsigned a0b=fr*128+((g^(fr&7))*16);
    const int r0=8*g+tq;
    unsigned bbb=r0*128+(((2*hd)^wswz(r0))*32)+8*tp;
    unsigned kA=AUX+(fr^3)*KVP_+w*64+(g&1)*16-(g>>1)*256, kB=AUX+(20+4*g-tq)*KVP_+w*64+8*tp;
    const int cch=127-4*idx-g;
    unsigned kvo=(8*w+tq)*32768+(cch>>6)*16384+((cch>>4)&3)*2048+(cch&15)*64+((tp^((cch>>2)&3))*16);
    unsigned sho=(xcd*128+32*w+fr)*8; const unsigned ctl=CT_OFF+4*w;
    u32x4 sel;
    { const unsigned b2=(g>>1)? 0x0302u : 0x0100u, sv=(fr&1)? (b2<<16|0x0c0cu) : (0x0c0c0000u|b2);
      #pragma unroll
      for(int i=0;i<4;i++) sel[i]= (fr>>1)==(g&1)*4+i ? sv : 0x0c0c0c0cu; }
    unsigned msk=0, lx=w*1024+l*16;
    #pragma unroll
    for(int e=0;e<4;e++){ const int d=4*g+e-fr; const unsigned pv=d<9, hv=par? pv : (unsigned)(d<1 || 4*g+e>7);
      msk|=((unsigned)(d+7)>7u?1u:0u)<<e | pv<<(4+e) | hv<<(8+e) | (d<=0?1u:0u)<<(12+e); }
    msk|=(unsigned)(g? l-16 : 48+((l-1)&15))<<26;
    const unsigned hb=__builtin_amdgcn_readfirstlane(rb==0 ? 0xcfa8965fu : 0u);
    unsigned ap=AUX+w*CV_W+(tq&1)*(CV_CP-4)+8+32*g+4*tq+8*tp;
    u32x4 bp;
    #pragma unroll
    for(int b=0;b<4;b++){ const int c=15+4*b-fr+g; bp[b]=c*64+((w^((c>>2)&3))*16); }
    asm volatile("" : "+v"(a0b), "+v"(bbb), "+v"(kA), "+v"(kB), "+v"(kvo), "+v"(sho), "+v"(msk), "+v"(lx), "+v"(ap), "+v"(bp), "+v"(sel));
    ARGS();
    const char* ow=ws+loc((rb*16+hp)*32768)+(par*2+hd)*1024+4096;
    asm volatile("" : "+s"(shk));
    asm volatile(C_ASM : "+{v52}"(a0b), "+{v54}"(bbb), "+{v0}"(kA), "+{v1}"(kB), "+{v2}"(kvo), "+{v51}"(sho), "+{v3}"(msk), "+{v4}"(lx), "+{v5}"(ap), "+{v[6:9]}"(bp), "+{v[14:17]}"(sel)
       : "s"(shk), "s"(ctl), "s"(w), "s"(ow), "s"(hb), "s"(0.17677669529663687f*1.44269504088896341f), "{s[92:93]}"(ws+loc(tm*16*32768)), "s"(korder()),
         "s"(ws+loc(flo(1,rb*16))+hp*4), "s"(AUX+w*(CV_W+64)), "s"(ws+loc(flo(1,tm*16+tn))),
         "s"(out+(size_t)(tm*128+w*32+1)*2048+1024+tn*64), "s"(8192+w*4096), "s"(ws+loc(WS_KV+xcd*32*32768)), "s"(ws+loc(flo(0,xcd*32))+idx*4), "s"(l1m), "s"(xcd==0 && idx<5 && w<3? (u64)(ws+WS_GEN+idx*3+w) : 0ull) : "memory", "scc", "vcc", "s68", "s69", "s70", "s71", "s74", "s75", "s76", "s77", "s88", "s89", "s90", "s91",
       C_CLOB);
    TSDECL();
    TS(20); TSDUMP();
  }
  return;
  }
  {
    ARGS0(); BLOCK(); constexpr int WB=0; (void)WB; LANES(); TSDECL();
    const unsigned vx=(l>>3)*4096+(((l&7)^(l>>3))*16), vxs=vx+(s0+32*w)*2048, vw=(l>>3)*6144+((((l&7)>>1)^((l>>4)&1))*32)+(l&1)*16+hp*128+w*98304, vw1=(vw^64)+49152-1024;
    u32x8 va; u32x2 vb; unsigned vsp;
    #pragma unroll
    for(int i=0;i<6;i++) va[i]=((i&1)? vw1 : vw)+(i>>1)*2048;
    va[6]=vxs; va[7]=vxs+32768-1024; vb[0]=vxs+2048; vb[1]=vxs+2048+32768-1024;
    vsp=vx+(rb? s0-16+w : 0)*2048;
    const unsigned sw1=w*1024, sw2=w*2048;
    unsigned rot=ROTF(rbb);
    asm volatile("" : "+v"(va), "+v"(vb), "+v"(vsp));
    asm volatile("" : "+s"(rot));
    unsigned vt=(w&1)? (xr*512+idx*16+(l>>1))*2048+(l&1)*1024 : (2*idx+(l&1))*6144+((l>>1)&3)*2048+xc*1024;
    const int kt0=idx*64; int kvs=KS0+KVSEL;
    unsigned kvl=(kt0+16*w+g)*2048+xcd*256+fr*16, l16=l*16;
    const bool hl= w==0 && kt0;
    unsigned c46=sg(w<2? 128u : w==2? 393216u : 0u), c49=sg(w<2? sw1 : (unsigned)ALL_LDS-96), c64=sg(hl? 8192u : 0u), c68=sg(hl? AUX : ALL_LDS-96);
    unsigned long long c50= w<2? ~0ull : w==2? 63ull : 1ull, c66= hl? ~0ull : 1ull;
    asm volatile("" : "+v"(vt), "+v"(kvl), "+v"(l16));
    asm volatile("" : "+s"(c46), "+s"(c49), "+s"(c64), "+s"(c68), "+s"(c50), "+s"(c66), "+s"(kvs));
    ARGS();
    TSB(16);
    asm volatile(H0_ASM : "+{s[40:41]}"(x), "+{s[42:43]}"(wqkv) : "{v[0:7]}"(va), "{v[8:9]}"(vb), "{v10}"(vsp), "{s52}"(rot), "{s53}"(sw2), "{s59}"(w)
      : "memory", "scc", "s76", "s77", "s78", "s79", "s82", "s83");
    const u16* spb= w==2? wqkv : x;
    const int hl3=(3-(int)((size_t)spb>>7))&7, roth=ROTF(hl3), xs=((hl3-ROTF(0))&7)-LEAD;
    unsigned i11=sg((w==2? roth+LEAD+1 : rot+1)&15), sbo, sst;
    if(w>=2){
      vsp=vt+hl3*128;
      touch(spb, vsp+((roth+(l>>3))&15)*c46, w==2? 0x3f3f3f3f3f3f3f3full>>(56-8*LEAD) : xs<=1? 0xffffffffull : 1ull);
    }
    TSB(17);
    unsigned gof=(((gi&15)*128+64*(w&1)+4*(w>>1)+16*((l>>5)&1)+((l>>3)&3))*DM+(gi>>4)*64)*2+(l16&127);
    unsigned wv[2];
    wv[0]=(((l>>5)*16+w*4+((l>>3)&3))*DM+tn*64)*2+((l16&127)^((l&16)*2)^((w&1)*64)); wv[1]=wv[0]+32*DM*2;
    unsigned zpa=(w>>1)*9216+(w&1)*512+l16, hda=AUX+w*CV_W+(l&1)*CV_CP+(l>>1)*16, yra=AUX+tq*(CV_W+64)+(g>>1)*128+(g&1)*32+tp*8+w*1024;
    const unsigned f0=loc(flo(0,cb)), f1=loc(flo(1,tm*16+tn)), f2=loc(flo(2,gi));
    unsigned vc=(w&1)? (g==0? WS_GEN : (g==1? f0 : g==2? f1 : f2)^((l&1)<<13)) : idx*CODE_ST+fr*128u;
    unsigned kfo=(f0^((l&32)<<8))+(l&31)*4, ofo= w==0? f1+fr*4 : WS_GEN;
    asm volatile("" : "+v"(kfo), "+v"(ofo), "+v"(gof), "+v"(wv[0]), "+v"(wv[1]), "+v"(hda), "+v"(yra), "+v"(zpa), "+v"(vc));
    unsigned long long kt=korder();
    asm volatile("" : "+s"(kt));
#define HDBG_O
#define HDBG_I
#define H_IN "{v[0:7]}"(va), "{v[8:9]}"(vb), "{v10}"(vsp), "{v11}"(kvl), "{s[40:41]}"(x), "{s[42:43]}"(wqkv), "{s[44:45]}"(spb), "{s46}"(c46), "{s49}"(c49), "{s[50:51]}"(c50), "{s53}"(sw2), \
      "{s58}"(kvs), "{s59}"(w), "{s[60:61]}"(kin), "{s[62:63]}"(vin), "{s64}"(c64), "{s[66:67]}"(c66), "{s68}"(c68) HDBG_I
#define H_CLOB "memory", "scc", "vcc", "s76", "s77", "s78", "s79", "s80", "s81", "s82", "s83", "s84", "s85", "v20", "v21", "v22", "v253", "v254", "v255"
    asm volatile(H1_ASM : "+{s47}"(i11), "+{s52}"(rot), "={s54}"(sbo), "={s55}"(sst) HDBG_O : H_IN : H_CLOB);
    const u16* hrow=hk+(size_t)(cb*4+w)*S;
    u32x4 h0; unsigned dvu;
    asm volatile("s_load_dwordx4 %0, %2, 0\n\ts_load_dword %1, %3, %4" : "=&s"(h0), "=&s"(dvu) : "s"(hrow), "s"(sD), "s"(cb*8+(w&2)*2) : "memory");
    unsigned long long pc; asm volatile("s_getpc_b64 %0" : "=s"(pc)); pc&=~4095ull;
    asm volatile("" : "+s"(wo), "+s"(qin), "+s"(out), "+s"(ws));
    __builtin_amdgcn_s_setprio(HP1);
    asm volatile(H_ASM : "+{s47}"(i11), "+{s52}"(rot), "+{s54}"(sbo), "+{s55}"(sst) HDBG_O
      : H_IN, "{v14}"(vc), "{v13}"(WS_GEN+256+(l&31)*4), "{s56}"(sg(3u<<(kvs-1) | 1u<<15)),
        "{s57}"(sg(((w&1)? (1u<<TGS | 3u<<PLS | (xs>1? 8u : 0u)) : 1u<<CPS)&~(unsigned)(c50>>32))), "{s[70:71]}"(ws), "{s[72:73]}"(pc) : H_CLOB);
    asm volatile("" : "+s"(h0), "+s"(dvu));
    asm volatile("" : "+s"(bid), "+s"(w)); __builtin_assume(bid>=0 && bid<256 && w>=0 && w<4);
    { DERIVE(); const unsigned sw1=w*1024, sw2=w*2048;
    u32x4 qv[4];
    __builtin_amdgcn_s_setprio(HP2);
    unsigned kf_;
    asm volatile("global_load_dword %0, %1, %2 sc1" : "=v"(kf_) : "v"(kfo), "s"(ws) : "memory");
    { const unsigned fa=AUX+w*CV_W+32;
      asm volatile("s_mov_b64 exec, -2\n\ts_add_u32 m0, %3, %4\n\ts_nop 0\n\tglobal_load_lds_dwordx4 %0, %1" LDPOL "\n\ts_mov_b32 m0, %3\n\ts_nop 0\n\tglobal_load_lds_dwordx4 %0, %2" LDPOL "\n\ts_mov_b64 exec, -1"
        :: "v"(l16), "s"(hrow), "s"(hrow-2), "s"(fa), "n"(CV_CP) : "memory", "scc");
      #pragma unroll
      for(int i=1;i<4;i++){ gldsn(hrow+i*512, l16, fa, CV_CP+i*1024); gldsn(hrow-2+i*512, l16, fa, i*1024); } }
    WAITV(9);
    vct[32+w]=1u;
    TSH(6);
    const unsigned tag=__builtin_amdgcn_readfirstlane(vct[26]);
    const unsigned pl=__builtin_amdgcn_readfirstlane(vct[6]); cpar=(xcd>>2)^(pl>>13); if(w==0) ofo^=pl;
    u32x4 hv;
    { const unsigned d0=(h0[0]&0xffff0000u)|(pack2(bflo(h0[0])+((w&1)? bfhi(dvu) : bflo(dvu)),0.f)&0xffffu);
      hv= l==4? u32x4{0,d0,h0[1],h0[2]} : l==5? u32x4{d0,h0[1],h0[2],h0[3]} : u32x4{0,0,0,0}; }
    asm volatile("s_mov_b64 exec, 63\n\tds_write_b128 %0, %1\n\ts_mov_b64 exec, %3\n\tds_write_b128 %2, %1 offset:7680\n\ts_mov_b64 exec, -1"
      :: "v"(hda), "v"(hv), "v"(zpa), "s"(0xffffffff00000000ull) : "memory");
#define QLD() asm volatile("global_load_dwordx4 %0, %4, %5" LDPOL "\n\tglobal_load_dwordx4 %1, %4, %6" LDPOL "\n\tglobal_load_dwordx4 %2, %4, %7" LDPOL "\n\tglobal_load_dwordx4 %3, %4, %8" LDPOL \
        : "=&v"(qv[0]), "=&v"(qv[1]), "=&v"(qv[2]), "=&v"(qv[3]) : "v"(gof), "s"(qin), "s"(qin+8*DM), "s"(qin+32*DM), "s"(qin+40*DM) : "memory")
    auto kvt=[&](){
      #pragma unroll
      for(int i=0;i<4;i++) gldsc(ws+loc(WS_KV+cb*32768)+(w>>1)*16384+(w&1)*4096+i*1024, l16, sw2, (i&1)*9216+(i>>1)*1024); };
    asm volatile("s_waitcnt vmcnt(8)" : "+v"(kf_) :: "memory");
    if((unsigned)(__ballot(kf_!=tag)>>(pl>>8))) wait_flag((const unsigned*)(ws+loc(flo(0,cb)))+(l&31), tag);
    kvt();
    TSH(7);
    auto wos=[&](int st){ if(NOWO && st>1) return; const u16* b=wo+(size_t)((kt>>(4*st))&15)*64*DM; glds(b,wv[0],sw1,WOB+st*8192); glds(b,wv[1],sw1,WOB+st*8192+4096); };
    wos(0); wos(1);
    while(!all4(36)) __builtin_amdgcn_s_sleep(1);
    #pragma unroll
    for(int st=2;st<NE;st++) wos(st);
    WAITV(2*NE); WAITL();
    vct[16+w]=1u;
    TSH(8);
    __builtin_amdgcn_s_setprio(HP4);
    if(GATE==1) while(!all4(28)) __builtin_amdgcn_s_sleep(1);
    if(GATE==2) while(vct[4]<4u) __builtin_amdgcn_s_sleep(1);
    TSF(24);
    QLD();
    #pragma unroll
    for(int st=NE;st<13;st++){ if(GATE==0 && st==7) while(!all4(0)) __builtin_amdgcn_s_sleep(1);
      WAITV(W1); TSF(24+st);
      if(st==EP+W1/2+1 || st==EP2+W1/2+1)
        asm volatile("s_cmp_lg_u32 %0, 0\n\ts_cbranch_scc1 1f\n\tv_cmp_ne_u32 vcc, %1, v253\n\tv_mov_b32 v253, %3\n\ts_nop 1\n\ts_and_b32 vcc_lo, vcc_lo, %2\n\ts_cbranch_scc1 1f\n\tv_mov_b32 v254, 1\n\tds_max_u32 v253, v254\n1:"
          :: "s"(w), "s"(tag), "s"(l1m), "n"(CT_OFF+96) : "memory", "scc", "vcc", "v253", "v254");
      if(st==EP || st==EP2) asm volatile("global_load_dword v253, %0, %1 sc1" :: "v"(ofo), "s"(ws) : "memory", "v253");
      wos(st); if(st==7+W1/2) vct[20+w]=7u; }
    unsigned op;
    asm volatile("global_load_dword %0, %1, %2 sc1" : "=v"(op) : "v"(ofo), "s"(ws) : "memory");
    WAITV(1); vct[20+w]=13u;
    TSH(9);
    asm volatile("s_waitcnt vmcnt(0)" : "+v"(qv[0]), "+v"(qv[1]), "+v"(qv[2]), "+v"(qv[3]), "+v"(op) :: "memory");
    unsigned oseen=0; bool ptch=false;
    auto olevel=[&](unsigned v){
      const unsigned ne=(unsigned)__ballot(v!=tag), lv= ne==0 ? 2u : (ne&l1m)==0 ? 1u : 0u;
      if(lv>oseen){ if(oseen==0){ TSH(10); } oseen=lv; if(l==0) __hip_atomic_fetch_max(ct+24, lv, __ATOMIC_RELAXED, __HIP_MEMORY_SCOPE_WORKGROUP); }
      if(PTCH && !ptch && !((ne>>(hp^8))&1)){ ptch=true; const char* b=ws+loc((tm*16+(hp^8))*32768);
        asm volatile("global_load_dword v255, %0, %1 sc1\n\tglobal_load_dword v255, %0, %2 sc1" :: "v"(l16*8), "s"(b), "s"(b+16384) : "memory", "v255"); } };
    auto opoll=[&](){ olevel(__hip_atomic_load((const unsigned*)(ws+ofo), __ATOMIC_RELAXED, __HIP_MEMORY_SCOPE_AGENT)); };
    if(w==0) olevel(op);
    float qf[32];
    auto qun=[&](){
    #pragma unroll
    for(int i=0;i<16;i++){ qf[2*i]=bflo(qv[i>>2][i&3]); qf[2*i+1]=bfhi(qv[i>>2][i&3]); }
    #pragma unroll
    for(int i=0;i<32;i+=8) asm volatile("" : "+v"(qf[i]), "+v"(qf[i+1]), "+v"(qf[i+2]), "+v"(qf[i+3]), "+v"(qf[i+4]), "+v"(qf[i+5]), "+v"(qf[i+6]), "+v"(qf[i+7]));
    };
    qun();
    auto wol=[&](int i){ if(!NOWO) glds(wo+(size_t)((kt>>(52+4*(i>>1)))&15)*64*DM, wv[i&1], sw1, i<4? AUX+4352+i*CV_W : (i&1)*4096); };
    while(!all4(44)){ if(w==0 && oseen<1) opoll(); else __builtin_amdgcn_s_sleep(1); }
    wol(0); wol(1); wol(2); wol(3);
    while(!all4(48)){ if(w==0 && oseen<1) opoll(); else __builtin_amdgcn_s_sleep(1); }
    wol(4); wol(5);
    __builtin_amdgcn_s_setprio(HP3);
    bool gen= !(bid==0 && w==1);
    auto trygen=[&](){
      unsigned v[4];
      #pragma unroll
      for(int i=0;i<4;i++) v[i]=__hip_atomic_load((const unsigned*)(ws+(loc(flo(0,i*64))^((i>>1)<<13))+(l>>5)*4096+(l&31)*4), __ATOMIC_RELAXED, __HIP_MEMORY_SCOPE_AGENT);
      if(__all(v[0]==tag && v[1]==tag && v[2]==tag && v[3]==tag)){ if(l==0) st4c(ws+WS_GEN, tag); gen=true; } };
    if(!gen) trygen();
    while(!((all4(8)>>w)&1)){
      if(w==0 && oseen<2) opoll();
      __builtin_amdgcn_s_sleep(OPOLLS);
    }
    TSH(11);
    __builtin_amdgcn_s_setprio(HP5);
    {
      const char* yb=smem+yra;
      s16x4 ya[4], yc[4];
      #pragma unroll
      for(int i=0;i<4;i++){ ya[i]=trread(yb+(i>>1)*512+(i&1)*64); yc[i]=trread(yb+(i>>1)*512+(i&1)*64+256); }
      #pragma unroll
      for(int i=0;i<4;i++) if(NOY!=1) st16c(ws+loc(WS_Y+(cb&~15)*32768)+(w*4+i)*32768+((cb>>3)&1)*16384+(cb&7)*1024, l16, __builtin_bit_cast(u32x4,__builtin_shufflevector(ya[i],yc[i],0,1,2,3,4,5,6,7)));
    }
    TSH(12);
    if(NOY){ WAITV(0); vct[20+w]=16u; for(int sp_=0; !gen && sp_<(1<<22); sp_++) trygen(); TS(20); TSDUMP(); return; }
    unsigned po=gof+(gof&~2047u);
    const char* p0=ws+loc(WS_Y+gi*32768);
    unsigned y0=g*1024+w*256+((fr^(g&2))*16);
    asm volatile("" : "+v"(y0), "+v"(po));
    WAITV(0);
    vct[20+w]=16u;
    if(l<4) st4c(ws+loc(flo(2,cb&~15))+(w*4+l)*128+(cb&15)*4, tag);
    TSH(13);
    if(w==0){ int sp_=0; while(oseen<2 && vct[24]<2u && ++sp_<(1<<22)){ opoll(); __builtin_amdgcn_s_sleep(8); } }
    if(w==0){
      const unsigned* fp=(const unsigned*)(ws+loc(flo(2,gi)))+(l&15); int spins=0;
      while(vct[27]==0u) __builtin_amdgcn_s_sleep(2);
      while(true){
        unsigned v=__hip_atomic_load(fp, __ATOMIC_RELAXED, __HIP_MEMORY_SCOPE_AGENT);
        if(__all(v==tag) || ++spins>(1<<20)) break;
        __builtin_amdgcn_s_sleep(POLLS);
      }
      { u32x4 v; do{ v=*(volatile LDS_AS u32x4*)(ct+20); }while(__builtin_amdgcn_readfirstlane(v[0]&v[1]&v[2]&v[3]&16u)==0); }
      vct[25]=1u;
    } else { while(vct[25]==0u) __builtin_amdgcn_s_sleep(2); }
    TSH(14);
    {
      u32x4 yv[4];
      #pragma unroll
      for(int i=0;i<4;i++) gldsc(p0+(i>>1)*16384+(i&1)*4096, y0^(i*64), w*CV_W, AUX+i*1024);
      const char* ys=smem+AUX+w*CV_W+(l&7)*512;
      const int yi=((l>>3)^((l&7)*2))*16;
      WAITV(0);
      yv[0]=*(const u32x4*)(ys+yi); yv[1]=*(const u32x4*)(ys+yi+256); yv[2]=*(const u32x4*)(ys+(yi^128)); yv[3]=*(const u32x4*)(ys+(yi^128)+256);
      TSH(15);
      #pragma unroll
      for(int r=0;r<4;r++){
        u32x4 ov;
        #pragma unroll
        for(int e=0;e<4;e++){ const unsigned y2=yv[(r>>1)*2+(e>>1)][(r&1)*2+(e&1)]; ov[e]=pack2(qf[r*8+2*e]*bflo(y2), qf[r*8+2*e+1]*bfhi(y2)); }
        stos(out+((r&1)*8+(r>>1)*32)*2048, po, ov);
      }
    }
    for(int sp_=0; !gen && sp_<(1<<22); sp_++) trygen();
    TS(20); TSDUMP();
    }
  }

}

extern "C" void kernel_launch(void* const* d_in, const int* in_sizes, int n_in, void* d_out, int out_size, void* d_ws, size_t ws_size, hipStream_t stream){
  const u16* q=(const u16*)d_in[0]; const u16* k=(const u16*)d_in[1]; const u16* v=(const u16*)d_in[2];
  const u16* shk=(const u16*)d_in[3]; const u16* hk=(const u16*)d_in[4]; const u16* sD=(const u16*)d_in[5];
  const u16* x=(const u16*)d_in[6]; const u16* wqkv=(const u16*)d_in[7]; const u16* wo=(const u16*)d_in[8];
  u16* ws=(u16*)(((size_t)d_ws+16383)&~(size_t)16383);
  fused_kernel<<<256,512,0,stream>>>(x,wqkv,k,v,shk,hk,wo,q,sD,(u16*)d_out,ws,ws);
}
```

```cpp
#include <hip/hip_runtime.h>
#include <hip/hip_bf16.h>
#include <stdint.h>

typedef unsigned short u16;
typedef short s16x4 __attribute__((ext_vector_type(4)));
typedef float f32x32 __attribute__((ext_vector_type(32)));
typedef unsigned u32x4 __attribute__((ext_vector_type(4)));
typedef unsigned u32x2 __attribute__((ext_vector_type(2)));
#define LDS_AS __attribute__((address_space(3)))

__device__ __forceinline__ float bflo(unsigned x){ return __uint_as_float(x<<16); }
__device__ __forceinline__ float bfhi(unsigned x){ return __uint_as_float(x&0xffff0000u); }
typedef float f32x2 __attribute__((ext_vector_type(2)));
typedef __bf16 bf16x2_t __attribute__((ext_vector_type(2)));
__device__ __forceinline__ unsigned pack2(float lo, float hi){ return __builtin_bit_cast(unsigned, __builtin_convertvector(f32x2{lo,hi}, bf16x2_t)); }

#define GLDS(name,pol) __device__ __forceinline__ void name(const void* sb, unsigned voff, unsigned ldsb, int imm){ \
  asm volatile("s_add_u32 m0, %2, %3\n\ts_nop 0\n\tglobal_load_lds_dwordx4 %0, %1" pol :: "v"(voff), "s"(sb), "s"(ldsb), "n"(imm) : "memory", "scc"); }
#define LDPOL " nt"
GLDS(glds,"") GLDS(gldsn,LDPOL) GLDS(gldsc," sc1")
__device__ __forceinline__ void touch(const void* sb, unsigned voff, unsigned long long mask){
  asm volatile("s_mov_b64 exec, %2\n\tglobal_load_dword v255, %0, %1\n\ts_mov_b64 exec, -1" :: "v"(voff), "s"(sb), "s"(mask) : "memory", "v255"); }
__device__ __forceinline__ void st16c(void* sb, unsigned vo, u32x4 v){ asm volatile("global_store_dwordx4 %0, %1, %2 sc1\n\ts_nop 1" :: "v"(vo), "v"(v), "s"(sb) : "memory"); }
#define OUTPOL " sc1 nt"
__device__ __forceinline__ void stos(void* sb, unsigned vo, u32x4 v){ asm volatile("global_store_dwordx4 %0, %1, %2" OUTPOL "\n\ts_nop 1" :: "v"(vo), "v"(v), "s"(sb) : "memory"); }
__device__ __forceinline__ void st4c(void* p, unsigned v){ asm volatile("global_store_dword %0, %1, off sc1" :: "v"(p), "v"(v) : "memory"); }
__device__ __forceinline__ unsigned ld4c(const void* sb, unsigned vo){ unsigned v; asm volatile("global_load_dword %0, %1, %2 sc1\n\ts_waitcnt vmcnt(0)" : "=v"(v) : "v"(vo), "s"(sb) : "memory"); return v; }
__device__ __forceinline__ void wait_flag(const void* sb, unsigned vo, unsigned tag){
  int spins=0;
  while(true){
    unsigned v=ld4c(sb,vo);
    if(__all(v==tag)) break;
    __builtin_amdgcn_s_sleep(2);
    if(++spins>(1<<22)) break;
  }
}
#define WAITV(n) asm volatile("s_waitcnt vmcnt(%0)"::"n"(n):"memory")
#define WAITL() asm volatile("s_waitcnt lgkmcnt(0)":::"memory")
#define LANES() int l; asm volatile("v_mbcnt_lo_u32_b32 %0, -1, 0\n\tv_mbcnt_hi_u32_b32 %0, -1, %0" : "=v"(l)); __builtin_assume(l>=0 && l<64); const int g=l>>4, fr=l&15, tq=(l&15)>>2, tp=l&3; (void)g; (void)fr; (void)tq; (void)tp;

__device__ __forceinline__ s16x4 trread(const char* p){ return __builtin_amdgcn_ds_read_tr16_b64_v4i16((LDS_AS s16x4*)p); }
__device__ __forceinline__ int wswz(int row){ return ((row>>1)&1) | (((row>>3)&1)<<1); }

constexpr int S=2048, DM=1024;
constexpr int K1_XB=144*128, K1_WB=3*64*128, K1_ST=K1_XB+K1_WB;
constexpr int AUX=3*K1_ST, KVP_=1040;
constexpr int WOB=22528, CV_W=8448, CV_CP=4224;
constexpr int CT_OFF=AUX+33*KVP_;
constexpr int ALL_LDS=CT_OFF+208+96;
constexpr unsigned WS_KV=8u<<20, WS_Y=16u<<20, WS_FL=24u<<20, WS_GEN=WS_FL+(1u<<20);
__device__ __forceinline__ unsigned flo(int kind, int c){ return WS_FL+kind*65536+(c>>6)*16384+(c&63)*128; }

#define CPRIO 2
#define CODE_ST 1600
#define LEAD 3
#define HP3 3
#define HP2 3
#define HP4 3
#define TNX 3
#define HP1 0
#define NE 2
#define HP5 3
#define KX 1
#define W1 2
#define OPOLLS 1
#define POLLS 3
#define KR ((rbb+2*ra)%7)
#define KVSEL (rbb==3? 0 : idx%KSN)
#define KS0 1
#define KSN 3
#define KVP 5
#define TGS 2
#define CPS 13
#define PLS 4
#define H0_ASM \
  "s_mul_i32 s76, s52, 0x60000\n\t" \
  "s_add_u32 s76, s42, s76\n\t" \
  "s_addc_u32 s77, s43, 0\n\t" \
  "s_add_u32 s82, s53, 86016\n\t" \
  "s_add_u32 m0, s82, 18432\n\t" \
  "s_lshl_b32 s78, s52, 7\n\t" \
  "global_load_lds_dwordx4 v0, s[76:77]\n\t" \
  "global_load_lds_dwordx4 v1, s[76:77] offset:1024\n\t" \
  "s_add_u32 s78, s40, s78\n\t" \
  "s_addc_u32 s79, s41, 0\n\t" \
  "s_add_u32 m0, s82, 2048\n\t" \
  "s_nop 0\n\t" \
  "global_load_lds_dwordx4 v6, s[78:79]\n\t" \
  "global_load_lds_dwordx4 v7, s[78:79] offset:1024\n\t" \
  "s_add_u32 m0, s82, 26624\n\t" \
  "s_nop 0\n\t" \
  "global_load_lds_dwordx4 v2, s[76:77]\n\t" \
  "global_load_lds_dwordx4 v3, s[76:77] offset:1024\n\t" \
  "s_add_u32 m0, s82, 10240\n\t" \
  "s_nop 0\n\t" \
  "global_load_lds_dwordx4 v8, s[78:79]\n\t" \
  "global_load_lds_dwordx4 v9, s[78:79] offset:1024\n\t" \
  "s_add_u32 m0, s82, 34816\n\t" \
  "s_nop 0\n\t" \
  "global_load_lds_dwordx4 v4, s[76:77]\n\t" \
  "global_load_lds_dwordx4 v5, s[76:77] offset:1024\n\t" \
  "s_cmp_gt_u32 s59, 1\n\t" \
  "s_cbranch_scc1 1f\n\t" \
  "s_lshr_b32 s83, s53, 1\n\t" \
  "s_add_u32 m0, s83, 86016\n\t" \
  "s_nop 0\n\t" \
  "global_load_lds_dwordx4 v10, s[78:79]\n\t" \
  "1:\n\t"
#define H1_ASM \
  "s_mov_b32 s55, 0\n\t" \
  "s_mov_b32 s54, 0\n\t" \
  "s_add_u32 s52, s52, 1\n\t" \
  "s_and_b32 s52, s52, 15\n\t" \
  "s_cmp_eq_u32 s58, 1\n\t" \
  "s_cbranch_scc1 1f\n\t" \
  "s_mul_i32 s76, s52, 0x60000\n\t" \
  "s_add_u32 s76, s42, s76\n\t" \
  "s_addc_u32 s77, s43, 0\n\t" \
  "s_lshl_b32 s78, s52, 7\n\t" \
  "s_add_u32 s78, s40, s78\n\t" \
  "s_addc_u32 s79, s41, 0\n\t" \
  "s_mul_i32 s80, s47, s46\n\t" \
  "s_add_u32 s80, s44, s80\n\t" \
  "s_addc_u32 s81, s45, 0\n\t" \
  "s_mov_b32 s82, s53\n\t" \
  "s_add_u32 m0, s82, 18432\n\t" \
  "s_add_u32 s52, s52, 1\n\t" \
  "global_load_lds_dwordx4 v0, s[76:77]\n\t" \
  "global_load_lds_dwordx4 v1, s[76:77] offset:1024\n\t" \
  "s_add_u32 m0, s82, 2048\n\t" \
  "s_and_b32 s52, s52, 15\n\t" \
  "global_load_lds_dwordx4 v6, s[78:79]\n\t" \
  "global_load_lds_dwordx4 v7, s[78:79] offset:1024\n\t" \
  "s_add_u32 m0, s82, 26624\n\t" \
  "s_add_u32 s47, s47, 1\n\t" \
  "global_load_lds_dwordx4 v2, s[76:77]\n\t" \
  "global_load_lds_dwordx4 v3, s[76:77] offset:1024\n\t" \
  "s_add_u32 m0, s82, 10240\n\t" \
  "s_and_b32 s47, s47, 15\n\t" \
  "global_load_lds_dwordx4 v8, s[78:79]\n\t" \
  "global_load_lds_dwordx4 v9, s[78:79] offset:1024\n\t" \
  "s_add_u32 m0, s82, 34816\n\t" \
  "s_and_b32 s83, s54, s51\n\t" \
  "global_load_lds_dwordx4 v4, s[76:77]\n\t" \
  "global_load_lds_dwordx4 v5, s[76:77] offset:1024\n\t" \
  "s_add_u32 m0, s83, s49\n\t" \
  "s_mov_b64 exec, s[50:51]\n\t" \
  "global_load_lds_dwordx4 v10, s[80:81]\n\t" \
  "s_mov_b64 exec, -1\n\t" \
  "s_mov_b32 s55, 1\n\t" \
  "s_mov_b32 s54, 43008\n\t" \
  "s_waitcnt vmcnt(11)\n\t" \
  "s_branch 2f\n\t" \
  "1:\n\t" \
  "s_mul_i32 s83, s59, 4160\n\t" \
  "v_add_u32 v20, 7152, v11\n\t" \
  "v_add_u32 v21, 14304, v11\n\t" \
  "v_add_u32 v22, 21456, v11\n\t" \
  "s_add_u32 m0, s83, 130064\n\t" \
  "s_nop 0\n\t" \
  "global_load_lds_dwordx4 v11, s[60:61]" LDPOL "\n\t" \
  "global_load_lds_dwordx4 v20, s[60:61] offset:1040" LDPOL "\n\t" \
  "global_load_lds_dwordx4 v21, s[60:61] offset:2080" LDPOL "\n\t" \
  "global_load_lds_dwordx4 v22, s[60:61] offset:3120" LDPOL "\n\t" \
  "s_add_u32 m0, s83, 146704\n\t" \
  "s_nop 0\n\t" \
  "global_load_lds_dwordx4 v11, s[62:63]" LDPOL "\n\t" \
  "global_load_lds_dwordx4 v20, s[62:63] offset:1040" LDPOL "\n\t" \
  "global_load_lds_dwordx4 v21, s[62:63] offset:2080" LDPOL "\n\t" \
  "global_load_lds_dwordx4 v22, s[62:63] offset:3120" LDPOL "\n\t" \
  "v_subrev_u32 v20, s64, v11\n\t" \
  "s_mov_b32 m0, s68\n\t" \
  "s_mov_b64 exec, s[66:67]\n\t" \
  "global_load_lds_dwordx4 v20, s[60:61]" LDPOL "\n\t" \
  "s_mov_b64 exec, -1\n\t" \
  "s_waitcnt vmcnt(9)\n\t" \
  "2:\n\t" \
  "s_waitcnt lgkmcnt(0)\n\t" \
  "s_barrier\n\t"
#define H_ASM \
  "s_mul_i32 s76, s52, 0x60000\n\t" \
  "s_add_u32 s76, s42, s76\n\t" \
  "s_addc_u32 s77, s43, 0\n\t" \
  "s_lshl_b32 s78, s52, 7\n\t" \
  "s_add_u32 s78, s40, s78\n\t" \
  "s_addc_u32 s79, s41, 0\n\t" \
  "s_mul_i32 s80, s47, s46\n\t" \
  "s_add_u32 s80, s44, s80\n\t" \
  "s_addc_u32 s81, s45, 0\n\t" \
  "s_add_u32 s82, s54, s53\n\t" \
  "s_cmp_eq_u32 s58, 2\n\t" \
  "s_cbranch_scc1 5f\n\t" \
  "s_branch 3f\n\t" \
  "1:\n\t" \
  "s_waitcnt vmcnt(11)\n\t" \
  "2:\n\t" \
  "s_barrier\n\t" \
  "3:\n\t" \
  "s_add_u32 s55, s55, 1\n\t" \
  "s_add_u32 m0, s82, 18432\n\t" \
  "s_add_u32 s52, s52, 1\n\t" \
  "global_load_lds_dwordx4 v0, s[76:77]\n\t" \
  "global_load_lds_dwordx4 v1, s[76:77] offset:1024\n\t" \
  "s_add_u32 m0, s82, 2048\n\t" \
  "s_and_b32 s52, s52, 15\n\t" \
  "global_load_lds_dwordx4 v6, s[78:79]\n\t" \
  "global_load_lds_dwordx4 v7, s[78:79] offset:1024\n\t" \
  "s_add_u32 m0, s82, 26624\n\t" \
  "s_add_u32 s47, s47, 1\n\t" \
  "global_load_lds_dwordx4 v2, s[76:77]\n\t" \
  "global_load_lds_dwordx4 v3, s[76:77] offset:1024\n\t" \
  "s_add_u32 m0, s82, 10240\n\t" \
  "s_and_b32 s47, s47, 15\n\t" \
  "global_load_lds_dwordx4 v8, s[78:79]\n\t" \
  "global_load_lds_dwordx4 v9, s[78:79] offset:1024\n\t" \
  "s_add_u32 m0, s82, 34816\n\t" \
  "s_and_b32 s83, s54, s51\n\t" \
  "global_load_lds_dwordx4 v4, s[76:77]\n\t" \
  "global_load_lds_dwordx4 v5, s[76:77] offset:1024\n\t" \
  "s_bitcmp1_b32 s57, s55\n\t" \
  "s_cbranch_scc1 10f\n\t" \
  "8:\n\t" \
  "s_add_u32 m0, s83, s49\n\t" \
  "s_mov_b64 exec, s[50:51]\n\t" \
  "global_load_lds_dwordx4 v10, s[80:81]\n\t" \
  "s_mov_b64 exec, -1\n\t" \
  "4:\n\t" \
  "s_mul_i32 s76, s52, 0x60000\n\t" \
  "s_add_u32 s76, s42, s76\n\t" \
  "s_addc_u32 s77, s43, 0\n\t" \
  "s_lshl_b32 s78, s52, 7\n\t" \
  "s_add_u32 s78, s40, s78\n\t" \
  "s_addc_u32 s79, s41, 0\n\t" \
  "s_mul_i32 s80, s47, s46\n\t" \
  "s_add_u32 s80, s44, s80\n\t" \
  "s_addc_u32 s81, s45, 0\n\t" \
  "s_add_u32 s54, s54, 43008\n\t" \
  "s_cmp_eq_u32 s54, 129024\n\t" \
  "s_cselect_b32 s54, 0, s54\n\t" \
  "s_add_u32 s82, s54, s53\n\t" \
  "s_bitcmp0_b32 s56, s55\n\t" \
  "s_cbranch_scc1 1b\n\t" \
  "s_cmp_eq_u32 s55, 15\n\t" \
  "s_cbranch_scc1 19f\n\t" \
  "s_cmp_eq_u32 s55, s58\n\t" \
  "s_cbranch_scc1 3b\n\t" \
  "s_waitcnt vmcnt(11)\n\t" \
  "s_barrier\n\t" \
  "5:\n\t" \
  "s_mul_i32 s83, s59, 4160\n\t" \
  "v_add_u32 v20, 7152, v11\n\t" \
  "v_add_u32 v21, 14304, v11\n\t" \
  "v_add_u32 v22, 21456, v11\n\t" \
  "s_add_u32 m0, s83, 130064\n\t" \
  "s_nop 0\n\t" \
  "global_load_lds_dwordx4 v11, s[60:61]" LDPOL "\n\t" \
  "global_load_lds_dwordx4 v20, s[60:61] offset:1040" LDPOL "\n\t" \
  "global_load_lds_dwordx4 v21, s[60:61] offset:2080" LDPOL "\n\t" \
  "global_load_lds_dwordx4 v22, s[60:61] offset:3120" LDPOL "\n\t" \
  "s_add_u32 m0, s83, 146704\n\t" \
  "s_nop 0\n\t" \
  "global_load_lds_dwordx4 v11, s[62:63]" LDPOL "\n\t" \
  "global_load_lds_dwordx4 v20, s[62:63] offset:1040" LDPOL "\n\t" \
  "global_load_lds_dwordx4 v21, s[62:63] offset:2080" LDPOL "\n\t" \
  "global_load_lds_dwordx4 v22, s[62:63] offset:3120" LDPOL "\n\t" \
  "v_subrev_u32 v20, s64, v11\n\t" \
  "s_mov_b32 m0, s68\n\t" \
  "s_mov_b64 exec, s[66:67]\n\t" \
  "global_load_lds_dwordx4 v20, s[60:61]" LDPOL "\n\t" \
  "s_mov_b64 exec, -1\n\t" \
  "s_waitcnt vmcnt(9)\n\t" \
  "s_branch 2b\n\t" \
  "10:\n\t" \
  "s_cmp_eq_u32 s59, 2\n\t" \
  "s_cbranch_scc0 11f\n\t" \
  "s_mov_b64 exec, 0xffff\n\t" \
  "global_load_dword v255, v14, s[72:73]\n\t" \
  "s_mov_b64 exec, -1\n\t" \
  "s_branch 4b\n\t" \
  "11:\n\t" \
  "s_cmp_eq_u32 s55, 2\n\t" \
  "s_cbranch_scc0 12f\n\t" \
  "global_load_dword v254, v14, s[70:71] sc1\n\t" \
  "s_branch 4b\n\t" \
  "12:\n\t" \
  "s_cmp_eq_u32 s55, 4\n\t" \
  "s_cbranch_scc0 15f\n\t" \
  "global_load_dword v253, v13, s[70:71] sc1\n\t" \
  "s_branch 4b\n\t" \
  "15:\n\t" \
  "s_cmp_eq_u32 s55, 5\n\t" \
  "s_cbranch_scc0 13f\n\t" \
  "v_readfirstlane_b32 s84, v254\n\t" \
  "s_mov_b32 m0, 0x400000\n\t" \
  "s_waitcnt vmcnt(10)\n\t" \
  "s_add_u32 s84, s84, 1\n\t" \
  "v_mov_b32 v254, s84\n\t" \
  "16:\n\t" \
  "v_cmp_eq_u32 vcc, v253, v254\n\t" \
  "s_nop 1\n\t" \
  "s_and_b32 s84, vcc_lo, 0x3fffffff\n\t" \
  "s_bcnt1_i32_b32 s85, s84\n\t" \
  "s_cmp_ge_u32 s85, 15\n\t" \
  "s_cbranch_scc1 17f\n\t" \
  "s_sub_u32 m0, m0, 1\n\t" \
  "s_cbranch_scc1 17f\n\t" \
  "global_load_dword v253, v13, s[70:71] sc1\n\t" \
  "s_waitcnt vmcnt(0)\n\t" \
  "s_branch 16b\n\t" \
  "17:\n\t" \
  "s_and_b32 s84, s84, 0xaaaaaaaa\n\t" \
  "s_bcnt1_i32_b32 s84, s84\n\t" \
  "s_and_b32 s84, s84, 8\n\t" \
  "s_lshl_b32 s84, s84, 10\n\t" \
  "v_mov_b32 v20, s84\n\t" \
  "v_mov_b32 v253, 163344\n\t" \
  "ds_write_b32 v253, v254 offset:104\n\t" \
  "ds_write_b32 v253, v20 offset:24\n\t" \
  "s_waitcnt lgkmcnt(0)\n\t" \
  "s_branch 8b\n\t" \
  "13:\n\t" \
  "s_mov_b32 exec_hi, 0\n\t" \
  "global_load_dword v255, v10, s[44:45]\n\t" \
  "s_mov_b64 exec, -1\n\t" \
  "s_branch 4b\n\t" \
  "19:\n\t" \
  "s_waitcnt vmcnt(11) lgkmcnt(0)\n\t" \
  "s_barrier\n\t"
#define C_ASM \
  "s_branch 29f\n\t" \
  "21:\n\t" \
  "s_mov_b64 s[68:69], %27\n\t" \
  "s_and_b32 s74, s68, 15\n\t" \
  "s_andn2_b32 s68, s68, 15\n\t" \
  "s_load_dword vcc_lo, s[68:69], 0 glc\n\t" \
  "s_and_b32 s75, s74, 1\n\t" \
  "s_lshl_b32 s70, s74, 9\n\t" \
  "s_lshl_b32 s71, s75, 13\n\t" \
  "s_or_b32 s70, s70, s71\n\t" \
  "s_waitcnt lgkmcnt(0)\n\t" \
  "s_or_b32 s68, s68, s70\n\t" \
  "s_add_u32 vcc_lo, vcc_lo, 1\n\t" \
  "v_mov_b32 v18, vcc_lo\n\t" \
  "s_memtime s[70:71]\n\t" \
  "s_waitcnt lgkmcnt(0)\n\t" \
  "s_load_dword s76, s[68:69], 16384 glc\n\t" \
  "s_waitcnt lgkmcnt(0)\n\t" \
  "s_xor_b32 s68, s68, 0x2000\n\t" \
  "s_memtime s[90:91]\n\t" \
  "s_waitcnt lgkmcnt(0)\n\t" \
  "s_load_dword s76, s[68:69], 16384 glc\n\t" \
  "s_waitcnt lgkmcnt(0)\n\t" \
  "s_sub_u32 vcc_lo, s90, s70\n\t" \
  "s_memtime s[70:71]\n\t" \
  "s_waitcnt lgkmcnt(0)\n\t" \
  "s_sub_u32 s70, s70, s90\n\t" \
  "s_cmp_gt_u32 vcc_lo, s70\n\t" \
  "s_cselect_b32 s70, 1, 0\n\t" \
  "s_xor_b32 s70, s70, s75\n\t" \
  "s_lshl1_add_u32 s70, s74, s70\n\t" \
  "s_lshl_b32 s70, s70, 2\n\t" \
  "v_mov_b32 v19, s70\n\t" \
  "s_and_b32 s68, s68, 0xffffc000\n\t" \
  "s_mov_b64 exec, 1\n\t" \
  "global_store_dword v19, v18, s[68:69] offset:256 sc1\n\t" \
  "s_branch 26f\n\t" \
  "28:\n\t" \
  "s_cmp_lg_u64 %27, 0\n\t" \
  "s_cbranch_scc1 21b\n\t" \
  "s_mov_b64 exec, 0\n\t" \
  "s_cmp_eq_u32 %13, 0\n\t" \
  "s_cbranch_scc1 500f\n\t" \
  "s_cmp_eq_u32 %13, 1\n\t" \
  "s_cbranch_scc1 501f\n\t" \
  "s_cmp_eq_u32 %13, 2\n\t" \
  "s_cbranch_scc1 502f\n\t" \
  "s_branch 503f\n\t" \
  "26:\n\t" \
  "s_mov_b64 exec, -1\n\t" \
  "s_add_u32 s88, s88, 4\n\t" \
  "s_addc_u32 s89, s89, 0\n\t" \
  "s_setpc_b64 s[88:89]\n\t" \
  ".p2align 12\n\t" \
  "29:\n\t" \
  "s_bitcmp0_b32 %13, 0\n\t" \
  "s_cbranch_scc1 8f\n\t" \
  "global_load_dwordx2 v[10:11], v51, %11\n\t" \
  "global_load_dwordx2 v[12:13], v51, %11 offset:128\n\t" \
  "v_xor_b32 v53, 64, v52\n\t" \
  "v_xor_b32 v55, 32, v54\n\t" \
  "v_add_u32 v56, 43008, v52\n\t" \
  "v_add_u32 v58, 43008, v54\n\t" \
  "v_xor_b32 v57, 64, v56\n\t" \
  "v_xor_b32 v59, 32, v58\n\t" \
  "v_add_u32 v60, 86016, v52\n\t" \
  "v_add_u32 v62, 86016, v54\n\t" \
  "v_xor_b32 v61, 64, v60\n\t" \
  "v_xor_b32 v63, 32, v62\n\t" \
  "s_mov_b32 vcc_hi, 0x05040100\n\t" \
  "s_waitcnt vmcnt(2)\n\t" \
  "s_barrier\n\t" \
  "ds_read_b64_tr_b16 v[64:65], v62 offset:26624\n\t" \
  "ds_read_b64_tr_b16 v[66:67], v62 offset:27136\n\t" \
  "ds_read_b128 v[68:71], v60 offset:10240\n\t" \
  "ds_read_b64_tr_b16 v[72:73], v62 offset:30720\n\t" \
  "ds_read_b64_tr_b16 v[74:75], v62 offset:31232\n\t" \
  "ds_read_b128 v[76:79], v61 offset:10240\n\t" \
  "ds_read_b128 v[80:83], v61 offset:12288\n\t" \
  "ds_read_b128 v[84:87], v60 offset:12288\n\t" \
  "ds_read_b128 v[88:91], v60 offset:14336\n\t" \
  "ds_read_b128 v[92:95], v61 offset:14336\n\t" \
  "ds_read_b128 v[96:99], v61 offset:16384\n\t" \
  "ds_read_b128 v[100:103], v60 offset:16384\n\t" \
  "ds_read_b64_tr_b16 v[104:105], v63 offset:26624\n\t" \
  "ds_read_b64_tr_b16 v[106:107], v63 offset:27136\n\t" \
  "ds_read_b64_tr_b16 v[108:109], v63 offset:30720\n\t" \
  "ds_read_b64_tr_b16 v[110:111], v63 offset:31232\n\t" \
  "s_waitcnt lgkmcnt(13)\n\t" \
  "v_mfma_f32_16x16x32_bf16 v[184:187], v[64:67], v[68:71], 0\n\t" \
  "ds_read_b64_tr_b16 v[112:113], v62 offset:34816\n\t" \
  "ds_read_b64_tr_b16 v[114:115], v62 offset:35328\n\t" \
  "s_waitcnt lgkmcnt(12)\n\t" \
  "v_mfma_f32_16x16x32_bf16 v[184:187], v[72:75], v[76:79], v[184:187]\n\t" \
  "ds_read_b64_tr_b16 v[116:117], v62 offset:38912\n\t" \
  "ds_read_b64_tr_b16 v[118:119], v62 offset:39424\n\t" \
  "s_waitcnt lgkmcnt(13)\n\t" \
  "v_mfma_f32_16x16x32_bf16 v[192:195], v[72:75], v[80:83], 0\n\t" \
  "ds_read_b128 v[120:123], v60 offset:0\n\t" \
  "s_waitcnt lgkmcnt(13)\n\t" \
  "v_mfma_f32_16x16x32_bf16 v[192:195], v[64:67], v[84:87], v[192:195]\n\t" \
  "ds_read_b128 v[124:127], v61 offset:0\n\t" \
  "s_waitcnt lgkmcnt(13)\n\t" \
  "v_mfma_f32_16x16x32_bf16 v[200:203], v[64:67], v[88:91], 0\n\t" \
  "ds_read_b64_tr_b16 v[128:129], v63 offset:34816\n\t" \
  "s_waitcnt lgkmcnt(13)\n\t" \
  "v_mfma_f32_16x16x32_bf16 v[200:203], v[72:75], v[92:95], v[200:203]\n\t" \
  "ds_read_b64_tr_b16 v[130:131], v63 offset:35328\n\t" \
  "s_waitcnt lgkmcnt(13)\n\t" \
  "v_mfma_f32_16x16x32_bf16 v[208:211], v[72:75], v[96:99], 0\n\t" \
  "ds_read_b64_tr_b16 v[132:133], v63 offset:38912\n\t" \
  "s_waitcnt lgkmcnt(9)\n\t" \
  "v_mfma_f32_16x16x32_bf16 v[208:211], v[64:67], v[100:103], v[208:211]\n\t" \
  "ds_read_b64_tr_b16 v[134:135], v63 offset:39424\n\t" \
  "v_mfma_f32_16x16x32_bf16 v[188:191], v[104:107], v[68:71], 0\n\t" \
  "ds_read_b64_tr_b16 v[136:137], v62 offset:18432\n\t" \
  "v_mfma_f32_16x16x32_bf16 v[188:191], v[108:111], v[76:79], v[188:191]\n\t" \
  "ds_read_b64_tr_b16 v[138:139], v62 offset:18944\n\t" \
  "v_mfma_f32_16x16x32_bf16 v[196:199], v[108:111], v[80:83], 0\n\t" \
  "ds_read_b64_tr_b16 v[140:141], v62 offset:22528\n\t" \
  "v_mfma_f32_16x16x32_bf16 v[196:199], v[104:107], v[84:87], v[196:199]\n\t" \
  "ds_read_b64_tr_b16 v[142:143], v62 offset:23040\n\t" \
  "v_mfma_f32_16x16x32_bf16 v[204:207], v[104:107], v[88:91], 0\n\t" \
  "ds_read_b64_tr_b16 v[144:145], v63 offset:18432\n\t" \
  "v_mfma_f32_16x16x32_bf16 v[204:207], v[108:111], v[92:95], v[204:207]\n\t" \
  "ds_read_b64_tr_b16 v[146:147], v63 offset:18944\n\t" \
  "v_mfma_f32_16x16x32_bf16 v[212:215], v[108:111], v[96:99], 0\n\t" \
  "ds_read_b64_tr_b16 v[148:149], v63 offset:22528\n\t" \
  "v_mfma_f32_16x16x32_bf16 v[212:215], v[104:107], v[100:103], v[212:215]\n\t" \
  "ds_read_b64_tr_b16 v[150:151], v63 offset:23040\n\t" \
  "s_waitcnt lgkmcnt(7)\n\t" \
  "v_mfma_f32_16x16x32_bf16 v[216:219], v[68:71], v[112:115], 0\n\t" \
  "v_mfma_f32_16x16x32_bf16 v[216:219], v[76:79], v[116:119], v[216:219]\n\t" \
  "v_mfma_f32_16x16x32_bf16 v[224:227], v[80:83], v[116:119], 0\n\t" \
  "v_mfma_f32_16x16x32_bf16 v[224:227], v[84:87], v[112:115], v[224:227]\n\t" \
  "v_mfma_f32_16x16x32_bf16 v[232:235], v[88:91], v[112:115], 0\n\t" \
  "v_mfma_f32_16x16x32_bf16 v[232:235], v[92:95], v[116:119], v[232:235]\n\t" \
  "v_mfma_f32_16x16x32_bf16 v[240:243], v[96:99], v[116:119], 0\n\t" \
  "v_mfma_f32_16x16x32_bf16 v[240:243], v[100:103], v[112:115], v[240:243]\n\t" \
  "v_mfma_f32_16x16x32_bf16 v[248:251], v[120:123], v[112:115], 0\n\t" \
  "v_mfma_f32_16x16x32_bf16 v[248:251], v[124:127], v[116:119], v[248:251]\n\t" \
  "v_mfma_f32_16x16x32_bf16 v[252:255], v[120:123], v[128:131], 0\n\t" \
  "v_mfma_f32_16x16x32_bf16 v[252:255], v[124:127], v[132:135], v[252:255]\n\t" \
  "v_mfma_f32_16x16x32_bf16 v[220:223], v[76:79], v[132:135], 0\n\t" \
  "v_mfma_f32_16x16x32_bf16 v[220:223], v[68:71], v[128:131], v[220:223]\n\t" \
  "s_waitcnt lgkmcnt(0)\n\t" \
  "s_getpc_b64 s[88:89]\n\t" \
  "s_branch 28b\n\t" \
  "s_barrier\n\t" \
  "v_mfma_f32_16x16x32_bf16 v[228:231], v[84:87], v[128:131], 0\n\t" \
  "ds_read_b64_tr_b16 v[72:73], v54 offset:26624\n\t" \
  "v_mfma_f32_16x16x32_bf16 v[228:231], v[80:83], v[132:135], v[228:231]\n\t" \
  "ds_read_b64_tr_b16 v[74:75], v54 offset:27136\n\t" \
  "v_mfma_f32_16x16x32_bf16 v[236:239], v[92:95], v[132:135], 0\n\t" \
  "ds_read_b128 v[64:67], v52 offset:10240\n\t" \
  "v_mfma_f32_16x16x32_bf16 v[236:239], v[88:91], v[128:131], v[236:239]\n\t" \
  "ds_read_b64_tr_b16 v[108:109], v54 offset:30720\n\t" \
  "v_mfma_f32_16x16x32_bf16 v[244:247], v[100:103], v[128:131], 0\n\t" \
  "ds_read_b64_tr_b16 v[110:111], v54 offset:31232\n\t" \
  "v_mfma_f32_16x16x32_bf16 v[244:247], v[96:99], v[132:135], v[244:247]\n\t" \
  "ds_read_b128 v[104:107], v53 offset:10240\n\t" \
  "v_mfma_f32_16x16x32_bf16 v[152:155], v[136:139], v[68:71], 0\n\t" \
  "ds_read_b128 v[112:115], v53 offset:12288\n\t" \
  "v_mfma_f32_16x16x32_bf16 v[152:155], v[140:143], v[76:79], v[152:155]\n\t" \
  "ds_read_b128 v[116:119], v52 offset:12288\n\t" \
  "v_mfma_f32_16x16x32_bf16 v[160:163], v[140:143], v[80:83], 0\n\t" \
  "ds_read_b128 v[120:123], v52 offset:14336\n\t" \
  "v_mfma_f32_16x16x32_bf16 v[160:163], v[136:139], v[84:87], v[160:163]\n\t" \
  "ds_read_b128 v[124:127], v53 offset:14336\n\t" \
  "v_mfma_f32_16x16x32_bf16 v[168:171], v[136:139], v[88:91], 0\n\t" \
  "ds_read_b128 v[128:131], v53 offset:16384\n\t" \
  "v_mfma_f32_16x16x32_bf16 v[168:171], v[140:143], v[92:95], v[168:171]\n\t" \
  "ds_read_b128 v[132:135], v52 offset:16384\n\t" \
  "v_mfma_f32_16x16x32_bf16 v[176:179], v[140:143], v[96:99], 0\n\t" \
  "ds_read_b64_tr_b16 v[140:141], v55 offset:26624\n\t" \
  "v_mfma_f32_16x16x32_bf16 v[176:179], v[136:139], v[100:103], v[176:179]\n\t" \
  "ds_read_b64_tr_b16 v[142:143], v55 offset:27136\n\t" \
  "v_mfma_f32_16x16x32_bf16 v[156:159], v[144:147], v[68:71], 0\n\t" \
  "ds_read_b64_tr_b16 v[136:137], v55 offset:30720\n\t" \
  "v_mfma_f32_16x16x32_bf16 v[156:159], v[148:151], v[76:79], v[156:159]\n\t" \
  "ds_read_b64_tr_b16 v[138:139], v55 offset:31232\n\t" \
  "v_mfma_f32_16x16x32_bf16 v[164:167], v[148:151], v[80:83], 0\n\t" \
  "ds_read_b64_tr_b16 v[68:69], v54 offset:34816\n\t" \
  "v_mfma_f32_16x16x32_bf16 v[164:167], v[144:147], v[84:87], v[164:167]\n\t" \
  "ds_read_b64_tr_b16 v[70:71], v54 offset:35328\n\t" \
  "v_mfma_f32_16x16x32_bf16 v[172:175], v[144:147], v[88:91], 0\n\t" \
  "ds_read_b64_tr_b16 v[76:77], v54 offset:38912\n\t" \
  "v_mfma_f32_16x16x32_bf16 v[172:175], v[148:151], v[92:95], v[172:175]\n\t" \
  "ds_read_b64_tr_b16 v[78:79], v54 offset:39424\n\t" \
  "v_mfma_f32_16x16x32_bf16 v[180:183], v[148:151], v[96:99], 0\n\t" \
  "ds_read_b128 v[80:83], v52 offset:0\n\t" \
  "v_mfma_f32_16x16x32_bf16 v[180:183], v[144:147], v[100:103], v[180:183]\n\t" \
  "ds_read_b128 v[84:87], v53 offset:0\n\t" \
  "s_waitcnt lgkmcnt(7)\n\t" \
  "v_mfma_f32_16x16x32_bf16 v[184:187], v[72:75], v[64:67], v[184:187]\n\t" \
  "ds_read_b64_tr_b16 v[88:89], v55 offset:34816\n\t" \
  "v_mfma_f32_16x16x32_bf16 v[184:187], v[108:111], v[104:107], v[184:187]\n\t" \
  "ds_read_b64_tr_b16 v[90:91], v55 offset:35328\n\t" \
  "v_mfma_f32_16x16x32_bf16 v[192:195], v[108:111], v[112:115], v[192:195]\n\t" \
  "ds_read_b64_tr_b16 v[92:93], v55 offset:38912\n\t" \
  "v_mfma_f32_16x16x32_bf16 v[192:195], v[72:75], v[116:119], v[192:195]\n\t" \
  "ds_read_b64_tr_b16 v[94:95], v55 offset:39424\n\t" \
  "v_mfma_f32_16x16x32_bf16 v[200:203], v[72:75], v[120:123], v[200:203]\n\t" \
  "ds_read_b64_tr_b16 v[96:97], v54 offset:18432\n\t" \
  "v_mfma_f32_16x16x32_bf16 v[200:203], v[108:111], v[124:127], v[200:203]\n\t" \
  "ds_read_b64_tr_b16 v[98:99], v54 offset:18944\n\t" \
  "v_mfma_f32_16x16x32_bf16 v[208:211], v[108:111], v[128:131], v[208:211]\n\t" \
  "ds_read_b64_tr_b16 v[148:149], v54 offset:22528\n\t" \
  "v_mfma_f32_16x16x32_bf16 v[208:211], v[72:75], v[132:135], v[208:211]\n\t" \
  "ds_read_b64_tr_b16 v[150:151], v54 offset:23040\n\t" \
  "v_mfma_f32_16x16x32_bf16 v[188:191], v[140:143], v[64:67], v[188:191]\n\t" \
  "ds_read_b64_tr_b16 v[100:101], v55 offset:18432\n\t" \
  "s_waitcnt lgkmcnt(7)\n\t" \
  "v_mfma_f32_16x16x32_bf16 v[188:191], v[136:139], v[104:107], v[188:191]\n\t" \
  "ds_read_b64_tr_b16 v[102:103], v55 offset:18944\n\t" \
  "v_mfma_f32_16x16x32_bf16 v[196:199], v[136:139], v[112:115], v[196:199]\n\t" \
  "ds_read_b64_tr_b16 v[144:145], v55 offset:22528\n\t" \
  "v_mfma_f32_16x16x32_bf16 v[196:199], v[140:143], v[116:119], v[196:199]\n\t" \
  "ds_read_b64_tr_b16 v[146:147], v55 offset:23040\n\t" \
  "v_mfma_f32_16x16x32_bf16 v[204:207], v[140:143], v[120:123], v[204:207]\n\t" \
  "v_mfma_f32_16x16x32_bf16 v[204:207], v[136:139], v[124:127], v[204:207]\n\t" \
  "v_mfma_f32_16x16x32_bf16 v[212:215], v[136:139], v[128:131], v[212:215]\n\t" \
  "v_mfma_f32_16x16x32_bf16 v[212:215], v[140:143], v[132:135], v[212:215]\n\t" \
  "v_mfma_f32_16x16x32_bf16 v[216:219], v[64:67], v[68:71], v[216:219]\n\t" \
  "v_mfma_f32_16x16x32_bf16 v[216:219], v[104:107], v[76:79], v[216:219]\n\t" \
  "v_mfma_f32_16x16x32_bf16 v[224:227], v[112:115], v[76:79], v[224:227]\n\t" \
  "v_mfma_f32_16x16x32_bf16 v[224:227], v[116:119], v[68:71], v[224:227]\n\t" \
  "v_mfma_f32_16x16x32_bf16 v[232:235], v[120:123], v[68:71], v[232:235]\n\t" \
  "v_mfma_f32_16x16x32_bf16 v[232:235], v[124:127], v[76:79], v[232:235]\n\t" \
  "v_mfma_f32_16x16x32_bf16 v[240:243], v[128:131], v[76:79], v[240:243]\n\t" \
  "v_mfma_f32_16x16x32_bf16 v[240:243], v[132:135], v[68:71], v[240:243]\n\t" \
  "v_mfma_f32_16x16x32_bf16 v[248:251], v[80:83], v[68:71], v[248:251]\n\t" \
  "v_mfma_f32_16x16x32_bf16 v[248:251], v[84:87], v[76:79], v[248:251]\n\t" \
  "v_mfma_f32_16x16x32_bf16 v[252:255], v[80:83], v[88:91], v[252:255]\n\t" \
  "s_waitcnt lgkmcnt(0)\n\t" \
  "v_mfma_f32_16x16x32_bf16 v[252:255], v[84:87], v[92:95], v[252:255]\n\t" \
  "v_mfma_f32_16x16x32_bf16 v[220:223], v[104:107], v[92:95], v[220:223]\n\t" \
  "v_mfma_f32_16x16x32_bf16 v[220:223], v[64:67], v[88:91], v[220:223]\n\t" \
  "s_barrier\n\t" \
  "v_mfma_f32_16x16x32_bf16 v[228:231], v[116:119], v[88:91], v[228:231]\n\t" \
  "ds_read_b64_tr_b16 v[108:109], v58 offset:26624\n\t" \
  "v_mfma_f32_16x16x32_bf16 v[228:231], v[112:115], v[92:95], v[228:231]\n\t" \
  "ds_read_b64_tr_b16 v[110:111], v58 offset:27136\n\t" \
  "v_mfma_f32_16x16x32_bf16 v[236:239], v[124:127], v[92:95], v[236:239]\n\t" \
  "ds_read_b128 v[72:75], v56 offset:10240\n\t" \
  "v_mfma_f32_16x16x32_bf16 v[236:239], v[120:123], v[88:91], v[236:239]\n\t" \
  "ds_read_b64_tr_b16 v[136:137], v58 offset:30720\n\t" \
  "v_mfma_f32_16x16x32_bf16 v[244:247], v[132:135], v[88:91], v[244:247]\n\t" \
  "ds_read_b64_tr_b16 v[138:139], v58 offset:31232\n\t" \
  "v_mfma_f32_16x16x32_bf16 v[244:247], v[128:131], v[92:95], v[244:247]\n\t" \
  "ds_read_b128 v[140:143], v57 offset:10240\n\t" \
  "v_mfma_f32_16x16x32_bf16 v[152:155], v[96:99], v[64:67], v[152:155]\n\t" \
  "ds_read_b128 v[68:71], v57 offset:12288\n\t" \
  "v_mfma_f32_16x16x32_bf16 v[152:155], v[148:151], v[104:107], v[152:155]\n\t" \
  "ds_read_b128 v[76:79], v56 offset:12288\n\t" \
  "v_mfma_f32_16x16x32_bf16 v[160:163], v[148:151], v[112:115], v[160:163]\n\t" \
  "ds_read_b128 v[80:83], v56 offset:14336\n\t" \
  "v_mfma_f32_16x16x32_bf16 v[160:163], v[96:99], v[116:119], v[160:163]\n\t" \
  "ds_read_b128 v[84:87], v57 offset:14336\n\t" \
  "v_mfma_f32_16x16x32_bf16 v[168:171], v[96:99], v[120:123], v[168:171]\n\t" \
  "ds_read_b128 v[88:91], v57 offset:16384\n\t" \
  "v_mfma_f32_16x16x32_bf16 v[168:171], v[148:151], v[124:127], v[168:171]\n\t" \
  "ds_read_b128 v[92:95], v56 offset:16384\n\t" \
  "v_mfma_f32_16x16x32_bf16 v[176:179], v[148:151], v[128:131], v[176:179]\n\t" \
  "ds_read_b64_tr_b16 v[148:149], v59 offset:26624\n\t" \
  "v_mfma_f32_16x16x32_bf16 v[176:179], v[96:99], v[132:135], v[176:179]\n\t" \
  "ds_read_b64_tr_b16 v[150:151], v59 offset:27136\n\t" \
  "v_mfma_f32_16x16x32_bf16 v[156:159], v[100:103], v[64:67], v[156:159]\n\t" \
  "ds_read_b64_tr_b16 v[96:97], v59 offset:30720\n\t" \
  "v_mfma_f32_16x16x32_bf16 v[156:159], v[144:147], v[104:107], v[156:159]\n\t" \
  "ds_read_b64_tr_b16 v[98:99], v59 offset:31232\n\t" \
  "v_mfma_f32_16x16x32_bf16 v[164:167], v[144:147], v[112:115], v[164:167]\n\t" \
  "ds_read_b64_tr_b16 v[64:65], v58 offset:34816\n\t" \
  "v_mfma_f32_16x16x32_bf16 v[164:167], v[100:103], v[116:119], v[164:167]\n\t" \
  "ds_read_b64_tr_b16 v[66:67], v58 offset:35328\n\t" \
  "v_mfma_f32_16x16x32_bf16 v[172:175], v[100:103], v[120:123], v[172:175]\n\t" \
  "ds_read_b64_tr_b16 v[104:105], v58 offset:38912\n\t" \
  "v_mfma_f32_16x16x32_bf16 v[172:175], v[144:147], v[124:127], v[172:175]\n\t" \
  "ds_read_b64_tr_b16 v[106:107], v58 offset:39424\n\t" \
  "v_mfma_f32_16x16x32_bf16 v[180:183], v[144:147], v[128:131], v[180:183]\n\t" \
  "ds_read_b128 v[112:115], v56 offset:0\n\t" \
  "v_mfma_f32_16x16x32_bf16 v[180:183], v[100:103], v[132:135], v[180:183]\n\t" \
  "ds_read_b128 v[116:119], v57 offset:0\n\t" \
  "s_waitcnt lgkmcnt(7)\n\t" \
  "v_mfma_f32_16x16x32_bf16 v[184:187], v[108:111], v[72:75], v[184:187]\n\t" \
  "ds_read_b64_tr_b16 v[120:121], v59 offset:34816\n\t" \
  "v_mfma_f32_16x16x32_bf16 v[184:187], v[136:139], v[140:143], v[184:187]\n\t" \
  "ds_read_b64_tr_b16 v[122:123], v59 offset:35328\n\t" \
  "v_mfma_f32_16x16x32_bf16 v[192:195], v[136:139], v[68:71], v[192:195]\n\t" \
  "ds_read_b64_tr_b16 v[124:125], v59 offset:38912\n\t" \
  "v_mfma_f32_16x16x32_bf16 v[192:195], v[108:111], v[76:79], v[192:195]\n\t" \
  "ds_read_b64_tr_b16 v[126:127], v59 offset:39424\n\t" \
  "v_mfma_f32_16x16x32_bf16 v[200:203], v[108:111], v[80:83], v[200:203]\n\t" \
  "ds_read_b64_tr_b16 v[128:129], v58 offset:18432\n\t" \
  "v_mfma_f32_16x16x32_bf16 v[200:203], v[136:139], v[84:87], v[200:203]\n\t" \
  "ds_read_b64_tr_b16 v[130:131], v58 offset:18944\n\t" \
  "v_mfma_f32_16x16x32_bf16 v[208:211], v[136:139], v[88:91], v[208:211]\n\t" \
  "ds_read_b64_tr_b16 v[144:145], v58 offset:22528\n\t" \
  "v_mfma_f32_16x16x32_bf16 v[208:211], v[108:111], v[92:95], v[208:211]\n\t" \
  "ds_read_b64_tr_b16 v[146:147], v58 offset:23040\n\t" \
  "v_mfma_f32_16x16x32_bf16 v[188:191], v[148:151], v[72:75], v[188:191]\n\t" \
  "ds_read_b64_tr_b16 v[100:101], v59 offset:18432\n\t" \
  "s_waitcnt lgkmcnt(7)\n\t" \
  "v_mfma_f32_16x16x32_bf16 v[188:191], v[96:99], v[140:143], v[188:191]\n\t" \
  "ds_read_b64_tr_b16 v[102:103], v59 offset:18944\n\t" \
  "v_mfma_f32_16x16x32_bf16 v[196:199], v[96:99], v[68:71], v[196:199]\n\t" \
  "ds_read_b64_tr_b16 v[132:133], v59 offset:22528\n\t" \
  "v_mfma_f32_16x16x32_bf16 v[196:199], v[148:151], v[76:79], v[196:199]\n\t" \
  "ds_read_b64_tr_b16 v[134:135], v59 offset:23040\n\t" \
  "v_mfma_f32_16x16x32_bf16 v[204:207], v[148:151], v[80:83], v[204:207]\n\t" \
  "v_mfma_f32_16x16x32_bf16 v[204:207], v[96:99], v[84:87], v[204:207]\n\t" \
  "v_mfma_f32_16x16x32_bf16 v[212:215], v[96:99], v[88:91], v[212:215]\n\t" \
  "v_mfma_f32_16x16x32_bf16 v[212:215], v[148:151], v[92:95], v[212:215]\n\t" \
  "v_mfma_f32_16x16x32_bf16 v[216:219], v[72:75], v[64:67], v[216:219]\n\t" \
  "v_mfma_f32_16x16x32_bf16 v[216:219], v[140:143], v[104:107], v[216:219]\n\t" \
  "v_mfma_f32_16x16x32_bf16 v[224:227], v[68:71], v[104:107], v[224:227]\n\t" \
  "v_mfma_f32_16x16x32_bf16 v[224:227], v[76:79], v[64:67], v[224:227]\n\t" \
  "v_mfma_f32_16x16x32_bf16 v[232:235], v[80:83], v[64:67], v[232:235]\n\t" \
  "v_mfma_f32_16x16x32_bf16 v[232:235], v[84:87], v[104:107], v[232:235]\n\t" \
  "v_mfma_f32_16x16x32_bf16 v[240:243], v[88:91], v[104:107], v[240:243]\n\t" \
  "v_mfma_f32_16x16x32_bf16 v[240:243], v[92:95], v[64:67], v[240:243]\n\t" \
  "v_mfma_f32_16x16x32_bf16 v[248:251], v[112:115], v[64:67], v[248:251]\n\t" \
  "v_mfma_f32_16x16x32_bf16 v[248:251], v[116:119], v[104:107], v[248:251]\n\t" \
  "v_mfma_f32_16x16x32_bf16 v[252:255], v[112:115], v[120:123], v[252:255]\n\t" \
  "s_waitcnt lgkmcnt(0)\n\t" \
  "v_mfma_f32_16x16x32_bf16 v[252:255], v[116:119], v[124:127], v[252:255]\n\t" \
  "v_mfma_f32_16x16x32_bf16 v[220:223], v[140:143], v[124:127], v[220:223]\n\t" \
  "v_mfma_f32_16x16x32_bf16 v[220:223], v[72:75], v[120:123], v[220:223]\n\t" \
  "s_barrier\n\t" \
  "v_mfma_f32_16x16x32_bf16 v[228:231], v[76:79], v[120:123], v[228:231]\n\t" \
  "ds_read_b64_tr_b16 v[136:137], v62 offset:26624\n\t" \
  "v_bfe_u32 v30, v4, 9, 1\n\t" \
  "v_mfma_f32_16x16x32_bf16 v[228:231], v[68:71], v[124:127], v[228:231]\n\t" \
  "ds_read_b64_tr_b16 v[138:139], v62 offset:27136\n\t" \
  "v_mad_i32_i24 v30, v30, -16, v0\n\t" \
  "v_mfma_f32_16x16x32_bf16 v[236:239], v[84:87], v[124:127], v[236:239]\n\t" \
  "ds_read_b128 v[108:111], v60 offset:10240\n\t" \
  "ds_read_b128 v[26:29], v0 offset:1552\n\t" \
  "v_mfma_f32_16x16x32_bf16 v[236:239], v[80:83], v[120:123], v[236:239]\n\t" \
  "ds_read_b64_tr_b16 v[96:97], v62 offset:30720\n\t" \
  "ds_read_b128 v[30:33], v30 offset:1040\n\t" \
  "v_mfma_f32_16x16x32_bf16 v[244:247], v[92:95], v[120:123], v[244:247]\n\t" \
  "ds_read_b64_tr_b16 v[98:99], v62 offset:31232\n\t" \
  "ds_read_b128 v[34:37], v0 offset:512\n\t" \
  "v_mfma_f32_16x16x32_bf16 v[244:247], v[88:91], v[124:127], v[244:247]\n\t" \
  "ds_read_b128 v[148:151], v61 offset:10240\n\t" \
  "ds_read_b64_tr_b16 v[38:39], v1 offset:512\n\t" \
  "v_mfma_f32_16x16x32_bf16 v[152:155], v[128:131], v[72:75], v[152:155]\n\t" \
  "ds_read_b128 v[64:67], v61 offset:12288\n\t" \
  "ds_read_b64_tr_b16 v[40:41], v1 offset:0\n\t" \
  "v_mfma_f32_16x16x32_bf16 v[152:155], v[144:147], v[140:143], v[152:155]\n\t" \
  "ds_read_b128 v[104:107], v60 offset:12288\n\t" \
  "s_waitcnt vmcnt(0)\n\t" \
  "v_mfma_f32_16x16x32_bf16 v[160:163], v[144:147], v[68:71], v[160:163]\n\t" \
  "ds_read_b128 v[112:115], v60 offset:14336\n\t" \
  "v_perm_b32 v18, v10, v10, v14\n\t" \
  "v_mfma_f32_16x16x32_bf16 v[160:163], v[128:131], v[76:79], v[160:163]\n\t" \
  "ds_read_b128 v[116:119], v61 offset:14336\n\t" \
  "v_perm_b32 v19, v10, v10, v15\n\t" \
  "v_mfma_f32_16x16x32_bf16 v[168:171], v[128:131], v[80:83], v[168:171]\n\t" \
  "ds_read_b128 v[120:123], v61 offset:16384\n\t" \
  "v_perm_b32 v20, v10, v10, v16\n\t" \
  "v_mfma_f32_16x16x32_bf16 v[168:171], v[144:147], v[84:87], v[168:171]\n\t" \
  "ds_read_b128 v[124:127], v60 offset:16384\n\t" \
  "v_perm_b32 v21, v10, v10, v17\n\t" \
  "v_mfma_f32_16x16x32_bf16 v[176:179], v[144:147], v[88:91], v[176:179]\n\t" \
  "ds_read_b64_tr_b16 v[144:145], v63 offset:26624\n\t" \
  "v_perm_b32 v22, v11, v11, v14\n\t" \
  "v_mfma_f32_16x16x32_bf16 v[176:179], v[128:131], v[92:95], v[176:179]\n\t" \
  "ds_read_b64_tr_b16 v[146:147], v63 offset:27136\n\t" \
  "v_perm_b32 v23, v11, v11, v15\n\t" \
  "v_mfma_f32_16x16x32_bf16 v[156:159], v[100:103], v[72:75], v[156:159]\n\t" \
  "ds_read_b64_tr_b16 v[128:129], v63 offset:30720\n\t" \
  "v_perm_b32 v24, v11, v11, v16\n\t" \
  "v_mfma_f32_16x16x32_bf16 v[156:159], v[132:135], v[140:143], v[156:159]\n\t" \
  "ds_read_b64_tr_b16 v[130:131], v63 offset:31232\n\t" \
  "v_perm_b32 v25, v11, v11, v17\n\t" \
  "v_mfma_f32_16x16x32_bf16 v[164:167], v[132:135], v[68:71], v[164:167]\n\t" \
  "ds_read_b64_tr_b16 v[72:73], v62 offset:34816\n\t" \
  "v_mfma_f32_16x16x32_bf16 v[164:167], v[100:103], v[76:79], v[164:167]\n\t" \
  "ds_read_b64_tr_b16 v[74:75], v62 offset:35328\n\t" \
  "v_mfma_f32_16x16x32_bf16 v[172:175], v[100:103], v[80:83], v[172:175]\n\t" \
  "ds_read_b64_tr_b16 v[140:141], v62 offset:38912\n\t" \
  "s_waitcnt lgkmcnt(6)\n\t" \
  "v_mfma_f32_16x16x32_bf16 v[42:45], v[26:29], v[18:21], 0\n\t" \
  "v_mfma_f32_16x16x32_bf16 v[42:45], v[30:33], v[22:25], v[42:45]\n\t" \
  "v_mfma_f32_16x16x32_bf16 v[46:49], v[30:33], v[18:21], 0\n\t" \
  "v_mfma_f32_16x16x32_bf16 v[46:49], v[34:37], v[22:25], v[46:49]\n\t" \
  "ds_read_b128 v[26:29], v0 offset:1808\n\t" \
  "ds_read_b128 v[30:33], v0 offset:1296\n\t" \
  "v_mfma_f32_16x16x32_bf16 v[172:175], v[132:135], v[84:87], v[172:175]\n\t" \
  "ds_read_b64_tr_b16 v[142:143], v62 offset:39424\n\t" \
  "ds_read_b128 v[34:37], v0 offset:768\n\t" \
  "v_mfma_f32_16x16x32_bf16 v[180:183], v[132:135], v[88:91], v[180:183]\n\t" \
  "ds_read_b128 v[68:71], v60 offset:0\n\t" \
  "v_mfma_f32_16x16x32_bf16 v[180:183], v[100:103], v[92:95], v[180:183]\n\t" \
  "ds_read_b128 v[76:79], v61 offset:0\n\t" \
  "v_mfma_f32_16x16x32_bf16 v[184:187], v[136:139], v[108:111], v[184:187]\n\t" \
  "ds_read_b64_tr_b16 v[80:81], v63 offset:34816\n\t" \
  "v_lshlrev_b32 v50, 16, v38\n\t" \
  "v_mfma_f32_16x16x32_bf16 v[184:187], v[96:99], v[148:151], v[184:187]\n\t" \
  "ds_read_b64_tr_b16 v[82:83], v63 offset:35328\n\t" \
  "v_lshlrev_b32 v51, 16, v40\n\t" \
  "v_mfma_f32_16x16x32_bf16 v[192:195], v[96:99], v[64:67], v[192:195]\n\t" \
  "ds_read_b64_tr_b16 v[84:85], v63 offset:38912\n\t" \
  "v_mul_f32 v42, v42, v50\n\t" \
  "v_mfma_f32_16x16x32_bf16 v[192:195], v[136:139], v[104:107], v[192:195]\n\t" \
  "ds_read_b64_tr_b16 v[86:87], v63 offset:39424\n\t" \
  "v_mul_f32 v46, v46, v51\n\t" \
  "v_mfma_f32_16x16x32_bf16 v[200:203], v[136:139], v[112:115], v[200:203]\n\t" \
  "ds_read_b64_tr_b16 v[88:89], v62 offset:18432\n\t" \
  "v_cvt_pk_bf16_f32 v42, v42, v46\n\t" \
  "v_mfma_f32_16x16x32_bf16 v[200:203], v[96:99], v[116:119], v[200:203]\n\t" \
  "ds_read_b64_tr_b16 v[90:91], v62 offset:18944\n\t" \
  "v_and_b32 v50, 0xffff0000, v38\n\t" \
  "v_mfma_f32_16x16x32_bf16 v[208:211], v[96:99], v[120:123], v[208:211]\n\t" \
  "ds_read_b64_tr_b16 v[132:133], v62 offset:22528\n\t" \
  "v_and_b32 v51, 0xffff0000, v40\n\t" \
  "v_mfma_f32_16x16x32_bf16 v[208:211], v[136:139], v[124:127], v[208:211]\n\t" \
  "ds_read_b64_tr_b16 v[134:135], v62 offset:23040\n\t" \
  "v_mul_f32 v43, v43, v50\n\t" \
  "s_waitcnt lgkmcnt(7)\n\t" \
  "v_mfma_f32_16x16x32_bf16 v[188:191], v[144:147], v[108:111], v[188:191]\n\t" \
  "ds_read_b64_tr_b16 v[92:93], v63 offset:18432\n\t" \
  "v_mul_f32 v47, v47, v51\n\t" \
  "v_mfma_f32_16x16x32_bf16 v[188:191], v[128:131], v[148:151], v[188:191]\n\t" \
  "ds_read_b64_tr_b16 v[94:95], v63 offset:18944\n\t" \
  "v_cvt_pk_bf16_f32 v43, v43, v47\n\t" \
  "v_mfma_f32_16x16x32_bf16 v[196:199], v[128:131], v[64:67], v[196:199]\n\t" \
  "ds_read_b64_tr_b16 v[100:101], v63 offset:22528\n\t" \
  "v_lshlrev_b32 v50, 16, v39\n\t" \
  "v_mfma_f32_16x16x32_bf16 v[196:199], v[144:147], v[104:107], v[196:199]\n\t" \
  "ds_read_b64_tr_b16 v[102:103], v63 offset:23040\n\t" \
  "v_lshlrev_b32 v51, 16, v41\n\t" \
  "v_mfma_f32_16x16x32_bf16 v[204:207], v[144:147], v[112:115], v[204:207]\n\t" \
  "v_mul_f32 v44, v44, v50\n\t" \
  "v_mul_f32 v48, v48, v51\n\t" \
  "v_mfma_f32_16x16x32_bf16 v[204:207], v[128:131], v[116:119], v[204:207]\n\t" \
  "v_cvt_pk_bf16_f32 v44, v44, v48\n\t" \
  "v_and_b32 v50, 0xffff0000, v39\n\t" \
  "v_mfma_f32_16x16x32_bf16 v[212:215], v[128:131], v[120:123], v[212:215]\n\t" \
  "v_and_b32 v51, 0xffff0000, v41\n\t" \
  "v_mul_f32 v45, v45, v50\n\t" \
  "v_mfma_f32_16x16x32_bf16 v[212:215], v[144:147], v[124:127], v[212:215]\n\t" \
  "v_mul_f32 v49, v49, v51\n\t" \
  "ds_read_b64_tr_b16 v[38:39], v1 offset:768\n\t" \
  "v_mfma_f32_16x16x32_bf16 v[216:219], v[108:111], v[72:75], v[216:219]\n\t" \
  "ds_read_b64_tr_b16 v[40:41], v1 offset:256\n\t" \
  "v_cvt_pk_bf16_f32 v45, v45, v49\n\t" \
  "v_mfma_f32_16x16x32_bf16 v[216:219], v[148:151], v[140:143], v[216:219]\n\t" \
  "v_mfma_f32_16x16x32_bf16 v[224:227], v[64:67], v[140:143], v[224:227]\n\t" \
  "v_mfma_f32_16x16x32_bf16 v[224:227], v[104:107], v[72:75], v[224:227]\n\t" \
  "v_mfma_f32_16x16x32_bf16 v[232:235], v[112:115], v[72:75], v[232:235]\n\t" \
  "v_mfma_f32_16x16x32_bf16 v[232:235], v[116:119], v[140:143], v[232:235]\n\t" \
  "v_mfma_f32_16x16x32_bf16 v[240:243], v[120:123], v[140:143], v[240:243]\n\t" \
  "v_mfma_f32_16x16x32_bf16 v[240:243], v[124:127], v[72:75], v[240:243]\n\t" \
  "v_mfma_f32_16x16x32_bf16 v[248:251], v[68:71], v[72:75], v[248:251]\n\t" \
  "v_mfma_f32_16x16x32_bf16 v[248:251], v[76:79], v[140:143], v[248:251]\n\t" \
  "s_waitcnt lgkmcnt(0)\n\t" \
  "v_mfma_f32_16x16x32_bf16 v[252:255], v[68:71], v[80:83], v[252:255]\n\t" \
  "v_mfma_f32_16x16x32_bf16 v[252:255], v[76:79], v[84:87], v[252:255]\n\t" \
  "v_mfma_f32_16x16x32_bf16 v[220:223], v[148:151], v[84:87], v[220:223]\n\t" \
  "v_mfma_f32_16x16x32_bf16 v[220:223], v[108:111], v[80:83], v[220:223]\n\t" \
  "s_barrier\n\t" \
  "v_mfma_f32_16x16x32_bf16 v[228:231], v[104:107], v[80:83], v[228:231]\n\t" \
  "ds_read_b64_tr_b16 v[96:97], v54 offset:26624\n\t" \
  "v_mfma_f32_16x16x32_bf16 v[228:231], v[64:67], v[84:87], v[228:231]\n\t" \
  "v_mov_b32 v50, 163344\n\t" \
  "ds_read_b64_tr_b16 v[98:99], v54 offset:27136\n\t" \
  "v_mfma_f32_16x16x32_bf16 v[236:239], v[116:119], v[84:87], v[236:239]\n\t" \
  "ds_read_b32 v50, v50 offset:24\n\t" \
  "ds_read_b128 v[136:139], v52 offset:10240\n\t" \
  "v_mfma_f32_16x16x32_bf16 v[236:239], v[112:115], v[80:83], v[236:239]\n\t" \
  "ds_read_b64_tr_b16 v[128:129], v54 offset:30720\n\t" \
  "v_mfma_f32_16x16x32_bf16 v[244:247], v[124:127], v[80:83], v[244:247]\n\t" \
  "ds_read_b64_tr_b16 v[130:131], v54 offset:31232\n\t" \
  "v_mfma_f32_16x16x32_bf16 v[244:247], v[120:123], v[84:87], v[244:247]\n\t" \
  "ds_read_b128 v[144:147], v53 offset:10240\n\t" \
  "v_mfma_f32_16x16x32_bf16 v[152:155], v[88:91], v[108:111], v[152:155]\n\t" \
  "ds_read_b128 v[72:75], v53 offset:12288\n\t" \
  "v_mfma_f32_16x16x32_bf16 v[152:155], v[132:135], v[148:151], v[152:155]\n\t" \
  "ds_read_b128 v[140:143], v52 offset:12288\n\t" \
  "v_mfma_f32_16x16x32_bf16 v[160:163], v[132:135], v[64:67], v[160:163]\n\t" \
  "ds_read_b128 v[68:71], v52 offset:14336\n\t" \
  "v_mfma_f32_16x16x32_bf16 v[160:163], v[88:91], v[104:107], v[160:163]\n\t" \
  "ds_read_b128 v[76:79], v53 offset:14336\n\t" \
  "v_mfma_f32_16x16x32_bf16 v[168:171], v[88:91], v[112:115], v[168:171]\n\t" \
  "ds_read_b128 v[80:83], v53 offset:16384\n\t" \
  "v_mfma_f32_16x16x32_bf16 v[168:171], v[132:135], v[116:119], v[168:171]\n\t" \
  "ds_read_b128 v[84:87], v52 offset:16384\n\t" \
  "v_mfma_f32_16x16x32_bf16 v[176:179], v[132:135], v[120:123], v[176:179]\n\t" \
  "s_waitcnt lgkmcnt(10)\n\t" \
  "v_readfirstlane_b32 s88, v50\n\t" \
  "s_mov_b32 s89, 0\n\t" \
  "ds_read_b64_tr_b16 v[132:133], v55 offset:26624\n\t" \
  "v_mfma_f32_16x16x32_bf16 v[176:179], v[88:91], v[124:127], v[176:179]\n\t" \
  "ds_read_b64_tr_b16 v[134:135], v55 offset:27136\n\t" \
  "v_mfma_f32_16x16x32_bf16 v[156:159], v[92:95], v[108:111], v[156:159]\n\t" \
  "s_xor_b64 %24, %24, s[88:89]\n\t" \
  "ds_read_b64_tr_b16 v[88:89], v55 offset:30720\n\t" \
  "v_mfma_f32_16x16x32_bf16 v[156:159], v[100:103], v[148:151], v[156:159]\n\t" \
  "s_xor_b64 %25, %25, s[88:89]\n\t" \
  "ds_read_b64_tr_b16 v[90:91], v55 offset:31232\n\t" \
  "v_mfma_f32_16x16x32_bf16 v[164:167], v[100:103], v[64:67], v[164:167]\n\t" \
  "s_xor_b64 %14, %14, s[88:89]\n\t" \
  "ds_read_b64_tr_b16 v[108:109], v54 offset:34816\n\t" \
  "v_mfma_f32_16x16x32_bf16 v[164:167], v[92:95], v[104:107], v[164:167]\n\t" \
  "s_xor_b64 %19, %19, s[88:89]\n\t" \
  "ds_read_b64_tr_b16 v[110:111], v54 offset:35328\n\t" \
  "v_mfma_f32_16x16x32_bf16 v[172:175], v[92:95], v[112:115], v[172:175]\n\t" \
  "s_xor_b64 %21, %21, s[88:89]\n\t" \
  "ds_read_b64_tr_b16 v[148:149], v54 offset:38912\n\t" \
  "v_mfma_f32_16x16x32_bf16 v[172:175], v[100:103], v[116:119], v[172:175]\n\t" \
  "s_xor_b64 s[92:93], s[92:93], s[88:89]\n\t" \
  "ds_read_b64_tr_b16 v[150:151], v54 offset:39424\n\t" \
  "v_mfma_f32_16x16x32_bf16 v[180:183], v[100:103], v[120:123], v[180:183]\n\t" \
  "ds_read_b128 v[64:67], v52 offset:0\n\t" \
  "v_mfma_f32_16x16x32_bf16 v[180:183], v[92:95], v[124:127], v[180:183]\n\t" \
  "ds_read_b128 v[104:107], v53 offset:0\n\t" \
  "s_waitcnt lgkmcnt(7)\n\t" \
  "v_mfma_f32_16x16x32_bf16 v[184:187], v[96:99], v[136:139], v[184:187]\n\t" \
  "ds_read_b64_tr_b16 v[112:113], v55 offset:34816\n\t" \
  "v_mfma_f32_16x16x32_bf16 v[184:187], v[128:131], v[144:147], v[184:187]\n\t" \
  "ds_read_b64_tr_b16 v[114:115], v55 offset:35328\n\t" \
  "v_mfma_f32_16x16x32_bf16 v[192:195], v[128:131], v[72:75], v[192:195]\n\t" \
  "ds_read_b64_tr_b16 v[116:117], v55 offset:38912\n\t" \
  "v_mfma_f32_16x16x32_bf16 v[192:195], v[96:99], v[140:143], v[192:195]\n\t" \
  "ds_read_b64_tr_b16 v[118:119], v55 offset:39424\n\t" \
  "v_mfma_f32_16x16x32_bf16 v[200:203], v[96:99], v[68:71], v[200:203]\n\t" \
  "ds_read_b64_tr_b16 v[100:101], v54 offset:18432\n\t" \
  "v_mfma_f32_16x16x32_bf16 v[200:203], v[128:131], v[76:79], v[200:203]\n\t" \
  "ds_read_b64_tr_b16 v[102:103], v54 offset:18944\n\t" \
  "v_mfma_f32_16x16x32_bf16 v[208:211], v[128:131], v[80:83], v[208:211]\n\t" \
  "ds_read_b64_tr_b16 v[120:121], v54 offset:22528\n\t" \
  "v_mfma_f32_16x16x32_bf16 v[208:211], v[96:99], v[84:87], v[208:211]\n\t" \
  "ds_read_b64_tr_b16 v[122:123], v54 offset:23040\n\t" \
  "v_mfma_f32_16x16x32_bf16 v[188:191], v[132:135], v[136:139], v[188:191]\n\t" \
  "ds_read_b64_tr_b16 v[92:93], v55 offset:18432\n\t" \
  "s_waitcnt lgkmcnt(7)\n\t" \
  "v_mfma_f32_16x16x32_bf16 v[188:191], v[88:91], v[144:147], v[188:191]\n\t" \
  "ds_read_b64_tr_b16 v[94:95], v55 offset:18944\n\t" \
  "v_mfma_f32_16x16x32_bf16 v[196:199], v[88:91], v[72:75], v[196:199]\n\t" \
  "ds_read_b64_tr_b16 v[124:125], v55 offset:22528\n\t" \
  "v_mfma_f32_16x16x32_bf16 v[196:199], v[132:135], v[140:143], v[196:199]\n\t" \
  "ds_read_b64_tr_b16 v[126:127], v55 offset:23040\n\t" \
  "v_mfma_f32_16x16x32_bf16 v[204:207], v[132:135], v[68:71], v[204:207]\n\t" \
  "v_mfma_f32_16x16x32_bf16 v[204:207], v[88:91], v[76:79], v[204:207]\n\t" \
  "v_mfma_f32_16x16x32_bf16 v[212:215], v[88:91], v[80:83], v[212:215]\n\t" \
  "v_mfma_f32_16x16x32_bf16 v[212:215], v[132:135], v[84:87], v[212:215]\n\t" \
  "v_mfma_f32_16x16x32_bf16 v[216:219], v[136:139], v[108:111], v[216:219]\n\t" \
  "v_mfma_f32_16x16x32_bf16 v[216:219], v[144:147], v[148:151], v[216:219]\n\t" \
  "v_mfma_f32_16x16x32_bf16 v[224:227], v[72:75], v[148:151], v[224:227]\n\t" \
  "v_mfma_f32_16x16x32_bf16 v[224:227], v[140:143], v[108:111], v[224:227]\n\t" \
  "v_mfma_f32_16x16x32_bf16 v[232:235], v[68:71], v[108:111], v[232:235]\n\t" \
  "global_store_dwordx4 v2, v[42:45], %24 offset:0 sc1\n\t" \
  "v_mfma_f32_16x16x32_bf16 v[232:235], v[76:79], v[148:151], v[232:235]\n\t" \
  "s_nop 0\n\t" \
  "v_mfma_f32_16x16x32_bf16 v[42:45], v[26:29], v[18:21], 0\n\t" \
  "v_mfma_f32_16x16x32_bf16 v[42:45], v[30:33], v[22:25], v[42:45]\n\t" \
  "v_mfma_f32_16x16x32_bf16 v[46:49], v[30:33], v[18:21], 0\n\t" \
  "v_mfma_f32_16x16x32_bf16 v[46:49], v[34:37], v[22:25], v[46:49]\n\t" \
  "v_bfe_u32 v30, v4, 9, 1\n\t" \
  "v_mad_i32_i24 v30, v30, -16, v0\n\t" \
  "v_mfma_f32_16x16x32_bf16 v[240:243], v[80:83], v[148:151], v[240:243]\n\t" \
  "ds_read_b128 v[26:29], v0 offset:1584\n\t" \
  "ds_read_b128 v[30:33], v30 offset:1072\n\t" \
  "v_mfma_f32_16x16x32_bf16 v[240:243], v[84:87], v[108:111], v[240:243]\n\t" \
  "ds_read_b128 v[34:37], v0 offset:544\n\t" \
  "v_perm_b32 v18, v12, v12, v14\n\t" \
  "v_mfma_f32_16x16x32_bf16 v[248:251], v[64:67], v[108:111], v[248:251]\n\t" \
  "v_perm_b32 v19, v12, v12, v15\n\t" \
  "v_perm_b32 v20, v12, v12, v16\n\t" \
  "v_mfma_f32_16x16x32_bf16 v[248:251], v[104:107], v[148:151], v[248:251]\n\t" \
  "v_perm_b32 v21, v12, v12, v17\n\t" \
  "v_perm_b32 v22, v13, v13, v14\n\t" \
  "v_mfma_f32_16x16x32_bf16 v[252:255], v[64:67], v[112:115], v[252:255]\n\t" \
  "v_perm_b32 v23, v13, v13, v15\n\t" \
  "v_perm_b32 v24, v13, v13, v16\n\t" \
  "s_waitcnt lgkmcnt(3)\n\t" \
  "v_mfma_f32_16x16x32_bf16 v[252:255], v[104:107], v[116:119], v[252:255]\n\t" \
  "v_perm_b32 v25, v13, v13, v17\n\t" \
  "v_lshlrev_b32 v50, 16, v38\n\t" \
  "v_mfma_f32_16x16x32_bf16 v[220:223], v[144:147], v[116:119], v[220:223]\n\t" \
  "v_lshlrev_b32 v51, 16, v40\n\t" \
  "v_mul_f32 v42, v42, v50\n\t" \
  "v_mfma_f32_16x16x32_bf16 v[220:223], v[136:139], v[112:115], v[220:223]\n\t" \
  "v_mul_f32 v46, v46, v51\n\t" \
  "v_cvt_pk_bf16_f32 v42, v42, v46\n\t" \
  "s_waitcnt lgkmcnt(0)\n\t" \
  "s_barrier\n\t" \
  "v_mfma_f32_16x16x32_bf16 v[228:231], v[140:143], v[112:115], v[228:231]\n\t" \
  "ds_read_b64_tr_b16 v[128:129], v58 offset:26624\n\t" \
  "v_and_b32 v50, 0xffff0000, v38\n\t" \
  "v_mfma_f32_16x16x32_bf16 v[228:231], v[72:75], v[116:119], v[228:231]\n\t" \
  "ds_read_b64_tr_b16 v[130:131], v58 offset:27136\n\t" \
  "v_and_b32 v51, 0xffff0000, v40\n\t" \
  "v_mfma_f32_16x16x32_bf16 v[236:239], v[76:79], v[116:119], v[236:239]\n\t" \
  "ds_read_b128 v[96:99], v56 offset:10240\n\t" \
  "v_mul_f32 v43, v43, v50\n\t" \
  "v_mfma_f32_16x16x32_bf16 v[236:239], v[68:71], v[112:115], v[236:239]\n\t" \
  "ds_read_b64_tr_b16 v[88:89], v58 offset:30720\n\t" \
  "500:\n\t" \
  "s_cbranch_execz 504f\n\t" \
  "v_mul_f32 v47, v47, v51\n\t" \
  "v_mfma_f32_16x16x32_bf16 v[244:247], v[84:87], v[112:115], v[244:247]\n\t" \
  "ds_read_b64_tr_b16 v[90:91], v58 offset:31232\n\t" \
  "v_cvt_pk_bf16_f32 v43, v43, v47\n\t" \
  "v_mfma_f32_16x16x32_bf16 v[244:247], v[80:83], v[116:119], v[244:247]\n\t" \
  "ds_read_b128 v[132:135], v57 offset:10240\n\t" \
  "v_lshlrev_b32 v50, 16, v39\n\t" \
  "v_mfma_f32_16x16x32_bf16 v[152:155], v[100:103], v[136:139], v[152:155]\n\t" \
  "ds_read_b128 v[108:111], v57 offset:12288\n\t" \
  "v_lshlrev_b32 v51, 16, v41\n\t" \
  "v_mfma_f32_16x16x32_bf16 v[152:155], v[120:123], v[144:147], v[152:155]\n\t" \
  "ds_read_b128 v[148:151], v56 offset:12288\n\t" \
  "v_mul_f32 v44, v44, v50\n\t" \
  "v_mfma_f32_16x16x32_bf16 v[160:163], v[120:123], v[72:75], v[160:163]\n\t" \
  "ds_read_b128 v[64:67], v56 offset:14336\n\t" \
  "v_mul_f32 v48, v48, v51\n\t" \
  "v_mfma_f32_16x16x32_bf16 v[160:163], v[100:103], v[140:143], v[160:163]\n\t" \
  "ds_read_b128 v[104:107], v57 offset:14336\n\t" \
  "v_cvt_pk_bf16_f32 v44, v44, v48\n\t" \
  "v_mfma_f32_16x16x32_bf16 v[168:171], v[100:103], v[68:71], v[168:171]\n\t" \
  "ds_read_b128 v[112:115], v57 offset:16384\n\t" \
  "v_and_b32 v50, 0xffff0000, v39\n\t" \
  "v_mfma_f32_16x16x32_bf16 v[168:171], v[120:123], v[76:79], v[168:171]\n\t" \
  "ds_read_b128 v[116:119], v56 offset:16384\n\t" \
  "v_and_b32 v51, 0xffff0000, v41\n\t" \
  "v_mfma_f32_16x16x32_bf16 v[176:179], v[120:123], v[80:83], v[176:179]\n\t" \
  "ds_read_b64_tr_b16 v[120:121], v59 offset:26624\n\t" \
  "v_mul_f32 v45, v45, v50\n\t" \
  "v_mfma_f32_16x16x32_bf16 v[176:179], v[100:103], v[84:87], v[176:179]\n\t" \
  "ds_read_b64_tr_b16 v[122:123], v59 offset:27136\n\t" \
  "v_mul_f32 v49, v49, v51\n\t" \
  "v_mfma_f32_16x16x32_bf16 v[156:159], v[92:95], v[136:139], v[156:159]\n\t" \
  "ds_read_b64_tr_b16 v[100:101], v59 offset:30720\n\t" \
  "ds_read_b64_tr_b16 v[38:39], v1 offset:544\n\t" \
  "v_mfma_f32_16x16x32_bf16 v[156:159], v[124:127], v[144:147], v[156:159]\n\t" \
  "ds_read_b64_tr_b16 v[102:103], v59 offset:31232\n\t" \
  "ds_read_b64_tr_b16 v[40:41], v1 offset:32\n\t" \
  "v_mfma_f32_16x16x32_bf16 v[164:167], v[124:127], v[72:75], v[164:167]\n\t" \
  "ds_read_b64_tr_b16 v[136:137], v58 offset:34816\n\t" \
  "v_cvt_pk_bf16_f32 v45, v45, v49\n\t" \
  "v_mfma_f32_16x16x32_bf16 v[164:167], v[92:95], v[140:143], v[164:167]\n\t" \
  "ds_read_b64_tr_b16 v[138:139], v58 offset:35328\n\t" \
  "v_mfma_f32_16x16x32_bf16 v[172:175], v[92:95], v[68:71], v[172:175]\n\t" \
  "ds_read_b64_tr_b16 v[144:145], v58 offset:38912\n\t" \
  "v_mfma_f32_16x16x32_bf16 v[172:175], v[124:127], v[76:79], v[172:175]\n\t" \
  "ds_read_b64_tr_b16 v[146:147], v58 offset:39424\n\t" \
  "v_mfma_f32_16x16x32_bf16 v[180:183], v[124:127], v[80:83], v[180:183]\n\t" \
  "ds_read_b128 v[72:75], v56 offset:0\n\t" \
  "v_mfma_f32_16x16x32_bf16 v[180:183], v[92:95], v[84:87], v[180:183]\n\t" \
  "ds_read_b128 v[140:143], v57 offset:0\n\t" \
  "s_waitcnt lgkmcnt(8)\n\t" \
  "v_mfma_f32_16x16x32_bf16 v[184:187], v[128:131], v[96:99], v[184:187]\n\t" \
  "ds_read_b64_tr_b16 v[68:69], v59 offset:34816\n\t" \
  "v_mfma_f32_16x16x32_bf16 v[184:187], v[88:91], v[132:135], v[184:187]\n\t" \
  "ds_read_b64_tr_b16 v[70:71], v59 offset:35328\n\t" \
  "v_mfma_f32_16x16x32_bf16 v[192:195], v[88:91], v[108:111], v[192:195]\n\t" \
  "ds_read_b64_tr_b16 v[76:77], v59 offset:38912\n\t" \
  "v_mfma_f32_16x16x32_bf16 v[192:195], v[128:131], v[148:151], v[192:195]\n\t" \
  "ds_read_b64_tr_b16 v[78:79], v59 offset:39424\n\t" \
  "v_mfma_f32_16x16x32_bf16 v[200:203], v[128:131], v[64:67], v[200:203]\n\t" \
  "ds_read_b64_tr_b16 v[80:81], v58 offset:18432\n\t" \
  "v_mfma_f32_16x16x32_bf16 v[200:203], v[88:91], v[104:107], v[200:203]\n\t" \
  "ds_read_b64_tr_b16 v[82:83], v58 offset:18944\n\t" \
  "v_mfma_f32_16x16x32_bf16 v[208:211], v[88:91], v[112:115], v[208:211]\n\t" \
  "ds_read_b64_tr_b16 v[124:125], v58 offset:22528\n\t" \
  "v_mfma_f32_16x16x32_bf16 v[208:211], v[128:131], v[116:119], v[208:211]\n\t" \
  "ds_read_b64_tr_b16 v[126:127], v58 offset:23040\n\t" \
  "v_mfma_f32_16x16x32_bf16 v[188:191], v[120:123], v[96:99], v[188:191]\n\t" \
  "ds_read_b64_tr_b16 v[84:85], v59 offset:18432\n\t" \
  "s_waitcnt lgkmcnt(7)\n\t" \
  "v_mfma_f32_16x16x32_bf16 v[188:191], v[100:103], v[132:135], v[188:191]\n\t" \
  "ds_read_b64_tr_b16 v[86:87], v59 offset:18944\n\t" \
  "v_mfma_f32_16x16x32_bf16 v[196:199], v[100:103], v[108:111], v[196:199]\n\t" \
  "ds_read_b64_tr_b16 v[92:93], v59 offset:22528\n\t" \
  "v_mfma_f32_16x16x32_bf16 v[196:199], v[120:123], v[148:151], v[196:199]\n\t" \
  "ds_read_b64_tr_b16 v[94:95], v59 offset:23040\n\t" \
  "v_mfma_f32_16x16x32_bf16 v[204:207], v[120:123], v[64:67], v[204:207]\n\t" \
  "v_mfma_f32_16x16x32_bf16 v[204:207], v[100:103], v[104:107], v[204:207]\n\t" \
  "v_mfma_f32_16x16x32_bf16 v[212:215], v[100:103], v[112:115], v[212:215]\n\t" \
  "v_mfma_f32_16x16x32_bf16 v[212:215], v[120:123], v[116:119], v[212:215]\n\t" \
  "v_mfma_f32_16x16x32_bf16 v[216:219], v[96:99], v[136:139], v[216:219]\n\t" \
  "v_mfma_f32_16x16x32_bf16 v[216:219], v[132:135], v[144:147], v[216:219]\n\t" \
  "v_mfma_f32_16x16x32_bf16 v[224:227], v[108:111], v[144:147], v[224:227]\n\t" \
  "v_mfma_f32_16x16x32_bf16 v[224:227], v[148:151], v[136:139], v[224:227]\n\t" \
  "v_mfma_f32_16x16x32_bf16 v[232:235], v[64:67], v[136:139], v[232:235]\n\t" \
  "global_store_dwordx4 v2, v[42:45], %24 offset:1024 sc1\n\t" \
  "v_add_u32 v2, 0x20000, v2\n\t" \
  "v_mfma_f32_16x16x32_bf16 v[232:235], v[104:107], v[144:147], v[232:235]\n\t" \
  "v_mfma_f32_16x16x32_bf16 v[240:243], v[112:115], v[144:147], v[240:243]\n\t" \
  "v_mfma_f32_16x16x32_bf16 v[42:45], v[26:29], v[18:21], 0\n\t" \
  "v_mfma_f32_16x16x32_bf16 v[42:45], v[30:33], v[22:25], v[42:45]\n\t" \
  "v_mfma_f32_16x16x32_bf16 v[46:49], v[30:33], v[18:21], 0\n\t" \
  "v_mfma_f32_16x16x32_bf16 v[46:49], v[34:37], v[22:25], v[46:49]\n\t" \
  "ds_read_b128 v[26:29], v0 offset:1840\n\t" \
  "ds_read_b128 v[30:33], v0 offset:1328\n\t" \
  "v_mfma_f32_16x16x32_bf16 v[240:243], v[116:119], v[136:139], v[240:243]\n\t" \
  "ds_read_b128 v[34:37], v0 offset:800\n\t" \
  "v_mfma_f32_16x16x32_bf16 v[248:251], v[72:75], v[136:139], v[248:251]\n\t" \
  "v_mfma_f32_16x16x32_bf16 v[248:251], v[140:143], v[144:147], v[248:251]\n\t" \
  "v_lshlrev_b32 v50, 16, v38\n\t" \
  "v_lshlrev_b32 v51, 16, v40\n\t" \
  "v_mfma_f32_16x16x32_bf16 v[252:255], v[72:75], v[68:71], v[252:255]\n\t" \
  "v_mul_f32 v42, v42, v50\n\t" \
  "v_mul_f32 v46, v46, v51\n\t" \
  "s_waitcnt lgkmcnt(3)\n\t" \
  "v_mfma_f32_16x16x32_bf16 v[252:255], v[140:143], v[76:79], v[252:255]\n\t" \
  "v_cvt_pk_bf16_f32 v42, v42, v46\n\t" \
  "v_and_b32 v50, 0xffff0000, v38\n\t" \
  "v_mfma_f32_16x16x32_bf16 v[220:223], v[132:135], v[76:79], v[220:223]\n\t" \
  "v_and_b32 v51, 0xffff0000, v40\n\t" \
  "v_mul_f32 v43, v43, v50\n\t" \
  "v_mfma_f32_16x16x32_bf16 v[220:223], v[96:99], v[68:71], v[220:223]\n\t" \
  "v_mul_f32 v47, v47, v51\n\t" \
  "v_cvt_pk_bf16_f32 v43, v43, v47\n\t" \
  "s_waitcnt lgkmcnt(0)\n\t" \
  "s_barrier\n\t" \
  "v_mfma_f32_16x16x32_bf16 v[228:231], v[148:151], v[68:71], v[228:231]\n\t" \
  "ds_read_b64_tr_b16 v[88:89], v62 offset:26624\n\t" \
  "v_lshlrev_b32 v50, 16, v39\n\t" \
  "v_mfma_f32_16x16x32_bf16 v[228:231], v[108:111], v[76:79], v[228:231]\n\t" \
  "ds_read_b64_tr_b16 v[90:91], v62 offset:27136\n\t" \
  "v_lshlrev_b32 v51, 16, v41\n\t" \
  "v_mfma_f32_16x16x32_bf16 v[236:239], v[104:107], v[76:79], v[236:239]\n\t" \
  "ds_read_b128 v[128:131], v60 offset:10240\n\t" \
  "v_mul_f32 v44, v44, v50\n\t" \
  "v_mfma_f32_16x16x32_bf16 v[236:239], v[64:67], v[68:71], v[236:239]\n\t" \
  "ds_read_b64_tr_b16 v[100:101], v62 offset:30720\n\t" \
  "v_mul_f32 v48, v48, v51\n\t" \
  "v_mfma_f32_16x16x32_bf16 v[244:247], v[116:119], v[68:71], v[244:247]\n\t" \
  "ds_read_b64_tr_b16 v[102:103], v62 offset:31232\n\t" \
  "v_cvt_pk_bf16_f32 v44, v44, v48\n\t" \
  "v_mfma_f32_16x16x32_bf16 v[244:247], v[112:115], v[76:79], v[244:247]\n\t" \
  "ds_read_b128 v[120:123], v61 offset:10240\n\t" \
  "v_and_b32 v50, 0xffff0000, v39\n\t" \
  "v_mfma_f32_16x16x32_bf16 v[152:155], v[80:83], v[96:99], v[152:155]\n\t" \
  "ds_read_b128 v[136:139], v61 offset:12288\n\t" \
  "v_and_b32 v51, 0xffff0000, v41\n\t" \
  "v_mfma_f32_16x16x32_bf16 v[152:155], v[124:127], v[132:135], v[152:155]\n\t" \
  "ds_read_b128 v[144:147], v60 offset:12288\n\t" \
  "v_mul_f32 v45, v45, v50\n\t" \
  "v_mfma_f32_16x16x32_bf16 v[160:163], v[124:127], v[108:111], v[160:163]\n\t" \
  "ds_read_b128 v[72:75], v60 offset:14336\n\t" \
  "v_mul_f32 v49, v49, v51\n\t" \
  "v_mfma_f32_16x16x32_bf16 v[160:163], v[80:83], v[148:151], v[160:163]\n\t" \
  "ds_read_b128 v[140:143], v61 offset:14336\n\t" \
  "ds_read_b64_tr_b16 v[38:39], v1 offset:800\n\t" \
  "v_mfma_f32_16x16x32_bf16 v[168:171], v[80:83], v[64:67], v[168:171]\n\t" \
  "ds_read_b128 v[68:71], v61 offset:16384\n\t" \
  "ds_read_b64_tr_b16 v[40:41], v1 offset:288\n\t" \
  "v_mfma_f32_16x16x32_bf16 v[168:171], v[124:127], v[104:107], v[168:171]\n\t" \
  "ds_read_b128 v[76:79], v60 offset:16384\n\t" \
  "v_cvt_pk_bf16_f32 v45, v45, v49\n\t" \
  "v_mfma_f32_16x16x32_bf16 v[176:179], v[124:127], v[112:115], v[176:179]\n\t" \
  "ds_read_b64_tr_b16 v[124:125], v63 offset:26624\n\t" \
  "v_mfma_f32_16x16x32_bf16 v[176:179], v[80:83], v[116:119], v[176:179]\n\t" \
  "ds_read_b64_tr_b16 v[126:127], v63 offset:27136\n\t" \
  "v_mfma_f32_16x16x32_bf16 v[156:159], v[84:87], v[96:99], v[156:159]\n\t" \
  "ds_read_b64_tr_b16 v[80:81], v63 offset:30720\n\t" \
  "v_mfma_f32_16x16x32_bf16 v[156:159], v[92:95], v[132:135], v[156:159]\n\t" \
  "ds_read_b64_tr_b16 v[82:83], v63 offset:31232\n\t" \
  "v_mfma_f32_16x16x32_bf16 v[164:167], v[92:95], v[108:111], v[164:167]\n\t" \
  "ds_read_b64_tr_b16 v[96:97], v62 offset:34816\n\t" \
  "v_mfma_f32_16x16x32_bf16 v[164:167], v[84:87], v[148:151], v[164:167]\n\t" \
  "ds_read_b64_tr_b16 v[98:99], v62 offset:35328\n\t" \
  "v_mfma_f32_16x16x32_bf16 v[172:175], v[84:87], v[64:67], v[172:175]\n\t" \
  "ds_read_b64_tr_b16 v[132:133], v62 offset:38912\n\t" \
  "v_mfma_f32_16x16x32_bf16 v[172:175], v[92:95], v[104:107], v[172:175]\n\t" \
  "ds_read_b64_tr_b16 v[134:135], v62 offset:39424\n\t" \
  "v_mfma_f32_16x16x32_bf16 v[180:183], v[92:95], v[112:115], v[180:183]\n\t" \
  "ds_read_b128 v[108:111], v60 offset:0\n\t" \
  "v_mfma_f32_16x16x32_bf16 v[180:183], v[84:87], v[116:119], v[180:183]\n\t" \
  "ds_read_b128 v[148:151], v61 offset:0\n\t" \
  "s_waitcnt lgkmcnt(7)\n\t" \
  "v_mfma_f32_16x16x32_bf16 v[184:187], v[88:91], v[128:131], v[184:187]\n\t" \
  "ds_read_b64_tr_b16 v[64:65], v63 offset:34816\n\t" \
  "v_mfma_f32_16x16x32_bf16 v[184:187], v[100:103], v[120:123], v[184:187]\n\t" \
  "ds_read_b64_tr_b16 v[66:67], v63 offset:35328\n\t" \
  "v_mfma_f32_16x16x32_bf16 v[192:195], v[100:103], v[136:139], v[192:195]\n\t" \
  "ds_read_b64_tr_b16 v[104:105], v63 offset:38912\n\t" \
  "v_mfma_f32_16x16x32_bf16 v[192:195], v[88:91], v[144:147], v[192:195]\n\t" \
  "ds_read_b64_tr_b16 v[106:107], v63 offset:39424\n\t" \
  "v_mfma_f32_16x16x32_bf16 v[200:203], v[88:91], v[72:75], v[200:203]\n\t" \
  "ds_read_b64_tr_b16 v[92:93], v62 offset:18432\n\t" \
  "v_mfma_f32_16x16x32_bf16 v[200:203], v[100:103], v[140:143], v[200:203]\n\t" \
  "ds_read_b64_tr_b16 v[94:95], v62 offset:18944\n\t" \
  "v_mfma_f32_16x16x32_bf16 v[208:211], v[100:103], v[68:71], v[208:211]\n\t" \
  "ds_read_b64_tr_b16 v[112:113], v62 offset:22528\n\t" \
  "v_mfma_f32_16x16x32_bf16 v[208:211], v[88:91], v[76:79], v[208:211]\n\t" \
  "ds_read_b64_tr_b16 v[114:115], v62 offset:23040\n\t" \
  "v_mfma_f32_16x16x32_bf16 v[188:191], v[124:127], v[128:131], v[188:191]\n\t" \
  "ds_read_b64_tr_b16 v[84:85], v63 offset:18432\n\t" \
  "s_waitcnt lgkmcnt(7)\n\t" \
  "v_mfma_f32_16x16x32_bf16 v[188:191], v[80:83], v[120:123], v[188:191]\n\t" \
  "ds_read_b64_tr_b16 v[86:87], v63 offset:18944\n\t" \
  "v_mfma_f32_16x16x32_bf16 v[196:199], v[80:83], v[136:139], v[196:199]\n\t" \
  "ds_read_b64_tr_b16 v[116:117], v63 offset:22528\n\t" \
  "v_mfma_f32_16x16x32_bf16 v[196:199], v[124:127], v[144:147], v[196:199]\n\t" \
  "ds_read_b64_tr_b16 v[118:119], v63 offset:23040\n\t" \
  "v_mfma_f32_16x16x32_bf16 v[204:207], v[124:127], v[72:75], v[204:207]\n\t" \
  "v_mfma_f32_16x16x32_bf16 v[204:207], v[80:83], v[140:143], v[204:207]\n\t" \
  "v_mfma_f32_16x16x32_bf16 v[212:215], v[80:83], v[68:71], v[212:215]\n\t" \
  "v_mfma_f32_16x16x32_bf16 v[212:215], v[124:127], v[76:79], v[212:215]\n\t" \
  "v_mfma_f32_16x16x32_bf16 v[216:219], v[128:131], v[96:99], v[216:219]\n\t" \
  "v_mfma_f32_16x16x32_bf16 v[216:219], v[120:123], v[132:135], v[216:219]\n\t" \
  "v_mfma_f32_16x16x32_bf16 v[224:227], v[136:139], v[132:135], v[224:227]\n\t" \
  "v_mfma_f32_16x16x32_bf16 v[224:227], v[144:147], v[96:99], v[224:227]\n\t" \
  "v_mfma_f32_16x16x32_bf16 v[232:235], v[72:75], v[96:99], v[232:235]\n\t" \
  "global_store_dwordx4 v2, v[42:45], %24 offset:0 sc1\n\t" \
  "v_mfma_f32_16x16x32_bf16 v[232:235], v[140:143], v[132:135], v[232:235]\n\t" \
  "s_nop 0\n\t" \
  "v_mfma_f32_16x16x32_bf16 v[42:45], v[26:29], v[18:21], 0\n\t" \
  "v_mfma_f32_16x16x32_bf16 v[42:45], v[30:33], v[22:25], v[42:45]\n\t" \
  "v_mfma_f32_16x16x32_bf16 v[46:49], v[30:33], v[18:21], 0\n\t" \
  "v_mfma_f32_16x16x32_bf16 v[46:49], v[34:37], v[22:25], v[46:49]\n\t" \
  "v_mfma_f32_16x16x32_bf16 v[240:243], v[68:71], v[132:135], v[240:243]\n\t" \
  "v_mfma_f32_16x16x32_bf16 v[240:243], v[76:79], v[96:99], v[240:243]\n\t" \
  "v_lshlrev_b32 v50, 16, v38\n\t" \
  "v_lshlrev_b32 v51, 16, v40\n\t" \
  "v_mfma_f32_16x16x32_bf16 v[248:251], v[108:111], v[96:99], v[248:251]\n\t" \
  "s_nop 0\n\t" \
  "v_mul_f32 v42, v42, v50\n\t" \
  "s_nop 0\n\t" \
  "v_mul_f32 v46, v46, v51\n\t" \
  "v_mfma_f32_16x16x32_bf16 v[248:251], v[148:151], v[132:135], v[248:251]\n\t" \
  "v_cvt_pk_bf16_f32 v42, v42, v46\n\t" \
  "v_and_b32 v50, 0xffff0000, v38\n\t" \
  "v_mfma_f32_16x16x32_bf16 v[252:255], v[108:111], v[64:67], v[252:255]\n\t" \
  "v_and_b32 v51, 0xffff0000, v40\n\t" \
  "v_mul_f32 v43, v43, v50\n\t" \
  "s_waitcnt lgkmcnt(0)\n\t" \
  "v_mfma_f32_16x16x32_bf16 v[252:255], v[148:151], v[104:107], v[252:255]\n\t" \
  "v_mul_f32 v47, v47, v51\n\t" \
  "v_cvt_pk_bf16_f32 v43, v43, v47\n\t" \
  "v_mfma_f32_16x16x32_bf16 v[220:223], v[120:123], v[104:107], v[220:223]\n\t" \
  "v_lshlrev_b32 v50, 16, v39\n\t" \
  "v_lshlrev_b32 v51, 16, v41\n\t" \
  "v_mfma_f32_16x16x32_bf16 v[220:223], v[128:131], v[64:67], v[220:223]\n\t" \
  "v_mul_f32 v44, v44, v50\n\t" \
  "v_mul_f32 v48, v48, v51\n\t" \
  "s_barrier\n\t" \
  "v_mfma_f32_16x16x32_bf16 v[228:231], v[144:147], v[64:67], v[228:231]\n\t" \
  "ds_read_b64_tr_b16 v[100:101], v54 offset:26624\n\t" \
  "v_cvt_pk_bf16_f32 v44, v44, v48\n\t" \
  "v_mfma_f32_16x16x32_bf16 v[228:231], v[136:139], v[104:107], v[228:231]\n\t" \
  "ds_read_b64_tr_b16 v[102:103], v54 offset:27136\n\t" \
  "v_and_b32 v50, 0xffff0000, v39\n\t" \
  "v_mfma_f32_16x16x32_bf16 v[236:239], v[140:143], v[104:107], v[236:239]\n\t" \
  "ds_read_b128 v[88:91], v52 offset:10240\n\t" \
  "v_and_b32 v51, 0xffff0000, v41\n\t" \
  "v_mfma_f32_16x16x32_bf16 v[236:239], v[72:75], v[64:67], v[236:239]\n\t" \
  "ds_read_b64_tr_b16 v[80:81], v54 offset:30720\n\t" \
  "v_mul_f32 v45, v45, v50\n\t" \
  "v_mfma_f32_16x16x32_bf16 v[244:247], v[76:79], v[64:67], v[244:247]\n\t" \
  "ds_read_b64_tr_b16 v[82:83], v54 offset:31232\n\t" \
  "v_mul_f32 v49, v49, v51\n\t" \
  "v_mfma_f32_16x16x32_bf16 v[244:247], v[68:71], v[104:107], v[244:247]\n\t" \
  "ds_read_b128 v[124:127], v53 offset:10240\n\t" \
  "v_cvt_pk_bf16_f32 v45, v45, v49\n\t" \
  "v_mfma_f32_16x16x32_bf16 v[152:155], v[92:95], v[128:131], v[152:155]\n\t" \
  "ds_read_b128 v[96:99], v53 offset:12288\n\t" \
  "v_mfma_f32_16x16x32_bf16 v[152:155], v[112:115], v[120:123], v[152:155]\n\t" \
  "ds_read_b128 v[132:135], v52 offset:12288\n\t" \
  "v_mfma_f32_16x16x32_bf16 v[160:163], v[112:115], v[136:139], v[160:163]\n\t" \
  "ds_read_b128 v[108:111], v52 offset:14336\n\t" \
  "v_mfma_f32_16x16x32_bf16 v[160:163], v[92:95], v[144:147], v[160:163]\n\t" \
  "ds_read_b128 v[148:151], v53 offset:14336\n\t" \
  "v_mfma_f32_16x16x32_bf16 v[168:171], v[92:95], v[72:75], v[168:171]\n\t" \
  "ds_read_b128 v[64:67], v53 offset:16384\n\t" \
  "v_mfma_f32_16x16x32_bf16 v[168:171], v[112:115], v[140:143], v[168:171]\n\t" \
  "ds_read_b128 v[104:107], v52 offset:16384\n\t" \
  "v_mfma_f32_16x16x32_bf16 v[176:179], v[112:115], v[68:71], v[176:179]\n\t" \
  "ds_read_b64_tr_b16 v[112:113], v55 offset:26624\n\t" \
  "v_mfma_f32_16x16x32_bf16 v[176:179], v[92:95], v[76:79], v[176:179]\n\t" \
  "ds_read_b64_tr_b16 v[114:115], v55 offset:27136\n\t" \
  "v_mfma_f32_16x16x32_bf16 v[156:159], v[84:87], v[128:131], v[156:159]\n\t" \
  "ds_read_b64_tr_b16 v[92:93], v55 offset:30720\n\t" \
  "v_mfma_f32_16x16x32_bf16 v[156:159], v[116:119], v[120:123], v[156:159]\n\t" \
  "ds_read_b64_tr_b16 v[94:95], v55 offset:31232\n\t" \
  "v_mfma_f32_16x16x32_bf16 v[164:167], v[116:119], v[136:139], v[164:167]\n\t" \
  "ds_read_b64_tr_b16 v[128:129], v54 offset:34816\n\t" \
  "v_mfma_f32_16x16x32_bf16 v[164:167], v[84:87], v[144:147], v[164:167]\n\t" \
  "ds_read_b64_tr_b16 v[130:131], v54 offset:35328\n\t" \
  "v_mfma_f32_16x16x32_bf16 v[172:175], v[84:87], v[72:75], v[172:175]\n\t" \
  "ds_read_b64_tr_b16 v[120:121], v54 offset:38912\n\t" \
  "v_mfma_f32_16x16x32_bf16 v[172:175], v[116:119], v[140:143], v[172:175]\n\t" \
  "ds_read_b64_tr_b16 v[122:123], v54 offset:39424\n\t" \
  "v_mfma_f32_16x16x32_bf16 v[180:183], v[116:119], v[68:71], v[180:183]\n\t" \
  "ds_read_b128 v[136:139], v52 offset:0\n\t" \
  "v_mfma_f32_16x16x32_bf16 v[180:183], v[84:87], v[76:79], v[180:183]\n\t" \
  "ds_read_b128 v[144:147], v53 offset:0\n\t" \
  "s_waitcnt lgkmcnt(7)\n\t" \
  "v_mfma_f32_16x16x32_bf16 v[184:187], v[100:103], v[88:91], v[184:187]\n\t" \
  "ds_read_b64_tr_b16 v[72:73], v55 offset:34816\n\t" \
  "v_mfma_f32_16x16x32_bf16 v[184:187], v[80:83], v[124:127], v[184:187]\n\t" \
  "ds_read_b64_tr_b16 v[74:75], v55 offset:35328\n\t" \
  "v_mfma_f32_16x16x32_bf16 v[192:195], v[80:83], v[96:99], v[192:195]\n\t" \
  "ds_read_b64_tr_b16 v[140:141], v55 offset:38912\n\t" \
  "v_mfma_f32_16x16x32_bf16 v[192:195], v[100:103], v[132:135], v[192:195]\n\t" \
  "ds_read_b64_tr_b16 v[142:143], v55 offset:39424\n\t" \
  "v_mfma_f32_16x16x32_bf16 v[200:203], v[100:103], v[108:111], v[200:203]\n\t" \
  "ds_read_b64_tr_b16 v[68:69], v54 offset:18432\n\t" \
  "v_mfma_f32_16x16x32_bf16 v[200:203], v[80:83], v[148:151], v[200:203]\n\t" \
  "ds_read_b64_tr_b16 v[70:71], v54 offset:18944\n\t" \
  "v_mfma_f32_16x16x32_bf16 v[208:211], v[80:83], v[64:67], v[208:211]\n\t" \
  "ds_read_b64_tr_b16 v[116:117], v54 offset:22528\n\t" \
  "v_mfma_f32_16x16x32_bf16 v[208:211], v[100:103], v[104:107], v[208:211]\n\t" \
  "ds_read_b64_tr_b16 v[118:119], v54 offset:23040\n\t" \
  "v_mfma_f32_16x16x32_bf16 v[188:191], v[112:115], v[88:91], v[188:191]\n\t" \
  "ds_read_b64_tr_b16 v[76:77], v55 offset:18432\n\t" \
  "s_waitcnt lgkmcnt(7)\n\t" \
  "v_mfma_f32_16x16x32_bf16 v[188:191], v[92:95], v[124:127], v[188:191]\n\t" \
  "ds_read_b64_tr_b16 v[78:79], v55 offset:18944\n\t" \
  "v_mfma_f32_16x16x32_bf16 v[196:199], v[92:95], v[96:99], v[196:199]\n\t" \
  "ds_read_b64_tr_b16 v[84:85], v55 offset:22528\n\t" \
  "v_mfma_f32_16x16x32_bf16 v[196:199], v[112:115], v[132:135], v[196:199]\n\t" \
  "ds_read_b64_tr_b16 v[86:87], v55 offset:23040\n\t" \
  "v_mfma_f32_16x16x32_bf16 v[204:207], v[112:115], v[108:111], v[204:207]\n\t" \
  "v_mfma_f32_16x16x32_bf16 v[204:207], v[92:95], v[148:151], v[204:207]\n\t" \
  "v_mfma_f32_16x16x32_bf16 v[212:215], v[92:95], v[64:67], v[212:215]\n\t" \
  "v_mfma_f32_16x16x32_bf16 v[212:215], v[112:115], v[104:107], v[212:215]\n\t" \
  "v_mfma_f32_16x16x32_bf16 v[216:219], v[88:91], v[128:131], v[216:219]\n\t" \
  "v_mfma_f32_16x16x32_bf16 v[216:219], v[124:127], v[120:123], v[216:219]\n\t" \
  "v_mfma_f32_16x16x32_bf16 v[224:227], v[96:99], v[120:123], v[224:227]\n\t" \
  "v_mfma_f32_16x16x32_bf16 v[224:227], v[132:135], v[128:131], v[224:227]\n\t" \
  "v_mfma_f32_16x16x32_bf16 v[232:235], v[108:111], v[128:131], v[232:235]\n\t" \
  "global_store_dwordx4 v2, v[42:45], %24 offset:1024 sc1\n\t" \
  "v_mfma_f32_16x16x32_bf16 v[232:235], v[148:151], v[120:123], v[232:235]\n\t" \
  "v_mfma_f32_16x16x32_bf16 v[240:243], v[64:67], v[120:123], v[240:243]\n\t" \
  "v_mfma_f32_16x16x32_bf16 v[240:243], v[104:107], v[128:131], v[240:243]\n\t" \
  "v_mov_b32 v30, 0xff61b1e6\n\t" \
  "v_bfe_i32 v34, v3, 0, 1\n\t" \
  "v_mfma_f32_16x16x32_bf16 v[248:251], v[136:139], v[128:131], v[248:251]\n\t" \
  "v_and_b32 v34, v30, v34\n\t" \
  "v_bfe_i32 v38, v3, 4, 1\n\t" \
  "v_mfma_f32_16x16x32_bf16 v[248:251], v[144:147], v[120:123], v[248:251]\n\t" \
  "v_and_b32 v38, v30, v38\n\t" \
  "v_bfe_i32 v42, v3, 8, 1\n\t" \
  "v_mfma_f32_16x16x32_bf16 v[252:255], v[136:139], v[72:75], v[252:255]\n\t" \
  "v_bfi_b32 v42, v42, v30, %15\n\t" \
  "v_mov_b32 v48, 0x3f803f80\n\t" \
  "s_waitcnt lgkmcnt(0)\n\t" \
  "v_mfma_f32_16x16x32_bf16 v[252:255], v[144:147], v[140:143], v[252:255]\n\t" \
  "v_bfe_i32 v35, v3, 1, 1\n\t" \
  "v_and_b32 v35, v30, v35\n\t" \
  "v_mfma_f32_16x16x32_bf16 v[220:223], v[124:127], v[140:143], v[220:223]\n\t" \
  "v_bfe_i32 v39, v3, 5, 1\n\t" \
  "v_and_b32 v39, v30, v39\n\t" \
  "v_mfma_f32_16x16x32_bf16 v[220:223], v[88:91], v[72:75], v[220:223]\n\t" \
  "v_bfe_i32 v43, v3, 9, 1\n\t" \
  "v_bfi_b32 v43, v43, v30, %15\n\t" \
  "s_barrier\n\t" \
  "v_mfma_f32_16x16x32_bf16 v[228:231], v[132:135], v[72:75], v[228:231]\n\t" \
  "ds_read_b64_tr_b16 v[80:81], v58 offset:26624\n\t" \
  "v_mov_b32 v49, 0x3f803f80\n\t" \
  "v_mfma_f32_16x16x32_bf16 v[228:231], v[96:99], v[140:143], v[228:231]\n\t" \
  "ds_read_b64_tr_b16 v[82:83], v58 offset:27136\n\t" \
  "v_bfe_i32 v36, v3, 2, 1\n\t" \
  "v_mfma_f32_16x16x32_bf16 v[236:239], v[148:151], v[140:143], v[236:239]\n\t" \
  "ds_read_b128 v[100:103], v56 offset:10240\n\t" \
  "v_and_b32 v36, v30, v36\n\t" \
  "v_mfma_f32_16x16x32_bf16 v[236:239], v[108:111], v[72:75], v[236:239]\n\t" \
  "ds_read_b64_tr_b16 v[92:93], v58 offset:30720\n\t" \
  "v_bfe_i32 v40, v3, 6, 1\n\t" \
  "v_mfma_f32_16x16x32_bf16 v[244:247], v[104:107], v[72:75], v[244:247]\n\t" \
  "ds_read_b64_tr_b16 v[94:95], v58 offset:31232\n\t" \
  "v_and_b32 v40, v30, v40\n\t" \
  "v_mfma_f32_16x16x32_bf16 v[244:247], v[64:67], v[140:143], v[244:247]\n\t" \
  "ds_read_b128 v[112:115], v57 offset:10240\n\t" \
  "v_bfe_i32 v44, v3, 10, 1\n\t" \
  "v_mfma_f32_16x16x32_bf16 v[152:155], v[68:71], v[88:91], v[152:155]\n\t" \
  "ds_read_b128 v[128:131], v57 offset:12288\n\t" \
  "v_bfi_b32 v44, v44, v30, %15\n\t" \
  "v_mfma_f32_16x16x32_bf16 v[152:155], v[116:119], v[124:127], v[152:155]\n\t" \
  "ds_read_b128 v[120:123], v56 offset:12288\n\t" \
  "v_mov_b32 v50, 0x3f803f80\n\t" \
  "v_mfma_f32_16x16x32_bf16 v[160:163], v[116:119], v[96:99], v[160:163]\n\t" \
  "ds_read_b128 v[136:139], v56 offset:14336\n\t" \
  "v_bfe_i32 v37, v3, 3, 1\n\t" \
  "v_mfma_f32_16x16x32_bf16 v[160:163], v[68:71], v[132:135], v[160:163]\n\t" \
  "ds_read_b128 v[144:147], v57 offset:14336\n\t" \
  "v_and_b32 v37, v30, v37\n\t" \
  "v_mfma_f32_16x16x32_bf16 v[168:171], v[68:71], v[108:111], v[168:171]\n\t" \
  "ds_read_b128 v[72:75], v57 offset:16384\n\t" \
  "v_bfe_i32 v41, v3, 7, 1\n\t" \
  "v_mfma_f32_16x16x32_bf16 v[168:171], v[116:119], v[148:151], v[168:171]\n\t" \
  "ds_read_b128 v[140:143], v56 offset:16384\n\t" \
  "v_and_b32 v41, v30, v41\n\t" \
  "v_mfma_f32_16x16x32_bf16 v[176:179], v[116:119], v[64:67], v[176:179]\n\t" \
  "ds_read_b64_tr_b16 v[116:117], v59 offset:26624\n\t" \
  "v_bfe_i32 v45, v3, 11, 1\n\t" \
  "v_mfma_f32_16x16x32_bf16 v[176:179], v[68:71], v[104:107], v[176:179]\n\t" \
  "ds_read_b64_tr_b16 v[118:119], v59 offset:27136\n\t" \
  "v_bfi_b32 v45, v45, v30, %15\n\t" \
  "v_mfma_f32_16x16x32_bf16 v[156:159], v[76:79], v[88:91], v[156:159]\n\t" \
  "ds_read_b64_tr_b16 v[68:69], v59 offset:30720\n\t" \
  "v_mov_b32 v51, 0x3f803f80\n\t" \
  "v_mfma_f32_16x16x32_bf16 v[156:159], v[84:87], v[124:127], v[156:159]\n\t" \
  "ds_read_b64_tr_b16 v[70:71], v59 offset:31232\n\t" \
  "v_bfe_i32 v46, v3, 12, 1\n\t" \
  "v_mfma_f32_16x16x32_bf16 v[164:167], v[84:87], v[96:99], v[164:167]\n\t" \
  "ds_read_b64_tr_b16 v[88:89], v58 offset:34816\n\t" \
  "v_bfe_i32 v31, v3, 13, 1\n\t" \
  "v_mfma_f32_16x16x32_bf16 v[164:167], v[76:79], v[132:135], v[164:167]\n\t" \
  "ds_read_b64_tr_b16 v[90:91], v58 offset:35328\n\t" \
  "v_perm_b32 v46, v31, v46, vcc_hi\n\t" \
  "v_mfma_f32_16x16x32_bf16 v[172:175], v[76:79], v[108:111], v[172:175]\n\t" \
  "ds_read_b64_tr_b16 v[124:125], v58 offset:38912\n\t" \
  "v_bfe_i32 v47, v3, 14, 1\n\t" \
  "v_mfma_f32_16x16x32_bf16 v[172:175], v[84:87], v[148:151], v[172:175]\n\t" \
  "ds_read_b64_tr_b16 v[126:127], v58 offset:39424\n\t" \
  "v_bfe_i32 v31, v3, 15, 1\n\t" \
  "v_mfma_f32_16x16x32_bf16 v[180:183], v[84:87], v[64:67], v[180:183]\n\t" \
  "ds_read_b128 v[96:99], v56 offset:0\n\t" \
  "v_perm_b32 v47, v31, v47, vcc_hi\n\t" \
  "v_mfma_f32_16x16x32_bf16 v[180:183], v[76:79], v[104:107], v[180:183]\n\t" \
  "ds_read_b128 v[132:135], v57 offset:0\n\t" \
  "v_add_u32 v30, 18432, v4\n\t" \
  "s_waitcnt lgkmcnt(7)\n\t" \
  "v_mfma_f32_16x16x32_bf16 v[184:187], v[80:83], v[100:103], v[184:187]\n\t" \
  "ds_read_b64_tr_b16 v[108:109], v59 offset:34816\n\t" \
  "v_xor_b32 v31, 1024, v30\n\t" \
  "v_mfma_f32_16x16x32_bf16 v[184:187], v[92:95], v[112:115], v[184:187]\n\t" \
  "ds_read_b64_tr_b16 v[110:111], v59 offset:35328\n\t" \
  "v_mov_b32 v32, %12\n\t" \
  "v_mfma_f32_16x16x32_bf16 v[192:195], v[92:95], v[128:131], v[192:195]\n\t" \
  "ds_read_b64_tr_b16 v[148:149], v59 offset:38912\n\t" \
  "v_xor_b32 v33, 4, v32\n\t" \
  "v_mfma_f32_16x16x32_bf16 v[192:195], v[80:83], v[120:123], v[192:195]\n\t" \
  "ds_read_b64_tr_b16 v[150:151], v59 offset:39424\n\t" \
  "v_mov_b32 v0, 1\n\t" \
  "v_mfma_f32_16x16x32_bf16 v[200:203], v[80:83], v[136:139], v[200:203]\n\t" \
  "ds_read_b64_tr_b16 v[64:65], v58 offset:18432\n\t" \
  "v_and_b32 v4, 1023, v4\n\t" \
  "v_mfma_f32_16x16x32_bf16 v[200:203], v[92:95], v[144:147], v[200:203]\n\t" \
  "ds_read_b64_tr_b16 v[66:67], v58 offset:18944\n\t" \
  "v_add_u32 v1, 16384, v4\n\t" \
  "v_mfma_f32_16x16x32_bf16 v[208:211], v[92:95], v[72:75], v[208:211]\n\t" \
  "ds_read_b64_tr_b16 v[84:85], v58 offset:22528\n\t" \
  "v_mfma_f32_16x16x32_bf16 v[208:211], v[80:83], v[140:143], v[208:211]\n\t" \
  "ds_read_b64_tr_b16 v[86:87], v58 offset:23040\n\t" \
  "v_mfma_f32_16x16x32_bf16 v[188:191], v[116:119], v[100:103], v[188:191]\n\t" \
  "ds_read_b64_tr_b16 v[76:77], v59 offset:18432\n\t" \
  "s_waitcnt lgkmcnt(7)\n\t" \
  "v_mfma_f32_16x16x32_bf16 v[188:191], v[68:71], v[112:115], v[188:191]\n\t" \
  "ds_read_b64_tr_b16 v[78:79], v59 offset:18944\n\t" \
  "v_mfma_f32_16x16x32_bf16 v[196:199], v[68:71], v[128:131], v[196:199]\n\t" \
  "ds_read_b64_tr_b16 v[104:105], v59 offset:22528\n\t" \
  "v_mfma_f32_16x16x32_bf16 v[196:199], v[116:119], v[120:123], v[196:199]\n\t" \
  "ds_read_b64_tr_b16 v[106:107], v59 offset:23040\n\t" \
  "v_mfma_f32_16x16x32_bf16 v[204:207], v[116:119], v[136:139], v[204:207]\n\t" \
  "v_mfma_f32_16x16x32_bf16 v[204:207], v[68:71], v[144:147], v[204:207]\n\t" \
  "v_mfma_f32_16x16x32_bf16 v[212:215], v[68:71], v[72:75], v[212:215]\n\t" \
  "v_mfma_f32_16x16x32_bf16 v[212:215], v[116:119], v[140:143], v[212:215]\n\t" \
  "v_mfma_f32_16x16x32_bf16 v[216:219], v[100:103], v[88:91], v[216:219]\n\t" \
  "v_mfma_f32_16x16x32_bf16 v[216:219], v[112:115], v[124:127], v[216:219]\n\t" \
  "v_mfma_f32_16x16x32_bf16 v[224:227], v[128:131], v[124:127], v[224:227]\n\t" \
  "v_mfma_f32_16x16x32_bf16 v[224:227], v[120:123], v[88:91], v[224:227]\n\t" \
  "v_mfma_f32_16x16x32_bf16 v[232:235], v[136:139], v[88:91], v[232:235]\n\t" \
  "v_mfma_f32_16x16x32_bf16 v[232:235], v[144:147], v[124:127], v[232:235]\n\t" \
  "v_mfma_f32_16x16x32_bf16 v[240:243], v[72:75], v[124:127], v[240:243]\n\t" \
  "v_mfma_f32_16x16x32_bf16 v[240:243], v[140:143], v[88:91], v[240:243]\n\t" \
  "v_mfma_f32_16x16x32_bf16 v[248:251], v[96:99], v[88:91], v[248:251]\n\t" \
  "v_mfma_f32_16x16x32_bf16 v[248:251], v[132:135], v[124:127], v[248:251]\n\t" \
  "v_mfma_f32_16x16x32_bf16 v[252:255], v[96:99], v[108:111], v[252:255]\n\t" \
  "s_waitcnt lgkmcnt(0)\n\t" \
  "v_mfma_f32_16x16x32_bf16 v[252:255], v[132:135], v[148:151], v[252:255]\n\t" \
  "v_mfma_f32_16x16x32_bf16 v[220:223], v[112:115], v[148:151], v[220:223]\n\t" \
  "v_mfma_f32_16x16x32_bf16 v[220:223], v[100:103], v[108:111], v[220:223]\n\t" \
  "s_barrier\n\t" \
  "v_mfma_f32_16x16x32_bf16 v[228:231], v[120:123], v[108:111], v[228:231]\n\t" \
  "ds_read_b64_tr_b16 v[92:93], v62 offset:26624\n\t" \
  "v_mfma_f32_16x16x32_bf16 v[228:231], v[128:131], v[148:151], v[228:231]\n\t" \
  "ds_read_b64_tr_b16 v[94:95], v62 offset:27136\n\t" \
  "v_mfma_f32_16x16x32_bf16 v[236:239], v[144:147], v[148:151], v[236:239]\n\t" \
  "ds_read_b128 v[80:83], v60 offset:10240\n\t" \
  "v_mfma_f32_16x16x32_bf16 v[236:239], v[136:139], v[108:111], v[236:239]\n\t" \
  "ds_read_b64_tr_b16 v[68:69], v62 offset:30720\n\t" \
  "v_mfma_f32_16x16x32_bf16 v[244:247], v[140:143], v[108:111], v[244:247]\n\t" \
  "ds_read_b64_tr_b16 v[70:71], v62 offset:31232\n\t" \
  "v_mfma_f32_16x16x32_bf16 v[244:247], v[72:75], v[148:151], v[244:247]\n\t" \
  "ds_read_b128 v[116:119], v61 offset:10240\n\t" \
  "v_mfma_f32_16x16x32_bf16 v[152:155], v[64:67], v[100:103], v[152:155]\n\t" \
  "ds_read_b128 v[88:91], v61 offset:12288\n\t" \
  "v_mfma_f32_16x16x32_bf16 v[152:155], v[84:87], v[112:115], v[152:155]\n\t" \
  "ds_read_b128 v[124:127], v60 offset:12288\n\t" \
  "v_mfma_f32_16x16x32_bf16 v[160:163], v[84:87], v[128:131], v[160:163]\n\t" \
  "ds_read_b128 v[96:99], v60 offset:14336\n\t" \
  "v_mfma_f32_16x16x32_bf16 v[160:163], v[64:67], v[120:123], v[160:163]\n\t" \
  "ds_read_b128 v[132:135], v61 offset:14336\n\t" \
  "v_mfma_f32_16x16x32_bf16 v[168:171], v[64:67], v[136:139], v[168:171]\n\t" \
  "ds_read_b128 v[108:111], v61 offset:16384\n\t" \
  "v_mfma_f32_16x16x32_bf16 v[168:171], v[84:87], v[144:147], v[168:171]\n\t" \
  "ds_read_b128 v[148:151], v60 offset:16384\n\t" \
  "v_mfma_f32_16x16x32_bf16 v[176:179], v[84:87], v[72:75], v[176:179]\n\t" \
  "ds_read_b64_tr_b16 v[84:85], v63 offset:26624\n\t" \
  "v_mfma_f32_16x16x32_bf16 v[176:179], v[64:67], v[140:143], v[176:179]\n\t" \
  "ds_read_b64_tr_b16 v[86:87], v63 offset:27136\n\t" \
  "v_mfma_f32_16x16x32_bf16 v[156:159], v[76:79], v[100:103], v[156:159]\n\t" \
  "ds_read_b64_tr_b16 v[64:65], v63 offset:30720\n\t" \
  "v_mfma_f32_16x16x32_bf16 v[156:159], v[104:107], v[112:115], v[156:159]\n\t" \
  "ds_read_b64_tr_b16 v[66:67], v63 offset:31232\n\t" \
  "v_mfma_f32_16x16x32_bf16 v[164:167], v[104:107], v[128:131], v[164:167]\n\t" \
  "ds_read_b64_tr_b16 v[100:101], v62 offset:34816\n\t" \
  "v_mfma_f32_16x16x32_bf16 v[164:167], v[76:79], v[120:123], v[164:167]\n\t" \
  "ds_read_b64_tr_b16 v[102:103], v62 offset:35328\n\t" \
  "v_mfma_f32_16x16x32_bf16 v[172:175], v[76:79], v[136:139], v[172:175]\n\t" \
  "ds_read_b64_tr_b16 v[112:113], v62 offset:38912\n\t" \
  "v_mfma_f32_16x16x32_bf16 v[172:175], v[104:107], v[144:147], v[172:175]\n\t" \
  "ds_read_b64_tr_b16 v[114:115], v62 offset:39424\n\t" \
  "v_mfma_f32_16x16x32_bf16 v[180:183], v[104:107], v[72:75], v[180:183]\n\t" \
  "ds_read_b128 v[128:131], v60 offset:0\n\t" \
  "v_mfma_f32_16x16x32_bf16 v[180:183], v[76:79], v[140:143], v[180:183]\n\t" \
  "ds_read_b128 v[120:123], v61 offset:0\n\t" \
  "s_waitcnt lgkmcnt(7)\n\t" \
  "v_mfma_f32_16x16x32_bf16 v[184:187], v[92:95], v[80:83], v[184:187]\n\t" \
  "ds_read_b64_tr_b16 v[136:137], v63 offset:34816\n\t" \
  "v_mfma_f32_16x16x32_bf16 v[184:187], v[68:71], v[116:119], v[184:187]\n\t" \
  "ds_read_b64_tr_b16 v[138:139], v63 offset:35328\n\t" \
  "v_mfma_f32_16x16x32_bf16 v[192:195], v[68:71], v[88:91], v[192:195]\n\t" \
  "ds_read_b64_tr_b16 v[144:145], v63 offset:38912\n\t" \
  "v_mfma_f32_16x16x32_bf16 v[192:195], v[92:95], v[124:127], v[192:195]\n\t" \
  "ds_read_b64_tr_b16 v[146:147], v63 offset:39424\n\t" \
  "v_mfma_f32_16x16x32_bf16 v[200:203], v[92:95], v[96:99], v[200:203]\n\t" \
  "ds_read_b64_tr_b16 v[72:73], v62 offset:18432\n\t" \
  "v_mfma_f32_16x16x32_bf16 v[200:203], v[68:71], v[132:135], v[200:203]\n\t" \
  "ds_read_b64_tr_b16 v[74:75], v62 offset:18944\n\t" \
  "v_mfma_f32_16x16x32_bf16 v[208:211], v[68:71], v[108:111], v[208:211]\n\t" \
  "ds_read_b64_tr_b16 v[104:105], v62 offset:22528\n\t" \
  "v_mfma_f32_16x16x32_bf16 v[208:211], v[92:95], v[148:151], v[208:211]\n\t" \
  "ds_read_b64_tr_b16 v[106:107], v62 offset:23040\n\t" \
  "v_mfma_f32_16x16x32_bf16 v[188:191], v[84:87], v[80:83], v[188:191]\n\t" \
  "ds_read_b64_tr_b16 v[76:77], v63 offset:18432\n\t" \
  "s_waitcnt lgkmcnt(7)\n\t" \
  "v_mfma_f32_16x16x32_bf16 v[188:191], v[64:67], v[116:119], v[188:191]\n\t" \
  "ds_read_b64_tr_b16 v[78:79], v63 offset:18944\n\t" \
  "v_mfma_f32_16x16x32_bf16 v[196:199], v[64:67], v[88:91], v[196:199]\n\t" \
  "ds_read_b64_tr_b16 v[140:141], v63 offset:22528\n\t" \
  "v_mfma_f32_16x16x32_bf16 v[196:199], v[84:87], v[124:127], v[196:199]\n\t" \
  "ds_read_b64_tr_b16 v[142:143], v63 offset:23040\n\t" \
  "v_mfma_f32_16x16x32_bf16 v[204:207], v[84:87], v[96:99], v[204:207]\n\t" \
  "v_mfma_f32_16x16x32_bf16 v[204:207], v[64:67], v[132:135], v[204:207]\n\t" \
  "v_mfma_f32_16x16x32_bf16 v[212:215], v[64:67], v[108:111], v[212:215]\n\t" \
  "v_mfma_f32_16x16x32_bf16 v[212:215], v[84:87], v[148:151], v[212:215]\n\t" \
  "v_mfma_f32_16x16x32_bf16 v[216:219], v[80:83], v[100:103], v[216:219]\n\t" \
  "501:\n\t" \
  "s_cbranch_execz 505f\n\t" \
  "v_mfma_f32_16x16x32_bf16 v[216:219], v[116:119], v[112:115], v[216:219]\n\t" \
  "v_mfma_f32_16x16x32_bf16 v[224:227], v[88:91], v[112:115], v[224:227]\n\t" \
  "v_mfma_f32_16x16x32_bf16 v[224:227], v[124:127], v[100:103], v[224:227]\n\t" \
  "v_mfma_f32_16x16x32_bf16 v[232:235], v[96:99], v[100:103], v[232:235]\n\t" \
  "v_mfma_f32_16x16x32_bf16 v[232:235], v[132:135], v[112:115], v[232:235]\n\t" \
  "v_mfma_f32_16x16x32_bf16 v[240:243], v[108:111], v[112:115], v[240:243]\n\t" \
  "v_mfma_f32_16x16x32_bf16 v[240:243], v[148:151], v[100:103], v[240:243]\n\t" \
  "v_mfma_f32_16x16x32_bf16 v[248:251], v[128:131], v[100:103], v[248:251]\n\t" \
  "v_mfma_f32_16x16x32_bf16 v[248:251], v[120:123], v[112:115], v[248:251]\n\t" \
  "v_mfma_f32_16x16x32_bf16 v[252:255], v[128:131], v[136:139], v[252:255]\n\t" \
  "s_waitcnt lgkmcnt(0)\n\t" \
  "v_mfma_f32_16x16x32_bf16 v[252:255], v[120:123], v[144:147], v[252:255]\n\t" \
  "v_mfma_f32_16x16x32_bf16 v[220:223], v[116:119], v[144:147], v[220:223]\n\t" \
  "v_mfma_f32_16x16x32_bf16 v[220:223], v[80:83], v[136:139], v[220:223]\n\t" \
  "s_waitcnt vmcnt(0)\n\t" \
  "s_barrier\n\t" \
  "v_mfma_f32_16x16x32_bf16 v[228:231], v[124:127], v[136:139], v[228:231]\n\t" \
  "ds_read_b64_tr_b16 v[68:69], v54 offset:26624\n\t" \
  "v_mfma_f32_16x16x32_bf16 v[228:231], v[88:91], v[144:147], v[228:231]\n\t" \
  "ds_read_b64_tr_b16 v[70:71], v54 offset:27136\n\t" \
  "v_mfma_f32_16x16x32_bf16 v[236:239], v[132:135], v[144:147], v[236:239]\n\t" \
  "ds_read_b128 v[92:95], v52 offset:10240\n\t" \
  "v_mfma_f32_16x16x32_bf16 v[236:239], v[96:99], v[136:139], v[236:239]\n\t" \
  "ds_read_b64_tr_b16 v[64:65], v54 offset:30720\n\t" \
  "v_mfma_f32_16x16x32_bf16 v[244:247], v[148:151], v[136:139], v[244:247]\n\t" \
  "ds_read_b64_tr_b16 v[66:67], v54 offset:31232\n\t" \
  "v_mfma_f32_16x16x32_bf16 v[244:247], v[108:111], v[144:147], v[244:247]\n\t" \
  "ds_read_b128 v[84:87], v53 offset:10240\n\t" \
  "v_mfma_f32_16x16x32_bf16 v[152:155], v[72:75], v[80:83], v[152:155]\n\t" \
  "ds_read_b128 v[100:103], v53 offset:12288\n\t" \
  "v_mfma_f32_16x16x32_bf16 v[152:155], v[104:107], v[116:119], v[152:155]\n\t" \
  "ds_read_b128 v[112:115], v52 offset:12288\n\t" \
  "v_mfma_f32_16x16x32_bf16 v[160:163], v[104:107], v[88:91], v[160:163]\n\t" \
  "ds_read_b128 v[128:131], v52 offset:14336\n\t" \
  "v_mfma_f32_16x16x32_bf16 v[160:163], v[72:75], v[124:127], v[160:163]\n\t" \
  "ds_read_b128 v[120:123], v53 offset:14336\n\t" \
  "v_mfma_f32_16x16x32_bf16 v[168:171], v[72:75], v[96:99], v[168:171]\n\t" \
  "ds_read_b128 v[136:139], v53 offset:16384\n\t" \
  "v_mfma_f32_16x16x32_bf16 v[168:171], v[104:107], v[132:135], v[168:171]\n\t" \
  "ds_read_b128 v[144:147], v52 offset:16384\n\t" \
  "v_mfma_f32_16x16x32_bf16 v[176:179], v[104:107], v[108:111], v[176:179]\n\t" \
  "ds_read_b64_tr_b16 v[104:105], v55 offset:26624\n\t" \
  "v_mfma_f32_16x16x32_bf16 v[176:179], v[72:75], v[148:151], v[176:179]\n\t" \
  "ds_read_b64_tr_b16 v[106:107], v55 offset:27136\n\t" \
  "v_mfma_f32_16x16x32_bf16 v[156:159], v[76:79], v[80:83], v[156:159]\n\t" \
  "ds_read_b64_tr_b16 v[72:73], v55 offset:30720\n\t" \
  "v_mfma_f32_16x16x32_bf16 v[156:159], v[140:143], v[116:119], v[156:159]\n\t" \
  "ds_read_b64_tr_b16 v[74:75], v55 offset:31232\n\t" \
  "v_mfma_f32_16x16x32_bf16 v[164:167], v[140:143], v[88:91], v[164:167]\n\t" \
  "ds_read_b64_tr_b16 v[80:81], v54 offset:34816\n\t" \
  "v_mfma_f32_16x16x32_bf16 v[164:167], v[76:79], v[124:127], v[164:167]\n\t" \
  "ds_read_b64_tr_b16 v[82:83], v54 offset:35328\n\t" \
  "v_mfma_f32_16x16x32_bf16 v[172:175], v[76:79], v[96:99], v[172:175]\n\t" \
  "ds_read_b64_tr_b16 v[116:117], v54 offset:38912\n\t" \
  "v_mfma_f32_16x16x32_bf16 v[172:175], v[140:143], v[132:135], v[172:175]\n\t" \
  "ds_read_b64_tr_b16 v[118:119], v54 offset:39424\n\t" \
  "v_mfma_f32_16x16x32_bf16 v[180:183], v[140:143], v[108:111], v[180:183]\n\t" \
  "ds_read_b128 v[88:91], v52 offset:0\n\t" \
  "v_mfma_f32_16x16x32_bf16 v[180:183], v[76:79], v[148:151], v[180:183]\n\t" \
  "ds_read_b128 v[124:127], v53 offset:0\n\t" \
  "s_waitcnt lgkmcnt(7)\n\t" \
  "v_mfma_f32_16x16x32_bf16 v[184:187], v[68:71], v[92:95], v[184:187]\n\t" \
  "ds_read_b64_tr_b16 v[96:97], v55 offset:34816\n\t" \
  "v_mfma_f32_16x16x32_bf16 v[184:187], v[64:67], v[84:87], v[184:187]\n\t" \
  "ds_read_b64_tr_b16 v[98:99], v55 offset:35328\n\t" \
  "v_mfma_f32_16x16x32_bf16 v[192:195], v[64:67], v[100:103], v[192:195]\n\t" \
  "ds_read_b64_tr_b16 v[132:133], v55 offset:38912\n\t" \
  "v_mfma_f32_16x16x32_bf16 v[192:195], v[68:71], v[112:115], v[192:195]\n\t" \
  "ds_read_b64_tr_b16 v[134:135], v55 offset:39424\n\t" \
  "v_mfma_f32_16x16x32_bf16 v[200:203], v[68:71], v[128:131], v[200:203]\n\t" \
  "ds_read_b64_tr_b16 v[108:109], v54 offset:18432\n\t" \
  "v_mfma_f32_16x16x32_bf16 v[200:203], v[64:67], v[120:123], v[200:203]\n\t" \
  "ds_read_b64_tr_b16 v[110:111], v54 offset:18944\n\t" \
  "v_mfma_f32_16x16x32_bf16 v[208:211], v[64:67], v[136:139], v[208:211]\n\t" \
  "ds_read_b64_tr_b16 v[140:141], v54 offset:22528\n\t" \
  "v_mfma_f32_16x16x32_bf16 v[208:211], v[68:71], v[144:147], v[208:211]\n\t" \
  "ds_read_b64_tr_b16 v[142:143], v54 offset:23040\n\t" \
  "v_mfma_f32_16x16x32_bf16 v[188:191], v[104:107], v[92:95], v[188:191]\n\t" \
  "ds_read_b64_tr_b16 v[76:77], v55 offset:18432\n\t" \
  "s_waitcnt lgkmcnt(7)\n\t" \
  "v_mfma_f32_16x16x32_bf16 v[188:191], v[72:75], v[84:87], v[188:191]\n\t" \
  "ds_read_b64_tr_b16 v[78:79], v55 offset:18944\n\t" \
  "v_mfma_f32_16x16x32_bf16 v[196:199], v[72:75], v[100:103], v[196:199]\n\t" \
  "ds_read_b64_tr_b16 v[148:149], v55 offset:22528\n\t" \
  "v_mfma_f32_16x16x32_bf16 v[196:199], v[104:107], v[112:115], v[196:199]\n\t" \
  "ds_read_b64_tr_b16 v[150:151], v55 offset:23040\n\t" \
  "v_mfma_f32_16x16x32_bf16 v[204:207], v[104:107], v[128:131], v[204:207]\n\t" \
  "v_mfma_f32_16x16x32_bf16 v[204:207], v[72:75], v[120:123], v[204:207]\n\t" \
  "v_mfma_f32_16x16x32_bf16 v[212:215], v[72:75], v[136:139], v[212:215]\n\t" \
  "v_mfma_f32_16x16x32_bf16 v[212:215], v[104:107], v[144:147], v[212:215]\n\t" \
  "v_mfma_f32_16x16x32_bf16 v[216:219], v[92:95], v[80:83], v[216:219]\n\t" \
  "v_mfma_f32_16x16x32_bf16 v[216:219], v[84:87], v[116:119], v[216:219]\n\t" \
  "v_mfma_f32_16x16x32_bf16 v[224:227], v[100:103], v[116:119], v[224:227]\n\t" \
  "v_mfma_f32_16x16x32_bf16 v[224:227], v[112:115], v[80:83], v[224:227]\n\t" \
  "v_mfma_f32_16x16x32_bf16 v[232:235], v[128:131], v[80:83], v[232:235]\n\t" \
  "v_mfma_f32_16x16x32_bf16 v[232:235], v[120:123], v[116:119], v[232:235]\n\t" \
  "v_mfma_f32_16x16x32_bf16 v[240:243], v[136:139], v[116:119], v[240:243]\n\t" \
  "v_mfma_f32_16x16x32_bf16 v[240:243], v[144:147], v[80:83], v[240:243]\n\t" \
  "v_mfma_f32_16x16x32_bf16 v[248:251], v[88:91], v[80:83], v[248:251]\n\t" \
  "v_mfma_f32_16x16x32_bf16 v[248:251], v[124:127], v[116:119], v[248:251]\n\t" \
  "v_mfma_f32_16x16x32_bf16 v[252:255], v[88:91], v[96:99], v[252:255]\n\t" \
  "s_waitcnt lgkmcnt(0)\n\t" \
  "v_mfma_f32_16x16x32_bf16 v[252:255], v[124:127], v[132:135], v[252:255]\n\t" \
  "v_mfma_f32_16x16x32_bf16 v[220:223], v[84:87], v[132:135], v[220:223]\n\t" \
  "v_mfma_f32_16x16x32_bf16 v[220:223], v[92:95], v[96:99], v[220:223]\n\t" \
  "s_barrier\n\t" \
  "v_mfma_f32_16x16x32_bf16 v[228:231], v[112:115], v[96:99], v[228:231]\n\t" \
  "ds_read_b64_tr_b16 v[64:65], v58 offset:26624\n\t" \
  "v_mfma_f32_16x16x32_bf16 v[228:231], v[100:103], v[132:135], v[228:231]\n\t" \
  "ds_read_b64_tr_b16 v[66:67], v58 offset:27136\n\t" \
  "v_mfma_f32_16x16x32_bf16 v[236:239], v[120:123], v[132:135], v[236:239]\n\t" \
  "ds_read_b128 v[68:71], v56 offset:10240\n\t" \
  "v_mfma_f32_16x16x32_bf16 v[236:239], v[128:131], v[96:99], v[236:239]\n\t" \
  "ds_read_b64_tr_b16 v[72:73], v58 offset:30720\n\t" \
  "v_mfma_f32_16x16x32_bf16 v[244:247], v[144:147], v[96:99], v[244:247]\n\t" \
  "ds_read_b64_tr_b16 v[74:75], v58 offset:31232\n\t" \
  "v_mfma_f32_16x16x32_bf16 v[244:247], v[136:139], v[132:135], v[244:247]\n\t" \
  "ds_read_b128 v[104:107], v57 offset:10240\n\t" \
  "v_mfma_f32_16x16x32_bf16 v[152:155], v[108:111], v[92:95], v[152:155]\n\t" \
  "ds_read_b128 v[80:83], v57 offset:12288\n\t" \
  "v_mfma_f32_16x16x32_bf16 v[152:155], v[140:143], v[84:87], v[152:155]\n\t" \
  "ds_read_b128 v[116:119], v56 offset:12288\n\t" \
  "v_mfma_f32_16x16x32_bf16 v[160:163], v[140:143], v[100:103], v[160:163]\n\t" \
  "ds_read_b128 v[88:91], v56 offset:14336\n\t" \
  "v_mfma_f32_16x16x32_bf16 v[160:163], v[108:111], v[112:115], v[160:163]\n\t" \
  "ds_read_b128 v[124:127], v57 offset:14336\n\t" \
  "v_mfma_f32_16x16x32_bf16 v[168:171], v[108:111], v[128:131], v[168:171]\n\t" \
  "ds_read_b128 v[96:99], v57 offset:16384\n\t" \
  "v_mfma_f32_16x16x32_bf16 v[168:171], v[140:143], v[120:123], v[168:171]\n\t" \
  "ds_read_b128 v[132:135], v56 offset:16384\n\t" \
  "v_mfma_f32_16x16x32_bf16 v[176:179], v[140:143], v[136:139], v[176:179]\n\t" \
  "ds_read_b64_tr_b16 v[140:141], v59 offset:26624\n\t" \
  "v_mfma_f32_16x16x32_bf16 v[176:179], v[108:111], v[144:147], v[176:179]\n\t" \
  "ds_read_b64_tr_b16 v[142:143], v59 offset:27136\n\t" \
  "v_mfma_f32_16x16x32_bf16 v[156:159], v[76:79], v[92:95], v[156:159]\n\t" \
  "ds_read_b64_tr_b16 v[108:109], v59 offset:30720\n\t" \
  "v_mfma_f32_16x16x32_bf16 v[156:159], v[148:151], v[84:87], v[156:159]\n\t" \
  "ds_read_b64_tr_b16 v[110:111], v59 offset:31232\n\t" \
  "v_mfma_f32_16x16x32_bf16 v[164:167], v[148:151], v[100:103], v[164:167]\n\t" \
  "ds_read_b64_tr_b16 v[92:93], v58 offset:34816\n\t" \
  "v_mfma_f32_16x16x32_bf16 v[164:167], v[76:79], v[112:115], v[164:167]\n\t" \
  "ds_read_b64_tr_b16 v[94:95], v58 offset:35328\n\t" \
  "v_mfma_f32_16x16x32_bf16 v[172:175], v[76:79], v[128:131], v[172:175]\n\t" \
  "ds_read_b64_tr_b16 v[84:85], v58 offset:38912\n\t" \
  "v_mfma_f32_16x16x32_bf16 v[172:175], v[148:151], v[120:123], v[172:175]\n\t" \
  "ds_read_b64_tr_b16 v[86:87], v58 offset:39424\n\t" \
  "v_mfma_f32_16x16x32_bf16 v[180:183], v[148:151], v[136:139], v[180:183]\n\t" \
  "ds_read_b128 v[100:103], v56 offset:0\n\t" \
  "v_mfma_f32_16x16x32_bf16 v[180:183], v[76:79], v[144:147], v[180:183]\n\t" \
  "ds_read_b128 v[112:115], v57 offset:0\n\t" \
  "s_waitcnt lgkmcnt(7)\n\t" \
  "v_mfma_f32_16x16x32_bf16 v[184:187], v[64:67], v[68:71], v[184:187]\n\t" \
  "ds_read_b64_tr_b16 v[128:129], v59 offset:34816\n\t" \
  "v_mfma_f32_16x16x32_bf16 v[184:187], v[72:75], v[104:107], v[184:187]\n\t" \
  "ds_read_b64_tr_b16 v[130:131], v59 offset:35328\n\t" \
  "v_mfma_f32_16x16x32_bf16 v[192:195], v[72:75], v[80:83], v[192:195]\n\t" \
  "ds_read_b64_tr_b16 v[120:121], v59 offset:38912\n\t" \
  "v_mfma_f32_16x16x32_bf16 v[192:195], v[64:67], v[116:119], v[192:195]\n\t" \
  "ds_read_b64_tr_b16 v[122:123], v59 offset:39424\n\t" \
  "v_mfma_f32_16x16x32_bf16 v[200:203], v[64:67], v[88:91], v[200:203]\n\t" \
  "ds_read_b64_tr_b16 v[136:137], v58 offset:18432\n\t" \
  "v_mfma_f32_16x16x32_bf16 v[200:203], v[72:75], v[124:127], v[200:203]\n\t" \
  "ds_read_b64_tr_b16 v[138:139], v58 offset:18944\n\t" \
  "v_mfma_f32_16x16x32_bf16 v[208:211], v[72:75], v[96:99], v[208:211]\n\t" \
  "ds_read_b64_tr_b16 v[148:149], v58 offset:22528\n\t" \
  "v_mfma_f32_16x16x32_bf16 v[208:211], v[64:67], v[132:135], v[208:211]\n\t" \
  "ds_read_b64_tr_b16 v[150:151], v58 offset:23040\n\t" \
  "v_mfma_f32_16x16x32_bf16 v[188:191], v[140:143], v[68:71], v[188:191]\n\t" \
  "ds_read_b64_tr_b16 v[76:77], v59 offset:18432\n\t" \
  "s_waitcnt lgkmcnt(7)\n\t" \
  "v_mfma_f32_16x16x32_bf16 v[188:191], v[108:111], v[104:107], v[188:191]\n\t" \
  "ds_read_b64_tr_b16 v[78:79], v59 offset:18944\n\t" \
  "v_mfma_f32_16x16x32_bf16 v[196:199], v[108:111], v[80:83], v[196:199]\n\t" \
  "ds_read_b64_tr_b16 v[144:145], v59 offset:22528\n\t" \
  "v_mfma_f32_16x16x32_bf16 v[196:199], v[140:143], v[116:119], v[196:199]\n\t" \
  "ds_read_b64_tr_b16 v[146:147], v59 offset:23040\n\t" \
  "v_mfma_f32_16x16x32_bf16 v[204:207], v[140:143], v[88:91], v[204:207]\n\t" \
  "v_mfma_f32_16x16x32_bf16 v[204:207], v[108:111], v[124:127], v[204:207]\n\t" \
  "v_mfma_f32_16x16x32_bf16 v[212:215], v[108:111], v[96:99], v[212:215]\n\t" \
  "v_mfma_f32_16x16x32_bf16 v[212:215], v[140:143], v[132:135], v[212:215]\n\t" \
  "v_mfma_f32_16x16x32_bf16 v[216:219], v[68:71], v[92:95], v[216:219]\n\t" \
  "v_mfma_f32_16x16x32_bf16 v[216:219], v[104:107], v[84:87], v[216:219]\n\t" \
  "v_mfma_f32_16x16x32_bf16 v[224:227], v[80:83], v[84:87], v[224:227]\n\t" \
  "v_mfma_f32_16x16x32_bf16 v[224:227], v[116:119], v[92:95], v[224:227]\n\t" \
  "v_mfma_f32_16x16x32_bf16 v[232:235], v[88:91], v[92:95], v[232:235]\n\t" \
  "v_mfma_f32_16x16x32_bf16 v[232:235], v[124:127], v[84:87], v[232:235]\n\t" \
  "v_mfma_f32_16x16x32_bf16 v[240:243], v[96:99], v[84:87], v[240:243]\n\t" \
  "v_mfma_f32_16x16x32_bf16 v[240:243], v[132:135], v[92:95], v[240:243]\n\t" \
  "v_mfma_f32_16x16x32_bf16 v[248:251], v[100:103], v[92:95], v[248:251]\n\t" \
  "v_mfma_f32_16x16x32_bf16 v[248:251], v[112:115], v[84:87], v[248:251]\n\t" \
  "v_mfma_f32_16x16x32_bf16 v[252:255], v[100:103], v[128:131], v[252:255]\n\t" \
  "s_waitcnt lgkmcnt(0)\n\t" \
  "v_mfma_f32_16x16x32_bf16 v[252:255], v[112:115], v[120:123], v[252:255]\n\t" \
  "v_mfma_f32_16x16x32_bf16 v[220:223], v[104:107], v[120:123], v[220:223]\n\t" \
  "v_mfma_f32_16x16x32_bf16 v[220:223], v[68:71], v[128:131], v[220:223]\n\t" \
  "s_barrier\n\t" \
  "v_mfma_f32_16x16x32_bf16 v[228:231], v[116:119], v[128:131], v[228:231]\n\t" \
  "ds_read_b64_tr_b16 v[72:73], v62 offset:26624\n\t" \
  "v_mfma_f32_16x16x32_bf16 v[228:231], v[80:83], v[120:123], v[228:231]\n\t" \
  "ds_read_b64_tr_b16 v[74:75], v62 offset:27136\n\t" \
  "v_mfma_f32_16x16x32_bf16 v[236:239], v[124:127], v[120:123], v[236:239]\n\t" \
  "ds_read_b128 v[64:67], v60 offset:10240\n\t" \
  "v_mfma_f32_16x16x32_bf16 v[236:239], v[88:91], v[128:131], v[236:239]\n\t" \
  "ds_read_b64_tr_b16 v[108:109], v62 offset:30720\n\t" \
  "v_mfma_f32_16x16x32_bf16 v[244:247], v[132:135], v[128:131], v[244:247]\n\t" \
  "ds_read_b64_tr_b16 v[110:111], v62 offset:31232\n\t" \
  "v_mfma_f32_16x16x32_bf16 v[244:247], v[96:99], v[120:123], v[244:247]\n\t" \
  "ds_read_b128 v[140:143], v61 offset:10240\n\t" \
  "v_mfma_f32_16x16x32_bf16 v[152:155], v[136:139], v[68:71], v[152:155]\n\t" \
  "ds_read_b128 v[92:95], v61 offset:12288\n\t" \
  "v_mfma_f32_16x16x32_bf16 v[152:155], v[148:151], v[104:107], v[152:155]\n\t" \
  "ds_read_b128 v[84:87], v60 offset:12288\n\t" \
  "v_mfma_f32_16x16x32_bf16 v[160:163], v[148:151], v[80:83], v[160:163]\n\t" \
  "ds_read_b128 v[100:103], v60 offset:14336\n\t" \
  "v_mfma_f32_16x16x32_bf16 v[160:163], v[136:139], v[116:119], v[160:163]\n\t" \
  "ds_read_b128 v[112:115], v61 offset:14336\n\t" \
  "v_mfma_f32_16x16x32_bf16 v[168:171], v[136:139], v[88:91], v[168:171]\n\t" \
  "ds_read_b128 v[128:131], v61 offset:16384\n\t" \
  "v_mfma_f32_16x16x32_bf16 v[168:171], v[148:151], v[124:127], v[168:171]\n\t" \
  "ds_read_b128 v[120:123], v60 offset:16384\n\t" \
  "v_mfma_f32_16x16x32_bf16 v[176:179], v[148:151], v[96:99], v[176:179]\n\t" \
  "ds_read_b64_tr_b16 v[148:149], v63 offset:26624\n\t" \
  "v_mfma_f32_16x16x32_bf16 v[176:179], v[136:139], v[132:135], v[176:179]\n\t" \
  "ds_read_b64_tr_b16 v[150:151], v63 offset:27136\n\t" \
  "v_mfma_f32_16x16x32_bf16 v[156:159], v[76:79], v[68:71], v[156:159]\n\t" \
  "ds_read_b64_tr_b16 v[136:137], v63 offset:30720\n\t" \
  "v_mfma_f32_16x16x32_bf16 v[156:159], v[144:147], v[104:107], v[156:159]\n\t" \
  "ds_read_b64_tr_b16 v[138:139], v63 offset:31232\n\t" \
  "v_mfma_f32_16x16x32_bf16 v[164:167], v[144:147], v[80:83], v[164:167]\n\t" \
  "ds_read_b64_tr_b16 v[68:69], v62 offset:34816\n\t" \
  "v_mfma_f32_16x16x32_bf16 v[164:167], v[76:79], v[116:119], v[164:167]\n\t" \
  "ds_read_b64_tr_b16 v[70:71], v62 offset:35328\n\t" \
  "v_mfma_f32_16x16x32_bf16 v[172:175], v[76:79], v[88:91], v[172:175]\n\t" \
  "ds_read_b64_tr_b16 v[104:105], v62 offset:38912\n\t" \
  "v_mfma_f32_16x16x32_bf16 v[172:175], v[144:147], v[124:127], v[172:175]\n\t" \
  "ds_read_b64_tr_b16 v[106:107], v62 offset:39424\n\t" \
  "v_mfma_f32_16x16x32_bf16 v[180:183], v[144:147], v[96:99], v[180:183]\n\t" \
  "ds_read_b128 v[80:83], v60 offset:0\n\t" \
  "v_mfma_f32_16x16x32_bf16 v[180:183], v[76:79], v[132:135], v[180:183]\n\t" \
  "ds_read_b128 v[116:119], v61 offset:0\n\t" \
  "s_waitcnt lgkmcnt(7)\n\t" \
  "v_mfma_f32_16x16x32_bf16 v[184:187], v[72:75], v[64:67], v[184:187]\n\t" \
  "ds_read_b64_tr_b16 v[88:89], v63 offset:34816\n\t" \
  "v_mfma_f32_16x16x32_bf16 v[184:187], v[108:111], v[140:143], v[184:187]\n\t" \
  "ds_read_b64_tr_b16 v[90:91], v63 offset:35328\n\t" \
  "v_mfma_f32_16x16x32_bf16 v[192:195], v[108:111], v[92:95], v[192:195]\n\t" \
  "ds_read_b64_tr_b16 v[124:125], v63 offset:38912\n\t" \
  "v_mfma_f32_16x16x32_bf16 v[192:195], v[72:75], v[84:87], v[192:195]\n\t" \
  "ds_read_b64_tr_b16 v[126:127], v63 offset:39424\n\t" \
  "v_mfma_f32_16x16x32_bf16 v[200:203], v[72:75], v[100:103], v[200:203]\n\t" \
  "ds_read_b64_tr_b16 v[96:97], v62 offset:18432\n\t" \
  "v_mfma_f32_16x16x32_bf16 v[200:203], v[108:111], v[112:115], v[200:203]\n\t" \
  "ds_read_b64_tr_b16 v[98:99], v62 offset:18944\n\t" \
  "v_mfma_f32_16x16x32_bf16 v[208:211], v[108:111], v[128:131], v[208:211]\n\t" \
  "ds_read_b64_tr_b16 v[144:145], v62 offset:22528\n\t" \
  "v_mfma_f32_16x16x32_bf16 v[208:211], v[72:75], v[120:123], v[208:211]\n\t" \
  "ds_read_b64_tr_b16 v[146:147], v62 offset:23040\n\t" \
  "v_mfma_f32_16x16x32_bf16 v[188:191], v[148:151], v[64:67], v[188:191]\n\t" \
  "ds_read_b64_tr_b16 v[76:77], v63 offset:18432\n\t" \
  "s_waitcnt lgkmcnt(7)\n\t" \
  "v_mfma_f32_16x16x32_bf16 v[188:191], v[136:139], v[140:143], v[188:191]\n\t" \
  "ds_read_b64_tr_b16 v[78:79], v63 offset:18944\n\t" \
  "v_mfma_f32_16x16x32_bf16 v[196:199], v[136:139], v[92:95], v[196:199]\n\t" \
  "ds_read_b64_tr_b16 v[132:133], v63 offset:22528\n\t" \
  "v_mfma_f32_16x16x32_bf16 v[196:199], v[148:151], v[84:87], v[196:199]\n\t" \
  "ds_read_b64_tr_b16 v[134:135], v63 offset:23040\n\t" \
  "v_mfma_f32_16x16x32_bf16 v[204:207], v[148:151], v[100:103], v[204:207]\n\t" \
  "v_mfma_f32_16x16x32_bf16 v[204:207], v[136:139], v[112:115], v[204:207]\n\t" \
  "v_mfma_f32_16x16x32_bf16 v[212:215], v[136:139], v[128:131], v[212:215]\n\t" \
  "v_mfma_f32_16x16x32_bf16 v[212:215], v[148:151], v[120:123], v[212:215]\n\t" \
  "v_mfma_f32_16x16x32_bf16 v[216:219], v[64:67], v[68:71], v[216:219]\n\t" \
  "v_mfma_f32_16x16x32_bf16 v[216:219], v[140:143], v[104:107], v[216:219]\n\t" \
  "v_mfma_f32_16x16x32_bf16 v[224:227], v[92:95], v[104:107], v[224:227]\n\t" \
  "v_mfma_f32_16x16x32_bf16 v[224:227], v[84:87], v[68:71], v[224:227]\n\t" \
  "v_mfma_f32_16x16x32_bf16 v[232:235], v[100:103], v[68:71], v[232:235]\n\t" \
  "v_mfma_f32_16x16x32_bf16 v[232:235], v[112:115], v[104:107], v[232:235]\n\t" \
  "v_mfma_f32_16x16x32_bf16 v[240:243], v[128:131], v[104:107], v[240:243]\n\t" \
  "v_mfma_f32_16x16x32_bf16 v[240:243], v[120:123], v[68:71], v[240:243]\n\t" \
  "v_mfma_f32_16x16x32_bf16 v[248:251], v[80:83], v[68:71], v[248:251]\n\t" \
  "v_mfma_f32_16x16x32_bf16 v[248:251], v[116:119], v[104:107], v[248:251]\n\t" \
  "v_mfma_f32_16x16x32_bf16 v[252:255], v[80:83], v[88:91], v[252:255]\n\t" \
  "s_waitcnt lgkmcnt(0)\n\t" \
  "v_mfma_f32_16x16x32_bf16 v[252:255], v[116:119], v[124:127], v[252:255]\n\t" \
  "v_mfma_f32_16x16x32_bf16 v[220:223], v[140:143], v[124:127], v[220:223]\n\t" \
  "v_mfma_f32_16x16x32_bf16 v[220:223], v[64:67], v[88:91], v[220:223]\n\t" \
  "s_barrier\n\t" \
  "v_mfma_f32_16x16x32_bf16 v[228:231], v[84:87], v[88:91], v[228:231]\n\t" \
  "ds_read_b64_tr_b16 v[108:109], v54 offset:26624\n\t" \
  "v_mfma_f32_16x16x32_bf16 v[228:231], v[92:95], v[124:127], v[228:231]\n\t" \
  "ds_read_b64_tr_b16 v[110:111], v54 offset:27136\n\t" \
  "v_mfma_f32_16x16x32_bf16 v[236:239], v[112:115], v[124:127], v[236:239]\n\t" \
  "ds_read_b128 v[72:75], v52 offset:10240\n\t" \
  "v_mfma_f32_16x16x32_bf16 v[236:239], v[100:103], v[88:91], v[236:239]\n\t" \
  "ds_read_b64_tr_b16 v[136:137], v54 offset:30720\n\t" \
  "v_mfma_f32_16x16x32_bf16 v[244:247], v[120:123], v[88:91], v[244:247]\n\t" \
  "ds_read_b64_tr_b16 v[138:139], v54 offset:31232\n\t" \
  "v_mfma_f32_16x16x32_bf16 v[244:247], v[128:131], v[124:127], v[244:247]\n\t" \
  "ds_read_b128 v[148:151], v53 offset:10240\n\t" \
  "v_mfma_f32_16x16x32_bf16 v[152:155], v[96:99], v[64:67], v[152:155]\n\t" \
  "ds_read_b128 v[68:71], v53 offset:12288\n\t" \
  "v_mfma_f32_16x16x32_bf16 v[152:155], v[144:147], v[140:143], v[152:155]\n\t" \
  "ds_read_b128 v[104:107], v52 offset:12288\n\t" \
  "v_mfma_f32_16x16x32_bf16 v[160:163], v[144:147], v[92:95], v[160:163]\n\t" \
  "ds_read_b128 v[80:83], v52 offset:14336\n\t" \
  "v_mfma_f32_16x16x32_bf16 v[160:163], v[96:99], v[84:87], v[160:163]\n\t" \
  "ds_read_b128 v[116:119], v53 offset:14336\n\t" \
  "v_mfma_f32_16x16x32_bf16 v[168:171], v[96:99], v[100:103], v[168:171]\n\t" \
  "ds_read_b128 v[88:91], v53 offset:16384\n\t" \
  "v_mfma_f32_16x16x32_bf16 v[168:171], v[144:147], v[112:115], v[168:171]\n\t" \
  "ds_read_b128 v[124:127], v52 offset:16384\n\t" \
  "v_mfma_f32_16x16x32_bf16 v[176:179], v[144:147], v[128:131], v[176:179]\n\t" \
  "ds_read_b64_tr_b16 v[144:145], v55 offset:26624\n\t" \
  "v_mfma_f32_16x16x32_bf16 v[176:179], v[96:99], v[120:123], v[176:179]\n\t" \
  "ds_read_b64_tr_b16 v[146:147], v55 offset:27136\n\t" \
  "v_mfma_f32_16x16x32_bf16 v[156:159], v[76:79], v[64:67], v[156:159]\n\t" \
  "ds_read_b64_tr_b16 v[96:97], v55 offset:30720\n\t" \
  "v_mfma_f32_16x16x32_bf16 v[156:159], v[132:135], v[140:143], v[156:159]\n\t" \
  "ds_read_b64_tr_b16 v[98:99], v55 offset:31232\n\t" \
  "v_mfma_f32_16x16x32_bf16 v[164:167], v[132:135], v[92:95], v[164:167]\n\t" \
  "ds_read_b64_tr_b16 v[64:65], v54 offset:34816\n\t" \
  "v_mfma_f32_16x16x32_bf16 v[164:167], v[76:79], v[84:87], v[164:167]\n\t" \
  "ds_read_b64_tr_b16 v[66:67], v54 offset:35328\n\t" \
  "v_mfma_f32_16x16x32_bf16 v[172:175], v[76:79], v[100:103], v[172:175]\n\t" \
  "ds_read_b64_tr_b16 v[140:141], v54 offset:38912\n\t" \
  "v_mfma_f32_16x16x32_bf16 v[172:175], v[132:135], v[112:115], v[172:175]\n\t" \
  "ds_read_b64_tr_b16 v[142:143], v54 offset:39424\n\t" \
  "v_mfma_f32_16x16x32_bf16 v[180:183], v[132:135], v[128:131], v[180:183]\n\t" \
  "ds_read_b128 v[92:95], v52 offset:0\n\t" \
  "v_mfma_f32_16x16x32_bf16 v[180:183], v[76:79], v[120:123], v[180:183]\n\t" \
  "ds_read_b128 v[84:87], v53 offset:0\n\t" \
  "s_waitcnt lgkmcnt(7)\n\t" \
  "v_mfma_f32_16x16x32_bf16 v[184:187], v[108:111], v[72:75], v[184:187]\n\t" \
  "ds_read_b64_tr_b16 v[100:101], v55 offset:34816\n\t" \
  "v_mfma_f32_16x16x32_bf16 v[184:187], v[136:139], v[148:151], v[184:187]\n\t" \
  "ds_read_b64_tr_b16 v[102:103], v55 offset:35328\n\t" \
  "v_mfma_f32_16x16x32_bf16 v[192:195], v[136:139], v[68:71], v[192:195]\n\t" \
  "ds_read_b64_tr_b16 v[112:113], v55 offset:38912\n\t" \
  "v_mfma_f32_16x16x32_bf16 v[192:195], v[108:111], v[104:107], v[192:195]\n\t" \
  "ds_read_b64_tr_b16 v[114:115], v55 offset:39424\n\t" \
  "v_mfma_f32_16x16x32_bf16 v[200:203], v[108:111], v[80:83], v[200:203]\n\t" \
  "ds_read_b64_tr_b16 v[128:129], v54 offset:18432\n\t" \
  "v_mfma_f32_16x16x32_bf16 v[200:203], v[136:139], v[116:119], v[200:203]\n\t" \
  "ds_read_b64_tr_b16 v[130:131], v54 offset:18944\n\t" \
  "v_mfma_f32_16x16x32_bf16 v[208:211], v[136:139], v[88:91], v[208:211]\n\t" \
  "ds_read_b64_tr_b16 v[132:133], v54 offset:22528\n\t" \
  "v_mfma_f32_16x16x32_bf16 v[208:211], v[108:111], v[124:127], v[208:211]\n\t" \
  "ds_read_b64_tr_b16 v[134:135], v54 offset:23040\n\t" \
  "v_mfma_f32_16x16x32_bf16 v[188:191], v[144:147], v[72:75], v[188:191]\n\t" \
  "ds_read_b64_tr_b16 v[76:77], v55 offset:18432\n\t" \
  "s_waitcnt lgkmcnt(7)\n\t" \
  "v_mfma_f32_16x16x32_bf16 v[188:191], v[96:99], v[148:151], v[188:191]\n\t" \
  "ds_read_b64_tr_b16 v[78:79], v55 offset:18944\n\t" \
  "v_mfma_f32_16x16x32_bf16 v[196:199], v[96:99], v[68:71], v[196:199]\n\t" \
  "ds_read_b64_tr_b16 v[120:121], v55 offset:22528\n\t" \
  "v_mfma_f32_16x16x32_bf16 v[196:199], v[144:147], v[104:107], v[196:199]\n\t" \
  "ds_read_b64_tr_b16 v[122:123], v55 offset:23040\n\t" \
  "v_mfma_f32_16x16x32_bf16 v[204:207], v[144:147], v[80:83], v[204:207]\n\t" \
  "v_mfma_f32_16x16x32_bf16 v[204:207], v[96:99], v[116:119], v[204:207]\n\t" \
  "v_mfma_f32_16x16x32_bf16 v[212:215], v[96:99], v[88:91], v[212:215]\n\t" \
  "v_mfma_f32_16x16x32_bf16 v[212:215], v[144:147], v[124:127], v[212:215]\n\t" \
  "v_mfma_f32_16x16x32_bf16 v[216:219], v[72:75], v[64:67], v[216:219]\n\t" \
  "v_mfma_f32_16x16x32_bf16 v[216:219], v[148:151], v[140:143], v[216:219]\n\t" \
  "v_mfma_f32_16x16x32_bf16 v[224:227], v[68:71], v[140:143], v[224:227]\n\t" \
  "v_mfma_f32_16x16x32_bf16 v[224:227], v[104:107], v[64:67], v[224:227]\n\t" \
  "v_mfma_f32_16x16x32_bf16 v[232:235], v[80:83], v[64:67], v[232:235]\n\t" \
  "v_mfma_f32_16x16x32_bf16 v[232:235], v[116:119], v[140:143], v[232:235]\n\t" \
  "v_mfma_f32_16x16x32_bf16 v[240:243], v[88:91], v[140:143], v[240:243]\n\t" \
  "v_mfma_f32_16x16x32_bf16 v[240:243], v[124:127], v[64:67], v[240:243]\n\t" \
  "v_mfma_f32_16x16x32_bf16 v[248:251], v[92:95], v[64:67], v[248:251]\n\t" \
  "v_mfma_f32_16x16x32_bf16 v[248:251], v[84:87], v[140:143], v[248:251]\n\t" \
  "v_mfma_f32_16x16x32_bf16 v[252:255], v[92:95], v[100:103], v[252:255]\n\t" \
  "s_waitcnt lgkmcnt(0)\n\t" \
  "v_mfma_f32_16x16x32_bf16 v[252:255], v[84:87], v[112:115], v[252:255]\n\t" \
  "v_mfma_f32_16x16x32_bf16 v[220:223], v[148:151], v[112:115], v[220:223]\n\t" \
  "v_mfma_f32_16x16x32_bf16 v[220:223], v[72:75], v[100:103], v[220:223]\n\t" \
  "s_barrier\n\t" \
  "v_mfma_f32_16x16x32_bf16 v[228:231], v[104:107], v[100:103], v[228:231]\n\t" \
  "ds_read_b64_tr_b16 v[96:97], v58 offset:26624\n\t" \
  "v_mfma_f32_16x16x32_bf16 v[228:231], v[68:71], v[112:115], v[228:231]\n\t" \
  "ds_read_b64_tr_b16 v[98:99], v58 offset:27136\n\t" \
  "v_mfma_f32_16x16x32_bf16 v[236:239], v[116:119], v[112:115], v[236:239]\n\t" \
  "ds_read_b128 v[136:139], v56 offset:10240\n\t" \
  "v_mfma_f32_16x16x32_bf16 v[236:239], v[80:83], v[100:103], v[236:239]\n\t" \
  "ds_read_b64_tr_b16 v[64:65], v58 offset:30720\n\t" \
  "v_mfma_f32_16x16x32_bf16 v[244:247], v[124:127], v[100:103], v[244:247]\n\t" \
  "ds_read_b64_tr_b16 v[66:67], v58 offset:31232\n\t" \
  "v_mfma_f32_16x16x32_bf16 v[244:247], v[88:91], v[112:115], v[244:247]\n\t" \
  "ds_read_b128 v[108:111], v57 offset:10240\n\t" \
  "v_mfma_f32_16x16x32_bf16 v[152:155], v[128:131], v[72:75], v[152:155]\n\t" \
  "ds_read_b128 v[144:147], v57 offset:12288\n\t" \
  "v_mfma_f32_16x16x32_bf16 v[152:155], v[132:135], v[148:151], v[152:155]\n\t" \
  "ds_read_b128 v[140:143], v56 offset:12288\n\t" \
  "v_mfma_f32_16x16x32_bf16 v[160:163], v[132:135], v[68:71], v[160:163]\n\t" \
  "ds_read_b128 v[112:115], v56 offset:14336\n\t" \
  "v_mfma_f32_16x16x32_bf16 v[160:163], v[128:131], v[104:107], v[160:163]\n\t" \
  "ds_read_b64_tr_b16 v[92:93], v59 offset:26624\n\t" \
  "v_mfma_f32_16x16x32_bf16 v[168:171], v[128:131], v[80:83], v[168:171]\n\t" \
  "ds_read_b64_tr_b16 v[94:95], v59 offset:27136\n\t" \
  "v_mfma_f32_16x16x32_bf16 v[168:171], v[132:135], v[116:119], v[168:171]\n\t" \
  "ds_read_b64_tr_b16 v[84:85], v59 offset:30720\n\t" \
  "v_mfma_f32_16x16x32_bf16 v[176:179], v[132:135], v[88:91], v[176:179]\n\t" \
  "ds_read_b128 v[132:135], v57 offset:14336\n\t" \
  "v_mfma_f32_16x16x32_bf16 v[176:179], v[128:131], v[124:127], v[176:179]\n\t" \
  "ds_read_b128 v[128:131], v57 offset:16384\n\t" \
  "v_mfma_f32_16x16x32_bf16 v[156:159], v[76:79], v[72:75], v[156:159]\n\t" \
  "ds_read_b64_tr_b16 v[86:87], v59 offset:31232\n\t" \
  "v_mfma_f32_16x16x32_bf16 v[156:159], v[120:123], v[148:151], v[156:159]\n\t" \
  "ds_read_b128 v[148:151], v56 offset:16384\n\t" \
  "v_mfma_f32_16x16x32_bf16 v[164:167], v[120:123], v[68:71], v[164:167]\n\t" \
  "ds_read_b64_tr_b16 v[100:101], v58 offset:34816\n\t" \
  "v_mfma_f32_16x16x32_bf16 v[164:167], v[76:79], v[104:107], v[164:167]\n\t" \
  "ds_read_b64_tr_b16 v[102:103], v58 offset:35328\n\t" \
  "v_mfma_f32_16x16x32_bf16 v[172:175], v[76:79], v[80:83], v[172:175]\n\t" \
  "ds_read_b64_tr_b16 v[72:73], v58 offset:38912\n\t" \
  "v_mfma_f32_16x16x32_bf16 v[172:175], v[120:123], v[116:119], v[172:175]\n\t" \
  "ds_read_b64_tr_b16 v[74:75], v58 offset:39424\n\t" \
  "v_mfma_f32_16x16x32_bf16 v[180:183], v[120:123], v[88:91], v[180:183]\n\t" \
  "ds_read_b128 v[68:71], v56 offset:0\n\t" \
  "v_mfma_f32_16x16x32_bf16 v[180:183], v[76:79], v[124:127], v[180:183]\n\t" \
  "ds_read_b128 v[104:107], v57 offset:0\n\t" \
  "s_waitcnt lgkmcnt(7)\n\t" \
  "v_mfma_f32_16x16x32_bf16 v[184:187], v[96:99], v[136:139], v[184:187]\n\t" \
  "ds_read_b64_tr_b16 v[80:81], v59 offset:34816\n\t" \
  "v_mfma_f32_16x16x32_bf16 v[184:187], v[64:67], v[108:111], v[184:187]\n\t" \
  "ds_read_b64_tr_b16 v[82:83], v59 offset:35328\n\t" \
  "v_mfma_f32_16x16x32_bf16 v[192:195], v[64:67], v[144:147], v[192:195]\n\t" \
  "ds_read_b64_tr_b16 v[88:89], v59 offset:38912\n\t" \
  "v_mfma_f32_16x16x32_bf16 v[192:195], v[96:99], v[140:143], v[192:195]\n\t" \
  "ds_read_b64_tr_b16 v[90:91], v59 offset:39424\n\t" \
  "v_mfma_f32_16x16x32_bf16 v[200:203], v[96:99], v[112:115], v[200:203]\n\t" \
  "ds_read_b64_tr_b16 v[116:117], v58 offset:18432\n\t" \
  "v_mfma_f32_16x16x32_bf16 v[200:203], v[64:67], v[132:135], v[200:203]\n\t" \
  "ds_read_b64_tr_b16 v[118:119], v58 offset:18944\n\t" \
  "v_mfma_f32_16x16x32_bf16 v[208:211], v[64:67], v[128:131], v[208:211]\n\t" \
  "ds_read_b64_tr_b16 v[120:121], v58 offset:22528\n\t" \
  "s_waitcnt lgkmcnt(7)\n\t" \
  "v_mfma_f32_16x16x32_bf16 v[208:211], v[96:99], v[148:151], v[208:211]\n\t" \
  "ds_read_b64_tr_b16 v[122:123], v58 offset:23040\n\t" \
  "v_mfma_f32_16x16x32_bf16 v[188:191], v[92:95], v[136:139], v[188:191]\n\t" \
  "ds_read_b64_tr_b16 v[124:125], v59 offset:18432\n\t" \
  "v_mfma_f32_16x16x32_bf16 v[188:191], v[84:87], v[108:111], v[188:191]\n\t" \
  "ds_read_b64_tr_b16 v[126:127], v59 offset:18944\n\t" \
  "v_mfma_f32_16x16x32_bf16 v[196:199], v[84:87], v[144:147], v[196:199]\n\t" \
  "ds_read_b64_tr_b16 v[76:77], v59 offset:22528\n\t" \
  "v_mfma_f32_16x16x32_bf16 v[196:199], v[92:95], v[140:143], v[196:199]\n\t" \
  "ds_read_b64_tr_b16 v[78:79], v59 offset:23040\n\t" \
  "v_mfma_f32_16x16x32_bf16 v[204:207], v[92:95], v[112:115], v[204:207]\n\t" \
  "v_mfma_f32_16x16x32_bf16 v[204:207], v[84:87], v[132:135], v[204:207]\n\t" \
  "v_mfma_f32_16x16x32_bf16 v[212:215], v[84:87], v[128:131], v[212:215]\n\t" \
  "v_mfma_f32_16x16x32_bf16 v[212:215], v[92:95], v[148:151], v[212:215]\n\t" \
  "v_mfma_f32_16x16x32_bf16 v[216:219], v[136:139], v[100:103], v[216:219]\n\t" \
  "v_mfma_f32_16x16x32_bf16 v[216:219], v[108:111], v[72:75], v[216:219]\n\t" \
  "v_mfma_f32_16x16x32_bf16 v[224:227], v[144:147], v[72:75], v[224:227]\n\t" \
  "v_and_b32 v14, -16, v32\n\t" \
  "v_mfma_f32_16x16x32_bf16 v[224:227], v[140:143], v[100:103], v[224:227]\n\t" \
  "v_mfma_f32_16x16x32_bf16 v[232:235], v[112:115], v[100:103], v[232:235]\n\t" \
  "ds_read_b128 v[10:13], v14 offset:128\n\t" \
  "v_mfma_f32_16x16x32_bf16 v[232:235], v[132:135], v[72:75], v[232:235]\n\t" \
  "v_mfma_f32_16x16x32_bf16 v[240:243], v[128:131], v[72:75], v[240:243]\n\t" \
  "v_mfma_f32_16x16x32_bf16 v[240:243], v[148:151], v[100:103], v[240:243]\n\t" \
  "v_mfma_f32_16x16x32_bf16 v[248:251], v[68:71], v[100:103], v[248:251]\n\t" \
  "v_mfma_f32_16x16x32_bf16 v[248:251], v[104:107], v[72:75], v[248:251]\n\t" \
  "s_waitcnt lgkmcnt(1)\n\t" \
  "v_mfma_f32_16x16x32_bf16 v[252:255], v[68:71], v[80:83], v[252:255]\n\t" \
  "s_waitcnt lgkmcnt(0)\n\t" \
  "v_and_b32 v10, v10, v11\n\t" \
  "v_and_b32 v12, v12, v13\n\t" \
  "v_mfma_f32_16x16x32_bf16 v[252:255], v[104:107], v[88:91], v[252:255]\n\t" \
  "v_and_b32 v10, v10, v12\n\t" \
  "v_mfma_f32_16x16x32_bf16 v[220:223], v[108:111], v[88:91], v[220:223]\n\t" \
  "v_readfirstlane_b32 vcc_lo, v10\n\t" \
  "v_mfma_f32_16x16x32_bf16 v[220:223], v[136:139], v[80:83], v[220:223]\n\t" \
  "ds_write_b32 v32, v0 offset:144\n\t" \
  "s_cmp_eq_u32 vcc_lo, 0\n\t" \
  "s_cbranch_scc1 15f\n\t" \
  "14:\n\t" \
  "v_mfma_f32_16x16x32_bf16 v[228:231], v[140:143], v[80:83], v[228:231]\n\t" \
  "ds_read_b64_tr_b16 v[64:65], v62 offset:26624\n\t" \
  "v_mfma_f32_16x16x32_bf16 v[228:231], v[144:147], v[88:91], v[228:231]\n\t" \
  "ds_read_b64_tr_b16 v[66:67], v62 offset:27136\n\t" \
  "v_mfma_f32_16x16x32_bf16 v[236:239], v[132:135], v[88:91], v[236:239]\n\t" \
  "ds_read_b128 v[96:99], v60 offset:10240\n\t" \
  "v_mfma_f32_16x16x32_bf16 v[236:239], v[112:115], v[80:83], v[236:239]\n\t" \
  "ds_read_b128 v[84:87], v60 offset:12288\n\t" \
  "v_mfma_f32_16x16x32_bf16 v[244:247], v[148:151], v[80:83], v[244:247]\n\t" \
  "ds_read_b128 v[92:95], v60 offset:14336\n\t" \
  "v_mfma_f32_16x16x32_bf16 v[244:247], v[128:131], v[88:91], v[244:247]\n\t" \
  "ds_read_b128 v[100:103], v60 offset:16384\n\t" \
  "v_mfma_f32_16x16x32_bf16 v[152:155], v[116:119], v[136:139], v[152:155]\n\t" \
  "ds_read_b64_tr_b16 v[72:73], v63 offset:26624\n\t" \
  "v_mfma_f32_16x16x32_bf16 v[152:155], v[120:123], v[108:111], v[152:155]\n\t" \
  "ds_read_b64_tr_b16 v[74:75], v63 offset:27136\n\t" \
  "v_mfma_f32_16x16x32_bf16 v[160:163], v[120:123], v[144:147], v[160:163]\n\t" \
  "ds_read_b64_tr_b16 v[68:69], v62 offset:34816\n\t" \
  "v_mfma_f32_16x16x32_bf16 v[160:163], v[116:119], v[140:143], v[160:163]\n\t" \
  "ds_read_b64_tr_b16 v[70:71], v62 offset:35328\n\t" \
  "v_mfma_f32_16x16x32_bf16 v[168:171], v[116:119], v[112:115], v[168:171]\n\t" \
  "ds_read_b128 v[104:107], v60 offset:0\n\t" \
  "v_mfma_f32_16x16x32_bf16 v[168:171], v[120:123], v[132:135], v[168:171]\n\t" \
  "ds_read_b64_tr_b16 v[80:81], v63 offset:34816\n\t" \
  "v_mfma_f32_16x16x32_bf16 v[176:179], v[120:123], v[128:131], v[176:179]\n\t" \
  "ds_read_b64_tr_b16 v[82:83], v63 offset:35328\n\t" \
  "v_mfma_f32_16x16x32_bf16 v[176:179], v[116:119], v[148:151], v[176:179]\n\t" \
  "ds_read_b64_tr_b16 v[88:89], v62 offset:18432\n\t" \
  "v_mfma_f32_16x16x32_bf16 v[156:159], v[124:127], v[136:139], v[156:159]\n\t" \
  "ds_read_b64_tr_b16 v[90:91], v62 offset:18944\n\t" \
  "v_mfma_f32_16x16x32_bf16 v[156:159], v[76:79], v[108:111], v[156:159]\n\t" \
  "ds_read_b64_tr_b16 v[120:121], v62 offset:30720\n\t" \
  "v_mfma_f32_16x16x32_bf16 v[164:167], v[76:79], v[144:147], v[164:167]\n\t" \
  "ds_read_b64_tr_b16 v[122:123], v62 offset:31232\n\t" \
  "v_mfma_f32_16x16x32_bf16 v[164:167], v[124:127], v[140:143], v[164:167]\n\t" \
  "ds_read_b128 v[116:119], v61 offset:10240\n\t" \
  "v_mfma_f32_16x16x32_bf16 v[172:175], v[124:127], v[112:115], v[172:175]\n\t" \
  "ds_read_b128 v[136:139], v61 offset:12288\n\t" \
  "v_mfma_f32_16x16x32_bf16 v[172:175], v[76:79], v[132:135], v[172:175]\n\t" \
  "ds_read_b128 v[108:111], v61 offset:14336\n\t" \
  "v_mfma_f32_16x16x32_bf16 v[180:183], v[76:79], v[128:131], v[180:183]\n\t" \
  "ds_read_b64_tr_b16 v[76:77], v63 offset:18432\n\t" \
  "v_mfma_f32_16x16x32_bf16 v[180:183], v[124:127], v[148:151], v[180:183]\n\t" \
  "ds_read_b64_tr_b16 v[78:79], v63 offset:18944\n\t" \
  "s_waitcnt lgkmcnt(7)\n\t" \
  "v_mfma_f32_16x16x32_bf16 v[184:187], v[64:67], v[96:99], v[184:187]\n\t" \
  "ds_read_b128 v[144:147], v61 offset:16384\n\t" \
  "v_mfma_f32_16x16x32_bf16 v[192:195], v[64:67], v[84:87], v[192:195]\n\t" \
  "ds_read_b64_tr_b16 v[140:141], v63 offset:30720\n\t" \
  "v_mfma_f32_16x16x32_bf16 v[200:203], v[64:67], v[92:95], v[200:203]\n\t" \
  "ds_read_b64_tr_b16 v[142:143], v63 offset:31232\n\t" \
  "v_mfma_f32_16x16x32_bf16 v[208:211], v[64:67], v[100:103], v[208:211]\n\t" \
  "502:\n\t" \
  "s_cbranch_execz 506f\n\t" \
  "ds_read_b64_tr_b16 v[112:113], v62 offset:22528\n\t" \
  "v_mfma_f32_16x16x32_bf16 v[188:191], v[72:75], v[96:99], v[188:191]\n\t" \
  "ds_read_b64_tr_b16 v[114:115], v62 offset:23040\n\t" \
  "v_mfma_f32_16x16x32_bf16 v[196:199], v[72:75], v[84:87], v[196:199]\n\t" \
  "ds_read_b64_tr_b16 v[132:133], v63 offset:22528\n\t" \
  "v_mfma_f32_16x16x32_bf16 v[204:207], v[72:75], v[92:95], v[204:207]\n\t" \
  "ds_read_b64_tr_b16 v[134:135], v63 offset:23040\n\t" \
  "v_mfma_f32_16x16x32_bf16 v[212:215], v[72:75], v[100:103], v[212:215]\n\t" \
  "ds_read_b64_tr_b16 v[128:129], v62 offset:38912\n\t" \
  "v_mfma_f32_16x16x32_bf16 v[248:251], v[104:107], v[68:71], v[248:251]\n\t" \
  "ds_read_b64_tr_b16 v[130:131], v62 offset:39424\n\t" \
  "v_mfma_f32_16x16x32_bf16 v[216:219], v[96:99], v[68:71], v[216:219]\n\t" \
  "ds_read_b128 v[124:127], v61 offset:0\n\t" \
  "v_mfma_f32_16x16x32_bf16 v[224:227], v[84:87], v[68:71], v[224:227]\n\t" \
  "ds_read_b64_tr_b16 v[148:149], v63 offset:38912\n\t" \
  "v_mfma_f32_16x16x32_bf16 v[232:235], v[92:95], v[68:71], v[232:235]\n\t" \
  "ds_read_b64_tr_b16 v[150:151], v63 offset:39424\n\t" \
  "v_mfma_f32_16x16x32_bf16 v[240:243], v[100:103], v[68:71], v[240:243]\n\t" \
  "v_mfma_f32_16x16x32_bf16 v[252:255], v[104:107], v[80:83], v[252:255]\n\t" \
  "v_mfma_f32_16x16x32_bf16 v[220:223], v[96:99], v[80:83], v[220:223]\n\t" \
  "v_mfma_f32_16x16x32_bf16 v[228:231], v[84:87], v[80:83], v[228:231]\n\t" \
  "v_mfma_f32_16x16x32_bf16 v[236:239], v[92:95], v[80:83], v[236:239]\n\t" \
  "v_mfma_f32_16x16x32_bf16 v[244:247], v[100:103], v[80:83], v[244:247]\n\t" \
  "v_mfma_f32_16x16x32_bf16 v[152:155], v[88:91], v[96:99], v[152:155]\n\t" \
  "v_mfma_f32_16x16x32_bf16 v[160:163], v[88:91], v[84:87], v[160:163]\n\t" \
  "v_mfma_f32_16x16x32_bf16 v[168:171], v[88:91], v[92:95], v[168:171]\n\t" \
  "v_mfma_f32_16x16x32_bf16 v[176:179], v[88:91], v[100:103], v[176:179]\n\t" \
  "s_waitcnt lgkmcnt(0)\n\t" \
  "v_mfma_f32_16x16x32_bf16 v[156:159], v[76:79], v[96:99], v[156:159]\n\t" \
  "v_mfma_f32_16x16x32_bf16 v[164:167], v[76:79], v[84:87], v[164:167]\n\t" \
  "ds_write_b32 v32, v0\n\t" \
  "v_mfma_f32_16x16x32_bf16 v[172:175], v[76:79], v[92:95], v[172:175]\n\t" \
  "v_mfma_f32_16x16x32_bf16 v[180:183], v[76:79], v[100:103], v[180:183]\n\t" \
  "v_mfma_f32_16x16x32_bf16 v[184:187], v[120:123], v[116:119], v[184:187]\n\t" \
  "v_and_b32 v56, -16, v32\n\t" \
  "v_mfma_f32_16x16x32_bf16 v[192:195], v[120:123], v[136:139], v[192:195]\n\t" \
  "v_mfma_f32_16x16x32_bf16 v[200:203], v[120:123], v[108:111], v[200:203]\n\t" \
  "v_mfma_f32_16x16x32_bf16 v[208:211], v[120:123], v[144:147], v[208:211]\n\t" \
  "v_mfma_f32_16x16x32_bf16 v[188:191], v[140:143], v[116:119], v[188:191]\n\t" \
  "v_mfma_f32_16x16x32_bf16 v[196:199], v[140:143], v[136:139], v[196:199]\n\t" \
  "v_mfma_f32_16x16x32_bf16 v[204:207], v[140:143], v[108:111], v[204:207]\n\t" \
  "v_mfma_f32_16x16x32_bf16 v[212:215], v[140:143], v[144:147], v[212:215]\n\t" \
  "v_cvt_pk_bf16_f32 v14, v184, v185\n\t" \
  "v_cvt_pk_bf16_f32 v15, v186, v187\n\t" \
  "v_mfma_f32_16x16x32_bf16 v[152:155], v[112:115], v[116:119], v[152:155]\n\t" \
  "v_cvt_pk_bf16_f32 v18, v192, v193\n\t" \
  "v_cvt_pk_bf16_f32 v19, v194, v195\n\t" \
  "v_mfma_f32_16x16x32_bf16 v[160:163], v[112:115], v[136:139], v[160:163]\n\t" \
  "v_cvt_pk_bf16_f32 v16, v188, v189\n\t" \
  "v_cvt_pk_bf16_f32 v17, v190, v191\n\t" \
  "v_mfma_f32_16x16x32_bf16 v[168:171], v[112:115], v[108:111], v[168:171]\n\t" \
  "v_cvt_pk_bf16_f32 v20, v196, v197\n\t" \
  "v_cvt_pk_bf16_f32 v21, v198, v199\n\t" \
  "v_mfma_f32_16x16x32_bf16 v[176:179], v[112:115], v[144:147], v[176:179]\n\t" \
  "v_cvt_pk_bf16_f32 v22, v200, v201\n\t" \
  "v_cvt_pk_bf16_f32 v23, v202, v203\n\t" \
  "v_mfma_f32_16x16x32_bf16 v[156:159], v[132:135], v[116:119], v[156:159]\n\t" \
  "v_cvt_pk_bf16_f32 v24, v204, v205\n\t" \
  "v_cvt_pk_bf16_f32 v25, v206, v207\n\t" \
  "v_mfma_f32_16x16x32_bf16 v[164:167], v[132:135], v[136:139], v[164:167]\n\t" \
  "v_cvt_pk_bf16_f32 v26, v208, v209\n\t" \
  "v_cvt_pk_bf16_f32 v27, v210, v211\n\t" \
  "v_mfma_f32_16x16x32_bf16 v[172:175], v[132:135], v[108:111], v[172:175]\n\t" \
  "v_cvt_pk_bf16_f32 v28, v212, v213\n\t" \
  "v_cvt_pk_bf16_f32 v29, v214, v215\n\t" \
  "v_mfma_f32_16x16x32_bf16 v[180:183], v[132:135], v[144:147], v[180:183]\n\t" \
  "v_cvt_pk_bf16_f32 v84, v152, v153\n\t" \
  "v_cvt_pk_bf16_f32 v85, v154, v155\n\t" \
  "v_mfma_f32_16x16x32_bf16 v[248:251], v[124:127], v[128:131], v[248:251]\n\t" \
  "v_cvt_pk_bf16_f32 v86, v156, v157\n\t" \
  "v_cvt_pk_bf16_f32 v87, v158, v159\n\t" \
  "v_mfma_f32_16x16x32_bf16 v[216:219], v[116:119], v[128:131], v[216:219]\n\t" \
  "v_cvt_pk_bf16_f32 v88, v160, v161\n\t" \
  "v_cvt_pk_bf16_f32 v89, v162, v163\n\t" \
  "v_mfma_f32_16x16x32_bf16 v[224:227], v[136:139], v[128:131], v[224:227]\n\t" \
  "v_cvt_pk_bf16_f32 v90, v164, v165\n\t" \
  "v_cvt_pk_bf16_f32 v91, v166, v167\n\t" \
  "v_mfma_f32_16x16x32_bf16 v[232:235], v[108:111], v[128:131], v[232:235]\n\t" \
  "v_cvt_pk_bf16_f32 v72, v248, v249\n\t" \
  "v_cvt_pk_bf16_f32 v73, v250, v251\n\t" \
  "v_mfma_f32_16x16x32_bf16 v[240:243], v[144:147], v[128:131], v[240:243]\n\t" \
  "ds_read_b32 v52, v33 offset:48\n\t" \
  "ds_read_b128 v[10:13], v31\n\t" \
  "v_cvt_pk_bf16_f32 v92, v168, v169\n\t" \
  "v_mfma_f32_16x16x32_bf16 v[252:255], v[124:127], v[148:151], v[252:255]\n\t" \
  "v_cvt_pk_bf16_f32 v93, v170, v171\n\t" \
  "v_cvt_pk_bf16_f32 v94, v172, v173\n\t" \
  "v_mfma_f32_16x16x32_bf16 v[220:223], v[116:119], v[148:151], v[220:223]\n\t" \
  "v_cvt_pk_bf16_f32 v95, v174, v175\n\t" \
  "v_cvt_pk_bf16_f32 v96, v176, v177\n\t" \
  "v_mfma_f32_16x16x32_bf16 v[228:231], v[136:139], v[148:151], v[228:231]\n\t" \
  "v_cvt_pk_bf16_f32 v97, v178, v179\n\t" \
  "v_cvt_pk_bf16_f32 v98, v180, v181\n\t" \
  "v_mfma_f32_16x16x32_bf16 v[236:239], v[108:111], v[148:151], v[236:239]\n\t" \
  "v_cvt_pk_bf16_f32 v82, v252, v253\n\t" \
  "v_cvt_pk_bf16_f32 v83, v254, v255\n\t" \
  "v_mfma_f32_16x16x32_bf16 v[244:247], v[144:147], v[148:151], v[244:247]\n\t" \
  "ds_write_b64 v30, v[72:73]\n\t" \
  "ds_write_b64 v30, v[82:83] offset:8\n\t" \
  "ds_write_b32 v32, v0 offset:48\n\t" \
  "v_mfma_f32_16x16x32_bf16 v[200:203], v[22:25], v[92:95], v[34:37]\n\t" \
  "v_cvt_pk_bf16_f32 v99, v182, v183\n\t" \
  "s_waitcnt lgkmcnt(3)\n\t" \
  "v_readfirstlane_b32 vcc_lo, v52\n\t" \
  "s_nop 1\n\t" \
  "s_cmp_eq_u32 vcc_lo, 0\n\t" \
  "s_cbranch_scc1 5f\n\t" \
  "4:\n\t" \
  "v_mfma_f32_16x16x32_bf16 v[204:207], v[18:21], v[92:95], v[38:41]\n\t" \
  "v_cvt_pk_bf16_f32 v66, v232, v233\n\t" \
  "v_cvt_pk_bf16_f32 v67, v234, v235\n\t" \
  "v_mfma_f32_16x16x32_bf16 v[208:211], v[26:29], v[96:99], v[34:37]\n\t" \
  "v_cvt_pk_bf16_f32 v76, v236, v237\n\t" \
  "v_cvt_pk_bf16_f32 v77, v238, v239\n\t" \
  "v_mfma_f32_16x16x32_bf16 v[212:215], v[22:25], v[96:99], v[38:41]\n\t" \
  "v_cvt_pk_bf16_f32 v64, v240, v241\n\t" \
  "v_cvt_pk_bf16_f32 v65, v242, v243\n\t" \
  "v_mfma_f32_16x16x32_bf16 v[192:195], v[18:21], v[88:91], v[34:37]\n\t" \
  "v_max_f32 v200, v200, v204\n\t" \
  "v_max_f32 v201, v201, v205\n\t" \
  "v_mfma_f32_16x16x32_bf16 v[196:199], v[14:17], v[88:91], v[38:41]\n\t" \
  "v_max_f32 v202, v202, v206\n\t" \
  "v_max_f32 v203, v203, v207\n\t" \
  "v_mfma_f32_16x16x32_bf16 v[184:187], v[14:17], v[84:87], v[34:37]\n\t" \
  "v_max_f32 v208, v208, v212\n\t" \
  "v_max_f32 v209, v209, v213\n\t" \
  "v_mfma_f32_16x16x32_bf16 v[188:191], v[10:13], v[84:87], v[42:45]\n\t" \
  "v_max_f32 v210, v210, v214\n\t" \
  "v_max_f32 v211, v211, v215\n\t" \
  "v_max_f32 v192, v192, v196\n\t" \
  "v_max_f32 v193, v193, v197\n\t" \
  "v_max_f32 v194, v194, v198\n\t" \
  "v_max3_f32 v106, v208, v209, v210\n\t" \
  "v_max3_f32 v104, v200, v201, v202\n\t" \
  "v_max_f32 v195, v195, v199\n\t" \
  "v_max_f32 v184, v184, v188\n\t" \
  "v_max_f32 v185, v185, v189\n\t" \
  "v_max_f32 v186, v186, v190\n\t" \
  "v_max3_f32 v102, v192, v193, v194\n\t" \
  "v_max_f32 v187, v187, v191\n\t" \
  "v_max3_f32 v100, v184, v185, v186\n\t" \
  "v_max_f32 v106, v106, v211\n\t" \
  "v_max_f32 v104, v104, v203\n\t" \
  "v_max_f32 v102, v102, v195\n\t" \
  "v_max_f32 v100, v100, v187\n\t" \
  "v_permlane16_swap_b32 v104, v106\n\t" \
  "v_cvt_pk_bf16_f32 v74, v244, v245\n\t" \
  "v_permlane16_swap_b32 v100, v102\n\t" \
  "v_max_f32 v104, v104, v106\n\t" \
  "v_max_f32 v100, v100, v102\n\t" \
  "v_cvt_pk_bf16_f32 v75, v246, v247\n\t" \
  "v_cvt_pk_bf16_f32 v68, v224, v225\n\t" \
  "v_permlane32_swap_b32 v100, v104\n\t" \
  "v_cvt_pk_bf16_f32 v69, v226, v227\n\t" \
  "v_max_f32 v100, v100, v104\n\t" \
  "v_mul_f32 v100, 0xbe8293ee, v100\n\t" \
  "v_mov_b32 v104, v100\n\t" \
  "v_cvt_pk_bf16_f32 v78, v228, v229\n\t" \
  "v_cvt_pk_bf16_f32 v79, v230, v231\n\t" \
  "v_permlane32_swap_b32 v100, v104\n\t" \
  "v_cvt_pk_bf16_f32 v70, v216, v217\n\t" \
  "v_mov_b32 v106, v104\n\t" \
  "v_mov_b32 v102, v100\n\t" \
  "v_cvt_pk_bf16_f32 v71, v218, v219\n\t" \
  "v_permlane16_swap_b32 v104, v106\n\t" \
  "v_permlane16_swap_b32 v100, v102\n\t" \
  "v_fma_f32 v208, v208, %16, v106\n\t" \
  "v_fma_f32 v209, v209, %16, v106\n\t" \
  "v_fma_f32 v210, v210, %16, v106\n\t" \
  "v_fma_f32 v211, v211, %16, v106\n\t" \
  "v_exp_f32 v208, v208\n\t" \
  "v_exp_f32 v209, v209\n\t" \
  "v_exp_f32 v210, v210\n\t" \
  "v_exp_f32 v211, v211\n\t" \
  "v_cvt_pk_bf16_f32 v212, v208, v209\n\t" \
  "v_cvt_pk_bf16_f32 v213, v210, v211\n\t" \
  "v_bfi_b32 v214, v46, 0, v212\n\t" \
  "v_bfi_b32 v215, v47, 0, v213\n\t" \
  "v_and_b32 v212, v46, v212\n\t" \
  "v_and_b32 v213, v47, v213\n\t" \
  "v_fma_f32 v200, v200, %16, v104\n\t" \
  "v_fma_f32 v201, v201, %16, v104\n\t" \
  "v_mfma_f32_16x16x32_bf16 v[208:211], v[48:51], v[212:215], 0\n\t" \
  "v_fma_f32 v202, v202, %16, v104\n\t" \
  "v_fma_f32 v203, v203, %16, v104\n\t" \
  "v_mfma_f32_16x16x32_bf16 v[176:179], v[64:67], v[212:215], 0\n\t" \
  "v_exp_f32 v200, v200\n\t" \
  "v_mfma_f32_16x16x32_bf16 v[180:183], v[74:77], v[212:215], 0\n\t" \
  "v_exp_f32 v201, v201\n\t" \
  "v_exp_f32 v202, v202\n\t" \
  "v_exp_f32 v203, v203\n\t" \
  "v_cvt_pk_bf16_f32 v204, v200, v201\n\t" \
  "v_cvt_pk_bf16_f32 v205, v202, v203\n\t" \
  "v_bfi_b32 v206, v46, 0, v204\n\t" \
  "v_bfi_b32 v207, v47, 0, v205\n\t" \
  "v_rcp_f32 v106, v208\n\t" \
  "v_and_b32 v204, v46, v204\n\t" \
  "v_and_b32 v205, v47, v205\n\t" \
  "v_pk_mul_f32 v[176:177], v[176:177], v[106:107] op_sel_hi:[1,0]\n\t" \
  "v_pk_mul_f32 v[178:179], v[178:179], v[106:107] op_sel_hi:[1,0]\n\t" \
  "v_mfma_f32_16x16x32_bf16 v[200:203], v[48:51], v[204:207], 0\n\t" \
  "v_pk_mul_f32 v[180:181], v[180:181], v[106:107] op_sel_hi:[1,0]\n\t" \
  "v_pk_mul_f32 v[182:183], v[182:183], v[106:107] op_sel_hi:[1,0]\n\t" \
  "v_mfma_f32_16x16x32_bf16 v[168:171], v[66:69], v[204:207], 0\n\t" \
  "v_cvt_pk_bf16_f32 v240, v176, v177\n\t" \
  "v_cvt_pk_bf16_f32 v241, v178, v179\n\t" \
  "v_mfma_f32_16x16x32_bf16 v[172:175], v[76:79], v[204:207], 0\n\t" \
  "v_cvt_pk_bf16_f32 v242, v180, v181\n\t" \
  "v_cvt_pk_bf16_f32 v243, v182, v183\n\t" \
  "global_store_dwordx4 v1, v[240:243], %14 offset:0 sc1\n\t" \
  "v_fma_f32 v192, v192, %16, v102\n\t" \
  "v_fma_f32 v193, v193, %16, v102\n\t" \
  "v_fma_f32 v194, v194, %16, v102\n\t" \
  "v_fma_f32 v195, v195, %16, v102\n\t" \
  "v_exp_f32 v192, v192\n\t" \
  "v_exp_f32 v193, v193\n\t" \
  "v_exp_f32 v194, v194\n\t" \
  "v_exp_f32 v195, v195\n\t" \
  "v_cvt_pk_bf16_f32 v196, v192, v193\n\t" \
  "v_cvt_pk_bf16_f32 v197, v194, v195\n\t" \
  "v_bfi_b32 v198, v46, 0, v196\n\t" \
  "v_bfi_b32 v199, v47, 0, v197\n\t" \
  "v_rcp_f32 v104, v200\n\t" \
  "v_and_b32 v196, v46, v196\n\t" \
  "v_and_b32 v197, v47, v197\n\t" \
  "v_cvt_pk_bf16_f32 v80, v220, v221\n\t" \
  "v_cvt_pk_bf16_f32 v81, v222, v223\n\t" \
  "v_mfma_f32_16x16x32_bf16 v[192:195], v[48:51], v[196:199], 0\n\t" \
  "v_pk_mul_f32 v[168:169], v[168:169], v[104:105] op_sel_hi:[1,0]\n\t" \
  "v_pk_mul_f32 v[170:171], v[170:171], v[104:105] op_sel_hi:[1,0]\n\t" \
  "v_mfma_f32_16x16x32_bf16 v[160:163], v[68:71], v[196:199], 0\n\t" \
  "v_pk_mul_f32 v[172:173], v[172:173], v[104:105] op_sel_hi:[1,0]\n\t" \
  "v_pk_mul_f32 v[174:175], v[174:175], v[104:105] op_sel_hi:[1,0]\n\t" \
  "v_mfma_f32_16x16x32_bf16 v[164:167], v[78:81], v[196:199], 0\n\t" \
  "v_cvt_pk_bf16_f32 v232, v168, v169\n\t" \
  "v_cvt_pk_bf16_f32 v233, v170, v171\n\t" \
  "v_cvt_pk_bf16_f32 v234, v172, v173\n\t" \
  "v_cvt_pk_bf16_f32 v235, v174, v175\n\t" \
  "global_store_dwordx4 v1, v[232:235], %14 offset:-4096 sc1\n\t" \
  "v_fma_f32 v184, v184, %16, v100\n\t" \
  "v_fma_f32 v185, v185, %16, v100\n\t" \
  "v_fma_f32 v186, v186, %16, v100\n\t" \
  "v_fma_f32 v187, v187, %16, v100\n\t" \
  "v_exp_f32 v184, v184\n\t" \
  "v_exp_f32 v185, v185\n\t" \
  "v_exp_f32 v186, v186\n\t" \
  "v_exp_f32 v187, v187\n\t" \
  "v_cvt_pk_bf16_f32 v188, v184, v185\n\t" \
  "v_cvt_pk_bf16_f32 v189, v186, v187\n\t" \
  "v_bfi_b32 v190, v46, 0, v188\n\t" \
  "v_bfi_b32 v191, v47, 0, v189\n\t" \
  "v_rcp_f32 v102, v192\n\t" \
  "v_and_b32 v188, v46, v188\n\t" \
  "v_and_b32 v189, v47, v189\n\t" \
  "v_pk_mul_f32 v[160:161], v[160:161], v[102:103] op_sel_hi:[1,0]\n\t" \
  "v_pk_mul_f32 v[162:163], v[162:163], v[102:103] op_sel_hi:[1,0]\n\t" \
  "v_mfma_f32_16x16x32_bf16 v[184:187], v[48:51], v[188:191], 0\n\t" \
  "v_pk_mul_f32 v[164:165], v[164:165], v[102:103] op_sel_hi:[1,0]\n\t" \
  "v_pk_mul_f32 v[166:167], v[166:167], v[102:103] op_sel_hi:[1,0]\n\t" \
  "v_mfma_f32_16x16x32_bf16 v[152:155], v[70:73], v[188:191], 0\n\t" \
  "v_cvt_pk_bf16_f32 v224, v160, v161\n\t" \
  "v_cvt_pk_bf16_f32 v225, v162, v163\n\t" \
  "v_mfma_f32_16x16x32_bf16 v[156:159], v[80:83], v[188:191], 0\n\t" \
  "v_cvt_pk_bf16_f32 v226, v164, v165\n\t" \
  "v_cvt_pk_bf16_f32 v227, v166, v167\n\t" \
  "global_store_dwordx4 v4, v[224:227], %14 offset:0 sc1\n\t" \
  "v_rcp_f32 v100, v184\n\t" \
  "ds_read_b128 v[60:63], v56 offset:64\n\t" \
  "ds_read_b64_tr_b16 v[108:109], v5 offset:0\n\t" \
  "ds_read_b64_tr_b16 v[110:111], v5 offset:16\n\t" \
  "ds_read_b128 v[112:115], v6 offset:16384\n\t" \
  "ds_read_b128 v[116:119], v6 offset:15360\n\t" \
  "ds_read_b64_tr_b16 v[120:121], v5 offset:512\n\t" \
  "ds_read_b64_tr_b16 v[122:123], v5 offset:528\n\t" \
  "ds_read_b128 v[124:127], v6 offset:14336\n\t" \
  "ds_read_b64_tr_b16 v[128:129], v5 offset:1024\n\t" \
  "ds_read_b64_tr_b16 v[130:131], v5 offset:1040\n\t" \
  "ds_read_b128 v[132:135], v6 offset:13312\n\t" \
  "ds_read_b64_tr_b16 v[136:137], v5 offset:1536\n\t" \
  "ds_read_b64_tr_b16 v[138:139], v5 offset:1552\n\t" \
  "ds_read_b128 v[140:143], v6 offset:12288\n\t" \
  "ds_read_b64_tr_b16 v[144:145], v5 offset:2048\n\t" \
  "v_pk_mul_f32 v[152:153], v[152:153], v[100:101] op_sel_hi:[1,0]\n\t" \
  "v_pk_mul_f32 v[154:155], v[154:155], v[100:101] op_sel_hi:[1,0]\n\t" \
  "v_cvt_pk_bf16_f32 v216, v152, v153\n\t" \
  "v_pk_mul_f32 v[156:157], v[156:157], v[100:101] op_sel_hi:[1,0]\n\t" \
  "v_pk_mul_f32 v[158:159], v[158:159], v[100:101] op_sel_hi:[1,0]\n\t" \
  "v_cvt_pk_bf16_f32 v217, v154, v155\n\t" \
  "v_cvt_pk_bf16_f32 v218, v156, v157\n\t" \
  "v_cvt_pk_bf16_f32 v219, v158, v159\n\t" \
  "global_store_dwordx4 v4, v[216:219], %14 offset:-4096 sc1\n\t" \
  "ds_write_b32 v32, v0 offset:112\n\t" \
  "s_branch 9f\n\t" \
  "5:\n\t" \
  "ds_read_b32 v52, v33 offset:48\n\t" \
  "ds_read_b128 v[10:13], v31\n\t" \
  "s_waitcnt lgkmcnt(0)\n\t" \
  "v_readfirstlane_b32 vcc_lo, v52\n\t" \
  "s_nop 1\n\t" \
  "s_cmp_eq_u32 vcc_lo, 0\n\t" \
  "s_cbranch_scc1 5b\n\t" \
  "s_branch 4b\n\t" \
  "15:\n\t" \
  "ds_read_b128 v[10:13], v14 offset:128\n\t" \
  "s_waitcnt lgkmcnt(0)\n\t" \
  "v_and_b32 v10, v10, v11\n\t" \
  "v_and_b32 v12, v12, v13\n\t" \
  "v_and_b32 v10, v10, v12\n\t" \
  "s_nop 1\n\t" \
  "v_readfirstlane_b32 vcc_lo, v10\n\t" \
  "s_nop 1\n\t" \
  "s_cmp_eq_u32 vcc_lo, 0\n\t" \
  "s_cbranch_scc1 15b\n\t" \
  "s_branch 14b\n\t" \
  "8:\n\t" \
  "global_load_dwordx2 v[10:11], v51, %11\n\t" \
  "global_load_dwordx2 v[12:13], v51, %11 offset:128\n\t" \
  "v_xor_b32 v53, 64, v52\n\t" \
  "v_xor_b32 v55, 32, v54\n\t" \
  "v_add_u32 v56, 43008, v52\n\t" \
  "v_add_u32 v58, 43008, v54\n\t" \
  "v_xor_b32 v57, 64, v56\n\t" \
  "v_xor_b32 v59, 32, v58\n\t" \
  "v_add_u32 v60, 86016, v52\n\t" \
  "v_add_u32 v62, 86016, v54\n\t" \
  "v_xor_b32 v61, 64, v60\n\t" \
  "v_xor_b32 v63, 32, v62\n\t" \
  "s_mov_b32 vcc_hi, 0x05040100\n\t" \
  "s_waitcnt vmcnt(2)\n\t" \
  "s_barrier\n\t" \
  "ds_read_b64_tr_b16 v[64:65], v62 offset:34816\n\t" \
  "ds_read_b64_tr_b16 v[66:67], v62 offset:35328\n\t" \
  "ds_read_b128 v[68:71], v60 offset:2048\n\t" \
  "ds_read_b64_tr_b16 v[72:73], v62 offset:38912\n\t" \
  "ds_read_b64_tr_b16 v[74:75], v62 offset:39424\n\t" \
  "ds_read_b128 v[76:79], v61 offset:2048\n\t" \
  "ds_read_b128 v[80:83], v61 offset:4096\n\t" \
  "ds_read_b128 v[84:87], v60 offset:4096\n\t" \
  "ds_read_b128 v[88:91], v60 offset:6144\n\t" \
  "ds_read_b128 v[92:95], v61 offset:6144\n\t" \
  "ds_read_b128 v[96:99], v61 offset:8192\n\t" \
  "ds_read_b128 v[100:103], v60 offset:8192\n\t" \
  "ds_read_b64_tr_b16 v[104:105], v63 offset:34816\n\t" \
  "ds_read_b64_tr_b16 v[106:107], v63 offset:35328\n\t" \
  "ds_read_b64_tr_b16 v[108:109], v63 offset:38912\n\t" \
  "ds_read_b64_tr_b16 v[110:111], v63 offset:39424\n\t" \
  "s_waitcnt lgkmcnt(13)\n\t" \
  "v_mfma_f32_16x16x32_bf16 v[216:219], v[68:71], v[64:67], 0\n\t" \
  "ds_read_b64_tr_b16 v[112:113], v62 offset:26624\n\t" \
  "ds_read_b64_tr_b16 v[114:115], v62 offset:27136\n\t" \
  "s_waitcnt lgkmcnt(12)\n\t" \
  "v_mfma_f32_16x16x32_bf16 v[216:219], v[76:79], v[72:75], v[216:219]\n\t" \
  "ds_read_b64_tr_b16 v[116:117], v62 offset:30720\n\t" \
  "ds_read_b64_tr_b16 v[118:119], v62 offset:31232\n\t" \
  "s_waitcnt lgkmcnt(13)\n\t" \
  "v_mfma_f32_16x16x32_bf16 v[224:227], v[80:83], v[72:75], 0\n\t" \
  "ds_read_b128 v[120:123], v60 offset:0\n\t" \
  "s_waitcnt lgkmcnt(13)\n\t" \
  "v_mfma_f32_16x16x32_bf16 v[224:227], v[84:87], v[64:67], v[224:227]\n\t" \
  "ds_read_b128 v[124:127], v61 offset:0\n\t" \
  "s_waitcnt lgkmcnt(13)\n\t" \
  "v_mfma_f32_16x16x32_bf16 v[232:235], v[88:91], v[64:67], 0\n\t" \
  "ds_read_b64_tr_b16 v[128:129], v63 offset:26624\n\t" \
  "s_waitcnt lgkmcnt(13)\n\t" \
  "v_mfma_f32_16x16x32_bf16 v[232:235], v[92:95], v[72:75], v[232:235]\n\t" \
  "ds_read_b64_tr_b16 v[130:131], v63 offset:27136\n\t" \
  "s_waitcnt lgkmcnt(13)\n\t" \
  "v_mfma_f32_16x16x32_bf16 v[240:243], v[96:99], v[72:75], 0\n\t" \
  "ds_read_b64_tr_b16 v[132:133], v63 offset:30720\n\t" \
  "s_waitcnt lgkmcnt(9)\n\t" \
  "v_mfma_f32_16x16x32_bf16 v[240:243], v[100:103], v[64:67], v[240:243]\n\t" \
  "ds_read_b64_tr_b16 v[134:135], v63 offset:31232\n\t" \
  "v_mfma_f32_16x16x32_bf16 v[220:223], v[68:71], v[104:107], 0\n\t" \
  "ds_read_b64_tr_b16 v[136:137], v62 offset:18432\n\t" \
  "v_mfma_f32_16x16x32_bf16 v[220:223], v[76:79], v[108:111], v[220:223]\n\t" \
  "ds_read_b64_tr_b16 v[138:139], v62 offset:18944\n\t" \
  "v_mfma_f32_16x16x32_bf16 v[228:231], v[80:83], v[108:111], 0\n\t" \
  "ds_read_b64_tr_b16 v[140:141], v62 offset:22528\n\t" \
  "v_mfma_f32_16x16x32_bf16 v[228:231], v[84:87], v[104:107], v[228:231]\n\t" \
  "ds_read_b64_tr_b16 v[142:143], v62 offset:23040\n\t" \
  "v_mfma_f32_16x16x32_bf16 v[236:239], v[88:91], v[104:107], 0\n\t" \
  "ds_read_b64_tr_b16 v[144:145], v63 offset:18432\n\t" \
  "v_mfma_f32_16x16x32_bf16 v[236:239], v[92:95], v[108:111], v[236:239]\n\t" \
  "ds_read_b64_tr_b16 v[146:147], v63 offset:18944\n\t" \
  "v_mfma_f32_16x16x32_bf16 v[244:247], v[96:99], v[108:111], 0\n\t" \
  "ds_read_b64_tr_b16 v[148:149], v63 offset:22528\n\t" \
  "v_mfma_f32_16x16x32_bf16 v[244:247], v[100:103], v[104:107], v[244:247]\n\t" \
  "ds_read_b64_tr_b16 v[150:151], v63 offset:23040\n\t" \
  "s_waitcnt lgkmcnt(7)\n\t" \
  "v_mfma_f32_16x16x32_bf16 v[184:187], v[112:115], v[68:71], 0\n\t" \
  "v_mfma_f32_16x16x32_bf16 v[184:187], v[116:119], v[76:79], v[184:187]\n\t" \
  "v_mfma_f32_16x16x32_bf16 v[192:195], v[116:119], v[80:83], 0\n\t" \
  "v_mfma_f32_16x16x32_bf16 v[192:195], v[112:115], v[84:87], v[192:195]\n\t" \
  "v_mfma_f32_16x16x32_bf16 v[200:203], v[112:115], v[88:91], 0\n\t" \
  "v_mfma_f32_16x16x32_bf16 v[200:203], v[116:119], v[92:95], v[200:203]\n\t" \
  "v_mfma_f32_16x16x32_bf16 v[208:211], v[116:119], v[96:99], 0\n\t" \
  "v_mfma_f32_16x16x32_bf16 v[208:211], v[112:115], v[100:103], v[208:211]\n\t" \
  "v_mfma_f32_16x16x32_bf16 v[248:251], v[112:115], v[120:123], 0\n\t" \
  "v_mfma_f32_16x16x32_bf16 v[248:251], v[116:119], v[124:127], v[248:251]\n\t" \
  "v_mfma_f32_16x16x32_bf16 v[252:255], v[128:131], v[120:123], 0\n\t" \
  "v_mfma_f32_16x16x32_bf16 v[252:255], v[132:135], v[124:127], v[252:255]\n\t" \
  "v_mfma_f32_16x16x32_bf16 v[188:191], v[132:135], v[76:79], 0\n\t" \
  "v_mfma_f32_16x16x32_bf16 v[188:191], v[128:131], v[68:71], v[188:191]\n\t" \
  "s_waitcnt lgkmcnt(0)\n\t" \
  "s_getpc_b64 s[88:89]\n\t" \
  "s_branch 28b\n\t" \
  "s_barrier\n\t" \
  "v_mfma_f32_16x16x32_bf16 v[196:199], v[128:131], v[84:87], 0\n\t" \
  "ds_read_b64_tr_b16 v[72:73], v54 offset:34816\n\t" \
  "v_mfma_f32_16x16x32_bf16 v[196:199], v[132:135], v[80:83], v[196:199]\n\t" \
  "ds_read_b64_tr_b16 v[74:75], v54 offset:35328\n\t" \
  "v_mfma_f32_16x16x32_bf16 v[204:207], v[132:135], v[92:95], 0\n\t" \
  "ds_read_b128 v[64:67], v52 offset:2048\n\t" \
  "v_mfma_f32_16x16x32_bf16 v[204:207], v[128:131], v[88:91], v[204:207]\n\t" \
  "ds_read_b64_tr_b16 v[108:109], v54 offset:38912\n\t" \
  "v_mfma_f32_16x16x32_bf16 v[212:215], v[128:131], v[100:103], 0\n\t" \
  "ds_read_b64_tr_b16 v[110:111], v54 offset:39424\n\t" \
  "v_mfma_f32_16x16x32_bf16 v[212:215], v[132:135], v[96:99], v[212:215]\n\t" \
  "ds_read_b128 v[104:107], v53 offset:2048\n\t" \
  "v_mfma_f32_16x16x32_bf16 v[152:155], v[136:139], v[68:71], 0\n\t" \
  "ds_read_b128 v[112:115], v53 offset:4096\n\t" \
  "v_mfma_f32_16x16x32_bf16 v[152:155], v[140:143], v[76:79], v[152:155]\n\t" \
  "ds_read_b128 v[116:119], v52 offset:4096\n\t" \
  "v_mfma_f32_16x16x32_bf16 v[160:163], v[140:143], v[80:83], 0\n\t" \
  "ds_read_b128 v[120:123], v52 offset:6144\n\t" \
  "v_mfma_f32_16x16x32_bf16 v[160:163], v[136:139], v[84:87], v[160:163]\n\t" \
  "ds_read_b128 v[124:127], v53 offset:6144\n\t" \
  "v_mfma_f32_16x16x32_bf16 v[168:171], v[136:139], v[88:91], 0\n\t" \
  "ds_read_b128 v[128:131], v53 offset:8192\n\t" \
  "v_mfma_f32_16x16x32_bf16 v[168:171], v[140:143], v[92:95], v[168:171]\n\t" \
  "ds_read_b128 v[132:135], v52 offset:8192\n\t" \
  "v_mfma_f32_16x16x32_bf16 v[176:179], v[140:143], v[96:99], 0\n\t" \
  "ds_read_b64_tr_b16 v[140:141], v55 offset:34816\n\t" \
  "v_mfma_f32_16x16x32_bf16 v[176:179], v[136:139], v[100:103], v[176:179]\n\t" \
  "ds_read_b64_tr_b16 v[142:143], v55 offset:35328\n\t" \
  "v_mfma_f32_16x16x32_bf16 v[156:159], v[144:147], v[68:71], 0\n\t" \
  "ds_read_b64_tr_b16 v[136:137], v55 offset:38912\n\t" \
  "v_mfma_f32_16x16x32_bf16 v[156:159], v[148:151], v[76:79], v[156:159]\n\t" \
  "ds_read_b64_tr_b16 v[138:139], v55 offset:39424\n\t" \
  "v_mfma_f32_16x16x32_bf16 v[164:167], v[148:151], v[80:83], 0\n\t" \
  "ds_read_b64_tr_b16 v[68:69], v54 offset:26624\n\t" \
  "v_mfma_f32_16x16x32_bf16 v[164:167], v[144:147], v[84:87], v[164:167]\n\t" \
  "ds_read_b64_tr_b16 v[70:71], v54 offset:27136\n\t" \
  "v_mfma_f32_16x16x32_bf16 v[172:175], v[144:147], v[88:91], 0\n\t" \
  "ds_read_b64_tr_b16 v[76:77], v54 offset:30720\n\t" \
  "v_mfma_f32_16x16x32_bf16 v[172:175], v[148:151], v[92:95], v[172:175]\n\t" \
  "ds_read_b64_tr_b16 v[78:79], v54 offset:31232\n\t" \
  "v_mfma_f32_16x16x32_bf16 v[180:183], v[148:151], v[96:99], 0\n\t" \
  "ds_read_b128 v[80:83], v52 offset:0\n\t" \
  "v_mfma_f32_16x16x32_bf16 v[180:183], v[144:147], v[100:103], v[180:183]\n\t" \
  "ds_read_b128 v[84:87], v53 offset:0\n\t" \
  "s_waitcnt lgkmcnt(7)\n\t" \
  "v_mfma_f32_16x16x32_bf16 v[216:219], v[64:67], v[72:75], v[216:219]\n\t" \
  "ds_read_b64_tr_b16 v[88:89], v55 offset:26624\n\t" \
  "v_mfma_f32_16x16x32_bf16 v[216:219], v[104:107], v[108:111], v[216:219]\n\t" \
  "ds_read_b64_tr_b16 v[90:91], v55 offset:27136\n\t" \
  "v_mfma_f32_16x16x32_bf16 v[224:227], v[112:115], v[108:111], v[224:227]\n\t" \
  "ds_read_b64_tr_b16 v[92:93], v55 offset:30720\n\t" \
  "v_mfma_f32_16x16x32_bf16 v[224:227], v[116:119], v[72:75], v[224:227]\n\t" \
  "ds_read_b64_tr_b16 v[94:95], v55 offset:31232\n\t" \
  "v_mfma_f32_16x16x32_bf16 v[232:235], v[120:123], v[72:75], v[232:235]\n\t" \
  "ds_read_b64_tr_b16 v[96:97], v54 offset:18432\n\t" \
  "v_mfma_f32_16x16x32_bf16 v[232:235], v[124:127], v[108:111], v[232:235]\n\t" \
  "ds_read_b64_tr_b16 v[98:99], v54 offset:18944\n\t" \
  "v_mfma_f32_16x16x32_bf16 v[240:243], v[128:131], v[108:111], v[240:243]\n\t" \
  "ds_read_b64_tr_b16 v[148:149], v54 offset:22528\n\t" \
  "v_mfma_f32_16x16x32_bf16 v[240:243], v[132:135], v[72:75], v[240:243]\n\t" \
  "ds_read_b64_tr_b16 v[150:151], v54 offset:23040\n\t" \
  "v_mfma_f32_16x16x32_bf16 v[220:223], v[64:67], v[140:143], v[220:223]\n\t" \
  "ds_read_b64_tr_b16 v[100:101], v55 offset:18432\n\t" \
  "s_waitcnt lgkmcnt(7)\n\t" \
  "v_mfma_f32_16x16x32_bf16 v[220:223], v[104:107], v[136:139], v[220:223]\n\t" \
  "ds_read_b64_tr_b16 v[102:103], v55 offset:18944\n\t" \
  "v_mfma_f32_16x16x32_bf16 v[228:231], v[112:115], v[136:139], v[228:231]\n\t" \
  "ds_read_b64_tr_b16 v[144:145], v55 offset:22528\n\t" \
  "v_mfma_f32_16x16x32_bf16 v[228:231], v[116:119], v[140:143], v[228:231]\n\t" \
  "ds_read_b64_tr_b16 v[146:147], v55 offset:23040\n\t" \
  "v_mfma_f32_16x16x32_bf16 v[236:239], v[120:123], v[140:143], v[236:239]\n\t" \
  "v_mfma_f32_16x16x32_bf16 v[236:239], v[124:127], v[136:139], v[236:239]\n\t" \
  "v_mfma_f32_16x16x32_bf16 v[244:247], v[128:131], v[136:139], v[244:247]\n\t" \
  "v_mfma_f32_16x16x32_bf16 v[244:247], v[132:135], v[140:143], v[244:247]\n\t" \
  "v_mfma_f32_16x16x32_bf16 v[184:187], v[68:71], v[64:67], v[184:187]\n\t" \
  "v_mfma_f32_16x16x32_bf16 v[184:187], v[76:79], v[104:107], v[184:187]\n\t" \
  "v_mfma_f32_16x16x32_bf16 v[192:195], v[76:79], v[112:115], v[192:195]\n\t" \
  "v_mfma_f32_16x16x32_bf16 v[192:195], v[68:71], v[116:119], v[192:195]\n\t" \
  "v_mfma_f32_16x16x32_bf16 v[200:203], v[68:71], v[120:123], v[200:203]\n\t" \
  "v_mfma_f32_16x16x32_bf16 v[200:203], v[76:79], v[124:127], v[200:203]\n\t" \
  "v_mfma_f32_16x16x32_bf16 v[208:211], v[76:79], v[128:131], v[208:211]\n\t" \
  "v_mfma_f32_16x16x32_bf16 v[208:211], v[68:71], v[132:135], v[208:211]\n\t" \
  "v_mfma_f32_16x16x32_bf16 v[248:251], v[68:71], v[80:83], v[248:251]\n\t" \
  "v_mfma_f32_16x16x32_bf16 v[248:251], v[76:79], v[84:87], v[248:251]\n\t" \
  "v_mfma_f32_16x16x32_bf16 v[252:255], v[88:91], v[80:83], v[252:255]\n\t" \
  "s_waitcnt lgkmcnt(0)\n\t" \
  "v_mfma_f32_16x16x32_bf16 v[252:255], v[92:95], v[84:87], v[252:255]\n\t" \
  "v_mfma_f32_16x16x32_bf16 v[188:191], v[92:95], v[104:107], v[188:191]\n\t" \
  "v_mfma_f32_16x16x32_bf16 v[188:191], v[88:91], v[64:67], v[188:191]\n\t" \
  "s_barrier\n\t" \
  "v_mfma_f32_16x16x32_bf16 v[196:199], v[88:91], v[116:119], v[196:199]\n\t" \
  "ds_read_b64_tr_b16 v[108:109], v58 offset:34816\n\t" \
  "v_mfma_f32_16x16x32_bf16 v[196:199], v[92:95], v[112:115], v[196:199]\n\t" \
  "ds_read_b64_tr_b16 v[110:111], v58 offset:35328\n\t" \
  "v_mfma_f32_16x16x32_bf16 v[204:207], v[92:95], v[124:127], v[204:207]\n\t" \
  "ds_read_b128 v[72:75], v56 offset:2048\n\t" \
  "v_mfma_f32_16x16x32_bf16 v[204:207], v[88:91], v[120:123], v[204:207]\n\t" \
  "ds_read_b64_tr_b16 v[136:137], v58 offset:38912\n\t" \
  "v_mfma_f32_16x16x32_bf16 v[212:215], v[88:91], v[132:135], v[212:215]\n\t" \
  "ds_read_b64_tr_b16 v[138:139], v58 offset:39424\n\t" \
  "v_mfma_f32_16x16x32_bf16 v[212:215], v[92:95], v[128:131], v[212:215]\n\t" \
  "ds_read_b128 v[140:143], v57 offset:2048\n\t" \
  "v_mfma_f32_16x16x32_bf16 v[152:155], v[96:99], v[64:67], v[152:155]\n\t" \
  "ds_read_b128 v[68:71], v57 offset:4096\n\t" \
  "v_mfma_f32_16x16x32_bf16 v[152:155], v[148:151], v[104:107], v[152:155]\n\t" \
  "ds_read_b128 v[76:79], v56 offset:4096\n\t" \
  "v_mfma_f32_16x16x32_bf16 v[160:163], v[148:151], v[112:115], v[160:163]\n\t" \
  "ds_read_b128 v[80:83], v56 offset:6144\n\t" \
  "v_mfma_f32_16x16x32_bf16 v[160:163], v[96:99], v[116:119], v[160:163]\n\t" \
  "ds_read_b128 v[84:87], v57 offset:6144\n\t" \
  "v_mfma_f32_16x16x32_bf16 v[168:171], v[96:99], v[120:123], v[168:171]\n\t" \
  "ds_read_b128 v[88:91], v57 offset:8192\n\t" \
  "v_mfma_f32_16x16x32_bf16 v[168:171], v[148:151], v[124:127], v[168:171]\n\t" \
  "ds_read_b128 v[92:95], v56 offset:8192\n\t" \
  "v_mfma_f32_16x16x32_bf16 v[176:179], v[148:151], v[128:131], v[176:179]\n\t" \
  "ds_read_b64_tr_b16 v[148:149], v59 offset:34816\n\t" \
  "v_mfma_f32_16x16x32_bf16 v[176:179], v[96:99], v[132:135], v[176:179]\n\t" \
  "ds_read_b64_tr_b16 v[150:151], v59 offset:35328\n\t" \
  "v_mfma_f32_16x16x32_bf16 v[156:159], v[100:103], v[64:67], v[156:159]\n\t" \
  "ds_read_b64_tr_b16 v[96:97], v59 offset:38912\n\t" \
  "v_mfma_f32_16x16x32_bf16 v[156:159], v[144:147], v[104:107], v[156:159]\n\t" \
  "ds_read_b64_tr_b16 v[98:99], v59 offset:39424\n\t" \
  "v_mfma_f32_16x16x32_bf16 v[164:167], v[144:147], v[112:115], v[164:167]\n\t" \
  "ds_read_b64_tr_b16 v[64:65], v58 offset:26624\n\t" \
  "v_mfma_f32_16x16x32_bf16 v[164:167], v[100:103], v[116:119], v[164:167]\n\t" \
  "ds_read_b64_tr_b16 v[66:67], v58 offset:27136\n\t" \
  "v_mfma_f32_16x16x32_bf16 v[172:175], v[100:103], v[120:123], v[172:175]\n\t" \
  "ds_read_b64_tr_b16 v[104:105], v58 offset:30720\n\t" \
  "v_mfma_f32_16x16x32_bf16 v[172:175], v[144:147], v[124:127], v[172:175]\n\t" \
  "ds_read_b64_tr_b16 v[106:107], v58 offset:31232\n\t" \
  "v_mfma_f32_16x16x32_bf16 v[180:183], v[144:147], v[128:131], v[180:183]\n\t" \
  "ds_read_b128 v[112:115], v56 offset:0\n\t" \
  "v_mfma_f32_16x16x32_bf16 v[180:183], v[100:103], v[132:135], v[180:183]\n\t" \
  "ds_read_b128 v[116:119], v57 offset:0\n\t" \
  "s_waitcnt lgkmcnt(7)\n\t" \
  "v_mfma_f32_16x16x32_bf16 v[216:219], v[72:75], v[108:111], v[216:219]\n\t" \
  "ds_read_b64_tr_b16 v[120:121], v59 offset:26624\n\t" \
  "v_mfma_f32_16x16x32_bf16 v[216:219], v[140:143], v[136:139], v[216:219]\n\t" \
  "ds_read_b64_tr_b16 v[122:123], v59 offset:27136\n\t" \
  "v_mfma_f32_16x16x32_bf16 v[224:227], v[68:71], v[136:139], v[224:227]\n\t" \
  "ds_read_b64_tr_b16 v[124:125], v59 offset:30720\n\t" \
  "v_mfma_f32_16x16x32_bf16 v[224:227], v[76:79], v[108:111], v[224:227]\n\t" \
  "ds_read_b64_tr_b16 v[126:127], v59 offset:31232\n\t" \
  "v_mfma_f32_16x16x32_bf16 v[232:235], v[80:83], v[108:111], v[232:235]\n\t" \
  "ds_read_b64_tr_b16 v[128:129], v58 offset:18432\n\t" \
  "v_mfma_f32_16x16x32_bf16 v[232:235], v[84:87], v[136:139], v[232:235]\n\t" \
  "ds_read_b64_tr_b16 v[130:131], v58 offset:18944\n\t" \
  "v_mfma_f32_16x16x32_bf16 v[240:243], v[88:91], v[136:139], v[240:243]\n\t" \
  "ds_read_b64_tr_b16 v[144:145], v58 offset:22528\n\t" \
  "v_mfma_f32_16x16x32_bf16 v[240:243], v[92:95], v[108:111], v[240:243]\n\t" \
  "ds_read_b64_tr_b16 v[146:147], v58 offset:23040\n\t" \
  "v_mfma_f32_16x16x32_bf16 v[220:223], v[72:75], v[148:151], v[220:223]\n\t" \
  "ds_read_b64_tr_b16 v[100:101], v59 offset:18432\n\t" \
  "s_waitcnt lgkmcnt(7)\n\t" \
  "v_mfma_f32_16x16x32_bf16 v[220:223], v[140:143], v[96:99], v[220:223]\n\t" \
  "ds_read_b64_tr_b16 v[102:103], v59 offset:18944\n\t" \
  "v_mfma_f32_16x16x32_bf16 v[228:231], v[68:71], v[96:99], v[228:231]\n\t" \
  "ds_read_b64_tr_b16 v[132:133], v59 offset:22528\n\t" \
  "v_mfma_f32_16x16x32_bf16 v[228:231], v[76:79], v[148:151], v[228:231]\n\t" \
  "ds_read_b64_tr_b16 v[134:135], v59 offset:23040\n\t" \
  "v_mfma_f32_16x16x32_bf16 v[236:239], v[80:83], v[148:151], v[236:239]\n\t" \
  "v_mfma_f32_16x16x32_bf16 v[236:239], v[84:87], v[96:99], v[236:239]\n\t" \
  "v_mfma_f32_16x16x32_bf16 v[244:247], v[88:91], v[96:99], v[244:247]\n\t" \
  "v_mfma_f32_16x16x32_bf16 v[244:247], v[92:95], v[148:151], v[244:247]\n\t" \
  "503:\n\t" \
  "s_cbranch_execz 507f\n\t" \
  "v_mfma_f32_16x16x32_bf16 v[184:187], v[64:67], v[72:75], v[184:187]\n\t" \
  "v_mfma_f32_16x16x32_bf16 v[184:187], v[104:107], v[140:143], v[184:187]\n\t" \
  "v_mfma_f32_16x16x32_bf16 v[192:195], v[104:107], v[68:71], v[192:195]\n\t" \
  "v_mfma_f32_16x16x32_bf16 v[192:195], v[64:67], v[76:79], v[192:195]\n\t" \
  "v_mfma_f32_16x16x32_bf16 v[200:203], v[64:67], v[80:83], v[200:203]\n\t" \
  "v_mfma_f32_16x16x32_bf16 v[200:203], v[104:107], v[84:87], v[200:203]\n\t" \
  "v_mfma_f32_16x16x32_bf16 v[208:211], v[104:107], v[88:91], v[208:211]\n\t" \
  "v_mfma_f32_16x16x32_bf16 v[208:211], v[64:67], v[92:95], v[208:211]\n\t" \
  "v_mfma_f32_16x16x32_bf16 v[248:251], v[64:67], v[112:115], v[248:251]\n\t" \
  "v_mfma_f32_16x16x32_bf16 v[248:251], v[104:107], v[116:119], v[248:251]\n\t" \
  "v_mfma_f32_16x16x32_bf16 v[252:255], v[120:123], v[112:115], v[252:255]\n\t" \
  "s_waitcnt lgkmcnt(0)\n\t" \
  "v_mfma_f32_16x16x32_bf16 v[252:255], v[124:127], v[116:119], v[252:255]\n\t" \
  "v_mfma_f32_16x16x32_bf16 v[188:191], v[124:127], v[140:143], v[188:191]\n\t" \
  "v_mfma_f32_16x16x32_bf16 v[188:191], v[120:123], v[72:75], v[188:191]\n\t" \
  "s_barrier\n\t" \
  "v_mfma_f32_16x16x32_bf16 v[196:199], v[120:123], v[76:79], v[196:199]\n\t" \
  "ds_read_b64_tr_b16 v[136:137], v62 offset:34816\n\t" \
  "v_bfe_u32 v30, v4, 9, 1\n\t" \
  "v_mfma_f32_16x16x32_bf16 v[196:199], v[124:127], v[68:71], v[196:199]\n\t" \
  "ds_read_b64_tr_b16 v[138:139], v62 offset:35328\n\t" \
  "v_mad_i32_i24 v30, v30, -16, v0\n\t" \
  "v_mfma_f32_16x16x32_bf16 v[204:207], v[124:127], v[84:87], v[204:207]\n\t" \
  "ds_read_b128 v[108:111], v60 offset:2048\n\t" \
  "ds_read_b128 v[26:29], v0 offset:1552\n\t" \
  "v_mfma_f32_16x16x32_bf16 v[204:207], v[120:123], v[80:83], v[204:207]\n\t" \
  "ds_read_b64_tr_b16 v[96:97], v62 offset:38912\n\t" \
  "ds_read_b128 v[30:33], v30 offset:1040\n\t" \
  "v_mfma_f32_16x16x32_bf16 v[212:215], v[120:123], v[92:95], v[212:215]\n\t" \
  "ds_read_b64_tr_b16 v[98:99], v62 offset:39424\n\t" \
  "ds_read_b128 v[34:37], v0 offset:512\n\t" \
  "v_mfma_f32_16x16x32_bf16 v[212:215], v[124:127], v[88:91], v[212:215]\n\t" \
  "ds_read_b128 v[148:151], v61 offset:2048\n\t" \
  "ds_read_b64_tr_b16 v[38:39], v1 offset:512\n\t" \
  "v_mfma_f32_16x16x32_bf16 v[152:155], v[128:131], v[72:75], v[152:155]\n\t" \
  "ds_read_b128 v[64:67], v61 offset:4096\n\t" \
  "ds_read_b64_tr_b16 v[40:41], v1 offset:0\n\t" \
  "v_mfma_f32_16x16x32_bf16 v[152:155], v[144:147], v[140:143], v[152:155]\n\t" \
  "ds_read_b128 v[104:107], v60 offset:4096\n\t" \
  "s_waitcnt vmcnt(0)\n\t" \
  "v_mfma_f32_16x16x32_bf16 v[160:163], v[144:147], v[68:71], v[160:163]\n\t" \
  "ds_read_b128 v[112:115], v60 offset:6144\n\t" \
  "v_perm_b32 v18, v10, v10, v14\n\t" \
  "v_mfma_f32_16x16x32_bf16 v[160:163], v[128:131], v[76:79], v[160:163]\n\t" \
  "ds_read_b128 v[116:119], v61 offset:6144\n\t" \
  "v_perm_b32 v19, v10, v10, v15\n\t" \
  "v_mfma_f32_16x16x32_bf16 v[168:171], v[128:131], v[80:83], v[168:171]\n\t" \
  "ds_read_b128 v[120:123], v61 offset:8192\n\t" \
  "v_perm_b32 v20, v10, v10, v16\n\t" \
  "v_mfma_f32_16x16x32_bf16 v[168:171], v[144:147], v[84:87], v[168:171]\n\t" \
  "ds_read_b128 v[124:127], v60 offset:8192\n\t" \
  "v_perm_b32 v21, v10, v10, v17\n\t" \
  "v_mfma_f32_16x16x32_bf16 v[176:179], v[144:147], v[88:91], v[176:179]\n\t" \
  "ds_read_b64_tr_b16 v[144:145], v63 offset:34816\n\t" \
  "v_perm_b32 v22, v11, v11, v14\n\t" \
  "v_mfma_f32_16x16x32_bf16 v[176:179], v[128:131], v[92:95], v[176:179]\n\t" \
  "ds_read_b64_tr_b16 v[146:147], v63 offset:35328\n\t" \
  "v_perm_b32 v23, v11, v11, v15\n\t" \
  "v_mfma_f32_16x16x32_bf16 v[156:159], v[100:103], v[72:75], v[156:159]\n\t" \
  "ds_read_b64_tr_b16 v[128:129], v63 offset:38912\n\t" \
  "v_perm_b32 v24, v11, v11, v16\n\t" \
  "v_mfma_f32_16x16x32_bf16 v[156:159], v[132:135], v[140:143], v[156:159]\n\t" \
  "ds_read_b64_tr_b16 v[130:131], v63 offset:39424\n\t" \
  "v_perm_b32 v25, v11, v11, v17\n\t" \
  "v_mfma_f32_16x16x32_bf16 v[164:167], v[132:135], v[68:71], v[164:167]\n\t" \
  "ds_read_b64_tr_b16 v[72:73], v62 offset:26624\n\t" \
  "v_mfma_f32_16x16x32_bf16 v[164:167], v[100:103], v[76:79], v[164:167]\n\t" \
  "ds_read_b64_tr_b16 v[74:75], v62 offset:27136\n\t" \
  "v_mfma_f32_16x16x32_bf16 v[172:175], v[100:103], v[80:83], v[172:175]\n\t" \
  "ds_read_b64_tr_b16 v[140:141], v62 offset:30720\n\t" \
  "s_waitcnt lgkmcnt(6)\n\t" \
  "v_mfma_f32_16x16x32_bf16 v[42:45], v[26:29], v[18:21], 0\n\t" \
  "v_mfma_f32_16x16x32_bf16 v[42:45], v[30:33], v[22:25], v[42:45]\n\t" \
  "v_mfma_f32_16x16x32_bf16 v[46:49], v[30:33], v[18:21], 0\n\t" \
  "v_mfma_f32_16x16x32_bf16 v[46:49], v[34:37], v[22:25], v[46:49]\n\t" \
  "ds_read_b128 v[26:29], v0 offset:1808\n\t" \
  "ds_read_b128 v[30:33], v0 offset:1296\n\t" \
  "v_mfma_f32_16x16x32_bf16 v[172:175], v[132:135], v[84:87], v[172:175]\n\t" \
  "ds_read_b64_tr_b16 v[142:143], v62 offset:31232\n\t" \
  "ds_read_b128 v[34:37], v0 offset:768\n\t" \
  "v_mfma_f32_16x16x32_bf16 v[180:183], v[132:135], v[88:91], v[180:183]\n\t" \
  "ds_read_b128 v[68:71], v60 offset:0\n\t" \
  "v_mfma_f32_16x16x32_bf16 v[180:183], v[100:103], v[92:95], v[180:183]\n\t" \
  "ds_read_b128 v[76:79], v61 offset:0\n\t" \
  "v_mfma_f32_16x16x32_bf16 v[216:219], v[108:111], v[136:139], v[216:219]\n\t" \
  "ds_read_b64_tr_b16 v[80:81], v63 offset:26624\n\t" \
  "v_lshlrev_b32 v50, 16, v38\n\t" \
  "v_mfma_f32_16x16x32_bf16 v[216:219], v[148:151], v[96:99], v[216:219]\n\t" \
  "ds_read_b64_tr_b16 v[82:83], v63 offset:27136\n\t" \
  "v_lshlrev_b32 v51, 16, v40\n\t" \
  "v_mfma_f32_16x16x32_bf16 v[224:227], v[64:67], v[96:99], v[224:227]\n\t" \
  "ds_read_b64_tr_b16 v[84:85], v63 offset:30720\n\t" \
  "v_mul_f32 v42, v42, v50\n\t" \
  "v_mfma_f32_16x16x32_bf16 v[224:227], v[104:107], v[136:139], v[224:227]\n\t" \
  "ds_read_b64_tr_b16 v[86:87], v63 offset:31232\n\t" \
  "v_mul_f32 v46, v46, v51\n\t" \
  "v_mfma_f32_16x16x32_bf16 v[232:235], v[112:115], v[136:139], v[232:235]\n\t" \
  "ds_read_b64_tr_b16 v[88:89], v62 offset:18432\n\t" \
  "v_cvt_pk_bf16_f32 v42, v42, v46\n\t" \
  "v_mfma_f32_16x16x32_bf16 v[232:235], v[116:119], v[96:99], v[232:235]\n\t" \
  "ds_read_b64_tr_b16 v[90:91], v62 offset:18944\n\t" \
  "v_and_b32 v50, 0xffff0000, v38\n\t" \
  "v_mfma_f32_16x16x32_bf16 v[240:243], v[120:123], v[96:99], v[240:243]\n\t" \
  "ds_read_b64_tr_b16 v[132:133], v62 offset:22528\n\t" \
  "v_and_b32 v51, 0xffff0000, v40\n\t" \
  "v_mfma_f32_16x16x32_bf16 v[240:243], v[124:127], v[136:139], v[240:243]\n\t" \
  "ds_read_b64_tr_b16 v[134:135], v62 offset:23040\n\t" \
  "v_mul_f32 v43, v43, v50\n\t" \
  "s_waitcnt lgkmcnt(7)\n\t" \
  "v_mfma_f32_16x16x32_bf16 v[220:223], v[108:111], v[144:147], v[220:223]\n\t" \
  "ds_read_b64_tr_b16 v[92:93], v63 offset:18432\n\t" \
  "v_mul_f32 v47, v47, v51\n\t" \
  "v_mfma_f32_16x16x32_bf16 v[220:223], v[148:151], v[128:131], v[220:223]\n\t" \
  "ds_read_b64_tr_b16 v[94:95], v63 offset:18944\n\t" \
  "v_cvt_pk_bf16_f32 v43, v43, v47\n\t" \
  "v_mfma_f32_16x16x32_bf16 v[228:231], v[64:67], v[128:131], v[228:231]\n\t" \
  "ds_read_b64_tr_b16 v[100:101], v63 offset:22528\n\t" \
  "v_lshlrev_b32 v50, 16, v39\n\t" \
  "v_mfma_f32_16x16x32_bf16 v[228:231], v[104:107], v[144:147], v[228:231]\n\t" \
  "ds_read_b64_tr_b16 v[102:103], v63 offset:23040\n\t" \
  "v_lshlrev_b32 v51, 16, v41\n\t" \
  "v_mfma_f32_16x16x32_bf16 v[236:239], v[112:115], v[144:147], v[236:239]\n\t" \
  "v_mul_f32 v44, v44, v50\n\t" \
  "v_mul_f32 v48, v48, v51\n\t" \
  "v_mfma_f32_16x16x32_bf16 v[236:239], v[116:119], v[128:131], v[236:239]\n\t" \
  "v_cvt_pk_bf16_f32 v44, v44, v48\n\t" \
  "v_and_b32 v50, 0xffff0000, v39\n\t" \
  "v_mfma_f32_16x16x32_bf16 v[244:247], v[120:123], v[128:131], v[244:247]\n\t" \
  "v_and_b32 v51, 0xffff0000, v41\n\t" \
  "v_mul_f32 v45, v45, v50\n\t" \
  "v_mfma_f32_16x16x32_bf16 v[244:247], v[124:127], v[144:147], v[244:247]\n\t" \
  "v_mul_f32 v49, v49, v51\n\t" \
  "ds_read_b64_tr_b16 v[38:39], v1 offset:768\n\t" \
  "v_mfma_f32_16x16x32_bf16 v[184:187], v[72:75], v[108:111], v[184:187]\n\t" \
  "ds_read_b64_tr_b16 v[40:41], v1 offset:256\n\t" \
  "v_cvt_pk_bf16_f32 v45, v45, v49\n\t" \
  "v_mfma_f32_16x16x32_bf16 v[184:187], v[140:143], v[148:151], v[184:187]\n\t" \
  "v_mfma_f32_16x16x32_bf16 v[192:195], v[140:143], v[64:67], v[192:195]\n\t" \
  "v_mfma_f32_16x16x32_bf16 v[192:195], v[72:75], v[104:107], v[192:195]\n\t" \
  "v_mfma_f32_16x16x32_bf16 v[200:203], v[72:75], v[112:115], v[200:203]\n\t" \
  "v_mfma_f32_16x16x32_bf16 v[200:203], v[140:143], v[116:119], v[200:203]\n\t" \
  "v_mfma_f32_16x16x32_bf16 v[208:211], v[140:143], v[120:123], v[208:211]\n\t" \
  "v_mfma_f32_16x16x32_bf16 v[208:211], v[72:75], v[124:127], v[208:211]\n\t" \
  "v_mfma_f32_16x16x32_bf16 v[248:251], v[72:75], v[68:71], v[248:251]\n\t" \
  "v_mfma_f32_16x16x32_bf16 v[248:251], v[140:143], v[76:79], v[248:251]\n\t" \
  "s_waitcnt lgkmcnt(0)\n\t" \
  "v_mfma_f32_16x16x32_bf16 v[252:255], v[80:83], v[68:71], v[252:255]\n\t" \
  "v_mfma_f32_16x16x32_bf16 v[252:255], v[84:87], v[76:79], v[252:255]\n\t" \
  "v_mfma_f32_16x16x32_bf16 v[188:191], v[84:87], v[148:151], v[188:191]\n\t" \
  "v_mfma_f32_16x16x32_bf16 v[188:191], v[80:83], v[108:111], v[188:191]\n\t" \
  "s_barrier\n\t" \
  "v_mfma_f32_16x16x32_bf16 v[196:199], v[80:83], v[104:107], v[196:199]\n\t" \
  "ds_read_b64_tr_b16 v[96:97], v54 offset:34816\n\t" \
  "v_mfma_f32_16x16x32_bf16 v[196:199], v[84:87], v[64:67], v[196:199]\n\t" \
  "v_mov_b32 v50, 163344\n\t" \
  "ds_read_b64_tr_b16 v[98:99], v54 offset:35328\n\t" \
  "v_mfma_f32_16x16x32_bf16 v[204:207], v[84:87], v[116:119], v[204:207]\n\t" \
  "ds_read_b32 v50, v50 offset:24\n\t" \
  "ds_read_b128 v[136:139], v52 offset:2048\n\t" \
  "v_mfma_f32_16x16x32_bf16 v[204:207], v[80:83], v[112:115], v[204:207]\n\t" \
  "ds_read_b64_tr_b16 v[128:129], v54 offset:38912\n\t" \
  "v_mfma_f32_16x16x32_bf16 v[212:215], v[80:83], v[124:127], v[212:215]\n\t" \
  "ds_read_b64_tr_b16 v[130:131], v54 offset:39424\n\t" \
  "v_mfma_f32_16x16x32_bf16 v[212:215], v[84:87], v[120:123], v[212:215]\n\t" \
  "ds_read_b128 v[144:147], v53 offset:2048\n\t" \
  "v_mfma_f32_16x16x32_bf16 v[152:155], v[88:91], v[108:111], v[152:155]\n\t" \
  "ds_read_b128 v[72:75], v53 offset:4096\n\t" \
  "v_mfma_f32_16x16x32_bf16 v[152:155], v[132:135], v[148:151], v[152:155]\n\t" \
  "ds_read_b128 v[140:143], v52 offset:4096\n\t" \
  "v_mfma_f32_16x16x32_bf16 v[160:163], v[132:135], v[64:67], v[160:163]\n\t" \
  "ds_read_b128 v[68:71], v52 offset:6144\n\t" \
  "v_mfma_f32_16x16x32_bf16 v[160:163], v[88:91], v[104:107], v[160:163]\n\t" \
  "ds_read_b128 v[76:79], v53 offset:6144\n\t" \
  "v_mfma_f32_16x16x32_bf16 v[168:171], v[88:91], v[112:115], v[168:171]\n\t" \
  "ds_read_b128 v[80:83], v53 offset:8192\n\t" \
  "v_mfma_f32_16x16x32_bf16 v[168:171], v[132:135], v[116:119], v[168:171]\n\t" \
  "ds_read_b128 v[84:87], v52 offset:8192\n\t" \
  "v_mfma_f32_16x16x32_bf16 v[176:179], v[132:135], v[120:123], v[176:179]\n\t" \
  "s_waitcnt lgkmcnt(10)\n\t" \
  "v_readfirstlane_b32 s88, v50\n\t" \
  "s_mov_b32 s89, 0\n\t" \
  "ds_read_b64_tr_b16 v[132:133], v55 offset:34816\n\t" \
  "v_mfma_f32_16x16x32_bf16 v[176:179], v[88:91], v[124:127], v[176:179]\n\t" \
  "ds_read_b64_tr_b16 v[134:135], v55 offset:35328\n\t" \
  "v_mfma_f32_16x16x32_bf16 v[156:159], v[92:95], v[108:111], v[156:159]\n\t" \
  "s_xor_b64 %24, %24, s[88:89]\n\t" \
  "ds_read_b64_tr_b16 v[88:89], v55 offset:38912\n\t" \
  "v_mfma_f32_16x16x32_bf16 v[156:159], v[100:103], v[148:151], v[156:159]\n\t" \
  "s_xor_b64 %25, %25, s[88:89]\n\t" \
  "ds_read_b64_tr_b16 v[90:91], v55 offset:39424\n\t" \
  "v_mfma_f32_16x16x32_bf16 v[164:167], v[100:103], v[64:67], v[164:167]\n\t" \
  "s_xor_b64 %14, %14, s[88:89]\n\t" \
  "ds_read_b64_tr_b16 v[108:109], v54 offset:26624\n\t" \
  "v_mfma_f32_16x16x32_bf16 v[164:167], v[92:95], v[104:107], v[164:167]\n\t" \
  "s_xor_b64 %19, %19, s[88:89]\n\t" \
  "ds_read_b64_tr_b16 v[110:111], v54 offset:27136\n\t" \
  "v_mfma_f32_16x16x32_bf16 v[172:175], v[92:95], v[112:115], v[172:175]\n\t" \
  "s_xor_b64 %21, %21, s[88:89]\n\t" \
  "ds_read_b64_tr_b16 v[148:149], v54 offset:30720\n\t" \
  "v_mfma_f32_16x16x32_bf16 v[172:175], v[100:103], v[116:119], v[172:175]\n\t" \
  "s_xor_b64 s[92:93], s[92:93], s[88:89]\n\t" \
  "ds_read_b64_tr_b16 v[150:151], v54 offset:31232\n\t" \
  "v_mfma_f32_16x16x32_bf16 v[180:183], v[100:103], v[120:123], v[180:183]\n\t" \
  "ds_read_b128 v[64:67], v52 offset:0\n\t" \
  "v_mfma_f32_16x16x32_bf16 v[180:183], v[92:95], v[124:127], v[180:183]\n\t" \
  "ds_read_b128 v[104:107], v53 offset:0\n\t" \
  "s_waitcnt lgkmcnt(7)\n\t" \
  "v_mfma_f32_16x16x32_bf16 v[216:219], v[136:139], v[96:99], v[216:219]\n\t" \
  "ds_read_b64_tr_b16 v[112:113], v55 offset:26624\n\t" \
  "v_mfma_f32_16x16x32_bf16 v[216:219], v[144:147], v[128:131], v[216:219]\n\t" \
  "ds_read_b64_tr_b16 v[114:115], v55 offset:27136\n\t" \
  "v_mfma_f32_16x16x32_bf16 v[224:227], v[72:75], v[128:131], v[224:227]\n\t" \
  "ds_read_b64_tr_b16 v[116:117], v55 offset:30720\n\t" \
  "v_mfma_f32_16x16x32_bf16 v[224:227], v[140:143], v[96:99], v[224:227]\n\t" \
  "ds_read_b64_tr_b16 v[118:119], v55 offset:31232\n\t" \
  "v_mfma_f32_16x16x32_bf16 v[232:235], v[68:71], v[96:99], v[232:235]\n\t" \
  "ds_read_b64_tr_b16 v[100:101], v54 offset:18432\n\t" \
  "v_mfma_f32_16x16x32_bf16 v[232:235], v[76:79], v[128:131], v[232:235]\n\t" \
  "ds_read_b64_tr_b16 v[102:103], v54 offset:18944\n\t" \
  "v_mfma_f32_16x16x32_bf16 v[240:243], v[80:83], v[128:131], v[240:243]\n\t" \
  "ds_read_b64_tr_b16 v[120:121], v54 offset:22528\n\t" \
  "v_mfma_f32_16x16x32_bf16 v[240:243], v[84:87], v[96:99], v[240:243]\n\t" \
  "ds_read_b64_tr_b16 v[122:123], v54 offset:23040\n\t" \
  "v_mfma_f32_16x16x32_bf16 v[220:223], v[136:139], v[132:135], v[220:223]\n\t" \
  "ds_read_b64_tr_b16 v[92:93], v55 offset:18432\n\t" \
  "s_waitcnt lgkmcnt(7)\n\t" \
  "v_mfma_f32_16x16x32_bf16 v[220:223], v[144:147], v[88:91], v[220:223]\n\t" \
  "ds_read_b64_tr_b16 v[94:95], v55 offset:18944\n\t" \
  "v_mfma_f32_16x16x32_bf16 v[228:231], v[72:75], v[88:91], v[228:231]\n\t" \
  "ds_read_b64_tr_b16 v[124:125], v55 offset:22528\n\t" \
  "v_mfma_f32_16x16x32_bf16 v[228:231], v[140:143], v[132:135], v[228:231]\n\t" \
  "ds_read_b64_tr_b16 v[126:127], v55 offset:23040\n\t" \
  "v_mfma_f32_16x16x32_bf16 v[236:239], v[68:71], v[132:135], v[236:239]\n\t" \
  "v_mfma_f32_16x16x32_bf16 v[236:239], v[76:79], v[88:91], v[236:239]\n\t" \
  "v_mfma_f32_16x16x32_bf16 v[244:247], v[80:83], v[88:91], v[244:247]\n\t" \
  "v_mfma_f32_16x16x32_bf16 v[244:247], v[84:87], v[132:135], v[244:247]\n\t" \
  "v_mfma_f32_16x16x32_bf16 v[184:187], v[108:111], v[136:139], v[184:187]\n\t" \
  "v_mfma_f32_16x16x32_bf16 v[184:187], v[148:151], v[144:147], v[184:187]\n\t" \
  "v_mfma_f32_16x16x32_bf16 v[192:195], v[148:151], v[72:75], v[192:195]\n\t" \
  "v_mfma_f32_16x16x32_bf16 v[192:195], v[108:111], v[140:143], v[192:195]\n\t" \
  "v_mfma_f32_16x16x32_bf16 v[200:203], v[108:111], v[68:71], v[200:203]\n\t" \
  "global_store_dwordx4 v2, v[42:45], %24 offset:0 sc1\n\t" \
  "v_mfma_f32_16x16x32_bf16 v[200:203], v[148:151], v[76:79], v[200:203]\n\t" \
  "s_nop 0\n\t" \
  "v_mfma_f32_16x16x32_bf16 v[42:45], v[26:29], v[18:21], 0\n\t" \
  "v_mfma_f32_16x16x32_bf16 v[42:45], v[30:33], v[22:25], v[42:45]\n\t" \
  "v_mfma_f32_16x16x32_bf16 v[46:49], v[30:33], v[18:21], 0\n\t" \
  "v_mfma_f32_16x16x32_bf16 v[46:49], v[34:37], v[22:25], v[46:49]\n\t" \
  "v_bfe_u32 v30, v4, 9, 1\n\t" \
  "v_mad_i32_i24 v30, v30, -16, v0\n\t" \
  "v_mfma_f32_16x16x32_bf16 v[208:211], v[148:151], v[80:83], v[208:211]\n\t" \
  "ds_read_b128 v[26:29], v0 offset:1584\n\t" \
  "ds_read_b128 v[30:33], v30 offset:1072\n\t" \
  "v_mfma_f32_16x16x32_bf16 v[208:211], v[108:111], v[84:87], v[208:211]\n\t" \
  "ds_read_b128 v[34:37], v0 offset:544\n\t" \
  "v_perm_b32 v18, v12, v12, v14\n\t" \
  "v_mfma_f32_16x16x32_bf16 v[248:251], v[108:111], v[64:67], v[248:251]\n\t" \
  "v_perm_b32 v19, v12, v12, v15\n\t" \
  "v_perm_b32 v20, v12, v12, v16\n\t" \
  "v_mfma_f32_16x16x32_bf16 v[248:251], v[148:151], v[104:107], v[248:251]\n\t" \
  "v_perm_b32 v21, v12, v12, v17\n\t" \
  "v_perm_b32 v22, v13, v13, v14\n\t" \
  "v_mfma_f32_16x16x32_bf16 v[252:255], v[112:115], v[64:67], v[252:255]\n\t" \
  "v_perm_b32 v23, v13, v13, v15\n\t" \
  "v_perm_b32 v24, v13, v13, v16\n\t" \
  "s_waitcnt lgkmcnt(3)\n\t" \
  "v_mfma_f32_16x16x32_bf16 v[252:255], v[116:119], v[104:107], v[252:255]\n\t" \
  "v_perm_b32 v25, v13, v13, v17\n\t" \
  "v_lshlrev_b32 v50, 16, v38\n\t" \
  "v_mfma_f32_16x16x32_bf16 v[188:191], v[116:119], v[144:147], v[188:191]\n\t" \
  "v_lshlrev_b32 v51, 16, v40\n\t" \
  "v_mul_f32 v42, v42, v50\n\t" \
  "v_mfma_f32_16x16x32_bf16 v[188:191], v[112:115], v[136:139], v[188:191]\n\t" \
  "v_mul_f32 v46, v46, v51\n\t" \
  "v_cvt_pk_bf16_f32 v42, v42, v46\n\t" \
  "s_waitcnt lgkmcnt(0)\n\t" \
  "s_barrier\n\t" \
  "v_mfma_f32_16x16x32_bf16 v[196:199], v[112:115], v[140:143], v[196:199]\n\t" \
  "ds_read_b64_tr_b16 v[128:129], v58 offset:34816\n\t" \
  "v_and_b32 v50, 0xffff0000, v38\n\t" \
  "v_mfma_f32_16x16x32_bf16 v[196:199], v[116:119], v[72:75], v[196:199]\n\t" \
  "ds_read_b64_tr_b16 v[130:131], v58 offset:35328\n\t" \
  "v_and_b32 v51, 0xffff0000, v40\n\t" \
  "v_mfma_f32_16x16x32_bf16 v[204:207], v[116:119], v[76:79], v[204:207]\n\t" \
  "ds_read_b128 v[96:99], v56 offset:2048\n\t" \
  "v_mul_f32 v43, v43, v50\n\t" \
  "v_mfma_f32_16x16x32_bf16 v[204:207], v[112:115], v[68:71], v[204:207]\n\t" \
  "ds_read_b64_tr_b16 v[88:89], v58 offset:38912\n\t" \
  "v_mul_f32 v47, v47, v51\n\t" \
  "v_mfma_f32_16x16x32_bf16 v[212:215], v[112:115], v[84:87], v[212:215]\n\t" \
  "ds_read_b64_tr_b16 v[90:91], v58 offset:39424\n\t" \
  "v_cvt_pk_bf16_f32 v43, v43, v47\n\t" \
  "v_mfma_f32_16x16x32_bf16 v[212:215], v[116:119], v[80:83], v[212:215]\n\t" \
  "ds_read_b128 v[132:135], v57 offset:2048\n\t" \
  "v_lshlrev_b32 v50, 16, v39\n\t" \
  "v_mfma_f32_16x16x32_bf16 v[152:155], v[100:103], v[136:139], v[152:155]\n\t" \
  "ds_read_b128 v[108:111], v57 offset:4096\n\t" \
  "v_lshlrev_b32 v51, 16, v41\n\t" \
  "v_mfma_f32_16x16x32_bf16 v[152:155], v[120:123], v[144:147], v[152:155]\n\t" \
  "ds_read_b128 v[148:151], v56 offset:4096\n\t" \
  "v_mul_f32 v44, v44, v50\n\t" \
  "v_mfma_f32_16x16x32_bf16 v[160:163], v[120:123], v[72:75], v[160:163]\n\t" \
  "ds_read_b128 v[64:67], v56 offset:6144\n\t" \
  "v_mul_f32 v48, v48, v51\n\t" \
  "v_mfma_f32_16x16x32_bf16 v[160:163], v[100:103], v[140:143], v[160:163]\n\t" \
  "ds_read_b128 v[104:107], v57 offset:6144\n\t" \
  "v_cvt_pk_bf16_f32 v44, v44, v48\n\t" \
  "v_mfma_f32_16x16x32_bf16 v[168:171], v[100:103], v[68:71], v[168:171]\n\t" \
  "ds_read_b128 v[112:115], v57 offset:8192\n\t" \
  "v_and_b32 v50, 0xffff0000, v39\n\t" \
  "v_mfma_f32_16x16x32_bf16 v[168:171], v[120:123], v[76:79], v[168:171]\n\t" \
  "ds_read_b128 v[116:119], v56 offset:8192\n\t" \
  "v_and_b32 v51, 0xffff0000, v41\n\t" \
  "v_mfma_f32_16x16x32_bf16 v[176:179], v[120:123], v[80:83], v[176:179]\n\t" \
  "ds_read_b64_tr_b16 v[120:121], v59 offset:34816\n\t" \
  "v_mul_f32 v45, v45, v50\n\t" \
  "v_mfma_f32_16x16x32_bf16 v[176:179], v[100:103], v[84:87], v[176:179]\n\t" \
  "ds_read_b64_tr_b16 v[122:123], v59 offset:35328\n\t" \
  "v_mul_f32 v49, v49, v51\n\t" \
  "v_mfma_f32_16x16x32_bf16 v[156:159], v[92:95], v[136:139], v[156:159]\n\t" \
  "ds_read_b64_tr_b16 v[100:101], v59 offset:38912\n\t" \
  "ds_read_b64_tr_b16 v[38:39], v1 offset:544\n\t" \
  "v_mfma_f32_16x16x32_bf16 v[156:159], v[124:127], v[144:147], v[156:159]\n\t" \
  "ds_read_b64_tr_b16 v[102:103], v59 offset:39424\n\t" \
  "ds_read_b64_tr_b16 v[40:41], v1 offset:32\n\t" \
  "v_mfma_f32_16x16x32_bf16 v[164:167], v[124:127], v[72:75], v[164:167]\n\t" \
  "ds_read_b64_tr_b16 v[136:137], v58 offset:26624\n\t" \
  "v_cvt_pk_bf16_f32 v45, v45, v49\n\t" \
  "v_mfma_f32_16x16x32_bf16 v[164:167], v[92:95], v[140:143], v[164:167]\n\t" \
  "ds_read_b64_tr_b16 v[138:139], v58 offset:27136\n\t" \
  "v_mfma_f32_16x16x32_bf16 v[172:175], v[92:95], v[68:71], v[172:175]\n\t" \
  "ds_read_b64_tr_b16 v[144:145], v58 offset:30720\n\t" \
  "v_mfma_f32_16x16x32_bf16 v[172:175], v[124:127], v[76:79], v[172:175]\n\t" \
  "ds_read_b64_tr_b16 v[146:147], v58 offset:31232\n\t" \
  "v_mfma_f32_16x16x32_bf16 v[180:183], v[124:127], v[80:83], v[180:183]\n\t" \
  "ds_read_b128 v[72:75], v56 offset:0\n\t" \
  "v_mfma_f32_16x16x32_bf16 v[180:183], v[92:95], v[84:87], v[180:183]\n\t" \
  "ds_read_b128 v[140:143], v57 offset:0\n\t" \
  "s_waitcnt lgkmcnt(8)\n\t" \
  "v_mfma_f32_16x16x32_bf16 v[216:219], v[96:99], v[128:131], v[216:219]\n\t" \
  "ds_read_b64_tr_b16 v[68:69], v59 offset:26624\n\t" \
  "v_mfma_f32_16x16x32_bf16 v[216:219], v[132:135], v[88:91], v[216:219]\n\t" \
  "ds_read_b64_tr_b16 v[70:71], v59 offset:27136\n\t" \
  "v_mfma_f32_16x16x32_bf16 v[224:227], v[108:111], v[88:91], v[224:227]\n\t" \
  "ds_read_b64_tr_b16 v[76:77], v59 offset:30720\n\t" \
  "v_mfma_f32_16x16x32_bf16 v[224:227], v[148:151], v[128:131], v[224:227]\n\t" \
  "ds_read_b64_tr_b16 v[78:79], v59 offset:31232\n\t" \
  "v_mfma_f32_16x16x32_bf16 v[232:235], v[64:67], v[128:131], v[232:235]\n\t" \
  "ds_read_b64_tr_b16 v[80:81], v58 offset:18432\n\t" \
  "v_mfma_f32_16x16x32_bf16 v[232:235], v[104:107], v[88:91], v[232:235]\n\t" \
  "ds_read_b64_tr_b16 v[82:83], v58 offset:18944\n\t" \
  "v_mfma_f32_16x16x32_bf16 v[240:243], v[112:115], v[88:91], v[240:243]\n\t" \
  "ds_read_b64_tr_b16 v[124:125], v58 offset:22528\n\t" \
  "v_mfma_f32_16x16x32_bf16 v[240:243], v[116:119], v[128:131], v[240:243]\n\t" \
  "ds_read_b64_tr_b16 v[126:127], v58 offset:23040\n\t" \
  "v_mfma_f32_16x16x32_bf16 v[220:223], v[96:99], v[120:123], v[220:223]\n\t" \
  "ds_read_b64_tr_b16 v[84:85], v59 offset:18432\n\t" \
  "s_waitcnt lgkmcnt(7)\n\t" \
  "v_mfma_f32_16x16x32_bf16 v[220:223], v[132:135], v[100:103], v[220:223]\n\t" \
  "ds_read_b64_tr_b16 v[86:87], v59 offset:18944\n\t" \
  "v_mfma_f32_16x16x32_bf16 v[228:231], v[108:111], v[100:103], v[228:231]\n\t" \
  "ds_read_b64_tr_b16 v[92:93], v59 offset:22528\n\t" \
  "v_mfma_f32_16x16x32_bf16 v[228:231], v[148:151], v[120:123], v[228:231]\n\t" \
  "ds_read_b64_tr_b16 v[94:95], v59 offset:23040\n\t" \
  "v_mfma_f32_16x16x32_bf16 v[236:239], v[64:67], v[120:123], v[236:239]\n\t" \
  "v_mfma_f32_16x16x32_bf16 v[236:239], v[104:107], v[100:103], v[236:239]\n\t" \
  "v_mfma_f32_16x16x32_bf16 v[244:247], v[112:115], v[100:103], v[244:247]\n\t" \
  "v_mfma_f32_16x16x32_bf16 v[244:247], v[116:119], v[120:123], v[244:247]\n\t" \
  "v_mfma_f32_16x16x32_bf16 v[184:187], v[136:139], v[96:99], v[184:187]\n\t" \
  "v_mfma_f32_16x16x32_bf16 v[184:187], v[144:147], v[132:135], v[184:187]\n\t" \
  "v_mfma_f32_16x16x32_bf16 v[192:195], v[144:147], v[108:111], v[192:195]\n\t" \
  "v_mfma_f32_16x16x32_bf16 v[192:195], v[136:139], v[148:151], v[192:195]\n\t" \
  "v_mfma_f32_16x16x32_bf16 v[200:203], v[136:139], v[64:67], v[200:203]\n\t" \
  "global_store_dwordx4 v2, v[42:45], %24 offset:1024 sc1\n\t" \
  "v_add_u32 v2, 0x20000, v2\n\t" \
  "v_mfma_f32_16x16x32_bf16 v[200:203], v[144:147], v[104:107], v[200:203]\n\t" \
  "v_mfma_f32_16x16x32_bf16 v[208:211], v[144:147], v[112:115], v[208:211]\n\t" \
  "v_mfma_f32_16x16x32_bf16 v[42:45], v[26:29], v[18:21], 0\n\t" \
  "v_mfma_f32_16x16x32_bf16 v[42:45], v[30:33], v[22:25], v[42:45]\n\t" \
  "v_mfma_f32_16x16x32_bf16 v[46:49], v[30:33], v[18:21], 0\n\t" \
  "v_mfma_f32_16x16x32_bf16 v[46:49], v[34:37], v[22:25], v[46:49]\n\t" \
  "ds_read_b128 v[26:29], v0 offset:1840\n\t" \
  "ds_read_b128 v[30:33], v0 offset:1328\n\t" \
  "v_mfma_f32_16x16x32_bf16 v[208:211], v[136:139], v[116:119], v[208:211]\n\t" \
  "ds_read_b128 v[34:37], v0 offset:800\n\t" \
  "v_mfma_f32_16x16x32_bf16 v[248:251], v[136:139], v[72:75], v[248:251]\n\t" \
  "v_mfma_f32_16x16x32_bf16 v[248:251], v[144:147], v[140:143], v[248:251]\n\t" \
  "v_lshlrev_b32 v50, 16, v38\n\t" \
  "v_lshlrev_b32 v51, 16, v40\n\t" \
  "v_mfma_f32_16x16x32_bf16 v[252:255], v[68:71], v[72:75], v[252:255]\n\t" \
  "v_mul_f32 v42, v42, v50\n\t" \
  "v_mul_f32 v46, v46, v51\n\t" \
  "s_waitcnt lgkmcnt(3)\n\t" \
  "v_mfma_f32_16x16x32_bf16 v[252:255], v[76:79], v[140:143], v[252:255]\n\t" \
  "v_cvt_pk_bf16_f32 v42, v42, v46\n\t" \
  "v_and_b32 v50, 0xffff0000, v38\n\t" \
  "v_mfma_f32_16x16x32_bf16 v[188:191], v[76:79], v[132:135], v[188:191]\n\t" \
  "v_and_b32 v51, 0xffff0000, v40\n\t" \
  "v_mul_f32 v43, v43, v50\n\t" \
  "v_mfma_f32_16x16x32_bf16 v[188:191], v[68:71], v[96:99], v[188:191]\n\t" \
  "v_mul_f32 v47, v47, v51\n\t" \
  "v_cvt_pk_bf16_f32 v43, v43, v47\n\t" \
  "s_waitcnt lgkmcnt(0)\n\t" \
  "s_barrier\n\t" \
  "v_mfma_f32_16x16x32_bf16 v[196:199], v[68:71], v[148:151], v[196:199]\n\t" \
  "ds_read_b64_tr_b16 v[88:89], v62 offset:34816\n\t" \
  "v_lshlrev_b32 v50, 16, v39\n\t" \
  "v_mfma_f32_16x16x32_bf16 v[196:199], v[76:79], v[108:111], v[196:199]\n\t" \
  "ds_read_b64_tr_b16 v[90:91], v62 offset:35328\n\t" \
  "v_lshlrev_b32 v51, 16, v41\n\t" \
  "v_mfma_f32_16x16x32_bf16 v[204:207], v[76:79], v[104:107], v[204:207]\n\t" \
  "ds_read_b128 v[128:131], v60 offset:2048\n\t" \
  "v_mul_f32 v44, v44, v50\n\t" \
  "v_mfma_f32_16x16x32_bf16 v[204:207], v[68:71], v[64:67], v[204:207]\n\t" \
  "ds_read_b64_tr_b16 v[100:101], v62 offset:38912\n\t" \
  "v_mul_f32 v48, v48, v51\n\t" \
  "v_mfma_f32_16x16x32_bf16 v[212:215], v[68:71], v[116:119], v[212:215]\n\t" \
  "ds_read_b64_tr_b16 v[102:103], v62 offset:39424\n\t" \
  "v_cvt_pk_bf16_f32 v44, v44, v48\n\t" \
  "v_mfma_f32_16x16x32_bf16 v[212:215], v[76:79], v[112:115], v[212:215]\n\t" \
  "ds_read_b128 v[120:123], v61 offset:2048\n\t" \
  "v_and_b32 v50, 0xffff0000, v39\n\t" \
  "v_mfma_f32_16x16x32_bf16 v[152:155], v[80:83], v[96:99], v[152:155]\n\t" \
  "ds_read_b128 v[136:139], v61 offset:4096\n\t" \
  "v_and_b32 v51, 0xffff0000, v41\n\t" \
  "v_mfma_f32_16x16x32_bf16 v[152:155], v[124:127], v[132:135], v[152:155]\n\t" \
  "ds_read_b128 v[144:147], v60 offset:4096\n\t" \
  "v_mul_f32 v45, v45, v50\n\t" \
  "v_mfma_f32_16x16x32_bf16 v[160:163], v[124:127], v[108:111], v[160:163]\n\t" \
  "ds_read_b128 v[72:75], v60 offset:6144\n\t" \
  "v_mul_f32 v49, v49, v51\n\t" \
  "v_mfma_f32_16x16x32_bf16 v[160:163], v[80:83], v[148:151], v[160:163]\n\t" \
  "ds_read_b128 v[140:143], v61 offset:6144\n\t" \
  "ds_read_b64_tr_b16 v[38:39], v1 offset:800\n\t" \
  "v_mfma_f32_16x16x32_bf16 v[168:171], v[80:83], v[64:67], v[168:171]\n\t" \
  "ds_read_b128 v[68:71], v61 offset:8192\n\t" \
  "ds_read_b64_tr_b16 v[40:41], v1 offset:288\n\t" \
  "v_mfma_f32_16x16x32_bf16 v[168:171], v[124:127], v[104:107], v[168:171]\n\t" \
  "ds_read_b128 v[76:79], v60 offset:8192\n\t" \
  "v_cvt_pk_bf16_f32 v45, v45, v49\n\t" \
  "v_mfma_f32_16x16x32_bf16 v[176:179], v[124:127], v[112:115], v[176:179]\n\t" \
  "ds_read_b64_tr_b16 v[124:125], v63 offset:34816\n\t" \
  "v_mfma_f32_16x16x32_bf16 v[176:179], v[80:83], v[116:119], v[176:179]\n\t" \
  "ds_read_b64_tr_b16 v[126:127], v63 offset:35328\n\t" \
  "v_mfma_f32_16x16x32_bf16 v[156:159], v[84:87], v[96:99], v[156:159]\n\t" \
  "ds_read_b64_tr_b16 v[80:81], v63 offset:38912\n\t" \
  "v_mfma_f32_16x16x32_bf16 v[156:159], v[92:95], v[132:135], v[156:159]\n\t" \
  "ds_read_b64_tr_b16 v[82:83], v63 offset:39424\n\t" \
  "v_mfma_f32_16x16x32_bf16 v[164:167], v[92:95], v[108:111], v[164:167]\n\t" \
  "ds_read_b64_tr_b16 v[96:97], v62 offset:26624\n\t" \
  "v_mfma_f32_16x16x32_bf16 v[164:167], v[84:87], v[148:151], v[164:167]\n\t" \
  "ds_read_b64_tr_b16 v[98:99], v62 offset:27136\n\t" \
  "v_mfma_f32_16x16x32_bf16 v[172:175], v[84:87], v[64:67], v[172:175]\n\t" \
  "ds_read_b64_tr_b16 v[132:133], v62 offset:30720\n\t" \
  "v_mfma_f32_16x16x32_bf16 v[172:175], v[92:95], v[104:107], v[172:175]\n\t" \
  "ds_read_b64_tr_b16 v[134:135], v62 offset:31232\n\t" \
  "v_mfma_f32_16x16x32_bf16 v[180:183], v[92:95], v[112:115], v[180:183]\n\t" \
  "ds_read_b128 v[108:111], v60 offset:0\n\t" \
  "v_mfma_f32_16x16x32_bf16 v[180:183], v[84:87], v[116:119], v[180:183]\n\t" \
  "ds_read_b128 v[148:151], v61 offset:0\n\t" \
  "s_waitcnt lgkmcnt(7)\n\t" \
  "v_mfma_f32_16x16x32_bf16 v[216:219], v[128:131], v[88:91], v[216:219]\n\t" \
  "ds_read_b64_tr_b16 v[64:65], v63 offset:26624\n\t" \
  "v_mfma_f32_16x16x32_bf16 v[216:219], v[120:123], v[100:103], v[216:219]\n\t" \
  "ds_read_b64_tr_b16 v[66:67], v63 offset:27136\n\t" \
  "v_mfma_f32_16x16x32_bf16 v[224:227], v[136:139], v[100:103], v[224:227]\n\t" \
  "ds_read_b64_tr_b16 v[104:105], v63 offset:30720\n\t" \
  "v_mfma_f32_16x16x32_bf16 v[224:227], v[144:147], v[88:91], v[224:227]\n\t" \
  "ds_read_b64_tr_b16 v[106:107], v63 offset:31232\n\t" \
  "v_mfma_f32_16x16x32_bf16 v[232:235], v[72:75], v[88:91], v[232:235]\n\t" \
  "ds_read_b64_tr_b16 v[92:93], v62 offset:18432\n\t" \
  "v_mfma_f32_16x16x32_bf16 v[232:235], v[140:143], v[100:103], v[232:235]\n\t" \
  "ds_read_b64_tr_b16 v[94:95], v62 offset:18944\n\t" \
  "v_mfma_f32_16x16x32_bf16 v[240:243], v[68:71], v[100:103], v[240:243]\n\t" \
  "ds_read_b64_tr_b16 v[112:113], v62 offset:22528\n\t" \
  "v_mfma_f32_16x16x32_bf16 v[240:243], v[76:79], v[88:91], v[240:243]\n\t" \
  "ds_read_b64_tr_b16 v[114:115], v62 offset:23040\n\t" \
  "v_mfma_f32_16x16x32_bf16 v[220:223], v[128:131], v[124:127], v[220:223]\n\t" \
  "ds_read_b64_tr_b16 v[84:85], v63 offset:18432\n\t" \
  "s_waitcnt lgkmcnt(7)\n\t" \
  "v_mfma_f32_16x16x32_bf16 v[220:223], v[120:123], v[80:83], v[220:223]\n\t" \
  "ds_read_b64_tr_b16 v[86:87], v63 offset:18944\n\t" \
  "v_mfma_f32_16x16x32_bf16 v[228:231], v[136:139], v[80:83], v[228:231]\n\t" \
  "ds_read_b64_tr_b16 v[116:117], v63 offset:22528\n\t" \
  "v_mfma_f32_16x16x32_bf16 v[228:231], v[144:147], v[124:127], v[228:231]\n\t" \
  "ds_read_b64_tr_b16 v[118:119], v63 offset:23040\n\t" \
  "v_mfma_f32_16x16x32_bf16 v[236:239], v[72:75], v[124:127], v[236:239]\n\t" \
  "v_mfma_f32_16x16x32_bf16 v[236:239], v[140:143], v[80:83], v[236:239]\n\t" \
  "v_mfma_f32_16x16x32_bf16 v[244:247], v[68:71], v[80:83], v[244:247]\n\t" \
  "v_mfma_f32_16x16x32_bf16 v[244:247], v[76:79], v[124:127], v[244:247]\n\t" \
  "v_mfma_f32_16x16x32_bf16 v[184:187], v[96:99], v[128:131], v[184:187]\n\t" \
  "v_mfma_f32_16x16x32_bf16 v[184:187], v[132:135], v[120:123], v[184:187]\n\t" \
  "v_mfma_f32_16x16x32_bf16 v[192:195], v[132:135], v[136:139], v[192:195]\n\t" \
  "v_mfma_f32_16x16x32_bf16 v[192:195], v[96:99], v[144:147], v[192:195]\n\t" \
  "v_mfma_f32_16x16x32_bf16 v[200:203], v[96:99], v[72:75], v[200:203]\n\t" \
  "global_store_dwordx4 v2, v[42:45], %24 offset:0 sc1\n\t" \
  "v_mfma_f32_16x16x32_bf16 v[200:203], v[132:135], v[140:143], v[200:203]\n\t" \
  "s_nop 0\n\t" \
  "v_mfma_f32_16x16x32_bf16 v[42:45], v[26:29], v[18:21], 0\n\t" \
  "v_mfma_f32_16x16x32_bf16 v[42:45], v[30:33], v[22:25], v[42:45]\n\t" \
  "v_mfma_f32_16x16x32_bf16 v[46:49], v[30:33], v[18:21], 0\n\t" \
  "v_mfma_f32_16x16x32_bf16 v[46:49], v[34:37], v[22:25], v[46:49]\n\t" \
  "v_mfma_f32_16x16x32_bf16 v[208:211], v[132:135], v[68:71], v[208:211]\n\t" \
  "v_mfma_f32_16x16x32_bf16 v[208:211], v[96:99], v[76:79], v[208:211]\n\t" \
  "v_lshlrev_b32 v50, 16, v38\n\t" \
  "v_lshlrev_b32 v51, 16, v40\n\t" \
  "v_mfma_f32_16x16x32_bf16 v[248:251], v[96:99], v[108:111], v[248:251]\n\t" \
  "s_nop 0\n\t" \
  "v_mul_f32 v42, v42, v50\n\t" \
  "s_nop 0\n\t" \
  "v_mul_f32 v46, v46, v51\n\t" \
  "v_mfma_f32_16x16x32_bf16 v[248:251], v[132:135], v[148:151], v[248:251]\n\t" \
  "v_cvt_pk_bf16_f32 v42, v42, v46\n\t" \
  "v_and_b32 v50, 0xffff0000, v38\n\t" \
  "v_mfma_f32_16x16x32_bf16 v[252:255], v[64:67], v[108:111], v[252:255]\n\t" \
  "v_and_b32 v51, 0xffff0000, v40\n\t" \
  "v_mul_f32 v43, v43, v50\n\t" \
  "s_waitcnt lgkmcnt(0)\n\t" \
  "v_mfma_f32_16x16x32_bf16 v[252:255], v[104:107], v[148:151], v[252:255]\n\t" \
  "v_mul_f32 v47, v47, v51\n\t" \
  "v_cvt_pk_bf16_f32 v43, v43, v47\n\t" \
  "v_mfma_f32_16x16x32_bf16 v[188:191], v[104:107], v[120:123], v[188:191]\n\t" \
  "v_lshlrev_b32 v50, 16, v39\n\t" \
  "v_lshlrev_b32 v51, 16, v41\n\t" \
  "v_mfma_f32_16x16x32_bf16 v[188:191], v[64:67], v[128:131], v[188:191]\n\t" \
  "v_mul_f32 v44, v44, v50\n\t" \
  "v_mul_f32 v48, v48, v51\n\t" \
  "s_barrier\n\t" \
  "v_mfma_f32_16x16x32_bf16 v[196:199], v[64:67], v[144:147], v[196:199]\n\t" \
  "ds_read_b64_tr_b16 v[100:101], v54 offset:34816\n\t" \
  "v_cvt_pk_bf16_f32 v44, v44, v48\n\t" \
  "v_mfma_f32_16x16x32_bf16 v[196:199], v[104:107], v[136:139], v[196:199]\n\t" \
  "ds_read_b64_tr_b16 v[102:103], v54 offset:35328\n\t" \
  "v_and_b32 v50, 0xffff0000, v39\n\t" \
  "v_mfma_f32_16x16x32_bf16 v[204:207], v[104:107], v[140:143], v[204:207]\n\t" \
  "ds_read_b128 v[88:91], v52 offset:2048\n\t" \
  "v_and_b32 v51, 0xffff0000, v41\n\t" \
  "v_mfma_f32_16x16x32_bf16 v[204:207], v[64:67], v[72:75], v[204:207]\n\t" \
  "ds_read_b64_tr_b16 v[80:81], v54 offset:38912\n\t" \
  "v_mul_f32 v45, v45, v50\n\t" \
  "v_mfma_f32_16x16x32_bf16 v[212:215], v[64:67], v[76:79], v[212:215]\n\t" \
  "ds_read_b64_tr_b16 v[82:83], v54 offset:39424\n\t" \
  "v_mul_f32 v49, v49, v51\n\t" \
  "v_mfma_f32_16x16x32_bf16 v[212:215], v[104:107], v[68:71], v[212:215]\n\t" \
  "ds_read_b128 v[124:127], v53 offset:2048\n\t" \
  "v_cvt_pk_bf16_f32 v45, v45, v49\n\t" \
  "v_mfma_f32_16x16x32_bf16 v[152:155], v[92:95], v[128:131], v[152:155]\n\t" \
  "ds_read_b128 v[96:99], v53 offset:4096\n\t" \
  "v_mfma_f32_16x16x32_bf16 v[152:155], v[112:115], v[120:123], v[152:155]\n\t" \
  "ds_read_b128 v[132:135], v52 offset:4096\n\t" \
  "v_mfma_f32_16x16x32_bf16 v[160:163], v[112:115], v[136:139], v[160:163]\n\t" \
  "ds_read_b128 v[108:111], v52 offset:6144\n\t" \
  "504:\n\t" \
  "s_cbranch_execz 508f\n\t" \
  "v_mfma_f32_16x16x32_bf16 v[160:163], v[92:95], v[144:147], v[160:163]\n\t" \
  "ds_read_b128 v[148:151], v53 offset:6144\n\t" \
  "v_mfma_f32_16x16x32_bf16 v[168:171], v[92:95], v[72:75], v[168:171]\n\t" \
  "ds_read_b128 v[64:67], v53 offset:8192\n\t" \
  "v_mfma_f32_16x16x32_bf16 v[168:171], v[112:115], v[140:143], v[168:171]\n\t" \
  "ds_read_b128 v[104:107], v52 offset:8192\n\t" \
  "v_mfma_f32_16x16x32_bf16 v[176:179], v[112:115], v[68:71], v[176:179]\n\t" \
  "ds_read_b64_tr_b16 v[112:113], v55 offset:34816\n\t" \
  "v_mfma_f32_16x16x32_bf16 v[176:179], v[92:95], v[76:79], v[176:179]\n\t" \
  "ds_read_b64_tr_b16 v[114:115], v55 offset:35328\n\t" \
  "v_mfma_f32_16x16x32_bf16 v[156:159], v[84:87], v[128:131], v[156:159]\n\t" \
  "ds_read_b64_tr_b16 v[92:93], v55 offset:38912\n\t" \
  "v_mfma_f32_16x16x32_bf16 v[156:159], v[116:119], v[120:123], v[156:159]\n\t" \
  "ds_read_b64_tr_b16 v[94:95], v55 offset:39424\n\t" \
  "v_mfma_f32_16x16x32_bf16 v[164:167], v[116:119], v[136:139], v[164:167]\n\t" \
  "ds_read_b64_tr_b16 v[128:129], v54 offset:26624\n\t" \
  "v_mfma_f32_16x16x32_bf16 v[164:167], v[84:87], v[144:147], v[164:167]\n\t" \
  "ds_read_b64_tr_b16 v[130:131], v54 offset:27136\n\t" \
  "v_mfma_f32_16x16x32_bf16 v[172:175], v[84:87], v[72:75], v[172:175]\n\t" \
  "ds_read_b64_tr_b16 v[120:121], v54 offset:30720\n\t" \
  "v_mfma_f32_16x16x32_bf16 v[172:175], v[116:119], v[140:143], v[172:175]\n\t" \
  "ds_read_b64_tr_b16 v[122:123], v54 offset:31232\n\t" \
  "v_mfma_f32_16x16x32_bf16 v[180:183], v[116:119], v[68:71], v[180:183]\n\t" \
  "ds_read_b128 v[136:139], v52 offset:0\n\t" \
  "v_mfma_f32_16x16x32_bf16 v[180:183], v[84:87], v[76:79], v[180:183]\n\t" \
  "ds_read_b128 v[144:147], v53 offset:0\n\t" \
  "s_waitcnt lgkmcnt(7)\n\t" \
  "v_mfma_f32_16x16x32_bf16 v[216:219], v[88:91], v[100:103], v[216:219]\n\t" \
  "ds_read_b64_tr_b16 v[72:73], v55 offset:26624\n\t" \
  "v_mfma_f32_16x16x32_bf16 v[216:219], v[124:127], v[80:83], v[216:219]\n\t" \
  "ds_read_b64_tr_b16 v[74:75], v55 offset:27136\n\t" \
  "v_mfma_f32_16x16x32_bf16 v[224:227], v[96:99], v[80:83], v[224:227]\n\t" \
  "ds_read_b64_tr_b16 v[140:141], v55 offset:30720\n\t" \
  "v_mfma_f32_16x16x32_bf16 v[224:227], v[132:135], v[100:103], v[224:227]\n\t" \
  "ds_read_b64_tr_b16 v[142:143], v55 offset:31232\n\t" \
  "v_mfma_f32_16x16x32_bf16 v[232:235], v[108:111], v[100:103], v[232:235]\n\t" \
  "ds_read_b64_tr_b16 v[68:69], v54 offset:18432\n\t" \
  "v_mfma_f32_16x16x32_bf16 v[232:235], v[148:151], v[80:83], v[232:235]\n\t" \
  "ds_read_b64_tr_b16 v[70:71], v54 offset:18944\n\t" \
  "v_mfma_f32_16x16x32_bf16 v[240:243], v[64:67], v[80:83], v[240:243]\n\t" \
  "ds_read_b64_tr_b16 v[116:117], v54 offset:22528\n\t" \
  "v_mfma_f32_16x16x32_bf16 v[240:243], v[104:107], v[100:103], v[240:243]\n\t" \
  "ds_read_b64_tr_b16 v[118:119], v54 offset:23040\n\t" \
  "v_mfma_f32_16x16x32_bf16 v[220:223], v[88:91], v[112:115], v[220:223]\n\t" \
  "ds_read_b64_tr_b16 v[76:77], v55 offset:18432\n\t" \
  "s_waitcnt lgkmcnt(7)\n\t" \
  "v_mfma_f32_16x16x32_bf16 v[220:223], v[124:127], v[92:95], v[220:223]\n\t" \
  "ds_read_b64_tr_b16 v[78:79], v55 offset:18944\n\t" \
  "v_mfma_f32_16x16x32_bf16 v[228:231], v[96:99], v[92:95], v[228:231]\n\t" \
  "ds_read_b64_tr_b16 v[84:85], v55 offset:22528\n\t" \
  "v_mfma_f32_16x16x32_bf16 v[228:231], v[132:135], v[112:115], v[228:231]\n\t" \
  "ds_read_b64_tr_b16 v[86:87], v55 offset:23040\n\t" \
  "v_mfma_f32_16x16x32_bf16 v[236:239], v[108:111], v[112:115], v[236:239]\n\t" \
  "v_mfma_f32_16x16x32_bf16 v[236:239], v[148:151], v[92:95], v[236:239]\n\t" \
  "v_mfma_f32_16x16x32_bf16 v[244:247], v[64:67], v[92:95], v[244:247]\n\t" \
  "v_mfma_f32_16x16x32_bf16 v[244:247], v[104:107], v[112:115], v[244:247]\n\t" \
  "v_mfma_f32_16x16x32_bf16 v[184:187], v[128:131], v[88:91], v[184:187]\n\t" \
  "v_mfma_f32_16x16x32_bf16 v[184:187], v[120:123], v[124:127], v[184:187]\n\t" \
  "v_mfma_f32_16x16x32_bf16 v[192:195], v[120:123], v[96:99], v[192:195]\n\t" \
  "v_mfma_f32_16x16x32_bf16 v[192:195], v[128:131], v[132:135], v[192:195]\n\t" \
  "v_mfma_f32_16x16x32_bf16 v[200:203], v[128:131], v[108:111], v[200:203]\n\t" \
  "global_store_dwordx4 v2, v[42:45], %24 offset:1024 sc1\n\t" \
  "v_mfma_f32_16x16x32_bf16 v[200:203], v[120:123], v[148:151], v[200:203]\n\t" \
  "v_mfma_f32_16x16x32_bf16 v[208:211], v[120:123], v[64:67], v[208:211]\n\t" \
  "v_mfma_f32_16x16x32_bf16 v[208:211], v[128:131], v[104:107], v[208:211]\n\t" \
  "v_mov_b32 v30, 0xff61b1e6\n\t" \
  "v_bfe_i32 v34, v3, 0, 1\n\t" \
  "v_mfma_f32_16x16x32_bf16 v[248:251], v[128:131], v[136:139], v[248:251]\n\t" \
  "v_and_b32 v34, v30, v34\n\t" \
  "v_bfe_i32 v38, v3, 4, 1\n\t" \
  "v_mfma_f32_16x16x32_bf16 v[248:251], v[120:123], v[144:147], v[248:251]\n\t" \
  "v_and_b32 v38, v30, v38\n\t" \
  "v_bfe_i32 v42, v3, 8, 1\n\t" \
  "v_mfma_f32_16x16x32_bf16 v[252:255], v[72:75], v[136:139], v[252:255]\n\t" \
  "v_bfi_b32 v42, v42, v30, %15\n\t" \
  "v_mov_b32 v48, 0x3f803f80\n\t" \
  "s_waitcnt lgkmcnt(0)\n\t" \
  "v_mfma_f32_16x16x32_bf16 v[252:255], v[140:143], v[144:147], v[252:255]\n\t" \
  "v_bfe_i32 v35, v3, 1, 1\n\t" \
  "v_and_b32 v35, v30, v35\n\t" \
  "v_mfma_f32_16x16x32_bf16 v[188:191], v[140:143], v[124:127], v[188:191]\n\t" \
  "v_bfe_i32 v39, v3, 5, 1\n\t" \
  "v_and_b32 v39, v30, v39\n\t" \
  "v_mfma_f32_16x16x32_bf16 v[188:191], v[72:75], v[88:91], v[188:191]\n\t" \
  "v_bfe_i32 v43, v3, 9, 1\n\t" \
  "v_bfi_b32 v43, v43, v30, %15\n\t" \
  "s_barrier\n\t" \
  "v_mfma_f32_16x16x32_bf16 v[196:199], v[72:75], v[132:135], v[196:199]\n\t" \
  "ds_read_b64_tr_b16 v[80:81], v58 offset:34816\n\t" \
  "v_mov_b32 v49, 0x3f803f80\n\t" \
  "v_mfma_f32_16x16x32_bf16 v[196:199], v[140:143], v[96:99], v[196:199]\n\t" \
  "ds_read_b64_tr_b16 v[82:83], v58 offset:35328\n\t" \
  "v_bfe_i32 v36, v3, 2, 1\n\t" \
  "v_mfma_f32_16x16x32_bf16 v[204:207], v[140:143], v[148:151], v[204:207]\n\t" \
  "ds_read_b128 v[100:103], v56 offset:2048\n\t" \
  "v_and_b32 v36, v30, v36\n\t" \
  "v_mfma_f32_16x16x32_bf16 v[204:207], v[72:75], v[108:111], v[204:207]\n\t" \
  "ds_read_b64_tr_b16 v[92:93], v58 offset:38912\n\t" \
  "v_bfe_i32 v40, v3, 6, 1\n\t" \
  "v_mfma_f32_16x16x32_bf16 v[212:215], v[72:75], v[104:107], v[212:215]\n\t" \
  "ds_read_b64_tr_b16 v[94:95], v58 offset:39424\n\t" \
  "v_and_b32 v40, v30, v40\n\t" \
  "v_mfma_f32_16x16x32_bf16 v[212:215], v[140:143], v[64:67], v[212:215]\n\t" \
  "ds_read_b128 v[112:115], v57 offset:2048\n\t" \
  "v_bfe_i32 v44, v3, 10, 1\n\t" \
  "v_mfma_f32_16x16x32_bf16 v[152:155], v[68:71], v[88:91], v[152:155]\n\t" \
  "ds_read_b128 v[128:131], v57 offset:4096\n\t" \
  "v_bfi_b32 v44, v44, v30, %15\n\t" \
  "v_mfma_f32_16x16x32_bf16 v[152:155], v[116:119], v[124:127], v[152:155]\n\t" \
  "ds_read_b128 v[120:123], v56 offset:4096\n\t" \
  "v_mov_b32 v50, 0x3f803f80\n\t" \
  "v_mfma_f32_16x16x32_bf16 v[160:163], v[116:119], v[96:99], v[160:163]\n\t" \
  "ds_read_b128 v[136:139], v56 offset:6144\n\t" \
  "v_bfe_i32 v37, v3, 3, 1\n\t" \
  "v_mfma_f32_16x16x32_bf16 v[160:163], v[68:71], v[132:135], v[160:163]\n\t" \
  "ds_read_b128 v[144:147], v57 offset:6144\n\t" \
  "v_and_b32 v37, v30, v37\n\t" \
  "v_mfma_f32_16x16x32_bf16 v[168:171], v[68:71], v[108:111], v[168:171]\n\t" \
  "ds_read_b128 v[72:75], v57 offset:8192\n\t" \
  "v_bfe_i32 v41, v3, 7, 1\n\t" \
  "v_mfma_f32_16x16x32_bf16 v[168:171], v[116:119], v[148:151], v[168:171]\n\t" \
  "ds_read_b128 v[140:143], v56 offset:8192\n\t" \
  "v_and_b32 v41, v30, v41\n\t" \
  "v_mfma_f32_16x16x32_bf16 v[176:179], v[116:119], v[64:67], v[176:179]\n\t" \
  "ds_read_b64_tr_b16 v[116:117], v59 offset:34816\n\t" \
  "v_bfe_i32 v45, v3, 11, 1\n\t" \
  "v_mfma_f32_16x16x32_bf16 v[176:179], v[68:71], v[104:107], v[176:179]\n\t" \
  "ds_read_b64_tr_b16 v[118:119], v59 offset:35328\n\t" \
  "v_bfi_b32 v45, v45, v30, %15\n\t" \
  "v_mfma_f32_16x16x32_bf16 v[156:159], v[76:79], v[88:91], v[156:159]\n\t" \
  "ds_read_b64_tr_b16 v[68:69], v59 offset:38912\n\t" \
  "v_mov_b32 v51, 0x3f803f80\n\t" \
  "v_mfma_f32_16x16x32_bf16 v[156:159], v[84:87], v[124:127], v[156:159]\n\t" \
  "ds_read_b64_tr_b16 v[70:71], v59 offset:39424\n\t" \
  "v_bfe_i32 v46, v3, 12, 1\n\t" \
  "v_mfma_f32_16x16x32_bf16 v[164:167], v[84:87], v[96:99], v[164:167]\n\t" \
  "ds_read_b64_tr_b16 v[88:89], v58 offset:26624\n\t" \
  "v_bfe_i32 v31, v3, 13, 1\n\t" \
  "v_mfma_f32_16x16x32_bf16 v[164:167], v[76:79], v[132:135], v[164:167]\n\t" \
  "ds_read_b64_tr_b16 v[90:91], v58 offset:27136\n\t" \
  "v_perm_b32 v46, v31, v46, vcc_hi\n\t" \
  "v_mfma_f32_16x16x32_bf16 v[172:175], v[76:79], v[108:111], v[172:175]\n\t" \
  "ds_read_b64_tr_b16 v[124:125], v58 offset:30720\n\t" \
  "v_bfe_i32 v47, v3, 14, 1\n\t" \
  "v_mfma_f32_16x16x32_bf16 v[172:175], v[84:87], v[148:151], v[172:175]\n\t" \
  "ds_read_b64_tr_b16 v[126:127], v58 offset:31232\n\t" \
  "v_bfe_i32 v31, v3, 15, 1\n\t" \
  "v_mfma_f32_16x16x32_bf16 v[180:183], v[84:87], v[64:67], v[180:183]\n\t" \
  "ds_read_b128 v[96:99], v56 offset:0\n\t" \
  "v_perm_b32 v47, v31, v47, vcc_hi\n\t" \
  "v_mfma_f32_16x16x32_bf16 v[180:183], v[76:79], v[104:107], v[180:183]\n\t" \
  "ds_read_b128 v[132:135], v57 offset:0\n\t" \
  "v_add_u32 v30, 18432, v4\n\t" \
  "s_waitcnt lgkmcnt(7)\n\t" \
  "v_mfma_f32_16x16x32_bf16 v[216:219], v[100:103], v[80:83], v[216:219]\n\t" \
  "ds_read_b64_tr_b16 v[108:109], v59 offset:26624\n\t" \
  "v_xor_b32 v31, 1024, v30\n\t" \
  "v_mfma_f32_16x16x32_bf16 v[216:219], v[112:115], v[92:95], v[216:219]\n\t" \
  "ds_read_b64_tr_b16 v[110:111], v59 offset:27136\n\t" \
  "v_mov_b32 v32, %12\n\t" \
  "v_mfma_f32_16x16x32_bf16 v[224:227], v[128:131], v[92:95], v[224:227]\n\t" \
  "ds_read_b64_tr_b16 v[148:149], v59 offset:30720\n\t" \
  "v_xor_b32 v33, 4, v32\n\t" \
  "v_mfma_f32_16x16x32_bf16 v[224:227], v[120:123], v[80:83], v[224:227]\n\t" \
  "ds_read_b64_tr_b16 v[150:151], v59 offset:31232\n\t" \
  "v_mov_b32 v0, 1\n\t" \
  "v_mfma_f32_16x16x32_bf16 v[232:235], v[136:139], v[80:83], v[232:235]\n\t" \
  "ds_read_b64_tr_b16 v[64:65], v58 offset:18432\n\t" \
  "v_and_b32 v4, 1023, v4\n\t" \
  "v_mfma_f32_16x16x32_bf16 v[232:235], v[144:147], v[92:95], v[232:235]\n\t" \
  "ds_read_b64_tr_b16 v[66:67], v58 offset:18944\n\t" \
  "v_add_u32 v1, 16384, v4\n\t" \
  "v_mfma_f32_16x16x32_bf16 v[240:243], v[72:75], v[92:95], v[240:243]\n\t" \
  "ds_read_b64_tr_b16 v[84:85], v58 offset:22528\n\t" \
  "v_mfma_f32_16x16x32_bf16 v[240:243], v[140:143], v[80:83], v[240:243]\n\t" \
  "ds_read_b64_tr_b16 v[86:87], v58 offset:23040\n\t" \
  "v_mfma_f32_16x16x32_bf16 v[220:223], v[100:103], v[116:119], v[220:223]\n\t" \
  "ds_read_b64_tr_b16 v[76:77], v59 offset:18432\n\t" \
  "s_waitcnt lgkmcnt(7)\n\t" \
  "v_mfma_f32_16x16x32_bf16 v[220:223], v[112:115], v[68:71], v[220:223]\n\t" \
  "ds_read_b64_tr_b16 v[78:79], v59 offset:18944\n\t" \
  "v_mfma_f32_16x16x32_bf16 v[228:231], v[128:131], v[68:71], v[228:231]\n\t" \
  "ds_read_b64_tr_b16 v[104:105], v59 offset:22528\n\t" \
  "v_mfma_f32_16x16x32_bf16 v[228:231], v[120:123], v[116:119], v[228:231]\n\t" \
  "ds_read_b64_tr_b16 v[106:107], v59 offset:23040\n\t" \
  "v_mfma_f32_16x16x32_bf16 v[236:239], v[136:139], v[116:119], v[236:239]\n\t" \
  "v_mfma_f32_16x16x32_bf16 v[236:239], v[144:147], v[68:71], v[236:239]\n\t" \
  "v_mfma_f32_16x16x32_bf16 v[244:247], v[72:75], v[68:71], v[244:247]\n\t" \
  "v_mfma_f32_16x16x32_bf16 v[244:247], v[140:143], v[116:119], v[244:247]\n\t" \
  "v_mfma_f32_16x16x32_bf16 v[184:187], v[88:91], v[100:103], v[184:187]\n\t" \
  "v_mfma_f32_16x16x32_bf16 v[184:187], v[124:127], v[112:115], v[184:187]\n\t" \
  "v_mfma_f32_16x16x32_bf16 v[192:195], v[124:127], v[128:131], v[192:195]\n\t" \
  "v_mfma_f32_16x16x32_bf16 v[192:195], v[88:91], v[120:123], v[192:195]\n\t" \
  "v_mfma_f32_16x16x32_bf16 v[200:203], v[88:91], v[136:139], v[200:203]\n\t" \
  "v_mfma_f32_16x16x32_bf16 v[200:203], v[124:127], v[144:147], v[200:203]\n\t" \
  "v_mfma_f32_16x16x32_bf16 v[208:211], v[124:127], v[72:75], v[208:211]\n\t" \
  "v_mfma_f32_16x16x32_bf16 v[208:211], v[88:91], v[140:143], v[208:211]\n\t" \
  "v_mfma_f32_16x16x32_bf16 v[248:251], v[88:91], v[96:99], v[248:251]\n\t" \
  "v_mfma_f32_16x16x32_bf16 v[248:251], v[124:127], v[132:135], v[248:251]\n\t" \
  "v_mfma_f32_16x16x32_bf16 v[252:255], v[108:111], v[96:99], v[252:255]\n\t" \
  "s_waitcnt lgkmcnt(0)\n\t" \
  "v_mfma_f32_16x16x32_bf16 v[252:255], v[148:151], v[132:135], v[252:255]\n\t" \
  "v_mfma_f32_16x16x32_bf16 v[188:191], v[148:151], v[112:115], v[188:191]\n\t" \
  "v_mfma_f32_16x16x32_bf16 v[188:191], v[108:111], v[100:103], v[188:191]\n\t" \
  "s_barrier\n\t" \
  "v_mfma_f32_16x16x32_bf16 v[196:199], v[108:111], v[120:123], v[196:199]\n\t" \
  "ds_read_b64_tr_b16 v[92:93], v62 offset:34816\n\t" \
  "v_mfma_f32_16x16x32_bf16 v[196:199], v[148:151], v[128:131], v[196:199]\n\t" \
  "ds_read_b64_tr_b16 v[94:95], v62 offset:35328\n\t" \
  "v_mfma_f32_16x16x32_bf16 v[204:207], v[148:151], v[144:147], v[204:207]\n\t" \
  "ds_read_b128 v[80:83], v60 offset:2048\n\t" \
  "v_mfma_f32_16x16x32_bf16 v[204:207], v[108:111], v[136:139], v[204:207]\n\t" \
  "ds_read_b64_tr_b16 v[68:69], v62 offset:38912\n\t" \
  "v_mfma_f32_16x16x32_bf16 v[212:215], v[108:111], v[140:143], v[212:215]\n\t" \
  "ds_read_b64_tr_b16 v[70:71], v62 offset:39424\n\t" \
  "v_mfma_f32_16x16x32_bf16 v[212:215], v[148:151], v[72:75], v[212:215]\n\t" \
  "ds_read_b128 v[116:119], v61 offset:2048\n\t" \
  "v_mfma_f32_16x16x32_bf16 v[152:155], v[64:67], v[100:103], v[152:155]\n\t" \
  "ds_read_b128 v[88:91], v61 offset:4096\n\t" \
  "v_mfma_f32_16x16x32_bf16 v[152:155], v[84:87], v[112:115], v[152:155]\n\t" \
  "ds_read_b128 v[124:127], v60 offset:4096\n\t" \
  "v_mfma_f32_16x16x32_bf16 v[160:163], v[84:87], v[128:131], v[160:163]\n\t" \
  "ds_read_b128 v[96:99], v60 offset:6144\n\t" \
  "v_mfma_f32_16x16x32_bf16 v[160:163], v[64:67], v[120:123], v[160:163]\n\t" \
  "ds_read_b128 v[132:135], v61 offset:6144\n\t" \
  "v_mfma_f32_16x16x32_bf16 v[168:171], v[64:67], v[136:139], v[168:171]\n\t" \
  "ds_read_b128 v[108:111], v61 offset:8192\n\t" \
  "v_mfma_f32_16x16x32_bf16 v[168:171], v[84:87], v[144:147], v[168:171]\n\t" \
  "ds_read_b128 v[148:151], v60 offset:8192\n\t" \
  "v_mfma_f32_16x16x32_bf16 v[176:179], v[84:87], v[72:75], v[176:179]\n\t" \
  "ds_read_b64_tr_b16 v[84:85], v63 offset:34816\n\t" \
  "v_mfma_f32_16x16x32_bf16 v[176:179], v[64:67], v[140:143], v[176:179]\n\t" \
  "ds_read_b64_tr_b16 v[86:87], v63 offset:35328\n\t" \
  "v_mfma_f32_16x16x32_bf16 v[156:159], v[76:79], v[100:103], v[156:159]\n\t" \
  "ds_read_b64_tr_b16 v[64:65], v63 offset:38912\n\t" \
  "v_mfma_f32_16x16x32_bf16 v[156:159], v[104:107], v[112:115], v[156:159]\n\t" \
  "ds_read_b64_tr_b16 v[66:67], v63 offset:39424\n\t" \
  "v_mfma_f32_16x16x32_bf16 v[164:167], v[104:107], v[128:131], v[164:167]\n\t" \
  "ds_read_b64_tr_b16 v[100:101], v62 offset:26624\n\t" \
  "v_mfma_f32_16x16x32_bf16 v[164:167], v[76:79], v[120:123], v[164:167]\n\t" \
  "ds_read_b64_tr_b16 v[102:103], v62 offset:27136\n\t" \
  "v_mfma_f32_16x16x32_bf16 v[172:175], v[76:79], v[136:139], v[172:175]\n\t" \
  "ds_read_b64_tr_b16 v[112:113], v62 offset:30720\n\t" \
  "v_mfma_f32_16x16x32_bf16 v[172:175], v[104:107], v[144:147], v[172:175]\n\t" \
  "ds_read_b64_tr_b16 v[114:115], v62 offset:31232\n\t" \
  "v_mfma_f32_16x16x32_bf16 v[180:183], v[104:107], v[72:75], v[180:183]\n\t" \
  "ds_read_b128 v[128:131], v60 offset:0\n\t" \
  "v_mfma_f32_16x16x32_bf16 v[180:183], v[76:79], v[140:143], v[180:183]\n\t" \
  "ds_read_b128 v[120:123], v61 offset:0\n\t" \
  "s_waitcnt lgkmcnt(7)\n\t" \
  "v_mfma_f32_16x16x32_bf16 v[216:219], v[80:83], v[92:95], v[216:219]\n\t" \
  "ds_read_b64_tr_b16 v[136:137], v63 offset:26624\n\t" \
  "v_mfma_f32_16x16x32_bf16 v[216:219], v[116:119], v[68:71], v[216:219]\n\t" \
  "ds_read_b64_tr_b16 v[138:139], v63 offset:27136\n\t" \
  "v_mfma_f32_16x16x32_bf16 v[224:227], v[88:91], v[68:71], v[224:227]\n\t" \
  "ds_read_b64_tr_b16 v[144:145], v63 offset:30720\n\t" \
  "v_mfma_f32_16x16x32_bf16 v[224:227], v[124:127], v[92:95], v[224:227]\n\t" \
  "ds_read_b64_tr_b16 v[146:147], v63 offset:31232\n\t" \
  "v_mfma_f32_16x16x32_bf16 v[232:235], v[96:99], v[92:95], v[232:235]\n\t" \
  "ds_read_b64_tr_b16 v[72:73], v62 offset:18432\n\t" \
  "v_mfma_f32_16x16x32_bf16 v[232:235], v[132:135], v[68:71], v[232:235]\n\t" \
  "ds_read_b64_tr_b16 v[74:75], v62 offset:18944\n\t" \
  "v_mfma_f32_16x16x32_bf16 v[240:243], v[108:111], v[68:71], v[240:243]\n\t" \
  "ds_read_b64_tr_b16 v[104:105], v62 offset:22528\n\t" \
  "v_mfma_f32_16x16x32_bf16 v[240:243], v[148:151], v[92:95], v[240:243]\n\t" \
  "ds_read_b64_tr_b16 v[106:107], v62 offset:23040\n\t" \
  "v_mfma_f32_16x16x32_bf16 v[220:223], v[80:83], v[84:87], v[220:223]\n\t" \
  "ds_read_b64_tr_b16 v[76:77], v63 offset:18432\n\t" \
  "s_waitcnt lgkmcnt(7)\n\t" \
  "v_mfma_f32_16x16x32_bf16 v[220:223], v[116:119], v[64:67], v[220:223]\n\t" \
  "ds_read_b64_tr_b16 v[78:79], v63 offset:18944\n\t" \
  "v_mfma_f32_16x16x32_bf16 v[228:231], v[88:91], v[64:67], v[228:231]\n\t" \
  "ds_read_b64_tr_b16 v[140:141], v63 offset:22528\n\t" \
  "v_mfma_f32_16x16x32_bf16 v[228:231], v[124:127], v[84:87], v[228:231]\n\t" \
  "ds_read_b64_tr_b16 v[142:143], v63 offset:23040\n\t" \
  "v_mfma_f32_16x16x32_bf16 v[236:239], v[96:99], v[84:87], v[236:239]\n\t" \
  "v_mfma_f32_16x16x32_bf16 v[236:239], v[132:135], v[64:67], v[236:239]\n\t" \
  "v_mfma_f32_16x16x32_bf16 v[244:247], v[108:111], v[64:67], v[244:247]\n\t" \
  "v_mfma_f32_16x16x32_bf16 v[244:247], v[148:151], v[84:87], v[244:247]\n\t" \
  "v_mfma_f32_16x16x32_bf16 v[184:187], v[100:103], v[80:83], v[184:187]\n\t" \
  "v_mfma_f32_16x16x32_bf16 v[184:187], v[112:115], v[116:119], v[184:187]\n\t" \
  "v_mfma_f32_16x16x32_bf16 v[192:195], v[112:115], v[88:91], v[192:195]\n\t" \
  "v_mfma_f32_16x16x32_bf16 v[192:195], v[100:103], v[124:127], v[192:195]\n\t" \
  "v_mfma_f32_16x16x32_bf16 v[200:203], v[100:103], v[96:99], v[200:203]\n\t" \
  "v_mfma_f32_16x16x32_bf16 v[200:203], v[112:115], v[132:135], v[200:203]\n\t" \
  "v_mfma_f32_16x16x32_bf16 v[208:211], v[112:115], v[108:111], v[208:211]\n\t" \
  "v_mfma_f32_16x16x32_bf16 v[208:211], v[100:103], v[148:151], v[208:211]\n\t" \
  "v_mfma_f32_16x16x32_bf16 v[248:251], v[100:103], v[128:131], v[248:251]\n\t" \
  "v_mfma_f32_16x16x32_bf16 v[248:251], v[112:115], v[120:123], v[248:251]\n\t" \
  "v_mfma_f32_16x16x32_bf16 v[252:255], v[136:139], v[128:131], v[252:255]\n\t" \
  "s_waitcnt lgkmcnt(0)\n\t" \
  "v_mfma_f32_16x16x32_bf16 v[252:255], v[144:147], v[120:123], v[252:255]\n\t" \
  "v_mfma_f32_16x16x32_bf16 v[188:191], v[144:147], v[116:119], v[188:191]\n\t" \
  "v_mfma_f32_16x16x32_bf16 v[188:191], v[136:139], v[80:83], v[188:191]\n\t" \
  "s_waitcnt vmcnt(0)\n\t" \
  "s_barrier\n\t" \
  "v_mfma_f32_16x16x32_bf16 v[196:199], v[136:139], v[124:127], v[196:199]\n\t" \
  "ds_read_b64_tr_b16 v[68:69], v54 offset:34816\n\t" \
  "v_mfma_f32_16x16x32_bf16 v[196:199], v[144:147], v[88:91], v[196:199]\n\t" \
  "ds_read_b64_tr_b16 v[70:71], v54 offset:35328\n\t" \
  "v_mfma_f32_16x16x32_bf16 v[204:207], v[144:147], v[132:135], v[204:207]\n\t" \
  "ds_read_b128 v[92:95], v52 offset:2048\n\t" \
  "v_mfma_f32_16x16x32_bf16 v[204:207], v[136:139], v[96:99], v[204:207]\n\t" \
  "ds_read_b64_tr_b16 v[64:65], v54 offset:38912\n\t" \
  "v_mfma_f32_16x16x32_bf16 v[212:215], v[136:139], v[148:151], v[212:215]\n\t" \
  "ds_read_b64_tr_b16 v[66:67], v54 offset:39424\n\t" \
  "v_mfma_f32_16x16x32_bf16 v[212:215], v[144:147], v[108:111], v[212:215]\n\t" \
  "ds_read_b128 v[84:87], v53 offset:2048\n\t" \
  "v_mfma_f32_16x16x32_bf16 v[152:155], v[72:75], v[80:83], v[152:155]\n\t" \
  "ds_read_b128 v[100:103], v53 offset:4096\n\t" \
  "v_mfma_f32_16x16x32_bf16 v[152:155], v[104:107], v[116:119], v[152:155]\n\t" \
  "ds_read_b128 v[112:115], v52 offset:4096\n\t" \
  "v_mfma_f32_16x16x32_bf16 v[160:163], v[104:107], v[88:91], v[160:163]\n\t" \
  "ds_read_b128 v[128:131], v52 offset:6144\n\t" \
  "v_mfma_f32_16x16x32_bf16 v[160:163], v[72:75], v[124:127], v[160:163]\n\t" \
  "ds_read_b128 v[120:123], v53 offset:6144\n\t" \
  "v_mfma_f32_16x16x32_bf16 v[168:171], v[72:75], v[96:99], v[168:171]\n\t" \
  "ds_read_b128 v[136:139], v53 offset:8192\n\t" \
  "v_mfma_f32_16x16x32_bf16 v[168:171], v[104:107], v[132:135], v[168:171]\n\t" \
  "ds_read_b128 v[144:147], v52 offset:8192\n\t" \
  "v_mfma_f32_16x16x32_bf16 v[176:179], v[104:107], v[108:111], v[176:179]\n\t" \
  "ds_read_b64_tr_b16 v[104:105], v55 offset:34816\n\t" \
  "v_mfma_f32_16x16x32_bf16 v[176:179], v[72:75], v[148:151], v[176:179]\n\t" \
  "ds_read_b64_tr_b16 v[106:107], v55 offset:35328\n\t" \
  "v_mfma_f32_16x16x32_bf16 v[156:159], v[76:79], v[80:83], v[156:159]\n\t" \
  "ds_read_b64_tr_b16 v[72:73], v55 offset:38912\n\t" \
  "v_mfma_f32_16x16x32_bf16 v[156:159], v[140:143], v[116:119], v[156:159]\n\t" \
  "ds_read_b64_tr_b16 v[74:75], v55 offset:39424\n\t" \
  "v_mfma_f32_16x16x32_bf16 v[164:167], v[140:143], v[88:91], v[164:167]\n\t" \
  "ds_read_b64_tr_b16 v[80:81], v54 offset:26624\n\t" \
  "v_mfma_f32_16x16x32_bf16 v[164:167], v[76:79], v[124:127], v[164:167]\n\t" \
  "ds_read_b64_tr_b16 v[82:83], v54 offset:27136\n\t" \
  "v_mfma_f32_16x16x32_bf16 v[172:175], v[76:79], v[96:99], v[172:175]\n\t" \
  "ds_read_b64_tr_b16 v[116:117], v54 offset:30720\n\t" \
  "v_mfma_f32_16x16x32_bf16 v[172:175], v[140:143], v[132:135], v[172:175]\n\t" \
  "ds_read_b64_tr_b16 v[118:119], v54 offset:31232\n\t" \
  "v_mfma_f32_16x16x32_bf16 v[180:183], v[140:143], v[108:111], v[180:183]\n\t" \
  "ds_read_b128 v[88:91], v52 offset:0\n\t" \
  "v_mfma_f32_16x16x32_bf16 v[180:183], v[76:79], v[148:151], v[180:183]\n\t" \
  "ds_read_b128 v[124:127], v53 offset:0\n\t" \
  "s_waitcnt lgkmcnt(7)\n\t" \
  "v_mfma_f32_16x16x32_bf16 v[216:219], v[92:95], v[68:71], v[216:219]\n\t" \
  "ds_read_b64_tr_b16 v[96:97], v55 offset:26624\n\t" \
  "v_mfma_f32_16x16x32_bf16 v[216:219], v[84:87], v[64:67], v[216:219]\n\t" \
  "ds_read_b64_tr_b16 v[98:99], v55 offset:27136\n\t" \
  "v_mfma_f32_16x16x32_bf16 v[224:227], v[100:103], v[64:67], v[224:227]\n\t" \
  "ds_read_b64_tr_b16 v[132:133], v55 offset:30720\n\t" \
  "v_mfma_f32_16x16x32_bf16 v[224:227], v[112:115], v[68:71], v[224:227]\n\t" \
  "ds_read_b64_tr_b16 v[134:135], v55 offset:31232\n\t" \
  "v_mfma_f32_16x16x32_bf16 v[232:235], v[128:131], v[68:71], v[232:235]\n\t" \
  "ds_read_b64_tr_b16 v[108:109], v54 offset:18432\n\t" \
  "v_mfma_f32_16x16x32_bf16 v[232:235], v[120:123], v[64:67], v[232:235]\n\t" \
  "ds_read_b64_tr_b16 v[110:111], v54 offset:18944\n\t" \
  "v_mfma_f32_16x16x32_bf16 v[240:243], v[136:139], v[64:67], v[240:243]\n\t" \
  "ds_read_b64_tr_b16 v[140:141], v54 offset:22528\n\t" \
  "v_mfma_f32_16x16x32_bf16 v[240:243], v[144:147], v[68:71], v[240:243]\n\t" \
  "ds_read_b64_tr_b16 v[142:143], v54 offset:23040\n\t" \
  "v_mfma_f32_16x16x32_bf16 v[220:223], v[92:95], v[104:107], v[220:223]\n\t" \
  "ds_read_b64_tr_b16 v[76:77], v55 offset:18432\n\t" \
  "s_waitcnt lgkmcnt(7)\n\t" \
  "v_mfma_f32_16x16x32_bf16 v[220:223], v[84:87], v[72:75], v[220:223]\n\t" \
  "ds_read_b64_tr_b16 v[78:79], v55 offset:18944\n\t" \
  "v_mfma_f32_16x16x32_bf16 v[228:231], v[100:103], v[72:75], v[228:231]\n\t" \
  "ds_read_b64_tr_b16 v[148:149], v55 offset:22528\n\t" \
  "v_mfma_f32_16x16x32_bf16 v[228:231], v[112:115], v[104:107], v[228:231]\n\t" \
  "ds_read_b64_tr_b16 v[150:151], v55 offset:23040\n\t" \
  "v_mfma_f32_16x16x32_bf16 v[236:239], v[128:131], v[104:107], v[236:239]\n\t" \
  "v_mfma_f32_16x16x32_bf16 v[236:239], v[120:123], v[72:75], v[236:239]\n\t" \
  "v_mfma_f32_16x16x32_bf16 v[244:247], v[136:139], v[72:75], v[244:247]\n\t" \
  "v_mfma_f32_16x16x32_bf16 v[244:247], v[144:147], v[104:107], v[244:247]\n\t" \
  "v_mfma_f32_16x16x32_bf16 v[184:187], v[80:83], v[92:95], v[184:187]\n\t" \
  "ds_read_b32 v10, v32 offset:104\n\t" \
  "v_mfma_f32_16x16x32_bf16 v[184:187], v[116:119], v[84:87], v[184:187]\n\t" \
  "v_lshlrev_b32 v11, 3, v4\n\t" \
  "v_mfma_f32_16x16x32_bf16 v[192:195], v[116:119], v[100:103], v[192:195]\n\t" \
  "v_mfma_f32_16x16x32_bf16 v[192:195], v[80:83], v[112:115], v[192:195]\n\t" \
  "v_mfma_f32_16x16x32_bf16 v[200:203], v[80:83], v[128:131], v[200:203]\n\t" \
  "s_waitcnt lgkmcnt(0)\n\t" \
  "s_cmp_lg_u32 %13, 0\n\t" \
  "s_cbranch_scc1 27f\n\t" \
  "s_mov_b32 exec_hi, 0\n\t" \
  "global_store_dword v11, v10, %25 sc1\n\t" \
  "s_mov_b32 exec_hi, -1\n\t" \
  "27:\n\t" \
  "v_mfma_f32_16x16x32_bf16 v[200:203], v[116:119], v[120:123], v[200:203]\n\t" \
  "v_mfma_f32_16x16x32_bf16 v[208:211], v[116:119], v[136:139], v[208:211]\n\t" \
  "v_mfma_f32_16x16x32_bf16 v[208:211], v[80:83], v[144:147], v[208:211]\n\t" \
  "v_mfma_f32_16x16x32_bf16 v[248:251], v[80:83], v[88:91], v[248:251]\n\t" \
  "v_mfma_f32_16x16x32_bf16 v[248:251], v[116:119], v[124:127], v[248:251]\n\t" \
  "v_mfma_f32_16x16x32_bf16 v[252:255], v[96:99], v[88:91], v[252:255]\n\t" \
  "v_mfma_f32_16x16x32_bf16 v[252:255], v[132:135], v[124:127], v[252:255]\n\t" \
  "v_mfma_f32_16x16x32_bf16 v[188:191], v[132:135], v[84:87], v[188:191]\n\t" \
  "v_mfma_f32_16x16x32_bf16 v[188:191], v[96:99], v[92:95], v[188:191]\n\t" \
  "s_barrier\n\t" \
  "v_mfma_f32_16x16x32_bf16 v[196:199], v[96:99], v[112:115], v[196:199]\n\t" \
  "ds_read_b64_tr_b16 v[64:65], v58 offset:34816\n\t" \
  "v_mfma_f32_16x16x32_bf16 v[196:199], v[132:135], v[100:103], v[196:199]\n\t" \
  "ds_read_b64_tr_b16 v[66:67], v58 offset:35328\n\t" \
  "v_mfma_f32_16x16x32_bf16 v[204:207], v[132:135], v[120:123], v[204:207]\n\t" \
  "ds_read_b128 v[68:71], v56 offset:2048\n\t" \
  "v_mfma_f32_16x16x32_bf16 v[204:207], v[96:99], v[128:131], v[204:207]\n\t" \
  "ds_read_b64_tr_b16 v[72:73], v58 offset:38912\n\t" \
  "v_mfma_f32_16x16x32_bf16 v[212:215], v[96:99], v[144:147], v[212:215]\n\t" \
  "ds_read_b64_tr_b16 v[74:75], v58 offset:39424\n\t" \
  "v_mfma_f32_16x16x32_bf16 v[212:215], v[132:135], v[136:139], v[212:215]\n\t" \
  "ds_read_b128 v[104:107], v57 offset:2048\n\t" \
  "v_mfma_f32_16x16x32_bf16 v[152:155], v[108:111], v[92:95], v[152:155]\n\t" \
  "ds_read_b128 v[80:83], v57 offset:4096\n\t" \
  "v_mfma_f32_16x16x32_bf16 v[152:155], v[140:143], v[84:87], v[152:155]\n\t" \
  "ds_read_b128 v[116:119], v56 offset:4096\n\t" \
  "v_mfma_f32_16x16x32_bf16 v[160:163], v[140:143], v[100:103], v[160:163]\n\t" \
  "ds_read_b128 v[88:91], v56 offset:6144\n\t" \
  "v_mfma_f32_16x16x32_bf16 v[160:163], v[108:111], v[112:115], v[160:163]\n\t" \
  "ds_read_b128 v[124:127], v57 offset:6144\n\t" \
  "v_mfma_f32_16x16x32_bf16 v[168:171], v[108:111], v[128:131], v[168:171]\n\t" \
  "ds_read_b128 v[96:99], v57 offset:8192\n\t" \
  "v_mfma_f32_16x16x32_bf16 v[168:171], v[140:143], v[120:123], v[168:171]\n\t" \
  "ds_read_b128 v[132:135], v56 offset:8192\n\t" \
  "v_mfma_f32_16x16x32_bf16 v[176:179], v[140:143], v[136:139], v[176:179]\n\t" \
  "ds_read_b64_tr_b16 v[140:141], v59 offset:34816\n\t" \
  "v_mfma_f32_16x16x32_bf16 v[176:179], v[108:111], v[144:147], v[176:179]\n\t" \
  "ds_read_b64_tr_b16 v[142:143], v59 offset:35328\n\t" \
  "v_mfma_f32_16x16x32_bf16 v[156:159], v[76:79], v[92:95], v[156:159]\n\t" \
  "ds_read_b64_tr_b16 v[108:109], v59 offset:38912\n\t" \
  "v_mfma_f32_16x16x32_bf16 v[156:159], v[148:151], v[84:87], v[156:159]\n\t" \
  "ds_read_b64_tr_b16 v[110:111], v59 offset:39424\n\t" \
  "v_mfma_f32_16x16x32_bf16 v[164:167], v[148:151], v[100:103], v[164:167]\n\t" \
  "ds_read_b64_tr_b16 v[92:93], v58 offset:26624\n\t" \
  "v_mfma_f32_16x16x32_bf16 v[164:167], v[76:79], v[112:115], v[164:167]\n\t" \
  "ds_read_b64_tr_b16 v[94:95], v58 offset:27136\n\t" \
  "v_mfma_f32_16x16x32_bf16 v[172:175], v[76:79], v[128:131], v[172:175]\n\t" \
  "ds_read_b64_tr_b16 v[84:85], v58 offset:30720\n\t" \
  "v_mfma_f32_16x16x32_bf16 v[172:175], v[148:151], v[120:123], v[172:175]\n\t" \
  "ds_read_b64_tr_b16 v[86:87], v58 offset:31232\n\t" \
  "v_mfma_f32_16x16x32_bf16 v[180:183], v[148:151], v[136:139], v[180:183]\n\t" \
  "ds_read_b128 v[100:103], v56 offset:0\n\t" \
  "v_mfma_f32_16x16x32_bf16 v[180:183], v[76:79], v[144:147], v[180:183]\n\t" \
  "ds_read_b128 v[112:115], v57 offset:0\n\t" \
  "s_waitcnt lgkmcnt(7)\n\t" \
  "v_mfma_f32_16x16x32_bf16 v[216:219], v[68:71], v[64:67], v[216:219]\n\t" \
  "ds_read_b64_tr_b16 v[128:129], v59 offset:26624\n\t" \
  "v_mfma_f32_16x16x32_bf16 v[216:219], v[104:107], v[72:75], v[216:219]\n\t" \
  "ds_read_b64_tr_b16 v[130:131], v59 offset:27136\n\t" \
  "v_mfma_f32_16x16x32_bf16 v[224:227], v[80:83], v[72:75], v[224:227]\n\t" \
  "ds_read_b64_tr_b16 v[120:121], v59 offset:30720\n\t" \
  "v_mfma_f32_16x16x32_bf16 v[224:227], v[116:119], v[64:67], v[224:227]\n\t" \
  "ds_read_b64_tr_b16 v[122:123], v59 offset:31232\n\t" \
  "v_mfma_f32_16x16x32_bf16 v[232:235], v[88:91], v[64:67], v[232:235]\n\t" \
  "ds_read_b64_tr_b16 v[136:137], v58 offset:18432\n\t" \
  "v_mfma_f32_16x16x32_bf16 v[232:235], v[124:127], v[72:75], v[232:235]\n\t" \
  "ds_read_b64_tr_b16 v[138:139], v58 offset:18944\n\t" \
  "v_mfma_f32_16x16x32_bf16 v[240:243], v[96:99], v[72:75], v[240:243]\n\t" \
  "ds_read_b64_tr_b16 v[148:149], v58 offset:22528\n\t" \
  "v_mfma_f32_16x16x32_bf16 v[240:243], v[132:135], v[64:67], v[240:243]\n\t" \
  "ds_read_b64_tr_b16 v[150:151], v58 offset:23040\n\t" \
  "v_mfma_f32_16x16x32_bf16 v[220:223], v[68:71], v[140:143], v[220:223]\n\t" \
  "ds_read_b64_tr_b16 v[76:77], v59 offset:18432\n\t" \
  "s_waitcnt lgkmcnt(7)\n\t" \
  "v_mfma_f32_16x16x32_bf16 v[220:223], v[104:107], v[108:111], v[220:223]\n\t" \
  "ds_read_b64_tr_b16 v[78:79], v59 offset:18944\n\t" \
  "v_mfma_f32_16x16x32_bf16 v[228:231], v[80:83], v[108:111], v[228:231]\n\t" \
  "ds_read_b64_tr_b16 v[144:145], v59 offset:22528\n\t" \
  "v_mfma_f32_16x16x32_bf16 v[228:231], v[116:119], v[140:143], v[228:231]\n\t" \
  "ds_read_b64_tr_b16 v[146:147], v59 offset:23040\n\t" \
  "v_mfma_f32_16x16x32_bf16 v[236:239], v[88:91], v[140:143], v[236:239]\n\t" \
  "v_mfma_f32_16x16x32_bf16 v[236:239], v[124:127], v[108:111], v[236:239]\n\t" \
  "v_mfma_f32_16x16x32_bf16 v[244:247], v[96:99], v[108:111], v[244:247]\n\t" \
  "v_mfma_f32_16x16x32_bf16 v[244:247], v[132:135], v[140:143], v[244:247]\n\t" \
  "v_mfma_f32_16x16x32_bf16 v[184:187], v[92:95], v[68:71], v[184:187]\n\t" \
  "v_mfma_f32_16x16x32_bf16 v[184:187], v[84:87], v[104:107], v[184:187]\n\t" \
  "v_mfma_f32_16x16x32_bf16 v[192:195], v[84:87], v[80:83], v[192:195]\n\t" \
  "v_mfma_f32_16x16x32_bf16 v[192:195], v[92:95], v[116:119], v[192:195]\n\t" \
  "v_mfma_f32_16x16x32_bf16 v[200:203], v[92:95], v[88:91], v[200:203]\n\t" \
  "v_mfma_f32_16x16x32_bf16 v[200:203], v[84:87], v[124:127], v[200:203]\n\t" \
  "v_mfma_f32_16x16x32_bf16 v[208:211], v[84:87], v[96:99], v[208:211]\n\t" \
  "v_mfma_f32_16x16x32_bf16 v[208:211], v[92:95], v[132:135], v[208:211]\n\t" \
  "v_mfma_f32_16x16x32_bf16 v[248:251], v[92:95], v[100:103], v[248:251]\n\t" \
  "v_mfma_f32_16x16x32_bf16 v[248:251], v[84:87], v[112:115], v[248:251]\n\t" \
  "v_mfma_f32_16x16x32_bf16 v[252:255], v[128:131], v[100:103], v[252:255]\n\t" \
  "s_waitcnt lgkmcnt(0)\n\t" \
  "v_mfma_f32_16x16x32_bf16 v[252:255], v[120:123], v[112:115], v[252:255]\n\t" \
  "v_mfma_f32_16x16x32_bf16 v[188:191], v[120:123], v[104:107], v[188:191]\n\t" \
  "v_mfma_f32_16x16x32_bf16 v[188:191], v[128:131], v[68:71], v[188:191]\n\t" \
  "s_barrier\n\t" \
  "v_mfma_f32_16x16x32_bf16 v[196:199], v[128:131], v[116:119], v[196:199]\n\t" \
  "ds_read_b64_tr_b16 v[72:73], v62 offset:34816\n\t" \
  "v_mfma_f32_16x16x32_bf16 v[196:199], v[120:123], v[80:83], v[196:199]\n\t" \
  "ds_read_b64_tr_b16 v[74:75], v62 offset:35328\n\t" \
  "v_mfma_f32_16x16x32_bf16 v[204:207], v[120:123], v[124:127], v[204:207]\n\t" \
  "ds_read_b128 v[64:67], v60 offset:2048\n\t" \
  "v_mfma_f32_16x16x32_bf16 v[204:207], v[128:131], v[88:91], v[204:207]\n\t" \
  "ds_read_b64_tr_b16 v[108:109], v62 offset:38912\n\t" \
  "v_mfma_f32_16x16x32_bf16 v[212:215], v[128:131], v[132:135], v[212:215]\n\t" \
  "ds_read_b64_tr_b16 v[110:111], v62 offset:39424\n\t" \
  "v_mfma_f32_16x16x32_bf16 v[212:215], v[120:123], v[96:99], v[212:215]\n\t" \
  "ds_read_b128 v[140:143], v61 offset:2048\n\t" \
  "v_mfma_f32_16x16x32_bf16 v[152:155], v[136:139], v[68:71], v[152:155]\n\t" \
  "ds_read_b128 v[92:95], v61 offset:4096\n\t" \
  "v_mfma_f32_16x16x32_bf16 v[152:155], v[148:151], v[104:107], v[152:155]\n\t" \
  "ds_read_b128 v[84:87], v60 offset:4096\n\t" \
  "v_mfma_f32_16x16x32_bf16 v[160:163], v[148:151], v[80:83], v[160:163]\n\t" \
  "ds_read_b128 v[100:103], v60 offset:6144\n\t" \
  "v_mfma_f32_16x16x32_bf16 v[160:163], v[136:139], v[116:119], v[160:163]\n\t" \
  "ds_read_b128 v[112:115], v61 offset:6144\n\t" \
  "v_mfma_f32_16x16x32_bf16 v[168:171], v[136:139], v[88:91], v[168:171]\n\t" \
  "ds_read_b128 v[128:131], v61 offset:8192\n\t" \
  "v_mfma_f32_16x16x32_bf16 v[168:171], v[148:151], v[124:127], v[168:171]\n\t" \
  "ds_read_b128 v[120:123], v60 offset:8192\n\t" \
  "v_mfma_f32_16x16x32_bf16 v[176:179], v[148:151], v[96:99], v[176:179]\n\t" \
  "ds_read_b64_tr_b16 v[148:149], v63 offset:34816\n\t" \
  "v_mfma_f32_16x16x32_bf16 v[176:179], v[136:139], v[132:135], v[176:179]\n\t" \
  "ds_read_b64_tr_b16 v[150:151], v63 offset:35328\n\t" \
  "v_mfma_f32_16x16x32_bf16 v[156:159], v[76:79], v[68:71], v[156:159]\n\t" \
  "ds_read_b64_tr_b16 v[136:137], v63 offset:38912\n\t" \
  "v_mfma_f32_16x16x32_bf16 v[156:159], v[144:147], v[104:107], v[156:159]\n\t" \
  "ds_read_b64_tr_b16 v[138:139], v63 offset:39424\n\t" \
  "v_mfma_f32_16x16x32_bf16 v[164:167], v[144:147], v[80:83], v[164:167]\n\t" \
  "ds_read_b64_tr_b16 v[68:69], v62 offset:26624\n\t" \
  "v_mfma_f32_16x16x32_bf16 v[164:167], v[76:79], v[116:119], v[164:167]\n\t" \
  "ds_read_b64_tr_b16 v[70:71], v62 offset:27136\n\t" \
  "v_mfma_f32_16x16x32_bf16 v[172:175], v[76:79], v[88:91], v[172:175]\n\t" \
  "ds_read_b64_tr_b16 v[104:105], v62 offset:30720\n\t" \
  "v_mfma_f32_16x16x32_bf16 v[172:175], v[144:147], v[124:127], v[172:175]\n\t" \
  "ds_read_b64_tr_b16 v[106:107], v62 offset:31232\n\t" \
  "v_mfma_f32_16x16x32_bf16 v[180:183], v[144:147], v[96:99], v[180:183]\n\t" \
  "ds_read_b128 v[80:83], v60 offset:0\n\t" \
  "v_mfma_f32_16x16x32_bf16 v[180:183], v[76:79], v[132:135], v[180:183]\n\t" \
  "ds_read_b128 v[116:119], v61 offset:0\n\t" \
  "s_waitcnt lgkmcnt(7)\n\t" \
  "v_mfma_f32_16x16x32_bf16 v[216:219], v[64:67], v[72:75], v[216:219]\n\t" \
  "ds_read_b64_tr_b16 v[88:89], v63 offset:26624\n\t" \
  "v_mfma_f32_16x16x32_bf16 v[216:219], v[140:143], v[108:111], v[216:219]\n\t" \
  "505:\n\t" \
  "s_cbranch_execz 509f\n\t" \
  "ds_read_b64_tr_b16 v[90:91], v63 offset:27136\n\t" \
  "v_mfma_f32_16x16x32_bf16 v[224:227], v[92:95], v[108:111], v[224:227]\n\t" \
  "ds_read_b64_tr_b16 v[124:125], v63 offset:30720\n\t" \
  "v_mfma_f32_16x16x32_bf16 v[224:227], v[84:87], v[72:75], v[224:227]\n\t" \
  "ds_read_b64_tr_b16 v[126:127], v63 offset:31232\n\t" \
  "v_mfma_f32_16x16x32_bf16 v[232:235], v[100:103], v[72:75], v[232:235]\n\t" \
  "ds_read_b64_tr_b16 v[96:97], v62 offset:18432\n\t" \
  "v_mfma_f32_16x16x32_bf16 v[232:235], v[112:115], v[108:111], v[232:235]\n\t" \
  "ds_read_b64_tr_b16 v[98:99], v62 offset:18944\n\t" \
  "v_mfma_f32_16x16x32_bf16 v[240:243], v[128:131], v[108:111], v[240:243]\n\t" \
  "ds_read_b64_tr_b16 v[144:145], v62 offset:22528\n\t" \
  "v_mfma_f32_16x16x32_bf16 v[240:243], v[120:123], v[72:75], v[240:243]\n\t" \
  "ds_read_b64_tr_b16 v[146:147], v62 offset:23040\n\t" \
  "v_mfma_f32_16x16x32_bf16 v[220:223], v[64:67], v[148:151], v[220:223]\n\t" \
  "ds_read_b64_tr_b16 v[76:77], v63 offset:18432\n\t" \
  "s_waitcnt lgkmcnt(7)\n\t" \
  "v_mfma_f32_16x16x32_bf16 v[220:223], v[140:143], v[136:139], v[220:223]\n\t" \
  "ds_read_b64_tr_b16 v[78:79], v63 offset:18944\n\t" \
  "v_mfma_f32_16x16x32_bf16 v[228:231], v[92:95], v[136:139], v[228:231]\n\t" \
  "ds_read_b64_tr_b16 v[132:133], v63 offset:22528\n\t" \
  "v_mfma_f32_16x16x32_bf16 v[228:231], v[84:87], v[148:151], v[228:231]\n\t" \
  "ds_read_b64_tr_b16 v[134:135], v63 offset:23040\n\t" \
  "v_mfma_f32_16x16x32_bf16 v[236:239], v[100:103], v[148:151], v[236:239]\n\t" \
  "v_mfma_f32_16x16x32_bf16 v[236:239], v[112:115], v[136:139], v[236:239]\n\t" \
  "v_mfma_f32_16x16x32_bf16 v[244:247], v[128:131], v[136:139], v[244:247]\n\t" \
  "v_mfma_f32_16x16x32_bf16 v[244:247], v[120:123], v[148:151], v[244:247]\n\t" \
  "v_mfma_f32_16x16x32_bf16 v[184:187], v[68:71], v[64:67], v[184:187]\n\t" \
  "v_mfma_f32_16x16x32_bf16 v[184:187], v[104:107], v[140:143], v[184:187]\n\t" \
  "v_mfma_f32_16x16x32_bf16 v[192:195], v[104:107], v[92:95], v[192:195]\n\t" \
  "v_mfma_f32_16x16x32_bf16 v[192:195], v[68:71], v[84:87], v[192:195]\n\t" \
  "v_mfma_f32_16x16x32_bf16 v[200:203], v[68:71], v[100:103], v[200:203]\n\t" \
  "v_mfma_f32_16x16x32_bf16 v[200:203], v[104:107], v[112:115], v[200:203]\n\t" \
  "v_mfma_f32_16x16x32_bf16 v[208:211], v[104:107], v[128:131], v[208:211]\n\t" \
  "v_mfma_f32_16x16x32_bf16 v[208:211], v[68:71], v[120:123], v[208:211]\n\t" \
  "v_mfma_f32_16x16x32_bf16 v[248:251], v[68:71], v[80:83], v[248:251]\n\t" \
  "v_mfma_f32_16x16x32_bf16 v[248:251], v[104:107], v[116:119], v[248:251]\n\t" \
  "v_mfma_f32_16x16x32_bf16 v[252:255], v[88:91], v[80:83], v[252:255]\n\t" \
  "s_waitcnt lgkmcnt(0)\n\t" \
  "v_mfma_f32_16x16x32_bf16 v[252:255], v[124:127], v[116:119], v[252:255]\n\t" \
  "v_mfma_f32_16x16x32_bf16 v[188:191], v[124:127], v[140:143], v[188:191]\n\t" \
  "v_mfma_f32_16x16x32_bf16 v[188:191], v[88:91], v[64:67], v[188:191]\n\t" \
  "s_barrier\n\t" \
  "v_mfma_f32_16x16x32_bf16 v[196:199], v[88:91], v[84:87], v[196:199]\n\t" \
  "ds_read_b64_tr_b16 v[108:109], v54 offset:34816\n\t" \
  "v_mfma_f32_16x16x32_bf16 v[196:199], v[124:127], v[92:95], v[196:199]\n\t" \
  "ds_read_b64_tr_b16 v[110:111], v54 offset:35328\n\t" \
  "v_mfma_f32_16x16x32_bf16 v[204:207], v[124:127], v[112:115], v[204:207]\n\t" \
  "ds_read_b128 v[72:75], v52 offset:2048\n\t" \
  "v_mfma_f32_16x16x32_bf16 v[204:207], v[88:91], v[100:103], v[204:207]\n\t" \
  "ds_read_b64_tr_b16 v[136:137], v54 offset:38912\n\t" \
  "v_mfma_f32_16x16x32_bf16 v[212:215], v[88:91], v[120:123], v[212:215]\n\t" \
  "ds_read_b64_tr_b16 v[138:139], v54 offset:39424\n\t" \
  "v_mfma_f32_16x16x32_bf16 v[212:215], v[124:127], v[128:131], v[212:215]\n\t" \
  "ds_read_b128 v[148:151], v53 offset:2048\n\t" \
  "v_mfma_f32_16x16x32_bf16 v[152:155], v[96:99], v[64:67], v[152:155]\n\t" \
  "ds_read_b128 v[68:71], v53 offset:4096\n\t" \
  "v_mfma_f32_16x16x32_bf16 v[152:155], v[144:147], v[140:143], v[152:155]\n\t" \
  "ds_read_b128 v[104:107], v52 offset:4096\n\t" \
  "v_mfma_f32_16x16x32_bf16 v[160:163], v[144:147], v[92:95], v[160:163]\n\t" \
  "ds_read_b128 v[80:83], v52 offset:6144\n\t" \
  "v_mfma_f32_16x16x32_bf16 v[160:163], v[96:99], v[84:87], v[160:163]\n\t" \
  "ds_read_b128 v[116:119], v53 offset:6144\n\t" \
  "v_mfma_f32_16x16x32_bf16 v[168:171], v[96:99], v[100:103], v[168:171]\n\t" \
  "ds_read_b128 v[88:91], v53 offset:8192\n\t" \
  "v_mfma_f32_16x16x32_bf16 v[168:171], v[144:147], v[112:115], v[168:171]\n\t" \
  "ds_read_b128 v[124:127], v52 offset:8192\n\t" \
  "v_mfma_f32_16x16x32_bf16 v[176:179], v[144:147], v[128:131], v[176:179]\n\t" \
  "ds_read_b64_tr_b16 v[144:145], v55 offset:34816\n\t" \
  "v_mfma_f32_16x16x32_bf16 v[176:179], v[96:99], v[120:123], v[176:179]\n\t" \
  "ds_read_b64_tr_b16 v[146:147], v55 offset:35328\n\t" \
  "v_mfma_f32_16x16x32_bf16 v[156:159], v[76:79], v[64:67], v[156:159]\n\t" \
  "ds_read_b64_tr_b16 v[96:97], v55 offset:38912\n\t" \
  "v_mfma_f32_16x16x32_bf16 v[156:159], v[132:135], v[140:143], v[156:159]\n\t" \
  "ds_read_b64_tr_b16 v[98:99], v55 offset:39424\n\t" \
  "v_mfma_f32_16x16x32_bf16 v[164:167], v[132:135], v[92:95], v[164:167]\n\t" \
  "ds_read_b64_tr_b16 v[64:65], v54 offset:26624\n\t" \
  "v_mfma_f32_16x16x32_bf16 v[164:167], v[76:79], v[84:87], v[164:167]\n\t" \
  "ds_read_b64_tr_b16 v[66:67], v54 offset:27136\n\t" \
  "v_mfma_f32_16x16x32_bf16 v[172:175], v[76:79], v[100:103], v[172:175]\n\t" \
  "ds_read_b64_tr_b16 v[140:141], v54 offset:30720\n\t" \
  "v_mfma_f32_16x16x32_bf16 v[172:175], v[132:135], v[112:115], v[172:175]\n\t" \
  "ds_read_b64_tr_b16 v[142:143], v54 offset:31232\n\t" \
  "v_mfma_f32_16x16x32_bf16 v[180:183], v[132:135], v[128:131], v[180:183]\n\t" \
  "ds_read_b128 v[92:95], v52 offset:0\n\t" \
  "v_mfma_f32_16x16x32_bf16 v[180:183], v[76:79], v[120:123], v[180:183]\n\t" \
  "ds_read_b128 v[84:87], v53 offset:0\n\t" \
  "s_waitcnt lgkmcnt(7)\n\t" \
  "v_mfma_f32_16x16x32_bf16 v[216:219], v[72:75], v[108:111], v[216:219]\n\t" \
  "ds_read_b64_tr_b16 v[100:101], v55 offset:26624\n\t" \
  "v_mfma_f32_16x16x32_bf16 v[216:219], v[148:151], v[136:139], v[216:219]\n\t" \
  "ds_read_b64_tr_b16 v[102:103], v55 offset:27136\n\t" \
  "v_mfma_f32_16x16x32_bf16 v[224:227], v[68:71], v[136:139], v[224:227]\n\t" \
  "ds_read_b64_tr_b16 v[112:113], v55 offset:30720\n\t" \
  "v_mfma_f32_16x16x32_bf16 v[224:227], v[104:107], v[108:111], v[224:227]\n\t" \
  "ds_read_b64_tr_b16 v[114:115], v55 offset:31232\n\t" \
  "v_mfma_f32_16x16x32_bf16 v[232:235], v[80:83], v[108:111], v[232:235]\n\t" \
  "ds_read_b64_tr_b16 v[128:129], v54 offset:18432\n\t" \
  "v_mfma_f32_16x16x32_bf16 v[232:235], v[116:119], v[136:139], v[232:235]\n\t" \
  "ds_read_b64_tr_b16 v[130:131], v54 offset:18944\n\t" \
  "v_mfma_f32_16x16x32_bf16 v[240:243], v[88:91], v[136:139], v[240:243]\n\t" \
  "ds_read_b64_tr_b16 v[132:133], v54 offset:22528\n\t" \
  "v_mfma_f32_16x16x32_bf16 v[240:243], v[124:127], v[108:111], v[240:243]\n\t" \
  "ds_read_b64_tr_b16 v[134:135], v54 offset:23040\n\t" \
  "v_mfma_f32_16x16x32_bf16 v[220:223], v[72:75], v[144:147], v[220:223]\n\t" \
  "ds_read_b64_tr_b16 v[76:77], v55 offset:18432\n\t" \
  "s_waitcnt lgkmcnt(7)\n\t" \
  "v_mfma_f32_16x16x32_bf16 v[220:223], v[148:151], v[96:99], v[220:223]\n\t" \
  "ds_read_b64_tr_b16 v[78:79], v55 offset:18944\n\t" \
  "v_mfma_f32_16x16x32_bf16 v[228:231], v[68:71], v[96:99], v[228:231]\n\t" \
  "ds_read_b64_tr_b16 v[120:121], v55 offset:22528\n\t" \
  "v_mfma_f32_16x16x32_bf16 v[228:231], v[104:107], v[144:147], v[228:231]\n\t" \
  "ds_read_b64_tr_b16 v[122:123], v55 offset:23040\n\t" \
  "v_mfma_f32_16x16x32_bf16 v[236:239], v[80:83], v[144:147], v[236:239]\n\t" \
  "v_mfma_f32_16x16x32_bf16 v[236:239], v[116:119], v[96:99], v[236:239]\n\t" \
  "v_mfma_f32_16x16x32_bf16 v[244:247], v[88:91], v[96:99], v[244:247]\n\t" \
  "v_mfma_f32_16x16x32_bf16 v[244:247], v[124:127], v[144:147], v[244:247]\n\t" \
  "v_mfma_f32_16x16x32_bf16 v[184:187], v[64:67], v[72:75], v[184:187]\n\t" \
  "v_mfma_f32_16x16x32_bf16 v[184:187], v[140:143], v[148:151], v[184:187]\n\t" \
  "v_mfma_f32_16x16x32_bf16 v[192:195], v[140:143], v[68:71], v[192:195]\n\t" \
  "v_mfma_f32_16x16x32_bf16 v[192:195], v[64:67], v[104:107], v[192:195]\n\t" \
  "v_mfma_f32_16x16x32_bf16 v[200:203], v[64:67], v[80:83], v[200:203]\n\t" \
  "v_mfma_f32_16x16x32_bf16 v[200:203], v[140:143], v[116:119], v[200:203]\n\t" \
  "v_mfma_f32_16x16x32_bf16 v[208:211], v[140:143], v[88:91], v[208:211]\n\t" \
  "v_mfma_f32_16x16x32_bf16 v[208:211], v[64:67], v[124:127], v[208:211]\n\t" \
  "v_mfma_f32_16x16x32_bf16 v[248:251], v[64:67], v[92:95], v[248:251]\n\t" \
  "v_mfma_f32_16x16x32_bf16 v[248:251], v[140:143], v[84:87], v[248:251]\n\t" \
  "v_mfma_f32_16x16x32_bf16 v[252:255], v[100:103], v[92:95], v[252:255]\n\t" \
  "s_waitcnt lgkmcnt(0)\n\t" \
  "v_mfma_f32_16x16x32_bf16 v[252:255], v[112:115], v[84:87], v[252:255]\n\t" \
  "v_mfma_f32_16x16x32_bf16 v[188:191], v[112:115], v[148:151], v[188:191]\n\t" \
  "v_mfma_f32_16x16x32_bf16 v[188:191], v[100:103], v[72:75], v[188:191]\n\t" \
  "s_barrier\n\t" \
  "v_mfma_f32_16x16x32_bf16 v[196:199], v[100:103], v[104:107], v[196:199]\n\t" \
  "ds_read_b64_tr_b16 v[96:97], v58 offset:34816\n\t" \
  "v_mfma_f32_16x16x32_bf16 v[196:199], v[112:115], v[68:71], v[196:199]\n\t" \
  "ds_read_b64_tr_b16 v[98:99], v58 offset:35328\n\t" \
  "v_mfma_f32_16x16x32_bf16 v[204:207], v[112:115], v[116:119], v[204:207]\n\t" \
  "ds_read_b128 v[136:139], v56 offset:2048\n\t" \
  "v_mfma_f32_16x16x32_bf16 v[204:207], v[100:103], v[80:83], v[204:207]\n\t" \
  "ds_read_b64_tr_b16 v[64:65], v58 offset:38912\n\t" \
  "v_mfma_f32_16x16x32_bf16 v[212:215], v[100:103], v[124:127], v[212:215]\n\t" \
  "ds_read_b64_tr_b16 v[66:67], v58 offset:39424\n\t" \
  "v_mfma_f32_16x16x32_bf16 v[212:215], v[112:115], v[88:91], v[212:215]\n\t" \
  "ds_read_b128 v[108:111], v57 offset:2048\n\t" \
  "v_mfma_f32_16x16x32_bf16 v[152:155], v[128:131], v[72:75], v[152:155]\n\t" \
  "ds_read_b128 v[144:147], v57 offset:4096\n\t" \
  "v_mfma_f32_16x16x32_bf16 v[152:155], v[132:135], v[148:151], v[152:155]\n\t" \
  "ds_read_b128 v[140:143], v56 offset:4096\n\t" \
  "v_mfma_f32_16x16x32_bf16 v[160:163], v[132:135], v[68:71], v[160:163]\n\t" \
  "ds_read_b128 v[112:115], v56 offset:6144\n\t" \
  "v_mfma_f32_16x16x32_bf16 v[160:163], v[128:131], v[104:107], v[160:163]\n\t" \
  "ds_read_b64_tr_b16 v[92:93], v59 offset:34816\n\t" \
  "v_mfma_f32_16x16x32_bf16 v[168:171], v[128:131], v[80:83], v[168:171]\n\t" \
  "ds_read_b64_tr_b16 v[94:95], v59 offset:35328\n\t" \
  "v_mfma_f32_16x16x32_bf16 v[168:171], v[132:135], v[116:119], v[168:171]\n\t" \
  "ds_read_b64_tr_b16 v[84:85], v59 offset:38912\n\t" \
  "v_mfma_f32_16x16x32_bf16 v[176:179], v[132:135], v[88:91], v[176:179]\n\t" \
  "ds_read_b128 v[132:135], v57 offset:6144\n\t" \
  "v_mfma_f32_16x16x32_bf16 v[176:179], v[128:131], v[124:127], v[176:179]\n\t" \
  "ds_read_b128 v[128:131], v57 offset:8192\n\t" \
  "v_mfma_f32_16x16x32_bf16 v[156:159], v[76:79], v[72:75], v[156:159]\n\t" \
  "ds_read_b64_tr_b16 v[86:87], v59 offset:39424\n\t" \
  "v_mfma_f32_16x16x32_bf16 v[156:159], v[120:123], v[148:151], v[156:159]\n\t" \
  "ds_read_b128 v[148:151], v56 offset:8192\n\t" \
  "v_mfma_f32_16x16x32_bf16 v[164:167], v[120:123], v[68:71], v[164:167]\n\t" \
  "ds_read_b64_tr_b16 v[100:101], v58 offset:26624\n\t" \
  "v_mfma_f32_16x16x32_bf16 v[164:167], v[76:79], v[104:107], v[164:167]\n\t" \
  "ds_read_b64_tr_b16 v[102:103], v58 offset:27136\n\t" \
  "v_mfma_f32_16x16x32_bf16 v[172:175], v[76:79], v[80:83], v[172:175]\n\t" \
  "ds_read_b64_tr_b16 v[72:73], v58 offset:30720\n\t" \
  "v_mfma_f32_16x16x32_bf16 v[172:175], v[120:123], v[116:119], v[172:175]\n\t" \
  "ds_read_b64_tr_b16 v[74:75], v58 offset:31232\n\t" \
  "v_mfma_f32_16x16x32_bf16 v[180:183], v[120:123], v[88:91], v[180:183]\n\t" \
  "ds_read_b128 v[68:71], v56 offset:0\n\t" \
  "v_mfma_f32_16x16x32_bf16 v[180:183], v[76:79], v[124:127], v[180:183]\n\t" \
  "ds_read_b128 v[104:107], v57 offset:0\n\t" \
  "s_waitcnt lgkmcnt(7)\n\t" \
  "v_mfma_f32_16x16x32_bf16 v[216:219], v[136:139], v[96:99], v[216:219]\n\t" \
  "ds_read_b64_tr_b16 v[80:81], v59 offset:26624\n\t" \
  "v_mfma_f32_16x16x32_bf16 v[216:219], v[108:111], v[64:67], v[216:219]\n\t" \
  "ds_read_b64_tr_b16 v[82:83], v59 offset:27136\n\t" \
  "v_mfma_f32_16x16x32_bf16 v[224:227], v[144:147], v[64:67], v[224:227]\n\t" \
  "ds_read_b64_tr_b16 v[88:89], v59 offset:30720\n\t" \
  "v_mfma_f32_16x16x32_bf16 v[224:227], v[140:143], v[96:99], v[224:227]\n\t" \
  "ds_read_b64_tr_b16 v[90:91], v59 offset:31232\n\t" \
  "v_mfma_f32_16x16x32_bf16 v[232:235], v[112:115], v[96:99], v[232:235]\n\t" \
  "ds_read_b64_tr_b16 v[116:117], v58 offset:18432\n\t" \
  "v_mfma_f32_16x16x32_bf16 v[232:235], v[132:135], v[64:67], v[232:235]\n\t" \
  "ds_read_b64_tr_b16 v[118:119], v58 offset:18944\n\t" \
  "v_mfma_f32_16x16x32_bf16 v[240:243], v[128:131], v[64:67], v[240:243]\n\t" \
  "ds_read_b64_tr_b16 v[120:121], v58 offset:22528\n\t" \
  "s_waitcnt lgkmcnt(7)\n\t" \
  "v_mfma_f32_16x16x32_bf16 v[240:243], v[148:151], v[96:99], v[240:243]\n\t" \
  "ds_read_b64_tr_b16 v[122:123], v58 offset:23040\n\t" \
  "v_mfma_f32_16x16x32_bf16 v[220:223], v[136:139], v[92:95], v[220:223]\n\t" \
  "ds_read_b64_tr_b16 v[124:125], v59 offset:18432\n\t" \
  "v_mfma_f32_16x16x32_bf16 v[220:223], v[108:111], v[84:87], v[220:223]\n\t" \
  "ds_read_b64_tr_b16 v[126:127], v59 offset:18944\n\t" \
  "v_mfma_f32_16x16x32_bf16 v[228:231], v[144:147], v[84:87], v[228:231]\n\t" \
  "ds_read_b64_tr_b16 v[76:77], v59 offset:22528\n\t" \
  "v_mfma_f32_16x16x32_bf16 v[228:231], v[140:143], v[92:95], v[228:231]\n\t" \
  "ds_read_b64_tr_b16 v[78:79], v59 offset:23040\n\t" \
  "v_mfma_f32_16x16x32_bf16 v[236:239], v[112:115], v[92:95], v[236:239]\n\t" \
  "v_mfma_f32_16x16x32_bf16 v[236:239], v[132:135], v[84:87], v[236:239]\n\t" \
  "v_mfma_f32_16x16x32_bf16 v[244:247], v[128:131], v[84:87], v[244:247]\n\t" \
  "v_mfma_f32_16x16x32_bf16 v[244:247], v[148:151], v[92:95], v[244:247]\n\t" \
  "v_mfma_f32_16x16x32_bf16 v[184:187], v[100:103], v[136:139], v[184:187]\n\t" \
  "v_mfma_f32_16x16x32_bf16 v[184:187], v[72:75], v[108:111], v[184:187]\n\t" \
  "v_mfma_f32_16x16x32_bf16 v[192:195], v[72:75], v[144:147], v[192:195]\n\t" \
  "v_and_b32 v14, -16, v32\n\t" \
  "v_mfma_f32_16x16x32_bf16 v[192:195], v[100:103], v[140:143], v[192:195]\n\t" \
  "v_mfma_f32_16x16x32_bf16 v[200:203], v[100:103], v[112:115], v[200:203]\n\t" \
  "ds_read_b128 v[10:13], v14 offset:128\n\t" \
  "v_mfma_f32_16x16x32_bf16 v[200:203], v[72:75], v[132:135], v[200:203]\n\t" \
  "v_mfma_f32_16x16x32_bf16 v[208:211], v[72:75], v[128:131], v[208:211]\n\t" \
  "v_mfma_f32_16x16x32_bf16 v[208:211], v[100:103], v[148:151], v[208:211]\n\t" \
  "v_mfma_f32_16x16x32_bf16 v[248:251], v[100:103], v[68:71], v[248:251]\n\t" \
  "v_mfma_f32_16x16x32_bf16 v[248:251], v[72:75], v[104:107], v[248:251]\n\t" \
  "s_waitcnt lgkmcnt(1)\n\t" \
  "v_mfma_f32_16x16x32_bf16 v[252:255], v[80:83], v[68:71], v[252:255]\n\t" \
  "s_waitcnt lgkmcnt(0)\n\t" \
  "v_and_b32 v10, v10, v11\n\t" \
  "v_and_b32 v12, v12, v13\n\t" \
  "v_mfma_f32_16x16x32_bf16 v[252:255], v[88:91], v[104:107], v[252:255]\n\t" \
  "v_and_b32 v10, v10, v12\n\t" \
  "v_mfma_f32_16x16x32_bf16 v[188:191], v[88:91], v[108:111], v[188:191]\n\t" \
  "v_readfirstlane_b32 vcc_lo, v10\n\t" \
  "v_mfma_f32_16x16x32_bf16 v[188:191], v[80:83], v[136:139], v[188:191]\n\t" \
  "ds_write_b32 v32, v0 offset:144\n\t" \
  "s_cmp_eq_u32 vcc_lo, 0\n\t" \
  "s_cbranch_scc1 15f\n\t" \
  "14:\n\t" \
  "v_mfma_f32_16x16x32_bf16 v[196:199], v[80:83], v[140:143], v[196:199]\n\t" \
  "ds_read_b64_tr_b16 v[64:65], v62 offset:26624\n\t" \
  "v_mfma_f32_16x16x32_bf16 v[196:199], v[88:91], v[144:147], v[196:199]\n\t" \
  "ds_read_b64_tr_b16 v[66:67], v62 offset:27136\n\t" \
  "v_mfma_f32_16x16x32_bf16 v[204:207], v[88:91], v[132:135], v[204:207]\n\t" \
  "ds_read_b128 v[96:99], v60 offset:0\n\t" \
  "v_mfma_f32_16x16x32_bf16 v[204:207], v[80:83], v[112:115], v[204:207]\n\t" \
  "ds_read_b128 v[84:87], v60 offset:2048\n\t" \
  "v_mfma_f32_16x16x32_bf16 v[212:215], v[80:83], v[148:151], v[212:215]\n\t" \
  "ds_read_b128 v[92:95], v60 offset:4096\n\t" \
  "v_mfma_f32_16x16x32_bf16 v[212:215], v[88:91], v[128:131], v[212:215]\n\t" \
  "ds_read_b128 v[100:103], v60 offset:6144\n\t" \
  "v_mfma_f32_16x16x32_bf16 v[152:155], v[116:119], v[136:139], v[152:155]\n\t" \
  "ds_read_b128 v[72:75], v60 offset:8192\n\t" \
  "v_mfma_f32_16x16x32_bf16 v[152:155], v[120:123], v[108:111], v[152:155]\n\t" \
  "ds_read_b64_tr_b16 v[68:69], v63 offset:26624\n\t" \
  "v_mfma_f32_16x16x32_bf16 v[160:163], v[120:123], v[144:147], v[160:163]\n\t" \
  "ds_read_b64_tr_b16 v[70:71], v63 offset:27136\n\t" \
  "v_mfma_f32_16x16x32_bf16 v[160:163], v[116:119], v[140:143], v[160:163]\n\t" \
  "ds_read_b64_tr_b16 v[104:105], v62 offset:34816\n\t" \
  "v_mfma_f32_16x16x32_bf16 v[168:171], v[116:119], v[112:115], v[168:171]\n\t" \
  "ds_read_b64_tr_b16 v[106:107], v62 offset:35328\n\t" \
  "v_mfma_f32_16x16x32_bf16 v[168:171], v[120:123], v[132:135], v[168:171]\n\t" \
  "ds_read_b64_tr_b16 v[80:81], v63 offset:34816\n\t" \
  "v_mfma_f32_16x16x32_bf16 v[176:179], v[120:123], v[128:131], v[176:179]\n\t" \
  "ds_read_b64_tr_b16 v[82:83], v63 offset:35328\n\t" \
  "v_mfma_f32_16x16x32_bf16 v[176:179], v[116:119], v[148:151], v[176:179]\n\t" \
  "ds_read_b64_tr_b16 v[88:89], v62 offset:18432\n\t" \
  "v_mfma_f32_16x16x32_bf16 v[156:159], v[124:127], v[136:139], v[156:159]\n\t" \
  "ds_read_b64_tr_b16 v[90:91], v62 offset:18944\n\t" \
  "v_mfma_f32_16x16x32_bf16 v[156:159], v[76:79], v[108:111], v[156:159]\n\t" \
  "ds_read_b64_tr_b16 v[120:121], v62 offset:30720\n\t" \
  "v_mfma_f32_16x16x32_bf16 v[164:167], v[76:79], v[144:147], v[164:167]\n\t" \
  "ds_read_b64_tr_b16 v[122:123], v62 offset:31232\n\t" \
  "v_mfma_f32_16x16x32_bf16 v[164:167], v[124:127], v[140:143], v[164:167]\n\t" \
  "ds_read_b128 v[116:119], v61 offset:0\n\t" \
  "v_mfma_f32_16x16x32_bf16 v[172:175], v[124:127], v[112:115], v[172:175]\n\t" \
  "ds_read_b128 v[136:139], v61 offset:2048\n\t" \
  "v_mfma_f32_16x16x32_bf16 v[172:175], v[76:79], v[132:135], v[172:175]\n\t" \
  "ds_read_b128 v[108:111], v61 offset:4096\n\t" \
  "v_mfma_f32_16x16x32_bf16 v[180:183], v[76:79], v[128:131], v[180:183]\n\t" \
  "ds_read_b64_tr_b16 v[76:77], v63 offset:18432\n\t" \
  "v_mfma_f32_16x16x32_bf16 v[180:183], v[124:127], v[148:151], v[180:183]\n\t" \
  "ds_read_b64_tr_b16 v[78:79], v63 offset:18944\n\t" \
  "s_waitcnt lgkmcnt(7)\n\t" \
  "v_mfma_f32_16x16x32_bf16 v[248:251], v[64:67], v[96:99], v[248:251]\n\t" \
  "ds_read_b128 v[144:147], v61 offset:6144\n\t" \
  "v_mfma_f32_16x16x32_bf16 v[184:187], v[64:67], v[84:87], v[184:187]\n\t" \
  "ds_read_b128 v[140:143], v61 offset:8192\n\t" \
  "v_mfma_f32_16x16x32_bf16 v[192:195], v[64:67], v[92:95], v[192:195]\n\t" \
  "ds_read_b64_tr_b16 v[112:113], v63 offset:30720\n\t" \
  "v_mfma_f32_16x16x32_bf16 v[200:203], v[64:67], v[100:103], v[200:203]\n\t" \
  "ds_read_b64_tr_b16 v[114:115], v63 offset:31232\n\t" \
  "v_mfma_f32_16x16x32_bf16 v[208:211], v[64:67], v[72:75], v[208:211]\n\t" \
  "ds_read_b64_tr_b16 v[132:133], v62 offset:22528\n\t" \
  "v_mfma_f32_16x16x32_bf16 v[252:255], v[68:71], v[96:99], v[252:255]\n\t" \
  "ds_read_b64_tr_b16 v[134:135], v62 offset:23040\n\t" \
  "v_mfma_f32_16x16x32_bf16 v[188:191], v[68:71], v[84:87], v[188:191]\n\t" \
  "ds_read_b64_tr_b16 v[128:129], v63 offset:22528\n\t" \
  "v_mfma_f32_16x16x32_bf16 v[196:199], v[68:71], v[92:95], v[196:199]\n\t" \
  "ds_read_b64_tr_b16 v[130:131], v63 offset:23040\n\t" \
  "v_mfma_f32_16x16x32_bf16 v[204:207], v[68:71], v[100:103], v[204:207]\n\t" \
  "ds_read_b64_tr_b16 v[124:125], v62 offset:38912\n\t" \
  "v_mfma_f32_16x16x32_bf16 v[212:215], v[68:71], v[72:75], v[212:215]\n\t" \
  "ds_read_b64_tr_b16 v[126:127], v62 offset:39424\n\t" \
  "v_mfma_f32_16x16x32_bf16 v[216:219], v[84:87], v[104:107], v[216:219]\n\t" \
  "ds_read_b64_tr_b16 v[148:149], v63 offset:38912\n\t" \
  "v_mfma_f32_16x16x32_bf16 v[224:227], v[92:95], v[104:107], v[224:227]\n\t" \
  "ds_read_b64_tr_b16 v[150:151], v63 offset:39424\n\t" \
  "v_mfma_f32_16x16x32_bf16 v[232:235], v[100:103], v[104:107], v[232:235]\n\t" \
  "v_mfma_f32_16x16x32_bf16 v[240:243], v[72:75], v[104:107], v[240:243]\n\t" \
  "v_mfma_f32_16x16x32_bf16 v[220:223], v[84:87], v[80:83], v[220:223]\n\t" \
  "v_mfma_f32_16x16x32_bf16 v[228:231], v[92:95], v[80:83], v[228:231]\n\t" \
  "v_mfma_f32_16x16x32_bf16 v[236:239], v[100:103], v[80:83], v[236:239]\n\t" \
  "v_mfma_f32_16x16x32_bf16 v[244:247], v[72:75], v[80:83], v[244:247]\n\t" \
  "v_mfma_f32_16x16x32_bf16 v[152:155], v[88:91], v[84:87], v[152:155]\n\t" \
  "v_mfma_f32_16x16x32_bf16 v[160:163], v[88:91], v[92:95], v[160:163]\n\t" \
  "v_mfma_f32_16x16x32_bf16 v[168:171], v[88:91], v[100:103], v[168:171]\n\t" \
  "v_mfma_f32_16x16x32_bf16 v[176:179], v[88:91], v[72:75], v[176:179]\n\t" \
  "s_waitcnt lgkmcnt(0)\n\t" \
  "v_mfma_f32_16x16x32_bf16 v[156:159], v[76:79], v[84:87], v[156:159]\n\t" \
  "v_mfma_f32_16x16x32_bf16 v[164:167], v[76:79], v[92:95], v[164:167]\n\t" \
  "ds_write_b32 v32, v0\n\t" \
  "v_mfma_f32_16x16x32_bf16 v[172:175], v[76:79], v[100:103], v[172:175]\n\t" \
  "v_mfma_f32_16x16x32_bf16 v[180:183], v[76:79], v[72:75], v[180:183]\n\t" \
  "v_mfma_f32_16x16x32_bf16 v[248:251], v[120:123], v[116:119], v[248:251]\n\t" \
  "v_and_b32 v56, -16, v32\n\t" \
  "v_mfma_f32_16x16x32_bf16 v[184:187], v[120:123], v[136:139], v[184:187]\n\t" \
  "v_mfma_f32_16x16x32_bf16 v[192:195], v[120:123], v[108:111], v[192:195]\n\t" \
  "v_mfma_f32_16x16x32_bf16 v[200:203], v[120:123], v[144:147], v[200:203]\n\t" \
  "v_mfma_f32_16x16x32_bf16 v[208:211], v[120:123], v[140:143], v[208:211]\n\t" \
  "v_mfma_f32_16x16x32_bf16 v[252:255], v[112:115], v[116:119], v[252:255]\n\t" \
  "v_mfma_f32_16x16x32_bf16 v[188:191], v[112:115], v[136:139], v[188:191]\n\t" \
  "v_mfma_f32_16x16x32_bf16 v[196:199], v[112:115], v[108:111], v[196:199]\n\t" \
  "v_cvt_pk_bf16_f32 v10, v248, v249\n\t" \
  "v_cvt_pk_bf16_f32 v11, v250, v251\n\t" \
  "v_mfma_f32_16x16x32_bf16 v[204:207], v[112:115], v[144:147], v[204:207]\n\t" \
  "v_cvt_pk_bf16_f32 v14, v184, v185\n\t" \
  "v_cvt_pk_bf16_f32 v15, v186, v187\n\t" \
  "v_mfma_f32_16x16x32_bf16 v[212:215], v[112:115], v[140:143], v[212:215]\n\t" \
  "v_cvt_pk_bf16_f32 v12, v252, v253\n\t" \
  "v_cvt_pk_bf16_f32 v13, v254, v255\n\t" \
  "v_mfma_f32_16x16x32_bf16 v[152:155], v[132:135], v[136:139], v[152:155]\n\t" \
  "ds_write_b128 v30, v[10:13]\n\t" \
  "ds_write_b32 v32, v0 offset:48\n\t" \
  "v_mfma_f32_16x16x32_bf16 v[160:163], v[132:135], v[108:111], v[160:163]\n\t" \
  "v_cvt_pk_bf16_f32 v16, v188, v189\n\t" \
  "v_cvt_pk_bf16_f32 v17, v190, v191\n\t" \
  "v_mfma_f32_16x16x32_bf16 v[168:171], v[132:135], v[144:147], v[168:171]\n\t" \
  "v_cvt_pk_bf16_f32 v18, v192, v193\n\t" \
  "v_cvt_pk_bf16_f32 v19, v194, v195\n\t" \
  "v_mfma_f32_16x16x32_bf16 v[176:179], v[132:135], v[140:143], v[176:179]\n\t" \
  "v_cvt_pk_bf16_f32 v20, v196, v197\n\t" \
  "v_cvt_pk_bf16_f32 v21, v198, v199\n\t" \
  "v_mfma_f32_16x16x32_bf16 v[156:159], v[128:131], v[136:139], v[156:159]\n\t" \
  "v_cvt_pk_bf16_f32 v22, v200, v201\n\t" \
  "v_cvt_pk_bf16_f32 v23, v202, v203\n\t" \
  "v_mfma_f32_16x16x32_bf16 v[164:167], v[128:131], v[108:111], v[164:167]\n\t" \
  "v_cvt_pk_bf16_f32 v24, v204, v205\n\t" \
  "v_cvt_pk_bf16_f32 v25, v206, v207\n\t" \
  "v_mfma_f32_16x16x32_bf16 v[172:175], v[128:131], v[144:147], v[172:175]\n\t" \
  "v_cvt_pk_bf16_f32 v26, v208, v209\n\t" \
  "v_cvt_pk_bf16_f32 v27, v210, v211\n\t" \
  "v_mfma_f32_16x16x32_bf16 v[180:183], v[128:131], v[140:143], v[180:183]\n\t" \
  "v_cvt_pk_bf16_f32 v28, v212, v213\n\t" \
  "v_cvt_pk_bf16_f32 v29, v214, v215\n\t" \
  "v_mfma_f32_16x16x32_bf16 v[216:219], v[136:139], v[124:127], v[216:219]\n\t" \
  "v_cvt_pk_bf16_f32 v84, v152, v153\n\t" \
  "v_cvt_pk_bf16_f32 v85, v154, v155\n\t" \
  "v_mfma_f32_16x16x32_bf16 v[224:227], v[108:111], v[124:127], v[224:227]\n\t" \
  "v_cvt_pk_bf16_f32 v86, v156, v157\n\t" \
  "v_cvt_pk_bf16_f32 v87, v158, v159\n\t" \
  "v_mfma_f32_16x16x32_bf16 v[232:235], v[144:147], v[124:127], v[232:235]\n\t" \
  "v_cvt_pk_bf16_f32 v88, v160, v161\n\t" \
  "v_cvt_pk_bf16_f32 v89, v162, v163\n\t" \
  "v_mfma_f32_16x16x32_bf16 v[240:243], v[140:143], v[124:127], v[240:243]\n\t" \
  "v_cvt_pk_bf16_f32 v90, v164, v165\n\t" \
  "v_cvt_pk_bf16_f32 v91, v166, v167\n\t" \
  "v_mfma_f32_16x16x32_bf16 v[220:223], v[136:139], v[148:151], v[220:223]\n\t" \
  "v_cvt_pk_bf16_f32 v92, v168, v169\n\t" \
  "v_cvt_pk_bf16_f32 v93, v170, v171\n\t" \
  "v_mfma_f32_16x16x32_bf16 v[228:231], v[108:111], v[148:151], v[228:231]\n\t" \
  "v_cvt_pk_bf16_f32 v94, v172, v173\n\t" \
  "v_cvt_pk_bf16_f32 v95, v174, v175\n\t" \
  "v_mfma_f32_16x16x32_bf16 v[236:239], v[144:147], v[148:151], v[236:239]\n\t" \
  "v_cvt_pk_bf16_f32 v96, v176, v177\n\t" \
  "v_cvt_pk_bf16_f32 v97, v178, v179\n\t" \
  "v_mfma_f32_16x16x32_bf16 v[244:247], v[140:143], v[148:151], v[244:247]\n\t" \
  "v_cvt_pk_bf16_f32 v98, v180, v181\n\t" \
  "v_cvt_pk_bf16_f32 v99, v182, v183\n\t" \
  "v_mfma_f32_16x16x32_bf16 v[200:203], v[22:25], v[92:95], v[34:37]\n\t" \
  "v_cvt_pk_bf16_f32 v66, v232, v233\n\t" \
  "v_cvt_pk_bf16_f32 v67, v234, v235\n\t" \
  "v_mfma_f32_16x16x32_bf16 v[208:211], v[26:29], v[96:99], v[34:37]\n\t" \
  "v_cvt_pk_bf16_f32 v76, v236, v237\n\t" \
  "v_cvt_pk_bf16_f32 v77, v238, v239\n\t" \
  "v_mfma_f32_16x16x32_bf16 v[212:215], v[22:25], v[96:99], v[38:41]\n\t" \
  "v_cvt_pk_bf16_f32 v64, v240, v241\n\t" \
  "v_cvt_pk_bf16_f32 v65, v242, v243\n\t" \
  "v_mfma_f32_16x16x32_bf16 v[204:207], v[18:21], v[92:95], v[38:41]\n\t" \
  "v_cvt_pk_bf16_f32 v74, v244, v245\n\t" \
  "v_cvt_pk_bf16_f32 v75, v246, v247\n\t" \
  "v_mfma_f32_16x16x32_bf16 v[192:195], v[18:21], v[88:91], v[34:37]\n\t" \
  "v_cvt_pk_bf16_f32 v68, v224, v225\n\t" \
  "v_cvt_pk_bf16_f32 v69, v226, v227\n\t" \
  "v_mfma_f32_16x16x32_bf16 v[196:199], v[14:17], v[88:91], v[38:41]\n\t" \
  "v_max_f32 v208, v208, v212\n\t" \
  "v_max_f32 v209, v209, v213\n\t" \
  "v_mfma_f32_16x16x32_bf16 v[184:187], v[14:17], v[84:87], v[34:37]\n\t" \
  "v_max_f32 v210, v210, v214\n\t" \
  "v_max_f32 v200, v200, v204\n\t" \
  "v_mfma_f32_16x16x32_bf16 v[188:191], v[10:13], v[84:87], v[42:45]\n\t" \
  "v_max_f32 v201, v201, v205\n\t" \
  "v_max_f32 v202, v202, v206\n\t" \
  "v_max_f32 v192, v192, v196\n\t" \
  "v_max_f32 v193, v193, v197\n\t" \
  "v_max_f32 v194, v194, v198\n\t" \
  "v_max_f32 v211, v211, v215\n\t" \
  "ds_read_b32 v52, v33 offset:48\n\t" \
  "ds_read_b64 v[72:73], v31\n\t" \
  "ds_read_b64 v[82:83], v31 offset:8\n\t" \
  "v_max3_f32 v106, v208, v209, v210\n\t" \
  "v_max_f32 v184, v184, v188\n\t" \
  "v_max_f32 v185, v185, v189\n\t" \
  "v_max_f32 v186, v186, v190\n\t" \
  "v_max_f32 v203, v203, v207\n\t" \
  "v_max3_f32 v104, v200, v201, v202\n\t" \
  "v_max_f32 v195, v195, v199\n\t" \
  "v_max3_f32 v102, v192, v193, v194\n\t" \
  "v_max_f32 v187, v187, v191\n\t" \
  "v_max3_f32 v100, v184, v185, v186\n\t" \
  "v_max_f32 v106, v106, v211\n\t" \
  "v_max_f32 v104, v104, v203\n\t" \
  "v_max_f32 v102, v102, v195\n\t" \
  "v_max_f32 v100, v100, v187\n\t" \
  "v_permlane16_swap_b32 v104, v106\n\t" \
  "v_cvt_pk_bf16_f32 v78, v228, v229\n\t" \
  "v_permlane16_swap_b32 v100, v102\n\t" \
  "v_max_f32 v104, v104, v106\n\t" \
  "v_max_f32 v100, v100, v102\n\t" \
  "v_cvt_pk_bf16_f32 v79, v230, v231\n\t" \
  "v_cvt_pk_bf16_f32 v70, v216, v217\n\t" \
  "v_permlane32_swap_b32 v100, v104\n\t" \
  "v_cvt_pk_bf16_f32 v71, v218, v219\n\t" \
  "v_max_f32 v100, v100, v104\n\t" \
  "v_mul_f32 v100, 0xbe8293ee, v100\n\t" \
  "v_mov_b32 v104, v100\n\t" \
  "v_cvt_pk_bf16_f32 v80, v220, v221\n\t" \
  "v_cvt_pk_bf16_f32 v81, v222, v223\n\t" \
  "v_permlane32_swap_b32 v100, v104\n\t" \
  "s_waitcnt lgkmcnt(0)\n\t" \
  "v_readfirstlane_b32 vcc_lo, v52\n\t" \
  "s_nop 1\n\t" \
  "s_cmp_eq_u32 vcc_lo, 0\n\t" \
  "s_cbranch_scc1 5f\n\t" \
  "4:\n\t" \
  "v_mov_b32 v106, v104\n\t" \
  "v_mov_b32 v102, v100\n\t" \
  "s_nop 0\n\t" \
  "v_permlane16_swap_b32 v104, v106\n\t" \
  "v_permlane16_swap_b32 v100, v102\n\t" \
  "v_fma_f32 v208, v208, %16, v106\n\t" \
  "v_fma_f32 v209, v209, %16, v106\n\t" \
  "v_fma_f32 v210, v210, %16, v106\n\t" \
  "v_fma_f32 v211, v211, %16, v106\n\t" \
  "v_exp_f32 v208, v208\n\t" \
  "v_exp_f32 v209, v209\n\t" \
  "v_exp_f32 v210, v210\n\t" \
  "v_exp_f32 v211, v211\n\t" \
  "v_cvt_pk_bf16_f32 v212, v208, v209\n\t" \
  "v_cvt_pk_bf16_f32 v213, v210, v211\n\t" \
  "v_bfi_b32 v214, v46, 0, v212\n\t" \
  "v_bfi_b32 v215, v47, 0, v213\n\t" \
  "v_and_b32 v212, v46, v212\n\t" \
  "v_and_b32 v213, v47, v213\n\t" \
  "v_fma_f32 v200, v200, %16, v104\n\t" \
  "v_fma_f32 v201, v201, %16, v104\n\t" \
  "v_mfma_f32_16x16x32_bf16 v[208:211], v[48:51], v[212:215], 0\n\t" \
  "v_fma_f32 v202, v202, %16, v104\n\t" \
  "v_fma_f32 v203, v203, %16, v104\n\t" \
  "v_mfma_f32_16x16x32_bf16 v[176:179], v[64:67], v[212:215], 0\n\t" \
  "v_exp_f32 v200, v200\n\t" \
  "v_mfma_f32_16x16x32_bf16 v[180:183], v[74:77], v[212:215], 0\n\t" \
  "v_exp_f32 v201, v201\n\t" \
  "v_exp_f32 v202, v202\n\t" \
  "v_exp_f32 v203, v203\n\t" \
  "v_cvt_pk_bf16_f32 v204, v200, v201\n\t" \
  "v_cvt_pk_bf16_f32 v205, v202, v203\n\t" \
  "v_bfi_b32 v206, v46, 0, v204\n\t" \
  "v_bfi_b32 v207, v47, 0, v205\n\t" \
  "v_rcp_f32 v106, v208\n\t" \
  "v_and_b32 v204, v46, v204\n\t" \
  "v_and_b32 v205, v47, v205\n\t" \
  "v_pk_mul_f32 v[176:177], v[176:177], v[106:107] op_sel_hi:[1,0]\n\t" \
  "v_pk_mul_f32 v[178:179], v[178:179], v[106:107] op_sel_hi:[1,0]\n\t" \
  "v_mfma_f32_16x16x32_bf16 v[200:203], v[48:51], v[204:207], 0\n\t" \
  "v_pk_mul_f32 v[180:181], v[180:181], v[106:107] op_sel_hi:[1,0]\n\t" \
  "v_pk_mul_f32 v[182:183], v[182:183], v[106:107] op_sel_hi:[1,0]\n\t" \
  "v_mfma_f32_16x16x32_bf16 v[168:171], v[66:69], v[204:207], 0\n\t" \
  "v_cvt_pk_bf16_f32 v240, v176, v177\n\t" \
  "v_cvt_pk_bf16_f32 v241, v178, v179\n\t" \
  "v_mfma_f32_16x16x32_bf16 v[172:175], v[76:79], v[204:207], 0\n\t" \
  "v_cvt_pk_bf16_f32 v242, v180, v181\n\t" \
  "v_cvt_pk_bf16_f32 v243, v182, v183\n\t" \
  "global_store_dwordx4 v1, v[240:243], %14 offset:0 sc1\n\t" \
  "v_fma_f32 v192, v192, %16, v102\n\t" \
  "v_fma_f32 v193, v193, %16, v102\n\t" \
  "v_fma_f32 v194, v194, %16, v102\n\t" \
  "v_fma_f32 v195, v195, %16, v102\n\t" \
  "v_exp_f32 v192, v192\n\t" \
  "v_exp_f32 v193, v193\n\t" \
  "v_exp_f32 v194, v194\n\t" \
  "v_exp_f32 v195, v195\n\t" \
  "v_cvt_pk_bf16_f32 v196, v192, v193\n\t" \
  "v_cvt_pk_bf16_f32 v197, v194, v195\n\t" \
  "v_bfi_b32 v198, v46, 0, v196\n\t" \
  "v_bfi_b32 v199, v47, 0, v197\n\t" \
  "v_rcp_f32 v104, v200\n\t" \
  "v_and_b32 v196, v46, v196\n\t" \
  "v_and_b32 v197, v47, v197\n\t" \
  "v_pk_mul_f32 v[168:169], v[168:169], v[104:105] op_sel_hi:[1,0]\n\t" \
  "v_pk_mul_f32 v[170:171], v[170:171], v[104:105] op_sel_hi:[1,0]\n\t" \
  "v_mfma_f32_16x16x32_bf16 v[192:195], v[48:51], v[196:199], 0\n\t" \
  "v_pk_mul_f32 v[172:173], v[172:173], v[104:105] op_sel_hi:[1,0]\n\t" \
  "v_pk_mul_f32 v[174:175], v[174:175], v[104:105] op_sel_hi:[1,0]\n\t" \
  "v_mfma_f32_16x16x32_bf16 v[160:163], v[68:71], v[196:199], 0\n\t" \
  "v_cvt_pk_bf16_f32 v232, v168, v169\n\t" \
  "v_cvt_pk_bf16_f32 v233, v170, v171\n\t" \
  "v_mfma_f32_16x16x32_bf16 v[164:167], v[78:81], v[196:199], 0\n\t" \
  "v_cvt_pk_bf16_f32 v234, v172, v173\n\t" \
  "v_cvt_pk_bf16_f32 v235, v174, v175\n\t" \
  "global_store_dwordx4 v1, v[232:235], %14 offset:-4096 sc1\n\t" \
  "v_fma_f32 v184, v184, %16, v100\n\t" \
  "v_fma_f32 v185, v185, %16, v100\n\t" \
  "v_fma_f32 v186, v186, %16, v100\n\t" \
  "v_fma_f32 v187, v187, %16, v100\n\t" \
  "v_exp_f32 v184, v184\n\t" \
  "v_exp_f32 v185, v185\n\t" \
  "v_exp_f32 v186, v186\n\t" \
  "v_exp_f32 v187, v187\n\t" \
  "v_cvt_pk_bf16_f32 v188, v184, v185\n\t" \
  "v_cvt_pk_bf16_f32 v189, v186, v187\n\t" \
  "v_bfi_b32 v190, v46, 0, v188\n\t" \
  "v_bfi_b32 v191, v47, 0, v189\n\t" \
  "v_rcp_f32 v102, v192\n\t" \
  "v_and_b32 v188, v46, v188\n\t" \
  "v_and_b32 v189, v47, v189\n\t" \
  "v_pk_mul_f32 v[160:161], v[160:161], v[102:103] op_sel_hi:[1,0]\n\t" \
  "v_pk_mul_f32 v[162:163], v[162:163], v[102:103] op_sel_hi:[1,0]\n\t" \
  "v_mfma_f32_16x16x32_bf16 v[184:187], v[48:51], v[188:191], 0\n\t" \
  "v_pk_mul_f32 v[164:165], v[164:165], v[102:103] op_sel_hi:[1,0]\n\t" \
  "v_pk_mul_f32 v[166:167], v[166:167], v[102:103] op_sel_hi:[1,0]\n\t" \
  "v_mfma_f32_16x16x32_bf16 v[152:155], v[70:73], v[188:191], 0\n\t" \
  "v_cvt_pk_bf16_f32 v224, v160, v161\n\t" \
  "v_cvt_pk_bf16_f32 v225, v162, v163\n\t" \
  "v_mfma_f32_16x16x32_bf16 v[156:159], v[80:83], v[188:191], 0\n\t" \
  "v_cvt_pk_bf16_f32 v226, v164, v165\n\t" \
  "v_cvt_pk_bf16_f32 v227, v166, v167\n\t" \
  "global_store_dwordx4 v4, v[224:227], %14 offset:0 sc1\n\t" \
  "v_rcp_f32 v100, v184\n\t" \
  "ds_read_b128 v[60:63], v56 offset:64\n\t" \
  "ds_read_b64_tr_b16 v[108:109], v5 offset:0\n\t" \
  "ds_read_b64_tr_b16 v[110:111], v5 offset:16\n\t" \
  "ds_read_b128 v[112:115], v6 offset:16384\n\t" \
  "ds_read_b128 v[116:119], v6 offset:15360\n\t" \
  "506:\n\t" \
  "s_cbranch_execz 510f\n\t" \
  "ds_read_b64_tr_b16 v[120:121], v5 offset:512\n\t" \
  "ds_read_b64_tr_b16 v[122:123], v5 offset:528\n\t" \
  "ds_read_b128 v[124:127], v6 offset:14336\n\t" \
  "ds_read_b64_tr_b16 v[128:129], v5 offset:1024\n\t" \
  "ds_read_b64_tr_b16 v[130:131], v5 offset:1040\n\t" \
  "ds_read_b128 v[132:135], v6 offset:13312\n\t" \
  "ds_read_b64_tr_b16 v[136:137], v5 offset:1536\n\t" \
  "ds_read_b64_tr_b16 v[138:139], v5 offset:1552\n\t" \
  "ds_read_b128 v[140:143], v6 offset:12288\n\t" \
  "ds_read_b64_tr_b16 v[144:145], v5 offset:2048\n\t" \
  "v_pk_mul_f32 v[152:153], v[152:153], v[100:101] op_sel_hi:[1,0]\n\t" \
  "v_pk_mul_f32 v[154:155], v[154:155], v[100:101] op_sel_hi:[1,0]\n\t" \
  "v_cvt_pk_bf16_f32 v216, v152, v153\n\t" \
  "v_pk_mul_f32 v[156:157], v[156:157], v[100:101] op_sel_hi:[1,0]\n\t" \
  "v_pk_mul_f32 v[158:159], v[158:159], v[100:101] op_sel_hi:[1,0]\n\t" \
  "v_cvt_pk_bf16_f32 v217, v154, v155\n\t" \
  "v_cvt_pk_bf16_f32 v218, v156, v157\n\t" \
  "v_cvt_pk_bf16_f32 v219, v158, v159\n\t" \
  "global_store_dwordx4 v4, v[216:219], %14 offset:-4096 sc1\n\t" \
  "ds_write_b32 v32, v0 offset:112\n\t" \
  "s_branch 9f\n\t" \
  "5:\n\t" \
  "ds_read_b32 v52, v33 offset:48\n\t" \
  "ds_read_b64 v[72:73], v31\n\t" \
  "ds_read_b64 v[82:83], v31 offset:8\n\t" \
  "s_waitcnt lgkmcnt(0)\n\t" \
  "v_readfirstlane_b32 vcc_lo, v52\n\t" \
  "s_nop 1\n\t" \
  "s_cmp_eq_u32 vcc_lo, 0\n\t" \
  "s_cbranch_scc1 5b\n\t" \
  "s_branch 4b\n\t" \
  "15:\n\t" \
  "ds_read_b128 v[10:13], v14 offset:128\n\t" \
  "s_waitcnt lgkmcnt(0)\n\t" \
  "v_and_b32 v10, v10, v11\n\t" \
  "v_and_b32 v12, v12, v13\n\t" \
  "v_and_b32 v10, v10, v12\n\t" \
  "s_nop 1\n\t" \
  "v_readfirstlane_b32 vcc_lo, v10\n\t" \
  "s_nop 1\n\t" \
  "s_cmp_eq_u32 vcc_lo, 0\n\t" \
  "s_cbranch_scc1 15b\n\t" \
  "s_branch 14b\n\t" \
  "9:\n\t" \
  "507:\n\t" \
  "s_cbranch_execz 26b\n\t" \
  "s_waitcnt lgkmcnt(14)\n\t" \
  "v_and_b32 v60, v60, v61\n\t" \
  "v_and_b32 v62, v62, v63\n\t" \
  "v_mov_b32 v12, %13\n\t" \
  "v_and_b32 v60, v60, v62\n\t" \
  "s_nop 0\n\t" \
  "v_readfirstlane_b32 vcc_lo, v60\n\t" \
  "s_nop 1\n\t" \
  "s_cmp_eq_u32 vcc_lo, 0\n\t" \
  "s_cbranch_scc1 7f\n\t" \
  "6:\n\t" \
  "s_waitcnt lgkmcnt(11)\n\t" \
  "v_mfma_f32_16x16x32_bf16 v[192:195], v[108:111], v[112:115], 0\n\t" \
  "ds_read_b64_tr_b16 v[146:147], v5 offset:2064\n\t" \
  "ds_read_b128 v[148:151], v6 offset:11264\n\t" \
  "s_waitcnt lgkmcnt(12)\n\t" \
  "v_mfma_f32_16x16x32_bf16 v[196:199], v[108:111], v[116:119], 0\n\t" \
  "ds_read_b64_tr_b16 v[64:65], v5 offset:2560\n\t" \
  "ds_read_b64_tr_b16 v[66:67], v5 offset:2576\n\t" \
  "s_waitcnt lgkmcnt(12)\n\t" \
  "v_mfma_f32_16x16x32_bf16 v[196:199], v[120:123], v[112:115], v[196:199]\n\t" \
  "ds_read_b128 v[68:71], v6 offset:10240\n\t" \
  "ds_read_b64_tr_b16 v[72:73], v5 offset:3072\n\t" \
  "s_waitcnt lgkmcnt(13)\n\t" \
  "v_mfma_f32_16x16x32_bf16 v[200:203], v[108:111], v[124:127], 0\n\t" \
  "ds_read_b64_tr_b16 v[74:75], v5 offset:3088\n\t" \
  "ds_read_b128 v[76:79], v6 offset:9216\n\t" \
  "v_mfma_f32_16x16x32_bf16 v[200:203], v[120:123], v[116:119], v[200:203]\n\t" \
  "ds_read_b64_tr_b16 v[80:81], v5 offset:3584\n\t" \
  "ds_read_b64_tr_b16 v[82:83], v5 offset:3600\n\t" \
  "s_waitcnt lgkmcnt(15)\n\t" \
  "v_mfma_f32_16x16x32_bf16 v[200:203], v[128:131], v[112:115], v[200:203]\n\t" \
  "ds_read_b128 v[84:87], v6 offset:0\n\t" \
  "ds_read_b128 v[88:91], v6 offset:1024\n\t" \
  "s_waitcnt lgkmcnt(15)\n\t" \
  "v_mfma_f32_16x16x32_bf16 v[204:207], v[108:111], v[132:135], 0\n\t" \
  "ds_read_b128 v[92:95], v6 offset:2048\n\t" \
  "ds_read_b128 v[96:99], v6 offset:3072\n\t" \
  "v_mfma_f32_16x16x32_bf16 v[204:207], v[120:123], v[124:127], v[204:207]\n\t" \
  "ds_read_b128 v[100:103], v6 offset:4096\n\t" \
  "ds_read_b128 v[104:107], v6 offset:5120\n\t" \
  "v_mfma_f32_16x16x32_bf16 v[204:207], v[128:131], v[116:119], v[204:207]\n\t" \
  "ds_read_b128 v[152:155], v6 offset:6144\n\t" \
  "ds_read_b128 v[156:159], v6 offset:7168\n\t" \
  "s_waitcnt lgkmcnt(14)\n\t" \
  "v_mfma_f32_16x16x32_bf16 v[204:207], v[136:139], v[112:115], v[204:207]\n\t" \
  "v_lshl_add_u32 v12, v12, 12, v4\n\t" \
  "s_lshl_b32 s88, %13, 5\n\t" \
  "v_mfma_f32_16x16x32_bf16 v[208:211], v[108:111], v[140:143], 0\n\t" \
  "v_mfma_f32_16x16x32_bf16 v[208:211], v[120:123], v[132:135], v[208:211]\n\t" \
  "v_mfma_f32_16x16x32_bf16 v[208:211], v[128:131], v[124:127], v[208:211]\n\t" \
  "s_and_b32 s88, s88, 64\n\t" \
  "v_xor_b32 v13, s88, v54\n\t" \
  "v_mfma_f32_16x16x32_bf16 v[208:211], v[136:139], v[116:119], v[208:211]\n\t" \
  "v_mfma_f32_16x16x32_bf16 v[208:211], v[144:147], v[112:115], v[208:211]\n\t" \
  "v_mfma_f32_16x16x32_bf16 v[212:215], v[108:111], v[148:151], 0\n\t" \
  "ds_read_b32 v55, v56 offset:104\n\t" \
  "v_mfma_f32_16x16x32_bf16 v[212:215], v[120:123], v[140:143], v[212:215]\n\t" \
  "v_mfma_f32_16x16x32_bf16 v[212:215], v[128:131], v[132:135], v[212:215]\n\t" \
  "v_bfe_u32 v11, v12, 4, 4\n\t" \
  "v_mfma_f32_16x16x32_bf16 v[212:215], v[136:139], v[124:127], v[212:215]\n\t" \
  "v_lshlrev_b32 v11, 7, v11\n\t" \
  "v_mfma_f32_16x16x32_bf16 v[212:215], v[144:147], v[116:119], v[212:215]\n\t" \
  "v_mov_b32 v10, 1\n\t" \
  "v_mfma_f32_16x16x32_bf16 v[212:215], v[64:67], v[112:115], v[212:215]\n\t" \
  "s_waitcnt lgkmcnt(1)\n\t" \
  "v_mfma_f32_16x16x32_bf16 v[216:219], v[108:111], v[68:71], 0\n\t" \
  "v_mfma_f32_16x16x32_bf16 v[216:219], v[120:123], v[148:151], v[216:219]\n\t" \
  "v_and_b32 v1, 0x180, v4\n\t" \
  "v_mfma_f32_16x16x32_bf16 v[216:219], v[128:131], v[140:143], v[216:219]\n\t" \
  "v_and_b32 v2, 0x70, v4\n\t" \
  "v_mfma_f32_16x16x32_bf16 v[216:219], v[136:139], v[132:135], v[216:219]\n\t" \
  "v_lshlrev_b32 v2, 3, v2\n\t" \
  "v_mfma_f32_16x16x32_bf16 v[216:219], v[144:147], v[124:127], v[216:219]\n\t" \
  "v_lshl_add_u32 v1, v1, 5, v2\n\t" \
  "v_mfma_f32_16x16x32_bf16 v[216:219], v[64:67], v[116:119], v[216:219]\n\t" \
  "v_and_b32 v2, 0x2000, v1\n\t" \
  "v_mfma_f32_16x16x32_bf16 v[216:219], v[72:75], v[112:115], v[216:219]\n\t" \
  "v_add_u32 v1, v1, v2\n\t" \
  "v_mfma_f32_16x16x32_bf16 v[220:223], v[108:111], v[76:79], 0\n\t" \
  "v_mfma_f32_16x16x32_bf16 v[220:223], v[120:123], v[68:71], v[220:223]\n\t" \
  "v_mfma_f32_16x16x32_bf16 v[220:223], v[128:131], v[148:151], v[220:223]\n\t" \
  "v_mfma_f32_16x16x32_bf16 v[220:223], v[136:139], v[140:143], v[220:223]\n\t" \
  "v_mfma_f32_16x16x32_bf16 v[220:223], v[144:147], v[132:135], v[220:223]\n\t" \
  "ds_read_b64_tr_b16 v[160:161], v5 offset:128\n\t" \
  "v_mfma_f32_16x16x32_bf16 v[220:223], v[64:67], v[124:127], v[220:223]\n\t" \
  "ds_read_b64_tr_b16 v[162:163], v5 offset:144\n\t" \
  "v_mfma_f32_16x16x32_bf16 v[220:223], v[72:75], v[116:119], v[220:223]\n\t" \
  "ds_read_b128 v[164:167], v7 offset:16384\n\t" \
  "v_mfma_f32_16x16x32_bf16 v[220:223], v[80:83], v[112:115], v[220:223]\n\t" \
  "ds_read_b128 v[168:171], v7 offset:15360\n\t" \
  "v_mfma_f32_16x16x32_bf16 v[252:255], v[108:111], v[84:87], 0\n\t" \
  "ds_read_b64_tr_b16 v[172:173], v5 offset:640\n\t" \
  "v_mfma_f32_16x16x32_bf16 v[252:255], v[120:123], v[88:91], v[252:255]\n\t" \
  "ds_read_b64_tr_b16 v[174:175], v5 offset:656\n\t" \
  "v_mfma_f32_16x16x32_bf16 v[252:255], v[128:131], v[92:95], v[252:255]\n\t" \
  "ds_read_b128 v[176:179], v7 offset:14336\n\t" \
  "v_mfma_f32_16x16x32_bf16 v[252:255], v[136:139], v[96:99], v[252:255]\n\t" \
  "ds_read_b64_tr_b16 v[180:181], v5 offset:1152\n\t" \
  "v_mfma_f32_16x16x32_bf16 v[252:255], v[144:147], v[100:103], v[252:255]\n\t" \
  "ds_read_b64_tr_b16 v[182:183], v5 offset:1168\n\t" \
  "v_mfma_f32_16x16x32_bf16 v[252:255], v[64:67], v[104:107], v[252:255]\n\t" \
  "ds_read_b128 v[184:187], v7 offset:13312\n\t" \
  "v_mfma_f32_16x16x32_bf16 v[252:255], v[72:75], v[152:155], v[252:255]\n\t" \
  "ds_read_b64_tr_b16 v[188:189], v5 offset:1664\n\t" \
  "v_mfma_f32_16x16x32_bf16 v[252:255], v[80:83], v[156:159], v[252:255]\n\t" \
  "ds_read_b64_tr_b16 v[190:191], v5 offset:1680\n\t" \
  "v_mfma_f32_16x16x32_bf16 v[248:251], v[108:111], v[88:91], 0\n\t" \
  "ds_read_b128 v[76:79], v7 offset:12288\n\t" \
  "v_mfma_f32_16x16x32_bf16 v[248:251], v[120:123], v[92:95], v[248:251]\n\t" \
  "v_mfma_f32_16x16x32_bf16 v[248:251], v[128:131], v[96:99], v[248:251]\n\t" \
  "ds_read_b64_tr_b16 v[68:69], v5 offset:2176\n\t" \
  "v_mfma_f32_16x16x32_bf16 v[248:251], v[136:139], v[100:103], v[248:251]\n\t" \
  "ds_read_b64_tr_b16 v[70:71], v5 offset:2192\n\t" \
  "v_mfma_f32_16x16x32_bf16 v[248:251], v[144:147], v[104:107], v[248:251]\n\t" \
  "ds_read_b128 v[148:151], v7 offset:11264\n\t" \
  "v_mfma_f32_16x16x32_bf16 v[248:251], v[64:67], v[152:155], v[248:251]\n\t" \
  "v_mfma_f32_16x16x32_bf16 v[248:251], v[72:75], v[156:159], v[248:251]\n\t" \
  "v_mfma_f32_16x16x32_bf16 v[244:247], v[108:111], v[92:95], 0\n\t" \
  "v_mfma_f32_16x16x32_bf16 v[244:247], v[120:123], v[96:99], v[244:247]\n\t" \
  "ds_read_b64_tr_b16 v[140:141], v5 offset:2688\n\t" \
  "v_mfma_f32_16x16x32_bf16 v[244:247], v[128:131], v[100:103], v[244:247]\n\t" \
  "ds_read_b64_tr_b16 v[142:143], v5 offset:2704\n\t" \
  "v_mfma_f32_16x16x32_bf16 v[244:247], v[136:139], v[104:107], v[244:247]\n\t" \
  "ds_read_b128 v[132:135], v7 offset:10240\n\t" \
  "v_mfma_f32_16x16x32_bf16 v[244:247], v[144:147], v[152:155], v[244:247]\n\t" \
  "v_mfma_f32_16x16x32_bf16 v[244:247], v[64:67], v[156:159], v[244:247]\n\t" \
  "v_mfma_f32_16x16x32_bf16 v[240:243], v[108:111], v[96:99], 0\n\t" \
  "v_mfma_f32_16x16x32_bf16 v[240:243], v[120:123], v[100:103], v[240:243]\n\t" \
  "v_mfma_f32_16x16x32_bf16 v[240:243], v[128:131], v[104:107], v[240:243]\n\t" \
  "ds_read_b64_tr_b16 v[124:125], v5 offset:3200\n\t" \
  "v_mfma_f32_16x16x32_bf16 v[240:243], v[136:139], v[152:155], v[240:243]\n\t" \
  "s_waitcnt vmcnt(0)\n\t" \
  "s_mov_b64 exec, 1\n\t" \
  "ds_add_rtn_u32 v10, v56, v10 offset:16\n\t" \
  "s_mov_b64 exec, -1\n\t" \
  "v_mfma_f32_16x16x32_bf16 v[240:243], v[144:147], v[156:159], v[240:243]\n\t" \
  "ds_read_b64_tr_b16 v[126:127], v5 offset:3216\n\t" \
  "v_mfma_f32_16x16x32_bf16 v[236:239], v[108:111], v[100:103], 0\n\t" \
  "ds_read_b128 v[116:119], v7 offset:9216\n\t" \
  "v_mfma_f32_16x16x32_bf16 v[236:239], v[120:123], v[104:107], v[236:239]\n\t" \
  "v_mfma_f32_16x16x32_bf16 v[236:239], v[128:131], v[152:155], v[236:239]\n\t" \
  "v_mfma_f32_16x16x32_bf16 v[236:239], v[136:139], v[156:159], v[236:239]\n\t" \
  "v_mfma_f32_16x16x32_bf16 v[232:235], v[108:111], v[104:107], 0\n\t" \
  "v_mfma_f32_16x16x32_bf16 v[232:235], v[120:123], v[152:155], v[232:235]\n\t" \
  "s_waitcnt lgkmcnt(2)\n\t" \
  "v_readfirstlane_b32 vcc_lo, v10\n\t" \
  "v_mfma_f32_16x16x32_bf16 v[232:235], v[128:131], v[156:159], v[232:235]\n\t" \
  "s_cmp_eq_u32 vcc_lo, 3\n\t" \
  "s_cbranch_scc1 23f\n\t" \
  "22:\n\t" \
  "v_mfma_f32_16x16x32_bf16 v[228:231], v[108:111], v[152:155], 0\n\t" \
  "ds_read_b64_tr_b16 v[64:65], v5 offset:3712\n\t" \
  "v_mfma_f32_16x16x32_bf16 v[228:231], v[120:123], v[156:159], v[228:231]\n\t" \
  "ds_read_b64_tr_b16 v[66:67], v5 offset:3728\n\t" \
  "v_mfma_f32_16x16x32_bf16 v[224:227], v[108:111], v[156:159], 0\n\t" \
  "ds_read_b128 v[72:75], v7 offset:0\n\t" \
  "v_mfma_f32_16x16x32_bf16 v[192:195], v[160:163], v[164:167], v[192:195]\n\t" \
  "ds_read_b128 v[80:83], v7 offset:1024\n\t" \
  "v_mfma_f32_16x16x32_bf16 v[196:199], v[160:163], v[168:171], v[196:199]\n\t" \
  "ds_read_b128 v[84:87], v7 offset:2048\n\t" \
  "v_mfma_f32_16x16x32_bf16 v[196:199], v[172:175], v[164:167], v[196:199]\n\t" \
  "ds_read_b128 v[88:91], v7 offset:3072\n\t" \
  "v_mfma_f32_16x16x32_bf16 v[200:203], v[160:163], v[176:179], v[200:203]\n\t" \
  "ds_read_b128 v[92:95], v7 offset:4096\n\t" \
  "v_mfma_f32_16x16x32_bf16 v[200:203], v[172:175], v[168:171], v[200:203]\n\t" \
  "v_and_b32 v53, 0x2000, v12\n\t" \
  "ds_read_b128 v[108:111], v7 offset:5120\n\t" \
  "v_mfma_f32_16x16x32_bf16 v[200:203], v[180:183], v[164:167], v[200:203]\n\t" \
  "ds_read_b128 v[112:115], v7 offset:6144\n\t" \
  "v_mfma_f32_16x16x32_bf16 v[204:207], v[160:163], v[184:187], v[204:207]\n\t" \
  "v_add_u32 v53, v53, v12\n\t" \
  "ds_read_b128 v[120:123], v7 offset:7168\n\t" \
  "v_mfma_f32_16x16x32_bf16 v[204:207], v[172:175], v[176:179], v[204:207]\n\t" \
  "v_mfma_f32_16x16x32_bf16 v[204:207], v[180:183], v[168:171], v[204:207]\n\t" \
  "v_lshrrev_b32 v14, 1, v4\n\t" \
  "v_mfma_f32_16x16x32_bf16 v[204:207], v[188:191], v[164:167], v[204:207]\n\t" \
  "v_mfma_f32_16x16x32_bf16 v[208:211], v[160:163], v[76:79], v[208:211]\n\t" \
  "v_lshrrev_b32 v15, 24, v3\n\t" \
  "v_mfma_f32_16x16x32_bf16 v[208:211], v[172:175], v[184:187], v[208:211]\n\t" \
  "v_mfma_f32_16x16x32_bf16 v[208:211], v[180:183], v[176:179], v[208:211]\n\t" \
  "v_add_u32 v14, %20, v14\n\t" \
  "v_mfma_f32_16x16x32_bf16 v[208:211], v[188:191], v[168:171], v[208:211]\n\t" \
  "v_mfma_f32_16x16x32_bf16 v[208:211], v[68:71], v[164:167], v[208:211]\n\t" \
  "v_lshrrev_b32 v18, 10, v12\n\t" \
  "v_mfma_f32_16x16x32_bf16 v[212:215], v[160:163], v[148:151], v[212:215]\n\t" \
  "v_mfma_f32_16x16x32_bf16 v[212:215], v[172:175], v[76:79], v[212:215]\n\t" \
  "v_and_b32 v18, 12, v18\n\t" \
  "v_mfma_f32_16x16x32_bf16 v[212:215], v[180:183], v[184:187], v[212:215]\n\t" \
  "v_mfma_f32_16x16x32_bf16 v[212:215], v[188:191], v[176:179], v[212:215]\n\t" \
  "v_mov_b32 v17, 16\n\t" \
  "v_mfma_f32_16x16x32_bf16 v[212:215], v[68:71], v[168:171], v[212:215]\n\t" \
  "v_mfma_f32_16x16x32_bf16 v[212:215], v[140:143], v[164:167], v[212:215]\n\t" \
  "v_add_u32 v18, v56, v18\n\t" \
  "v_mfma_f32_16x16x32_bf16 v[216:219], v[160:163], v[132:135], v[216:219]\n\t" \
  "v_mfma_f32_16x16x32_bf16 v[216:219], v[172:175], v[148:151], v[216:219]\n\t" \
  "s_mov_b32 s76, 0\n\t" \
  "v_mfma_f32_16x16x32_bf16 v[216:219], v[180:183], v[76:79], v[216:219]\n\t" \
  "v_mfma_f32_16x16x32_bf16 v[216:219], v[188:191], v[184:187], v[216:219]\n\t" \
  "s_brev_b32 s77, 1\n\t" \
  "v_mfma_f32_16x16x32_bf16 v[216:219], v[68:71], v[176:179], v[216:219]\n\t" \
  "v_mfma_f32_16x16x32_bf16 v[216:219], v[140:143], v[168:171], v[216:219]\n\t" \
  "v_add_u32 v28, 0, v13\n\t" \
  "s_waitcnt lgkmcnt(0)\n\t" \
  "v_mfma_f32_16x16x32_bf16 v[216:219], v[124:127], v[164:167], v[216:219]\n\t" \
  "v_mfma_f32_16x16x32_bf16 v[220:223], v[160:163], v[116:119], v[220:223]\n\t" \
  "v_xor_b32 v29, 32, v28\n\t" \
  "v_mfma_f32_16x16x32_bf16 v[220:223], v[172:175], v[132:135], v[220:223]\n\t" \
  "v_mfma_f32_16x16x32_bf16 v[220:223], v[180:183], v[148:151], v[220:223]\n\t" \
  "v_xor_b32 v30, 64, v28\n\t" \
  "v_mfma_f32_16x16x32_bf16 v[220:223], v[188:191], v[76:79], v[220:223]\n\t" \
  "v_mfma_f32_16x16x32_bf16 v[220:223], v[68:71], v[184:187], v[220:223]\n\t" \
  "v_xor_b32 v31, 96, v28\n\t" \
  "ds_read_b64_tr_b16 v[76:77], v5 offset:256\n\t" \
  "v_mfma_f32_16x16x32_bf16 v[220:223], v[140:143], v[176:179], v[220:223]\n\t" \
  "ds_read_b64_tr_b16 v[78:79], v5 offset:272\n\t" \
  "v_mfma_f32_16x16x32_bf16 v[220:223], v[124:127], v[168:171], v[220:223]\n\t" \
  "v_add_u32 v32, 65536, v13\n\t" \
  "ds_read_b128 v[128:131], v8 offset:16384\n\t" \
  "v_mfma_f32_16x16x32_bf16 v[220:223], v[64:67], v[164:167], v[220:223]\n\t" \
  "ds_read_b128 v[136:139], v8 offset:15360\n\t" \
  "v_mfma_f32_16x16x32_bf16 v[252:255], v[160:163], v[72:75], v[252:255]\n\t" \
  "v_xor_b32 v33, 32, v32\n\t" \
  "ds_read_b64_tr_b16 v[144:145], v5 offset:768\n\t" \
  "v_mfma_f32_16x16x32_bf16 v[252:255], v[172:175], v[80:83], v[252:255]\n\t" \
  "ds_read_b64_tr_b16 v[146:147], v5 offset:784\n\t" \
  "v_mfma_f32_16x16x32_bf16 v[252:255], v[180:183], v[84:87], v[252:255]\n\t" \
  "s_mov_b32 exec_hi, 0\n\t" \
  "global_load_dword v2, v1, %14 offset:-4096 sc1\n\t" \
  "s_mov_b32 exec_hi, -1\n\t" \
  "v_xor_b32 v34, 64, v32\n\t" \
  "v_mfma_f32_16x16x32_bf16 v[252:255], v[188:191], v[88:91], v[252:255]\n\t" \
  "ds_read_b128 v[96:99], v8 offset:14336\n\t" \
  "v_mfma_f32_16x16x32_bf16 v[252:255], v[68:71], v[92:95], v[252:255]\n\t" \
  "v_xor_b32 v35, 96, v32\n\t" \
  "ds_read_b64_tr_b16 v[72:73], v5 offset:1280\n\t" \
  "v_mfma_f32_16x16x32_bf16 v[252:255], v[140:143], v[108:111], v[252:255]\n\t" \
  "ds_read_b64_tr_b16 v[74:75], v5 offset:1296\n\t" \
  "v_mfma_f32_16x16x32_bf16 v[252:255], v[124:127], v[112:115], v[252:255]\n\t" \
  "v_add_u32 v36, 131072, v13\n\t" \
  "ds_read_b128 v[100:103], v8 offset:13312\n\t" \
  "v_mfma_f32_16x16x32_bf16 v[252:255], v[64:67], v[120:123], v[252:255]\n\t" \
  "ds_read_b64_tr_b16 v[104:105], v5 offset:1792\n\t" \
  "v_mfma_f32_16x16x32_bf16 v[248:251], v[160:163], v[80:83], v[248:251]\n\t" \
  "v_xor_b32 v37, 32, v36\n\t" \
  "ds_read_b64_tr_b16 v[106:107], v5 offset:1808\n\t" \
  "v_mfma_f32_16x16x32_bf16 v[248:251], v[172:175], v[84:87], v[248:251]\n\t" \
  "ds_read_b128 v[152:155], v8 offset:12288\n\t" \
  "v_mfma_f32_16x16x32_bf16 v[248:251], v[180:183], v[88:91], v[248:251]\n\t" \
  "v_xor_b32 v38, 64, v36\n\t" \
  "ds_read_b64_tr_b16 v[64:65], v5 offset:2304\n\t" \
  "v_mfma_f32_16x16x32_bf16 v[248:251], v[188:191], v[92:95], v[248:251]\n\t" \
  "ds_read_b64_tr_b16 v[66:67], v5 offset:2320\n\t" \
  "v_mfma_f32_16x16x32_bf16 v[248:251], v[68:71], v[108:111], v[248:251]\n\t" \
  "v_xor_b32 v39, 96, v36\n\t" \
  "ds_read_b128 v[80:83], v8 offset:11264\n\t" \
  "v_mfma_f32_16x16x32_bf16 v[248:251], v[140:143], v[112:115], v[248:251]\n\t" \
  "v_mfma_f32_16x16x32_bf16 v[248:251], v[124:127], v[120:123], v[248:251]\n\t" \
  "v_lshrrev_b32 v57, 5, v4\n\t" \
  "v_mfma_f32_16x16x32_bf16 v[244:247], v[160:163], v[84:87], v[244:247]\n\t" \
  "v_mfma_f32_16x16x32_bf16 v[244:247], v[172:175], v[88:91], v[244:247]\n\t" \
  "v_and_b32 v57, 0x18, v57\n\t" \
  "ds_read_b64_tr_b16 v[84:85], v5 offset:2816\n\t" \
  "v_mfma_f32_16x16x32_bf16 v[244:247], v[180:183], v[92:95], v[244:247]\n\t" \
  "ds_read_b64_tr_b16 v[86:87], v5 offset:2832\n\t" \
  "v_mfma_f32_16x16x32_bf16 v[244:247], v[188:191], v[108:111], v[244:247]\n\t" \
  "v_and_b32 v62, 0x70, v4\n\t" \
  "ds_read_b128 v[156:159], v8 offset:10240\n\t" \
  "v_mfma_f32_16x16x32_bf16 v[244:247], v[68:71], v[112:115], v[244:247]\n\t" \
  "v_mfma_f32_16x16x32_bf16 v[244:247], v[140:143], v[120:123], v[244:247]\n\t" \
  "v_xor_b32 v57, v57, v62\n\t" \
  "v_mfma_f32_16x16x32_bf16 v[240:243], v[160:163], v[88:91], v[240:243]\n\t" \
  "v_mfma_f32_16x16x32_bf16 v[240:243], v[172:175], v[92:95], v[240:243]\n\t" \
  "v_and_b32 v58, 0xf0, v4\n\t" \
  "v_mfma_f32_16x16x32_bf16 v[240:243], v[180:183], v[108:111], v[240:243]\n\t" \
  "ds_read_b64_tr_b16 v[88:89], v5 offset:3328\n\t" \
  "v_mfma_f32_16x16x32_bf16 v[240:243], v[188:191], v[112:115], v[240:243]\n\t" \
  "v_lshl_add_u32 v57, v58, 3, v57\n\t" \
  "ds_read_b64_tr_b16 v[90:91], v5 offset:3344\n\t" \
  "v_mfma_f32_16x16x32_bf16 v[240:243], v[68:71], v[120:123], v[240:243]\n\t" \
  "ds_read_b128 v[164:167], v8 offset:9216\n\t" \
  "v_mfma_f32_16x16x32_bf16 v[236:239], v[160:163], v[92:95], v[236:239]\n\t" \
  "v_add_u32 v57, %23, v57\n\t" \
  "v_mfma_f32_16x16x32_bf16 v[236:239], v[172:175], v[108:111], v[236:239]\n\t" \
  "v_mfma_f32_16x16x32_bf16 v[236:239], v[180:183], v[112:115], v[236:239]\n\t" \
  "v_xor_b32 v58, 32, v57\n\t" \
  "v_mfma_f32_16x16x32_bf16 v[236:239], v[188:191], v[120:123], v[236:239]\n\t" \
  "v_mfma_f32_16x16x32_bf16 v[232:235], v[160:163], v[108:111], v[232:235]\n\t" \
  "v_xor_b32 v59, 64, v57\n\t" \
  "v_mfma_f32_16x16x32_bf16 v[232:235], v[172:175], v[112:115], v[232:235]\n\t" \
  "ds_read_b64_tr_b16 v[68:69], v5 offset:3840\n\t" \
  "v_mfma_f32_16x16x32_bf16 v[232:235], v[180:183], v[120:123], v[232:235]\n\t" \
  "v_xor_b32 v60, 96, v57\n\t" \
  "ds_read_b64_tr_b16 v[70:71], v5 offset:3856\n\t" \
  "v_mfma_f32_16x16x32_bf16 v[228:231], v[160:163], v[112:115], v[228:231]\n\t" \
  "ds_read_b128 v[92:95], v8 offset:0\n\t" \
  "v_mfma_f32_16x16x32_bf16 v[228:231], v[172:175], v[120:123], v[228:231]\n\t" \
  "v_lshrrev_b32 v61, 3, v4\n\t" \
  "ds_read_b128 v[168:171], v8 offset:1024\n\t" \
  "v_mfma_f32_16x16x32_bf16 v[224:227], v[160:163], v[120:123], v[224:227]\n\t" \
  "ds_read_b128 v[172:175], v8 offset:2048\n\t" \
  "s_waitcnt lgkmcnt(5)\n\t" \
  "v_mfma_f32_16x16x32_bf16 v[192:195], v[76:79], v[128:131], v[192:195]\n\t" \
  "v_and_b32 v61, 0x70, v61\n\t" \
  "ds_read_b128 v[160:163], v8 offset:3072\n\t" \
  "v_mfma_f32_16x16x32_bf16 v[196:199], v[76:79], v[136:139], v[196:199]\n\t" \
  "ds_read_b128 v[176:179], v8 offset:4096\n\t" \
  "v_mfma_f32_16x16x32_bf16 v[196:199], v[144:147], v[128:131], v[196:199]\n\t" \
  "v_xor_b32 v61, v61, v4\n\t" \
  "ds_read_b128 v[180:183], v8 offset:5120\n\t" \
  "v_mfma_f32_16x16x32_bf16 v[200:203], v[76:79], v[96:99], v[200:203]\n\t" \
  "ds_read_b128 v[184:187], v8 offset:6144\n\t" \
  "v_mfma_f32_16x16x32_bf16 v[200:203], v[144:147], v[136:139], v[200:203]\n\t" \
  "v_add_u32 v61, %23, v61\n\t" \
  "ds_read_b128 v[188:191], v8 offset:7168\n\t" \
  "v_mfma_f32_16x16x32_bf16 v[200:203], v[72:75], v[128:131], v[200:203]\n\t" \
  "v_mfma_f32_16x16x32_bf16 v[204:207], v[76:79], v[100:103], v[204:207]\n\t" \
  "v_and_b32 v63, 0x380, v4\n\t" \
  "v_mfma_f32_16x16x32_bf16 v[204:207], v[144:147], v[96:99], v[204:207]\n\t" \
  "v_mfma_f32_16x16x32_bf16 v[204:207], v[72:75], v[136:139], v[204:207]\n\t" \
  "v_lshl_add_u32 v62, v63, 6, v62\n\t" \
  "v_mfma_f32_16x16x32_bf16 v[204:207], v[104:107], v[128:131], v[204:207]\n\t" \
  "v_mfma_f32_16x16x32_bf16 v[208:211], v[76:79], v[152:155], v[208:211]\n\t" \
  "v_add_u32 v63, 0x10000, v62\n\t" \
  "v_mfma_f32_16x16x32_bf16 v[208:211], v[144:147], v[100:103], v[208:211]\n\t" \
  "v_mfma_f32_16x16x32_bf16 v[208:211], v[72:75], v[96:99], v[208:211]\n\t" \
  "v_mfma_f32_16x16x32_bf16 v[208:211], v[104:107], v[136:139], v[208:211]\n\t" \
  "v_mfma_f32_16x16x32_bf16 v[208:211], v[64:67], v[128:131], v[208:211]\n\t" \
  "v_mfma_f32_16x16x32_bf16 v[212:215], v[76:79], v[80:83], v[212:215]\n\t" \
  "v_mfma_f32_16x16x32_bf16 v[212:215], v[144:147], v[152:155], v[212:215]\n\t" \
  "v_mfma_f32_16x16x32_bf16 v[212:215], v[72:75], v[100:103], v[212:215]\n\t" \
  "v_mfma_f32_16x16x32_bf16 v[212:215], v[104:107], v[96:99], v[212:215]\n\t" \
  "v_mfma_f32_16x16x32_bf16 v[212:215], v[64:67], v[136:139], v[212:215]\n\t" \
  "v_mfma_f32_16x16x32_bf16 v[212:215], v[84:87], v[128:131], v[212:215]\n\t" \
  "v_mfma_f32_16x16x32_bf16 v[216:219], v[76:79], v[156:159], v[216:219]\n\t" \
  "v_mfma_f32_16x16x32_bf16 v[216:219], v[144:147], v[80:83], v[216:219]\n\t" \
  "v_mfma_f32_16x16x32_bf16 v[216:219], v[72:75], v[152:155], v[216:219]\n\t" \
  "v_mfma_f32_16x16x32_bf16 v[216:219], v[104:107], v[100:103], v[216:219]\n\t" \
  "v_mfma_f32_16x16x32_bf16 v[216:219], v[64:67], v[96:99], v[216:219]\n\t" \
  "v_mfma_f32_16x16x32_bf16 v[216:219], v[84:87], v[136:139], v[216:219]\n\t" \
  "v_mfma_f32_16x16x32_bf16 v[216:219], v[88:91], v[128:131], v[216:219]\n\t" \
  "v_mfma_f32_16x16x32_bf16 v[220:223], v[76:79], v[164:167], v[220:223]\n\t" \
  "v_mfma_f32_16x16x32_bf16 v[220:223], v[144:147], v[156:159], v[220:223]\n\t" \
  "v_mfma_f32_16x16x32_bf16 v[220:223], v[72:75], v[80:83], v[220:223]\n\t" \
  "v_mfma_f32_16x16x32_bf16 v[220:223], v[104:107], v[152:155], v[220:223]\n\t" \
  "v_mfma_f32_16x16x32_bf16 v[220:223], v[64:67], v[100:103], v[220:223]\n\t" \
  "ds_read_b64_tr_b16 v[148:149], v5 offset:384\n\t" \
  "v_mfma_f32_16x16x32_bf16 v[220:223], v[84:87], v[96:99], v[220:223]\n\t" \
  "ds_read_b64_tr_b16 v[150:151], v5 offset:400\n\t" \
  "v_mfma_f32_16x16x32_bf16 v[220:223], v[88:91], v[136:139], v[220:223]\n\t" \
  "ds_read_b128 v[80:83], v9 offset:16384\n\t" \
  "s_waitcnt lgkmcnt(3)\n\t" \
  "v_mfma_f32_16x16x32_bf16 v[220:223], v[68:71], v[128:131], v[220:223]\n\t" \
  "ds_read_b128 v[140:143], v9 offset:15360\n\t" \
  "v_mfma_f32_16x16x32_bf16 v[252:255], v[76:79], v[92:95], v[252:255]\n\t" \
  "ds_read_b64_tr_b16 v[132:133], v5 offset:896\n\t" \
  "v_mfma_f32_16x16x32_bf16 v[252:255], v[144:147], v[168:171], v[252:255]\n\t" \
  "ds_read_b64_tr_b16 v[134:135], v5 offset:912\n\t" \
  "v_mfma_f32_16x16x32_bf16 v[252:255], v[72:75], v[172:175], v[252:255]\n\t" \
  "ds_read_b128 v[124:127], v9 offset:14336\n\t" \
  "v_mfma_f32_16x16x32_bf16 v[252:255], v[104:107], v[160:163], v[252:255]\n\t" \
  "ds_read_b64_tr_b16 v[116:117], v5 offset:1408\n\t" \
  "v_mfma_f32_16x16x32_bf16 v[252:255], v[64:67], v[176:179], v[252:255]\n\t" \
  "ds_read_b64_tr_b16 v[118:119], v5 offset:1424\n\t" \
  "v_mfma_f32_16x16x32_bf16 v[252:255], v[84:87], v[180:183], v[252:255]\n\t" \
  "ds_read_b128 v[92:95], v9 offset:13312\n\t" \
  "v_mfma_f32_16x16x32_bf16 v[252:255], v[88:91], v[184:187], v[252:255]\n\t" \
  "ds_read_b64_tr_b16 v[108:109], v5 offset:1920\n\t" \
  "v_mfma_f32_16x16x32_bf16 v[252:255], v[68:71], v[188:191], v[252:255]\n\t" \
  "ds_read_b64_tr_b16 v[110:111], v5 offset:1936\n\t" \
  "v_mfma_f32_16x16x32_bf16 v[248:251], v[76:79], v[168:171], v[248:251]\n\t" \
  "ds_read_b128 v[112:115], v9 offset:12288\n\t" \
  "v_mfma_f32_16x16x32_bf16 v[248:251], v[144:147], v[172:175], v[248:251]\n\t" \
  "v_mfma_f32_16x16x32_bf16 v[248:251], v[72:75], v[160:163], v[248:251]\n\t" \
  "ds_read_b64_tr_b16 v[120:121], v5 offset:2432\n\t" \
  "v_mfma_f32_16x16x32_bf16 v[248:251], v[104:107], v[176:179], v[248:251]\n\t" \
  "ds_read_b64_tr_b16 v[122:123], v5 offset:2448\n\t" \
  "v_mfma_f32_16x16x32_bf16 v[248:251], v[64:67], v[180:183], v[248:251]\n\t" \
  "ds_read_b128 v[68:71], v9 offset:11264\n\t" \
  "v_mfma_f32_16x16x32_bf16 v[248:251], v[84:87], v[184:187], v[248:251]\n\t" \
  "v_mfma_f32_16x16x32_bf16 v[248:251], v[88:91], v[188:191], v[248:251]\n\t" \
  "v_mfma_f32_16x16x32_bf16 v[244:247], v[76:79], v[172:175], v[244:247]\n\t" \
  "v_mfma_f32_16x16x32_bf16 v[244:247], v[144:147], v[160:163], v[244:247]\n\t" \
  "ds_read_b64_tr_b16 v[128:129], v5 offset:2944\n\t" \
  "v_mfma_f32_16x16x32_bf16 v[244:247], v[72:75], v[176:179], v[244:247]\n\t" \
  "ds_read_b64_tr_b16 v[130:131], v5 offset:2960\n\t" \
  "v_mfma_f32_16x16x32_bf16 v[244:247], v[104:107], v[180:183], v[244:247]\n\t" \
  "ds_read_b128 v[88:91], v9 offset:10240\n\t" \
  "v_mfma_f32_16x16x32_bf16 v[244:247], v[64:67], v[184:187], v[244:247]\n\t" \
  "v_mfma_f32_16x16x32_bf16 v[244:247], v[84:87], v[188:191], v[244:247]\n\t" \
  "v_mfma_f32_16x16x32_bf16 v[240:243], v[76:79], v[160:163], v[240:243]\n\t" \
  "v_mfma_f32_16x16x32_bf16 v[240:243], v[144:147], v[176:179], v[240:243]\n\t" \
  "v_mfma_f32_16x16x32_bf16 v[240:243], v[72:75], v[180:183], v[240:243]\n\t" \
  "ds_read_b64_tr_b16 v[136:137], v5 offset:3456\n\t" \
  "v_mfma_f32_16x16x32_bf16 v[240:243], v[104:107], v[184:187], v[240:243]\n\t" \
  "ds_read_b64_tr_b16 v[138:139], v5 offset:3472\n\t" \
  "v_mfma_f32_16x16x32_bf16 v[240:243], v[64:67], v[188:191], v[240:243]\n\t" \
  "ds_read_b128 v[84:87], v9 offset:9216\n\t" \
  "v_mfma_f32_16x16x32_bf16 v[236:239], v[76:79], v[176:179], v[236:239]\n\t" \
  "v_mfma_f32_16x16x32_bf16 v[236:239], v[144:147], v[180:183], v[236:239]\n\t" \
  "v_mfma_f32_16x16x32_bf16 v[236:239], v[72:75], v[184:187], v[236:239]\n\t" \
  "v_mfma_f32_16x16x32_bf16 v[236:239], v[104:107], v[188:191], v[236:239]\n\t" \
  "v_mfma_f32_16x16x32_bf16 v[232:235], v[76:79], v[180:183], v[232:235]\n\t" \
  "v_mfma_f32_16x16x32_bf16 v[232:235], v[144:147], v[184:187], v[232:235]\n\t" \
  "ds_read_b64_tr_b16 v[96:97], v5 offset:3968\n\t" \
  "v_mfma_f32_16x16x32_bf16 v[232:235], v[72:75], v[188:191], v[232:235]\n\t" \
  "ds_read_b64_tr_b16 v[98:99], v5 offset:3984\n\t" \
  "ds_write_b32 v18, v17 offset:176\n\t" \
  "v_mfma_f32_16x16x32_bf16 v[228:231], v[76:79], v[184:187], v[228:231]\n\t" \
  "ds_read_b128 v[100:103], v9 offset:7168\n\t" \
  "v_mfma_f32_16x16x32_bf16 v[228:231], v[144:147], v[188:191], v[228:231]\n\t" \
  "ds_read_b128 v[104:107], v9 offset:6144\n\t" \
  "v_mfma_f32_16x16x32_bf16 v[224:227], v[76:79], v[188:191], v[224:227]\n\t" \
  "ds_read_b128 v[144:147], v9 offset:5120\n\t" \
  "s_waitcnt lgkmcnt(6)\n\t" \
  "v_mfma_f32_16x16x32_bf16 v[192:195], v[148:151], v[80:83], v[192:195]\n\t" \
  "v_mfma_f32_16x16x32_bf16 v[196:199], v[148:151], v[140:143], v[196:199]\n\t" \
  "v_mfma_f32_16x16x32_bf16 v[196:199], v[132:135], v[80:83], v[196:199]\n\t" \
  "ds_read_b128 v[152:155], v9 offset:4096\n\t" \
  "v_mfma_f32_16x16x32_bf16 v[200:203], v[148:151], v[124:127], v[200:203]\n\t" \
  "v_mfma_f32_16x16x32_bf16 v[200:203], v[132:135], v[140:143], v[200:203]\n\t" \
  "v_mfma_f32_16x16x32_bf16 v[200:203], v[116:119], v[80:83], v[200:203]\n\t" \
  "v_mfma_f32_16x16x32_bf16 v[204:207], v[148:151], v[92:95], v[204:207]\n\t" \
  "ds_read_b128 v[156:159], v9 offset:3072\n\t" \
  "v_cndmask_b32 v20, v195, 0, s[76:77]\n\t" \
  "v_mfma_f32_16x16x32_bf16 v[204:207], v[132:135], v[124:127], v[204:207]\n\t" \
  "ds_bpermute_b32 v20, v15, v20\n\t" \
  "v_add_f32 v225, v225, v192\n\t" \
  "v_mfma_f32_16x16x32_bf16 v[204:207], v[116:119], v[140:143], v[204:207]\n\t" \
  "v_add_f32 v226, v226, v193\n\t" \
  "v_add_f32 v227, v227, v194\n\t" \
  "v_mfma_f32_16x16x32_bf16 v[204:207], v[108:111], v[80:83], v[204:207]\n\t" \
  "ds_read_b128 v[40:43], v56 offset:80\n\t" \
  "v_cndmask_b32 v21, v199, v195, s[76:77]\n\t" \
  "v_mfma_f32_16x16x32_bf16 v[208:211], v[148:151], v[112:115], v[208:211]\n\t" \
  "ds_bpermute_b32 v21, v15, v21\n\t" \
  "v_add_f32 v229, v229, v196\n\t" \
  "v_mfma_f32_16x16x32_bf16 v[208:211], v[132:135], v[92:95], v[208:211]\n\t" \
  "ds_read_b128 v[164:167], v9 offset:2048\n\t" \
  "v_add_f32 v230, v230, v197\n\t" \
  "v_mfma_f32_16x16x32_bf16 v[208:211], v[116:119], v[124:127], v[208:211]\n\t" \
  "v_add_f32 v231, v231, v198\n\t" \
  "v_cndmask_b32 v22, v203, v199, s[76:77]\n\t" \
  "v_mfma_f32_16x16x32_bf16 v[208:211], v[108:111], v[140:143], v[208:211]\n\t" \
  "ds_bpermute_b32 v22, v15, v22\n\t" \
  "v_add_f32 v233, v233, v200\n\t" \
  "v_mfma_f32_16x16x32_bf16 v[208:211], v[120:123], v[80:83], v[208:211]\n\t" \
  "v_add_f32 v234, v234, v201\n\t" \
  "v_add_f32 v235, v235, v202\n\t" \
  "v_mfma_f32_16x16x32_bf16 v[212:215], v[148:151], v[68:71], v[212:215]\n\t" \
  "s_waitcnt lgkmcnt(4)\n\t" \
  "v_add_f32 v224, v224, v20\n\t" \
  "v_cndmask_b32 v23, v207, v203, s[76:77]\n\t" \
  "v_mfma_f32_16x16x32_bf16 v[212:215], v[132:135], v[112:115], v[212:215]\n\t" \
  "s_waitcnt lgkmcnt(3)\n\t" \
  "v_min_u32 v40, v40, v41\n\t" \
  "v_min3_u32 v40, v40, v42, v43\n\t" \
  "v_mfma_f32_16x16x32_bf16 v[212:215], v[116:119], v[92:95], v[212:215]\n\t" \
  "v_readfirstlane_b32 vcc_lo, v40\n\t" \
  "ds_read_b128 v[168:171], v9 offset:1024\n\t" \
  "v_mfma_f32_16x16x32_bf16 v[212:215], v[108:111], v[124:127], v[212:215]\n\t" \
  "s_cmp_ge_u32 vcc_lo, 7\n\t" \
  "s_cbranch_scc0 30f\n\t" \
  "31:\n\t" \
  "v_mfma_f32_16x16x32_bf16 v[212:215], v[120:123], v[140:143], v[212:215]\n\t" \
  "ds_bpermute_b32 v23, v15, v23\n\t" \
  "v_add_f32 v237, v237, v204\n\t" \
  "v_mfma_f32_16x16x32_bf16 v[212:215], v[128:131], v[80:83], v[212:215]\n\t" \
  "v_add_f32 v238, v238, v205\n\t" \
  "v_add_f32 v239, v239, v206\n\t" \
  "v_mfma_f32_16x16x32_bf16 v[216:219], v[148:151], v[88:91], v[216:219]\n\t" \
  "s_waitcnt lgkmcnt(4)\n\t" \
  "v_add_f32 v228, v228, v21\n\t" \
  "s_waitcnt lgkmcnt(2)\n\t" \
  "v_add_f32 v232, v232, v22\n\t" \
  "v_mfma_f32_16x16x32_bf16 v[216:219], v[132:135], v[68:71], v[216:219]\n\t" \
  "v_cndmask_b32 v24, v211, v207, s[76:77]\n\t" \
  "ds_bpermute_b32 v24, v15, v24\n\t" \
  "v_mfma_f32_16x16x32_bf16 v[216:219], v[116:119], v[112:115], v[216:219]\n\t" \
  "v_add_f32 v241, v241, v208\n\t" \
  "v_add_f32 v242, v242, v209\n\t" \
  "v_mfma_f32_16x16x32_bf16 v[216:219], v[108:111], v[92:95], v[216:219]\n\t" \
  "ds_read_b128 v[172:175], v9 offset:0\n\t" \
  "ds_write_b32 v18, v17 offset:192\n\t" \
  "v_mfma_f32_16x16x32_bf16 v[216:219], v[120:123], v[124:127], v[216:219]\n\t" \
  "ds_read_b32 v16, v56 offset:96\n\t" \
  "v_add_f32 v243, v243, v210\n\t" \
  "v_mfma_f32_16x16x32_bf16 v[216:219], v[128:131], v[140:143], v[216:219]\n\t" \
  "v_cndmask_b32 v25, v215, v211, s[76:77]\n\t" \
  "ds_bpermute_b32 v25, v15, v25\n\t" \
  "v_mfma_f32_16x16x32_bf16 v[216:219], v[136:139], v[80:83], v[216:219]\n\t" \
  "s_waitcnt lgkmcnt(5)\n\t" \
  "v_add_f32 v236, v236, v23\n\t" \
  "v_add_f32 v245, v245, v212\n\t" \
  "v_mfma_f32_16x16x32_bf16 v[220:223], v[148:151], v[84:87], v[220:223]\n\t" \
  "v_add_f32 v246, v246, v213\n\t" \
  "v_add_f32 v247, v247, v214\n\t" \
  "v_mfma_f32_16x16x32_bf16 v[220:223], v[132:135], v[88:91], v[220:223]\n\t" \
  "ds_read_b64_tr_b16 v[192:193], v28 offset:22528\n\t" \
  "v_mfma_f32_16x16x32_bf16 v[220:223], v[116:119], v[68:71], v[220:223]\n\t" \
  "s_waitcnt lgkmcnt(5)\n\t" \
  "v_add_f32 v240, v240, v24\n\t" \
  "v_mfma_f32_16x16x32_bf16 v[220:223], v[108:111], v[112:115], v[220:223]\n\t" \
  "ds_read_b64_tr_b16 v[194:195], v28 offset:23040\n\t" \
  "v_mfma_f32_16x16x32_bf16 v[220:223], v[120:123], v[92:95], v[220:223]\n\t" \
  "s_waitcnt lgkmcnt(3)\n\t" \
  "v_readfirstlane_b32 s75, v16\n\t" \
  "v_mfma_f32_16x16x32_bf16 v[220:223], v[128:131], v[124:127], v[220:223]\n\t" \
  "s_cmp_lg_u32 s75, 0\n\t" \
  "s_cselect_b32 s69, -1, 0\n\t" \
  "v_mfma_f32_16x16x32_bf16 v[220:223], v[136:139], v[140:143], v[220:223]\n\t" \
  "s_lshr_b64 s[90:91], %18, 0\n\t" \
  "s_and_b32 s90, s90, 15\n\t" \
  "s_lshl_b32 s90, s90, 15\n\t" \
  "v_and_b32 v52, s69, v53\n\t" \
  "v_mfma_f32_16x16x32_bf16 v[220:223], v[96:99], v[80:83], v[220:223]\n\t" \
  "s_add_u32 s90, s90, s92\n\t" \
  "s_addc_u32 s91, s93, 0\n\t" \
  "s_cmp_lg_u32 s75, 0\n\t" \
  "v_mfma_f32_16x16x32_bf16 v[224:227], v[148:151], v[100:103], v[224:227]\n\t" \
  "s_cselect_b64 s[90:91], s[90:91], %11\n\t" \
  "v_cndmask_b32 v26, v219, v215, s[76:77]\n\t" \
  "v_mfma_f32_16x16x32_bf16 v[228:231], v[148:151], v[104:107], v[228:231]\n\t" \
  "global_load_dwordx4 v[64:67], v52, s[90:91] offset:0 sc1\n\t" \
  "ds_bpermute_b32 v26, v15, v26\n\t" \
  "v_mfma_f32_16x16x32_bf16 v[228:231], v[132:135], v[100:103], v[228:231]\n\t" \
  "v_add_f32 v249, v249, v216\n\t" \
  "v_add_f32 v250, v250, v217\n\t" \
  "v_mfma_f32_16x16x32_bf16 v[232:235], v[148:151], v[144:147], v[232:235]\n\t" \
  "v_add_f32 v251, v251, v218\n\t" \
  "s_waitcnt lgkmcnt(3)\n\t" \
  "v_add_f32 v244, v244, v25\n\t" \
  "v_mfma_f32_16x16x32_bf16 v[232:235], v[132:135], v[104:107], v[232:235]\n\t" \
  "global_load_dwordx4 v[68:71], v52, s[90:91] offset:1024 sc1\n\t" \
  "ds_read_b64_tr_b16 v[196:197], v28 offset:26624\n\t" \
  "v_mfma_f32_16x16x32_bf16 v[232:235], v[116:119], v[100:103], v[232:235]\n\t" \
  "v_cndmask_b32 v27, v223, v219, s[76:77]\n\t" \
  "ds_bpermute_b32 v27, v15, v27\n\t" \
  "508:\n\t" \
  "s_cbranch_execz 26b\n\t" \
  "v_mfma_f32_16x16x32_bf16 v[236:239], v[148:151], v[152:155], v[236:239]\n\t" \
  "v_add_f32 v253, v253, v220\n\t" \
  "v_add_f32 v254, v254, v221\n\t" \
  "v_mfma_f32_16x16x32_bf16 v[236:239], v[132:135], v[144:147], v[236:239]\n\t" \
  "global_load_dwordx4 v[72:75], v52, s[90:91] offset:2048 sc1\n\t" \
  "v_add_f32 v255, v255, v222\n\t" \
  "v_mfma_f32_16x16x32_bf16 v[236:239], v[116:119], v[104:107], v[236:239]\n\t" \
  "v_cvt_pk_bf16_f32 v45, v226, v227\n\t" \
  "v_cvt_pk_bf16_f32 v44, v224, v225\n\t" \
  "v_mfma_f32_16x16x32_bf16 v[236:239], v[108:111], v[100:103], v[236:239]\n\t" \
  "s_waitcnt lgkmcnt(2)\n\t" \
  "v_add_f32 v248, v248, v26\n\t" \
  "ds_write_b64 v14, v[44:45] offset:0\n\t" \
  "v_mfma_f32_16x16x32_bf16 v[240:243], v[148:151], v[156:159], v[240:243]\n\t" \
  "global_load_dwordx4 v[76:79], v52, s[90:91] offset:3072 sc1\n\t" \
  "v_cvt_pk_bf16_f32 v47, v230, v231\n\t" \
  "v_mfma_f32_16x16x32_bf16 v[240:243], v[132:135], v[152:155], v[240:243]\n\t" \
  "v_cvt_pk_bf16_f32 v46, v228, v229\n\t" \
  "v_cvt_pk_bf16_f32 v49, v234, v235\n\t" \
  "v_mfma_f32_16x16x32_bf16 v[240:243], v[116:119], v[144:147], v[240:243]\n\t" \
  "ds_write_b64 v14, v[46:47] offset:512\n\t" \
  "v_cvt_pk_bf16_f32 v48, v232, v233\n\t" \
  "v_mfma_f32_16x16x32_bf16 v[240:243], v[108:111], v[104:107], v[240:243]\n\t" \
  "ds_write_b64 v14, v[48:49] offset:1024\n\t" \
  "s_waitcnt lgkmcnt(3)\n\t" \
  "v_add_f32 v252, v252, v27\n\t" \
  "v_mfma_f32_16x16x32_bf16 v[240:243], v[120:123], v[100:103], v[240:243]\n\t" \
  "s_lshr_b64 s[90:91], %18, 4\n\t" \
  "s_and_b32 s90, s90, 15\n\t" \
  "s_lshl_b32 s90, s90, 15\n\t" \
  "v_mfma_f32_16x16x32_bf16 v[244:247], v[148:151], v[164:167], v[244:247]\n\t" \
  "s_add_u32 s90, s90, s92\n\t" \
  "s_addc_u32 s91, s93, 0\n\t" \
  "s_cmp_lg_u32 s75, 0\n\t" \
  "v_mfma_f32_16x16x32_bf16 v[244:247], v[132:135], v[156:159], v[244:247]\n\t" \
  "s_cselect_b64 s[90:91], s[90:91], %11\n\t" \
  "v_cvt_pk_bf16_f32 v51, v238, v239\n\t" \
  "v_mfma_f32_16x16x32_bf16 v[244:247], v[116:119], v[152:155], v[244:247]\n\t" \
  "global_load_dwordx4 v[80:83], v52, s[90:91] offset:0 sc1\n\t" \
  "v_cvt_pk_bf16_f32 v50, v236, v237\n\t" \
  "v_mfma_f32_16x16x32_bf16 v[244:247], v[108:111], v[144:147], v[244:247]\n\t" \
  "ds_write_b64 v14, v[50:51] offset:1536\n\t" \
  "ds_read_b64_tr_b16 v[198:199], v28 offset:27136\n\t" \
  "v_mfma_f32_16x16x32_bf16 v[244:247], v[120:123], v[104:107], v[244:247]\n\t" \
  "ds_read_b64_tr_b16 v[200:201], v29 offset:22528\n\t" \
  "v_mfma_f32_16x16x32_bf16 v[244:247], v[128:131], v[100:103], v[244:247]\n\t" \
  "global_load_dwordx4 v[84:87], v52, s[90:91] offset:1024 sc1\n\t" \
  "v_cvt_pk_bf16_f32 v45, v242, v243\n\t" \
  "v_mfma_f32_16x16x32_bf16 v[248:251], v[148:151], v[168:171], v[248:251]\n\t" \
  "v_cvt_pk_bf16_f32 v44, v240, v241\n\t" \
  "ds_read_b64_tr_b16 v[202:203], v29 offset:23040\n\t" \
  "v_mfma_f32_16x16x32_bf16 v[248:251], v[132:135], v[164:167], v[248:251]\n\t" \
  "ds_write_b64 v14, v[44:45] offset:2048\n\t" \
  "ds_read_b64_tr_b16 v[204:205], v29 offset:26624\n\t" \
  "v_mfma_f32_16x16x32_bf16 v[248:251], v[116:119], v[156:159], v[248:251]\n\t" \
  "global_load_dwordx4 v[88:91], v52, s[90:91] offset:2048 sc1\n\t" \
  "ds_read_b64_tr_b16 v[206:207], v29 offset:27136\n\t" \
  "v_mfma_f32_16x16x32_bf16 v[248:251], v[108:111], v[152:155], v[248:251]\n\t" \
  "ds_read_b64_tr_b16 v[208:209], v30 offset:22528\n\t" \
  "v_mfma_f32_16x16x32_bf16 v[248:251], v[120:123], v[144:147], v[248:251]\n\t" \
  "ds_read_b64_tr_b16 v[210:211], v30 offset:23040\n\t" \
  "v_mfma_f32_16x16x32_bf16 v[248:251], v[128:131], v[104:107], v[248:251]\n\t" \
  "global_load_dwordx4 v[92:95], v52, s[90:91] offset:3072 sc1\n\t" \
  "v_cvt_pk_bf16_f32 v47, v246, v247\n\t" \
  "v_mfma_f32_16x16x32_bf16 v[248:251], v[136:139], v[100:103], v[248:251]\n\t" \
  "v_cvt_pk_bf16_f32 v46, v244, v245\n\t" \
  "ds_read_b64_tr_b16 v[212:213], v30 offset:26624\n\t" \
  "v_mfma_f32_16x16x32_bf16 v[252:255], v[148:151], v[172:175], v[252:255]\n\t" \
  "ds_write_b64 v14, v[46:47] offset:2560\n\t" \
  "ds_read_b64_tr_b16 v[214:215], v30 offset:27136\n\t" \
  "v_mfma_f32_16x16x32_bf16 v[252:255], v[132:135], v[168:171], v[252:255]\n\t" \
  "ds_read_b64_tr_b16 v[216:217], v31 offset:22528\n\t" \
  "v_mfma_f32_16x16x32_bf16 v[252:255], v[116:119], v[164:167], v[252:255]\n\t" \
  "ds_read_b64_tr_b16 v[218:219], v31 offset:23040\n\t" \
  "v_mfma_f32_16x16x32_bf16 v[252:255], v[108:111], v[156:159], v[252:255]\n\t" \
  "ds_read_b64_tr_b16 v[220:221], v31 offset:26624\n\t" \
  "v_mfma_f32_16x16x32_bf16 v[252:255], v[120:123], v[152:155], v[252:255]\n\t" \
  "v_mov_b32 v19, 15\n\t" \
  "ds_read_b64_tr_b16 v[222:223], v31 offset:27136\n\t" \
  "v_mfma_f32_16x16x32_bf16 v[252:255], v[128:131], v[144:147], v[252:255]\n\t" \
  "v_cvt_pk_bf16_f32 v49, v250, v251\n\t" \
  "v_cvt_pk_bf16_f32 v48, v248, v249\n\t" \
  "v_mfma_f32_16x16x32_bf16 v[252:255], v[136:139], v[104:107], v[252:255]\n\t" \
  "ds_write_b64 v14, v[48:49] offset:3072\n\t" \
  "v_mfma_f32_16x16x32_bf16 v[252:255], v[96:99], v[100:103], v[252:255]\n\t" \
  "ds_read_b32 v16, v56 offset:96\n\t" \
  "s_nop 6\n\t" \
  "v_cvt_pk_bf16_f32 v51, v254, v255\n\t" \
  "v_cvt_pk_bf16_f32 v50, v252, v253\n\t" \
  "ds_write_b64 v14, v[50:51] offset:3584\n\t" \
  "ds_write_b32 v18, v19 offset:32\n\t" \
  "s_cmp_eq_u32 s75, 0\n\t" \
  "s_cbranch_scc1 38f\n\t" \
  "39:\n\t" \
  "s_waitcnt vmcnt(6)\n\t" \
  "v_mfma_f32_16x16x32_bf16 v[224:227], v[192:195], v[64:67], 0\n\t" \
  "s_waitcnt lgkmcnt(6)\n\t" \
  "v_mfma_f32_16x16x32_bf16 v[224:227], v[196:199], v[68:71], v[224:227]\n\t" \
  "s_lshr_b64 s[90:91], %18, 8\n\t" \
  "s_and_b32 s90, s90, 15\n\t" \
  "s_lshl_b32 s90, s90, 15\n\t" \
  "s_add_u32 s90, s90, s92\n\t" \
  "s_addc_u32 s91, s93, 0\n\t" \
  "global_load_dwordx4 v[96:99], v53, s[90:91] offset:0 sc1\n\t" \
  "s_waitcnt vmcnt(5)\n\t" \
  "v_mfma_f32_16x16x32_bf16 v[228:231], v[196:199], v[76:79], 0\n\t" \
  "v_mfma_f32_16x16x32_bf16 v[228:231], v[192:195], v[72:75], v[228:231]\n\t" \
  "global_load_dwordx4 v[100:103], v53, s[90:91] offset:1024 sc1\n\t" \
  "v_mfma_f32_16x16x32_bf16 v[232:235], v[200:203], v[64:67], 0\n\t" \
  "ds_read_b64_tr_b16 v[192:193], v28 offset:30720\n\t" \
  "v_mfma_f32_16x16x32_bf16 v[232:235], v[204:207], v[68:71], v[232:235]\n\t" \
  "ds_read_b64_tr_b16 v[194:195], v28 offset:31232\n\t" \
  "global_load_dwordx4 v[104:107], v53, s[90:91] offset:2048 sc1\n\t" \
  "v_mfma_f32_16x16x32_bf16 v[236:239], v[204:207], v[76:79], 0\n\t" \
  "ds_read_b64_tr_b16 v[196:197], v28 offset:34816\n\t" \
  "v_mfma_f32_16x16x32_bf16 v[236:239], v[200:203], v[72:75], v[236:239]\n\t" \
  "ds_read_b64_tr_b16 v[198:199], v28 offset:35328\n\t" \
  "global_load_dwordx4 v[108:111], v53, s[90:91] offset:3072 sc1\n\t" \
  "v_mfma_f32_16x16x32_bf16 v[240:243], v[208:211], v[64:67], 0\n\t" \
  "ds_read_b64_tr_b16 v[200:201], v29 offset:30720\n\t" \
  "v_mfma_f32_16x16x32_bf16 v[240:243], v[212:215], v[68:71], v[240:243]\n\t" \
  "ds_read_b64_tr_b16 v[202:203], v29 offset:31232\n\t" \
  "s_lshr_b64 s[90:91], %18, 12\n\t" \
  "s_and_b32 s90, s90, 15\n\t" \
  "s_lshl_b32 s90, s90, 15\n\t" \
  "s_add_u32 s90, s90, s92\n\t" \
  "s_addc_u32 s91, s93, 0\n\t" \
  "global_load_dwordx4 v[112:115], v53, s[90:91] offset:0 sc1\n\t" \
  "v_mfma_f32_16x16x32_bf16 v[244:247], v[212:215], v[76:79], 0\n\t" \
  "ds_read_b64_tr_b16 v[204:205], v29 offset:34816\n\t" \
  "v_mfma_f32_16x16x32_bf16 v[244:247], v[208:211], v[72:75], v[244:247]\n\t" \
  "ds_read_b64_tr_b16 v[206:207], v29 offset:35328\n\t" \
  "global_load_dwordx4 v[116:119], v53, s[90:91] offset:1024 sc1\n\t" \
  "v_mfma_f32_16x16x32_bf16 v[248:251], v[216:219], v[64:67], 0\n\t" \
  "ds_read_b64_tr_b16 v[208:209], v30 offset:30720\n\t" \
  "s_waitcnt lgkmcnt(6)\n\t" \
  "v_mfma_f32_16x16x32_bf16 v[248:251], v[220:223], v[68:71], v[248:251]\n\t" \
  "ds_read_b64_tr_b16 v[210:211], v30 offset:31232\n\t" \
  "global_load_dwordx4 v[120:123], v53, s[90:91] offset:2048 sc1\n\t" \
  "v_mfma_f32_16x16x32_bf16 v[252:255], v[220:223], v[76:79], 0\n\t" \
  "ds_read_b64_tr_b16 v[212:213], v30 offset:34816\n\t" \
  "v_mfma_f32_16x16x32_bf16 v[252:255], v[216:219], v[72:75], v[252:255]\n\t" \
  "ds_read_b64_tr_b16 v[214:215], v30 offset:35328\n\t" \
  "global_load_dwordx4 v[124:127], v53, s[90:91] offset:3072 sc1\n\t" \
  "s_waitcnt vmcnt(10)\n\t" \
  "v_mfma_f32_16x16x32_bf16 v[224:227], v[192:195], v[80:83], v[224:227]\n\t" \
  "ds_read_b64_tr_b16 v[216:217], v31 offset:30720\n\t" \
  "s_lshr_b64 s[90:91], %18, 16\n\t" \
  "s_and_b32 s90, s90, 15\n\t" \
  "s_lshl_b32 s90, s90, 15\n\t" \
  "s_waitcnt lgkmcnt(6)\n\t" \
  "v_mfma_f32_16x16x32_bf16 v[224:227], v[196:199], v[84:87], v[224:227]\n\t" \
  "ds_read_b64_tr_b16 v[218:219], v31 offset:31232\n\t" \
  "s_add_u32 s90, s90, s92\n\t" \
  "s_addc_u32 s91, s93, 0\n\t" \
  "s_waitcnt vmcnt(8)\n\t" \
  "v_mfma_f32_16x16x32_bf16 v[228:231], v[196:199], v[92:95], v[228:231]\n\t" \
  "ds_read_b64_tr_b16 v[220:221], v31 offset:34816\n\t" \
  "global_load_dwordx4 v[128:131], v53, s[90:91] offset:0 sc1\n\t" \
  "v_mfma_f32_16x16x32_bf16 v[228:231], v[192:195], v[88:91], v[228:231]\n\t" \
  "ds_read_b64_tr_b16 v[222:223], v31 offset:35328\n\t" \
  "v_mfma_f32_16x16x32_bf16 v[232:235], v[200:203], v[80:83], v[232:235]\n\t" \
  "ds_read_b64_tr_b16 v[192:193], v28 offset:38912\n\t" \
  "s_waitcnt lgkmcnt(6)\n\t" \
  "v_mfma_f32_16x16x32_bf16 v[232:235], v[204:207], v[84:87], v[232:235]\n\t" \
  "ds_read_b64_tr_b16 v[194:195], v28 offset:39424\n\t" \
  "ds_write_b32 v18, v17 offset:160\n\t" \
  "v_mfma_f32_16x16x32_bf16 v[236:239], v[204:207], v[92:95], v[236:239]\n\t" \
  "ds_read_b64_tr_b16 v[196:197], v28 offset:43008\n\t" \
  "global_load_dwordx4 v[132:135], v53, s[90:91] offset:1024 sc1\n\t" \
  "v_mfma_f32_16x16x32_bf16 v[236:239], v[200:203], v[88:91], v[236:239]\n\t" \
  "ds_read_b64_tr_b16 v[198:199], v28 offset:43520\n\t" \
  "v_mfma_f32_16x16x32_bf16 v[240:243], v[208:211], v[80:83], v[240:243]\n\t" \
  "ds_read_b64_tr_b16 v[200:201], v29 offset:38912\n\t" \
  "s_waitcnt lgkmcnt(7)\n\t" \
  "v_mfma_f32_16x16x32_bf16 v[240:243], v[212:215], v[84:87], v[240:243]\n\t" \
  "ds_read_b64_tr_b16 v[202:203], v29 offset:39424\n\t" \
  "v_mfma_f32_16x16x32_bf16 v[244:247], v[212:215], v[92:95], v[244:247]\n\t" \
  "ds_read_b64_tr_b16 v[204:205], v29 offset:43008\n\t" \
  "global_load_dwordx4 v[136:139], v53, s[90:91] offset:2048 sc1\n\t" \
  "v_mfma_f32_16x16x32_bf16 v[244:247], v[208:211], v[88:91], v[244:247]\n\t" \
  "ds_read_b64_tr_b16 v[206:207], v29 offset:43520\n\t" \
  "v_mfma_f32_16x16x32_bf16 v[248:251], v[216:219], v[80:83], v[248:251]\n\t" \
  "ds_read_b64_tr_b16 v[208:209], v30 offset:38912\n\t" \
  "s_waitcnt lgkmcnt(6)\n\t" \
  "v_mfma_f32_16x16x32_bf16 v[248:251], v[220:223], v[84:87], v[248:251]\n\t" \
  "ds_read_b64_tr_b16 v[210:211], v30 offset:39424\n\t" \
  "v_mfma_f32_16x16x32_bf16 v[252:255], v[220:223], v[92:95], v[252:255]\n\t" \
  "ds_read_b64_tr_b16 v[212:213], v30 offset:43008\n\t" \
  "global_load_dwordx4 v[140:143], v53, s[90:91] offset:3072 sc1\n\t" \
  "v_mfma_f32_16x16x32_bf16 v[252:255], v[216:219], v[88:91], v[252:255]\n\t" \
  "ds_read_b64_tr_b16 v[214:215], v30 offset:43520\n\t" \
  "s_waitcnt vmcnt(10)\n\t" \
  "v_mfma_f32_16x16x32_bf16 v[224:227], v[192:195], v[96:99], v[224:227]\n\t" \
  "ds_read_b64_tr_b16 v[216:217], v31 offset:38912\n\t" \
  "s_lshr_b64 s[90:91], %18, 20\n\t" \
  "s_and_b32 s90, s90, 15\n\t" \
  "s_lshl_b32 s90, s90, 15\n\t" \
  "s_waitcnt lgkmcnt(6)\n\t" \
  "v_mfma_f32_16x16x32_bf16 v[224:227], v[196:199], v[100:103], v[224:227]\n\t" \
  "ds_read_b64_tr_b16 v[218:219], v31 offset:39424\n\t" \
  "s_add_u32 s90, s90, s92\n\t" \
  "s_addc_u32 s91, s93, 0\n\t" \
  "s_waitcnt vmcnt(8)\n\t" \
  "v_mfma_f32_16x16x32_bf16 v[228:231], v[196:199], v[108:111], v[228:231]\n\t" \
  "ds_read_b64_tr_b16 v[220:221], v31 offset:43008\n\t" \
  "global_load_dwordx4 v[144:147], v53, s[90:91] offset:0 sc1\n\t" \
  "v_mfma_f32_16x16x32_bf16 v[228:231], v[192:195], v[104:107], v[228:231]\n\t" \
  "ds_read_b64_tr_b16 v[222:223], v31 offset:43520\n\t" \
  "v_mfma_f32_16x16x32_bf16 v[232:235], v[200:203], v[96:99], v[232:235]\n\t" \
  "ds_read_b64_tr_b16 v[192:193], v28 offset:47104\n\t" \
  "s_waitcnt lgkmcnt(6)\n\t" \
  "v_mfma_f32_16x16x32_bf16 v[232:235], v[204:207], v[100:103], v[232:235]\n\t" \
  "ds_read_b64_tr_b16 v[194:195], v28 offset:47616\n\t" \
  "v_mfma_f32_16x16x32_bf16 v[236:239], v[204:207], v[108:111], v[236:239]\n\t" \
  "ds_read_b64_tr_b16 v[196:197], v28 offset:51200\n\t" \
  "global_load_dwordx4 v[148:151], v53, s[90:91] offset:1024 sc1\n\t" \
  "v_mfma_f32_16x16x32_bf16 v[236:239], v[200:203], v[104:107], v[236:239]\n\t" \
  "ds_read_b64_tr_b16 v[198:199], v28 offset:51712\n\t" \
  "v_mfma_f32_16x16x32_bf16 v[240:243], v[208:211], v[96:99], v[240:243]\n\t" \
  "ds_read_b64_tr_b16 v[200:201], v29 offset:47104\n\t" \
  "s_waitcnt lgkmcnt(6)\n\t" \
  "v_mfma_f32_16x16x32_bf16 v[240:243], v[212:215], v[100:103], v[240:243]\n\t" \
  "ds_read_b64_tr_b16 v[202:203], v29 offset:47616\n\t" \
  "v_mfma_f32_16x16x32_bf16 v[244:247], v[212:215], v[108:111], v[244:247]\n\t" \
  "ds_read_b64_tr_b16 v[204:205], v29 offset:51200\n\t" \
  "global_load_dwordx4 v[152:155], v53, s[90:91] offset:2048 sc1\n\t" \
  "v_mfma_f32_16x16x32_bf16 v[244:247], v[208:211], v[104:107], v[244:247]\n\t" \
  "ds_read_b64_tr_b16 v[206:207], v29 offset:51712\n\t" \
  "v_mfma_f32_16x16x32_bf16 v[248:251], v[216:219], v[96:99], v[248:251]\n\t" \
  "ds_read_b64_tr_b16 v[208:209], v30 offset:47104\n\t" \
  "s_waitcnt lgkmcnt(6)\n\t" \
  "v_mfma_f32_16x16x32_bf16 v[248:251], v[220:223], v[100:103], v[248:251]\n\t" \
  "ds_read_b64_tr_b16 v[210:211], v30 offset:47616\n\t" \
  "v_mfma_f32_16x16x32_bf16 v[252:255], v[220:223], v[108:111], v[252:255]\n\t" \
  "ds_read_b64_tr_b16 v[212:213], v30 offset:51200\n\t" \
  "global_load_dwordx4 v[156:159], v53, s[90:91] offset:3072 sc1\n\t" \
  "v_mfma_f32_16x16x32_bf16 v[252:255], v[216:219], v[104:107], v[252:255]\n\t" \
  "ds_read_b64_tr_b16 v[214:215], v30 offset:51712\n\t" \
  "s_waitcnt vmcnt(10)\n\t" \
  "v_mfma_f32_16x16x32_bf16 v[224:227], v[192:195], v[112:115], v[224:227]\n\t" \
  "ds_read_b64_tr_b16 v[216:217], v31 offset:47104\n\t" \
  "s_lshr_b64 s[90:91], %18, 24\n\t" \
  "s_and_b32 s90, s90, 15\n\t" \
  "s_lshl_b32 s90, s90, 15\n\t" \
  "s_waitcnt lgkmcnt(6)\n\t" \
  "v_mfma_f32_16x16x32_bf16 v[224:227], v[196:199], v[116:119], v[224:227]\n\t" \
  "ds_read_b64_tr_b16 v[218:219], v31 offset:47616\n\t" \
  "s_add_u32 s90, s90, s92\n\t" \
  "s_addc_u32 s91, s93, 0\n\t" \
  "s_waitcnt vmcnt(8)\n\t" \
  "v_mfma_f32_16x16x32_bf16 v[228:231], v[196:199], v[124:127], v[228:231]\n\t" \
  "ds_read_b64_tr_b16 v[220:221], v31 offset:51200\n\t" \
  "global_load_dwordx4 v[160:163], v53, s[90:91] offset:0 sc1\n\t" \
  "v_mfma_f32_16x16x32_bf16 v[228:231], v[192:195], v[120:123], v[228:231]\n\t" \
  "ds_read_b64_tr_b16 v[222:223], v31 offset:51712\n\t" \
  "v_mfma_f32_16x16x32_bf16 v[232:235], v[200:203], v[112:115], v[232:235]\n\t" \
  "ds_read_b64_tr_b16 v[192:193], v28 offset:55296\n\t" \
  "s_waitcnt lgkmcnt(6)\n\t" \
  "v_mfma_f32_16x16x32_bf16 v[232:235], v[204:207], v[116:119], v[232:235]\n\t" \
  "ds_read_b64_tr_b16 v[194:195], v28 offset:55808\n\t" \
  "v_mfma_f32_16x16x32_bf16 v[236:239], v[204:207], v[124:127], v[236:239]\n\t" \
  "ds_read_b64_tr_b16 v[196:197], v28 offset:59392\n\t" \
  "global_load_dwordx4 v[164:167], v53, s[90:91] offset:1024 sc1\n\t" \
  "ds_read_b32 v16, v56 offset:96\n\t" \
  "v_mfma_f32_16x16x32_bf16 v[236:239], v[200:203], v[120:123], v[236:239]\n\t" \
  "ds_read_b64_tr_b16 v[198:199], v28 offset:59904\n\t" \
  "v_mfma_f32_16x16x32_bf16 v[240:243], v[208:211], v[112:115], v[240:243]\n\t" \
  "ds_read_b64_tr_b16 v[200:201], v29 offset:55296\n\t" \
  "s_waitcnt lgkmcnt(7)\n\t" \
  "v_mfma_f32_16x16x32_bf16 v[240:243], v[212:215], v[116:119], v[240:243]\n\t" \
  "ds_read_b64_tr_b16 v[202:203], v29 offset:55808\n\t" \
  "v_mfma_f32_16x16x32_bf16 v[244:247], v[212:215], v[124:127], v[244:247]\n\t" \
  "ds_read_b64_tr_b16 v[204:205], v29 offset:59392\n\t" \
  "global_load_dwordx4 v[168:171], v53, s[90:91] offset:2048 sc1\n\t" \
  "v_mfma_f32_16x16x32_bf16 v[244:247], v[208:211], v[120:123], v[244:247]\n\t" \
  "ds_read_b64_tr_b16 v[206:207], v29 offset:59904\n\t" \
  "v_mfma_f32_16x16x32_bf16 v[248:251], v[216:219], v[112:115], v[248:251]\n\t" \
  "ds_read_b64_tr_b16 v[208:209], v30 offset:55296\n\t" \
  "s_waitcnt lgkmcnt(6)\n\t" \
  "v_mfma_f32_16x16x32_bf16 v[248:251], v[220:223], v[116:119], v[248:251]\n\t" \
  "ds_read_b64_tr_b16 v[210:211], v30 offset:55808\n\t" \
  "v_mfma_f32_16x16x32_bf16 v[252:255], v[220:223], v[124:127], v[252:255]\n\t" \
  "ds_read_b64_tr_b16 v[212:213], v30 offset:59392\n\t" \
  "global_load_dwordx4 v[172:175], v53, s[90:91] offset:3072 sc1\n\t" \
  "v_readfirstlane_b32 vcc_lo, v16\n\t" \
  "v_mfma_f32_16x16x32_bf16 v[252:255], v[216:219], v[120:123], v[252:255]\n\t" \
  "ds_read_b64_tr_b16 v[214:215], v30 offset:59904\n\t" \
  "s_cmp_ge_u32 vcc_lo, 2\n\t" \
  "s_cbranch_scc0 43f\n\t" \
  "44:\n\t" \
  "s_waitcnt vmcnt(10)\n\t" \
  "v_mfma_f32_16x16x32_bf16 v[224:227], v[192:195], v[128:131], v[224:227]\n\t" \
  "ds_read_b64_tr_b16 v[216:217], v31 offset:55296\n\t" \
  "s_lshr_b64 s[90:91], %18, 28\n\t" \
  "s_and_b32 s90, s90, 15\n\t" \
  "s_lshl_b32 s90, s90, 15\n\t" \
  "s_waitcnt lgkmcnt(6)\n\t" \
  "v_mfma_f32_16x16x32_bf16 v[224:227], v[196:199], v[132:135], v[224:227]\n\t" \
  "ds_read_b64_tr_b16 v[218:219], v31 offset:55808\n\t" \
  "s_add_u32 s90, s90, s92\n\t" \
  "s_addc_u32 s91, s93, 0\n\t" \
  "s_waitcnt vmcnt(8)\n\t" \
  "v_mfma_f32_16x16x32_bf16 v[228:231], v[196:199], v[140:143], v[228:231]\n\t" \
  "ds_read_b64_tr_b16 v[220:221], v31 offset:59392\n\t" \
  "global_load_dwordx4 v[176:179], v53, s[90:91] offset:0 sc1\n\t" \
  "v_mfma_f32_16x16x32_bf16 v[228:231], v[192:195], v[136:139], v[228:231]\n\t" \
  "ds_read_b64_tr_b16 v[222:223], v31 offset:59904\n\t" \
  "v_mfma_f32_16x16x32_bf16 v[232:235], v[200:203], v[128:131], v[232:235]\n\t" \
  "ds_read_b64_tr_b16 v[192:193], v28 offset:63488\n\t" \
  "s_waitcnt lgkmcnt(6)\n\t" \
  "v_mfma_f32_16x16x32_bf16 v[232:235], v[204:207], v[132:135], v[232:235]\n\t" \
  "ds_read_b64_tr_b16 v[194:195], v28 offset:64000\n\t" \
  "v_mfma_f32_16x16x32_bf16 v[236:239], v[204:207], v[140:143], v[236:239]\n\t" \
  "ds_read_b64_tr_b16 v[196:197], v32 offset:2048\n\t" \
  "global_load_dwordx4 v[180:183], v53, s[90:91] offset:1024 sc1\n\t" \
  "v_mfma_f32_16x16x32_bf16 v[236:239], v[200:203], v[136:139], v[236:239]\n\t" \
  "ds_read_b64_tr_b16 v[198:199], v32 offset:2560\n\t" \
  "v_mfma_f32_16x16x32_bf16 v[240:243], v[208:211], v[128:131], v[240:243]\n\t" \
  "ds_read_b64_tr_b16 v[200:201], v29 offset:63488\n\t" \
  "s_waitcnt lgkmcnt(6)\n\t" \
  "v_mfma_f32_16x16x32_bf16 v[240:243], v[212:215], v[132:135], v[240:243]\n\t" \
  "ds_read_b64_tr_b16 v[202:203], v29 offset:64000\n\t" \
  "v_mfma_f32_16x16x32_bf16 v[244:247], v[212:215], v[140:143], v[244:247]\n\t" \
  "ds_read_b64_tr_b16 v[204:205], v33 offset:2048\n\t" \
  "global_load_dwordx4 v[184:187], v53, s[90:91] offset:2048 sc1\n\t" \
  "v_mfma_f32_16x16x32_bf16 v[244:247], v[208:211], v[136:139], v[244:247]\n\t" \
  "ds_read_b64_tr_b16 v[206:207], v33 offset:2560\n\t" \
  "v_mfma_f32_16x16x32_bf16 v[248:251], v[216:219], v[128:131], v[248:251]\n\t" \
  "ds_read_b64_tr_b16 v[208:209], v30 offset:63488\n\t" \
  "s_waitcnt lgkmcnt(6)\n\t" \
  "v_mfma_f32_16x16x32_bf16 v[248:251], v[220:223], v[132:135], v[248:251]\n\t" \
  "ds_read_b64_tr_b16 v[210:211], v30 offset:64000\n\t" \
  "v_mfma_f32_16x16x32_bf16 v[252:255], v[220:223], v[140:143], v[252:255]\n\t" \
  "ds_read_b64_tr_b16 v[212:213], v34 offset:2048\n\t" \
  "global_load_dwordx4 v[188:191], v53, s[90:91] offset:3072 sc1\n\t" \
  "v_mfma_f32_16x16x32_bf16 v[252:255], v[216:219], v[136:139], v[252:255]\n\t" \
  "ds_read_b64_tr_b16 v[214:215], v34 offset:2560\n\t" \
  "s_waitcnt vmcnt(10)\n\t" \
  "v_mfma_f32_16x16x32_bf16 v[224:227], v[192:195], v[144:147], v[224:227]\n\t" \
  "ds_read_b64_tr_b16 v[216:217], v31 offset:63488\n\t" \
  "s_lshr_b64 s[90:91], %18, 32\n\t" \
  "s_and_b32 s90, s90, 15\n\t" \
  "s_lshl_b32 s90, s90, 15\n\t" \
  "s_waitcnt lgkmcnt(6)\n\t" \
  "v_mfma_f32_16x16x32_bf16 v[224:227], v[196:199], v[148:151], v[224:227]\n\t" \
  "ds_read_b64_tr_b16 v[218:219], v31 offset:64000\n\t" \
  "s_add_u32 s90, s90, s92\n\t" \
  "s_addc_u32 s91, s93, 0\n\t" \
  "s_waitcnt vmcnt(8)\n\t" \
  "v_mfma_f32_16x16x32_bf16 v[228:231], v[196:199], v[156:159], v[228:231]\n\t" \
  "ds_read_b64_tr_b16 v[220:221], v35 offset:2048\n\t" \
  "global_load_dwordx4 v[64:67], v53, s[90:91] offset:0 sc1\n\t" \
  "v_mfma_f32_16x16x32_bf16 v[228:231], v[192:195], v[152:155], v[228:231]\n\t" \
  "ds_read_b64_tr_b16 v[222:223], v35 offset:2560\n\t" \
  "v_mfma_f32_16x16x32_bf16 v[232:235], v[200:203], v[144:147], v[232:235]\n\t" \
  "ds_read_b64_tr_b16 v[192:193], v32 offset:6144\n\t" \
  "s_waitcnt lgkmcnt(6)\n\t" \
  "v_mfma_f32_16x16x32_bf16 v[232:235], v[204:207], v[148:151], v[232:235]\n\t" \
  "ds_read_b64_tr_b16 v[194:195], v32 offset:6656\n\t" \
  "v_mfma_f32_16x16x32_bf16 v[236:239], v[204:207], v[156:159], v[236:239]\n\t" \
  "ds_read_b64_tr_b16 v[196:197], v32 offset:10240\n\t" \
  "global_load_dwordx4 v[68:71], v53, s[90:91] offset:1024 sc1\n\t" \
  "v_mfma_f32_16x16x32_bf16 v[236:239], v[200:203], v[152:155], v[236:239]\n\t" \
  "ds_read_b64_tr_b16 v[198:199], v32 offset:10752\n\t" \
  "v_mfma_f32_16x16x32_bf16 v[240:243], v[208:211], v[144:147], v[240:243]\n\t" \
  "ds_read_b64_tr_b16 v[200:201], v33 offset:6144\n\t" \
  "s_waitcnt lgkmcnt(6)\n\t" \
  "v_mfma_f32_16x16x32_bf16 v[240:243], v[212:215], v[148:151], v[240:243]\n\t" \
  "ds_read_b128 v[40:43], v56 offset:80\n\t" \
  "ds_read_b64_tr_b16 v[202:203], v33 offset:6656\n\t" \
  "v_mfma_f32_16x16x32_bf16 v[244:247], v[212:215], v[156:159], v[244:247]\n\t" \
  "ds_read_b64_tr_b16 v[204:205], v33 offset:10240\n\t" \
  "global_load_dwordx4 v[72:75], v53, s[90:91] offset:2048 sc1\n\t" \
  "v_mfma_f32_16x16x32_bf16 v[244:247], v[208:211], v[152:155], v[244:247]\n\t" \
  "ds_read_b64_tr_b16 v[206:207], v33 offset:10752\n\t" \
  "v_mfma_f32_16x16x32_bf16 v[248:251], v[216:219], v[144:147], v[248:251]\n\t" \
  "ds_read_b64_tr_b16 v[208:209], v34 offset:6144\n\t" \
  "s_waitcnt lgkmcnt(7)\n\t" \
  "v_mfma_f32_16x16x32_bf16 v[248:251], v[220:223], v[148:151], v[248:251]\n\t" \
  "ds_read_b64_tr_b16 v[210:211], v34 offset:6656\n\t" \
  "v_mfma_f32_16x16x32_bf16 v[252:255], v[220:223], v[156:159], v[252:255]\n\t" \
  "ds_read_b64_tr_b16 v[212:213], v34 offset:10240\n\t" \
  "global_load_dwordx4 v[76:79], v53, s[90:91] offset:3072 sc1\n\t" \
  "v_mfma_f32_16x16x32_bf16 v[252:255], v[216:219], v[152:155], v[252:255]\n\t" \
  "ds_read_b64_tr_b16 v[214:215], v34 offset:10752\n\t" \
  "s_waitcnt vmcnt(10)\n\t" \
  "v_mfma_f32_16x16x32_bf16 v[224:227], v[192:195], v[160:163], v[224:227]\n\t" \
  "s_waitcnt lgkmcnt(7)\n\t" \
  "v_min_u32 v40, v40, v41\n\t" \
  "v_min3_u32 v40, v40, v42, v43\n\t" \
  "ds_read_b64_tr_b16 v[216:217], v35 offset:6144\n\t" \
  "s_lshr_b64 s[90:91], %18, 36\n\t" \
  "s_and_b32 s90, s90, 15\n\t" \
  "s_lshl_b32 s90, s90, 15\n\t" \
  "v_mfma_f32_16x16x32_bf16 v[224:227], v[196:199], v[164:167], v[224:227]\n\t" \
  "v_readfirstlane_b32 vcc_lo, v40\n\t" \
  "ds_read_b64_tr_b16 v[218:219], v35 offset:6656\n\t" \
  "s_add_u32 s90, s90, s92\n\t" \
  "s_addc_u32 s91, s93, 0\n\t" \
  "s_waitcnt vmcnt(8)\n\t" \
  "v_mfma_f32_16x16x32_bf16 v[228:231], v[196:199], v[172:175], v[228:231]\n\t" \
  "s_cmp_ge_u32 vcc_lo, 13\n\t" \
  "s_cbranch_scc0 32f\n\t" \
  "33:\n\t" \
  "ds_read_b64_tr_b16 v[220:221], v35 offset:10240\n\t" \
  "global_load_dwordx4 v[80:83], v53, s[90:91] offset:0 sc1\n\t" \
  "v_mfma_f32_16x16x32_bf16 v[228:231], v[192:195], v[168:171], v[228:231]\n\t" \
  "ds_read_b64_tr_b16 v[222:223], v35 offset:10752\n\t" \
  "s_waitcnt lgkmcnt(6)\n\t" \
  "v_mfma_f32_16x16x32_bf16 v[232:235], v[200:203], v[160:163], v[232:235]\n\t" \
  "ds_read_b64_tr_b16 v[192:193], v32 offset:14336\n\t" \
  "v_mfma_f32_16x16x32_bf16 v[232:235], v[204:207], v[164:167], v[232:235]\n\t" \
  "ds_read_b64_tr_b16 v[194:195], v32 offset:14848\n\t" \
  "v_mfma_f32_16x16x32_bf16 v[236:239], v[204:207], v[172:175], v[236:239]\n\t" \
  "ds_read_b64_tr_b16 v[196:197], v32 offset:18432\n\t" \
  "global_load_dwordx4 v[84:87], v53, s[90:91] offset:1024 sc1\n\t" \
  "v_mfma_f32_16x16x32_bf16 v[236:239], v[200:203], v[168:171], v[236:239]\n\t" \
  "ds_read_b64_tr_b16 v[198:199], v32 offset:18944\n\t" \
  "v_mfma_f32_16x16x32_bf16 v[240:243], v[208:211], v[160:163], v[240:243]\n\t" \
  "ds_read_b64_tr_b16 v[200:201], v33 offset:14336\n\t" \
  "s_waitcnt lgkmcnt(6)\n\t" \
  "v_mfma_f32_16x16x32_bf16 v[240:243], v[212:215], v[164:167], v[240:243]\n\t" \
  "ds_read_b64_tr_b16 v[202:203], v33 offset:14848\n\t" \
  "v_mfma_f32_16x16x32_bf16 v[244:247], v[212:215], v[172:175], v[244:247]\n\t" \
  "ds_read_b64_tr_b16 v[204:205], v33 offset:18432\n\t" \
  "global_load_dwordx4 v[88:91], v53, s[90:91] offset:2048 sc1\n\t" \
  "v_mfma_f32_16x16x32_bf16 v[244:247], v[208:211], v[168:171], v[244:247]\n\t" \
  "ds_read_b64_tr_b16 v[206:207], v33 offset:18944\n\t" \
  "v_mfma_f32_16x16x32_bf16 v[248:251], v[216:219], v[160:163], v[248:251]\n\t" \
  "ds_read_b64_tr_b16 v[208:209], v34 offset:14336\n\t" \
  "s_waitcnt lgkmcnt(6)\n\t" \
  "v_mfma_f32_16x16x32_bf16 v[248:251], v[220:223], v[164:167], v[248:251]\n\t" \
  "ds_read_b64_tr_b16 v[210:211], v34 offset:14848\n\t" \
  "v_mfma_f32_16x16x32_bf16 v[252:255], v[220:223], v[172:175], v[252:255]\n\t" \
  "ds_read_b64_tr_b16 v[212:213], v34 offset:18432\n\t" \
  "global_load_dwordx4 v[92:95], v53, s[90:91] offset:3072 sc1\n\t" \
  "v_mfma_f32_16x16x32_bf16 v[252:255], v[216:219], v[168:171], v[252:255]\n\t" \
  "ds_read_b64_tr_b16 v[214:215], v34 offset:18944\n\t" \
  "s_waitcnt vmcnt(10)\n\t" \
  "v_mfma_f32_16x16x32_bf16 v[224:227], v[192:195], v[176:179], v[224:227]\n\t" \
  "ds_read_b64_tr_b16 v[216:217], v35 offset:14336\n\t" \
  "s_lshr_b64 s[90:91], %18, 40\n\t" \
  "s_and_b32 s90, s90, 15\n\t" \
  "s_lshl_b32 s90, s90, 15\n\t" \
  "s_waitcnt lgkmcnt(6)\n\t" \
  "v_mfma_f32_16x16x32_bf16 v[224:227], v[196:199], v[180:183], v[224:227]\n\t" \
  "ds_read_b64_tr_b16 v[218:219], v35 offset:14848\n\t" \
  "s_add_u32 s90, s90, s92\n\t" \
  "s_addc_u32 s91, s93, 0\n\t" \
  "s_waitcnt vmcnt(8)\n\t" \
  "v_mfma_f32_16x16x32_bf16 v[228:231], v[196:199], v[188:191], v[228:231]\n\t" \
  "ds_read_b64_tr_b16 v[220:221], v35 offset:18432\n\t" \
  "global_load_dwordx4 v[96:99], v53, s[90:91] offset:0 sc1\n\t" \
  "v_mfma_f32_16x16x32_bf16 v[228:231], v[192:195], v[184:187], v[228:231]\n\t" \
  "ds_read_b64_tr_b16 v[222:223], v35 offset:18944\n\t" \
  "v_mfma_f32_16x16x32_bf16 v[232:235], v[200:203], v[176:179], v[232:235]\n\t" \
  "ds_read_b64_tr_b16 v[192:193], v32 offset:22528\n\t" \
  "s_waitcnt lgkmcnt(6)\n\t" \
  "v_mfma_f32_16x16x32_bf16 v[232:235], v[204:207], v[180:183], v[232:235]\n\t" \
  "ds_read_b64_tr_b16 v[194:195], v32 offset:23040\n\t" \
  "v_mfma_f32_16x16x32_bf16 v[236:239], v[204:207], v[188:191], v[236:239]\n\t" \
  "ds_read_b64_tr_b16 v[196:197], v32 offset:26624\n\t" \
  "global_load_dwordx4 v[100:103], v53, s[90:91] offset:1024 sc1\n\t" \
  "v_mfma_f32_16x16x32_bf16 v[236:239], v[200:203], v[184:187], v[236:239]\n\t" \
  "ds_read_b64_tr_b16 v[198:199], v32 offset:27136\n\t" \
  "v_mfma_f32_16x16x32_bf16 v[240:243], v[208:211], v[176:179], v[240:243]\n\t" \
  "ds_read_b64_tr_b16 v[200:201], v33 offset:22528\n\t" \
  "s_waitcnt lgkmcnt(6)\n\t" \
  "v_mfma_f32_16x16x32_bf16 v[240:243], v[212:215], v[180:183], v[240:243]\n\t" \
  "ds_read_b64_tr_b16 v[202:203], v33 offset:23040\n\t" \
  "v_mfma_f32_16x16x32_bf16 v[244:247], v[212:215], v[188:191], v[244:247]\n\t" \
  "ds_read_b64_tr_b16 v[204:205], v33 offset:26624\n\t" \
  "global_load_dwordx4 v[104:107], v53, s[90:91] offset:2048 sc1\n\t" \
  "v_mfma_f32_16x16x32_bf16 v[244:247], v[208:211], v[184:187], v[244:247]\n\t" \
  "ds_read_b64_tr_b16 v[206:207], v33 offset:27136\n\t" \
  "v_mfma_f32_16x16x32_bf16 v[248:251], v[216:219], v[176:179], v[248:251]\n\t" \
  "ds_read_b64_tr_b16 v[208:209], v34 offset:22528\n\t" \
  "s_waitcnt lgkmcnt(6)\n\t" \
  "v_mfma_f32_16x16x32_bf16 v[248:251], v[220:223], v[180:183], v[248:251]\n\t" \
  "ds_read_b64_tr_b16 v[210:211], v34 offset:23040\n\t" \
  "v_mfma_f32_16x16x32_bf16 v[252:255], v[220:223], v[188:191], v[252:255]\n\t" \
  "ds_read_b64_tr_b16 v[212:213], v34 offset:26624\n\t" \
  "global_load_dwordx4 v[108:111], v53, s[90:91] offset:3072 sc1\n\t" \
  "ds_write_b32 v56, v56 offset:108\n\t" \
  "v_mfma_f32_16x16x32_bf16 v[252:255], v[216:219], v[184:187], v[252:255]\n\t" \
  "ds_read_b64_tr_b16 v[214:215], v34 offset:27136\n\t" \
  "s_waitcnt vmcnt(10)\n\t" \
  "v_mfma_f32_16x16x32_bf16 v[224:227], v[192:195], v[64:67], v[224:227]\n\t" \
  "ds_read_b64_tr_b16 v[216:217], v35 offset:22528\n\t" \
  "s_lshr_b64 s[90:91], %18, 44\n\t" \
  "s_and_b32 s90, s90, 15\n\t" \
  "s_lshl_b32 s90, s90, 15\n\t" \
  "s_waitcnt lgkmcnt(7)\n\t" \
  "v_mfma_f32_16x16x32_bf16 v[224:227], v[196:199], v[68:71], v[224:227]\n\t" \
  "ds_read_b64_tr_b16 v[218:219], v35 offset:23040\n\t" \
  "s_add_u32 s90, s90, s92\n\t" \
  "s_addc_u32 s91, s93, 0\n\t" \
  "s_waitcnt vmcnt(8)\n\t" \
  "v_mfma_f32_16x16x32_bf16 v[228:231], v[196:199], v[76:79], v[228:231]\n\t" \
  "ds_read_b64_tr_b16 v[220:221], v35 offset:26624\n\t" \
  "global_load_dwordx4 v[112:115], v53, s[90:91] offset:0 sc1\n\t" \
  "v_mfma_f32_16x16x32_bf16 v[228:231], v[192:195], v[72:75], v[228:231]\n\t" \
  "ds_read_b64_tr_b16 v[222:223], v35 offset:27136\n\t" \
  "v_mfma_f32_16x16x32_bf16 v[232:235], v[200:203], v[64:67], v[232:235]\n\t" \
  "ds_read_b64_tr_b16 v[192:193], v32 offset:30720\n\t" \
  "s_waitcnt lgkmcnt(6)\n\t" \
  "v_mfma_f32_16x16x32_bf16 v[232:235], v[204:207], v[68:71], v[232:235]\n\t" \
  "ds_read_b64_tr_b16 v[194:195], v32 offset:31232\n\t" \
  "v_mfma_f32_16x16x32_bf16 v[236:239], v[204:207], v[76:79], v[236:239]\n\t" \
  "ds_read_b64_tr_b16 v[196:197], v32 offset:34816\n\t" \
  "global_load_dwordx4 v[116:119], v53, s[90:91] offset:1024 sc1\n\t" \
  "v_mfma_f32_16x16x32_bf16 v[236:239], v[200:203], v[72:75], v[236:239]\n\t" \
  "ds_read_b64_tr_b16 v[198:199], v32 offset:35328\n\t" \
  "v_mfma_f32_16x16x32_bf16 v[240:243], v[208:211], v[64:67], v[240:243]\n\t" \
  "ds_read_b64_tr_b16 v[200:201], v33 offset:30720\n\t" \
  "s_waitcnt lgkmcnt(6)\n\t" \
  "v_mfma_f32_16x16x32_bf16 v[240:243], v[212:215], v[68:71], v[240:243]\n\t" \
  "ds_read_b64_tr_b16 v[202:203], v33 offset:31232\n\t" \
  "v_mfma_f32_16x16x32_bf16 v[244:247], v[212:215], v[76:79], v[244:247]\n\t" \
  "ds_read_b64_tr_b16 v[204:205], v33 offset:34816\n\t" \
  "global_load_dwordx4 v[120:123], v53, s[90:91] offset:2048 sc1\n\t" \
  "v_mfma_f32_16x16x32_bf16 v[244:247], v[208:211], v[72:75], v[244:247]\n\t" \
  "ds_read_b64_tr_b16 v[206:207], v33 offset:35328\n\t" \
  "v_mfma_f32_16x16x32_bf16 v[248:251], v[216:219], v[64:67], v[248:251]\n\t" \
  "ds_read_b64_tr_b16 v[208:209], v34 offset:30720\n\t" \
  "s_waitcnt lgkmcnt(6)\n\t" \
  "v_mfma_f32_16x16x32_bf16 v[248:251], v[220:223], v[68:71], v[248:251]\n\t" \
  "ds_read_b64_tr_b16 v[210:211], v34 offset:31232\n\t" \
  "v_mfma_f32_16x16x32_bf16 v[252:255], v[220:223], v[76:79], v[252:255]\n\t" \
  "ds_read_b64_tr_b16 v[212:213], v34 offset:34816\n\t" \
  "global_load_dwordx4 v[124:127], v53, s[90:91] offset:3072 sc1\n\t" \
  "v_mfma_f32_16x16x32_bf16 v[252:255], v[216:219], v[72:75], v[252:255]\n\t" \
  "ds_read_b64_tr_b16 v[214:215], v34 offset:35328\n\t" \
  "s_waitcnt vmcnt(10)\n\t" \
  "v_mfma_f32_16x16x32_bf16 v[224:227], v[192:195], v[80:83], v[224:227]\n\t" \
  "ds_read_b64_tr_b16 v[216:217], v35 offset:30720\n\t" \
  "s_lshr_b64 s[90:91], %18, 48\n\t" \
  "s_and_b32 s90, s90, 15\n\t" \
  "s_lshl_b32 s90, s90, 15\n\t" \
  "s_waitcnt lgkmcnt(6)\n\t" \
  "v_mfma_f32_16x16x32_bf16 v[224:227], v[196:199], v[84:87], v[224:227]\n\t" \
  "ds_read_b64_tr_b16 v[218:219], v35 offset:31232\n\t" \
  "s_add_u32 s90, s90, s92\n\t" \
  "s_addc_u32 s91, s93, 0\n\t" \
  "s_waitcnt vmcnt(8)\n\t" \
  "v_mfma_f32_16x16x32_bf16 v[228:231], v[196:199], v[92:95], v[228:231]\n\t" \
  "ds_read_b64_tr_b16 v[220:221], v35 offset:34816\n\t" \
  "global_load_dwordx4 v[128:131], v53, s[90:91] offset:0 sc1\n\t" \
  "v_mfma_f32_16x16x32_bf16 v[228:231], v[192:195], v[88:91], v[228:231]\n\t" \
  "ds_read_b64_tr_b16 v[222:223], v35 offset:35328\n\t" \
  "v_mfma_f32_16x16x32_bf16 v[232:235], v[200:203], v[80:83], v[232:235]\n\t" \
  "ds_read_b64_tr_b16 v[192:193], v32 offset:38912\n\t" \
  "s_waitcnt lgkmcnt(6)\n\t" \
  "v_mfma_f32_16x16x32_bf16 v[232:235], v[204:207], v[84:87], v[232:235]\n\t" \
  "ds_read_b64_tr_b16 v[194:195], v32 offset:39424\n\t" \
  "v_mfma_f32_16x16x32_bf16 v[236:239], v[204:207], v[92:95], v[236:239]\n\t" \
  "ds_read_b64_tr_b16 v[196:197], v32 offset:43008\n\t" \
  "global_load_dwordx4 v[132:135], v53, s[90:91] offset:1024 sc1\n\t" \
  "v_mfma_f32_16x16x32_bf16 v[236:239], v[200:203], v[88:91], v[236:239]\n\t" \
  "ds_read_b64_tr_b16 v[198:199], v32 offset:43520\n\t" \
  "v_mfma_f32_16x16x32_bf16 v[240:243], v[208:211], v[80:83], v[240:243]\n\t" \
  "ds_read_b64_tr_b16 v[200:201], v33 offset:38912\n\t" \
  "s_waitcnt lgkmcnt(6)\n\t" \
  "v_mfma_f32_16x16x32_bf16 v[240:243], v[212:215], v[84:87], v[240:243]\n\t" \
  "ds_read_b64_tr_b16 v[202:203], v33 offset:39424\n\t" \
  "v_mfma_f32_16x16x32_bf16 v[244:247], v[212:215], v[92:95], v[244:247]\n\t" \
  "ds_read_b64_tr_b16 v[204:205], v33 offset:43008\n\t" \
  "global_load_dwordx4 v[136:139], v53, s[90:91] offset:2048 sc1\n\t" \
  "v_mfma_f32_16x16x32_bf16 v[244:247], v[208:211], v[88:91], v[244:247]\n\t" \
  "ds_read_b64_tr_b16 v[206:207], v33 offset:43520\n\t" \
  "v_mfma_f32_16x16x32_bf16 v[248:251], v[216:219], v[80:83], v[248:251]\n\t" \
  "ds_read_b64_tr_b16 v[208:209], v34 offset:38912\n\t" \
  "s_waitcnt lgkmcnt(6)\n\t" \
  "v_mfma_f32_16x16x32_bf16 v[248:251], v[220:223], v[84:87], v[248:251]\n\t" \
  "ds_read_b64_tr_b16 v[210:211], v34 offset:39424\n\t" \
  "v_mfma_f32_16x16x32_bf16 v[252:255], v[220:223], v[92:95], v[252:255]\n\t" \
  "ds_read_b64_tr_b16 v[212:213], v34 offset:43008\n\t" \
  "global_load_dwordx4 v[140:143], v53, s[90:91] offset:3072 sc1\n\t" \
  "v_mfma_f32_16x16x32_bf16 v[252:255], v[216:219], v[88:91], v[252:255]\n\t" \
  "ds_read_b64_tr_b16 v[214:215], v34 offset:43520\n\t" \
  "s_waitcnt vmcnt(10)\n\t" \
  "v_mfma_f32_16x16x32_bf16 v[224:227], v[192:195], v[96:99], v[224:227]\n\t" \
  "ds_read_b64_tr_b16 v[216:217], v35 offset:38912\n\t" \
  "509:\n\t" \
  "s_cbranch_execz 26b\n\t" \
  "s_lshr_b64 s[90:91], %18, 52\n\t" \
  "s_and_b32 s90, s90, 15\n\t" \
  "s_lshl_b32 s90, s90, 15\n\t" \
  "s_waitcnt lgkmcnt(6)\n\t" \
  "v_mfma_f32_16x16x32_bf16 v[224:227], v[196:199], v[100:103], v[224:227]\n\t" \
  "ds_read_b64_tr_b16 v[218:219], v35 offset:39424\n\t" \
  "s_add_u32 s90, s90, s92\n\t" \
  "s_addc_u32 s91, s93, 0\n\t" \
  "s_waitcnt vmcnt(8)\n\t" \
  "v_mfma_f32_16x16x32_bf16 v[228:231], v[196:199], v[108:111], v[228:231]\n\t" \
  "ds_read_b64_tr_b16 v[220:221], v35 offset:43008\n\t" \
  "global_load_dwordx4 v[144:147], v53, s[90:91] offset:0 sc1\n\t" \
  "v_mfma_f32_16x16x32_bf16 v[228:231], v[192:195], v[104:107], v[228:231]\n\t" \
  "ds_read_b64_tr_b16 v[222:223], v35 offset:43520\n\t" \
  "v_mfma_f32_16x16x32_bf16 v[232:235], v[200:203], v[96:99], v[232:235]\n\t" \
  "ds_read_b64_tr_b16 v[192:193], v32 offset:47104\n\t" \
  "s_waitcnt lgkmcnt(6)\n\t" \
  "v_mfma_f32_16x16x32_bf16 v[232:235], v[204:207], v[100:103], v[232:235]\n\t" \
  "ds_read_b64_tr_b16 v[194:195], v32 offset:47616\n\t" \
  "v_mfma_f32_16x16x32_bf16 v[236:239], v[204:207], v[108:111], v[236:239]\n\t" \
  "ds_read_b64_tr_b16 v[196:197], v32 offset:51200\n\t" \
  "global_load_dwordx4 v[148:151], v53, s[90:91] offset:1024 sc1\n\t" \
  "v_mfma_f32_16x16x32_bf16 v[236:239], v[200:203], v[104:107], v[236:239]\n\t" \
  "ds_read_b64_tr_b16 v[198:199], v32 offset:51712\n\t" \
  "v_mfma_f32_16x16x32_bf16 v[240:243], v[208:211], v[96:99], v[240:243]\n\t" \
  "ds_read_b64_tr_b16 v[200:201], v33 offset:47104\n\t" \
  "s_waitcnt lgkmcnt(6)\n\t" \
  "v_mfma_f32_16x16x32_bf16 v[240:243], v[212:215], v[100:103], v[240:243]\n\t" \
  "ds_read_b64_tr_b16 v[202:203], v33 offset:47616\n\t" \
  "v_mfma_f32_16x16x32_bf16 v[244:247], v[212:215], v[108:111], v[244:247]\n\t" \
  "ds_read_b64_tr_b16 v[204:205], v33 offset:51200\n\t" \
  "global_load_dwordx4 v[152:155], v53, s[90:91] offset:2048 sc1\n\t" \
  "v_mfma_f32_16x16x32_bf16 v[244:247], v[208:211], v[104:107], v[244:247]\n\t" \
  "ds_read_b64_tr_b16 v[206:207], v33 offset:51712\n\t" \
  "v_mfma_f32_16x16x32_bf16 v[248:251], v[216:219], v[96:99], v[248:251]\n\t" \
  "ds_read_b64_tr_b16 v[208:209], v34 offset:47104\n\t" \
  "s_waitcnt lgkmcnt(6)\n\t" \
  "v_mfma_f32_16x16x32_bf16 v[248:251], v[220:223], v[100:103], v[248:251]\n\t" \
  "ds_read_b64_tr_b16 v[210:211], v34 offset:47616\n\t" \
  "v_mfma_f32_16x16x32_bf16 v[252:255], v[220:223], v[108:111], v[252:255]\n\t" \
  "ds_read_b64_tr_b16 v[212:213], v34 offset:51200\n\t" \
  "global_load_dwordx4 v[156:159], v53, s[90:91] offset:3072 sc1\n\t" \
  "v_mfma_f32_16x16x32_bf16 v[252:255], v[216:219], v[104:107], v[252:255]\n\t" \
  "ds_read_b64_tr_b16 v[214:215], v34 offset:51712\n\t" \
  "s_waitcnt vmcnt(10)\n\t" \
  "v_mfma_f32_16x16x32_bf16 v[224:227], v[192:195], v[112:115], v[224:227]\n\t" \
  "ds_read_b64_tr_b16 v[216:217], v35 offset:47104\n\t" \
  "s_lshr_b64 s[90:91], %18, 56\n\t" \
  "s_and_b32 s90, s90, 15\n\t" \
  "s_lshl_b32 s90, s90, 15\n\t" \
  "s_waitcnt lgkmcnt(6)\n\t" \
  "v_mfma_f32_16x16x32_bf16 v[224:227], v[196:199], v[116:119], v[224:227]\n\t" \
  "ds_read_b64_tr_b16 v[218:219], v35 offset:47616\n\t" \
  "s_add_u32 s90, s90, s92\n\t" \
  "s_addc_u32 s91, s93, 0\n\t" \
  "s_waitcnt vmcnt(8)\n\t" \
  "v_mfma_f32_16x16x32_bf16 v[228:231], v[196:199], v[124:127], v[228:231]\n\t" \
  "ds_read_b64_tr_b16 v[220:221], v35 offset:51200\n\t" \
  "global_load_dwordx4 v[160:163], v53, s[90:91] offset:0 sc1\n\t" \
  "v_mfma_f32_16x16x32_bf16 v[228:231], v[192:195], v[120:123], v[228:231]\n\t" \
  "ds_read_b64_tr_b16 v[222:223], v35 offset:51712\n\t" \
  "v_mfma_f32_16x16x32_bf16 v[232:235], v[200:203], v[112:115], v[232:235]\n\t" \
  "ds_read_b64_tr_b16 v[192:193], v32 offset:55296\n\t" \
  "s_waitcnt lgkmcnt(6)\n\t" \
  "v_mfma_f32_16x16x32_bf16 v[232:235], v[204:207], v[116:119], v[232:235]\n\t" \
  "ds_read_b64_tr_b16 v[194:195], v32 offset:55808\n\t" \
  "v_mfma_f32_16x16x32_bf16 v[236:239], v[204:207], v[124:127], v[236:239]\n\t" \
  "ds_read_b64_tr_b16 v[196:197], v32 offset:59392\n\t" \
  "global_load_dwordx4 v[164:167], v53, s[90:91] offset:1024 sc1\n\t" \
  "v_mfma_f32_16x16x32_bf16 v[236:239], v[200:203], v[120:123], v[236:239]\n\t" \
  "ds_read_b64_tr_b16 v[198:199], v32 offset:59904\n\t" \
  "v_mfma_f32_16x16x32_bf16 v[240:243], v[208:211], v[112:115], v[240:243]\n\t" \
  "ds_read_b64_tr_b16 v[200:201], v33 offset:55296\n\t" \
  "s_waitcnt lgkmcnt(6)\n\t" \
  "v_mfma_f32_16x16x32_bf16 v[240:243], v[212:215], v[116:119], v[240:243]\n\t" \
  "ds_read_b128 v[40:43], v56 offset:80\n\t" \
  "ds_read_b64_tr_b16 v[202:203], v33 offset:55808\n\t" \
  "v_mfma_f32_16x16x32_bf16 v[244:247], v[212:215], v[124:127], v[244:247]\n\t" \
  "ds_read_b64_tr_b16 v[204:205], v33 offset:59392\n\t" \
  "global_load_dwordx4 v[168:171], v53, s[90:91] offset:2048 sc1\n\t" \
  "v_mfma_f32_16x16x32_bf16 v[244:247], v[208:211], v[120:123], v[244:247]\n\t" \
  "ds_read_b64_tr_b16 v[206:207], v33 offset:59904\n\t" \
  "v_mfma_f32_16x16x32_bf16 v[248:251], v[216:219], v[112:115], v[248:251]\n\t" \
  "ds_read_b64_tr_b16 v[208:209], v34 offset:55296\n\t" \
  "s_waitcnt lgkmcnt(7)\n\t" \
  "v_mfma_f32_16x16x32_bf16 v[248:251], v[220:223], v[116:119], v[248:251]\n\t" \
  "ds_read_b64_tr_b16 v[210:211], v34 offset:55808\n\t" \
  "v_mfma_f32_16x16x32_bf16 v[252:255], v[220:223], v[124:127], v[252:255]\n\t" \
  "ds_read_b64_tr_b16 v[212:213], v34 offset:59392\n\t" \
  "global_load_dwordx4 v[172:175], v53, s[90:91] offset:3072 sc1\n\t" \
  "v_mfma_f32_16x16x32_bf16 v[252:255], v[216:219], v[120:123], v[252:255]\n\t" \
  "ds_read_b64_tr_b16 v[214:215], v34 offset:59904\n\t" \
  "s_waitcnt vmcnt(10)\n\t" \
  "v_mfma_f32_16x16x32_bf16 v[224:227], v[192:195], v[128:131], v[224:227]\n\t" \
  "s_waitcnt lgkmcnt(7)\n\t" \
  "v_min_u32 v40, v40, v41\n\t" \
  "v_min3_u32 v40, v40, v42, v43\n\t" \
  "ds_read_b64_tr_b16 v[216:217], v35 offset:55296\n\t" \
  "s_lshr_b64 s[90:91], %18, 60\n\t" \
  "s_and_b32 s90, s90, 15\n\t" \
  "s_lshl_b32 s90, s90, 15\n\t" \
  "v_mfma_f32_16x16x32_bf16 v[224:227], v[196:199], v[132:135], v[224:227]\n\t" \
  "v_readfirstlane_b32 vcc_lo, v40\n\t" \
  "ds_read_b64_tr_b16 v[218:219], v35 offset:55808\n\t" \
  "s_add_u32 s90, s90, s92\n\t" \
  "s_addc_u32 s91, s93, 0\n\t" \
  "s_waitcnt vmcnt(8)\n\t" \
  "v_mfma_f32_16x16x32_bf16 v[228:231], v[196:199], v[140:143], v[228:231]\n\t" \
  "s_cmp_ge_u32 vcc_lo, 16\n\t" \
  "s_cbranch_scc0 34f\n\t" \
  "35:\n\t" \
  "ds_read_b64_tr_b16 v[220:221], v35 offset:59392\n\t" \
  "global_load_dwordx4 v[176:179], v53, s[90:91] offset:0 sc1\n\t" \
  "v_mfma_f32_16x16x32_bf16 v[228:231], v[192:195], v[136:139], v[228:231]\n\t" \
  "ds_read_b64_tr_b16 v[222:223], v35 offset:59904\n\t" \
  "s_waitcnt lgkmcnt(6)\n\t" \
  "v_mfma_f32_16x16x32_bf16 v[232:235], v[200:203], v[128:131], v[232:235]\n\t" \
  "ds_read_b64_tr_b16 v[192:193], v36 offset:2304\n\t" \
  "v_mfma_f32_16x16x32_bf16 v[232:235], v[204:207], v[132:135], v[232:235]\n\t" \
  "ds_read_b64_tr_b16 v[194:195], v36 offset:2816\n\t" \
  "v_mfma_f32_16x16x32_bf16 v[236:239], v[204:207], v[140:143], v[236:239]\n\t" \
  "ds_read_b64_tr_b16 v[196:197], v36 offset:10752\n\t" \
  "global_load_dwordx4 v[180:183], v53, s[90:91] offset:1024 sc1\n\t" \
  "v_mfma_f32_16x16x32_bf16 v[236:239], v[200:203], v[136:139], v[236:239]\n\t" \
  "ds_read_b64_tr_b16 v[198:199], v36 offset:11264\n\t" \
  "v_mfma_f32_16x16x32_bf16 v[240:243], v[208:211], v[128:131], v[240:243]\n\t" \
  "ds_read_b64_tr_b16 v[200:201], v37 offset:2304\n\t" \
  "s_waitcnt lgkmcnt(6)\n\t" \
  "v_mfma_f32_16x16x32_bf16 v[240:243], v[212:215], v[132:135], v[240:243]\n\t" \
  "ds_read_b64_tr_b16 v[202:203], v37 offset:2816\n\t" \
  "v_mfma_f32_16x16x32_bf16 v[244:247], v[212:215], v[140:143], v[244:247]\n\t" \
  "ds_read_b64_tr_b16 v[204:205], v37 offset:10752\n\t" \
  "global_load_dwordx4 v[184:187], v53, s[90:91] offset:2048 sc1\n\t" \
  "v_mfma_f32_16x16x32_bf16 v[244:247], v[208:211], v[136:139], v[244:247]\n\t" \
  "ds_read_b64_tr_b16 v[206:207], v37 offset:11264\n\t" \
  "v_mfma_f32_16x16x32_bf16 v[248:251], v[216:219], v[128:131], v[248:251]\n\t" \
  "ds_read_b64_tr_b16 v[208:209], v38 offset:2304\n\t" \
  "s_waitcnt lgkmcnt(6)\n\t" \
  "v_mfma_f32_16x16x32_bf16 v[248:251], v[220:223], v[132:135], v[248:251]\n\t" \
  "ds_read_b64_tr_b16 v[210:211], v38 offset:2816\n\t" \
  "v_mfma_f32_16x16x32_bf16 v[252:255], v[220:223], v[140:143], v[252:255]\n\t" \
  "ds_read_b64_tr_b16 v[212:213], v38 offset:10752\n\t" \
  "global_load_dwordx4 v[188:191], v53, s[90:91] offset:3072 sc1\n\t" \
  "v_mfma_f32_16x16x32_bf16 v[252:255], v[216:219], v[136:139], v[252:255]\n\t" \
  "ds_read_b64_tr_b16 v[214:215], v38 offset:11264\n\t" \
  "s_waitcnt vmcnt(10)\n\t" \
  "v_mfma_f32_16x16x32_bf16 v[224:227], v[192:195], v[144:147], v[224:227]\n\t" \
  "ds_read_b64_tr_b16 v[216:217], v39 offset:2304\n\t" \
  "s_waitcnt lgkmcnt(6)\n\t" \
  "v_mfma_f32_16x16x32_bf16 v[224:227], v[196:199], v[148:151], v[224:227]\n\t" \
  "ds_read_b64_tr_b16 v[218:219], v39 offset:2816\n\t" \
  "s_waitcnt vmcnt(8)\n\t" \
  "v_mfma_f32_16x16x32_bf16 v[228:231], v[196:199], v[156:159], v[228:231]\n\t" \
  "ds_read_b64_tr_b16 v[220:221], v39 offset:10752\n\t" \
  "v_mfma_f32_16x16x32_bf16 v[228:231], v[192:195], v[152:155], v[228:231]\n\t" \
  "ds_read_b64_tr_b16 v[222:223], v39 offset:11264\n\t" \
  "v_mfma_f32_16x16x32_bf16 v[232:235], v[200:203], v[144:147], v[232:235]\n\t" \
  "ds_read_b64_tr_b16 v[192:193], v36 offset:19200\n\t" \
  "s_waitcnt lgkmcnt(6)\n\t" \
  "v_mfma_f32_16x16x32_bf16 v[232:235], v[204:207], v[148:151], v[232:235]\n\t" \
  "ds_read_b64_tr_b16 v[194:195], v36 offset:19712\n\t" \
  "v_mfma_f32_16x16x32_bf16 v[236:239], v[204:207], v[156:159], v[236:239]\n\t" \
  "ds_read_b64_tr_b16 v[196:197], v36 offset:27648\n\t" \
  "v_mfma_f32_16x16x32_bf16 v[236:239], v[200:203], v[152:155], v[236:239]\n\t" \
  "ds_read_b64_tr_b16 v[198:199], v36 offset:28160\n\t" \
  "v_mfma_f32_16x16x32_bf16 v[240:243], v[208:211], v[144:147], v[240:243]\n\t" \
  "ds_read_b64_tr_b16 v[200:201], v37 offset:19200\n\t" \
  "s_waitcnt lgkmcnt(6)\n\t" \
  "v_mfma_f32_16x16x32_bf16 v[240:243], v[212:215], v[148:151], v[240:243]\n\t" \
  "ds_read_b128 v[40:43], v56 offset:160\n\t" \
  "ds_read_b64_tr_b16 v[202:203], v37 offset:19712\n\t" \
  "v_mfma_f32_16x16x32_bf16 v[244:247], v[212:215], v[156:159], v[244:247]\n\t" \
  "ds_read_b64_tr_b16 v[204:205], v37 offset:27648\n\t" \
  "v_mfma_f32_16x16x32_bf16 v[244:247], v[208:211], v[152:155], v[244:247]\n\t" \
  "ds_read_b64_tr_b16 v[206:207], v37 offset:28160\n\t" \
  "v_mfma_f32_16x16x32_bf16 v[248:251], v[216:219], v[144:147], v[248:251]\n\t" \
  "ds_read_b64_tr_b16 v[208:209], v38 offset:19200\n\t" \
  "s_waitcnt lgkmcnt(7)\n\t" \
  "v_mfma_f32_16x16x32_bf16 v[248:251], v[220:223], v[148:151], v[248:251]\n\t" \
  "ds_read_b64_tr_b16 v[210:211], v38 offset:19712\n\t" \
  "v_mfma_f32_16x16x32_bf16 v[252:255], v[220:223], v[156:159], v[252:255]\n\t" \
  "ds_read_b64_tr_b16 v[212:213], v38 offset:27648\n\t" \
  "v_mfma_f32_16x16x32_bf16 v[252:255], v[216:219], v[152:155], v[252:255]\n\t" \
  "ds_read_b64_tr_b16 v[214:215], v38 offset:28160\n\t" \
  "s_waitcnt vmcnt(6)\n\t" \
  "v_mfma_f32_16x16x32_bf16 v[224:227], v[192:195], v[160:163], v[224:227]\n\t" \
  "s_waitcnt lgkmcnt(7)\n\t" \
  "v_min_u32 v40, v40, v41\n\t" \
  "v_min3_u32 v40, v40, v42, v43\n\t" \
  "ds_read_b64_tr_b16 v[216:217], v39 offset:19200\n\t" \
  "v_mfma_f32_16x16x32_bf16 v[224:227], v[196:199], v[164:167], v[224:227]\n\t" \
  "v_readfirstlane_b32 vcc_lo, v40\n\t" \
  "ds_read_b64_tr_b16 v[218:219], v39 offset:19712\n\t" \
  "s_waitcnt vmcnt(4)\n\t" \
  "v_mfma_f32_16x16x32_bf16 v[228:231], v[196:199], v[172:175], v[228:231]\n\t" \
  "s_cmp_ge_u32 vcc_lo, 16\n\t" \
  "s_cbranch_scc0 36f\n\t" \
  "37:\n\t" \
  "ds_read_b64_tr_b16 v[220:221], v39 offset:27648\n\t" \
  "v_mfma_f32_16x16x32_bf16 v[228:231], v[192:195], v[168:171], v[228:231]\n\t" \
  "ds_read_b64_tr_b16 v[222:223], v39 offset:28160\n\t" \
  "s_waitcnt lgkmcnt(6)\n\t" \
  "v_mfma_f32_16x16x32_bf16 v[232:235], v[200:203], v[160:163], v[232:235]\n\t" \
  "ds_read_b64_tr_b16 v[192:193], v28 offset:0\n\t" \
  "v_mfma_f32_16x16x32_bf16 v[232:235], v[204:207], v[164:167], v[232:235]\n\t" \
  "ds_read_b64_tr_b16 v[194:195], v28 offset:512\n\t" \
  "v_mfma_f32_16x16x32_bf16 v[236:239], v[204:207], v[172:175], v[236:239]\n\t" \
  "ds_read_b64_tr_b16 v[196:197], v28 offset:4096\n\t" \
  "v_mfma_f32_16x16x32_bf16 v[236:239], v[200:203], v[168:171], v[236:239]\n\t" \
  "ds_read_b64_tr_b16 v[198:199], v28 offset:4608\n\t" \
  "v_mfma_f32_16x16x32_bf16 v[240:243], v[208:211], v[160:163], v[240:243]\n\t" \
  "ds_read_b64_tr_b16 v[200:201], v29 offset:0\n\t" \
  "s_waitcnt lgkmcnt(6)\n\t" \
  "v_mfma_f32_16x16x32_bf16 v[240:243], v[212:215], v[164:167], v[240:243]\n\t" \
  "ds_read_b64_tr_b16 v[202:203], v29 offset:512\n\t" \
  "v_mfma_f32_16x16x32_bf16 v[244:247], v[212:215], v[172:175], v[244:247]\n\t" \
  "ds_read_b64_tr_b16 v[204:205], v29 offset:4096\n\t" \
  "v_mfma_f32_16x16x32_bf16 v[244:247], v[208:211], v[168:171], v[244:247]\n\t" \
  "ds_read_b64_tr_b16 v[206:207], v29 offset:4608\n\t" \
  "v_mfma_f32_16x16x32_bf16 v[248:251], v[216:219], v[160:163], v[248:251]\n\t" \
  "ds_read_b64_tr_b16 v[208:209], v30 offset:0\n\t" \
  "s_waitcnt lgkmcnt(6)\n\t" \
  "v_mfma_f32_16x16x32_bf16 v[248:251], v[220:223], v[164:167], v[248:251]\n\t" \
  "ds_read_b64_tr_b16 v[210:211], v30 offset:512\n\t" \
  "v_mfma_f32_16x16x32_bf16 v[252:255], v[220:223], v[172:175], v[252:255]\n\t" \
  "ds_read_b64_tr_b16 v[212:213], v30 offset:4096\n\t" \
  "v_mfma_f32_16x16x32_bf16 v[252:255], v[216:219], v[168:171], v[252:255]\n\t" \
  "ds_read_b64_tr_b16 v[214:215], v30 offset:4608\n\t" \
  "s_waitcnt vmcnt(2)\n\t" \
  "v_mfma_f32_16x16x32_bf16 v[224:227], v[192:195], v[176:179], v[224:227]\n\t" \
  "ds_read_b64_tr_b16 v[216:217], v31 offset:0\n\t" \
  "s_waitcnt lgkmcnt(6)\n\t" \
  "v_mfma_f32_16x16x32_bf16 v[224:227], v[196:199], v[180:183], v[224:227]\n\t" \
  "ds_read_b64_tr_b16 v[218:219], v31 offset:512\n\t" \
  "s_waitcnt vmcnt(0)\n\t" \
  "v_mfma_f32_16x16x32_bf16 v[228:231], v[196:199], v[188:191], v[228:231]\n\t" \
  "ds_read_b64_tr_b16 v[220:221], v31 offset:4096\n\t" \
  "v_mfma_f32_16x16x32_bf16 v[228:231], v[192:195], v[184:187], v[228:231]\n\t" \
  "ds_read_b64_tr_b16 v[222:223], v31 offset:4608\n\t" \
  "v_mfma_f32_16x16x32_bf16 v[232:235], v[200:203], v[176:179], v[232:235]\n\t" \
  "s_nop 0\n\t" \
  "v_cvt_pk_bf16_f32 v224, v224, v225\n\t" \
  "v_cvt_pk_bf16_f32 v225, v226, v227\n\t" \
  "s_waitcnt lgkmcnt(5)\n\t" \
  "v_mfma_f32_16x16x32_bf16 v[232:235], v[204:207], v[180:183], v[232:235]\n\t" \
  "ds_write_b64 v57, v[224:225] offset:0\n\t" \
  "v_mfma_f32_16x16x32_bf16 v[236:239], v[204:207], v[188:191], v[236:239]\n\t" \
  "v_cvt_pk_bf16_f32 v228, v228, v229\n\t" \
  "v_cvt_pk_bf16_f32 v229, v230, v231\n\t" \
  "v_mfma_f32_16x16x32_bf16 v[236:239], v[200:203], v[184:187], v[236:239]\n\t" \
  "ds_write_b64 v57, v[228:229] offset:2048\n\t" \
  "v_mfma_f32_16x16x32_bf16 v[240:243], v[208:211], v[176:179], v[240:243]\n\t" \
  "s_nop 0\n\t" \
  "v_cvt_pk_bf16_f32 v232, v232, v233\n\t" \
  "v_cvt_pk_bf16_f32 v233, v234, v235\n\t" \
  "s_waitcnt lgkmcnt(3)\n\t" \
  "v_mfma_f32_16x16x32_bf16 v[240:243], v[212:215], v[180:183], v[240:243]\n\t" \
  "ds_write_b64 v58, v[232:233] offset:0\n\t" \
  "s_waitcnt lgkmcnt(3)\n\t" \
  "v_mfma_f32_16x16x32_bf16 v[248:251], v[220:223], v[180:183], v[248:251]\n\t" \
  "v_cvt_pk_bf16_f32 v236, v236, v237\n\t" \
  "v_cvt_pk_bf16_f32 v237, v238, v239\n\t" \
  "v_mfma_f32_16x16x32_bf16 v[248:251], v[216:219], v[176:179], v[248:251]\n\t" \
  "ds_write_b64 v58, v[236:237] offset:2048\n\t" \
  "v_mfma_f32_16x16x32_bf16 v[244:247], v[208:211], v[184:187], v[244:247]\n\t" \
  "v_cvt_pk_bf16_f32 v240, v240, v241\n\t" \
  "v_cvt_pk_bf16_f32 v241, v242, v243\n\t" \
  "v_mfma_f32_16x16x32_bf16 v[244:247], v[212:215], v[188:191], v[244:247]\n\t" \
  "ds_write_b64 v59, v[240:241] offset:0\n\t" \
  "v_mfma_f32_16x16x32_bf16 v[252:255], v[220:223], v[188:191], v[252:255]\n\t" \
  "s_nop 0\n\t" \
  "v_cvt_pk_bf16_f32 v248, v248, v249\n\t" \
  "v_cvt_pk_bf16_f32 v249, v250, v251\n\t" \
  "v_mfma_f32_16x16x32_bf16 v[252:255], v[216:219], v[184:187], v[252:255]\n\t" \
  "ds_write_b64 v60, v[248:249] offset:0\n\t" \
  "ds_read_b128 v[64:67], v61 offset:0\n\t" \
  "ds_read_b128 v[68:71], v61 offset:1024\n\t" \
  "v_cvt_pk_bf16_f32 v244, v244, v245\n\t" \
  "v_cvt_pk_bf16_f32 v245, v246, v247\n\t" \
  "s_nop 2\n\t" \
  "v_cvt_pk_bf16_f32 v252, v252, v253\n\t" \
  "v_cvt_pk_bf16_f32 v253, v254, v255\n\t" \
  "ds_write_b64 v59, v[244:245] offset:2048\n\t" \
  "ds_write_b64 v60, v[252:253] offset:2048\n\t" \
  "ds_read_b128 v[72:75], v61 offset:2048\n\t" \
  "ds_read_b128 v[76:79], v61 offset:3072\n\t" \
  "s_waitcnt lgkmcnt(5)\n\t" \
  "global_store_dwordx4 v62, v[64:67], %22 offset:-4096" OUTPOL "\n\t" \
  "s_waitcnt lgkmcnt(4)\n\t" \
  "global_store_dwordx4 v63, v[68:71], %22 offset:-4096" OUTPOL "\n\t" \
  "s_waitcnt lgkmcnt(1)\n\t" \
  "global_store_dwordx4 v62, v[72:75], %22 offset:0" OUTPOL "\n\t" \
  "s_waitcnt lgkmcnt(0)\n\t" \
  "global_store_dwordx4 v63, v[76:79], %22 offset:0" OUTPOL "\n\t" \
  "s_branch 19f\n\t" \
  "23:\n\t" \
  "510:\n\t" \
  "s_cbranch_execz 26b\n\t" \
  "s_mov_b64 exec, 0xffff\n\t" \
  "global_store_dword v11, v55, %19 sc1\n\t" \
  "s_mov_b64 exec, -1\n\t" \
  "s_branch 22b\n\t" \
  "7:\n\t" \
  "s_sleep 1\n\t" \
  "ds_read_b128 v[60:63], v56 offset:64\n\t" \
  "ds_read_b64_tr_b16 v[108:109], v5 offset:0\n\t" \
  "ds_read_b64_tr_b16 v[110:111], v5 offset:16\n\t" \
  "ds_read_b128 v[112:115], v6 offset:16384\n\t" \
  "ds_read_b128 v[116:119], v6 offset:15360\n\t" \
  "ds_read_b64_tr_b16 v[120:121], v5 offset:512\n\t" \
  "ds_read_b64_tr_b16 v[122:123], v5 offset:528\n\t" \
  "ds_read_b128 v[124:127], v6 offset:14336\n\t" \
  "ds_read_b64_tr_b16 v[128:129], v5 offset:1024\n\t" \
  "ds_read_b64_tr_b16 v[130:131], v5 offset:1040\n\t" \
  "ds_read_b128 v[132:135], v6 offset:13312\n\t" \
  "ds_read_b64_tr_b16 v[136:137], v5 offset:1536\n\t" \
  "ds_read_b64_tr_b16 v[138:139], v5 offset:1552\n\t" \
  "ds_read_b128 v[140:143], v6 offset:12288\n\t" \
  "ds_read_b64_tr_b16 v[144:145], v5 offset:2048\n\t" \
  "s_waitcnt lgkmcnt(0)\n\t" \
  "v_and_b32 v60, v60, v61\n\t" \
  "v_and_b32 v62, v62, v63\n\t" \
  "v_mov_b32 v12, %13\n\t" \
  "v_and_b32 v60, v60, v62\n\t" \
  "s_nop 0\n\t" \
  "v_readfirstlane_b32 vcc_lo, v60\n\t" \
  "s_nop 1\n\t" \
  "s_cmp_eq_u32 vcc_lo, 0\n\t" \
  "s_cbranch_scc1 7b\n\t" \
  "s_branch 6b\n\t" \
  "30:\n\t" \
  "ds_read_b128 v[40:43], v56 offset:80\n\t" \
  "s_waitcnt lgkmcnt(0)\n\t" \
  "v_min_u32 v40, v40, v41\n\t" \
  "v_min3_u32 v40, v40, v42, v43\n\t" \
  "s_nop 1\n\t" \
  "v_readfirstlane_b32 vcc_lo, v40\n\t" \
  "s_nop 1\n\t" \
  "s_cmp_ge_u32 vcc_lo, 7\n\t" \
  "s_cbranch_scc0 30b\n\t" \
  "s_branch 31b\n\t" \
  "32:\n\t" \
  "ds_read_b128 v[40:43], v56 offset:80\n\t" \
  "s_waitcnt lgkmcnt(0)\n\t" \
  "v_min_u32 v40, v40, v41\n\t" \
  "v_min3_u32 v40, v40, v42, v43\n\t" \
  "s_nop 1\n\t" \
  "v_readfirstlane_b32 vcc_lo, v40\n\t" \
  "s_nop 1\n\t" \
  "s_cmp_ge_u32 vcc_lo, 13\n\t" \
  "s_cbranch_scc0 32b\n\t" \
  "s_branch 33b\n\t" \
  "34:\n\t" \
  "ds_read_b128 v[40:43], v56 offset:80\n\t" \
  "s_waitcnt lgkmcnt(0)\n\t" \
  "v_min_u32 v40, v40, v41\n\t" \
  "v_min3_u32 v40, v40, v42, v43\n\t" \
  "s_nop 1\n\t" \
  "v_readfirstlane_b32 vcc_lo, v40\n\t" \
  "s_nop 1\n\t" \
  "s_cmp_ge_u32 vcc_lo, 16\n\t" \
  "s_cbranch_scc0 34b\n\t" \
  "s_branch 35b\n\t" \
  "36:\n\t" \
  "ds_read_b128 v[40:43], v56 offset:160\n\t" \
  "s_waitcnt lgkmcnt(0)\n\t" \
  "v_min_u32 v40, v40, v41\n\t" \
  "v_min3_u32 v40, v40, v42, v43\n\t" \
  "s_nop 1\n\t" \
  "v_readfirstlane_b32 vcc_lo, v40\n\t" \
  "s_nop 1\n\t" \
  "s_cmp_ge_u32 vcc_lo, 16\n\t" \
  "s_cbranch_scc0 36b\n\t" \
  "s_branch 37b\n\t" \
  "38:\n\t" \
  "s_waitcnt vmcnt(0) lgkmcnt(0)\n\t" \
  "v_readfirstlane_b32 vcc_lo, v16\n\t" \
  "s_nop 1\n\t" \
  "s_cmp_ge_u32 vcc_lo, 1\n\t" \
  "s_cbranch_scc0 40f\n\t" \
  "41:\n\t" \
  "s_lshr_b64 s[90:91], %18, 0\n\t" \
  "s_and_b32 s90, s90, 15\n\t" \
  "s_lshl_b32 s90, s90, 15\n\t" \
  "s_add_u32 s90, s90, s92\n\t" \
  "s_addc_u32 s91, s93, 0\n\t" \
  "global_load_dwordx4 v[64:67], v53, s[90:91] offset:0 sc1\n\t" \
  "global_load_dwordx4 v[68:71], v53, s[90:91] offset:1024 sc1\n\t" \
  "global_load_dwordx4 v[72:75], v53, s[90:91] offset:2048 sc1\n\t" \
  "global_load_dwordx4 v[76:79], v53, s[90:91] offset:3072 sc1\n\t" \
  "s_lshr_b64 s[90:91], %18, 4\n\t" \
  "s_and_b32 s90, s90, 15\n\t" \
  "s_lshl_b32 s90, s90, 15\n\t" \
  "s_add_u32 s90, s90, s92\n\t" \
  "s_addc_u32 s91, s93, 0\n\t" \
  "global_load_dwordx4 v[80:83], v53, s[90:91] offset:0 sc1\n\t" \
  "global_load_dwordx4 v[84:87], v53, s[90:91] offset:1024 sc1\n\t" \
  "global_load_dwordx4 v[88:91], v53, s[90:91] offset:2048 sc1\n\t" \
  "global_load_dwordx4 v[92:95], v53, s[90:91] offset:3072 sc1\n\t" \
  "s_branch 39b\n\t" \
  "40:\n\t" \
  "s_cmp_eq_u32 %13, 0\n\t" \
  "s_cbranch_scc1 42f\n\t" \
  "s_sleep 1\n\t" \
  "ds_read_b32 v16, v56 offset:96\n\t" \
  "s_waitcnt lgkmcnt(0)\n\t" \
  "v_readfirstlane_b32 vcc_lo, v16\n\t" \
  "s_nop 1\n\t" \
  "s_cmp_ge_u32 vcc_lo, 1\n\t" \
  "s_cbranch_scc0 40b\n\t" \
  "s_branch 41b\n\t" \
  "42:\n\t" \
  "v_bfe_u32 v19, v12, 4, 4\n\t" \
  "v_lshlrev_b32 v19, 2, v19\n\t" \
  "s_mov_b32 s90, 0\n\t" \
  "142:\n\t" \
  "global_load_dword v16, v19, %21 sc1\n\t" \
  "s_add_u32 s90, s90, 1\n\t" \
  "s_waitcnt vmcnt(0)\n\t" \
  "v_cmp_ne_u32 vcc, v55, v16\n\t" \
  "s_mov_b32 s88, %26\n\t" \
  "s_mov_b32 s89, s88\n\t" \
  "s_and_b64 vcc, vcc, s[88:89]\n\t" \
  "s_cbranch_scc0 242f\n\t" \
  "s_sleep 2\n\t" \
  "s_cmp_lt_u32 s90, 0x400000\n\t" \
  "s_cbranch_scc1 142b\n\t" \
  "242:\n\t" \
  "v_mov_b32 v16, 1\n\t" \
  "ds_max_u32 v56, v16 offset:96\n\t" \
  "s_branch 41b\n\t" \
  "43:\n\t" \
  "s_cmp_eq_u32 %13, 0\n\t" \
  "s_cbranch_scc1 45f\n\t" \
  "s_sleep 1\n\t" \
  "ds_read_b32 v16, v56 offset:96\n\t" \
  "s_waitcnt lgkmcnt(0)\n\t" \
  "v_readfirstlane_b32 vcc_lo, v16\n\t" \
  "s_nop 1\n\t" \
  "s_cmp_ge_u32 vcc_lo, 2\n\t" \
  "s_cbranch_scc0 43b\n\t" \
  "s_branch 44b\n\t" \
  "45:\n\t" \
  "v_bfe_u32 v19, v12, 4, 4\n\t" \
  "v_lshlrev_b32 v19, 2, v19\n\t" \
  "s_mov_b32 s90, 0\n\t" \
  "145:\n\t" \
  "global_load_dword v16, v19, %21 sc1\n\t" \
  "s_add_u32 s90, s90, 1\n\t" \
  "s_waitcnt vmcnt(0)\n\t" \
  "v_cmp_ne_u32 vcc, v55, v16\n\t" \
  "s_mov_b32 s88, %26\n\t" \
  "s_mov_b32 s89, s88\n\t" \
  "s_and_b64 vcc, vcc, -1\n\t" \
  "s_cbranch_scc0 245f\n\t" \
  "s_sleep 2\n\t" \
  "s_cmp_lt_u32 s90, 0x400000\n\t" \
  "s_cbranch_scc1 145b\n\t" \
  "245:\n\t" \
  "v_mov_b32 v16, 2\n\t" \
  "ds_max_u32 v56, v16 offset:96\n\t" \
  "s_branch 44b\n\t" \
  "19:\n\t"
#define C_CLOB "v10","v11","v12","v13","v18","v19","v20","v21","v22","v23","v24","v25","v26","v27","v28","v29","v30","v31","v32","v33","v34","v35","v36","v37","v38","v39","v40","v41","v42","v43","v44","v45","v46","v47","v48","v49","v50","v53","v55","v56","v57","v58","v59","v60","v61","v62","v63","v64","v65","v66","v67","v68","v69","v70","v71","v72","v73","v74","v75","v76","v77","v78","v79","v80","v81","v82","v83","v84","v85","v86","v87","v88","v89","v90","v91","v92","v93","v94","v95","v96","v97","v98","v99","v100","v101","v102","v103","v104","v105","v106","v107","v108","v109","v110","v111","v112","v113","v114","v115","v116","v117","v118","v119","v120","v121","v122","v123","v124","v125","v126","v127","v128","v129","v130","v131","v132","v133","v134","v135","v136","v137","v138","v139","v140","v141","v142","v143","v144","v145","v146","v147","v148","v149","v150","v151","v152","v153","v154","v155","v156","v157","v158","v159","v160","v161","v162","v163","v164","v165","v166","v167","v168","v169","v170","v171","v172","v173","v174","v175","v176","v177","v178","v179","v180","v181","v182","v183","v184","v185","v186","v187","v188","v189","v190","v191","v192","v193","v194","v195","v196","v197","v198","v199","v200","v201","v202","v203","v204","v205","v206","v207","v208","v209","v210","v211","v212","v213","v214","v215","v216","v217","v218","v219","v220","v221","v222","v223","v224","v225","v226","v227","v228","v229","v230","v231","v232","v233","v234","v235","v236","v237","v238","v239","v240","v241","v242","v243","v244","v245","v246","v247","v248","v249","v250","v251","v252","v253","v254","v255"

static_assert(KVP<13 && KVP>TGS+1, "");

typedef unsigned long long u64;
typedef u64 u64x8 __attribute__((ext_vector_type(8)));
typedef u64 u64x4 __attribute__((ext_vector_type(4)));
typedef unsigned u32x8 __attribute__((ext_vector_type(8)));
typedef unsigned u32x16 __attribute__((ext_vector_type(16)));
template<class T> __device__ __forceinline__ T sg(T v){
  if constexpr(sizeof(T)==4) return (T)__builtin_amdgcn_readfirstlane((unsigned)v); else return v; }
#define GP(T,v) ((T*)(__attribute__((address_space(1))) T*)(v))
__global__ __launch_bounds__(512,2) __attribute__((aligned(4096))) void fused_kernel(const u16* x_, const u16* wqkv_, const u16* kin_, const u16* vin_, const u16* shk_, const u16* hk_,
    const u16* wo_, const u16* qin_, const u16* sD_, u16* out_, u16* ws_, u16* unused_)
{
#define ARGS0() u64x8 ka; u64x4 kb; asm volatile("s_load_dwordx16 %0, %2, 0\n\ts_load_dwordx8 %1, %2, 64" : "=&s"(ka), "=&s"(kb) : "s"(__builtin_amdgcn_kernarg_segment_ptr()) : "memory")
#define ARGS() asm volatile("s_waitcnt lgkmcnt(0)" : "+s"(ka), "+s"(kb) :: "memory"); \
  const u16 *x=GP(const u16,ka[0]), *wqkv=GP(const u16,ka[1]), *kin=GP(const u16,ka[2]), *vin=GP(const u16,ka[3]), *shk=GP(const u16,ka[4]), *hk=GP(const u16,ka[5]), *wo=GP(const u16,ka[6]), *qin=GP(const u16,ka[7]), *sD=GP(const u16,kb[0]); \
  u16 *out=GP(u16,kb[1]), *o=GP(u16,kb[2]); char* ws=(char*)o; unsigned cpar=xcd>>2; \
  auto loc=[&](unsigned off)->unsigned{ return off|(((cpar^(off>>21))&1)<<13); }; \
  (void)x; (void)wqkv; (void)kin; (void)vin; (void)shk; (void)hk; (void)wo; (void)qin; (void)sD; (void)out; (void)loc
  __shared__ __attribute__((aligned(1024))) char smem[ALL_LDS];
  const int tid=threadIdx.x;
  const int w8=__builtin_amdgcn_readfirstlane(tid>>6);
  asm volatile("s_sub_u32 vcc_lo, %0, 4\n\ts_cbranch_scc1 1f\n\ts_cmp_lg_u32 %0, 7\n\ts_cbranch_scc1 2f\n\t"
      "s_lshr_b32 vcc_lo, %2, 3\n\ts_mul_i32 vcc_lo, vcc_lo, %5\n\tv_and_b32 v254, 15, %1\n\tv_lshl_add_u32 v254, v254, 7, vcc_lo\n\ts_getpc_b64 vcc\n\tglobal_load_dword v255, v254, vcc\n\ts_branch 1f\n"
      "2:\n\ts_and_b32 vcc_hi, %2, 7\n\ts_cbranch_scc1 1f\n\ts_cmp_gt_u32 %2, 32\n\ts_cbranch_scc1 1f\n\ts_mul_i32 vcc_hi, %2, 3\n\ts_lshr_b32 vcc_hi, vcc_hi, 3\n\ts_add_u32 vcc_lo, vcc_lo, vcc_hi\n\t"
      "v_and_b32 v253, 3, %1\n\tv_and_b32 v254, 4, %1\n\tv_lshlrev_b32 v253, 7, v253\n\tv_lshl_add_u32 v253, v254, 11, v253\n\tv_lshl_add_u32 v253, vcc_lo, 9, v253\n\tv_add_u32 v253, %4, v253\n\t"
      "s_load_dwordx2 vcc, %3, 80\n\ts_mov_b64 exec, 255\n\ts_waitcnt lgkmcnt(0)\n\tglobal_store_dword v253, v253, vcc sc1\n\ts_mov_b64 exec, -1\n1:"
      :: "s"(w8), "v"(tid), "s"(blockIdx.x), "s"(__builtin_amdgcn_kernarg_segment_ptr()), "n"(WS_GEN+16384), "n"(CODE_ST) : "memory", "scc", "vcc", "v253", "v254", "v255");
  LDS_AS unsigned* const ct=(LDS_AS unsigned*)(LDS_AS char*)(smem+CT_OFF);
  volatile LDS_AS unsigned* const vct=ct;
  auto all4=[&](int i)->unsigned{ u32x4 v=*(volatile LDS_AS u32x4*)(ct+i); return __builtin_amdgcn_readfirstlane(v[0]&v[1]&v[2]&v[3]); };
#define ROTF(b) ((4+4*xr+4*xc+8*(b))&15)
#define BLOCK() int bid=blockIdx.x, w=w8&3; asm volatile("" : "+s"(bid), "+s"(w)); __builtin_assume(bid>=0 && bid<256 && w>=0 && w<4); DERIVE()
#define DERIVE() const int xcd=bid&7, idx=bid>>3, ra=idx&3, rbb=idx>>2, xr=xcd>>1, xc=xcd&1, rb=xr*4+ra, hp=xc*8+rbb, s0=rb*128, tm=rb, tn=hp^TNX, cb=(xcd>>2)*128+idx*4+(xcd&3), gi=cb^16; (void)s0; (void)gi; \
  auto korder=[&]()->unsigned long long{ const unsigned N=0x7654210u, sh=4*(KR), q=(((N>>sh)|(N<<(28-sh)))&0xfffffffu)|0x30000000u; \
    return ((unsigned long long)q<<32|q)^(xc? 0x88888888ull : 0x8888888800000000ull); }; \
  const unsigned l1m=0xf7u<<(8*xc); (void)l1m; (void)korder

  if(__builtin_expect(w8>=4,0)){
  BLOCK();
  const int hd=w>>1, par=w&1;
  __builtin_amdgcn_s_setprio(CPRIO);
  {
    ARGS0(); LANES();
    if(w==0){ if(l<52) ct[l]=0u; if(idx==0) *(u32x4*)(smem+AUX+l*16)=u32x4{0,0,0,0}; }
    unsigned a0b=fr*128+((g^(fr&7))*16);
    const int r0=8*g+tq;
    unsigned bbb=r0*128+(((2*hd)^wswz(r0))*32)+8*tp;
    unsigned kA=AUX+(fr^3)*KVP_+w*64+(g&1)*16-(g>>1)*256, kB=AUX+(20+4*g-tq)*KVP_+w*64+8*tp;
    const int cch=127-4*idx-g;
    unsigned kvo=(8*w+tq)*32768+(cch>>6)*16384+((cch>>4)&3)*2048+(cch&15)*64+((tp^((cch>>2)&3))*16);
    unsigned sho=((xcd^KX)*128+32*w+fr)*8; const unsigned ctl=CT_OFF+4*w;
    u32x4 sel;
    { const unsigned b2=(g>>1)? 0x0302u : 0x0100u, sv=(fr&1)? (b2<<16|0x0c0cu) : (0x0c0c0000u|b2);
      #pragma unroll
      for(int i=0;i<4;i++) sel[i]= (fr>>1)==(g&1)*4+i ? sv : 0x0c0c0c0cu; }
    unsigned msk=0, lx=w*1024+l*16;
    #pragma unroll
    for(int e=0;e<4;e++){ const int d=4*g+e-fr; const unsigned pv=d<9, hv=par? pv : (unsigned)(d<1 || 4*g+e>7);
      msk|=((unsigned)(d+7)>7u?1u:0u)<<e | pv<<(4+e) | hv<<(8+e) | (d<=0?1u:0u)<<(12+e); }
    msk|=(unsigned)(g? l-16 : 48+((l-1)&15))<<26;
    const unsigned hb=__builtin_amdgcn_readfirstlane(rb==0 ? 0xcfa8965fu : 0u);
    unsigned ap=AUX+w*CV_W+(tq&1)*(CV_CP-4)+8+32*g+4*tq+8*tp;
    u32x4 bp;
    #pragma unroll
    for(int b=0;b<4;b++){ const int c=15+4*b-fr+g; bp[b]=c*64+((w^((c>>2)&3))*16); }
    asm volatile("" : "+v"(a0b), "+v"(bbb), "+v"(kA), "+v"(kB), "+v"(kvo), "+v"(sho), "+v"(msk), "+v"(lx), "+v"(ap), "+v"(bp), "+v"(sel));
    ARGS();
    const char* ow=ws+loc((rb*16+hp)*32768)+(par*2+hd)*1024+4096;
    asm volatile("" : "+s"(shk));
    asm volatile(C_ASM : "+{v52}"(a0b), "+{v54}"(bbb), "+{v0}"(kA), "+{v1}"(kB), "+{v2}"(kvo), "+{v51}"(sho), "+{v3}"(msk), "+{v4}"(lx), "+{v5}"(ap), "+{v[6:9]}"(bp), "+{v[14:17]}"(sel)
       : "s"(shk), "s"(ctl), "s"(w), "s"(ow), "s"(hb), "s"(0.17677669529663687f*1.44269504088896341f), "{s[92:93]}"(ws+loc(tm*16*32768)), "s"(korder()),
         "s"(ws+loc(flo(1,rb*16))+hp*4), "s"(AUX+w*(CV_W+64)), "s"(ws+loc(flo(1,tm*16+tn))),
         "s"(out+(size_t)(tm*128+w*32+1)*2048+1024+tn*64), "s"(8192+w*4096), "s"(ws+loc(WS_KV+(xcd^KX)*32*32768)), "s"(ws+loc(flo(0,(xcd^KX)*32))+idx*4), "s"(l1m), "s"(xcd==0 && idx<5 && w<3? (u64)(ws+WS_GEN+idx*3+w) : 0ull) : "memory", "scc", "vcc", "s68", "s69", "s70", "s71", "s74", "s75", "s76", "s77", "s88", "s89", "s90", "s91",
       C_CLOB);

  }
  return;
  }
  {
    ARGS0(); BLOCK(); LANES();
    const unsigned vx=(l>>3)*4096+(((l&7)^(l>>3))*16), vxs=vx+(s0+32*w)*2048, vw=(l>>3)*6144+((((l&7)>>1)^((l>>4)&1))*32)+(l&1)*16+hp*128+w*98304, vw1=(vw^64)+49152-1024;
    u32x8 va; u32x2 vb; unsigned vsp;
    #pragma unroll
    for(int i=0;i<6;i++) va[i]=((i&1)? vw1 : vw)+(i>>1)*2048;
    va[6]=vxs; va[7]=vxs+32768-1024; vb[0]=vxs+2048; vb[1]=vxs+2048+32768-1024;
    vsp=vx+(rb? s0-16+w : 0)*2048;
    const unsigned sw1=w*1024, sw2=w*2048;
    unsigned rot=ROTF(rbb);
    asm volatile("" : "+v"(va), "+v"(vb), "+v"(vsp));
    asm volatile("" : "+s"(rot));
    unsigned vt=(w&1)? (xr*512+idx*16+(l>>1))*2048+(l&1)*1024 : (2*idx+(l&1))*6144+((l>>1)&3)*2048+xc*1024;
    const int kt0=idx*64; int kvs=KS0+KVSEL;
    unsigned kvl=(kt0+16*w+g)*2048+(xcd^KX)*256+fr*16, l16=l*16;
    const bool hl= w==0 && kt0;
    unsigned c46=sg(w<2? 128u : w==2? 393216u : 0u), c49=sg(w<2? sw1 : (unsigned)ALL_LDS-96), c64=sg(hl? 8192u : 0u), c68=sg(hl? AUX : ALL_LDS-96);
    unsigned long long c50= w<2? ~0ull : w==2? 63ull : 1ull, c66= hl? ~0ull : 1ull;
    asm volatile("" : "+v"(vt), "+v"(kvl), "+v"(l16));
    asm volatile("" : "+s"(c46), "+s"(c49), "+s"(c64), "+s"(c68), "+s"(c50), "+s"(c66), "+s"(kvs));
    ARGS();
    asm volatile(H0_ASM : "+{s[40:41]}"(x), "+{s[42:43]}"(wqkv) : "{v[0:7]}"(va), "{v[8:9]}"(vb), "{v10}"(vsp), "{s52}"(rot), "{s53}"(sw2), "{s59}"(w)
      : "memory", "scc", "s76", "s77", "s78", "s79", "s82", "s83");
    const u16* spb= w==2? wqkv : x;
    const int hl3=(3-(int)((size_t)spb>>7))&7, roth=ROTF(hl3), xs=((hl3-ROTF(0))&7)-LEAD;
    unsigned i11=sg((w==2? roth+LEAD+1 : rot+1)&15), sbo, sst;
    if(w>=2){
      vsp=vt+hl3*128;
      touch(spb, vsp+((roth+(l>>3))&15)*c46, w==2? 0x3f3f3f3f3f3f3f3full>>(56-8*LEAD) : xs<=1? 0xffffffffull : 1ull);
    }
    unsigned gof=(((gi&15)*128+64*(w&1)+4*(w>>1)+16*((l>>5)&1)+((l>>3)&3))*DM+(gi>>4)*64)*2+(l16&127);
    unsigned wv[2];
    wv[0]=(((l>>5)*16+w*4+((l>>3)&3))*DM+tn*64)*2+((l16&127)^((l&16)*2)^((w&1)*64)); wv[1]=wv[0]+32*DM*2;
    unsigned zpa=(w>>1)*9216+(w&1)*512+l16, hda=AUX+w*CV_W+(l&1)*CV_CP+(l>>1)*16, yra=AUX+tq*(CV_W+64)+(g>>1)*128+(g&1)*32+tp*8+w*1024;
    const unsigned f0=loc(flo(0,cb)), f1=loc(flo(1,tm*16+tn)), f2=loc(flo(2,gi));
    unsigned vc=(w&1)? (g==0? WS_GEN : (g==1? f0 : g==2? f1 : f2)^((l&1)<<13)) : idx*CODE_ST+fr*128u;
    unsigned kfo=(f0^((l&32)<<8))+(l&31)*4, ofo= w==0? f1+fr*4 : WS_GEN;
    asm volatile("" : "+v"(kfo), "+v"(ofo), "+v"(gof), "+v"(wv[0]), "+v"(wv[1]), "+v"(hda), "+v"(yra), "+v"(zpa), "+v"(vc));
    unsigned long long kt=korder();
    asm volatile("" : "+s"(kt));
#define H_IN "{v[0:7]}"(va), "{v[8:9]}"(vb), "{v10}"(vsp), "{v11}"(kvl), "{s[40:41]}"(x), "{s[42:43]}"(wqkv), "{s[44:45]}"(spb), "{s46}"(c46), "{s49}"(c49), "{s[50:51]}"(c50), "{s53}"(sw2), \
      "{s58}"(kvs), "{s59}"(w), "{s[60:61]}"(kin), "{s[62:63]}"(vin), "{s64}"(c64), "{s[66:67]}"(c66), "{s68}"(c68)
#define H_CLOB "memory", "scc", "vcc", "s76", "s77", "s78", "s79", "s80", "s81", "s82", "s83", "s84", "s85", "v20", "v21", "v22", "v253", "v254", "v255"
    asm volatile(H1_ASM : "+{s47}"(i11), "+{s52}"(rot), "={s54}"(sbo), "={s55}"(sst) : H_IN : H_CLOB);
    const u16* hrow=hk+(size_t)(cb*4+w)*S;
    u32x4 h0; unsigned dvu;
    asm volatile("s_load_dwordx4 %0, %2, 0\n\ts_load_dword %1, %3, %4" : "=&s"(h0), "=&s"(dvu) : "s"(hrow), "s"(sD), "s"(cb*8+(w&2)*2) : "memory");
    unsigned long long pc; asm volatile("s_getpc_b64 %0" : "=s"(pc)); pc&=~4095ull;
    asm volatile("" : "+s"(wo), "+s"(qin), "+s"(out), "+s"(ws));
    __builtin_amdgcn_s_setprio(HP1);
    asm volatile(H_ASM : "+{s47}"(i11), "+{s52}"(rot), "+{s54}"(sbo), "+{s55}"(sst)
      : H_IN, "{v14}"(vc), "{v13}"(WS_GEN+256+(l&31)*4), "{s56}"(sg(3u<<(kvs-1) | 1u<<15)),
        "{s57}"(sg(((w&1)? (1u<<TGS | 3u<<PLS | (xs>1? 8u : 0u)) : 1u<<CPS)&~(unsigned)(c50>>32))), "{s[70:71]}"(ws), "{s[72:73]}"(pc) : H_CLOB);
    asm volatile("" : "+s"(h0), "+s"(dvu));
    asm volatile("" : "+s"(bid), "+s"(w)); __builtin_assume(bid>=0 && bid<256 && w>=0 && w<4);
    { DERIVE(); const unsigned sw1=w*1024, sw2=w*2048;
    u32x4 qv[4];
    __builtin_amdgcn_s_setprio(HP2);
    unsigned kf_;
    asm volatile("global_load_dword %0, %1, %2 sc1" : "=v"(kf_) : "v"(kfo), "s"(ws) : "memory");
    { const unsigned fa=AUX+w*CV_W+32;
      asm volatile("s_mov_b64 exec, -2\n\ts_add_u32 m0, %3, %4\n\ts_nop 0\n\tglobal_load_lds_dwordx4 %0, %1" LDPOL "\n\ts_mov_b32 m0, %3\n\ts_nop 0\n\tglobal_load_lds_dwordx4 %0, %2" LDPOL "\n\ts_mov_b64 exec, -1"
        :: "v"(l16), "s"(hrow), "s"(hrow-2), "s"(fa), "n"(CV_CP) : "memory", "scc");
      #pragma unroll
      for(int i=1;i<4;i++){ gldsn(hrow+i*512, l16, fa, CV_CP+i*1024); gldsn(hrow-2+i*512, l16, fa, i*1024); } }
    WAITV(9);
    vct[32+w]=1u;
    const unsigned tag=__builtin_amdgcn_readfirstlane(vct[26]);
    const unsigned pl=__builtin_amdgcn_readfirstlane(vct[6]); cpar=(xcd>>2)^(pl>>13); if(w==0) ofo^=pl;
    u32x4 hv;
    { const unsigned d0=(h0[0]&0xffff0000u)|(pack2(bflo(h0[0])+((w&1)? bfhi(dvu) : bflo(dvu)),0.f)&0xffffu);
      hv= l==4? u32x4{0,d0,h0[1],h0[2]} : l==5? u32x4{d0,h0[1],h0[2],h0[3]} : u32x4{0,0,0,0}; }
    asm volatile("s_mov_b64 exec, 63\n\tds_write_b128 %0, %1\n\ts_mov_b64 exec, %3\n\tds_write_b128 %2, %1 offset:7680\n\ts_mov_b64 exec, -1"
      :: "v"(hda), "v"(hv), "v"(zpa), "s"(0xffffffff00000000ull) : "memory");
#define QLD() asm volatile("global_load_dwordx4 %0, %4, %5" LDPOL "\n\tglobal_load_dwordx4 %1, %4, %6" LDPOL "\n\tglobal_load_dwordx4 %2, %4, %7" LDPOL "\n\tglobal_load_dwordx4 %3, %4, %8" LDPOL \
        : "=&v"(qv[0]), "=&v"(qv[1]), "=&v"(qv[2]), "=&v"(qv[3]) : "v"(gof), "s"(qin), "s"(qin+8*DM), "s"(qin+32*DM), "s"(qin+40*DM) : "memory")
    auto kvt=[&](){
      #pragma unroll
      for(int i=0;i<4;i++) gldsc(ws+loc(WS_KV+cb*32768)+(w>>1)*16384+(w&1)*4096+i*1024, l16, sw2, (i&1)*9216+(i>>1)*1024); };
    asm volatile("s_waitcnt vmcnt(8)" : "+v"(kf_) :: "memory");
    if((unsigned)(__ballot(kf_!=tag)>>(pl>>8))) wait_flag(ws+loc(flo(0,cb)), (l&31)*4, tag);
    kvt();
    auto wos=[&](int st){ const u16* b=wo+(size_t)((kt>>(4*st))&15)*64*DM; glds(b,wv[0],sw1,WOB+st*8192); glds(b,wv[1],sw1,WOB+st*8192+4096); };
    wos(0); wos(1);
    while(!all4(36)) __builtin_amdgcn_s_sleep(1);
    WAITV(2*NE); WAITL();
    vct[16+w]=1u;
    __builtin_amdgcn_s_setprio(HP4);
    QLD();
    #pragma unroll
    for(int st=NE;st<13;st++){ if(st==7) while(!all4(0)) __builtin_amdgcn_s_sleep(1);
      WAITV(W1);
      wos(st); if(st==7+W1/2) vct[20+w]=7u; }
    unsigned op;
    asm volatile("global_load_dword %0, %1, %2 sc1" : "=v"(op) : "v"(ofo), "s"(ws) : "memory");
    WAITV(1); vct[20+w]=13u;
    asm volatile("s_waitcnt vmcnt(0)" : "+v"(qv[0]), "+v"(qv[1]), "+v"(qv[2]), "+v"(qv[3]), "+v"(op) :: "memory");
    unsigned oseen=0;
    auto olevel=[&](unsigned v){
      const unsigned ne=(unsigned)__ballot(v!=tag), lv= ne==0 ? 2u : (ne&l1m)==0 ? 1u : 0u;
      if(lv>oseen){ oseen=lv; if(l==0) __hip_atomic_fetch_max(ct+24, lv, __ATOMIC_RELAXED, __HIP_MEMORY_SCOPE_WORKGROUP); } };
    auto opoll=[&](){ olevel(ld4c(ws,ofo)); };
    if(w==0) olevel(op);
    float qf[32];
    auto qun=[&](){
    #pragma unroll
    for(int i=0;i<16;i++){ qf[2*i]=bflo(qv[i>>2][i&3]); qf[2*i+1]=bfhi(qv[i>>2][i&3]); }
    #pragma unroll
    for(int i=0;i<32;i+=8) asm volatile("" : "+v"(qf[i]), "+v"(qf[i+1]), "+v"(qf[i+2]), "+v"(qf[i+3]), "+v"(qf[i+4]), "+v"(qf[i+5]), "+v"(qf[i+6]), "+v"(qf[i+7]));
    };
    qun();
    auto wol=[&](int i){ glds(wo+(size_t)((kt>>(52+4*(i>>1)))&15)*64*DM, wv[i&1], sw1, i<4? AUX+4352+i*CV_W : (i&1)*4096); };
    while(!all4(44)){ if(w==0 && oseen<1) opoll(); else __builtin_amdgcn_s_sleep(1); }
    wol(0); wol(1); wol(2); wol(3);
    while(!all4(48)){ if(w==0 && oseen<1) opoll(); else __builtin_amdgcn_s_sleep(1); }
    wol(4); wol(5);
    __builtin_amdgcn_s_setprio(HP3);
    bool gen= !(bid==0 && w==1);
    auto trygen=[&](){
      u32x4 v; asm volatile("global_load_dwordx4 %0, %1, %2 sc1\n\ts_waitcnt vmcnt(0)" : "=v"(v) : "v"((loc(flo(0,(l>>3)*32))^((l&32)<<8))+(l16&127)), "s"(ws) : "memory");
      if(__all(v[0]==tag && v[1]==tag && v[2]==tag && v[3]==tag)){ if(l==0) st4c(ws+WS_GEN, tag); gen=true; } };
    if(!gen) trygen();
    while(!((all4(8)>>w)&1)){
      if(w==0 && oseen<2) opoll();
      __builtin_amdgcn_s_sleep(OPOLLS);
    }
    __builtin_amdgcn_s_setprio(HP5);
    {
      const char* yb=smem+yra;
      s16x4 ya[4], yc[4];
      #pragma unroll
      for(int i=0;i<4;i++){ ya[i]=trread(yb+(i>>1)*512+(i&1)*64); yc[i]=trread(yb+(i>>1)*512+(i&1)*64+256); }
      #pragma unroll
      for(int i=0;i<4;i++) st16c(ws+loc(WS_Y+(cb&~15)*32768)+(w*4+i)*32768+((cb>>3)&1)*16384+(cb&7)*1024, l16, __builtin_bit_cast(u32x4,__builtin_shufflevector(ya[i],yc[i],0,1,2,3,4,5,6,7)));
    }
    unsigned po=gof+(gof&~2047u);
    const char* p0=ws+loc(WS_Y+gi*32768);
    unsigned y0=g*1024+w*256+((fr^(g&2))*16);
    asm volatile("" : "+v"(y0), "+v"(po));
    asm volatile("" : "+s"(p0));
    asm volatile("s_waitcnt vmcnt(0)\n\ts_mov_b64 exec, 15\n\tglobal_store_dword %0, %1, %2 sc1\n\ts_mov_b64 exec, -1" :: "v"(l16*8), "v"(tag), "s"(ws+loc(flo(2,cb&~15))+w*512+(cb&15)*4) : "memory");
    vct[20+w]=16u;
    if(w==0){ int sp_=0; while(oseen<2 && vct[24]<2u && ++sp_<(1<<22)){ opoll(); __builtin_amdgcn_s_sleep(8); } }
    if(w==0){
      const char* fp=ws+loc(flo(2,gi)); unsigned fo=fr*4; int spins=0; asm volatile("" : "+s"(fp)); asm volatile("" : "+v"(fo));
      while(vct[27]==0u) __builtin_amdgcn_s_sleep(2);
      while(true){
        unsigned v=ld4c(fp,fo);
        if(__all(v==tag) || ++spins>(1<<20)) break;
        __builtin_amdgcn_s_sleep(POLLS);
      }
      { u32x4 v; do{ v=*(volatile LDS_AS u32x4*)(ct+20); }while(__builtin_amdgcn_readfirstlane(v[0]&v[1]&v[2]&v[3]&16u)==0); }
      vct[25]=1u;
    } else { while(vct[25]==0u) __builtin_amdgcn_s_sleep(2); }
    {
      u32x4 yv[4];
      #pragma unroll
      for(int i=0;i<4;i++) gldsc(p0+(i>>1)*16384+(i&1)*4096, y0^(i*64), w*CV_W, AUX+i*1024);
      const char* ys=smem+AUX+w*CV_W+(l&7)*512;
      const int yi=((l>>3)^((l&7)*2))*16;
      WAITV(0);
      yv[0]=*(const u32x4*)(ys+yi); yv[1]=*(const u32x4*)(ys+yi+256); yv[2]=*(const u32x4*)(ys+(yi^128)); yv[3]=*(const u32x4*)(ys+(yi^128)+256);
      #pragma unroll
      for(int r=0;r<4;r++){
        u32x4 ov;
        #pragma unroll
        for(int e=0;e<4;e++){ const unsigned y2=yv[(r>>1)*2+(e>>1)][(r&1)*2+(e&1)]; ov[e]=pack2(qf[r*8+2*e]*bflo(y2), qf[r*8+2*e+1]*bfhi(y2)); }
        stos(out+((r&1)*8+(r>>1)*32)*2048, po, ov);
      }
    }
    for(int sp_=0; !gen && sp_<(1<<22); sp_++) trygen();
    }
  }

}

extern "C" void kernel_launch(void* const* d_in, const int* in_sizes, int n_in, void* d_out, int out_size, void* d_ws, size_t ws_size, hipStream_t stream){
  const u16* q=(const u16*)d_in[0]; const u16* k=(const u16*)d_in[1]; const u16* v=(const u16*)d_in[2];
  const u16* shk=(const u16*)d_in[3]; const u16* hk=(const u16*)d_in[4]; const u16* sD=(const u16*)d_in[5];
  const u16* x=(const u16*)d_in[6]; const u16* wqkv=(const u16*)d_in[7]; const u16* wo=(const u16*)d_in[8];
  u16* ws=(u16*)(((size_t)d_ws+16383)&~(size_t)16383);
  fused_kernel<<<256,512,0,stream>>>(x,wqkv,k,v,shk,hk,wo,q,sD,(u16*)d_out,ws,ws);
}
```

```cpp
#include <hip/hip_runtime.h>
#include <hip/hip_bf16.h>
#include <stdint.h>
#define DBG_OFF (28u<<20)
#define SHOWC

typedef unsigned short u16;
typedef short s16x4 __attribute__((ext_vector_type(4)));
typedef float f32x32 __attribute__((ext_vector_type(32)));
typedef unsigned u32x4 __attribute__((ext_vector_type(4)));
typedef unsigned u32x2 __attribute__((ext_vector_type(2)));
#define LDS_AS __attribute__((address_space(3)))

__device__ __forceinline__ float bflo(unsigned x){ return __uint_as_float(x<<16); }
__device__ __forceinline__ float bfhi(unsigned x){ return __uint_as_float(x&0xffff0000u); }
typedef float f32x2 __attribute__((ext_vector_type(2)));
typedef __bf16 bf16x2_t __attribute__((ext_vector_type(2)));
__device__ __forceinline__ unsigned pack2(float lo, float hi){ return __builtin_bit_cast(unsigned, __builtin_convertvector(f32x2{lo,hi}, bf16x2_t)); }

#define GLDS(name,pol) __device__ __forceinline__ void name(const void* sb, unsigned voff, unsigned ldsb, int imm){ \
  asm volatile("s_add_u32 m0, %2, %3\n\ts_nop 0\n\tglobal_load_lds_dwordx4 %0, %1" pol :: "v"(voff), "s"(sb), "s"(ldsb), "n"(imm) : "memory", "scc"); }
#define LDPOL " nt"
GLDS(glds,"") GLDS(gldsn,LDPOL) GLDS(gldsc," sc1")
#define PACE 63
__device__ __forceinline__ void gldsp(const void* sb, unsigned voff, unsigned ldsb, int imm){
  asm volatile("s_add_u32 m0, %2, %3\n\ts_waitcnt vmcnt(%4)\n\tglobal_load_lds_dwordx4 %0, %1" :: "v"(voff), "s"(sb), "s"(ldsb), "n"(imm), "n"(PACE) : "memory", "scc"); }
__device__ __forceinline__ void touch(const void* sb, unsigned voff, unsigned long long mask){
  asm volatile("s_mov_b64 exec, %2\n\tglobal_load_dword v255, %0, %1\n\ts_mov_b64 exec, -1" :: "v"(voff), "s"(sb), "s"(mask) : "memory", "v255"); }
__device__ __forceinline__ void st16c(void* sb, unsigned vo, u32x4 v){ asm volatile("global_store_dwordx4 %0, %1, %2 sc1\n\ts_nop 1" :: "v"(vo), "v"(v), "s"(sb) : "memory"); }
#define OUTPOL " sc1 nt"
__device__ __forceinline__ void stos(void* sb, unsigned vo, u32x4 v){ asm volatile("global_store_dwordx4 %0, %1, %2" OUTPOL "\n\ts_nop 1" :: "v"(vo), "v"(v), "s"(sb) : "memory"); }
__device__ __forceinline__ void stoa(void* p, u32x4 v){ asm volatile("global_store_dwordx4 %0, %1, off" OUTPOL "\n\ts_nop 1" :: "v"(p), "v"(v) : "memory"); }
__device__ __forceinline__ void st4c(void* p, unsigned v){ asm volatile("global_store_dword %0, %1, off sc1" :: "v"(p), "v"(v) : "memory"); }
__device__ __forceinline__ void wait_flag(const unsigned* p, unsigned tag){
  int spins=0;
  while(true){
    unsigned v=__hip_atomic_load(p, __ATOMIC_RELAXED, __HIP_MEMORY_SCOPE_AGENT);
    if(__all(v==tag)) break;
    __builtin_amdgcn_s_sleep(2);
    if(++spins>(1<<22)) break;
  }
}
#define WAITV(n) asm volatile("s_waitcnt vmcnt(%0)"::"n"(n):"memory")
#define WAITL() asm volatile("s_waitcnt lgkmcnt(0)":::"memory")
#define BAR() do{ asm volatile("s_waitcnt lgkmcnt(0)":::"memory"); __builtin_amdgcn_s_barrier(); asm volatile("":::"memory"); }while(0)
#define LANES() int l; asm volatile("v_mbcnt_lo_u32_b32 %0, -1, 0\n\tv_mbcnt_hi_u32_b32 %0, -1, %0" : "=v"(l)); __builtin_assume(l>=0 && l<64); const int g=l>>4, fr=l&15, tq=(l&15)>>2, tp=l&3; (void)g; (void)fr; (void)tq; (void)tp;

__device__ __forceinline__ s16x4 trread(const char* p){ return __builtin_amdgcn_ds_read_tr16_b64_v4i16((LDS_AS s16x4*)p); }
__device__ __forceinline__ int wswz(int row){ return ((row>>1)&1) | (((row>>3)&1)<<1); }
__device__ __forceinline__ u32x4 widen(u32x2 a, u32x2 b){
  auto r0=__builtin_amdgcn_permlane16_swap(a[0],b[0],false,false);
  auto r1=__builtin_amdgcn_permlane16_swap(a[1],b[1],false,false);
  return u32x4{r0[0],r1[0],r0[1],r1[1]};
}

constexpr int S=2048, DM=1024;
constexpr int K1_XB=144*128, K1_WB=3*64*128, K1_ST=K1_XB+K1_WB;
constexpr int AUX=3*K1_ST, KVP_=1040;
constexpr int WOB=22528, CV_W=8448, CV_CP=4224;
constexpr int CT_OFF=AUX+33*KVP_;
constexpr int ALL_LDS=CT_OFF+208+96;
constexpr unsigned WS_KV=8u<<20, WS_Y=16u<<20, WS_FL=24u<<20, WS_GEN=WS_FL+(1u<<20);
__device__ __forceinline__ unsigned flo(int kind, int c){ return WS_FL+kind*65536+(c>>6)*16384+(c&63)*128; }

#define CPRIO 2
#define CODE_ST 1610
#define LEAD 3
#define HP3 3
#define HP2 3
#define HP4 3
#define QP 1
#define TNX 1
#define HWS 99
#define HP1 0
#define HUNR 15
#define NE 2
#define WOC 0
#define NS 4
#define PTCH 1
#define GATE 0
#define WRD 2
#define EP 99
#define EP2 99
#define HP5 3
#define NOWO 0
#define W0 6
#define OPD 8
#define WT0 99
#define W1 2
#define W2 2
#define W3 4
#define OPOLLS 1
#define POLLS 3
#define HPRE 0
#define CPS2 99
#define KR ((rbb+2*ra)%7)
#define NOY 0
#define YLDS 1
#define OLDS 0
#define KVSEL (rbb==3? 0 : idx%KSN)
#define KS0 1
#define KSN 3
#define KST 10
#define KVP 5
#define TGS 2
#define CPS 13
#define PLS 4
#define H0_ASM \
  "s_mul_i32 s76, s52, 0x60000\n\t" \
  "s_add_u32 s76, s42, s76\n\t" \
  "s_addc_u32 s77, s43, 0\n\t" \
  "s_add_u32 s82, s53, 86016\n\t" \
  "s_add_u32 m0, s82, 18432\n\t" \
  "s_lshl_b32 s78, s52, 7\n\t" \
  "global_load_lds_dwordx4 v0, s[76:77]\n\t" \
  "global_load_lds_dwordx4 v1, s[76:77] offset:1024\n\t" \
  "s_add_u32 s78, s40, s78\n\t" \
  "s_addc_u32 s79, s41, 0\n\t" \
  "s_add_u32 m0, s82, 2048\n\t" \
  "s_nop 0\n\t" \
  "global_load_lds_dwordx4 v6, s[78:79]\n\t" \
  "global_load_lds_dwordx4 v7, s[78:79] offset:1024\n\t" \
  "s_add_u32 m0, s82, 26624\n\t" \
  "s_nop 0\n\t" \
  "global_load_lds_dwordx4 v2, s[76:77]\n\t" \
  "global_load_lds_dwordx4 v3, s[76:77] offset:1024\n\t" \
  "s_add_u32 m0, s82, 10240\n\t" \
  "s_nop 0\n\t" \
  "global_load_lds_dwordx4 v8, s[78:79]\n\t" \
  "global_load_lds_dwordx4 v9, s[78:79] offset:1024\n\t" \
  "s_add_u32 m0, s82, 34816\n\t" \
  "s_nop 0\n\t" \
  "global_load_lds_dwordx4 v4, s[76:77]\n\t" \
  "global_load_lds_dwordx4 v5, s[76:77] offset:1024\n\t" \
  "s_cmp_gt_u32 s59, 1\n\t" \
  "s_cbranch_scc1 1f\n\t" \
  "s_lshr_b32 s83, s53, 1\n\t" \
  "s_add_u32 m0, s83, 86016\n\t" \
  "s_nop 0\n\t" \
  "global_load_lds_dwordx4 v10, s[78:79]\n\t" \
  "1:\n\t"
#define H1_ASM \
  "s_mov_b32 s55, 0\n\t" \
  "s_mov_b32 s54, 0\n\t" \
  "s_add_u32 s52, s52, 1\n\t" \
  "s_and_b32 s52, s52, 15\n\t" \
  "s_cmp_eq_u32 s58, 1\n\t" \
  "s_cbranch_scc1 1f\n\t" \
  "s_mul_i32 s76, s52, 0x60000\n\t" \
  "s_add_u32 s76, s42, s76\n\t" \
  "s_addc_u32 s77, s43, 0\n\t" \
  "s_lshl_b32 s78, s52, 7\n\t" \
  "s_add_u32 s78, s40, s78\n\t" \
  "s_addc_u32 s79, s41, 0\n\t" \
  "s_mul_i32 s80, s47, s46\n\t" \
  "s_add_u32 s80, s44, s80\n\t" \
  "s_addc_u32 s81, s45, 0\n\t" \
  "s_mov_b32 s82, s53\n\t" \
  "s_add_u32 m0, s82, 18432\n\t" \
  "s_add_u32 s52, s52, 1\n\t" \
  "global_load_lds_dwordx4 v0, s[76:77]\n\t" \
  "global_load_lds_dwordx4 v1, s[76:77] offset:1024\n\t" \
  "s_add_u32 m0, s82, 2048\n\t" \
  "s_and_b32 s52, s52, 15\n\t" \
  "global_load_lds_dwordx4 v6, s[78:79]\n\t" \
  "global_load_lds_dwordx4 v7, s[78:79] offset:1024\n\t" \
  "s_add_u32 m0, s82, 26624\n\t" \
  "s_add_u32 s47, s47, 1\n\t" \
  "global_load_lds_dwordx4 v2, s[76:77]\n\t" \
  "global_load_lds_dwordx4 v3, s[76:77] offset:1024\n\t" \
  "s_add_u32 m0, s82, 10240\n\t" \
  "s_and_b32 s47, s47, 15\n\t" \
  "global_load_lds_dwordx4 v8, s[78:79]\n\t" \
  "global_load_lds_dwordx4 v9, s[78:79] offset:1024\n\t" \
  "s_add_u32 m0, s82, 34816\n\t" \
  "s_and_b32 s83, s54, s51\n\t" \
  "global_load_lds_dwordx4 v4, s[76:77]\n\t" \
  "global_load_lds_dwordx4 v5, s[76:77] offset:1024\n\t" \
  "s_add_u32 m0, s83, s49\n\t" \
  "s_mov_b64 exec, s[50:51]\n\t" \
  "global_load_lds_dwordx4 v10, s[80:81]\n\t" \
  "s_mov_b64 exec, -1\n\t" \
  "s_mov_b32 s55, 1\n\t" \
  "s_mov_b32 s54, 43008\n\t" \
  "s_waitcnt vmcnt(11)\n\t" \
  "s_branch 2f\n\t" \
  "1:\n\t" \
  "s_mul_i32 s83, s59, 4160\n\t" \
  "v_add_u32 v20, 7152, v11\n\t" \
  "v_add_u32 v21, 14304, v11\n\t" \
  "v_add_u32 v22, 21456, v11\n\t" \
  "s_add_u32 m0, s83, 130064\n\t" \
  "s_nop 0\n\t" \
  "global_load_lds_dwordx4 v11, s[60:61]" LDPOL "\n\t" \
  "global_load_lds_dwordx4 v20, s[60:61] offset:1040" LDPOL "\n\t" \
  "global_load_lds_dwordx4 v21, s[60:61] offset:2080" LDPOL "\n\t" \
  "global_load_lds_dwordx4 v22, s[60:61] offset:3120" LDPOL "\n\t" \
  "s_add_u32 m0, s83, 146704\n\t" \
  "s_nop 0\n\t" \
  "global_load_lds_dwordx4 v11, s[62:63]" LDPOL "\n\t" \
  "global_load_lds_dwordx4 v20, s[62:63] offset:1040" LDPOL "\n\t" \
  "global_load_lds_dwordx4 v21, s[62:63] offset:2080" LDPOL "\n\t" \
  "global_load_lds_dwordx4 v22, s[62:63] offset:3120" LDPOL "\n\t" \
  "v_subrev_u32 v20, s64, v11\n\t" \
  "s_mov_b32 m0, s68\n\t" \
  "s_mov_b64 exec, s[66:67]\n\t" \
  "global_load_lds_dwordx4 v20, s[60:61]" LDPOL "\n\t" \
  "s_mov_b64 exec, -1\n\t" \
  "s_waitcnt vmcnt(9)\n\t" \
  "2:\n\t" \
  "s_waitcnt lgkmcnt(0)\n\t" \
  "s_barrier\n\t"
#define H_ASM \
  "s_mul_i32 s76, s52, 0x60000\n\t" \
  "s_add_u32 s76, s42, s76\n\t" \
  "s_addc_u32 s77, s43, 0\n\t" \
  "s_lshl_b32 s78, s52, 7\n\t" \
  "s_add_u32 s78, s40, s78\n\t" \
  "s_addc_u32 s79, s41, 0\n\t" \
  "s_mul_i32 s80, s47, s46\n\t" \
  "s_add_u32 s80, s44, s80\n\t" \
  "s_addc_u32 s81, s45, 0\n\t" \
  "s_add_u32 s82, s54, s53\n\t" \
  "s_cmp_eq_u32 s58, 2\n\t" \
  "s_cbranch_scc1 5f\n\t" \
  "s_branch 3f\n\t" \
  "1:\n\t" \
  "s_waitcnt vmcnt(11)\n\t" \
  "2:\n\t" \
  "s_barrier\n\t" \
  "3:\n\t" \
  "s_add_u32 s55, s55, 1\n\t" \
  "s_add_u32 m0, s82, 18432\n\t" \
  "s_add_u32 s52, s52, 1\n\t" \
  "global_load_lds_dwordx4 v0, s[76:77]\n\t" \
  "global_load_lds_dwordx4 v1, s[76:77] offset:1024\n\t" \
  "s_add_u32 m0, s82, 2048\n\t" \
  "s_and_b32 s52, s52, 15\n\t" \
  "global_load_lds_dwordx4 v6, s[78:79]\n\t" \
  "global_load_lds_dwordx4 v7, s[78:79] offset:1024\n\t" \
  "s_add_u32 m0, s82, 26624\n\t" \
  "s_add_u32 s47, s47, 1\n\t" \
  "global_load_lds_dwordx4 v2, s[76:77]\n\t" \
  "global_load_lds_dwordx4 v3, s[76:77] offset:1024\n\t" \
  "s_add_u32 m0, s82, 10240\n\t" \
  "s_and_b32 s47, s47, 15\n\t" \
  "global_load_lds_dwordx4 v8, s[78:79]\n\t" \
  "global_load_lds_dwordx4 v9, s[78:79] offset:1024\n\t" \
  "s_add_u32 m0, s82, 34816\n\t" \
  "s_and_b32 s83, s54, s51\n\t" \
  "global_load_lds_dwordx4 v4, s[76:77]\n\t" \
  "global_load_lds_dwordx4 v5, s[76:77] offset:1024\n\t" \
  "s_bitcmp1_b32 s57, s55\n\t" \
  "s_cbranch_scc1 10f\n\t" \
  "8:\n\t" \
  "s_add_u32 m0, s83, s49\n\t" \
  "s_mov_b64 exec, s[50:51]\n\t" \
  "global_load_lds_dwordx4 v10, s[80:81]\n\t" \
  "s_mov_b64 exec, -1\n\t" \
  "4:\n\t" \
  "s_mul_i32 s76, s52, 0x60000\n\t" \
  "s_add_u32 s76, s42, s76\n\t" \
  "s_addc_u32 s77, s43, 0\n\t" \
  "s_lshl_b32 s78, s52, 7\n\t" \
  "s_add_u32 s78, s40, s78\n\t" \
  "s_addc_u32 s79, s41, 0\n\t" \
  "s_mul_i32 s80, s47, s46\n\t" \
  "s_add_u32 s80, s44, s80\n\t" \
  "s_addc_u32 s81, s45, 0\n\t" \
  "s_add_u32 s54, s54, 43008\n\t" \
  "s_cmp_eq_u32 s54, 129024\n\t" \
  "s_cselect_b32 s54, 0, s54\n\t" \
  "s_add_u32 s82, s54, s53\n\t" \
  "s_bitcmp0_b32 s56, s55\n\t" \
  "s_cbranch_scc1 1b\n\t" \
  "s_cmp_eq_u32 s55, 15\n\t" \
  "s_cbranch_scc1 19f\n\t" \
  "s_cmp_eq_u32 s55, s58\n\t" \
  "s_cbranch_scc1 3b\n\t" \
  "s_waitcnt vmcnt(11)\n\t" \
  "s_barrier\n\t" \
  "5:\n\t" \
  "s_mul_i32 s83, s59, 4160\n\t" \
  "v_add_u32 v20, 7152, v11\n\t" \
  "v_add_u32 v21, 14304, v11\n\t" \
  "v_add_u32 v22, 21456, v11\n\t" \
  "s_add_u32 m0, s83, 130064\n\t" \
  "s_nop 0\n\t" \
  "global_load_lds_dwordx4 v11, s[60:61]" LDPOL "\n\t" \
  "global_load_lds_dwordx4 v20, s[60:61] offset:1040" LDPOL "\n\t" \
  "global_load_lds_dwordx4 v21, s[60:61] offset:2080" LDPOL "\n\t" \
  "global_load_lds_dwordx4 v22, s[60:61] offset:3120" LDPOL "\n\t" \
  "s_add_u32 m0, s83, 146704\n\t" \
  "s_nop 0\n\t" \
  "global_load_lds_dwordx4 v11, s[62:63]" LDPOL "\n\t" \
  "global_load_lds_dwordx4 v20, s[62:63] offset:1040" LDPOL "\n\t" \
  "global_load_lds_dwordx4 v21, s[62:63] offset:2080" LDPOL "\n\t" \
  "global_load_lds_dwordx4 v22, s[62:63] offset:3120" LDPOL "\n\t" \
  "v_subrev_u32 v20, s64, v11\n\t" \
  "s_mov_b32 m0, s68\n\t" \
  "s_mov_b64 exec, s[66:67]\n\t" \
  "global_load_lds_dwordx4 v20, s[60:61]" LDPOL "\n\t" \
  "s_mov_b64 exec, -1\n\t" \
  "s_waitcnt vmcnt(9)\n\t" \
  "s_branch 2b\n\t" \
  "10:\n\t" \
  "s_cmp_eq_u32 s59, 2\n\t" \
  "s_cbranch_scc0 11f\n\t" \
  "s_mov_b64 exec, 0xffff\n\t" \
  "global_load_dword v255, v14, s[72:73]\n\t" \
  "s_mov_b64 exec, -1\n\t" \
  "s_branch 4b\n\t" \
  "11:\n\t" \
  "s_cmp_eq_u32 s55, 2\n\t" \
  "s_cbranch_scc0 12f\n\t" \
  "global_load_dword v254, v14, s[70:71] sc1\n\t" \
  "s_branch 4b\n\t" \
  "12:\n\t" \
  "s_cmp_eq_u32 s55, 4\n\t" \
  "s_cbranch_scc0 15f\n\t" \
  "global_load_dword v253, v13, s[70:71] sc1\n\t" \
  "s_branch 4b\n\t" \
  "15:\n\t" \
  "s_cmp_eq_u32 s55, 5\n\t" \
  "s_cbranch_scc0 13f\n\t" \
  "v_readfirstlane_b32 s84, v254\n\t" \
  "s_mov_b32 m0, 0x400000\n\t" \
  "s_waitcnt vmcnt(10)\n\t" \
  "s_add_u32 s84, s84, 1\n\t" \
  "v_mov_b32 v254, s84\n\t" \
  "16:\n\t" \
  "v_cmp_eq_u32 vcc, v253, v254\n\t" \
  "s_nop 1\n\t" \
  "s_and_b32 s84, vcc_lo, 0x3fffffff\n\t" \
  "s_bcnt1_i32_b32 s85, s84\n\t" \
  "s_cmp_ge_u32 s85, 15\n\t" \
  "s_cbranch_scc1 17f\n\t" \
  "s_sub_u32 m0, m0, 1\n\t" \
  "s_cbranch_scc1 17f\n\t" \
  "global_load_dword v253, v13, s[70:71] sc1\n\t" \
  "s_waitcnt vmcnt(0)\n\t" \
  "s_branch 16b\n\t" \
  "17:\n\t" \
  "s_and_b32 s84, s84, 0xaaaaaaaa\n\t" \
  "s_bcnt1_i32_b32 s84, s84\n\t" \
  "s_and_b32 s84, s84, 8\n\t" \
  "s_lshl_b32 s84, s84, 10\n\t" \
  "v_xor_b32 v16, s84, v16\n\t" \
  "v_mov_b32 v20, s84\n\t" \
  "v_mov_b32 v253, 163344\n\t" \
  "ds_write_b32 v253, v254 offset:104\n\t" \
  "ds_write_b32 v253, v20 offset:24\n\t" \
  "s_waitcnt lgkmcnt(0)\n\t" \
  "s_branch 8b\n\t" \
  "13:\n\t" \
  "s_cmp_eq_u32 s55, 14\n\t" \
  "s_cbranch_scc0 14f\n\t" \
  "global_load_dword v253, v16, s[70:71] sc1\n\t" \
  "s_branch 4b\n\t" \
  "14:\n\t" \
  "s_mov_b32 exec_hi, 0\n\t" \
  "global_load_dword v255, v10, s[44:45]\n\t" \
  "s_mov_b64 exec, -1\n\t" \
  "s_branch 4b\n\t" \
  "19:\n\t" \
  "s_waitcnt vmcnt(11)\n\t" \
  "s_cmp_lg_u32 s59, 3\n\t" \
  "s_cbranch_scc1 18f\n\t" \
  "v_cmp_ne_u32 vcc, v253, v254\n\t" \
  "v_mov_b32 v253, 163364\n\t" \
  "s_nop 1\n\t" \
  "s_cmp_eq_u64 vcc, 0\n\t" \
  "s_cselect_b32 s83, 1, 0\n\t" \
  "v_mov_b32 v254, s83\n\t" \
  "ds_write_b32 v253, v254\n\t" \
  "18:\n\t"
#define C_ASM \
  "s_branch 29f\n\t" \
  "21:\n\t" \
  "s_mov_b64 s[68:69], %27\n\t" \
  "s_and_b32 s74, s68, 15\n\t" \
  "s_andn2_b32 s68, s68, 15\n\t" \
  "s_load_dword vcc_lo, s[68:69], 0 glc\n\t" \
  "s_and_b32 s75, s74, 1\n\t" \
  "s_lshl_b32 s70, s74, 9\n\t" \
  "s_lshl_b32 s71, s75, 13\n\t" \
  "s_or_b32 s70, s70, s71\n\t" \
  "s_waitcnt lgkmcnt(0)\n\t" \
  "s_or_b32 s68, s68, s70\n\t" \
  "s_add_u32 vcc_lo, vcc_lo, 1\n\t" \
  "v_mov_b32 v18, vcc_lo\n\t" \
  "s_memtime s[70:71]\n\t" \
  "s_waitcnt lgkmcnt(0)\n\t" \
  "s_load_dword s76, s[68:69], 16384 glc\n\t" \
  "s_waitcnt lgkmcnt(0)\n\t" \
  "s_xor_b32 s68, s68, 0x2000\n\t" \
  "s_memtime s[90:91]\n\t" \
  "s_waitcnt lgkmcnt(0)\n\t" \
  "s_load_dword s76, s[68:69], 16384 glc\n\t" \
  "s_waitcnt lgkmcnt(0)\n\t" \
  "s_sub_u32 vcc_lo, s90, s70\n\t" \
  "s_memtime s[70:71]\n\t" \
  "s_waitcnt lgkmcnt(0)\n\t" \
  "s_sub_u32 s70, s70, s90\n\t" \
  "s_cmp_gt_u32 vcc_lo, s70\n\t" \
  "s_cselect_b32 s70, 1, 0\n\t" \
  "s_xor_b32 s70, s70, s75\n\t" \
  "s_lshl1_add_u32 s70, s74, s70\n\t" \
  "s_lshl_b32 s70, s70, 2\n\t" \
  "v_mov_b32 v19, s70\n\t" \
  "s_and_b32 s68, s68, 0xffffc000\n\t" \
  "s_mov_b64 exec, 1\n\t" \
  "global_store_dword v19, v18, s[68:69] offset:256 sc1\n\t" \
  "s_branch 26f\n\t" \
  "28:\n\t" \
  "s_cmp_lg_u64 %27, 0\n\t" \
  "s_cbranch_scc1 21b\n\t" \
  "s_mov_b64 exec, 0\n\t" \
  "s_cmp_eq_u32 %13, 0\n\t" \
  "s_cbranch_scc1 500f\n\t" \
  "s_cmp_eq_u32 %13, 1\n\t" \
  "s_cbranch_scc1 501f\n\t" \
  "s_cmp_eq_u32 %13, 2\n\t" \
  "s_cbranch_scc1 502f\n\t" \
  "s_branch 503f\n\t" \
  "26:\n\t" \
  "s_mov_b64 exec, -1\n\t" \
  "s_add_u32 s88, s88, 4\n\t" \
  "s_addc_u32 s89, s89, 0\n\t" \
  "s_setpc_b64 s[88:89]\n\t" \
  ".p2align 12\n\t" \
  "29:\n\t" \
  "s_bitcmp0_b32 %13, 0\n\t" \
  "s_cbranch_scc1 8f\n\t" \
  "global_load_dwordx2 v[10:11], v51, %11\n\t" \
  "global_load_dwordx2 v[12:13], v51, %11 offset:128\n\t" \
  "v_xor_b32 v53, 64, v52\n\t" \
  "v_xor_b32 v55, 32, v54\n\t" \
  "v_add_u32 v56, 43008, v52\n\t" \
  "v_add_u32 v58, 43008, v54\n\t" \
  "v_xor_b32 v57, 64, v56\n\t" \
  "v_xor_b32 v59, 32, v58\n\t" \
  "v_add_u32 v60, 86016, v52\n\t" \
  "v_add_u32 v62, 86016, v54\n\t" \
  "v_xor_b32 v61, 64, v60\n\t" \
  "v_xor_b32 v63, 32, v62\n\t" \
  "s_mov_b32 vcc_hi, 0x05040100\n\t" \
  "s_waitcnt vmcnt(2)\n\t" \
  "s_barrier\n\t" \
  "ds_read_b64_tr_b16 v[64:65], v62 offset:26624\n\t" \
  "ds_read_b64_tr_b16 v[66:67], v62 offset:27136\n\t" \
  "ds_read_b128 v[68:71], v60 offset:10240\n\t" \
  "ds_read_b64_tr_b16 v[72:73], v62 offset:30720\n\t" \
  "ds_read_b64_tr_b16 v[74:75], v62 offset:31232\n\t" \
  "ds_read_b128 v[76:79], v61 offset:10240\n\t" \
  "ds_read_b128 v[80:83], v61 offset:12288\n\t" \
  "ds_read_b128 v[84:87], v60 offset:12288\n\t" \
  "ds_read_b128 v[88:91], v60 offset:14336\n\t" \
  "ds_read_b128 v[92:95], v61 offset:14336\n\t" \
  "ds_read_b128 v[96:99], v61 offset:16384\n\t" \
  "ds_read_b128 v[100:103], v60 offset:16384\n\t" \
  "ds_read_b64_tr_b16 v[104:105], v63 offset:26624\n\t" \
  "ds_read_b64_tr_b16 v[106:107], v63 offset:27136\n\t" \
  "ds_read_b64_tr_b16 v[108:109], v63 offset:30720\n\t" \
  "ds_read_b64_tr_b16 v[110:111], v63 offset:31232\n\t" \
  "s_waitcnt lgkmcnt(13)\n\t" \
  "v_mfma_f32_16x16x32_bf16 v[184:187], v[64:67], v[68:71], 0\n\t" \
  "ds_read_b64_tr_b16 v[112:113], v62 offset:34816\n\t" \
  "ds_read_b64_tr_b16 v[114:115], v62 offset:35328\n\t" \
  "s_waitcnt lgkmcnt(12)\n\t" \
  "v_mfma_f32_16x16x32_bf16 v[184:187], v[72:75], v[76:79], v[184:187]\n\t" \
  "ds_read_b64_tr_b16 v[116:117], v62 offset:38912\n\t" \
  "ds_read_b64_tr_b16 v[118:119], v62 offset:39424\n\t" \
  "s_waitcnt lgkmcnt(13)\n\t" \
  "v_mfma_f32_16x16x32_bf16 v[192:195], v[72:75], v[80:83], 0\n\t" \
  "ds_read_b128 v[120:123], v60 offset:0\n\t" \
  "s_waitcnt lgkmcnt(13)\n\t" \
  "v_mfma_f32_16x16x32_bf16 v[192:195], v[64:67], v[84:87], v[192:195]\n\t" \
  "ds_read_b128 v[124:127], v61 offset:0\n\t" \
  "s_waitcnt lgkmcnt(13)\n\t" \
  "v_mfma_f32_16x16x32_bf16 v[200:203], v[64:67], v[88:91], 0\n\t" \
  "ds_read_b64_tr_b16 v[128:129], v63 offset:34816\n\t" \
  "s_waitcnt lgkmcnt(13)\n\t" \
  "v_mfma_f32_16x16x32_bf16 v[200:203], v[72:75], v[92:95], v[200:203]\n\t" \
  "ds_read_b64_tr_b16 v[130:131], v63 offset:35328\n\t" \
  "s_waitcnt lgkmcnt(13)\n\t" \
  "v_mfma_f32_16x16x32_bf16 v[208:211], v[72:75], v[96:99], 0\n\t" \
  "ds_read_b64_tr_b16 v[132:133], v63 offset:38912\n\t" \
  "s_waitcnt lgkmcnt(9)\n\t" \
  "v_mfma_f32_16x16x32_bf16 v[208:211], v[64:67], v[100:103], v[208:211]\n\t" \
  "ds_read_b64_tr_b16 v[134:135], v63 offset:39424\n\t" \
  "v_mfma_f32_16x16x32_bf16 v[188:191], v[104:107], v[68:71], 0\n\t" \
  "ds_read_b64_tr_b16 v[136:137], v62 offset:18432\n\t" \
  "v_mfma_f32_16x16x32_bf16 v[188:191], v[108:111], v[76:79], v[188:191]\n\t" \
  "ds_read_b64_tr_b16 v[138:139], v62 offset:18944\n\t" \
  "v_mfma_f32_16x16x32_bf16 v[196:199], v[108:111], v[80:83], 0\n\t" \
  "ds_read_b64_tr_b16 v[140:141], v62 offset:22528\n\t" \
  "v_mfma_f32_16x16x32_bf16 v[196:199], v[104:107], v[84:87], v[196:199]\n\t" \
  "ds_read_b64_tr_b16 v[142:143], v62 offset:23040\n\t" \
  "v_mfma_f32_16x16x32_bf16 v[204:207], v[104:107], v[88:91], 0\n\t" \
  "ds_read_b64_tr_b16 v[144:145], v63 offset:18432\n\t" \
  "v_mfma_f32_16x16x32_bf16 v[204:207], v[108:111], v[92:95], v[204:207]\n\t" \
  "ds_read_b64_tr_b16 v[146:147], v63 offset:18944\n\t" \
  "v_mfma_f32_16x16x32_bf16 v[212:215], v[108:111], v[96:99], 0\n\t" \
  "ds_read_b64_tr_b16 v[148:149], v63 offset:22528\n\t" \
  "v_mfma_f32_16x16x32_bf16 v[212:215], v[104:107], v[100:103], v[212:215]\n\t" \
  "ds_read_b64_tr_b16 v[150:151], v63 offset:23040\n\t" \
  "s_waitcnt lgkmcnt(7)\n\t" \
  "v_mfma_f32_16x16x32_bf16 v[216:219], v[68:71], v[112:115], 0\n\t" \
  "v_mfma_f32_16x16x32_bf16 v[216:219], v[76:79], v[116:119], v[216:219]\n\t" \
  "v_mfma_f32_16x16x32_bf16 v[224:227], v[80:83], v[116:119], 0\n\t" \
  "v_mfma_f32_16x16x32_bf16 v[224:227], v[84:87], v[112:115], v[224:227]\n\t" \
  "v_mfma_f32_16x16x32_bf16 v[232:235], v[88:91], v[112:115], 0\n\t" \
  "v_mfma_f32_16x16x32_bf16 v[232:235], v[92:95], v[116:119], v[232:235]\n\t" \
  "v_mfma_f32_16x16x32_bf16 v[240:243], v[96:99], v[116:119], 0\n\t" \
  "v_mfma_f32_16x16x32_bf16 v[240:243], v[100:103], v[112:115], v[240:243]\n\t" \
  "v_mfma_f32_16x16x32_bf16 v[248:251], v[120:123], v[112:115], 0\n\t" \
  "v_mfma_f32_16x16x32_bf16 v[248:251], v[124:127], v[116:119], v[248:251]\n\t" \
  "v_mfma_f32_16x16x32_bf16 v[252:255], v[120:123], v[128:131], 0\n\t" \
  "v_mfma_f32_16x16x32_bf16 v[252:255], v[124:127], v[132:135], v[252:255]\n\t" \
  "v_mfma_f32_16x16x32_bf16 v[220:223], v[76:79], v[132:135], 0\n\t" \
  "v_mfma_f32_16x16x32_bf16 v[220:223], v[68:71], v[128:131], v[220:223]\n\t" \
  "s_waitcnt lgkmcnt(0)\n\t" \
  "s_getpc_b64 s[88:89]\n\t" \
  "s_branch 28b\n\t" \
  "s_barrier\n\t" \
  "v_mfma_f32_16x16x32_bf16 v[228:231], v[84:87], v[128:131], 0\n\t" \
  "ds_read_b64_tr_b16 v[72:73], v54 offset:26624\n\t" \
  "v_mfma_f32_16x16x32_bf16 v[228:231], v[80:83], v[132:135], v[228:231]\n\t" \
  "ds_read_b64_tr_b16 v[74:75], v54 offset:27136\n\t" \
  "v_mfma_f32_16x16x32_bf16 v[236:239], v[92:95], v[132:135], 0\n\t" \
  "ds_read_b128 v[64:67], v52 offset:10240\n\t" \
  "v_mfma_f32_16x16x32_bf16 v[236:239], v[88:91], v[128:131], v[236:239]\n\t" \
  "ds_read_b64_tr_b16 v[108:109], v54 offset:30720\n\t" \
  "v_mfma_f32_16x16x32_bf16 v[244:247], v[100:103], v[128:131], 0\n\t" \
  "ds_read_b64_tr_b16 v[110:111], v54 offset:31232\n\t" \
  "v_mfma_f32_16x16x32_bf16 v[244:247], v[96:99], v[132:135], v[244:247]\n\t" \
  "ds_read_b128 v[104:107], v53 offset:10240\n\t" \
  "v_mfma_f32_16x16x32_bf16 v[152:155], v[136:139], v[68:71], 0\n\t" \
  "ds_read_b128 v[112:115], v53 offset:12288\n\t" \
  "v_mfma_f32_16x16x32_bf16 v[152:155], v[140:143], v[76:79], v[152:155]\n\t" \
  "ds_read_b128 v[116:119], v52 offset:12288\n\t" \
  "v_mfma_f32_16x16x32_bf16 v[160:163], v[140:143], v[80:83], 0\n\t" \
  "ds_read_b128 v[120:123], v52 offset:14336\n\t" \
  "v_mfma_f32_16x16x32_bf16 v[160:163], v[136:139], v[84:87], v[160:163]\n\t" \
  "ds_read_b128 v[124:127], v53 offset:14336\n\t" \
  "v_mfma_f32_16x16x32_bf16 v[168:171], v[136:139], v[88:91], 0\n\t" \
  "ds_read_b128 v[128:131], v53 offset:16384\n\t" \
  "v_mfma_f32_16x16x32_bf16 v[168:171], v[140:143], v[92:95], v[168:171]\n\t" \
  "ds_read_b128 v[132:135], v52 offset:16384\n\t" \
  "v_mfma_f32_16x16x32_bf16 v[176:179], v[140:143], v[96:99], 0\n\t" \
  "ds_read_b64_tr_b16 v[140:141], v55 offset:26624\n\t" \
  "v_mfma_f32_16x16x32_bf16 v[176:179], v[136:139], v[100:103], v[176:179]\n\t" \
  "ds_read_b64_tr_b16 v[142:143], v55 offset:27136\n\t" \
  "v_mfma_f32_16x16x32_bf16 v[156:159], v[144:147], v[68:71], 0\n\t" \
  "ds_read_b64_tr_b16 v[136:137], v55 offset:30720\n\t" \
  "v_mfma_f32_16x16x32_bf16 v[156:159], v[148:151], v[76:79], v[156:159]\n\t" \
  "ds_read_b64_tr_b16 v[138:139], v55 offset:31232\n\t" \
  "v_mfma_f32_16x16x32_bf16 v[164:167], v[148:151], v[80:83], 0\n\t" \
  "ds_read_b64_tr_b16 v[68:69], v54 offset:34816\n\t" \
  "v_mfma_f32_16x16x32_bf16 v[164:167], v[144:147], v[84:87], v[164:167]\n\t" \
  "ds_read_b64_tr_b16 v[70:71], v54 offset:35328\n\t" \
  "v_mfma_f32_16x16x32_bf16 v[172:175], v[144:147], v[88:91], 0\n\t" \
  "ds_read_b64_tr_b16 v[76:77], v54 offset:38912\n\t" \
  "v_mfma_f32_16x16x32_bf16 v[172:175], v[148:151], v[92:95], v[172:175]\n\t" \
  "ds_read_b64_tr_b16 v[78:79], v54 offset:39424\n\t" \
  "v_mfma_f32_16x16x32_bf16 v[180:183], v[148:151], v[96:99], 0\n\t" \
  "ds_read_b128 v[80:83], v52 offset:0\n\t" \
  "v_mfma_f32_16x16x32_bf16 v[180:183], v[144:147], v[100:103], v[180:183]\n\t" \
  "ds_read_b128 v[84:87], v53 offset:0\n\t" \
  "s_waitcnt lgkmcnt(7)\n\t" \
  "v_mfma_f32_16x16x32_bf16 v[184:187], v[72:75], v[64:67], v[184:187]\n\t" \
  "ds_read_b64_tr_b16 v[88:89], v55 offset:34816\n\t" \
  "v_mfma_f32_16x16x32_bf16 v[184:187], v[108:111], v[104:107], v[184:187]\n\t" \
  "ds_read_b64_tr_b16 v[90:91], v55 offset:35328\n\t" \
  "v_mfma_f32_16x16x32_bf16 v[192:195], v[108:111], v[112:115], v[192:195]\n\t" \
  "ds_read_b64_tr_b16 v[92:93], v55 offset:38912\n\t" \
  "v_mfma_f32_16x16x32_bf16 v[192:195], v[72:75], v[116:119], v[192:195]\n\t" \
  "ds_read_b64_tr_b16 v[94:95], v55 offset:39424\n\t" \
  "v_mfma_f32_16x16x32_bf16 v[200:203], v[72:75], v[120:123], v[200:203]\n\t" \
  "ds_read_b64_tr_b16 v[96:97], v54 offset:18432\n\t" \
  "v_mfma_f32_16x16x32_bf16 v[200:203], v[108:111], v[124:127], v[200:203]\n\t" \
  "ds_read_b64_tr_b16 v[98:99], v54 offset:18944\n\t" \
  "v_mfma_f32_16x16x32_bf16 v[208:211], v[108:111], v[128:131], v[208:211]\n\t" \
  "ds_read_b64_tr_b16 v[148:149], v54 offset:22528\n\t" \
  "v_mfma_f32_16x16x32_bf16 v[208:211], v[72:75], v[132:135], v[208:211]\n\t" \
  "ds_read_b64_tr_b16 v[150:151], v54 offset:23040\n\t" \
  "v_mfma_f32_16x16x32_bf16 v[188:191], v[140:143], v[64:67], v[188:191]\n\t" \
  "ds_read_b64_tr_b16 v[100:101], v55 offset:18432\n\t" \
  "s_waitcnt lgkmcnt(7)\n\t" \
  "v_mfma_f32_16x16x32_bf16 v[188:191], v[136:139], v[104:107], v[188:191]\n\t" \
  "ds_read_b64_tr_b16 v[102:103], v55 offset:18944\n\t" \
  "v_mfma_f32_16x16x32_bf16 v[196:199], v[136:139], v[112:115], v[196:199]\n\t" \
  "ds_read_b64_tr_b16 v[144:145], v55 offset:22528\n\t" \
  "v_mfma_f32_16x16x32_bf16 v[196:199], v[140:143], v[116:119], v[196:199]\n\t" \
  "ds_read_b64_tr_b16 v[146:147], v55 offset:23040\n\t" \
  "v_mfma_f32_16x16x32_bf16 v[204:207], v[140:143], v[120:123], v[204:207]\n\t" \
  "v_mfma_f32_16x16x32_bf16 v[204:207], v[136:139], v[124:127], v[204:207]\n\t" \
  "v_mfma_f32_16x16x32_bf16 v[212:215], v[136:139], v[128:131], v[212:215]\n\t" \
  "v_mfma_f32_16x16x32_bf16 v[212:215], v[140:143], v[132:135], v[212:215]\n\t" \
  "v_mfma_f32_16x16x32_bf16 v[216:219], v[64:67], v[68:71], v[216:219]\n\t" \
  "v_mfma_f32_16x16x32_bf16 v[216:219], v[104:107], v[76:79], v[216:219]\n\t" \
  "v_mfma_f32_16x16x32_bf16 v[224:227], v[112:115], v[76:79], v[224:227]\n\t" \
  "v_mfma_f32_16x16x32_bf16 v[224:227], v[116:119], v[68:71], v[224:227]\n\t" \
  "v_mfma_f32_16x16x32_bf16 v[232:235], v[120:123], v[68:71], v[232:235]\n\t" \
  "v_mfma_f32_16x16x32_bf16 v[232:235], v[124:127], v[76:79], v[232:235]\n\t" \
  "v_mfma_f32_16x16x32_bf16 v[240:243], v[128:131], v[76:79], v[240:243]\n\t" \
  "v_mfma_f32_16x16x32_bf16 v[240:243], v[132:135], v[68:71], v[240:243]\n\t" \
  "v_mfma_f32_16x16x32_bf16 v[248:251], v[80:83], v[68:71], v[248:251]\n\t" \
  "v_mfma_f32_16x16x32_bf16 v[248:251], v[84:87], v[76:79], v[248:251]\n\t" \
  "v_mfma_f32_16x16x32_bf16 v[252:255], v[80:83], v[88:91], v[252:255]\n\t" \
  "s_waitcnt lgkmcnt(0)\n\t" \
  "v_mfma_f32_16x16x32_bf16 v[252:255], v[84:87], v[92:95], v[252:255]\n\t" \
  "v_mfma_f32_16x16x32_bf16 v[220:223], v[104:107], v[92:95], v[220:223]\n\t" \
  "v_mfma_f32_16x16x32_bf16 v[220:223], v[64:67], v[88:91], v[220:223]\n\t" \
  "s_barrier\n\t" \
  "v_mfma_f32_16x16x32_bf16 v[228:231], v[116:119], v[88:91], v[228:231]\n\t" \
  "ds_read_b64_tr_b16 v[108:109], v58 offset:26624\n\t" \
  "v_mfma_f32_16x16x32_bf16 v[228:231], v[112:115], v[92:95], v[228:231]\n\t" \
  "ds_read_b64_tr_b16 v[110:111], v58 offset:27136\n\t" \
  "v_mfma_f32_16x16x32_bf16 v[236:239], v[124:127], v[92:95], v[236:239]\n\t" \
  "ds_read_b128 v[72:75], v56 offset:10240\n\t" \
  "v_mfma_f32_16x16x32_bf16 v[236:239], v[120:123], v[88:91], v[236:239]\n\t" \
  "ds_read_b64_tr_b16 v[136:137], v58 offset:30720\n\t" \
  "v_mfma_f32_16x16x32_bf16 v[244:247], v[132:135], v[88:91], v[244:247]\n\t" \
  "ds_read_b64_tr_b16 v[138:139], v58 offset:31232\n\t" \
  "v_mfma_f32_16x16x32_bf16 v[244:247], v[128:131], v[92:95], v[244:247]\n\t" \
  "ds_read_b128 v[140:143], v57 offset:10240\n\t" \
  "v_mfma_f32_16x16x32_bf16 v[152:155], v[96:99], v[64:67], v[152:155]\n\t" \
  "ds_read_b128 v[68:71], v57 offset:12288\n\t" \
  "v_mfma_f32_16x16x32_bf16 v[152:155], v[148:151], v[104:107], v[152:155]\n\t" \
  "ds_read_b128 v[76:79], v56 offset:12288\n\t" \
  "v_mfma_f32_16x16x32_bf16 v[160:163], v[148:151], v[112:115], v[160:163]\n\t" \
  "ds_read_b128 v[80:83], v56 offset:14336\n\t" \
  "v_mfma_f32_16x16x32_bf16 v[160:163], v[96:99], v[116:119], v[160:163]\n\t" \
  "ds_read_b128 v[84:87], v57 offset:14336\n\t" \
  "v_mfma_f32_16x16x32_bf16 v[168:171], v[96:99], v[120:123], v[168:171]\n\t" \
  "ds_read_b128 v[88:91], v57 offset:16384\n\t" \
  "v_mfma_f32_16x16x32_bf16 v[168:171], v[148:151], v[124:127], v[168:171]\n\t" \
  "ds_read_b128 v[92:95], v56 offset:16384\n\t" \
  "v_mfma_f32_16x16x32_bf16 v[176:179], v[148:151], v[128:131], v[176:179]\n\t" \
  "ds_read_b64_tr_b16 v[148:149], v59 offset:26624\n\t" \
  "v_mfma_f32_16x16x32_bf16 v[176:179], v[96:99], v[132:135], v[176:179]\n\t" \
  "ds_read_b64_tr_b16 v[150:151], v59 offset:27136\n\t" \
  "v_mfma_f32_16x16x32_bf16 v[156:159], v[100:103], v[64:67], v[156:159]\n\t" \
  "ds_read_b64_tr_b16 v[96:97], v59 offset:30720\n\t" \
  "v_mfma_f32_16x16x32_bf16 v[156:159], v[144:147], v[104:107], v[156:159]\n\t" \
  "ds_read_b64_tr_b16 v[98:99], v59 offset:31232\n\t" \
  "v_mfma_f32_16x16x32_bf16 v[164:167], v[144:147], v[112:115], v[164:167]\n\t" \
  "ds_read_b64_tr_b16 v[64:65], v58 offset:34816\n\t" \
  "v_mfma_f32_16x16x32_bf16 v[164:167], v[100:103], v[116:119], v[164:167]\n\t" \
  "ds_read_b64_tr_b16 v[66:67], v58 offset:35328\n\t" \
  "v_mfma_f32_16x16x32_bf16 v[172:175], v[100:103], v[120:123], v[172:175]\n\t" \
  "ds_read_b64_tr_b16 v[104:105], v58 offset:38912\n\t" \
  "v_mfma_f32_16x16x32_bf16 v[172:175], v[144:147], v[124:127], v[172:175]\n\t" \
  "ds_read_b64_tr_b16 v[106:107], v58 offset:39424\n\t" \
  "v_mfma_f32_16x16x32_bf16 v[180:183], v[144:147], v[128:131], v[180:183]\n\t" \
  "ds_read_b128 v[112:115], v56 offset:0\n\t" \
  "v_mfma_f32_16x16x32_bf16 v[180:183], v[100:103], v[132:135], v[180:183]\n\t" \
  "ds_read_b128 v[116:119], v57 offset:0\n\t" \
  "s_waitcnt lgkmcnt(7)\n\t" \
  "v_mfma_f32_16x16x32_bf16 v[184:187], v[108:111], v[72:75], v[184:187]\n\t" \
  "ds_read_b64_tr_b16 v[120:121], v59 offset:34816\n\t" \
  "v_mfma_f32_16x16x32_bf16 v[184:187], v[136:139], v[140:143], v[184:187]\n\t" \
  "ds_read_b64_tr_b16 v[122:123], v59 offset:35328\n\t" \
  "v_mfma_f32_16x16x32_bf16 v[192:195], v[136:139], v[68:71], v[192:195]\n\t" \
  "ds_read_b64_tr_b16 v[124:125], v59 offset:38912\n\t" \
  "v_mfma_f32_16x16x32_bf16 v[192:195], v[108:111], v[76:79], v[192:195]\n\t" \
  "ds_read_b64_tr_b16 v[126:127], v59 offset:39424\n\t" \
  "v_mfma_f32_16x16x32_bf16 v[200:203], v[108:111], v[80:83], v[200:203]\n\t" \
  "ds_read_b64_tr_b16 v[128:129], v58 offset:18432\n\t" \
  "v_mfma_f32_16x16x32_bf16 v[200:203], v[136:139], v[84:87], v[200:203]\n\t" \
  "ds_read_b64_tr_b16 v[130:131], v58 offset:18944\n\t" \
  "v_mfma_f32_16x16x32_bf16 v[208:211], v[136:139], v[88:91], v[208:211]\n\t" \
  "ds_read_b64_tr_b16 v[144:145], v58 offset:22528\n\t" \
  "v_mfma_f32_16x16x32_bf16 v[208:211], v[108:111], v[92:95], v[208:211]\n\t" \
  "ds_read_b64_tr_b16 v[146:147], v58 offset:23040\n\t" \
  "v_mfma_f32_16x16x32_bf16 v[188:191], v[148:151], v[72:75], v[188:191]\n\t" \
  "ds_read_b64_tr_b16 v[100:101], v59 offset:18432\n\t" \
  "s_waitcnt lgkmcnt(7)\n\t" \
  "v_mfma_f32_16x16x32_bf16 v[188:191], v[96:99], v[140:143], v[188:191]\n\t" \
  "ds_read_b64_tr_b16 v[102:103], v59 offset:18944\n\t" \
  "v_mfma_f32_16x16x32_bf16 v[196:199], v[96:99], v[68:71], v[196:199]\n\t" \
  "ds_read_b64_tr_b16 v[132:133], v59 offset:22528\n\t" \
  "v_mfma_f32_16x16x32_bf16 v[196:199], v[148:151], v[76:79], v[196:199]\n\t" \
  "ds_read_b64_tr_b16 v[134:135], v59 offset:23040\n\t" \
  "v_mfma_f32_16x16x32_bf16 v[204:207], v[148:151], v[80:83], v[204:207]\n\t" \
  "v_mfma_f32_16x16x32_bf16 v[204:207], v[96:99], v[84:87], v[204:207]\n\t" \
  "v_mfma_f32_16x16x32_bf16 v[212:215], v[96:99], v[88:91], v[212:215]\n\t" \
  "v_mfma_f32_16x16x32_bf16 v[212:215], v[148:151], v[92:95], v[212:215]\n\t" \
  "v_mfma_f32_16x16x32_bf16 v[216:219], v[72:75], v[64:67], v[216:219]\n\t" \
  "v_mfma_f32_16x16x32_bf16 v[216:219], v[140:143], v[104:107], v[216:219]\n\t" \
  "v_mfma_f32_16x16x32_bf16 v[224:227], v[68:71], v[104:107], v[224:227]\n\t" \
  "v_mfma_f32_16x16x32_bf16 v[224:227], v[76:79], v[64:67], v[224:227]\n\t" \
  "v_mfma_f32_16x16x32_bf16 v[232:235], v[80:83], v[64:67], v[232:235]\n\t" \
  "v_mfma_f32_16x16x32_bf16 v[232:235], v[84:87], v[104:107], v[232:235]\n\t" \
  "v_mfma_f32_16x16x32_bf16 v[240:243], v[88:91], v[104:107], v[240:243]\n\t" \
  "v_mfma_f32_16x16x32_bf16 v[240:243], v[92:95], v[64:67], v[240:243]\n\t" \
  "v_mfma_f32_16x16x32_bf16 v[248:251], v[112:115], v[64:67], v[248:251]\n\t" \
  "v_mfma_f32_16x16x32_bf16 v[248:251], v[116:119], v[104:107], v[248:251]\n\t" \
  "v_mfma_f32_16x16x32_bf16 v[252:255], v[112:115], v[120:123], v[252:255]\n\t" \
  "s_waitcnt lgkmcnt(0)\n\t" \
  "v_mfma_f32_16x16x32_bf16 v[252:255], v[116:119], v[124:127], v[252:255]\n\t" \
  "v_mfma_f32_16x16x32_bf16 v[220:223], v[140:143], v[124:127], v[220:223]\n\t" \
  "v_mfma_f32_16x16x32_bf16 v[220:223], v[72:75], v[120:123], v[220:223]\n\t" \
  "s_barrier\n\t" \
  "v_mfma_f32_16x16x32_bf16 v[228:231], v[76:79], v[120:123], v[228:231]\n\t" \
  "ds_read_b64_tr_b16 v[136:137], v62 offset:26624\n\t" \
  "v_bfe_u32 v30, v4, 9, 1\n\t" \
  "v_mfma_f32_16x16x32_bf16 v[228:231], v[68:71], v[124:127], v[228:231]\n\t" \
  "ds_read_b64_tr_b16 v[138:139], v62 offset:27136\n\t" \
  "v_mad_i32_i24 v30, v30, -16, v0\n\t" \
  "v_mfma_f32_16x16x32_bf16 v[236:239], v[84:87], v[124:127], v[236:239]\n\t" \
  "ds_read_b128 v[108:111], v60 offset:10240\n\t" \
  "ds_read_b128 v[26:29], v0 offset:1552\n\t" \
  "v_mfma_f32_16x16x32_bf16 v[236:239], v[80:83], v[120:123], v[236:239]\n\t" \
  "ds_read_b64_tr_b16 v[96:97], v62 offset:30720\n\t" \
  "ds_read_b128 v[30:33], v30 offset:1040\n\t" \
  "v_mfma_f32_16x16x32_bf16 v[244:247], v[92:95], v[120:123], v[244:247]\n\t" \
  "ds_read_b64_tr_b16 v[98:99], v62 offset:31232\n\t" \
  "ds_read_b128 v[34:37], v0 offset:512\n\t" \
  "v_mfma_f32_16x16x32_bf16 v[244:247], v[88:91], v[124:127], v[244:247]\n\t" \
  "ds_read_b128 v[148:151], v61 offset:10240\n\t" \
  "ds_read_b64_tr_b16 v[38:39], v1 offset:512\n\t" \
  "v_mfma_f32_16x16x32_bf16 v[152:155], v[128:131], v[72:75], v[152:155]\n\t" \
  "ds_read_b128 v[64:67], v61 offset:12288\n\t" \
  "ds_read_b64_tr_b16 v[40:41], v1 offset:0\n\t" \
  "v_mfma_f32_16x16x32_bf16 v[152:155], v[144:147], v[140:143], v[152:155]\n\t" \
  "ds_read_b128 v[104:107], v60 offset:12288\n\t" \
  "s_waitcnt vmcnt(0)\n\t" \
  "v_mfma_f32_16x16x32_bf16 v[160:163], v[144:147], v[68:71], v[160:163]\n\t" \
  "ds_read_b128 v[112:115], v60 offset:14336\n\t" \
  "v_perm_b32 v18, v10, v10, v14\n\t" \
  "v_mfma_f32_16x16x32_bf16 v[160:163], v[128:131], v[76:79], v[160:163]\n\t" \
  "ds_read_b128 v[116:119], v61 offset:14336\n\t" \
  "v_perm_b32 v19, v10, v10, v15\n\t" \
  "v_mfma_f32_16x16x32_bf16 v[168:171], v[128:131], v[80:83], v[168:171]\n\t" \
  "ds_read_b128 v[120:123], v61 offset:16384\n\t" \
  "v_perm_b32 v20, v10, v10, v16\n\t" \
  "v_mfma_f32_16x16x32_bf16 v[168:171], v[144:147], v[84:87], v[168:171]\n\t" \
  "ds_read_b128 v[124:127], v60 offset:16384\n\t" \
  "v_perm_b32 v21, v10, v10, v17\n\t" \
  "v_mfma_f32_16x16x32_bf16 v[176:179], v[144:147], v[88:91], v[176:179]\n\t" \
  "ds_read_b64_tr_b16 v[144:145], v63 offset:26624\n\t" \
  "v_perm_b32 v22, v11, v11, v14\n\t" \
  "v_mfma_f32_16x16x32_bf16 v[176:179], v[128:131], v[92:95], v[176:179]\n\t" \
  "ds_read_b64_tr_b16 v[146:147], v63 offset:27136\n\t" \
  "v_perm_b32 v23, v11, v11, v15\n\t" \
  "v_mfma_f32_16x16x32_bf16 v[156:159], v[100:103], v[72:75], v[156:159]\n\t" \
  "ds_read_b64_tr_b16 v[128:129], v63 offset:30720\n\t" \
  "v_perm_b32 v24, v11, v11, v16\n\t" \
  "v_mfma_f32_16x16x32_bf16 v[156:159], v[132:135], v[140:143], v[156:159]\n\t" \
  "ds_read_b64_tr_b16 v[130:131], v63 offset:31232\n\t" \
  "v_perm_b32 v25, v11, v11, v17\n\t" \
  "v_mfma_f32_16x16x32_bf16 v[164:167], v[132:135], v[68:71], v[164:167]\n\t" \
  "ds_read_b64_tr_b16 v[72:73], v62 offset:34816\n\t" \
  "v_mfma_f32_16x16x32_bf16 v[164:167], v[100:103], v[76:79], v[164:167]\n\t" \
  "ds_read_b64_tr_b16 v[74:75], v62 offset:35328\n\t" \
  "v_mfma_f32_16x16x32_bf16 v[172:175], v[100:103], v[80:83], v[172:175]\n\t" \
  "ds_read_b64_tr_b16 v[140:141], v62 offset:38912\n\t" \
  "s_waitcnt lgkmcnt(6)\n\t" \
  "v_mfma_f32_16x16x32_bf16 v[42:45], v[26:29], v[18:21], 0\n\t" \
  "v_mfma_f32_16x16x32_bf16 v[42:45], v[30:33], v[22:25], v[42:45]\n\t" \
  "v_mfma_f32_16x16x32_bf16 v[46:49], v[30:33], v[18:21], 0\n\t" \
  "v_mfma_f32_16x16x32_bf16 v[46:49], v[34:37], v[22:25], v[46:49]\n\t" \
  "ds_read_b128 v[26:29], v0 offset:1808\n\t" \
  "ds_read_b128 v[30:33], v0 offset:1296\n\t" \
  "v_mfma_f32_16x16x32_bf16 v[172:175], v[132:135], v[84:87], v[172:175]\n\t" \
  "ds_read_b64_tr_b16 v[142:143], v62 offset:39424\n\t" \
  "ds_read_b128 v[34:37], v0 offset:768\n\t" \
  "v_mfma_f32_16x16x32_bf16 v[180:183], v[132:135], v[88:91], v[180:183]\n\t" \
  "ds_read_b128 v[68:71], v60 offset:0\n\t" \
  "v_mfma_f32_16x16x32_bf16 v[180:183], v[100:103], v[92:95], v[180:183]\n\t" \
  "ds_read_b128 v[76:79], v61 offset:0\n\t" \
  "v_mfma_f32_16x16x32_bf16 v[184:187], v[136:139], v[108:111], v[184:187]\n\t" \
  "ds_read_b64_tr_b16 v[80:81], v63 offset:34816\n\t" \
  "v_lshlrev_b32 v50, 16, v38\n\t" \
  "v_mfma_f32_16x16x32_bf16 v[184:187], v[96:99], v[148:151], v[184:187]\n\t" \
  "ds_read_b64_tr_b16 v[82:83], v63 offset:35328\n\t" \
  "v_lshlrev_b32 v51, 16, v40\n\t" \
  "v_mfma_f32_16x16x32_bf16 v[192:195], v[96:99], v[64:67], v[192:195]\n\t" \
  "ds_read_b64_tr_b16 v[84:85], v63 offset:38912\n\t" \
  "v_mul_f32 v42, v42, v50\n\t" \
  "v_mfma_f32_16x16x32_bf16 v[192:195], v[136:139], v[104:107], v[192:195]\n\t" \
  "ds_read_b64_tr_b16 v[86:87], v63 offset:39424\n\t" \
  "v_mul_f32 v46, v46, v51\n\t" \
  "v_mfma_f32_16x16x32_bf16 v[200:203], v[136:139], v[112:115], v[200:203]\n\t" \
  "ds_read_b64_tr_b16 v[88:89], v62 offset:18432\n\t" \
  "v_cvt_pk_bf16_f32 v42, v42, v46\n\t" \
  "v_mfma_f32_16x16x32_bf16 v[200:203], v[96:99], v[116:119], v[200:203]\n\t" \
  "ds_read_b64_tr_b16 v[90:91], v62 offset:18944\n\t" \
  "v_and_b32 v50, 0xffff0000, v38\n\t" \
  "v_mfma_f32_16x16x32_bf16 v[208:211], v[96:99], v[120:123], v[208:211]\n\t" \
  "ds_read_b64_tr_b16 v[132:133], v62 offset:22528\n\t" \
  "v_and_b32 v51, 0xffff0000, v40\n\t" \
  "v_mfma_f32_16x16x32_bf16 v[208:211], v[136:139], v[124:127], v[208:211]\n\t" \
  "ds_read_b64_tr_b16 v[134:135], v62 offset:23040\n\t" \
  "v_mul_f32 v43, v43, v50\n\t" \
  "s_waitcnt lgkmcnt(7)\n\t" \
  "v_mfma_f32_16x16x32_bf16 v[188:191], v[144:147], v[108:111], v[188:191]\n\t" \
  "ds_read_b64_tr_b16 v[92:93], v63 offset:18432\n\t" \
  "v_mul_f32 v47, v47, v51\n\t" \
  "v_mfma_f32_16x16x32_bf16 v[188:191], v[128:131], v[148:151], v[188:191]\n\t" \
  "ds_read_b64_tr_b16 v[94:95], v63 offset:18944\n\t" \
  "v_cvt_pk_bf16_f32 v43, v43, v47\n\t" \
  "v_mfma_f32_16x16x32_bf16 v[196:199], v[128:131], v[64:67], v[196:199]\n\t" \
  "ds_read_b64_tr_b16 v[100:101], v63 offset:22528\n\t" \
  "v_lshlrev_b32 v50, 16, v39\n\t" \
  "v_mfma_f32_16x16x32_bf16 v[196:199], v[144:147], v[104:107], v[196:199]\n\t" \
  "ds_read_b64_tr_b16 v[102:103], v63 offset:23040\n\t" \
  "v_lshlrev_b32 v51, 16, v41\n\t" \
  "v_mfma_f32_16x16x32_bf16 v[204:207], v[144:147], v[112:115], v[204:207]\n\t" \
  "v_mul_f32 v44, v44, v50\n\t" \
  "v_mul_f32 v48, v48, v51\n\t" \
  "v_mfma_f32_16x16x32_bf16 v[204:207], v[128:131], v[116:119], v[204:207]\n\t" \
  "v_cvt_pk_bf16_f32 v44, v44, v48\n\t" \
  "v_and_b32 v50, 0xffff0000, v39\n\t" \
  "v_mfma_f32_16x16x32_bf16 v[212:215], v[128:131], v[120:123], v[212:215]\n\t" \
  "v_and_b32 v51, 0xffff0000, v41\n\t" \
  "v_mul_f32 v45, v45, v50\n\t" \
  "v_mfma_f32_16x16x32_bf16 v[212:215], v[144:147], v[124:127], v[212:215]\n\t" \
  "v_mul_f32 v49, v49, v51\n\t" \
  "ds_read_b64_tr_b16 v[38:39], v1 offset:768\n\t" \
  "v_mfma_f32_16x16x32_bf16 v[216:219], v[108:111], v[72:75], v[216:219]\n\t" \
  "ds_read_b64_tr_b16 v[40:41], v1 offset:256\n\t" \
  "v_cvt_pk_bf16_f32 v45, v45, v49\n\t" \
  "v_mfma_f32_16x16x32_bf16 v[216:219], v[148:151], v[140:143], v[216:219]\n\t" \
  "v_mfma_f32_16x16x32_bf16 v[224:227], v[64:67], v[140:143], v[224:227]\n\t" \
  "v_mfma_f32_16x16x32_bf16 v[224:227], v[104:107], v[72:75], v[224:227]\n\t" \
  "v_mfma_f32_16x16x32_bf16 v[232:235], v[112:115], v[72:75], v[232:235]\n\t" \
  "v_mfma_f32_16x16x32_bf16 v[232:235], v[116:119], v[140:143], v[232:235]\n\t" \
  "v_mfma_f32_16x16x32_bf16 v[240:243], v[120:123], v[140:143], v[240:243]\n\t" \
  "v_mfma_f32_16x16x32_bf16 v[240:243], v[124:127], v[72:75], v[240:243]\n\t" \
  "v_mfma_f32_16x16x32_bf16 v[248:251], v[68:71], v[72:75], v[248:251]\n\t" \
  "v_mfma_f32_16x16x32_bf16 v[248:251], v[76:79], v[140:143], v[248:251]\n\t" \
  "s_waitcnt lgkmcnt(0)\n\t" \
  "v_mfma_f32_16x16x32_bf16 v[252:255], v[68:71], v[80:83], v[252:255]\n\t" \
  "v_mfma_f32_16x16x32_bf16 v[252:255], v[76:79], v[84:87], v[252:255]\n\t" \
  "v_mfma_f32_16x16x32_bf16 v[220:223], v[148:151], v[84:87], v[220:223]\n\t" \
  "v_mfma_f32_16x16x32_bf16 v[220:223], v[108:111], v[80:83], v[220:223]\n\t" \
  "s_barrier\n\t" \
  "v_mfma_f32_16x16x32_bf16 v[228:231], v[104:107], v[80:83], v[228:231]\n\t" \
  "ds_read_b64_tr_b16 v[96:97], v54 offset:26624\n\t" \
  "v_mfma_f32_16x16x32_bf16 v[228:231], v[64:67], v[84:87], v[228:231]\n\t" \
  "v_mov_b32 v50, 163344\n\t" \
  "ds_read_b64_tr_b16 v[98:99], v54 offset:27136\n\t" \
  "v_mfma_f32_16x16x32_bf16 v[236:239], v[116:119], v[84:87], v[236:239]\n\t" \
  "ds_read_b32 v50, v50 offset:24\n\t" \
  "ds_read_b128 v[136:139], v52 offset:10240\n\t" \
  "v_mfma_f32_16x16x32_bf16 v[236:239], v[112:115], v[80:83], v[236:239]\n\t" \
  "ds_read_b64_tr_b16 v[128:129], v54 offset:30720\n\t" \
  "v_mfma_f32_16x16x32_bf16 v[244:247], v[124:127], v[80:83], v[244:247]\n\t" \
  "ds_read_b64_tr_b16 v[130:131], v54 offset:31232\n\t" \
  "v_mfma_f32_16x16x32_bf16 v[244:247], v[120:123], v[84:87], v[244:247]\n\t" \
  "ds_read_b128 v[144:147], v53 offset:10240\n\t" \
  "v_mfma_f32_16x16x32_bf16 v[152:155], v[88:91], v[108:111], v[152:155]\n\t" \
  "ds_read_b128 v[72:75], v53 offset:12288\n\t" \
  "v_mfma_f32_16x16x32_bf16 v[152:155], v[132:135], v[148:151], v[152:155]\n\t" \
  "ds_read_b128 v[140:143], v52 offset:12288\n\t" \
  "v_mfma_f32_16x16x32_bf16 v[160:163], v[132:135], v[64:67], v[160:163]\n\t" \
  "ds_read_b128 v[68:71], v52 offset:14336\n\t" \
  "v_mfma_f32_16x16x32_bf16 v[160:163], v[88:91], v[104:107], v[160:163]\n\t" \
  "ds_read_b128 v[76:79], v53 offset:14336\n\t" \
  "v_mfma_f32_16x16x32_bf16 v[168:171], v[88:91], v[112:115], v[168:171]\n\t" \
  "ds_read_b128 v[80:83], v53 offset:16384\n\t" \
  "v_mfma_f32_16x16x32_bf16 v[168:171], v[132:135], v[116:119], v[168:171]\n\t" \
  "ds_read_b128 v[84:87], v52 offset:16384\n\t" \
  "v_mfma_f32_16x16x32_bf16 v[176:179], v[132:135], v[120:123], v[176:179]\n\t" \
  "s_waitcnt lgkmcnt(10)\n\t" \
  "v_readfirstlane_b32 s88, v50\n\t" \
  "s_mov_b32 s89, 0\n\t" \
  "ds_read_b64_tr_b16 v[132:133], v55 offset:26624\n\t" \
  "v_mfma_f32_16x16x32_bf16 v[176:179], v[88:91], v[124:127], v[176:179]\n\t" \
  "ds_read_b64_tr_b16 v[134:135], v55 offset:27136\n\t" \
  "v_mfma_f32_16x16x32_bf16 v[156:159], v[92:95], v[108:111], v[156:159]\n\t" \
  "s_xor_b64 %24, %24, s[88:89]\n\t" \
  "ds_read_b64_tr_b16 v[88:89], v55 offset:30720\n\t" \
  "v_mfma_f32_16x16x32_bf16 v[156:159], v[100:103], v[148:151], v[156:159]\n\t" \
  "s_xor_b64 %25, %25, s[88:89]\n\t" \
  "ds_read_b64_tr_b16 v[90:91], v55 offset:31232\n\t" \
  "v_mfma_f32_16x16x32_bf16 v[164:167], v[100:103], v[64:67], v[164:167]\n\t" \
  "s_xor_b64 %14, %14, s[88:89]\n\t" \
  "ds_read_b64_tr_b16 v[108:109], v54 offset:34816\n\t" \
  "v_mfma_f32_16x16x32_bf16 v[164:167], v[92:95], v[104:107], v[164:167]\n\t" \
  "s_xor_b64 %19, %19, s[88:89]\n\t" \
  "ds_read_b64_tr_b16 v[110:111], v54 offset:35328\n\t" \
  "v_mfma_f32_16x16x32_bf16 v[172:175], v[92:95], v[112:115], v[172:175]\n\t" \
  "s_xor_b64 %21, %21, s[88:89]\n\t" \
  "ds_read_b64_tr_b16 v[148:149], v54 offset:38912\n\t" \
  "v_mfma_f32_16x16x32_bf16 v[172:175], v[100:103], v[116:119], v[172:175]\n\t" \
  "s_xor_b64 s[92:93], s[92:93], s[88:89]\n\t" \
  "ds_read_b64_tr_b16 v[150:151], v54 offset:39424\n\t" \
  "v_mfma_f32_16x16x32_bf16 v[180:183], v[100:103], v[120:123], v[180:183]\n\t" \
  "ds_read_b128 v[64:67], v52 offset:0\n\t" \
  "v_mfma_f32_16x16x32_bf16 v[180:183], v[92:95], v[124:127], v[180:183]\n\t" \
  "ds_read_b128 v[104:107], v53 offset:0\n\t" \
  "s_waitcnt lgkmcnt(7)\n\t" \
  "v_mfma_f32_16x16x32_bf16 v[184:187], v[96:99], v[136:139], v[184:187]\n\t" \
  "ds_read_b64_tr_b16 v[112:113], v55 offset:34816\n\t" \
  "v_mfma_f32_16x16x32_bf16 v[184:187], v[128:131], v[144:147], v[184:187]\n\t" \
  "ds_read_b64_tr_b16 v[114:115], v55 offset:35328\n\t" \
  "v_mfma_f32_16x16x32_bf16 v[192:195], v[128:131], v[72:75], v[192:195]\n\t" \
  "ds_read_b64_tr_b16 v[116:117], v55 offset:38912\n\t" \
  "v_mfma_f32_16x16x32_bf16 v[192:195], v[96:99], v[140:143], v[192:195]\n\t" \
  "ds_read_b64_tr_b16 v[118:119], v55 offset:39424\n\t" \
  "v_mfma_f32_16x16x32_bf16 v[200:203], v[96:99], v[68:71], v[200:203]\n\t" \
  "ds_read_b64_tr_b16 v[100:101], v54 offset:18432\n\t" \
  "v_mfma_f32_16x16x32_bf16 v[200:203], v[128:131], v[76:79], v[200:203]\n\t" \
  "ds_read_b64_tr_b16 v[102:103], v54 offset:18944\n\t" \
  "v_mfma_f32_16x16x32_bf16 v[208:211], v[128:131], v[80:83], v[208:211]\n\t" \
  "ds_read_b64_tr_b16 v[120:121], v54 offset:22528\n\t" \
  "v_mfma_f32_16x16x32_bf16 v[208:211], v[96:99], v[84:87], v[208:211]\n\t" \
  "ds_read_b64_tr_b16 v[122:123], v54 offset:23040\n\t" \
  "v_mfma_f32_16x16x32_bf16 v[188:191], v[132:135], v[136:139], v[188:191]\n\t" \
  "ds_read_b64_tr_b16 v[92:93], v55 offset:18432\n\t" \
  "s_waitcnt lgkmcnt(7)\n\t" \
  "v_mfma_f32_16x16x32_bf16 v[188:191], v[88:91], v[144:147], v[188:191]\n\t" \
  "ds_read_b64_tr_b16 v[94:95], v55 offset:18944\n\t" \
  "v_mfma_f32_16x16x32_bf16 v[196:199], v[88:91], v[72:75], v[196:199]\n\t" \
  "ds_read_b64_tr_b16 v[124:125], v55 offset:22528\n\t" \
  "v_mfma_f32_16x16x32_bf16 v[196:199], v[132:135], v[140:143], v[196:199]\n\t" \
  "ds_read_b64_tr_b16 v[126:127], v55 offset:23040\n\t" \
  "v_mfma_f32_16x16x32_bf16 v[204:207], v[132:135], v[68:71], v[204:207]\n\t" \
  "v_mfma_f32_16x16x32_bf16 v[204:207], v[88:91], v[76:79], v[204:207]\n\t" \
  "v_mfma_f32_16x16x32_bf16 v[212:215], v[88:91], v[80:83], v[212:215]\n\t" \
  "v_mfma_f32_16x16x32_bf16 v[212:215], v[132:135], v[84:87], v[212:215]\n\t" \
  "v_mfma_f32_16x16x32_bf16 v[216:219], v[136:139], v[108:111], v[216:219]\n\t" \
  "v_mfma_f32_16x16x32_bf16 v[216:219], v[144:147], v[148:151], v[216:219]\n\t" \
  "v_mfma_f32_16x16x32_bf16 v[224:227], v[72:75], v[148:151], v[224:227]\n\t" \
  "v_mfma_f32_16x16x32_bf16 v[224:227], v[140:143], v[108:111], v[224:227]\n\t" \
  "v_mfma_f32_16x16x32_bf16 v[232:235], v[68:71], v[108:111], v[232:235]\n\t" \
  "global_store_dwordx4 v2, v[42:45], %24 offset:0 sc1\n\t" \
  "v_mfma_f32_16x16x32_bf16 v[232:235], v[76:79], v[148:151], v[232:235]\n\t" \
  "s_nop 0\n\t" \
  "v_mfma_f32_16x16x32_bf16 v[42:45], v[26:29], v[18:21], 0\n\t" \
  "v_mfma_f32_16x16x32_bf16 v[42:45], v[30:33], v[22:25], v[42:45]\n\t" \
  "v_mfma_f32_16x16x32_bf16 v[46:49], v[30:33], v[18:21], 0\n\t" \
  "v_mfma_f32_16x16x32_bf16 v[46:49], v[34:37], v[22:25], v[46:49]\n\t" \
  "v_bfe_u32 v30, v4, 9, 1\n\t" \
  "v_mad_i32_i24 v30, v30, -16, v0\n\t" \
  "v_mfma_f32_16x16x32_bf16 v[240:243], v[80:83], v[148:151], v[240:243]\n\t" \
  "ds_read_b128 v[26:29], v0 offset:1584\n\t" \
  "ds_read_b128 v[30:33], v30 offset:1072\n\t" \
  "v_mfma_f32_16x16x32_bf16 v[240:243], v[84:87], v[108:111], v[240:243]\n\t" \
  "ds_read_b128 v[34:37], v0 offset:544\n\t" \
  "v_perm_b32 v18, v12, v12, v14\n\t" \
  "v_mfma_f32_16x16x32_bf16 v[248:251], v[64:67], v[108:111], v[248:251]\n\t" \
  "v_perm_b32 v19, v12, v12, v15\n\t" \
  "v_perm_b32 v20, v12, v12, v16\n\t" \
  "v_mfma_f32_16x16x32_bf16 v[248:251], v[104:107], v[148:151], v[248:251]\n\t" \
  "v_perm_b32 v21, v12, v12, v17\n\t" \
  "v_perm_b32 v22, v13, v13, v14\n\t" \
  "v_mfma_f32_16x16x32_bf16 v[252:255], v[64:67], v[112:115], v[252:255]\n\t" \
  "v_perm_b32 v23, v13, v13, v15\n\t" \
  "v_perm_b32 v24, v13, v13, v16\n\t" \
  "s_waitcnt lgkmcnt(3)\n\t" \
  "v_mfma_f32_16x16x32_bf16 v[252:255], v[104:107], v[116:119], v[252:255]\n\t" \
  "v_perm_b32 v25, v13, v13, v17\n\t" \
  "v_lshlrev_b32 v50, 16, v38\n\t" \
  "v_mfma_f32_16x16x32_bf16 v[220:223], v[144:147], v[116:119], v[220:223]\n\t" \
  "v_lshlrev_b32 v51, 16, v40\n\t" \
  "v_mul_f32 v42, v42, v50\n\t" \
  "v_mfma_f32_16x16x32_bf16 v[220:223], v[136:139], v[112:115], v[220:223]\n\t" \
  "v_mul_f32 v46, v46, v51\n\t" \
  "v_cvt_pk_bf16_f32 v42, v42, v46\n\t" \
  "s_waitcnt lgkmcnt(0)\n\t" \
  "s_barrier\n\t" \
  "v_mfma_f32_16x16x32_bf16 v[228:231], v[140:143], v[112:115], v[228:231]\n\t" \
  "ds_read_b64_tr_b16 v[128:129], v58 offset:26624\n\t" \
  "v_and_b32 v50, 0xffff0000, v38\n\t" \
  "v_mfma_f32_16x16x32_bf16 v[228:231], v[72:75], v[116:119], v[228:231]\n\t" \
  "ds_read_b64_tr_b16 v[130:131], v58 offset:27136\n\t" \
  "v_and_b32 v51, 0xffff0000, v40\n\t" \
  "v_mfma_f32_16x16x32_bf16 v[236:239], v[76:79], v[116:119], v[236:239]\n\t" \
  "ds_read_b128 v[96:99], v56 offset:10240\n\t" \
  "v_mul_f32 v43, v43, v50\n\t" \
  "v_mfma_f32_16x16x32_bf16 v[236:239], v[68:71], v[112:115], v[236:239]\n\t" \
  "ds_read_b64_tr_b16 v[88:89], v58 offset:30720\n\t" \
  "500:\n\t" \
  "s_cbranch_execz 504f\n\t" \
  "v_mul_f32 v47, v47, v51\n\t" \
  "v_mfma_f32_16x16x32_bf16 v[244:247], v[84:87], v[112:115], v[244:247]\n\t" \
  "ds_read_b64_tr_b16 v[90:91], v58 offset:31232\n\t" \
  "v_cvt_pk_bf16_f32 v43, v43, v47\n\t" \
  "v_mfma_f32_16x16x32_bf16 v[244:247], v[80:83], v[116:119], v[244:247]\n\t" \
  "ds_read_b128 v[132:135], v57 offset:10240\n\t" \
  "v_lshlrev_b32 v50, 16, v39\n\t" \
  "v_mfma_f32_16x16x32_bf16 v[152:155], v[100:103], v[136:139], v[152:155]\n\t" \
  "ds_read_b128 v[108:111], v57 offset:12288\n\t" \
  "v_lshlrev_b32 v51, 16, v41\n\t" \
  "v_mfma_f32_16x16x32_bf16 v[152:155], v[120:123], v[144:147], v[152:155]\n\t" \
  "ds_read_b128 v[148:151], v56 offset:12288\n\t" \
  "v_mul_f32 v44, v44, v50\n\t" \
  "v_mfma_f32_16x16x32_bf16 v[160:163], v[120:123], v[72:75], v[160:163]\n\t" \
  "ds_read_b128 v[64:67], v56 offset:14336\n\t" \
  "v_mul_f32 v48, v48, v51\n\t" \
  "v_mfma_f32_16x16x32_bf16 v[160:163], v[100:103], v[140:143], v[160:163]\n\t" \
  "ds_read_b128 v[104:107], v57 offset:14336\n\t" \
  "v_cvt_pk_bf16_f32 v44, v44, v48\n\t" \
  "v_mfma_f32_16x16x32_bf16 v[168:171], v[100:103], v[68:71], v[168:171]\n\t" \
  "ds_read_b128 v[112:115], v57 offset:16384\n\t" \
  "v_and_b32 v50, 0xffff0000, v39\n\t" \
  "v_mfma_f32_16x16x32_bf16 v[168:171], v[120:123], v[76:79], v[168:171]\n\t" \
  "ds_read_b128 v[116:119], v56 offset:16384\n\t" \
  "v_and_b32 v51, 0xffff0000, v41\n\t" \
  "v_mfma_f32_16x16x32_bf16 v[176:179], v[120:123], v[80:83], v[176:179]\n\t" \
  "ds_read_b64_tr_b16 v[120:121], v59 offset:26624\n\t" \
  "v_mul_f32 v45, v45, v50\n\t" \
  "v_mfma_f32_16x16x32_bf16 v[176:179], v[100:103], v[84:87], v[176:179]\n\t" \
  "ds_read_b64_tr_b16 v[122:123], v59 offset:27136\n\t" \
  "v_mul_f32 v49, v49, v51\n\t" \
  "v_mfma_f32_16x16x32_bf16 v[156:159], v[92:95], v[136:139], v[156:159]\n\t" \
  "ds_read_b64_tr_b16 v[100:101], v59 offset:30720\n\t" \
  "ds_read_b64_tr_b16 v[38:39], v1 offset:544\n\t" \
  "v_mfma_f32_16x16x32_bf16 v[156:159], v[124:127], v[144:147], v[156:159]\n\t" \
  "ds_read_b64_tr_b16 v[102:103], v59 offset:31232\n\t" \
  "ds_read_b64_tr_b16 v[40:41], v1 offset:32\n\t" \
  "v_mfma_f32_16x16x32_bf16 v[164:167], v[124:127], v[72:75], v[164:167]\n\t" \
  "ds_read_b64_tr_b16 v[136:137], v58 offset:34816\n\t" \
  "v_cvt_pk_bf16_f32 v45, v45, v49\n\t" \
  "v_mfma_f32_16x16x32_bf16 v[164:167], v[92:95], v[140:143], v[164:167]\n\t" \
  "ds_read_b64_tr_b16 v[138:139], v58 offset:35328\n\t" \
  "v_mfma_f32_16x16x32_bf16 v[172:175], v[92:95], v[68:71], v[172:175]\n\t" \
  "ds_read_b64_tr_b16 v[144:145], v58 offset:38912\n\t" \
  "v_mfma_f32_16x16x32_bf16 v[172:175], v[124:127], v[76:79], v[172:175]\n\t" \
  "ds_read_b64_tr_b16 v[146:147], v58 offset:39424\n\t" \
  "v_mfma_f32_16x16x32_bf16 v[180:183], v[124:127], v[80:83], v[180:183]\n\t" \
  "ds_read_b128 v[72:75], v56 offset:0\n\t" \
  "v_mfma_f32_16x16x32_bf16 v[180:183], v[92:95], v[84:87], v[180:183]\n\t" \
  "ds_read_b128 v[140:143], v57 offset:0\n\t" \
  "s_waitcnt lgkmcnt(8)\n\t" \
  "v_mfma_f32_16x16x32_bf16 v[184:187], v[128:131], v[96:99], v[184:187]\n\t" \
  "ds_read_b64_tr_b16 v[68:69], v59 offset:34816\n\t" \
  "v_mfma_f32_16x16x32_bf16 v[184:187], v[88:91], v[132:135], v[184:187]\n\t" \
  "ds_read_b64_tr_b16 v[70:71], v59 offset:35328\n\t" \
  "v_mfma_f32_16x16x32_bf16 v[192:195], v[88:91], v[108:111], v[192:195]\n\t" \
  "ds_read_b64_tr_b16 v[76:77], v59 offset:38912\n\t" \
  "v_mfma_f32_16x16x32_bf16 v[192:195], v[128:131], v[148:151], v[192:195]\n\t" \
  "ds_read_b64_tr_b16 v[78:79], v59 offset:39424\n\t" \
  "v_mfma_f32_16x16x32_bf16 v[200:203], v[128:131], v[64:67], v[200:203]\n\t" \
  "ds_read_b64_tr_b16 v[80:81], v58 offset:18432\n\t" \
  "v_mfma_f32_16x16x32_bf16 v[200:203], v[88:91], v[104:107], v[200:203]\n\t" \
  "ds_read_b64_tr_b16 v[82:83], v58 offset:18944\n\t" \
  "v_mfma_f32_16x16x32_bf16 v[208:211], v[88:91], v[112:115], v[208:211]\n\t" \
  "ds_read_b64_tr_b16 v[124:125], v58 offset:22528\n\t" \
  "v_mfma_f32_16x16x32_bf16 v[208:211], v[128:131], v[116:119], v[208:211]\n\t" \
  "ds_read_b64_tr_b16 v[126:127], v58 offset:23040\n\t" \
  "v_mfma_f32_16x16x32_bf16 v[188:191], v[120:123], v[96:99], v[188:191]\n\t" \
  "ds_read_b64_tr_b16 v[84:85], v59 offset:18432\n\t" \
  "s_waitcnt lgkmcnt(7)\n\t" \
  "v_mfma_f32_16x16x32_bf16 v[188:191], v[100:103], v[132:135], v[188:191]\n\t" \
  "ds_read_b64_tr_b16 v[86:87], v59 offset:18944\n\t" \
  "v_mfma_f32_16x16x32_bf16 v[196:199], v[100:103], v[108:111], v[196:199]\n\t" \
  "ds_read_b64_tr_b16 v[92:93], v59 offset:22528\n\t" \
  "v_mfma_f32_16x16x32_bf16 v[196:199], v[120:123], v[148:151], v[196:199]\n\t" \
  "ds_read_b64_tr_b16 v[94:95], v59 offset:23040\n\t" \
  "v_mfma_f32_16x16x32_bf16 v[204:207], v[120:123], v[64:67], v[204:207]\n\t" \
  "v_mfma_f32_16x16x32_bf16 v[204:207], v[100:103], v[104:107], v[204:207]\n\t" \
  "v_mfma_f32_16x16x32_bf16 v[212:215], v[100:103], v[112:115], v[212:215]\n\t" \
  "v_mfma_f32_16x16x32_bf16 v[212:215], v[120:123], v[116:119], v[212:215]\n\t" \
  "v_mfma_f32_16x16x32_bf16 v[216:219], v[96:99], v[136:139], v[216:219]\n\t" \
  "v_mfma_f32_16x16x32_bf16 v[216:219], v[132:135], v[144:147], v[216:219]\n\t" \
  "v_mfma_f32_16x16x32_bf16 v[224:227], v[108:111], v[144:147], v[224:227]\n\t" \
  "v_mfma_f32_16x16x32_bf16 v[224:227], v[148:151], v[136:139], v[224:227]\n\t" \
  "v_mfma_f32_16x16x32_bf16 v[232:235], v[64:67], v[136:139], v[232:235]\n\t" \
  "global_store_dwordx4 v2, v[42:45], %24 offset:1024 sc1\n\t" \
  "v_add_u32 v2, 0x20000, v2\n\t" \
  "v_mfma_f32_16x16x32_bf16 v[232:235], v[104:107], v[144:147], v[232:235]\n\t" \
  "v_mfma_f32_16x16x32_bf16 v[240:243], v[112:115], v[144:147], v[240:243]\n\t" \
  "v_mfma_f32_16x16x32_bf16 v[42:45], v[26:29], v[18:21], 0\n\t" \
  "v_mfma_f32_16x16x32_bf16 v[42:45], v[30:33], v[22:25], v[42:45]\n\t" \
  "v_mfma_f32_16x16x32_bf16 v[46:49], v[30:33], v[18:21], 0\n\t" \
  "v_mfma_f32_16x16x32_bf16 v[46:49], v[34:37], v[22:25], v[46:49]\n\t" \
  "ds_read_b128 v[26:29], v0 offset:1840\n\t" \
  "ds_read_b128 v[30:33], v0 offset:1328\n\t" \
  "v_mfma_f32_16x16x32_bf16 v[240:243], v[116:119], v[136:139], v[240:243]\n\t" \
  "ds_read_b128 v[34:37], v0 offset:800\n\t" \
  "v_mfma_f32_16x16x32_bf16 v[248:251], v[72:75], v[136:139], v[248:251]\n\t" \
  "v_mfma_f32_16x16x32_bf16 v[248:251], v[140:143], v[144:147], v[248:251]\n\t" \
  "v_lshlrev_b32 v50, 16, v38\n\t" \
  "v_lshlrev_b32 v51, 16, v40\n\t" \
  "v_mfma_f32_16x16x32_bf16 v[252:255], v[72:75], v[68:71], v[252:255]\n\t" \
  "v_mul_f32 v42, v42, v50\n\t" \
  "v_mul_f32 v46, v46, v51\n\t" \
  "s_waitcnt lgkmcnt(3)\n\t" \
  "v_mfma_f32_16x16x32_bf16 v[252:255], v[140:143], v[76:79], v[252:255]\n\t" \
  "v_cvt_pk_bf16_f32 v42, v42, v46\n\t" \
  "v_and_b32 v50, 0xffff0000, v38\n\t" \
  "v_mfma_f32_16x16x32_bf16 v[220:223], v[132:135], v[76:79], v[220:223]\n\t" \
  "v_and_b32 v51, 0xffff0000, v40\n\t" \
  "v_mul_f32 v43, v43, v50\n\t" \
  "v_mfma_f32_16x16x32_bf16 v[220:223], v[96:99], v[68:71], v[220:223]\n\t" \
  "v_mul_f32 v47, v47, v51\n\t" \
  "v_cvt_pk_bf16_f32 v43, v43, v47\n\t" \
  "s_waitcnt lgkmcnt(0)\n\t" \
  "s_barrier\n\t" \
  "v_mfma_f32_16x16x32_bf16 v[228:231], v[148:151], v[68:71], v[228:231]\n\t" \
  "ds_read_b64_tr_b16 v[88:89], v62 offset:26624\n\t" \
  "v_lshlrev_b32 v50, 16, v39\n\t" \
  "v_mfma_f32_16x16x32_bf16 v[228:231], v[108:111], v[76:79], v[228:231]\n\t" \
  "ds_read_b64_tr_b16 v[90:91], v62 offset:27136\n\t" \
  "v_lshlrev_b32 v51, 16, v41\n\t" \
  "v_mfma_f32_16x16x32_bf16 v[236:239], v[104:107], v[76:79], v[236:239]\n\t" \
  "ds_read_b128 v[128:131], v60 offset:10240\n\t" \
  "v_mul_f32 v44, v44, v50\n\t" \
  "v_mfma_f32_16x16x32_bf16 v[236:239], v[64:67], v[68:71], v[236:239]\n\t" \
  "ds_read_b64_tr_b16 v[100:101], v62 offset:30720\n\t" \
  "v_mul_f32 v48, v48, v51\n\t" \
  "v_mfma_f32_16x16x32_bf16 v[244:247], v[116:119], v[68:71], v[244:247]\n\t" \
  "ds_read_b64_tr_b16 v[102:103], v62 offset:31232\n\t" \
  "v_cvt_pk_bf16_f32 v44, v44, v48\n\t" \
  "v_mfma_f32_16x16x32_bf16 v[244:247], v[112:115], v[76:79], v[244:247]\n\t" \
  "ds_read_b128 v[120:123], v61 offset:10240\n\t" \
  "v_and_b32 v50, 0xffff0000, v39\n\t" \
  "v_mfma_f32_16x16x32_bf16 v[152:155], v[80:83], v[96:99], v[152:155]\n\t" \
  "ds_read_b128 v[136:139], v61 offset:12288\n\t" \
  "v_and_b32 v51, 0xffff0000, v41\n\t" \
  "v_mfma_f32_16x16x32_bf16 v[152:155], v[124:127], v[132:135], v[152:155]\n\t" \
  "ds_read_b128 v[144:147], v60 offset:12288\n\t" \
  "v_mul_f32 v45, v45, v50\n\t" \
  "v_mfma_f32_16x16x32_bf16 v[160:163], v[124:127], v[108:111], v[160:163]\n\t" \
  "ds_read_b128 v[72:75], v60 offset:14336\n\t" \
  "v_mul_f32 v49, v49, v51\n\t" \
  "v_mfma_f32_16x16x32_bf16 v[160:163], v[80:83], v[148:151], v[160:163]\n\t" \
  "ds_read_b128 v[140:143], v61 offset:14336\n\t" \
  "ds_read_b64_tr_b16 v[38:39], v1 offset:800\n\t" \
  "v_mfma_f32_16x16x32_bf16 v[168:171], v[80:83], v[64:67], v[168:171]\n\t" \
  "ds_read_b128 v[68:71], v61 offset:16384\n\t" \
  "ds_read_b64_tr_b16 v[40:41], v1 offset:288\n\t" \
  "v_mfma_f32_16x16x32_bf16 v[168:171], v[124:127], v[104:107], v[168:171]\n\t" \
  "ds_read_b128 v[76:79], v60 offset:16384\n\t" \
  "v_cvt_pk_bf16_f32 v45, v45, v49\n\t" \
  "v_mfma_f32_16x16x32_bf16 v[176:179], v[124:127], v[112:115], v[176:179]\n\t" \
  "ds_read_b64_tr_b16 v[124:125], v63 offset:26624\n\t" \
  "v_mfma_f32_16x16x32_bf16 v[176:179], v[80:83], v[116:119], v[176:179]\n\t" \
  "ds_read_b64_tr_b16 v[126:127], v63 offset:27136\n\t" \
  "v_mfma_f32_16x16x32_bf16 v[156:159], v[84:87], v[96:99], v[156:159]\n\t" \
  "ds_read_b64_tr_b16 v[80:81], v63 offset:30720\n\t" \
  "v_mfma_f32_16x16x32_bf16 v[156:159], v[92:95], v[132:135], v[156:159]\n\t" \
  "ds_read_b64_tr_b16 v[82:83], v63 offset:31232\n\t" \
  "v_mfma_f32_16x16x32_bf16 v[164:167], v[92:95], v[108:111], v[164:167]\n\t" \
  "ds_read_b64_tr_b16 v[96:97], v62 offset:34816\n\t" \
  "v_mfma_f32_16x16x32_bf16 v[164:167], v[84:87], v[148:151], v[164:167]\n\t" \
  "ds_read_b64_tr_b16 v[98:99], v62 offset:35328\n\t" \
  "v_mfma_f32_16x16x32_bf16 v[172:175], v[84:87], v[64:67], v[172:175]\n\t" \
  "ds_read_b64_tr_b16 v[132:133], v62 offset:38912\n\t" \
  "v_mfma_f32_16x16x32_bf16 v[172:175], v[92:95], v[104:107], v[172:175]\n\t" \
  "ds_read_b64_tr_b16 v[134:135], v62 offset:39424\n\t" \
  "v_mfma_f32_16x16x32_bf16 v[180:183], v[92:95], v[112:115], v[180:183]\n\t" \
  "ds_read_b128 v[108:111], v60 offset:0\n\t" \
  "v_mfma_f32_16x16x32_bf16 v[180:183], v[84:87], v[116:119], v[180:183]\n\t" \
  "ds_read_b128 v[148:151], v61 offset:0\n\t" \
  "s_waitcnt lgkmcnt(7)\n\t" \
  "v_mfma_f32_16x16x32_bf16 v[184:187], v[88:91], v[128:131], v[184:187]\n\t" \
  "ds_read_b64_tr_b16 v[64:65], v63 offset:34816\n\t" \
  "v_mfma_f32_16x16x32_bf16 v[184:187], v[100:103], v[120:123], v[184:187]\n\t" \
  "ds_read_b64_tr_b16 v[66:67], v63 offset:35328\n\t" \
  "v_mfma_f32_16x16x32_bf16 v[192:195], v[100:103], v[136:139], v[192:195]\n\t" \
  "ds_read_b64_tr_b16 v[104:105], v63 offset:38912\n\t" \
  "v_mfma_f32_16x16x32_bf16 v[192:195], v[88:91], v[144:147], v[192:195]\n\t" \
  "ds_read_b64_tr_b16 v[106:107], v63 offset:39424\n\t" \
  "v_mfma_f32_16x16x32_bf16 v[200:203], v[88:91], v[72:75], v[200:203]\n\t" \
  "ds_read_b64_tr_b16 v[92:93], v62 offset:18432\n\t" \
  "v_mfma_f32_16x16x32_bf16 v[200:203], v[100:103], v[140:143], v[200:203]\n\t" \
  "ds_read_b64_tr_b16 v[94:95], v62 offset:18944\n\t" \
  "v_mfma_f32_16x16x32_bf16 v[208:211], v[100:103], v[68:71], v[208:211]\n\t" \
  "ds_read_b64_tr_b16 v[112:113], v62 offset:22528\n\t" \
  "v_mfma_f32_16x16x32_bf16 v[208:211], v[88:91], v[76:79], v[208:211]\n\t" \
  "ds_read_b64_tr_b16 v[114:115], v62 offset:23040\n\t" \
  "v_mfma_f32_16x16x32_bf16 v[188:191], v[124:127], v[128:131], v[188:191]\n\t" \
  "ds_read_b64_tr_b16 v[84:85], v63 offset:18432\n\t" \
  "s_waitcnt lgkmcnt(7)\n\t" \
  "v_mfma_f32_16x16x32_bf16 v[188:191], v[80:83], v[120:123], v[188:191]\n\t" \
  "ds_read_b64_tr_b16 v[86:87], v63 offset:18944\n\t" \
  "v_mfma_f32_16x16x32_bf16 v[196:199], v[80:83], v[136:139], v[196:199]\n\t" \
  "ds_read_b64_tr_b16 v[116:117], v63 offset:22528\n\t" \
  "v_mfma_f32_16x16x32_bf16 v[196:199], v[124:127], v[144:147], v[196:199]\n\t" \
  "ds_read_b64_tr_b16 v[118:119], v63 offset:23040\n\t" \
  "v_mfma_f32_16x16x32_bf16 v[204:207], v[124:127], v[72:75], v[204:207]\n\t" \
  "v_mfma_f32_16x16x32_bf16 v[204:207], v[80:83], v[140:143], v[204:207]\n\t" \
  "v_mfma_f32_16x16x32_bf16 v[212:215], v[80:83], v[68:71], v[212:215]\n\t" \
  "v_mfma_f32_16x16x32_bf16 v[212:215], v[124:127], v[76:79], v[212:215]\n\t" \
  "v_mfma_f32_16x16x32_bf16 v[216:219], v[128:131], v[96:99], v[216:219]\n\t" \
  "v_mfma_f32_16x16x32_bf16 v[216:219], v[120:123], v[132:135], v[216:219]\n\t" \
  "v_mfma_f32_16x16x32_bf16 v[224:227], v[136:139], v[132:135], v[224:227]\n\t" \
  "v_mfma_f32_16x16x32_bf16 v[224:227], v[144:147], v[96:99], v[224:227]\n\t" \
  "v_mfma_f32_16x16x32_bf16 v[232:235], v[72:75], v[96:99], v[232:235]\n\t" \
  "global_store_dwordx4 v2, v[42:45], %24 offset:0 sc1\n\t" \
  "v_mfma_f32_16x16x32_bf16 v[232:235], v[140:143], v[132:135], v[232:235]\n\t" \
  "s_nop 0\n\t" \
  "v_mfma_f32_16x16x32_bf16 v[42:45], v[26:29], v[18:21], 0\n\t" \
  "v_mfma_f32_16x16x32_bf16 v[42:45], v[30:33], v[22:25], v[42:45]\n\t" \
  "v_mfma_f32_16x16x32_bf16 v[46:49], v[30:33], v[18:21], 0\n\t" \
  "v_mfma_f32_16x16x32_bf16 v[46:49], v[34:37], v[22:25], v[46:49]\n\t" \
  "v_mfma_f32_16x16x32_bf16 v[240:243], v[68:71], v[132:135], v[240:243]\n\t" \
  "v_mfma_f32_16x16x32_bf16 v[240:243], v[76:79], v[96:99], v[240:243]\n\t" \
  "v_lshlrev_b32 v50, 16, v38\n\t" \
  "v_lshlrev_b32 v51, 16, v40\n\t" \
  "v_mfma_f32_16x16x32_bf16 v[248:251], v[108:111], v[96:99], v[248:251]\n\t" \
  "s_nop 0\n\t" \
  "v_mul_f32 v42, v42, v50\n\t" \
  "s_nop 0\n\t" \
  "v_mul_f32 v46, v46, v51\n\t" \
  "v_mfma_f32_16x16x32_bf16 v[248:251], v[148:151], v[132:135], v[248:251]\n\t" \
  "v_cvt_pk_bf16_f32 v42, v42, v46\n\t" \
  "v_and_b32 v50, 0xffff0000, v38\n\t" \
  "v_mfma_f32_16x16x32_bf16 v[252:255], v[108:111], v[64:67], v[252:255]\n\t" \
  "v_and_b32 v51, 0xffff0000, v40\n\t" \
  "v_mul_f32 v43, v43, v50\n\t" \
  "s_waitcnt lgkmcnt(0)\n\t" \
  "v_mfma_f32_16x16x32_bf16 v[252:255], v[148:151], v[104:107], v[252:255]\n\t" \
  "v_mul_f32 v47, v47, v51\n\t" \
  "v_cvt_pk_bf16_f32 v43, v43, v47\n\t" \
  "v_mfma_f32_16x16x32_bf16 v[220:223], v[120:123], v[104:107], v[220:223]\n\t" \
  "v_lshlrev_b32 v50, 16, v39\n\t" \
  "v_lshlrev_b32 v51, 16, v41\n\t" \
  "v_mfma_f32_16x16x32_bf16 v[220:223], v[128:131], v[64:67], v[220:223]\n\t" \
  "v_mul_f32 v44, v44, v50\n\t" \
  "v_mul_f32 v48, v48, v51\n\t" \
  "s_barrier\n\t" \
  "v_mfma_f32_16x16x32_bf16 v[228:231], v[144:147], v[64:67], v[228:231]\n\t" \
  "ds_read_b64_tr_b16 v[100:101], v54 offset:26624\n\t" \
  "v_cvt_pk_bf16_f32 v44, v44, v48\n\t" \
  "v_mfma_f32_16x16x32_bf16 v[228:231], v[136:139], v[104:107], v[228:231]\n\t" \
  "ds_read_b64_tr_b16 v[102:103], v54 offset:27136\n\t" \
  "v_and_b32 v50, 0xffff0000, v39\n\t" \
  "v_mfma_f32_16x16x32_bf16 v[236:239], v[140:143], v[104:107], v[236:239]\n\t" \
  "ds_read_b128 v[88:91], v52 offset:10240\n\t" \
  "v_and_b32 v51, 0xffff0000, v41\n\t" \
  "v_mfma_f32_16x16x32_bf16 v[236:239], v[72:75], v[64:67], v[236:239]\n\t" \
  "ds_read_b64_tr_b16 v[80:81], v54 offset:30720\n\t" \
  "v_mul_f32 v45, v45, v50\n\t" \
  "v_mfma_f32_16x16x32_bf16 v[244:247], v[76:79], v[64:67], v[244:247]\n\t" \
  "ds_read_b64_tr_b16 v[82:83], v54 offset:31232\n\t" \
  "v_mul_f32 v49, v49, v51\n\t" \
  "v_mfma_f32_16x16x32_bf16 v[244:247], v[68:71], v[104:107], v[244:247]\n\t" \
  "ds_read_b128 v[124:127], v53 offset:10240\n\t" \
  "v_cvt_pk_bf16_f32 v45, v45, v49\n\t" \
  "v_mfma_f32_16x16x32_bf16 v[152:155], v[92:95], v[128:131], v[152:155]\n\t" \
  "ds_read_b128 v[96:99], v53 offset:12288\n\t" \
  "v_mfma_f32_16x16x32_bf16 v[152:155], v[112:115], v[120:123], v[152:155]\n\t" \
  "ds_read_b128 v[132:135], v52 offset:12288\n\t" \
  "v_mfma_f32_16x16x32_bf16 v[160:163], v[112:115], v[136:139], v[160:163]\n\t" \
  "ds_read_b128 v[108:111], v52 offset:14336\n\t" \
  "v_mfma_f32_16x16x32_bf16 v[160:163], v[92:95], v[144:147], v[160:163]\n\t" \
  "ds_read_b128 v[148:151], v53 offset:14336\n\t" \
  "v_mfma_f32_16x16x32_bf16 v[168:171], v[92:95], v[72:75], v[168:171]\n\t" \
  "ds_read_b128 v[64:67], v53 offset:16384\n\t" \
  "v_mfma_f32_16x16x32_bf16 v[168:171], v[112:115], v[140:143], v[168:171]\n\t" \
  "ds_read_b128 v[104:107], v52 offset:16384\n\t" \
  "v_mfma_f32_16x16x32_bf16 v[176:179], v[112:115], v[68:71], v[176:179]\n\t" \
  "ds_read_b64_tr_b16 v[112:113], v55 offset:26624\n\t" \
  "v_mfma_f32_16x16x32_bf16 v[176:179], v[92:95], v[76:79], v[176:179]\n\t" \
  "ds_read_b64_tr_b16 v[114:115], v55 offset:27136\n\t" \
  "v_mfma_f32_16x16x32_bf16 v[156:159], v[84:87], v[128:131], v[156:159]\n\t" \
  "ds_read_b64_tr_b16 v[92:93], v55 offset:30720\n\t" \
  "v_mfma_f32_16x16x32_bf16 v[156:159], v[116:119], v[120:123], v[156:159]\n\t" \
  "ds_read_b64_tr_b16 v[94:95], v55 offset:31232\n\t" \
  "v_mfma_f32_16x16x32_bf16 v[164:167], v[116:119], v[136:139], v[164:167]\n\t" \
  "ds_read_b64_tr_b16 v[128:129], v54 offset:34816\n\t" \
  "v_mfma_f32_16x16x32_bf16 v[164:167], v[84:87], v[144:147], v[164:167]\n\t" \
  "ds_read_b64_tr_b16 v[130:131], v54 offset:35328\n\t" \
  "v_mfma_f32_16x16x32_bf16 v[172:175], v[84:87], v[72:75], v[172:175]\n\t" \
  "ds_read_b64_tr_b16 v[120:121], v54 offset:38912\n\t" \
  "v_mfma_f32_16x16x32_bf16 v[172:175], v[116:119], v[140:143], v[172:175]\n\t" \
  "ds_read_b64_tr_b16 v[122:123], v54 offset:39424\n\t" \
  "v_mfma_f32_16x16x32_bf16 v[180:183], v[116:119], v[68:71], v[180:183]\n\t" \
  "ds_read_b128 v[136:139], v52 offset:0\n\t" \
  "v_mfma_f32_16x16x32_bf16 v[180:183], v[84:87], v[76:79], v[180:183]\n\t" \
  "ds_read_b128 v[144:147], v53 offset:0\n\t" \
  "s_waitcnt lgkmcnt(7)\n\t" \
  "v_mfma_f32_16x16x32_bf16 v[184:187], v[100:103], v[88:91], v[184:187]\n\t" \
  "ds_read_b64_tr_b16 v[72:73], v55 offset:34816\n\t" \
  "v_mfma_f32_16x16x32_bf16 v[184:187], v[80:83], v[124:127], v[184:187]\n\t" \
  "ds_read_b64_tr_b16 v[74:75], v55 offset:35328\n\t" \
  "v_mfma_f32_16x16x32_bf16 v[192:195], v[80:83], v[96:99], v[192:195]\n\t" \
  "ds_read_b64_tr_b16 v[140:141], v55 offset:38912\n\t" \
  "v_mfma_f32_16x16x32_bf16 v[192:195], v[100:103], v[132:135], v[192:195]\n\t" \
  "ds_read_b64_tr_b16 v[142:143], v55 offset:39424\n\t" \
  "v_mfma_f32_16x16x32_bf16 v[200:203], v[100:103], v[108:111], v[200:203]\n\t" \
  "ds_read_b64_tr_b16 v[68:69], v54 offset:18432\n\t" \
  "v_mfma_f32_16x16x32_bf16 v[200:203], v[80:83], v[148:151], v[200:203]\n\t" \
  "ds_read_b64_tr_b16 v[70:71], v54 offset:18944\n\t" \
  "v_mfma_f32_16x16x32_bf16 v[208:211], v[80:83], v[64:67], v[208:211]\n\t" \
  "ds_read_b64_tr_b16 v[116:117], v54 offset:22528\n\t" \
  "v_mfma_f32_16x16x32_bf16 v[208:211], v[100:103], v[104:107], v[208:211]\n\t" \
  "ds_read_b64_tr_b16 v[118:119], v54 offset:23040\n\t" \
  "v_mfma_f32_16x16x32_bf16 v[188:191], v[112:115], v[88:91], v[188:191]\n\t" \
  "ds_read_b64_tr_b16 v[76:77], v55 offset:18432\n\t" \
  "s_waitcnt lgkmcnt(7)\n\t" \
  "v_mfma_f32_16x16x32_bf16 v[188:191], v[92:95], v[124:127], v[188:191]\n\t" \
  "ds_read_b64_tr_b16 v[78:79], v55 offset:18944\n\t" \
  "v_mfma_f32_16x16x32_bf16 v[196:199], v[92:95], v[96:99], v[196:199]\n\t" \
  "ds_read_b64_tr_b16 v[84:85], v55 offset:22528\n\t" \
  "v_mfma_f32_16x16x32_bf16 v[196:199], v[112:115], v[132:135], v[196:199]\n\t" \
  "ds_read_b64_tr_b16 v[86:87], v55 offset:23040\n\t" \
  "v_mfma_f32_16x16x32_bf16 v[204:207], v[112:115], v[108:111], v[204:207]\n\t" \
  "v_mfma_f32_16x16x32_bf16 v[204:207], v[92:95], v[148:151], v[204:207]\n\t" \
  "v_mfma_f32_16x16x32_bf16 v[212:215], v[92:95], v[64:67], v[212:215]\n\t" \
  "v_mfma_f32_16x16x32_bf16 v[212:215], v[112:115], v[104:107], v[212:215]\n\t" \
  "v_mfma_f32_16x16x32_bf16 v[216:219], v[88:91], v[128:131], v[216:219]\n\t" \
  "v_mfma_f32_16x16x32_bf16 v[216:219], v[124:127], v[120:123], v[216:219]\n\t" \
  "v_mfma_f32_16x16x32_bf16 v[224:227], v[96:99], v[120:123], v[224:227]\n\t" \
  "v_mfma_f32_16x16x32_bf16 v[224:227], v[132:135], v[128:131], v[224:227]\n\t" \
  "v_mfma_f32_16x16x32_bf16 v[232:235], v[108:111], v[128:131], v[232:235]\n\t" \
  "global_store_dwordx4 v2, v[42:45], %24 offset:1024 sc1\n\t" \
  "v_mfma_f32_16x16x32_bf16 v[232:235], v[148:151], v[120:123], v[232:235]\n\t" \
  "v_mfma_f32_16x16x32_bf16 v[240:243], v[64:67], v[120:123], v[240:243]\n\t" \
  "v_mfma_f32_16x16x32_bf16 v[240:243], v[104:107], v[128:131], v[240:243]\n\t" \
  "v_mov_b32 v30, 0xff61b1e6\n\t" \
  "v_bfe_i32 v34, v3, 0, 1\n\t" \
  "v_mfma_f32_16x16x32_bf16 v[248:251], v[136:139], v[128:131], v[248:251]\n\t" \
  "v_and_b32 v34, v30, v34\n\t" \
  "v_bfe_i32 v38, v3, 4, 1\n\t" \
  "v_mfma_f32_16x16x32_bf16 v[248:251], v[144:147], v[120:123], v[248:251]\n\t" \
  "v_and_b32 v38, v30, v38\n\t" \
  "v_bfe_i32 v42, v3, 8, 1\n\t" \
  "v_mfma_f32_16x16x32_bf16 v[252:255], v[136:139], v[72:75], v[252:255]\n\t" \
  "v_bfi_b32 v42, v42, v30, %15\n\t" \
  "v_mov_b32 v48, 0x3f803f80\n\t" \
  "s_waitcnt lgkmcnt(0)\n\t" \
  "v_mfma_f32_16x16x32_bf16 v[252:255], v[144:147], v[140:143], v[252:255]\n\t" \
  "v_bfe_i32 v35, v3, 1, 1\n\t" \
  "v_and_b32 v35, v30, v35\n\t" \
  "v_mfma_f32_16x16x32_bf16 v[220:223], v[124:127], v[140:143], v[220:223]\n\t" \
  "v_bfe_i32 v39, v3, 5, 1\n\t" \
  "v_and_b32 v39, v30, v39\n\t" \
  "v_mfma_f32_16x16x32_bf16 v[220:223], v[88:91], v[72:75], v[220:223]\n\t" \
  "v_bfe_i32 v43, v3, 9, 1\n\t" \
  "v_bfi_b32 v43, v43, v30, %15\n\t" \
  "s_barrier\n\t" \
  "v_mfma_f32_16x16x32_bf16 v[228:231], v[132:135], v[72:75], v[228:231]\n\t" \
  "ds_read_b64_tr_b16 v[80:81], v58 offset:26624\n\t" \
  "v_mov_b32 v49, 0x3f803f80\n\t" \
  "v_mfma_f32_16x16x32_bf16 v[228:231], v[96:99], v[140:143], v[228:231]\n\t" \
  "ds_read_b64_tr_b16 v[82:83], v58 offset:27136\n\t" \
  "v_bfe_i32 v36, v3, 2, 1\n\t" \
  "v_mfma_f32_16x16x32_bf16 v[236:239], v[148:151], v[140:143], v[236:239]\n\t" \
  "ds_read_b128 v[100:103], v56 offset:10240\n\t" \
  "v_and_b32 v36, v30, v36\n\t" \
  "v_mfma_f32_16x16x32_bf16 v[236:239], v[108:111], v[72:75], v[236:239]\n\t" \
  "ds_read_b64_tr_b16 v[92:93], v58 offset:30720\n\t" \
  "v_bfe_i32 v40, v3, 6, 1\n\t" \
  "v_mfma_f32_16x16x32_bf16 v[244:247], v[104:107], v[72:75], v[244:247]\n\t" \
  "ds_read_b64_tr_b16 v[94:95], v58 offset:31232\n\t" \
  "v_and_b32 v40, v30, v40\n\t" \
  "v_mfma_f32_16x16x32_bf16 v[244:247], v[64:67], v[140:143], v[244:247]\n\t" \
  "ds_read_b128 v[112:115], v57 offset:10240\n\t" \
  "v_bfe_i32 v44, v3, 10, 1\n\t" \
  "v_mfma_f32_16x16x32_bf16 v[152:155], v[68:71], v[88:91], v[152:155]\n\t" \
  "ds_read_b128 v[128:131], v57 offset:12288\n\t" \
  "v_bfi_b32 v44, v44, v30, %15\n\t" \
  "v_mfma_f32_16x16x32_bf16 v[152:155], v[116:119], v[124:127], v[152:155]\n\t" \
  "ds_read_b128 v[120:123], v56 offset:12288\n\t" \
  "v_mov_b32 v50, 0x3f803f80\n\t" \
  "v_mfma_f32_16x16x32_bf16 v[160:163], v[116:119], v[96:99], v[160:163]\n\t" \
  "ds_read_b128 v[136:139], v56 offset:14336\n\t" \
  "v_bfe_i32 v37, v3, 3, 1\n\t" \
  "v_mfma_f32_16x16x32_bf16 v[160:163], v[68:71], v[132:135], v[160:163]\n\t" \
  "ds_read_b128 v[144:147], v57 offset:14336\n\t" \
  "v_and_b32 v37, v30, v37\n\t" \
  "v_mfma_f32_16x16x32_bf16 v[168:171], v[68:71], v[108:111], v[168:171]\n\t" \
  "ds_read_b128 v[72:75], v57 offset:16384\n\t" \
  "v_bfe_i32 v41, v3, 7, 1\n\t" \
  "v_mfma_f32_16x16x32_bf16 v[168:171], v[116:119], v[148:151], v[168:171]\n\t" \
  "ds_read_b128 v[140:143], v56 offset:16384\n\t" \
  "v_and_b32 v41, v30, v41\n\t" \
  "v_mfma_f32_16x16x32_bf16 v[176:179], v[116:119], v[64:67], v[176:179]\n\t" \
  "ds_read_b64_tr_b16 v[116:117], v59 offset:26624\n\t" \
  "v_bfe_i32 v45, v3, 11, 1\n\t" \
  "v_mfma_f32_16x16x32_bf16 v[176:179], v[68:71], v[104:107], v[176:179]\n\t" \
  "ds_read_b64_tr_b16 v[118:119], v59 offset:27136\n\t" \
  "v_bfi_b32 v45, v45, v30, %15\n\t" \
  "v_mfma_f32_16x16x32_bf16 v[156:159], v[76:79], v[88:91], v[156:159]\n\t" \
  "ds_read_b64_tr_b16 v[68:69], v59 offset:30720\n\t" \
  "v_mov_b32 v51, 0x3f803f80\n\t" \
  "v_mfma_f32_16x16x32_bf16 v[156:159], v[84:87], v[124:127], v[156:159]\n\t" \
  "ds_read_b64_tr_b16 v[70:71], v59 offset:31232\n\t" \
  "v_bfe_i32 v46, v3, 12, 1\n\t" \
  "v_mfma_f32_16x16x32_bf16 v[164:167], v[84:87], v[96:99], v[164:167]\n\t" \
  "ds_read_b64_tr_b16 v[88:89], v58 offset:34816\n\t" \
  "v_bfe_i32 v31, v3, 13, 1\n\t" \
  "v_mfma_f32_16x16x32_bf16 v[164:167], v[76:79], v[132:135], v[164:167]\n\t" \
  "ds_read_b64_tr_b16 v[90:91], v58 offset:35328\n\t" \
  "v_perm_b32 v46, v31, v46, vcc_hi\n\t" \
  "v_mfma_f32_16x16x32_bf16 v[172:175], v[76:79], v[108:111], v[172:175]\n\t" \
  "ds_read_b64_tr_b16 v[124:125], v58 offset:38912\n\t" \
  "v_bfe_i32 v47, v3, 14, 1\n\t" \
  "v_mfma_f32_16x16x32_bf16 v[172:175], v[84:87], v[148:151], v[172:175]\n\t" \
  "ds_read_b64_tr_b16 v[126:127], v58 offset:39424\n\t" \
  "v_bfe_i32 v31, v3, 15, 1\n\t" \
  "v_mfma_f32_16x16x32_bf16 v[180:183], v[84:87], v[64:67], v[180:183]\n\t" \
  "ds_read_b128 v[96:99], v56 offset:0\n\t" \
  "v_perm_b32 v47, v31, v47, vcc_hi\n\t" \
  "v_mfma_f32_16x16x32_bf16 v[180:183], v[76:79], v[104:107], v[180:183]\n\t" \
  "ds_read_b128 v[132:135], v57 offset:0\n\t" \
  "v_add_u32 v30, 18432, v4\n\t" \
  "s_waitcnt lgkmcnt(7)\n\t" \
  "v_mfma_f32_16x16x32_bf16 v[184:187], v[80:83], v[100:103], v[184:187]\n\t" \
  "ds_read_b64_tr_b16 v[108:109], v59 offset:34816\n\t" \
  "v_xor_b32 v31, 1024, v30\n\t" \
  "v_mfma_f32_16x16x32_bf16 v[184:187], v[92:95], v[112:115], v[184:187]\n\t" \
  "ds_read_b64_tr_b16 v[110:111], v59 offset:35328\n\t" \
  "v_mov_b32 v32, %12\n\t" \
  "v_mfma_f32_16x16x32_bf16 v[192:195], v[92:95], v[128:131], v[192:195]\n\t" \
  "ds_read_b64_tr_b16 v[148:149], v59 offset:38912\n\t" \
  "v_xor_b32 v33, 4, v32\n\t" \
  "v_mfma_f32_16x16x32_bf16 v[192:195], v[80:83], v[120:123], v[192:195]\n\t" \
  "ds_read_b64_tr_b16 v[150:151], v59 offset:39424\n\t" \
  "v_mov_b32 v0, 1\n\t" \
  "v_mfma_f32_16x16x32_bf16 v[200:203], v[80:83], v[136:139], v[200:203]\n\t" \
  "ds_read_b64_tr_b16 v[64:65], v58 offset:18432\n\t" \
  "v_and_b32 v4, 1023, v4\n\t" \
  "v_mfma_f32_16x16x32_bf16 v[200:203], v[92:95], v[144:147], v[200:203]\n\t" \
  "ds_read_b64_tr_b16 v[66:67], v58 offset:18944\n\t" \
  "v_add_u32 v1, 16384, v4\n\t" \
  "v_mfma_f32_16x16x32_bf16 v[208:211], v[92:95], v[72:75], v[208:211]\n\t" \
  "ds_read_b64_tr_b16 v[84:85], v58 offset:22528\n\t" \
  "v_mfma_f32_16x16x32_bf16 v[208:211], v[80:83], v[140:143], v[208:211]\n\t" \
  "ds_read_b64_tr_b16 v[86:87], v58 offset:23040\n\t" \
  "v_mfma_f32_16x16x32_bf16 v[188:191], v[116:119], v[100:103], v[188:191]\n\t" \
  "ds_read_b64_tr_b16 v[76:77], v59 offset:18432\n\t" \
  "s_waitcnt lgkmcnt(7)\n\t" \
  "v_mfma_f32_16x16x32_bf16 v[188:191], v[68:71], v[112:115], v[188:191]\n\t" \
  "ds_read_b64_tr_b16 v[78:79], v59 offset:18944\n\t" \
  "v_mfma_f32_16x16x32_bf16 v[196:199], v[68:71], v[128:131], v[196:199]\n\t" \
  "ds_read_b64_tr_b16 v[104:105], v59 offset:22528\n\t" \
  "v_mfma_f32_16x16x32_bf16 v[196:199], v[116:119], v[120:123], v[196:199]\n\t" \
  "ds_read_b64_tr_b16 v[106:107], v59 offset:23040\n\t" \
  "v_mfma_f32_16x16x32_bf16 v[204:207], v[116:119], v[136:139], v[204:207]\n\t" \
  "v_mfma_f32_16x16x32_bf16 v[204:207], v[68:71], v[144:147], v[204:207]\n\t" \
  "v_mfma_f32_16x16x32_bf16 v[212:215], v[68:71], v[72:75], v[212:215]\n\t" \
  "v_mfma_f32_16x16x32_bf16 v[212:215], v[116:119], v[140:143], v[212:215]\n\t" \
  "v_mfma_f32_16x16x32_bf16 v[216:219], v[100:103], v[88:91], v[216:219]\n\t" \
  "v_mfma_f32_16x16x32_bf16 v[216:219], v[112:115], v[124:127], v[216:219]\n\t" \
  "v_mfma_f32_16x16x32_bf16 v[224:227], v[128:131], v[124:127], v[224:227]\n\t" \
  "v_mfma_f32_16x16x32_bf16 v[224:227], v[120:123], v[88:91], v[224:227]\n\t" \
  "v_mfma_f32_16x16x32_bf16 v[232:235], v[136:139], v[88:91], v[232:235]\n\t" \
  "v_mfma_f32_16x16x32_bf16 v[232:235], v[144:147], v[124:127], v[232:235]\n\t" \
  "v_mfma_f32_16x16x32_bf16 v[240:243], v[72:75], v[124:127], v[240:243]\n\t" \
  "v_mfma_f32_16x16x32_bf16 v[240:243], v[140:143], v[88:91], v[240:243]\n\t" \
  "v_mfma_f32_16x16x32_bf16 v[248:251], v[96:99], v[88:91], v[248:251]\n\t" \
  "v_mfma_f32_16x16x32_bf16 v[248:251], v[132:135], v[124:127], v[248:251]\n\t" \
  "v_mfma_f32_16x16x32_bf16 v[252:255], v[96:99], v[108:111], v[252:255]\n\t" \
  "s_waitcnt lgkmcnt(0)\n\t" \
  "v_mfma_f32_16x16x32_bf16 v[252:255], v[132:135], v[148:151], v[252:255]\n\t" \
  "v_mfma_f32_16x16x32_bf16 v[220:223], v[112:115], v[148:151], v[220:223]\n\t" \
  "v_mfma_f32_16x16x32_bf16 v[220:223], v[100:103], v[108:111], v[220:223]\n\t" \
  "s_barrier\n\t" \
  "v_mfma_f32_16x16x32_bf16 v[228:231], v[120:123], v[108:111], v[228:231]\n\t" \
  "ds_read_b64_tr_b16 v[92:93], v62 offset:26624\n\t" \
  "v_mfma_f32_16x16x32_bf16 v[228:231], v[128:131], v[148:151], v[228:231]\n\t" \
  "ds_read_b64_tr_b16 v[94:95], v62 offset:27136\n\t" \
  "v_mfma_f32_16x16x32_bf16 v[236:239], v[144:147], v[148:151], v[236:239]\n\t" \
  "ds_read_b128 v[80:83], v60 offset:10240\n\t" \
  "v_mfma_f32_16x16x32_bf16 v[236:239], v[136:139], v[108:111], v[236:239]\n\t" \
  "ds_read_b64_tr_b16 v[68:69], v62 offset:30720\n\t" \
  "v_mfma_f32_16x16x32_bf16 v[244:247], v[140:143], v[108:111], v[244:247]\n\t" \
  "ds_read_b64_tr_b16 v[70:71], v62 offset:31232\n\t" \
  "v_mfma_f32_16x16x32_bf16 v[244:247], v[72:75], v[148:151], v[244:247]\n\t" \
  "ds_read_b128 v[116:119], v61 offset:10240\n\t" \
  "v_mfma_f32_16x16x32_bf16 v[152:155], v[64:67], v[100:103], v[152:155]\n\t" \
  "ds_read_b128 v[88:91], v61 offset:12288\n\t" \
  "v_mfma_f32_16x16x32_bf16 v[152:155], v[84:87], v[112:115], v[152:155]\n\t" \
  "ds_read_b128 v[124:127], v60 offset:12288\n\t" \
  "v_mfma_f32_16x16x32_bf16 v[160:163], v[84:87], v[128:131], v[160:163]\n\t" \
  "ds_read_b128 v[96:99], v60 offset:14336\n\t" \
  "v_mfma_f32_16x16x32_bf16 v[160:163], v[64:67], v[120:123], v[160:163]\n\t" \
  "ds_read_b128 v[132:135], v61 offset:14336\n\t" \
  "v_mfma_f32_16x16x32_bf16 v[168:171], v[64:67], v[136:139], v[168:171]\n\t" \
  "ds_read_b128 v[108:111], v61 offset:16384\n\t" \
  "v_mfma_f32_16x16x32_bf16 v[168:171], v[84:87], v[144:147], v[168:171]\n\t" \
  "ds_read_b128 v[148:151], v60 offset:16384\n\t" \
  "v_mfma_f32_16x16x32_bf16 v[176:179], v[84:87], v[72:75], v[176:179]\n\t" \
  "ds_read_b64_tr_b16 v[84:85], v63 offset:26624\n\t" \
  "v_mfma_f32_16x16x32_bf16 v[176:179], v[64:67], v[140:143], v[176:179]\n\t" \
  "ds_read_b64_tr_b16 v[86:87], v63 offset:27136\n\t" \
  "v_mfma_f32_16x16x32_bf16 v[156:159], v[76:79], v[100:103], v[156:159]\n\t" \
  "ds_read_b64_tr_b16 v[64:65], v63 offset:30720\n\t" \
  "v_mfma_f32_16x16x32_bf16 v[156:159], v[104:107], v[112:115], v[156:159]\n\t" \
  "ds_read_b64_tr_b16 v[66:67], v63 offset:31232\n\t" \
  "v_mfma_f32_16x16x32_bf16 v[164:167], v[104:107], v[128:131], v[164:167]\n\t" \
  "ds_read_b64_tr_b16 v[100:101], v62 offset:34816\n\t" \
  "v_mfma_f32_16x16x32_bf16 v[164:167], v[76:79], v[120:123], v[164:167]\n\t" \
  "ds_read_b64_tr_b16 v[102:103], v62 offset:35328\n\t" \
  "v_mfma_f32_16x16x32_bf16 v[172:175], v[76:79], v[136:139], v[172:175]\n\t" \
  "ds_read_b64_tr_b16 v[112:113], v62 offset:38912\n\t" \
  "v_mfma_f32_16x16x32_bf16 v[172:175], v[104:107], v[144:147], v[172:175]\n\t" \
  "ds_read_b64_tr_b16 v[114:115], v62 offset:39424\n\t" \
  "v_mfma_f32_16x16x32_bf16 v[180:183], v[104:107], v[72:75], v[180:183]\n\t" \
  "ds_read_b128 v[128:131], v60 offset:0\n\t" \
  "v_mfma_f32_16x16x32_bf16 v[180:183], v[76:79], v[140:143], v[180:183]\n\t" \
  "ds_read_b128 v[120:123], v61 offset:0\n\t" \
  "s_waitcnt lgkmcnt(7)\n\t" \
  "v_mfma_f32_16x16x32_bf16 v[184:187], v[92:95], v[80:83], v[184:187]\n\t" \
  "ds_read_b64_tr_b16 v[136:137], v63 offset:34816\n\t" \
  "v_mfma_f32_16x16x32_bf16 v[184:187], v[68:71], v[116:119], v[184:187]\n\t" \
  "ds_read_b64_tr_b16 v[138:139], v63 offset:35328\n\t" \
  "v_mfma_f32_16x16x32_bf16 v[192:195], v[68:71], v[88:91], v[192:195]\n\t" \
  "ds_read_b64_tr_b16 v[144:145], v63 offset:38912\n\t" \
  "v_mfma_f32_16x16x32_bf16 v[192:195], v[92:95], v[124:127], v[192:195]\n\t" \
  "ds_read_b64_tr_b16 v[146:147], v63 offset:39424\n\t" \
  "v_mfma_f32_16x16x32_bf16 v[200:203], v[92:95], v[96:99], v[200:203]\n\t" \
  "ds_read_b64_tr_b16 v[72:73], v62 offset:18432\n\t" \
  "v_mfma_f32_16x16x32_bf16 v[200:203], v[68:71], v[132:135], v[200:203]\n\t" \
  "ds_read_b64_tr_b16 v[74:75], v62 offset:18944\n\t" \
  "v_mfma_f32_16x16x32_bf16 v[208:211], v[68:71], v[108:111], v[208:211]\n\t" \
  "ds_read_b64_tr_b16 v[104:105], v62 offset:22528\n\t" \
  "v_mfma_f32_16x16x32_bf16 v[208:211], v[92:95], v[148:151], v[208:211]\n\t" \
  "ds_read_b64_tr_b16 v[106:107], v62 offset:23040\n\t" \
  "v_mfma_f32_16x16x32_bf16 v[188:191], v[84:87], v[80:83], v[188:191]\n\t" \
  "ds_read_b64_tr_b16 v[76:77], v63 offset:18432\n\t" \
  "s_waitcnt lgkmcnt(7)\n\t" \
  "v_mfma_f32_16x16x32_bf16 v[188:191], v[64:67], v[116:119], v[188:191]\n\t" \
  "ds_read_b64_tr_b16 v[78:79], v63 offset:18944\n\t" \
  "v_mfma_f32_16x16x32_bf16 v[196:199], v[64:67], v[88:91], v[196:199]\n\t" \
  "ds_read_b64_tr_b16 v[140:141], v63 offset:22528\n\t" \
  "v_mfma_f32_16x16x32_bf16 v[196:199], v[84:87], v[124:127], v[196:199]\n\t" \
  "ds_read_b64_tr_b16 v[142:143], v63 offset:23040\n\t" \
  "v_mfma_f32_16x16x32_bf16 v[204:207], v[84:87], v[96:99], v[204:207]\n\t" \
  "v_mfma_f32_16x16x32_bf16 v[204:207], v[64:67], v[132:135], v[204:207]\n\t" \
  "v_mfma_f32_16x16x32_bf16 v[212:215], v[64:67], v[108:111], v[212:215]\n\t" \
  "v_mfma_f32_16x16x32_bf16 v[212:215], v[84:87], v[148:151], v[212:215]\n\t" \
  "v_mfma_f32_16x16x32_bf16 v[216:219], v[80:83], v[100:103], v[216:219]\n\t" \
  "501:\n\t" \
  "s_cbranch_execz 505f\n\t" \
  "v_mfma_f32_16x16x32_bf16 v[216:219], v[116:119], v[112:115], v[216:219]\n\t" \
  "v_mfma_f32_16x16x32_bf16 v[224:227], v[88:91], v[112:115], v[224:227]\n\t" \
  "v_mfma_f32_16x16x32_bf16 v[224:227], v[124:127], v[100:103], v[224:227]\n\t" \
  "v_mfma_f32_16x16x32_bf16 v[232:235], v[96:99], v[100:103], v[232:235]\n\t" \
  "v_mfma_f32_16x16x32_bf16 v[232:235], v[132:135], v[112:115], v[232:235]\n\t" \
  "v_mfma_f32_16x16x32_bf16 v[240:243], v[108:111], v[112:115], v[240:243]\n\t" \
  "v_mfma_f32_16x16x32_bf16 v[240:243], v[148:151], v[100:103], v[240:243]\n\t" \
  "v_mfma_f32_16x16x32_bf16 v[248:251], v[128:131], v[100:103], v[248:251]\n\t" \
  "v_mfma_f32_16x16x32_bf16 v[248:251], v[120:123], v[112:115], v[248:251]\n\t" \
  "v_mfma_f32_16x16x32_bf16 v[252:255], v[128:131], v[136:139], v[252:255]\n\t" \
  "s_waitcnt lgkmcnt(0)\n\t" \
  "v_mfma_f32_16x16x32_bf16 v[252:255], v[120:123], v[144:147], v[252:255]\n\t" \
  "v_mfma_f32_16x16x32_bf16 v[220:223], v[116:119], v[144:147], v[220:223]\n\t" \
  "v_mfma_f32_16x16x32_bf16 v[220:223], v[80:83], v[136:139], v[220:223]\n\t" \
  "s_waitcnt vmcnt(0)\n\t" \
  "s_barrier\n\t" \
  "v_mfma_f32_16x16x32_bf16 v[228:231], v[124:127], v[136:139], v[228:231]\n\t" \
  "ds_read_b64_tr_b16 v[68:69], v54 offset:26624\n\t" \
  "v_mfma_f32_16x16x32_bf16 v[228:231], v[88:91], v[144:147], v[228:231]\n\t" \
  "ds_read_b64_tr_b16 v[70:71], v54 offset:27136\n\t" \
  "v_mfma_f32_16x16x32_bf16 v[236:239], v[132:135], v[144:147], v[236:239]\n\t" \
  "ds_read_b128 v[92:95], v52 offset:10240\n\t" \
  "v_mfma_f32_16x16x32_bf16 v[236:239], v[96:99], v[136:139], v[236:239]\n\t" \
  "ds_read_b64_tr_b16 v[64:65], v54 offset:30720\n\t" \
  "v_mfma_f32_16x16x32_bf16 v[244:247], v[148:151], v[136:139], v[244:247]\n\t" \
  "ds_read_b64_tr_b16 v[66:67], v54 offset:31232\n\t" \
  "v_mfma_f32_16x16x32_bf16 v[244:247], v[108:111], v[144:147], v[244:247]\n\t" \
  "ds_read_b128 v[84:87], v53 offset:10240\n\t" \
  "v_mfma_f32_16x16x32_bf16 v[152:155], v[72:75], v[80:83], v[152:155]\n\t" \
  "ds_read_b128 v[100:103], v53 offset:12288\n\t" \
  "v_mfma_f32_16x16x32_bf16 v[152:155], v[104:107], v[116:119], v[152:155]\n\t" \
  "ds_read_b128 v[112:115], v52 offset:12288\n\t" \
  "v_mfma_f32_16x16x32_bf16 v[160:163], v[104:107], v[88:91], v[160:163]\n\t" \
  "ds_read_b128 v[128:131], v52 offset:14336\n\t" \
  "v_mfma_f32_16x16x32_bf16 v[160:163], v[72:75], v[124:127], v[160:163]\n\t" \
  "ds_read_b128 v[120:123], v53 offset:14336\n\t" \
  "v_mfma_f32_16x16x32_bf16 v[168:171], v[72:75], v[96:99], v[168:171]\n\t" \
  "ds_read_b128 v[136:139], v53 offset:16384\n\t" \
  "v_mfma_f32_16x16x32_bf16 v[168:171], v[104:107], v[132:135], v[168:171]\n\t" \
  "ds_read_b128 v[144:147], v52 offset:16384\n\t" \
  "v_mfma_f32_16x16x32_bf16 v[176:179], v[104:107], v[108:111], v[176:179]\n\t" \
  "ds_read_b64_tr_b16 v[104:105], v55 offset:26624\n\t" \
  "v_mfma_f32_16x16x32_bf16 v[176:179], v[72:75], v[148:151], v[176:179]\n\t" \
  "ds_read_b64_tr_b16 v[106:107], v55 offset:27136\n\t" \
  "v_mfma_f32_16x16x32_bf16 v[156:159], v[76:79], v[80:83], v[156:159]\n\t" \
  "ds_read_b64_tr_b16 v[72:73], v55 offset:30720\n\t" \
  "v_mfma_f32_16x16x32_bf16 v[156:159], v[140:143], v[116:119], v[156:159]\n\t" \
  "ds_read_b64_tr_b16 v[74:75], v55 offset:31232\n\t" \
  "v_mfma_f32_16x16x32_bf16 v[164:167], v[140:143], v[88:91], v[164:167]\n\t" \
  "ds_read_b64_tr_b16 v[80:81], v54 offset:34816\n\t" \
  "v_mfma_f32_16x16x32_bf16 v[164:167], v[76:79], v[124:127], v[164:167]\n\t" \
  "ds_read_b64_tr_b16 v[82:83], v54 offset:35328\n\t" \
  "v_mfma_f32_16x16x32_bf16 v[172:175], v[76:79], v[96:99], v[172:175]\n\t" \
  "ds_read_b64_tr_b16 v[116:117], v54 offset:38912\n\t" \
  "v_mfma_f32_16x16x32_bf16 v[172:175], v[140:143], v[132:135], v[172:175]\n\t" \
  "ds_read_b64_tr_b16 v[118:119], v54 offset:39424\n\t" \
  "v_mfma_f32_16x16x32_bf16 v[180:183], v[140:143], v[108:111], v[180:183]\n\t" \
  "ds_read_b128 v[88:91], v52 offset:0\n\t" \
  "v_mfma_f32_16x16x32_bf16 v[180:183], v[76:79], v[148:151], v[180:183]\n\t" \
  "ds_read_b128 v[124:127], v53 offset:0\n\t" \
  "s_waitcnt lgkmcnt(7)\n\t" \
  "v_mfma_f32_16x16x32_bf16 v[184:187], v[68:71], v[92:95], v[184:187]\n\t" \
  "ds_read_b64_tr_b16 v[96:97], v55 offset:34816\n\t" \
  "v_mfma_f32_16x16x32_bf16 v[184:187], v[64:67], v[84:87], v[184:187]\n\t" \
  "ds_read_b64_tr_b16 v[98:99], v55 offset:35328\n\t" \
  "v_mfma_f32_16x16x32_bf16 v[192:195], v[64:67], v[100:103], v[192:195]\n\t" \
  "ds_read_b64_tr_b16 v[132:133], v55 offset:38912\n\t" \
  "v_mfma_f32_16x16x32_bf16 v[192:195], v[68:71], v[112:115], v[192:195]\n\t" \
  "ds_read_b64_tr_b16 v[134:135], v55 offset:39424\n\t" \
  "v_mfma_f32_16x16x32_bf16 v[200:203], v[68:71], v[128:131], v[200:203]\n\t" \
  "ds_read_b64_tr_b16 v[108:109], v54 offset:18432\n\t" \
  "v_mfma_f32_16x16x32_bf16 v[200:203], v[64:67], v[120:123], v[200:203]\n\t" \
  "ds_read_b64_tr_b16 v[110:111], v54 offset:18944\n\t" \
  "v_mfma_f32_16x16x32_bf16 v[208:211], v[64:67], v[136:139], v[208:211]\n\t" \
  "ds_read_b64_tr_b16 v[140:141], v54 offset:22528\n\t" \
  "v_mfma_f32_16x16x32_bf16 v[208:211], v[68:71], v[144:147], v[208:211]\n\t" \
  "ds_read_b64_tr_b16 v[142:143], v54 offset:23040\n\t" \
  "v_mfma_f32_16x16x32_bf16 v[188:191], v[104:107], v[92:95], v[188:191]\n\t" \
  "ds_read_b64_tr_b16 v[76:77], v55 offset:18432\n\t" \
  "s_waitcnt lgkmcnt(7)\n\t" \
  "v_mfma_f32_16x16x32_bf16 v[188:191], v[72:75], v[84:87], v[188:191]\n\t" \
  "ds_read_b64_tr_b16 v[78:79], v55 offset:18944\n\t" \
  "v_mfma_f32_16x16x32_bf16 v[196:199], v[72:75], v[100:103], v[196:199]\n\t" \
  "ds_read_b64_tr_b16 v[148:149], v55 offset:22528\n\t" \
  "v_mfma_f32_16x16x32_bf16 v[196:199], v[104:107], v[112:115], v[196:199]\n\t" \
  "ds_read_b64_tr_b16 v[150:151], v55 offset:23040\n\t" \
  "v_mfma_f32_16x16x32_bf16 v[204:207], v[104:107], v[128:131], v[204:207]\n\t" \
  "v_mfma_f32_16x16x32_bf16 v[204:207], v[72:75], v[120:123], v[204:207]\n\t" \
  "v_mfma_f32_16x16x32_bf16 v[212:215], v[72:75], v[136:139], v[212:215]\n\t" \
  "v_mfma_f32_16x16x32_bf16 v[212:215], v[104:107], v[144:147], v[212:215]\n\t" \
  "v_mfma_f32_16x16x32_bf16 v[216:219], v[92:95], v[80:83], v[216:219]\n\t" \
  "v_mfma_f32_16x16x32_bf16 v[216:219], v[84:87], v[116:119], v[216:219]\n\t" \
  "v_mfma_f32_16x16x32_bf16 v[224:227], v[100:103], v[116:119], v[224:227]\n\t" \
  "v_mfma_f32_16x16x32_bf16 v[224:227], v[112:115], v[80:83], v[224:227]\n\t" \
  "v_mfma_f32_16x16x32_bf16 v[232:235], v[128:131], v[80:83], v[232:235]\n\t" \
  "v_mfma_f32_16x16x32_bf16 v[232:235], v[120:123], v[116:119], v[232:235]\n\t" \
  "v_mfma_f32_16x16x32_bf16 v[240:243], v[136:139], v[116:119], v[240:243]\n\t" \
  "v_mfma_f32_16x16x32_bf16 v[240:243], v[144:147], v[80:83], v[240:243]\n\t" \
  "v_mfma_f32_16x16x32_bf16 v[248:251], v[88:91], v[80:83], v[248:251]\n\t" \
  "v_mfma_f32_16x16x32_bf16 v[248:251], v[124:127], v[116:119], v[248:251]\n\t" \
  "v_mfma_f32_16x16x32_bf16 v[252:255], v[88:91], v[96:99], v[252:255]\n\t" \
  "s_waitcnt lgkmcnt(0)\n\t" \
  "v_mfma_f32_16x16x32_bf16 v[252:255], v[124:127], v[132:135], v[252:255]\n\t" \
  "v_mfma_f32_16x16x32_bf16 v[220:223], v[84:87], v[132:135], v[220:223]\n\t" \
  "v_mfma_f32_16x16x32_bf16 v[220:223], v[92:95], v[96:99], v[220:223]\n\t" \
  "s_barrier\n\t" \
  "v_mfma_f32_16x16x32_bf16 v[228:231], v[112:115], v[96:99], v[228:231]\n\t" \
  "ds_read_b64_tr_b16 v[64:65], v58 offset:26624\n\t" \
  "v_mfma_f32_16x16x32_bf16 v[228:231], v[100:103], v[132:135], v[228:231]\n\t" \
  "ds_read_b64_tr_b16 v[66:67], v58 offset:27136\n\t" \
  "v_mfma_f32_16x16x32_bf16 v[236:239], v[120:123], v[132:135], v[236:239]\n\t" \
  "ds_read_b128 v[68:71], v56 offset:10240\n\t" \
  "v_mfma_f32_16x16x32_bf16 v[236:239], v[128:131], v[96:99], v[236:239]\n\t" \
  "ds_read_b64_tr_b16 v[72:73], v58 offset:30720\n\t" \
  "v_mfma_f32_16x16x32_bf16 v[244:247], v[144:147], v[96:99], v[244:247]\n\t" \
  "ds_read_b64_tr_b16 v[74:75], v58 offset:31232\n\t" \
  "v_mfma_f32_16x16x32_bf16 v[244:247], v[136:139], v[132:135], v[244:247]\n\t" \
  "ds_read_b128 v[104:107], v57 offset:10240\n\t" \
  "v_mfma_f32_16x16x32_bf16 v[152:155], v[108:111], v[92:95], v[152:155]\n\t" \
  "ds_read_b128 v[80:83], v57 offset:12288\n\t" \
  "v_mfma_f32_16x16x32_bf16 v[152:155], v[140:143], v[84:87], v[152:155]\n\t" \
  "ds_read_b128 v[116:119], v56 offset:12288\n\t" \
  "v_mfma_f32_16x16x32_bf16 v[160:163], v[140:143], v[100:103], v[160:163]\n\t" \
  "ds_read_b128 v[88:91], v56 offset:14336\n\t" \
  "v_mfma_f32_16x16x32_bf16 v[160:163], v[108:111], v[112:115], v[160:163]\n\t" \
  "ds_read_b128 v[124:127], v57 offset:14336\n\t" \
  "v_mfma_f32_16x16x32_bf16 v[168:171], v[108:111], v[128:131], v[168:171]\n\t" \
  "ds_read_b128 v[96:99], v57 offset:16384\n\t" \
  "v_mfma_f32_16x16x32_bf16 v[168:171], v[140:143], v[120:123], v[168:171]\n\t" \
  "ds_read_b128 v[132:135], v56 offset:16384\n\t" \
  "v_mfma_f32_16x16x32_bf16 v[176:179], v[140:143], v[136:139], v[176:179]\n\t" \
  "ds_read_b64_tr_b16 v[140:141], v59 offset:26624\n\t" \
  "v_mfma_f32_16x16x32_bf16 v[176:179], v[108:111], v[144:147], v[176:179]\n\t" \
  "ds_read_b64_tr_b16 v[142:143], v59 offset:27136\n\t" \
  "v_mfma_f32_16x16x32_bf16 v[156:159], v[76:79], v[92:95], v[156:159]\n\t" \
  "ds_read_b64_tr_b16 v[108:109], v59 offset:30720\n\t" \
  "v_mfma_f32_16x16x32_bf16 v[156:159], v[148:151], v[84:87], v[156:159]\n\t" \
  "ds_read_b64_tr_b16 v[110:111], v59 offset:31232\n\t" \
  "v_mfma_f32_16x16x32_bf16 v[164:167], v[148:151], v[100:103], v[164:167]\n\t" \
  "ds_read_b64_tr_b16 v[92:93], v58 offset:34816\n\t" \
  "v_mfma_f32_16x16x32_bf16 v[164:167], v[76:79], v[112:115], v[164:167]\n\t" \
  "ds_read_b64_tr_b16 v[94:95], v58 offset:35328\n\t" \
  "v_mfma_f32_16x16x32_bf16 v[172:175], v[76:79], v[128:131], v[172:175]\n\t" \
  "ds_read_b64_tr_b16 v[84:85], v58 offset:38912\n\t" \
  "v_mfma_f32_16x16x32_bf16 v[172:175], v[148:151], v[120:123], v[172:175]\n\t" \
  "ds_read_b64_tr_b16 v[86:87], v58 offset:39424\n\t" \
  "v_mfma_f32_16x16x32_bf16 v[180:183], v[148:151], v[136:139], v[180:183]\n\t" \
  "ds_read_b128 v[100:103], v56 offset:0\n\t" \
  "v_mfma_f32_16x16x32_bf16 v[180:183], v[76:79], v[144:147], v[180:183]\n\t" \
  "ds_read_b128 v[112:115], v57 offset:0\n\t" \
  "s_waitcnt lgkmcnt(7)\n\t" \
  "v_mfma_f32_16x16x32_bf16 v[184:187], v[64:67], v[68:71], v[184:187]\n\t" \
  "ds_read_b64_tr_b16 v[128:129], v59 offset:34816\n\t" \
  "v_mfma_f32_16x16x32_bf16 v[184:187], v[72:75], v[104:107], v[184:187]\n\t" \
  "ds_read_b64_tr_b16 v[130:131], v59 offset:35328\n\t" \
  "v_mfma_f32_16x16x32_bf16 v[192:195], v[72:75], v[80:83], v[192:195]\n\t" \
  "ds_read_b64_tr_b16 v[120:121], v59 offset:38912\n\t" \
  "v_mfma_f32_16x16x32_bf16 v[192:195], v[64:67], v[116:119], v[192:195]\n\t" \
  "ds_read_b64_tr_b16 v[122:123], v59 offset:39424\n\t" \
  "v_mfma_f32_16x16x32_bf16 v[200:203], v[64:67], v[88:91], v[200:203]\n\t" \
  "ds_read_b64_tr_b16 v[136:137], v58 offset:18432\n\t" \
  "v_mfma_f32_16x16x32_bf16 v[200:203], v[72:75], v[124:127], v[200:203]\n\t" \
  "ds_read_b64_tr_b16 v[138:139], v58 offset:18944\n\t" \
  "v_mfma_f32_16x16x32_bf16 v[208:211], v[72:75], v[96:99], v[208:211]\n\t" \
  "ds_read_b64_tr_b16 v[148:149], v58 offset:22528\n\t" \
  "v_mfma_f32_16x16x32_bf16 v[208:211], v[64:67], v[132:135], v[208:211]\n\t" \
  "ds_read_b64_tr_b16 v[150:151], v58 offset:23040\n\t" \
  "v_mfma_f32_16x16x32_bf16 v[188:191], v[140:143], v[68:71], v[188:191]\n\t" \
  "ds_read_b64_tr_b16 v[76:77], v59 offset:18432\n\t" \
  "s_waitcnt lgkmcnt(7)\n\t" \
  "v_mfma_f32_16x16x32_bf16 v[188:191], v[108:111], v[104:107], v[188:191]\n\t" \
  "ds_read_b64_tr_b16 v[78:79], v59 offset:18944\n\t" \
  "v_mfma_f32_16x16x32_bf16 v[196:199], v[108:111], v[80:83], v[196:199]\n\t" \
  "ds_read_b64_tr_b16 v[144:145], v59 offset:22528\n\t" \
  "v_mfma_f32_16x16x32_bf16 v[196:199], v[140:143], v[116:119], v[196:199]\n\t" \
  "ds_read_b64_tr_b16 v[146:147], v59 offset:23040\n\t" \
  "v_mfma_f32_16x16x32_bf16 v[204:207], v[140:143], v[88:91], v[204:207]\n\t" \
  "v_mfma_f32_16x16x32_bf16 v[204:207], v[108:111], v[124:127], v[204:207]\n\t" \
  "v_mfma_f32_16x16x32_bf16 v[212:215], v[108:111], v[96:99], v[212:215]\n\t" \
  "v_mfma_f32_16x16x32_bf16 v[212:215], v[140:143], v[132:135], v[212:215]\n\t" \
  "v_mfma_f32_16x16x32_bf16 v[216:219], v[68:71], v[92:95], v[216:219]\n\t" \
  "v_mfma_f32_16x16x32_bf16 v[216:219], v[104:107], v[84:87], v[216:219]\n\t" \
  "v_mfma_f32_16x16x32_bf16 v[224:227], v[80:83], v[84:87], v[224:227]\n\t" \
  "v_mfma_f32_16x16x32_bf16 v[224:227], v[116:119], v[92:95], v[224:227]\n\t" \
  "v_mfma_f32_16x16x32_bf16 v[232:235], v[88:91], v[92:95], v[232:235]\n\t" \
  "v_mfma_f32_16x16x32_bf16 v[232:235], v[124:127], v[84:87], v[232:235]\n\t" \
  "v_mfma_f32_16x16x32_bf16 v[240:243], v[96:99], v[84:87], v[240:243]\n\t" \
  "v_mfma_f32_16x16x32_bf16 v[240:243], v[132:135], v[92:95], v[240:243]\n\t" \
  "v_mfma_f32_16x16x32_bf16 v[248:251], v[100:103], v[92:95], v[248:251]\n\t" \
  "v_mfma_f32_16x16x32_bf16 v[248:251], v[112:115], v[84:87], v[248:251]\n\t" \
  "v_mfma_f32_16x16x32_bf16 v[252:255], v[100:103], v[128:131], v[252:255]\n\t" \
  "s_waitcnt lgkmcnt(0)\n\t" \
  "v_mfma_f32_16x16x32_bf16 v[252:255], v[112:115], v[120:123], v[252:255]\n\t" \
  "v_mfma_f32_16x16x32_bf16 v[220:223], v[104:107], v[120:123], v[220:223]\n\t" \
  "v_mfma_f32_16x16x32_bf16 v[220:223], v[68:71], v[128:131], v[220:223]\n\t" \
  "s_barrier\n\t" \
  "v_mfma_f32_16x16x32_bf16 v[228:231], v[116:119], v[128:131], v[228:231]\n\t" \
  "ds_read_b64_tr_b16 v[72:73], v62 offset:26624\n\t" \
  "v_mfma_f32_16x16x32_bf16 v[228:231], v[80:83], v[120:123], v[228:231]\n\t" \
  "ds_read_b64_tr_b16 v[74:75], v62 offset:27136\n\t" \
  "v_mfma_f32_16x16x32_bf16 v[236:239], v[124:127], v[120:123], v[236:239]\n\t" \
  "ds_read_b128 v[64:67], v60 offset:10240\n\t" \
  "v_mfma_f32_16x16x32_bf16 v[236:239], v[88:91], v[128:131], v[236:239]\n\t" \
  "ds_read_b64_tr_b16 v[108:109], v62 offset:30720\n\t" \
  "v_mfma_f32_16x16x32_bf16 v[244:247], v[132:135], v[128:131], v[244:247]\n\t" \
  "ds_read_b64_tr_b16 v[110:111], v62 offset:31232\n\t" \
  "v_mfma_f32_16x16x32_bf16 v[244:247], v[96:99], v[120:123], v[244:247]\n\t" \
  "ds_read_b128 v[140:143], v61 offset:10240\n\t" \
  "v_mfma_f32_16x16x32_bf16 v[152:155], v[136:139], v[68:71], v[152:155]\n\t" \
  "ds_read_b128 v[92:95], v61 offset:12288\n\t" \
  "v_mfma_f32_16x16x32_bf16 v[152:155], v[148:151], v[104:107], v[152:155]\n\t" \
  "ds_read_b128 v[84:87], v60 offset:12288\n\t" \
  "v_mfma_f32_16x16x32_bf16 v[160:163], v[148:151], v[80:83], v[160:163]\n\t" \
  "ds_read_b128 v[100:103], v60 offset:14336\n\t" \
  "v_mfma_f32_16x16x32_bf16 v[160:163], v[136:139], v[116:119], v[160:163]\n\t" \
  "ds_read_b128 v[112:115], v61 offset:14336\n\t" \
  "v_mfma_f32_16x16x32_bf16 v[168:171], v[136:139], v[88:91], v[168:171]\n\t" \
  "ds_read_b128 v[128:131], v61 offset:16384\n\t" \
  "v_mfma_f32_16x16x32_bf16 v[168:171], v[148:151], v[124:127], v[168:171]\n\t" \
  "ds_read_b128 v[120:123], v60 offset:16384\n\t" \
  "v_mfma_f32_16x16x32_bf16 v[176:179], v[148:151], v[96:99], v[176:179]\n\t" \
  "ds_read_b64_tr_b16 v[148:149], v63 offset:26624\n\t" \
  "v_mfma_f32_16x16x32_bf16 v[176:179], v[136:139], v[132:135], v[176:179]\n\t" \
  "ds_read_b64_tr_b16 v[150:151], v63 offset:27136\n\t" \
  "v_mfma_f32_16x16x32_bf16 v[156:159], v[76:79], v[68:71], v[156:159]\n\t" \
  "ds_read_b64_tr_b16 v[136:137], v63 offset:30720\n\t" \
  "v_mfma_f32_16x16x32_bf16 v[156:159], v[144:147], v[104:107], v[156:159]\n\t" \
  "ds_read_b64_tr_b16 v[138:139], v63 offset:31232\n\t" \
  "v_mfma_f32_16x16x32_bf16 v[164:167], v[144:147], v[80:83], v[164:167]\n\t" \
  "ds_read_b64_tr_b16 v[68:69], v62 offset:34816\n\t" \
  "v_mfma_f32_16x16x32_bf16 v[164:167], v[76:79], v[116:119], v[164:167]\n\t" \
  "ds_read_b64_tr_b16 v[70:71], v62 offset:35328\n\t" \
  "v_mfma_f32_16x16x32_bf16 v[172:175], v[76:79], v[88:91], v[172:175]\n\t" \
  "ds_read_b64_tr_b16 v[104:105], v62 offset:38912\n\t" \
  "v_mfma_f32_16x16x32_bf16 v[172:175], v[144:147], v[124:127], v[172:175]\n\t" \
  "ds_read_b64_tr_b16 v[106:107], v62 offset:39424\n\t" \
  "v_mfma_f32_16x16x32_bf16 v[180:183], v[144:147], v[96:99], v[180:183]\n\t" \
  "ds_read_b128 v[80:83], v60 offset:0\n\t" \
  "v_mfma_f32_16x16x32_bf16 v[180:183], v[76:79], v[132:135], v[180:183]\n\t" \
  "ds_read_b128 v[116:119], v61 offset:0\n\t" \
  "s_waitcnt lgkmcnt(7)\n\t" \
  "v_mfma_f32_16x16x32_bf16 v[184:187], v[72:75], v[64:67], v[184:187]\n\t" \
  "ds_read_b64_tr_b16 v[88:89], v63 offset:34816\n\t" \
  "v_mfma_f32_16x16x32_bf16 v[184:187], v[108:111], v[140:143], v[184:187]\n\t" \
  "ds_read_b64_tr_b16 v[90:91], v63 offset:35328\n\t" \
  "v_mfma_f32_16x16x32_bf16 v[192:195], v[108:111], v[92:95], v[192:195]\n\t" \
  "ds_read_b64_tr_b16 v[124:125], v63 offset:38912\n\t" \
  "v_mfma_f32_16x16x32_bf16 v[192:195], v[72:75], v[84:87], v[192:195]\n\t" \
  "ds_read_b64_tr_b16 v[126:127], v63 offset:39424\n\t" \
  "v_mfma_f32_16x16x32_bf16 v[200:203], v[72:75], v[100:103], v[200:203]\n\t" \
  "ds_read_b64_tr_b16 v[96:97], v62 offset:18432\n\t" \
  "v_mfma_f32_16x16x32_bf16 v[200:203], v[108:111], v[112:115], v[200:203]\n\t" \
  "ds_read_b64_tr_b16 v[98:99], v62 offset:18944\n\t" \
  "v_mfma_f32_16x16x32_bf16 v[208:211], v[108:111], v[128:131], v[208:211]\n\t" \
  "ds_read_b64_tr_b16 v[144:145], v62 offset:22528\n\t" \
  "v_mfma_f32_16x16x32_bf16 v[208:211], v[72:75], v[120:123], v[208:211]\n\t" \
  "ds_read_b64_tr_b16 v[146:147], v62 offset:23040\n\t" \
  "v_mfma_f32_16x16x32_bf16 v[188:191], v[148:151], v[64:67], v[188:191]\n\t" \
  "ds_read_b64_tr_b16 v[76:77], v63 offset:18432\n\t" \
  "s_waitcnt lgkmcnt(7)\n\t" \
  "v_mfma_f32_16x16x32_bf16 v[188:191], v[136:139], v[140:143], v[188:191]\n\t" \
  "ds_read_b64_tr_b16 v[78:79], v63 offset:18944\n\t" \
  "v_mfma_f32_16x16x32_bf16 v[196:199], v[136:139], v[92:95], v[196:199]\n\t" \
  "ds_read_b64_tr_b16 v[132:133], v63 offset:22528\n\t" \
  "v_mfma_f32_16x16x32_bf16 v[196:199], v[148:151], v[84:87], v[196:199]\n\t" \
  "ds_read_b64_tr_b16 v[134:135], v63 offset:23040\n\t" \
  "v_mfma_f32_16x16x32_bf16 v[204:207], v[148:151], v[100:103], v[204:207]\n\t" \
  "v_mfma_f32_16x16x32_bf16 v[204:207], v[136:139], v[112:115], v[204:207]\n\t" \
  "v_mfma_f32_16x16x32_bf16 v[212:215], v[136:139], v[128:131], v[212:215]\n\t" \
  "v_mfma_f32_16x16x32_bf16 v[212:215], v[148:151], v[120:123], v[212:215]\n\t" \
  "v_mfma_f32_16x16x32_bf16 v[216:219], v[64:67], v[68:71], v[216:219]\n\t" \
  "v_mfma_f32_16x16x32_bf16 v[216:219], v[140:143], v[104:107], v[216:219]\n\t" \
  "v_mfma_f32_16x16x32_bf16 v[224:227], v[92:95], v[104:107], v[224:227]\n\t" \
  "v_mfma_f32_16x16x32_bf16 v[224:227], v[84:87], v[68:71], v[224:227]\n\t" \
  "v_mfma_f32_16x16x32_bf16 v[232:235], v[100:103], v[68:71], v[232:235]\n\t" \
  "v_mfma_f32_16x16x32_bf16 v[232:235], v[112:115], v[104:107], v[232:235]\n\t" \
  "v_mfma_f32_16x16x32_bf16 v[240:243], v[128:131], v[104:107], v[240:243]\n\t" \
  "v_mfma_f32_16x16x32_bf16 v[240:243], v[120:123], v[68:71], v[240:243]\n\t" \
  "v_mfma_f32_16x16x32_bf16 v[248:251], v[80:83], v[68:71], v[248:251]\n\t" \
  "v_mfma_f32_16x16x32_bf16 v[248:251], v[116:119], v[104:107], v[248:251]\n\t" \
  "v_mfma_f32_16x16x32_bf16 v[252:255], v[80:83], v[88:91], v[252:255]\n\t" \
  "s_waitcnt lgkmcnt(0)\n\t" \
  "v_mfma_f32_16x16x32_bf16 v[252:255], v[116:119], v[124:127], v[252:255]\n\t" \
  "v_mfma_f32_16x16x32_bf16 v[220:223], v[140:143], v[124:127], v[220:223]\n\t" \
  "v_mfma_f32_16x16x32_bf16 v[220:223], v[64:67], v[88:91], v[220:223]\n\t" \
  "s_barrier\n\t" \
  "v_mfma_f32_16x16x32_bf16 v[228:231], v[84:87], v[88:91], v[228:231]\n\t" \
  "ds_read_b64_tr_b16 v[108:109], v54 offset:26624\n\t" \
  "v_mfma_f32_16x16x32_bf16 v[228:231], v[92:95], v[124:127], v[228:231]\n\t" \
  "ds_read_b64_tr_b16 v[110:111], v54 offset:27136\n\t" \
  "v_mfma_f32_16x16x32_bf16 v[236:239], v[112:115], v[124:127], v[236:239]\n\t" \
  "ds_read_b128 v[72:75], v52 offset:10240\n\t" \
  "v_mfma_f32_16x16x32_bf16 v[236:239], v[100:103], v[88:91], v[236:239]\n\t" \
  "ds_read_b64_tr_b16 v[136:137], v54 offset:30720\n\t" \
  "v_mfma_f32_16x16x32_bf16 v[244:247], v[120:123], v[88:91], v[244:247]\n\t" \
  "ds_read_b64_tr_b16 v[138:139], v54 offset:31232\n\t" \
  "v_mfma_f32_16x16x32_bf16 v[244:247], v[128:131], v[124:127], v[244:247]\n\t" \
  "ds_read_b128 v[148:151], v53 offset:10240\n\t" \
  "v_mfma_f32_16x16x32_bf16 v[152:155], v[96:99], v[64:67], v[152:155]\n\t" \
  "ds_read_b128 v[68:71], v53 offset:12288\n\t" \
  "v_mfma_f32_16x16x32_bf16 v[152:155], v[144:147], v[140:143], v[152:155]\n\t" \
  "ds_read_b128 v[104:107], v52 offset:12288\n\t" \
  "v_mfma_f32_16x16x32_bf16 v[160:163], v[144:147], v[92:95], v[160:163]\n\t" \
  "ds_read_b128 v[80:83], v52 offset:14336\n\t" \
  "v_mfma_f32_16x16x32_bf16 v[160:163], v[96:99], v[84:87], v[160:163]\n\t" \
  "ds_read_b128 v[116:119], v53 offset:14336\n\t" \
  "v_mfma_f32_16x16x32_bf16 v[168:171], v[96:99], v[100:103], v[168:171]\n\t" \
  "ds_read_b128 v[88:91], v53 offset:16384\n\t" \
  "v_mfma_f32_16x16x32_bf16 v[168:171], v[144:147], v[112:115], v[168:171]\n\t" \
  "ds_read_b128 v[124:127], v52 offset:16384\n\t" \
  "v_mfma_f32_16x16x32_bf16 v[176:179], v[144:147], v[128:131], v[176:179]\n\t" \
  "ds_read_b64_tr_b16 v[144:145], v55 offset:26624\n\t" \
  "v_mfma_f32_16x16x32_bf16 v[176:179], v[96:99], v[120:123], v[176:179]\n\t" \
  "ds_read_b64_tr_b16 v[146:147], v55 offset:27136\n\t" \
  "v_mfma_f32_16x16x32_bf16 v[156:159], v[76:79], v[64:67], v[156:159]\n\t" \
  "ds_read_b64_tr_b16 v[96:97], v55 offset:30720\n\t" \
  "v_mfma_f32_16x16x32_bf16 v[156:159], v[132:135], v[140:143], v[156:159]\n\t" \
  "ds_read_b64_tr_b16 v[98:99], v55 offset:31232\n\t" \
  "v_mfma_f32_16x16x32_bf16 v[164:167], v[132:135], v[92:95], v[164:167]\n\t" \
  "ds_read_b64_tr_b16 v[64:65], v54 offset:34816\n\t" \
  "v_mfma_f32_16x16x32_bf16 v[164:167], v[76:79], v[84:87], v[164:167]\n\t" \
  "ds_read_b64_tr_b16 v[66:67], v54 offset:35328\n\t" \
  "v_mfma_f32_16x16x32_bf16 v[172:175], v[76:79], v[100:103], v[172:175]\n\t" \
  "ds_read_b64_tr_b16 v[140:141], v54 offset:38912\n\t" \
  "v_mfma_f32_16x16x32_bf16 v[172:175], v[132:135], v[112:115], v[172:175]\n\t" \
  "ds_read_b64_tr_b16 v[142:143], v54 offset:39424\n\t" \
  "v_mfma_f32_16x16x32_bf16 v[180:183], v[132:135], v[128:131], v[180:183]\n\t" \
  "ds_read_b128 v[92:95], v52 offset:0\n\t" \
  "v_mfma_f32_16x16x32_bf16 v[180:183], v[76:79], v[120:123], v[180:183]\n\t" \
  "ds_read_b128 v[84:87], v53 offset:0\n\t" \
  "s_waitcnt lgkmcnt(7)\n\t" \
  "v_mfma_f32_16x16x32_bf16 v[184:187], v[108:111], v[72:75], v[184:187]\n\t" \
  "ds_read_b64_tr_b16 v[100:101], v55 offset:34816\n\t" \
  "v_mfma_f32_16x16x32_bf16 v[184:187], v[136:139], v[148:151], v[184:187]\n\t" \
  "ds_read_b64_tr_b16 v[102:103], v55 offset:35328\n\t" \
  "v_mfma_f32_16x16x32_bf16 v[192:195], v[136:139], v[68:71], v[192:195]\n\t" \
  "ds_read_b64_tr_b16 v[112:113], v55 offset:38912\n\t" \
  "v_mfma_f32_16x16x32_bf16 v[192:195], v[108:111], v[104:107], v[192:195]\n\t" \
  "ds_read_b64_tr_b16 v[114:115], v55 offset:39424\n\t" \
  "v_mfma_f32_16x16x32_bf16 v[200:203], v[108:111], v[80:83], v[200:203]\n\t" \
  "ds_read_b64_tr_b16 v[128:129], v54 offset:18432\n\t" \
  "v_mfma_f32_16x16x32_bf16 v[200:203], v[136:139], v[116:119], v[200:203]\n\t" \
  "ds_read_b64_tr_b16 v[130:131], v54 offset:18944\n\t" \
  "v_mfma_f32_16x16x32_bf16 v[208:211], v[136:139], v[88:91], v[208:211]\n\t" \
  "ds_read_b64_tr_b16 v[132:133], v54 offset:22528\n\t" \
  "v_mfma_f32_16x16x32_bf16 v[208:211], v[108:111], v[124:127], v[208:211]\n\t" \
  "ds_read_b64_tr_b16 v[134:135], v54 offset:23040\n\t" \
  "v_mfma_f32_16x16x32_bf16 v[188:191], v[144:147], v[72:75], v[188:191]\n\t" \
  "ds_read_b64_tr_b16 v[76:77], v55 offset:18432\n\t" \
  "s_waitcnt lgkmcnt(7)\n\t" \
  "v_mfma_f32_16x16x32_bf16 v[188:191], v[96:99], v[148:151], v[188:191]\n\t" \
  "ds_read_b64_tr_b16 v[78:79], v55 offset:18944\n\t" \
  "v_mfma_f32_16x16x32_bf16 v[196:199], v[96:99], v[68:71], v[196:199]\n\t" \
  "ds_read_b64_tr_b16 v[120:121], v55 offset:22528\n\t" \
  "v_mfma_f32_16x16x32_bf16 v[196:199], v[144:147], v[104:107], v[196:199]\n\t" \
  "ds_read_b64_tr_b16 v[122:123], v55 offset:23040\n\t" \
  "v_mfma_f32_16x16x32_bf16 v[204:207], v[144:147], v[80:83], v[204:207]\n\t" \
  "v_mfma_f32_16x16x32_bf16 v[204:207], v[96:99], v[116:119], v[204:207]\n\t" \
  "v_mfma_f32_16x16x32_bf16 v[212:215], v[96:99], v[88:91], v[212:215]\n\t" \
  "v_mfma_f32_16x16x32_bf16 v[212:215], v[144:147], v[124:127], v[212:215]\n\t" \
  "v_mfma_f32_16x16x32_bf16 v[216:219], v[72:75], v[64:67], v[216:219]\n\t" \
  "v_mfma_f32_16x16x32_bf16 v[216:219], v[148:151], v[140:143], v[216:219]\n\t" \
  "v_mfma_f32_16x16x32_bf16 v[224:227], v[68:71], v[140:143], v[224:227]\n\t" \
  "v_mfma_f32_16x16x32_bf16 v[224:227], v[104:107], v[64:67], v[224:227]\n\t" \
  "v_mfma_f32_16x16x32_bf16 v[232:235], v[80:83], v[64:67], v[232:235]\n\t" \
  "v_mfma_f32_16x16x32_bf16 v[232:235], v[116:119], v[140:143], v[232:235]\n\t" \
  "v_mfma_f32_16x16x32_bf16 v[240:243], v[88:91], v[140:143], v[240:243]\n\t" \
  "v_mfma_f32_16x16x32_bf16 v[240:243], v[124:127], v[64:67], v[240:243]\n\t" \
  "v_mfma_f32_16x16x32_bf16 v[248:251], v[92:95], v[64:67], v[248:251]\n\t" \
  "v_mfma_f32_16x16x32_bf16 v[248:251], v[84:87], v[140:143], v[248:251]\n\t" \
  "v_mfma_f32_16x16x32_bf16 v[252:255], v[92:95], v[100:103], v[252:255]\n\t" \
  "s_waitcnt lgkmcnt(0)\n\t" \
  "v_mfma_f32_16x16x32_bf16 v[252:255], v[84:87], v[112:115], v[252:255]\n\t" \
  "v_mfma_f32_16x16x32_bf16 v[220:223], v[148:151], v[112:115], v[220:223]\n\t" \
  "v_mfma_f32_16x16x32_bf16 v[220:223], v[72:75], v[100:103], v[220:223]\n\t" \
  "s_barrier\n\t" \
  "v_mfma_f32_16x16x32_bf16 v[228:231], v[104:107], v[100:103], v[228:231]\n\t" \
  "ds_read_b64_tr_b16 v[96:97], v58 offset:26624\n\t" \
  "v_mfma_f32_16x16x32_bf16 v[228:231], v[68:71], v[112:115], v[228:231]\n\t" \
  "ds_read_b64_tr_b16 v[98:99], v58 offset:27136\n\t" \
  "v_mfma_f32_16x16x32_bf16 v[236:239], v[116:119], v[112:115], v[236:239]\n\t" \
  "ds_read_b128 v[136:139], v56 offset:10240\n\t" \
  "v_mfma_f32_16x16x32_bf16 v[236:239], v[80:83], v[100:103], v[236:239]\n\t" \
  "ds_read_b64_tr_b16 v[64:65], v58 offset:30720\n\t" \
  "v_mfma_f32_16x16x32_bf16 v[244:247], v[124:127], v[100:103], v[244:247]\n\t" \
  "ds_read_b64_tr_b16 v[66:67], v58 offset:31232\n\t" \
  "v_mfma_f32_16x16x32_bf16 v[244:247], v[88:91], v[112:115], v[244:247]\n\t" \
  "ds_read_b128 v[108:111], v57 offset:10240\n\t" \
  "v_mfma_f32_16x16x32_bf16 v[152:155], v[128:131], v[72:75], v[152:155]\n\t" \
  "ds_read_b128 v[144:147], v57 offset:12288\n\t" \
  "v_mfma_f32_16x16x32_bf16 v[152:155], v[132:135], v[148:151], v[152:155]\n\t" \
  "ds_read_b128 v[140:143], v56 offset:12288\n\t" \
  "v_mfma_f32_16x16x32_bf16 v[160:163], v[132:135], v[68:71], v[160:163]\n\t" \
  "ds_read_b128 v[112:115], v56 offset:14336\n\t" \
  "v_mfma_f32_16x16x32_bf16 v[160:163], v[128:131], v[104:107], v[160:163]\n\t" \
  "ds_read_b64_tr_b16 v[92:93], v59 offset:26624\n\t" \
  "v_mfma_f32_16x16x32_bf16 v[168:171], v[128:131], v[80:83], v[168:171]\n\t" \
  "ds_read_b64_tr_b16 v[94:95], v59 offset:27136\n\t" \
  "v_mfma_f32_16x16x32_bf16 v[168:171], v[132:135], v[116:119], v[168:171]\n\t" \
  "ds_read_b64_tr_b16 v[84:85], v59 offset:30720\n\t" \
  "v_mfma_f32_16x16x32_bf16 v[176:179], v[132:135], v[88:91], v[176:179]\n\t" \
  "ds_read_b128 v[132:135], v57 offset:14336\n\t" \
  "v_mfma_f32_16x16x32_bf16 v[176:179], v[128:131], v[124:127], v[176:179]\n\t" \
  "ds_read_b128 v[128:131], v57 offset:16384\n\t" \
  "v_mfma_f32_16x16x32_bf16 v[156:159], v[76:79], v[72:75], v[156:159]\n\t" \
  "ds_read_b64_tr_b16 v[86:87], v59 offset:31232\n\t" \
  "v_mfma_f32_16x16x32_bf16 v[156:159], v[120:123], v[148:151], v[156:159]\n\t" \
  "ds_read_b128 v[148:151], v56 offset:16384\n\t" \
  "v_mfma_f32_16x16x32_bf16 v[164:167], v[120:123], v[68:71], v[164:167]\n\t" \
  "ds_read_b64_tr_b16 v[100:101], v58 offset:34816\n\t" \
  "v_mfma_f32_16x16x32_bf16 v[164:167], v[76:79], v[104:107], v[164:167]\n\t" \
  "ds_read_b64_tr_b16 v[102:103], v58 offset:35328\n\t" \
  "v_mfma_f32_16x16x32_bf16 v[172:175], v[76:79], v[80:83], v[172:175]\n\t" \
  "ds_read_b64_tr_b16 v[72:73], v58 offset:38912\n\t" \
  "v_mfma_f32_16x16x32_bf16 v[172:175], v[120:123], v[116:119], v[172:175]\n\t" \
  "ds_read_b64_tr_b16 v[74:75], v58 offset:39424\n\t" \
  "v_mfma_f32_16x16x32_bf16 v[180:183], v[120:123], v[88:91], v[180:183]\n\t" \
  "ds_read_b128 v[68:71], v56 offset:0\n\t" \
  "v_mfma_f32_16x16x32_bf16 v[180:183], v[76:79], v[124:127], v[180:183]\n\t" \
  "ds_read_b128 v[104:107], v57 offset:0\n\t" \
  "s_waitcnt lgkmcnt(7)\n\t" \
  "v_mfma_f32_16x16x32_bf16 v[184:187], v[96:99], v[136:139], v[184:187]\n\t" \
  "ds_read_b64_tr_b16 v[80:81], v59 offset:34816\n\t" \
  "v_mfma_f32_16x16x32_bf16 v[184:187], v[64:67], v[108:111], v[184:187]\n\t" \
  "ds_read_b64_tr_b16 v[82:83], v59 offset:35328\n\t" \
  "v_mfma_f32_16x16x32_bf16 v[192:195], v[64:67], v[144:147], v[192:195]\n\t" \
  "ds_read_b64_tr_b16 v[88:89], v59 offset:38912\n\t" \
  "v_mfma_f32_16x16x32_bf16 v[192:195], v[96:99], v[140:143], v[192:195]\n\t" \
  "ds_read_b64_tr_b16 v[90:91], v59 offset:39424\n\t" \
  "v_mfma_f32_16x16x32_bf16 v[200:203], v[96:99], v[112:115], v[200:203]\n\t" \
  "ds_read_b64_tr_b16 v[116:117], v58 offset:18432\n\t" \
  "v_mfma_f32_16x16x32_bf16 v[200:203], v[64:67], v[132:135], v[200:203]\n\t" \
  "ds_read_b64_tr_b16 v[118:119], v58 offset:18944\n\t" \
  "v_mfma_f32_16x16x32_bf16 v[208:211], v[64:67], v[128:131], v[208:211]\n\t" \
  "ds_read_b64_tr_b16 v[120:121], v58 offset:22528\n\t" \
  "s_waitcnt lgkmcnt(7)\n\t" \
  "v_mfma_f32_16x16x32_bf16 v[208:211], v[96:99], v[148:151], v[208:211]\n\t" \
  "ds_read_b64_tr_b16 v[122:123], v58 offset:23040\n\t" \
  "v_mfma_f32_16x16x32_bf16 v[188:191], v[92:95], v[136:139], v[188:191]\n\t" \
  "ds_read_b64_tr_b16 v[124:125], v59 offset:18432\n\t" \
  "v_mfma_f32_16x16x32_bf16 v[188:191], v[84:87], v[108:111], v[188:191]\n\t" \
  "ds_read_b64_tr_b16 v[126:127], v59 offset:18944\n\t" \
  "v_mfma_f32_16x16x32_bf16 v[196:199], v[84:87], v[144:147], v[196:199]\n\t" \
  "ds_read_b64_tr_b16 v[76:77], v59 offset:22528\n\t" \
  "v_mfma_f32_16x16x32_bf16 v[196:199], v[92:95], v[140:143], v[196:199]\n\t" \
  "ds_read_b64_tr_b16 v[78:79], v59 offset:23040\n\t" \
  "v_mfma_f32_16x16x32_bf16 v[204:207], v[92:95], v[112:115], v[204:207]\n\t" \
  "v_mfma_f32_16x16x32_bf16 v[204:207], v[84:87], v[132:135], v[204:207]\n\t" \
  "v_mfma_f32_16x16x32_bf16 v[212:215], v[84:87], v[128:131], v[212:215]\n\t" \
  "v_mfma_f32_16x16x32_bf16 v[212:215], v[92:95], v[148:151], v[212:215]\n\t" \
  "v_mfma_f32_16x16x32_bf16 v[216:219], v[136:139], v[100:103], v[216:219]\n\t" \
  "v_mfma_f32_16x16x32_bf16 v[216:219], v[108:111], v[72:75], v[216:219]\n\t" \
  "v_mfma_f32_16x16x32_bf16 v[224:227], v[144:147], v[72:75], v[224:227]\n\t" \
  "v_and_b32 v14, -16, v32\n\t" \
  "v_mfma_f32_16x16x32_bf16 v[224:227], v[140:143], v[100:103], v[224:227]\n\t" \
  "v_mfma_f32_16x16x32_bf16 v[232:235], v[112:115], v[100:103], v[232:235]\n\t" \
  "ds_read_b128 v[10:13], v14 offset:128\n\t" \
  "v_mfma_f32_16x16x32_bf16 v[232:235], v[132:135], v[72:75], v[232:235]\n\t" \
  "v_mfma_f32_16x16x32_bf16 v[240:243], v[128:131], v[72:75], v[240:243]\n\t" \
  "v_mfma_f32_16x16x32_bf16 v[240:243], v[148:151], v[100:103], v[240:243]\n\t" \
  "v_mfma_f32_16x16x32_bf16 v[248:251], v[68:71], v[100:103], v[248:251]\n\t" \
  "v_mfma_f32_16x16x32_bf16 v[248:251], v[104:107], v[72:75], v[248:251]\n\t" \
  "s_waitcnt lgkmcnt(1)\n\t" \
  "v_mfma_f32_16x16x32_bf16 v[252:255], v[68:71], v[80:83], v[252:255]\n\t" \
  "s_waitcnt lgkmcnt(0)\n\t" \
  "v_and_b32 v10, v10, v11\n\t" \
  "v_and_b32 v12, v12, v13\n\t" \
  "v_mfma_f32_16x16x32_bf16 v[252:255], v[104:107], v[88:91], v[252:255]\n\t" \
  "v_and_b32 v10, v10, v12\n\t" \
  "v_mfma_f32_16x16x32_bf16 v[220:223], v[108:111], v[88:91], v[220:223]\n\t" \
  "v_readfirstlane_b32 vcc_lo, v10\n\t" \
  "v_mfma_f32_16x16x32_bf16 v[220:223], v[136:139], v[80:83], v[220:223]\n\t" \
  "ds_write_b32 v32, v0 offset:144\n\t" \
  "s_cmp_eq_u32 vcc_lo, 0\n\t" \
  "s_cbranch_scc1 15f\n\t" \
  "14:\n\t" \
  "v_mfma_f32_16x16x32_bf16 v[228:231], v[140:143], v[80:83], v[228:231]\n\t" \
  "ds_read_b64_tr_b16 v[64:65], v62 offset:26624\n\t" \
  "v_mfma_f32_16x16x32_bf16 v[228:231], v[144:147], v[88:91], v[228:231]\n\t" \
  "ds_read_b64_tr_b16 v[66:67], v62 offset:27136\n\t" \
  "v_mfma_f32_16x16x32_bf16 v[236:239], v[132:135], v[88:91], v[236:239]\n\t" \
  "ds_read_b128 v[96:99], v60 offset:10240\n\t" \
  "v_mfma_f32_16x16x32_bf16 v[236:239], v[112:115], v[80:83], v[236:239]\n\t" \
  "ds_read_b128 v[84:87], v60 offset:12288\n\t" \
  "v_mfma_f32_16x16x32_bf16 v[244:247], v[148:151], v[80:83], v[244:247]\n\t" \
  "ds_read_b128 v[92:95], v60 offset:14336\n\t" \
  "v_mfma_f32_16x16x32_bf16 v[244:247], v[128:131], v[88:91], v[244:247]\n\t" \
  "ds_read_b128 v[100:103], v60 offset:16384\n\t" \
  "v_mfma_f32_16x16x32_bf16 v[152:155], v[116:119], v[136:139], v[152:155]\n\t" \
  "ds_read_b64_tr_b16 v[72:73], v63 offset:26624\n\t" \
  "v_mfma_f32_16x16x32_bf16 v[152:155], v[120:123], v[108:111], v[152:155]\n\t" \
  "ds_read_b64_tr_b16 v[74:75], v63 offset:27136\n\t" \
  "v_mfma_f32_16x16x32_bf16 v[160:163], v[120:123], v[144:147], v[160:163]\n\t" \
  "ds_read_b64_tr_b16 v[68:69], v62 offset:34816\n\t" \
  "v_mfma_f32_16x16x32_bf16 v[160:163], v[116:119], v[140:143], v[160:163]\n\t" \
  "ds_read_b64_tr_b16 v[70:71], v62 offset:35328\n\t" \
  "v_mfma_f32_16x16x32_bf16 v[168:171], v[116:119], v[112:115], v[168:171]\n\t" \
  "ds_read_b128 v[104:107], v60 offset:0\n\t" \
  "v_mfma_f32_16x16x32_bf16 v[168:171], v[120:123], v[132:135], v[168:171]\n\t" \
  "ds_read_b64_tr_b16 v[80:81], v63 offset:34816\n\t" \
  "v_mfma_f32_16x16x32_bf16 v[176:179], v[120:123], v[128:131], v[176:179]\n\t" \
  "ds_read_b64_tr_b16 v[82:83], v63 offset:35328\n\t" \
  "v_mfma_f32_16x16x32_bf16 v[176:179], v[116:119], v[148:151], v[176:179]\n\t" \
  "ds_read_b64_tr_b16 v[88:89], v62 offset:18432\n\t" \
  "v_mfma_f32_16x16x32_bf16 v[156:159], v[124:127], v[136:139], v[156:159]\n\t" \
  "ds_read_b64_tr_b16 v[90:91], v62 offset:18944\n\t" \
  "v_mfma_f32_16x16x32_bf16 v[156:159], v[76:79], v[108:111], v[156:159]\n\t" \
  "ds_read_b64_tr_b16 v[120:121], v62 offset:30720\n\t" \
  "v_mfma_f32_16x16x32_bf16 v[164:167], v[76:79], v[144:147], v[164:167]\n\t" \
  "ds_read_b64_tr_b16 v[122:123], v62 offset:31232\n\t" \
  "v_mfma_f32_16x16x32_bf16 v[164:167], v[124:127], v[140:143], v[164:167]\n\t" \
  "ds_read_b128 v[116:119], v61 offset:10240\n\t" \
  "v_mfma_f32_16x16x32_bf16 v[172:175], v[124:127], v[112:115], v[172:175]\n\t" \
  "ds_read_b128 v[136:139], v61 offset:12288\n\t" \
  "v_mfma_f32_16x16x32_bf16 v[172:175], v[76:79], v[132:135], v[172:175]\n\t" \
  "ds_read_b128 v[108:111], v61 offset:14336\n\t" \
  "v_mfma_f32_16x16x32_bf16 v[180:183], v[76:79], v[128:131], v[180:183]\n\t" \
  "ds_read_b64_tr_b16 v[76:77], v63 offset:18432\n\t" \
  "v_mfma_f32_16x16x32_bf16 v[180:183], v[124:127], v[148:151], v[180:183]\n\t" \
  "ds_read_b64_tr_b16 v[78:79], v63 offset:18944\n\t" \
  "s_waitcnt lgkmcnt(7)\n\t" \
  "v_mfma_f32_16x16x32_bf16 v[184:187], v[64:67], v[96:99], v[184:187]\n\t" \
  "ds_read_b128 v[144:147], v61 offset:16384\n\t" \
  "v_mfma_f32_16x16x32_bf16 v[192:195], v[64:67], v[84:87], v[192:195]\n\t" \
  "ds_read_b64_tr_b16 v[140:141], v63 offset:30720\n\t" \
  "v_mfma_f32_16x16x32_bf16 v[200:203], v[64:67], v[92:95], v[200:203]\n\t" \
  "ds_read_b64_tr_b16 v[142:143], v63 offset:31232\n\t" \
  "v_mfma_f32_16x16x32_bf16 v[208:211], v[64:67], v[100:103], v[208:211]\n\t" \
  "502:\n\t" \
  "s_cbranch_execz 506f\n\t" \
  "ds_read_b64_tr_b16 v[112:113], v62 offset:22528\n\t" \
  "v_mfma_f32_16x16x32_bf16 v[188:191], v[72:75], v[96:99], v[188:191]\n\t" \
  "ds_read_b64_tr_b16 v[114:115], v62 offset:23040\n\t" \
  "v_mfma_f32_16x16x32_bf16 v[196:199], v[72:75], v[84:87], v[196:199]\n\t" \
  "ds_read_b64_tr_b16 v[132:133], v63 offset:22528\n\t" \
  "v_mfma_f32_16x16x32_bf16 v[204:207], v[72:75], v[92:95], v[204:207]\n\t" \
  "ds_read_b64_tr_b16 v[134:135], v63 offset:23040\n\t" \
  "v_mfma_f32_16x16x32_bf16 v[212:215], v[72:75], v[100:103], v[212:215]\n\t" \
  "ds_read_b64_tr_b16 v[128:129], v62 offset:38912\n\t" \
  "v_mfma_f32_16x16x32_bf16 v[248:251], v[104:107], v[68:71], v[248:251]\n\t" \
  "ds_read_b64_tr_b16 v[130:131], v62 offset:39424\n\t" \
  "v_mfma_f32_16x16x32_bf16 v[216:219], v[96:99], v[68:71], v[216:219]\n\t" \
  "ds_read_b128 v[124:127], v61 offset:0\n\t" \
  "v_mfma_f32_16x16x32_bf16 v[224:227], v[84:87], v[68:71], v[224:227]\n\t" \
  "ds_read_b64_tr_b16 v[148:149], v63 offset:38912\n\t" \
  "v_mfma_f32_16x16x32_bf16 v[232:235], v[92:95], v[68:71], v[232:235]\n\t" \
  "ds_read_b64_tr_b16 v[150:151], v63 offset:39424\n\t" \
  "v_mfma_f32_16x16x32_bf16 v[240:243], v[100:103], v[68:71], v[240:243]\n\t" \
  "v_mfma_f32_16x16x32_bf16 v[252:255], v[104:107], v[80:83], v[252:255]\n\t" \
  "v_mfma_f32_16x16x32_bf16 v[220:223], v[96:99], v[80:83], v[220:223]\n\t" \
  "v_mfma_f32_16x16x32_bf16 v[228:231], v[84:87], v[80:83], v[228:231]\n\t" \
  "v_mfma_f32_16x16x32_bf16 v[236:239], v[92:95], v[80:83], v[236:239]\n\t" \
  "v_mfma_f32_16x16x32_bf16 v[244:247], v[100:103], v[80:83], v[244:247]\n\t" \
  "v_mfma_f32_16x16x32_bf16 v[152:155], v[88:91], v[96:99], v[152:155]\n\t" \
  "v_mfma_f32_16x16x32_bf16 v[160:163], v[88:91], v[84:87], v[160:163]\n\t" \
  "v_mfma_f32_16x16x32_bf16 v[168:171], v[88:91], v[92:95], v[168:171]\n\t" \
  "v_mfma_f32_16x16x32_bf16 v[176:179], v[88:91], v[100:103], v[176:179]\n\t" \
  "s_waitcnt lgkmcnt(0)\n\t" \
  "v_mfma_f32_16x16x32_bf16 v[156:159], v[76:79], v[96:99], v[156:159]\n\t" \
  "v_mfma_f32_16x16x32_bf16 v[164:167], v[76:79], v[84:87], v[164:167]\n\t" \
  "ds_write_b32 v32, v0\n\t" \
  "v_mfma_f32_16x16x32_bf16 v[172:175], v[76:79], v[92:95], v[172:175]\n\t" \
  "v_mfma_f32_16x16x32_bf16 v[180:183], v[76:79], v[100:103], v[180:183]\n\t" \
  "v_mfma_f32_16x16x32_bf16 v[184:187], v[120:123], v[116:119], v[184:187]\n\t" \
  "v_and_b32 v56, -16, v32\n\t" \
  "v_mfma_f32_16x16x32_bf16 v[192:195], v[120:123], v[136:139], v[192:195]\n\t" \
  "v_mfma_f32_16x16x32_bf16 v[200:203], v[120:123], v[108:111], v[200:203]\n\t" \
  "v_mfma_f32_16x16x32_bf16 v[208:211], v[120:123], v[144:147], v[208:211]\n\t" \
  "v_mfma_f32_16x16x32_bf16 v[188:191], v[140:143], v[116:119], v[188:191]\n\t" \
  "v_mfma_f32_16x16x32_bf16 v[196:199], v[140:143], v[136:139], v[196:199]\n\t" \
  "v_mfma_f32_16x16x32_bf16 v[204:207], v[140:143], v[108:111], v[204:207]\n\t" \
  "v_mfma_f32_16x16x32_bf16 v[212:215], v[140:143], v[144:147], v[212:215]\n\t" \
  "v_cvt_pk_bf16_f32 v14, v184, v185\n\t" \
  "v_cvt_pk_bf16_f32 v15, v186, v187\n\t" \
  "v_mfma_f32_16x16x32_bf16 v[152:155], v[112:115], v[116:119], v[152:155]\n\t" \
  "v_cvt_pk_bf16_f32 v18, v192, v193\n\t" \
  "v_cvt_pk_bf16_f32 v19, v194, v195\n\t" \
  "v_mfma_f32_16x16x32_bf16 v[160:163], v[112:115], v[136:139], v[160:163]\n\t" \
  "v_cvt_pk_bf16_f32 v16, v188, v189\n\t" \
  "v_cvt_pk_bf16_f32 v17, v190, v191\n\t" \
  "v_mfma_f32_16x16x32_bf16 v[168:171], v[112:115], v[108:111], v[168:171]\n\t" \
  "v_cvt_pk_bf16_f32 v20, v196, v197\n\t" \
  "v_cvt_pk_bf16_f32 v21, v198, v199\n\t" \
  "v_mfma_f32_16x16x32_bf16 v[176:179], v[112:115], v[144:147], v[176:179]\n\t" \
  "v_cvt_pk_bf16_f32 v22, v200, v201\n\t" \
  "v_cvt_pk_bf16_f32 v23, v202, v203\n\t" \
  "v_mfma_f32_16x16x32_bf16 v[156:159], v[132:135], v[116:119], v[156:159]\n\t" \
  "v_cvt_pk_bf16_f32 v24, v204, v205\n\t" \
  "v_cvt_pk_bf16_f32 v25, v206, v207\n\t" \
  "v_mfma_f32_16x16x32_bf16 v[164:167], v[132:135], v[136:139], v[164:167]\n\t" \
  "v_cvt_pk_bf16_f32 v26, v208, v209\n\t" \
  "v_cvt_pk_bf16_f32 v27, v210, v211\n\t" \
  "v_mfma_f32_16x16x32_bf16 v[172:175], v[132:135], v[108:111], v[172:175]\n\t" \
  "v_cvt_pk_bf16_f32 v28, v212, v213\n\t" \
  "v_cvt_pk_bf16_f32 v29, v214, v215\n\t" \
  "v_mfma_f32_16x16x32_bf16 v[180:183], v[132:135], v[144:147], v[180:183]\n\t" \
  "v_cvt_pk_bf16_f32 v84, v152, v153\n\t" \
  "v_cvt_pk_bf16_f32 v85, v154, v155\n\t" \
  "v_mfma_f32_16x16x32_bf16 v[248:251], v[124:127], v[128:131], v[248:251]\n\t" \
  "v_cvt_pk_bf16_f32 v86, v156, v157\n\t" \
  "v_cvt_pk_bf16_f32 v87, v158, v159\n\t" \
  "v_mfma_f32_16x16x32_bf16 v[216:219], v[116:119], v[128:131], v[216:219]\n\t" \
  "v_cvt_pk_bf16_f32 v88, v160, v161\n\t" \
  "v_cvt_pk_bf16_f32 v89, v162, v163\n\t" \
  "v_mfma_f32_16x16x32_bf16 v[224:227], v[136:139], v[128:131], v[224:227]\n\t" \
  "v_cvt_pk_bf16_f32 v90, v164, v165\n\t" \
  "v_cvt_pk_bf16_f32 v91, v166, v167\n\t" \
  "v_mfma_f32_16x16x32_bf16 v[232:235], v[108:111], v[128:131], v[232:235]\n\t" \
  "v_cvt_pk_bf16_f32 v72, v248, v249\n\t" \
  "v_cvt_pk_bf16_f32 v73, v250, v251\n\t" \
  "v_mfma_f32_16x16x32_bf16 v[240:243], v[144:147], v[128:131], v[240:243]\n\t" \
  "ds_read_b32 v52, v33 offset:48\n\t" \
  "ds_read_b128 v[10:13], v31\n\t" \
  "v_cvt_pk_bf16_f32 v92, v168, v169\n\t" \
  "v_mfma_f32_16x16x32_bf16 v[252:255], v[124:127], v[148:151], v[252:255]\n\t" \
  "v_cvt_pk_bf16_f32 v93, v170, v171\n\t" \
  "v_cvt_pk_bf16_f32 v94, v172, v173\n\t" \
  "v_mfma_f32_16x16x32_bf16 v[220:223], v[116:119], v[148:151], v[220:223]\n\t" \
  "v_cvt_pk_bf16_f32 v95, v174, v175\n\t" \
  "v_cvt_pk_bf16_f32 v96, v176, v177\n\t" \
  "v_mfma_f32_16x16x32_bf16 v[228:231], v[136:139], v[148:151], v[228:231]\n\t" \
  "v_cvt_pk_bf16_f32 v97, v178, v179\n\t" \
  "v_cvt_pk_bf16_f32 v98, v180, v181\n\t" \
  "v_mfma_f32_16x16x32_bf16 v[236:239], v[108:111], v[148:151], v[236:239]\n\t" \
  "v_cvt_pk_bf16_f32 v82, v252, v253\n\t" \
  "v_cvt_pk_bf16_f32 v83, v254, v255\n\t" \
  "v_mfma_f32_16x16x32_bf16 v[244:247], v[144:147], v[148:151], v[244:247]\n\t" \
  "ds_write_b64 v30, v[72:73]\n\t" \
  "ds_write_b64 v30, v[82:83] offset:8\n\t" \
  "ds_write_b32 v32, v0 offset:48\n\t" \
  "v_mfma_f32_16x16x32_bf16 v[200:203], v[22:25], v[92:95], v[34:37]\n\t" \
  "v_cvt_pk_bf16_f32 v99, v182, v183\n\t" \
  "s_waitcnt lgkmcnt(0)\n\t" \
  "v_readfirstlane_b32 vcc_lo, v52\n\t" \
  "s_nop 1\n\t" \
  "s_cmp_eq_u32 vcc_lo, 0\n\t" \
  "s_cbranch_scc1 5f\n\t" \
  "4:\n\t" \
  "v_mfma_f32_16x16x32_bf16 v[204:207], v[18:21], v[92:95], v[38:41]\n\t" \
  "v_cvt_pk_bf16_f32 v66, v232, v233\n\t" \
  "v_cvt_pk_bf16_f32 v67, v234, v235\n\t" \
  "v_mfma_f32_16x16x32_bf16 v[208:211], v[26:29], v[96:99], v[34:37]\n\t" \
  "v_cvt_pk_bf16_f32 v76, v236, v237\n\t" \
  "v_cvt_pk_bf16_f32 v77, v238, v239\n\t" \
  "v_mfma_f32_16x16x32_bf16 v[212:215], v[22:25], v[96:99], v[38:41]\n\t" \
  "v_cvt_pk_bf16_f32 v64, v240, v241\n\t" \
  "v_cvt_pk_bf16_f32 v65, v242, v243\n\t" \
  "v_mfma_f32_16x16x32_bf16 v[192:195], v[18:21], v[88:91], v[34:37]\n\t" \
  "v_max_f32 v200, v200, v204\n\t" \
  "v_max_f32 v201, v201, v205\n\t" \
  "v_mfma_f32_16x16x32_bf16 v[196:199], v[14:17], v[88:91], v[38:41]\n\t" \
  "v_max_f32 v202, v202, v206\n\t" \
  "v_max_f32 v203, v203, v207\n\t" \
  "v_mfma_f32_16x16x32_bf16 v[184:187], v[14:17], v[84:87], v[34:37]\n\t" \
  "v_max_f32 v208, v208, v212\n\t" \
  "v_max_f32 v209, v209, v213\n\t" \
  "v_mfma_f32_16x16x32_bf16 v[188:191], v[10:13], v[84:87], v[42:45]\n\t" \
  "v_max_f32 v210, v210, v214\n\t" \
  "v_max_f32 v211, v211, v215\n\t" \
  "v_max_f32 v192, v192, v196\n\t" \
  "v_max_f32 v193, v193, v197\n\t" \
  "v_max_f32 v194, v194, v198\n\t" \
  "v_max3_f32 v106, v208, v209, v210\n\t" \
  "v_max3_f32 v104, v200, v201, v202\n\t" \
  "v_max_f32 v195, v195, v199\n\t" \
  "v_max_f32 v184, v184, v188\n\t" \
  "v_max_f32 v185, v185, v189\n\t" \
  "v_max_f32 v186, v186, v190\n\t" \
  "v_max3_f32 v102, v192, v193, v194\n\t" \
  "v_max_f32 v187, v187, v191\n\t" \
  "v_max3_f32 v100, v184, v185, v186\n\t" \
  "v_max_f32 v106, v106, v211\n\t" \
  "v_max_f32 v104, v104, v203\n\t" \
  "v_max_f32 v102, v102, v195\n\t" \
  "v_max_f32 v100, v100, v187\n\t" \
  "v_permlane16_swap_b32 v104, v106\n\t" \
  "v_cvt_pk_bf16_f32 v74, v244, v245\n\t" \
  "v_permlane16_swap_b32 v100, v102\n\t" \
  "v_max_f32 v104, v104, v106\n\t" \
  "v_max_f32 v100, v100, v102\n\t" \
  "v_cvt_pk_bf16_f32 v75, v246, v247\n\t" \
  "v_cvt_pk_bf16_f32 v68, v224, v225\n\t" \
  "v_permlane32_swap_b32 v100, v104\n\t" \
  "v_cvt_pk_bf16_f32 v69, v226, v227\n\t" \
  "v_max_f32 v100, v100, v104\n\t" \
  "v_mul_f32 v100, 0xbe8293ee, v100\n\t" \
  "v_mov_b32 v104, v100\n\t" \
  "v_cvt_pk_bf16_f32 v78, v228, v229\n\t" \
  "v_cvt_pk_bf16_f32 v79, v230, v231\n\t" \
  "v_permlane32_swap_b32 v100, v104\n\t" \
  "v_cvt_pk_bf16_f32 v70, v216, v217\n\t" \
  "v_mov_b32 v106, v104\n\t" \
  "v_mov_b32 v102, v100\n\t" \
  "v_cvt_pk_bf16_f32 v71, v218, v219\n\t" \
  "v_permlane16_swap_b32 v104, v106\n\t" \
  "v_permlane16_swap_b32 v100, v102\n\t" \
  "v_fma_f32 v208, v208, %16, v106\n\t" \
  "v_fma_f32 v209, v209, %16, v106\n\t" \
  "v_fma_f32 v210, v210, %16, v106\n\t" \
  "v_fma_f32 v211, v211, %16, v106\n\t" \
  "v_exp_f32 v208, v208\n\t" \
  "v_exp_f32 v209, v209\n\t" \
  "v_exp_f32 v210, v210\n\t" \
  "v_exp_f32 v211, v211\n\t" \
  "v_cvt_pk_bf16_f32 v212, v208, v209\n\t" \
  "v_cvt_pk_bf16_f32 v213, v210, v211\n\t" \
  "v_bfi_b32 v214, v46, 0, v212\n\t" \
  "v_bfi_b32 v215, v47, 0, v213\n\t" \
  "v_and_b32 v212, v46, v212\n\t" \
  "v_and_b32 v213, v47, v213\n\t" \
  "v_fma_f32 v200, v200, %16, v104\n\t" \
  "v_fma_f32 v201, v201, %16, v104\n\t" \
  "v_mfma_f32_16x16x32_bf16 v[208:211], v[48:51], v[212:215], 0\n\t" \
  "v_fma_f32 v202, v202, %16, v104\n\t" \
  "v_fma_f32 v203, v203, %16, v104\n\t" \
  "v_mfma_f32_16x16x32_bf16 v[176:179], v[64:67], v[212:215], 0\n\t" \
  "v_exp_f32 v200, v200\n\t" \
  "v_mfma_f32_16x16x32_bf16 v[180:183], v[74:77], v[212:215], 0\n\t" \
  "v_exp_f32 v201, v201\n\t" \
  "v_exp_f32 v202, v202\n\t" \
  "v_exp_f32 v203, v203\n\t" \
  "v_cvt_pk_bf16_f32 v204, v200, v201\n\t" \
  "v_cvt_pk_bf16_f32 v205, v202, v203\n\t" \
  "v_bfi_b32 v206, v46, 0, v204\n\t" \
  "v_bfi_b32 v207, v47, 0, v205\n\t" \
  "v_rcp_f32 v106, v208\n\t" \
  "v_and_b32 v204, v46, v204\n\t" \
  "v_and_b32 v205, v47, v205\n\t" \
  "v_pk_mul_f32 v[176:177], v[176:177], v[106:107] op_sel_hi:[1,0]\n\t" \
  "v_pk_mul_f32 v[178:179], v[178:179], v[106:107] op_sel_hi:[1,0]\n\t" \
  "v_mfma_f32_16x16x32_bf16 v[200:203], v[48:51], v[204:207], 0\n\t" \
  "v_pk_mul_f32 v[180:181], v[180:181], v[106:107] op_sel_hi:[1,0]\n\t" \
  "v_pk_mul_f32 v[182:183], v[182:183], v[106:107] op_sel_hi:[1,0]\n\t" \
  "v_mfma_f32_16x16x32_bf16 v[168:171], v[66:69], v[204:207], 0\n\t" \
  "v_cvt_pk_bf16_f32 v240, v176, v177\n\t" \
  "v_cvt_pk_bf16_f32 v241, v178, v179\n\t" \
  "v_mfma_f32_16x16x32_bf16 v[172:175], v[76:79], v[204:207], 0\n\t" \
  "v_cvt_pk_bf16_f32 v242, v180, v181\n\t" \
  "v_cvt_pk_bf16_f32 v243, v182, v183\n\t" \
  "global_store_dwordx4 v1, v[240:243], %14 offset:0 sc1\n\t" \
  "v_fma_f32 v192, v192, %16, v102\n\t" \
  "v_fma_f32 v193, v193, %16, v102\n\t" \
  "v_fma_f32 v194, v194, %16, v102\n\t" \
  "v_fma_f32 v195, v195, %16, v102\n\t" \
  "v_exp_f32 v192, v192\n\t" \
  "v_exp_f32 v193, v193\n\t" \
  "v_exp_f32 v194, v194\n\t" \
  "v_exp_f32 v195, v195\n\t" \
  "v_cvt_pk_bf16_f32 v196, v192, v193\n\t" \
  "v_cvt_pk_bf16_f32 v197, v194, v195\n\t" \
  "v_bfi_b32 v198, v46, 0, v196\n\t" \
  "v_bfi_b32 v199, v47, 0, v197\n\t" \
  "v_rcp_f32 v104, v200\n\t" \
  "v_and_b32 v196, v46, v196\n\t" \
  "v_and_b32 v197, v47, v197\n\t" \
  "v_cvt_pk_bf16_f32 v80, v220, v221\n\t" \
  "v_cvt_pk_bf16_f32 v81, v222, v223\n\t" \
  "v_mfma_f32_16x16x32_bf16 v[192:195], v[48:51], v[196:199], 0\n\t" \
  "v_pk_mul_f32 v[168:169], v[168:169], v[104:105] op_sel_hi:[1,0]\n\t" \
  "v_pk_mul_f32 v[170:171], v[170:171], v[104:105] op_sel_hi:[1,0]\n\t" \
  "v_mfma_f32_16x16x32_bf16 v[160:163], v[68:71], v[196:199], 0\n\t" \
  "v_pk_mul_f32 v[172:173], v[172:173], v[104:105] op_sel_hi:[1,0]\n\t" \
  "v_pk_mul_f32 v[174:175], v[174:175], v[104:105] op_sel_hi:[1,0]\n\t" \
  "v_mfma_f32_16x16x32_bf16 v[164:167], v[78:81], v[196:199], 0\n\t" \
  "v_cvt_pk_bf16_f32 v232, v168, v169\n\t" \
  "v_cvt_pk_bf16_f32 v233, v170, v171\n\t" \
  "v_cvt_pk_bf16_f32 v234, v172, v173\n\t" \
  "v_cvt_pk_bf16_f32 v235, v174, v175\n\t" \
  "global_store_dwordx4 v1, v[232:235], %14 offset:-4096 sc1\n\t" \
  "v_fma_f32 v184, v184, %16, v100\n\t" \
  "v_fma_f32 v185, v185, %16, v100\n\t" \
  "v_fma_f32 v186, v186, %16, v100\n\t" \
  "v_fma_f32 v187, v187, %16, v100\n\t" \
  "v_exp_f32 v184, v184\n\t" \
  "v_exp_f32 v185, v185\n\t" \
  "v_exp_f32 v186, v186\n\t" \
  "v_exp_f32 v187, v187\n\t" \
  "v_cvt_pk_bf16_f32 v188, v184, v185\n\t" \
  "v_cvt_pk_bf16_f32 v189, v186, v187\n\t" \
  "v_bfi_b32 v190, v46, 0, v188\n\t" \
  "v_bfi_b32 v191, v47, 0, v189\n\t" \
  "v_rcp_f32 v102, v192\n\t" \
  "v_and_b32 v188, v46, v188\n\t" \
  "v_and_b32 v189, v47, v189\n\t" \
  "v_pk_mul_f32 v[160:161], v[160:161], v[102:103] op_sel_hi:[1,0]\n\t" \
  "v_pk_mul_f32 v[162:163], v[162:163], v[102:103] op_sel_hi:[1,0]\n\t" \
  "v_mfma_f32_16x16x32_bf16 v[184:187], v[48:51], v[188:191], 0\n\t" \
  "v_pk_mul_f32 v[164:165], v[164:165], v[102:103] op_sel_hi:[1,0]\n\t" \
  "v_pk_mul_f32 v[166:167], v[166:167], v[102:103] op_sel_hi:[1,0]\n\t" \
  "v_mfma_f32_16x16x32_bf16 v[152:155], v[70:73], v[188:191], 0\n\t" \
  "v_cvt_pk_bf16_f32 v224, v160, v161\n\t" \
  "v_cvt_pk_bf16_f32 v225, v162, v163\n\t" \
  "v_mfma_f32_16x16x32_bf16 v[156:159], v[80:83], v[188:191], 0\n\t" \
  "v_cvt_pk_bf16_f32 v226, v164, v165\n\t" \
  "v_cvt_pk_bf16_f32 v227, v166, v167\n\t" \
  "global_store_dwordx4 v4, v[224:227], %14 offset:0 sc1\n\t" \
  "v_rcp_f32 v100, v184\n\t" \
  "ds_read_b128 v[60:63], v56 offset:64\n\t" \
  "ds_read_b64_tr_b16 v[108:109], v5 offset:0\n\t" \
  "ds_read_b64_tr_b16 v[110:111], v5 offset:16\n\t" \
  "ds_read_b128 v[112:115], v6 offset:16384\n\t" \
  "ds_read_b128 v[116:119], v6 offset:15360\n\t" \
  "ds_read_b64_tr_b16 v[120:121], v5 offset:512\n\t" \
  "ds_read_b64_tr_b16 v[122:123], v5 offset:528\n\t" \
  "ds_read_b128 v[124:127], v6 offset:14336\n\t" \
  "ds_read_b64_tr_b16 v[128:129], v5 offset:1024\n\t" \
  "ds_read_b64_tr_b16 v[130:131], v5 offset:1040\n\t" \
  "ds_read_b128 v[132:135], v6 offset:13312\n\t" \
  "ds_read_b64_tr_b16 v[136:137], v5 offset:1536\n\t" \
  "ds_read_b64_tr_b16 v[138:139], v5 offset:1552\n\t" \
  "ds_read_b128 v[140:143], v6 offset:12288\n\t" \
  "ds_read_b64_tr_b16 v[144:145], v5 offset:2048\n\t" \
  "v_pk_mul_f32 v[152:153], v[152:153], v[100:101] op_sel_hi:[1,0]\n\t" \
  "v_pk_mul_f32 v[154:155], v[154:155], v[100:101] op_sel_hi:[1,0]\n\t" \
  "v_cvt_pk_bf16_f32 v216, v152, v153\n\t" \
  "v_pk_mul_f32 v[156:157], v[156:157], v[100:101] op_sel_hi:[1,0]\n\t" \
  "v_pk_mul_f32 v[158:159], v[158:159], v[100:101] op_sel_hi:[1,0]\n\t" \
  "v_cvt_pk_bf16_f32 v217, v154, v155\n\t" \
  "v_cvt_pk_bf16_f32 v218, v156, v157\n\t" \
  "v_cvt_pk_bf16_f32 v219, v158, v159\n\t" \
  "global_store_dwordx4 v4, v[216:219], %14 offset:-4096 sc1\n\t" \
  "ds_write_b32 v32, v0 offset:112\n\t" \
  "s_branch 9f\n\t" \
  "5:\n\t" \
  "ds_read_b32 v52, v33 offset:48\n\t" \
  "ds_read_b128 v[10:13], v31\n\t" \
  "s_waitcnt lgkmcnt(0)\n\t" \
  "v_readfirstlane_b32 vcc_lo, v52\n\t" \
  "s_nop 1\n\t" \
  "s_cmp_eq_u32 vcc_lo, 0\n\t" \
  "s_cbranch_scc1 5b\n\t" \
  "s_branch 4b\n\t" \
  "15:\n\t" \
  "ds_read_b128 v[10:13], v14 offset:128\n\t" \
  "s_waitcnt lgkmcnt(0)\n\t" \
  "v_and_b32 v10, v10, v11\n\t" \
  "v_and_b32 v12, v12, v13\n\t" \
  "v_and_b32 v10, v10, v12\n\t" \
  "s_nop 1\n\t" \
  "v_readfirstlane_b32 vcc_lo, v10\n\t" \
  "s_nop 1\n\t" \
  "s_cmp_eq_u32 vcc_lo, 0\n\t" \
  "s_cbranch_scc1 15b\n\t" \
  "s_branch 14b\n\t" \
  "8:\n\t" \
  "global_load_dwordx2 v[10:11], v51, %11\n\t" \
  "global_load_dwordx2 v[12:13], v51, %11 offset:128\n\t" \
  "v_xor_b32 v53, 64, v52\n\t" \
  "v_xor_b32 v55, 32, v54\n\t" \
  "v_add_u32 v56, 43008, v52\n\t" \
  "v_add_u32 v58, 43008, v54\n\t" \
  "v_xor_b32 v57, 64, v56\n\t" \
  "v_xor_b32 v59, 32, v58\n\t" \
  "v_add_u32 v60, 86016, v52\n\t" \
  "v_add_u32 v62, 86016, v54\n\t" \
  "v_xor_b32 v61, 64, v60\n\t" \
  "v_xor_b32 v63, 32, v62\n\t" \
  "s_mov_b32 vcc_hi, 0x05040100\n\t" \
  "s_waitcnt vmcnt(2)\n\t" \
  "s_barrier\n\t" \
  "ds_read_b64_tr_b16 v[64:65], v62 offset:34816\n\t" \
  "ds_read_b64_tr_b16 v[66:67], v62 offset:35328\n\t" \
  "ds_read_b128 v[68:71], v60 offset:2048\n\t" \
  "ds_read_b64_tr_b16 v[72:73], v62 offset:38912\n\t" \
  "ds_read_b64_tr_b16 v[74:75], v62 offset:39424\n\t" \
  "ds_read_b128 v[76:79], v61 offset:2048\n\t" \
  "ds_read_b128 v[80:83], v61 offset:4096\n\t" \
  "ds_read_b128 v[84:87], v60 offset:4096\n\t" \
  "ds_read_b128 v[88:91], v60 offset:6144\n\t" \
  "ds_read_b128 v[92:95], v61 offset:6144\n\t" \
  "ds_read_b128 v[96:99], v61 offset:8192\n\t" \
  "ds_read_b128 v[100:103], v60 offset:8192\n\t" \
  "ds_read_b64_tr_b16 v[104:105], v63 offset:34816\n\t" \
  "ds_read_b64_tr_b16 v[106:107], v63 offset:35328\n\t" \
  "ds_read_b64_tr_b16 v[108:109], v63 offset:38912\n\t" \
  "ds_read_b64_tr_b16 v[110:111], v63 offset:39424\n\t" \
  "s_waitcnt lgkmcnt(13)\n\t" \
  "v_mfma_f32_16x16x32_bf16 v[216:219], v[68:71], v[64:67], 0\n\t" \
  "ds_read_b64_tr_b16 v[112:113], v62 offset:26624\n\t" \
  "ds_read_b64_tr_b16 v[114:115], v62 offset:27136\n\t" \
  "s_waitcnt lgkmcnt(12)\n\t" \
  "v_mfma_f32_16x16x32_bf16 v[216:219], v[76:79], v[72:75], v[216:219]\n\t" \
  "ds_read_b64_tr_b16 v[116:117], v62 offset:30720\n\t" \
  "ds_read_b64_tr_b16 v[118:119], v62 offset:31232\n\t" \
  "s_waitcnt lgkmcnt(13)\n\t" \
  "v_mfma_f32_16x16x32_bf16 v[224:227], v[80:83], v[72:75], 0\n\t" \
  "ds_read_b128 v[120:123], v60 offset:0\n\t" \
  "s_waitcnt lgkmcnt(13)\n\t" \
  "v_mfma_f32_16x16x32_bf16 v[224:227], v[84:87], v[64:67], v[224:227]\n\t" \
  "ds_read_b128 v[124:127], v61 offset:0\n\t" \
  "s_waitcnt lgkmcnt(13)\n\t" \
  "v_mfma_f32_16x16x32_bf16 v[232:235], v[88:91], v[64:67], 0\n\t" \
  "ds_read_b64_tr_b16 v[128:129], v63 offset:26624\n\t" \
  "s_waitcnt lgkmcnt(13)\n\t" \
  "v_mfma_f32_16x16x32_bf16 v[232:235], v[92:95], v[72:75], v[232:235]\n\t" \
  "ds_read_b64_tr_b16 v[130:131], v63 offset:27136\n\t" \
  "s_waitcnt lgkmcnt(13)\n\t" \
  "v_mfma_f32_16x16x32_bf16 v[240:243], v[96:99], v[72:75], 0\n\t" \
  "ds_read_b64_tr_b16 v[132:133], v63 offset:30720\n\t" \
  "s_waitcnt lgkmcnt(9)\n\t" \
  "v_mfma_f32_16x16x32_bf16 v[240:243], v[100:103], v[64:67], v[240:243]\n\t" \
  "ds_read_b64_tr_b16 v[134:135], v63 offset:31232\n\t" \
  "v_mfma_f32_16x16x32_bf16 v[220:223], v[68:71], v[104:107], 0\n\t" \
  "ds_read_b64_tr_b16 v[136:137], v62 offset:18432\n\t" \
  "v_mfma_f32_16x16x32_bf16 v[220:223], v[76:79], v[108:111], v[220:223]\n\t" \
  "ds_read_b64_tr_b16 v[138:139], v62 offset:18944\n\t" \
  "v_mfma_f32_16x16x32_bf16 v[228:231], v[80:83], v[108:111], 0\n\t" \
  "ds_read_b64_tr_b16 v[140:141], v62 offset:22528\n\t" \
  "v_mfma_f32_16x16x32_bf16 v[228:231], v[84:87], v[104:107], v[228:231]\n\t" \
  "ds_read_b64_tr_b16 v[142:143], v62 offset:23040\n\t" \
  "v_mfma_f32_16x16x32_bf16 v[236:239], v[88:91], v[104:107], 0\n\t" \
  "ds_read_b64_tr_b16 v[144:145], v63 offset:18432\n\t" \
  "v_mfma_f32_16x16x32_bf16 v[236:239], v[92:95], v[108:111], v[236:239]\n\t" \
  "ds_read_b64_tr_b16 v[146:147], v63 offset:18944\n\t" \
  "v_mfma_f32_16x16x32_bf16 v[244:247], v[96:99], v[108:111], 0\n\t" \
  "ds_read_b64_tr_b16 v[148:149], v63 offset:22528\n\t" \
  "v_mfma_f32_16x16x32_bf16 v[244:247], v[100:103], v[104:107], v[244:247]\n\t" \
  "ds_read_b64_tr_b16 v[150:151], v63 offset:23040\n\t" \
  "s_waitcnt lgkmcnt(7)\n\t" \
  "v_mfma_f32_16x16x32_bf16 v[184:187], v[112:115], v[68:71], 0\n\t" \
  "v_mfma_f32_16x16x32_bf16 v[184:187], v[116:119], v[76:79], v[184:187]\n\t" \
  "v_mfma_f32_16x16x32_bf16 v[192:195], v[116:119], v[80:83], 0\n\t" \
  "v_mfma_f32_16x16x32_bf16 v[192:195], v[112:115], v[84:87], v[192:195]\n\t" \
  "v_mfma_f32_16x16x32_bf16 v[200:203], v[112:115], v[88:91], 0\n\t" \
  "v_mfma_f32_16x16x32_bf16 v[200:203], v[116:119], v[92:95], v[200:203]\n\t" \
  "v_mfma_f32_16x16x32_bf16 v[208:211], v[116:119], v[96:99], 0\n\t" \
  "v_mfma_f32_16x16x32_bf16 v[208:211], v[112:115], v[100:103], v[208:211]\n\t" \
  "v_mfma_f32_16x16x32_bf16 v[248:251], v[112:115], v[120:123], 0\n\t" \
  "v_mfma_f32_16x16x32_bf16 v[248:251], v[116:119], v[124:127], v[248:251]\n\t" \
  "v_mfma_f32_16x16x32_bf16 v[252:255], v[128:131], v[120:123], 0\n\t" \
  "v_mfma_f32_16x16x32_bf16 v[252:255], v[132:135], v[124:127], v[252:255]\n\t" \
  "v_mfma_f32_16x16x32_bf16 v[188:191], v[132:135], v[76:79], 0\n\t" \
  "v_mfma_f32_16x16x32_bf16 v[188:191], v[128:131], v[68:71], v[188:191]\n\t" \
  "s_waitcnt lgkmcnt(0)\n\t" \
  "s_getpc_b64 s[88:89]\n\t" \
  "s_branch 28b\n\t" \
  "s_barrier\n\t" \
  "v_mfma_f32_16x16x32_bf16 v[196:199], v[128:131], v[84:87], 0\n\t" \
  "ds_read_b64_tr_b16 v[72:73], v54 offset:34816\n\t" \
  "v_mfma_f32_16x16x32_bf16 v[196:199], v[132:135], v[80:83], v[196:199]\n\t" \
  "ds_read_b64_tr_b16 v[74:75], v54 offset:35328\n\t" \
  "v_mfma_f32_16x16x32_bf16 v[204:207], v[132:135], v[92:95], 0\n\t" \
  "ds_read_b128 v[64:67], v52 offset:2048\n\t" \
  "v_mfma_f32_16x16x32_bf16 v[204:207], v[128:131], v[88:91], v[204:207]\n\t" \
  "ds_read_b64_tr_b16 v[108:109], v54 offset:38912\n\t" \
  "v_mfma_f32_16x16x32_bf16 v[212:215], v[128:131], v[100:103], 0\n\t" \
  "ds_read_b64_tr_b16 v[110:111], v54 offset:39424\n\t" \
  "v_mfma_f32_16x16x32_bf16 v[212:215], v[132:135], v[96:99], v[212:215]\n\t" \
  "ds_read_b128 v[104:107], v53 offset:2048\n\t" \
  "v_mfma_f32_16x16x32_bf16 v[152:155], v[136:139], v[68:71], 0\n\t" \
  "ds_read_b128 v[112:115], v53 offset:4096\n\t" \
  "v_mfma_f32_16x16x32_bf16 v[152:155], v[140:143], v[76:79], v[152:155]\n\t" \
  "ds_read_b128 v[116:119], v52 offset:4096\n\t" \
  "v_mfma_f32_16x16x32_bf16 v[160:163], v[140:143], v[80:83], 0\n\t" \
  "ds_read_b128 v[120:123], v52 offset:6144\n\t" \
  "v_mfma_f32_16x16x32_bf16 v[160:163], v[136:139], v[84:87], v[160:163]\n\t" \
  "ds_read_b128 v[124:127], v53 offset:6144\n\t" \
  "v_mfma_f32_16x16x32_bf16 v[168:171], v[136:139], v[88:91], 0\n\t" \
  "ds_read_b128 v[128:131], v53 offset:8192\n\t" \
  "v_mfma_f32_16x16x32_bf16 v[168:171], v[140:143], v[92:95], v[168:171]\n\t" \
  "ds_read_b128 v[132:135], v52 offset:8192\n\t" \
  "v_mfma_f32_16x16x32_bf16 v[176:179], v[140:143], v[96:99], 0\n\t" \
  "ds_read_b64_tr_b16 v[140:141], v55 offset:34816\n\t" \
  "v_mfma_f32_16x16x32_bf16 v[176:179], v[136:139], v[100:103], v[176:179]\n\t" \
  "ds_read_b64_tr_b16 v[142:143], v55 offset:35328\n\t" \
  "v_mfma_f32_16x16x32_bf16 v[156:159], v[144:147], v[68:71], 0\n\t" \
  "ds_read_b64_tr_b16 v[136:137], v55 offset:38912\n\t" \
  "v_mfma_f32_16x16x32_bf16 v[156:159], v[148:151], v[76:79], v[156:159]\n\t" \
  "ds_read_b64_tr_b16 v[138:139], v55 offset:39424\n\t" \
  "v_mfma_f32_16x16x32_bf16 v[164:167], v[148:151], v[80:83], 0\n\t" \
  "ds_read_b64_tr_b16 v[68:69], v54 offset:26624\n\t" \
  "v_mfma_f32_16x16x32_bf16 v[164:167], v[144:147], v[84:87], v[164:167]\n\t" \
  "ds_read_b64_tr_b16 v[70:71], v54 offset:27136\n\t" \
  "v_mfma_f32_16x16x32_bf16 v[172:175], v[144:147], v[88:91], 0\n\t" \
  "ds_read_b64_tr_b16 v[76:77], v54 offset:30720\n\t" \
  "v_mfma_f32_16x16x32_bf16 v[172:175], v[148:151], v[92:95], v[172:175]\n\t" \
  "ds_read_b64_tr_b16 v[78:79], v54 offset:31232\n\t" \
  "v_mfma_f32_16x16x32_bf16 v[180:183], v[148:151], v[96:99], 0\n\t" \
  "ds_read_b128 v[80:83], v52 offset:0\n\t" \
  "v_mfma_f32_16x16x32_bf16 v[180:183], v[144:147], v[100:103], v[180:183]\n\t" \
  "ds_read_b128 v[84:87], v53 offset:0\n\t" \
  "s_waitcnt lgkmcnt(7)\n\t" \
  "v_mfma_f32_16x16x32_bf16 v[216:219], v[64:67], v[72:75], v[216:219]\n\t" \
  "ds_read_b64_tr_b16 v[88:89], v55 offset:26624\n\t" \
  "v_mfma_f32_16x16x32_bf16 v[216:219], v[104:107], v[108:111], v[216:219]\n\t" \
  "ds_read_b64_tr_b16 v[90:91], v55 offset:27136\n\t" \
  "v_mfma_f32_16x16x32_bf16 v[224:227], v[112:115], v[108:111], v[224:227]\n\t" \
  "ds_read_b64_tr_b16 v[92:93], v55 offset:30720\n\t" \
  "v_mfma_f32_16x16x32_bf16 v[224:227], v[116:119], v[72:75], v[224:227]\n\t" \
  "ds_read_b64_tr_b16 v[94:95], v55 offset:31232\n\t" \
  "v_mfma_f32_16x16x32_bf16 v[232:235], v[120:123], v[72:75], v[232:235]\n\t" \
  "ds_read_b64_tr_b16 v[96:97], v54 offset:18432\n\t" \
  "v_mfma_f32_16x16x32_bf16 v[232:235], v[124:127], v[108:111], v[232:235]\n\t" \
  "ds_read_b64_tr_b16 v[98:99], v54 offset:18944\n\t" \
  "v_mfma_f32_16x16x32_bf16 v[240:243], v[128:131], v[108:111], v[240:243]\n\t" \
  "ds_read_b64_tr_b16 v[148:149], v54 offset:22528\n\t" \
  "v_mfma_f32_16x16x32_bf16 v[240:243], v[132:135], v[72:75], v[240:243]\n\t" \
  "ds_read_b64_tr_b16 v[150:151], v54 offset:23040\n\t" \
  "v_mfma_f32_16x16x32_bf16 v[220:223], v[64:67], v[140:143], v[220:223]\n\t" \
  "ds_read_b64_tr_b16 v[100:101], v55 offset:18432\n\t" \
  "s_waitcnt lgkmcnt(7)\n\t" \
  "v_mfma_f32_16x16x32_bf16 v[220:223], v[104:107], v[136:139], v[220:223]\n\t" \
  "ds_read_b64_tr_b16 v[102:103], v55 offset:18944\n\t" \
  "v_mfma_f32_16x16x32_bf16 v[228:231], v[112:115], v[136:139], v[228:231]\n\t" \
  "ds_read_b64_tr_b16 v[144:145], v55 offset:22528\n\t" \
  "v_mfma_f32_16x16x32_bf16 v[228:231], v[116:119], v[140:143], v[228:231]\n\t" \
  "ds_read_b64_tr_b16 v[146:147], v55 offset:23040\n\t" \
  "v_mfma_f32_16x16x32_bf16 v[236:239], v[120:123], v[140:143], v[236:239]\n\t" \
  "v_mfma_f32_16x16x32_bf16 v[236:239], v[124:127], v[136:139], v[236:239]\n\t" \
  "v_mfma_f32_16x16x32_bf16 v[244:247], v[128:131], v[136:139], v[244:247]\n\t" \
  "v_mfma_f32_16x16x32_bf16 v[244:247], v[132:135], v[140:143], v[244:247]\n\t" \
  "v_mfma_f32_16x16x32_bf16 v[184:187], v[68:71], v[64:67], v[184:187]\n\t" \
  "v_mfma_f32_16x16x32_bf16 v[184:187], v[76:79], v[104:107], v[184:187]\n\t" \
  "v_mfma_f32_16x16x32_bf16 v[192:195], v[76:79], v[112:115], v[192:195]\n\t" \
  "v_mfma_f32_16x16x32_bf16 v[192:195], v[68:71], v[116:119], v[192:195]\n\t" \
  "v_mfma_f32_16x16x32_bf16 v[200:203], v[68:71], v[120:123], v[200:203]\n\t" \
  "v_mfma_f32_16x16x32_bf16 v[200:203], v[76:79], v[124:127], v[200:203]\n\t" \
  "v_mfma_f32_16x16x32_bf16 v[208:211], v[76:79], v[128:131], v[208:211]\n\t" \
  "v_mfma_f32_16x16x32_bf16 v[208:211], v[68:71], v[132:135], v[208:211]\n\t" \
  "v_mfma_f32_16x16x32_bf16 v[248:251], v[68:71], v[80:83], v[248:251]\n\t" \
  "v_mfma_f32_16x16x32_bf16 v[248:251], v[76:79], v[84:87], v[248:251]\n\t" \
  "v_mfma_f32_16x16x32_bf16 v[252:255], v[88:91], v[80:83], v[252:255]\n\t" \
  "s_waitcnt lgkmcnt(0)\n\t" \
  "v_mfma_f32_16x16x32_bf16 v[252:255], v[92:95], v[84:87], v[252:255]\n\t" \
  "v_mfma_f32_16x16x32_bf16 v[188:191], v[92:95], v[104:107], v[188:191]\n\t" \
  "v_mfma_f32_16x16x32_bf16 v[188:191], v[88:91], v[64:67], v[188:191]\n\t" \
  "s_barrier\n\t" \
  "v_mfma_f32_16x16x32_bf16 v[196:199], v[88:91], v[116:119], v[196:199]\n\t" \
  "ds_read_b64_tr_b16 v[108:109], v58 offset:34816\n\t" \
  "v_mfma_f32_16x16x32_bf16 v[196:199], v[92:95], v[112:115], v[196:199]\n\t" \
  "ds_read_b64_tr_b16 v[110:111], v58 offset:35328\n\t" \
  "v_mfma_f32_16x16x32_bf16 v[204:207], v[92:95], v[124:127], v[204:207]\n\t" \
  "ds_read_b128 v[72:75], v56 offset:2048\n\t" \
  "v_mfma_f32_16x16x32_bf16 v[204:207], v[88:91], v[120:123], v[204:207]\n\t" \
  "ds_read_b64_tr_b16 v[136:137], v58 offset:38912\n\t" \
  "v_mfma_f32_16x16x32_bf16 v[212:215], v[88:91], v[132:135], v[212:215]\n\t" \
  "ds_read_b64_tr_b16 v[138:139], v58 offset:39424\n\t" \
  "v_mfma_f32_16x16x32_bf16 v[212:215], v[92:95], v[128:131], v[212:215]\n\t" \
  "ds_read_b128 v[140:143], v57 offset:2048\n\t" \
  "v_mfma_f32_16x16x32_bf16 v[152:155], v[96:99], v[64:67], v[152:155]\n\t" \
  "ds_read_b128 v[68:71], v57 offset:4096\n\t" \
  "v_mfma_f32_16x16x32_bf16 v[152:155], v[148:151], v[104:107], v[152:155]\n\t" \
  "ds_read_b128 v[76:79], v56 offset:4096\n\t" \
  "v_mfma_f32_16x16x32_bf16 v[160:163], v[148:151], v[112:115], v[160:163]\n\t" \
  "ds_read_b128 v[80:83], v56 offset:6144\n\t" \
  "v_mfma_f32_16x16x32_bf16 v[160:163], v[96:99], v[116:119], v[160:163]\n\t" \
  "ds_read_b128 v[84:87], v57 offset:6144\n\t" \
  "v_mfma_f32_16x16x32_bf16 v[168:171], v[96:99], v[120:123], v[168:171]\n\t" \
  "ds_read_b128 v[88:91], v57 offset:8192\n\t" \
  "v_mfma_f32_16x16x32_bf16 v[168:171], v[148:151], v[124:127], v[168:171]\n\t" \
  "ds_read_b128 v[92:95], v56 offset:8192\n\t" \
  "v_mfma_f32_16x16x32_bf16 v[176:179], v[148:151], v[128:131], v[176:179]\n\t" \
  "ds_read_b64_tr_b16 v[148:149], v59 offset:34816\n\t" \
  "v_mfma_f32_16x16x32_bf16 v[176:179], v[96:99], v[132:135], v[176:179]\n\t" \
  "ds_read_b64_tr_b16 v[150:151], v59 offset:35328\n\t" \
  "v_mfma_f32_16x16x32_bf16 v[156:159], v[100:103], v[64:67], v[156:159]\n\t" \
  "ds_read_b64_tr_b16 v[96:97], v59 offset:38912\n\t" \
  "v_mfma_f32_16x16x32_bf16 v[156:159], v[144:147], v[104:107], v[156:159]\n\t" \
  "ds_read_b64_tr_b16 v[98:99], v59 offset:39424\n\t" \
  "v_mfma_f32_16x16x32_bf16 v[164:167], v[144:147], v[112:115], v[164:167]\n\t" \
  "ds_read_b64_tr_b16 v[64:65], v58 offset:26624\n\t" \
  "v_mfma_f32_16x16x32_bf16 v[164:167], v[100:103], v[116:119], v[164:167]\n\t" \
  "ds_read_b64_tr_b16 v[66:67], v58 offset:27136\n\t" \
  "v_mfma_f32_16x16x32_bf16 v[172:175], v[100:103], v[120:123], v[172:175]\n\t" \
  "ds_read_b64_tr_b16 v[104:105], v58 offset:30720\n\t" \
  "v_mfma_f32_16x16x32_bf16 v[172:175], v[144:147], v[124:127], v[172:175]\n\t" \
  "ds_read_b64_tr_b16 v[106:107], v58 offset:31232\n\t" \
  "v_mfma_f32_16x16x32_bf16 v[180:183], v[144:147], v[128:131], v[180:183]\n\t" \
  "ds_read_b128 v[112:115], v56 offset:0\n\t" \
  "v_mfma_f32_16x16x32_bf16 v[180:183], v[100:103], v[132:135], v[180:183]\n\t" \
  "ds_read_b128 v[116:119], v57 offset:0\n\t" \
  "s_waitcnt lgkmcnt(7)\n\t" \
  "v_mfma_f32_16x16x32_bf16 v[216:219], v[72:75], v[108:111], v[216:219]\n\t" \
  "ds_read_b64_tr_b16 v[120:121], v59 offset:26624\n\t" \
  "v_mfma_f32_16x16x32_bf16 v[216:219], v[140:143], v[136:139], v[216:219]\n\t" \
  "ds_read_b64_tr_b16 v[122:123], v59 offset:27136\n\t" \
  "v_mfma_f32_16x16x32_bf16 v[224:227], v[68:71], v[136:139], v[224:227]\n\t" \
  "ds_read_b64_tr_b16 v[124:125], v59 offset:30720\n\t" \
  "v_mfma_f32_16x16x32_bf16 v[224:227], v[76:79], v[108:111], v[224:227]\n\t" \
  "ds_read_b64_tr_b16 v[126:127], v59 offset:31232\n\t" \
  "v_mfma_f32_16x16x32_bf16 v[232:235], v[80:83], v[108:111], v[232:235]\n\t" \
  "ds_read_b64_tr_b16 v[128:129], v58 offset:18432\n\t" \
  "v_mfma_f32_16x16x32_bf16 v[232:235], v[84:87], v[136:139], v[232:235]\n\t" \
  "ds_read_b64_tr_b16 v[130:131], v58 offset:18944\n\t" \
  "v_mfma_f32_16x16x32_bf16 v[240:243], v[88:91], v[136:139], v[240:243]\n\t" \
  "ds_read_b64_tr_b16 v[144:145], v58 offset:22528\n\t" \
  "v_mfma_f32_16x16x32_bf16 v[240:243], v[92:95], v[108:111], v[240:243]\n\t" \
  "ds_read_b64_tr_b16 v[146:147], v58 offset:23040\n\t" \
  "v_mfma_f32_16x16x32_bf16 v[220:223], v[72:75], v[148:151], v[220:223]\n\t" \
  "ds_read_b64_tr_b16 v[100:101], v59 offset:18432\n\t" \
  "s_waitcnt lgkmcnt(7)\n\t" \
  "v_mfma_f32_16x16x32_bf16 v[220:223], v[140:143], v[96:99], v[220:223]\n\t" \
  "ds_read_b64_tr_b16 v[102:103], v59 offset:18944\n\t" \
  "v_mfma_f32_16x16x32_bf16 v[228:231], v[68:71], v[96:99], v[228:231]\n\t" \
  "ds_read_b64_tr_b16 v[132:133], v59 offset:22528\n\t" \
  "v_mfma_f32_16x16x32_bf16 v[228:231], v[76:79], v[148:151], v[228:231]\n\t" \
  "ds_read_b64_tr_b16 v[134:135], v59 offset:23040\n\t" \
  "v_mfma_f32_16x16x32_bf16 v[236:239], v[80:83], v[148:151], v[236:239]\n\t" \
  "v_mfma_f32_16x16x32_bf16 v[236:239], v[84:87], v[96:99], v[236:239]\n\t" \
  "v_mfma_f32_16x16x32_bf16 v[244:247], v[88:91], v[96:99], v[244:247]\n\t" \
  "v_mfma_f32_16x16x32_bf16 v[244:247], v[92:95], v[148:151], v[244:247]\n\t" \
  "503:\n\t" \
  "s_cbranch_execz 507f\n\t" \
  "v_mfma_f32_16x16x32_bf16 v[184:187], v[64:67], v[72:75], v[184:187]\n\t" \
  "v_mfma_f32_16x16x32_bf16 v[184:187], v[104:107], v[140:143], v[184:187]\n\t" \
  "v_mfma_f32_16x16x32_bf16 v[192:195], v[104:107], v[68:71], v[192:195]\n\t" \
  "v_mfma_f32_16x16x32_bf16 v[192:195], v[64:67], v[76:79], v[192:195]\n\t" \
  "v_mfma_f32_16x16x32_bf16 v[200:203], v[64:67], v[80:83], v[200:203]\n\t" \
  "v_mfma_f32_16x16x32_bf16 v[200:203], v[104:107], v[84:87], v[200:203]\n\t" \
  "v_mfma_f32_16x16x32_bf16 v[208:211], v[104:107], v[88:91], v[208:211]\n\t" \
  "v_mfma_f32_16x16x32_bf16 v[208:211], v[64:67], v[92:95], v[208:211]\n\t" \
  "v_mfma_f32_16x16x32_bf16 v[248:251], v[64:67], v[112:115], v[248:251]\n\t" \
  "v_mfma_f32_16x16x32_bf16 v[248:251], v[104:107], v[116:119], v[248:251]\n\t" \
  "v_mfma_f32_16x16x32_bf16 v[252:255], v[120:123], v[112:115], v[252:255]\n\t" \
  "s_waitcnt lgkmcnt(0)\n\t" \
  "v_mfma_f32_16x16x32_bf16 v[252:255], v[124:127], v[116:119], v[252:255]\n\t" \
  "v_mfma_f32_16x16x32_bf16 v[188:191], v[124:127], v[140:143], v[188:191]\n\t" \
  "v_mfma_f32_16x16x32_bf16 v[188:191], v[120:123], v[72:75], v[188:191]\n\t" \
  "s_barrier\n\t" \
  "v_mfma_f32_16x16x32_bf16 v[196:199], v[120:123], v[76:79], v[196:199]\n\t" \
  "ds_read_b64_tr_b16 v[136:137], v62 offset:34816\n\t" \
  "v_bfe_u32 v30, v4, 9, 1\n\t" \
  "v_mfma_f32_16x16x32_bf16 v[196:199], v[124:127], v[68:71], v[196:199]\n\t" \
  "ds_read_b64_tr_b16 v[138:139], v62 offset:35328\n\t" \
  "v_mad_i32_i24 v30, v30, -16, v0\n\t" \
  "v_mfma_f32_16x16x32_bf16 v[204:207], v[124:127], v[84:87], v[204:207]\n\t" \
  "ds_read_b128 v[108:111], v60 offset:2048\n\t" \
  "ds_read_b128 v[26:29], v0 offset:1552\n\t" \
  "v_mfma_f32_16x16x32_bf16 v[204:207], v[120:123], v[80:83], v[204:207]\n\t" \
  "ds_read_b64_tr_b16 v[96:97], v62 offset:38912\n\t" \
  "ds_read_b128 v[30:33], v30 offset:1040\n\t" \
  "v_mfma_f32_16x16x32_bf16 v[212:215], v[120:123], v[92:95], v[212:215]\n\t" \
  "ds_read_b64_tr_b16 v[98:99], v62 offset:39424\n\t" \
  "ds_read_b128 v[34:37], v0 offset:512\n\t" \
  "v_mfma_f32_16x16x32_bf16 v[212:215], v[124:127], v[88:91], v[212:215]\n\t" \
  "ds_read_b128 v[148:151], v61 offset:2048\n\t" \
  "ds_read_b64_tr_b16 v[38:39], v1 offset:512\n\t" \
  "v_mfma_f32_16x16x32_bf16 v[152:155], v[128:131], v[72:75], v[152:155]\n\t" \
  "ds_read_b128 v[64:67], v61 offset:4096\n\t" \
  "ds_read_b64_tr_b16 v[40:41], v1 offset:0\n\t" \
  "v_mfma_f32_16x16x32_bf16 v[152:155], v[144:147], v[140:143], v[152:155]\n\t" \
  "ds_read_b128 v[104:107], v60 offset:4096\n\t" \
  "s_waitcnt vmcnt(0)\n\t" \
  "v_mfma_f32_16x16x32_bf16 v[160:163], v[144:147], v[68:71], v[160:163]\n\t" \
  "ds_read_b128 v[112:115], v60 offset:6144\n\t" \
  "v_perm_b32 v18, v10, v10, v14\n\t" \
  "v_mfma_f32_16x16x32_bf16 v[160:163], v[128:131], v[76:79], v[160:163]\n\t" \
  "ds_read_b128 v[116:119], v61 offset:6144\n\t" \
  "v_perm_b32 v19, v10, v10, v15\n\t" \
  "v_mfma_f32_16x16x32_bf16 v[168:171], v[128:131], v[80:83], v[168:171]\n\t" \
  "ds_read_b128 v[120:123], v61 offset:8192\n\t" \
  "v_perm_b32 v20, v10, v10, v16\n\t" \
  "v_mfma_f32_16x16x32_bf16 v[168:171], v[144:147], v[84:87], v[168:171]\n\t" \
  "ds_read_b128 v[124:127], v60 offset:8192\n\t" \
  "v_perm_b32 v21, v10, v10, v17\n\t" \
  "v_mfma_f32_16x16x32_bf16 v[176:179], v[144:147], v[88:91], v[176:179]\n\t" \
  "ds_read_b64_tr_b16 v[144:145], v63 offset:34816\n\t" \
  "v_perm_b32 v22, v11, v11, v14\n\t" \
  "v_mfma_f32_16x16x32_bf16 v[176:179], v[128:131], v[92:95], v[176:179]\n\t" \
  "ds_read_b64_tr_b16 v[146:147], v63 offset:35328\n\t" \
  "v_perm_b32 v23, v11, v11, v15\n\t" \
  "v_mfma_f32_16x16x32_bf16 v[156:159], v[100:103], v[72:75], v[156:159]\n\t" \
  "ds_read_b64_tr_b16 v[128:129], v63 offset:38912\n\t" \
  "v_perm_b32 v24, v11, v11, v16\n\t" \
  "v_mfma_f32_16x16x32_bf16 v[156:159], v[132:135], v[140:143], v[156:159]\n\t" \
  "ds_read_b64_tr_b16 v[130:131], v63 offset:39424\n\t" \
  "v_perm_b32 v25, v11, v11, v17\n\t" \
  "v_mfma_f32_16x16x32_bf16 v[164:167], v[132:135], v[68:71], v[164:167]\n\t" \
  "ds_read_b64_tr_b16 v[72:73], v62 offset:26624\n\t" \
  "v_mfma_f32_16x16x32_bf16 v[164:167], v[100:103], v[76:79], v[164:167]\n\t" \
  "ds_read_b64_tr_b16 v[74:75], v62 offset:27136\n\t" \
  "v_mfma_f32_16x16x32_bf16 v[172:175], v[100:103], v[80:83], v[172:175]\n\t" \
  "ds_read_b64_tr_b16 v[140:141], v62 offset:30720\n\t" \
  "s_waitcnt lgkmcnt(6)\n\t" \
  "v_mfma_f32_16x16x32_bf16 v[42:45], v[26:29], v[18:21], 0\n\t" \
  "v_mfma_f32_16x16x32_bf16 v[42:45], v[30:33], v[22:25], v[42:45]\n\t" \
  "v_mfma_f32_16x16x32_bf16 v[46:49], v[30:33], v[18:21], 0\n\t" \
  "v_mfma_f32_16x16x32_bf16 v[46:49], v[34:37], v[22:25], v[46:49]\n\t" \
  "ds_read_b128 v[26:29], v0 offset:1808\n\t" \
  "ds_read_b128 v[30:33], v0 offset:1296\n\t" \
  "v_mfma_f32_16x16x32_bf16 v[172:175], v[132:135], v[84:87], v[172:175]\n\t" \
  "ds_read_b64_tr_b16 v[142:143], v62 offset:31232\n\t" \
  "ds_read_b128 v[34:37], v0 offset:768\n\t" \
  "v_mfma_f32_16x16x32_bf16 v[180:183], v[132:135], v[88:91], v[180:183]\n\t" \
  "ds_read_b128 v[68:71], v60 offset:0\n\t" \
  "v_mfma_f32_16x16x32_bf16 v[180:183], v[100:103], v[92:95], v[180:183]\n\t" \
  "ds_read_b128 v[76:79], v61 offset:0\n\t" \
  "v_mfma_f32_16x16x32_bf16 v[216:219], v[108:111], v[136:139], v[216:219]\n\t" \
  "ds_read_b64_tr_b16 v[80:81], v63 offset:26624\n\t" \
  "v_lshlrev_b32 v50, 16, v38\n\t" \
  "v_mfma_f32_16x16x32_bf16 v[216:219], v[148:151], v[96:99], v[216:219]\n\t" \
  "ds_read_b64_tr_b16 v[82:83], v63 offset:27136\n\t" \
  "v_lshlrev_b32 v51, 16, v40\n\t" \
  "v_mfma_f32_16x16x32_bf16 v[224:227], v[64:67], v[96:99], v[224:227]\n\t" \
  "ds_read_b64_tr_b16 v[84:85], v63 offset:30720\n\t" \
  "v_mul_f32 v42, v42, v50\n\t" \
  "v_mfma_f32_16x16x32_bf16 v[224:227], v[104:107], v[136:139], v[224:227]\n\t" \
  "ds_read_b64_tr_b16 v[86:87], v63 offset:31232\n\t" \
  "v_mul_f32 v46, v46, v51\n\t" \
  "v_mfma_f32_16x16x32_bf16 v[232:235], v[112:115], v[136:139], v[232:235]\n\t" \
  "ds_read_b64_tr_b16 v[88:89], v62 offset:18432\n\t" \
  "v_cvt_pk_bf16_f32 v42, v42, v46\n\t" \
  "v_mfma_f32_16x16x32_bf16 v[232:235], v[116:119], v[96:99], v[232:235]\n\t" \
  "ds_read_b64_tr_b16 v[90:91], v62 offset:18944\n\t" \
  "v_and_b32 v50, 0xffff0000, v38\n\t" \
  "v_mfma_f32_16x16x32_bf16 v[240:243], v[120:123], v[96:99], v[240:243]\n\t" \
  "ds_read_b64_tr_b16 v[132:133], v62 offset:22528\n\t" \
  "v_and_b32 v51, 0xffff0000, v40\n\t" \
  "v_mfma_f32_16x16x32_bf16 v[240:243], v[124:127], v[136:139], v[240:243]\n\t" \
  "ds_read_b64_tr_b16 v[134:135], v62 offset:23040\n\t" \
  "v_mul_f32 v43, v43, v50\n\t" \
  "s_waitcnt lgkmcnt(7)\n\t" \
  "v_mfma_f32_16x16x32_bf16 v[220:223], v[108:111], v[144:147], v[220:223]\n\t" \
  "ds_read_b64_tr_b16 v[92:93], v63 offset:18432\n\t" \
  "v_mul_f32 v47, v47, v51\n\t" \
  "v_mfma_f32_16x16x32_bf16 v[220:223], v[148:151], v[128:131], v[220:223]\n\t" \
  "ds_read_b64_tr_b16 v[94:95], v63 offset:18944\n\t" \
  "v_cvt_pk_bf16_f32 v43, v43, v47\n\t" \
  "v_mfma_f32_16x16x32_bf16 v[228:231], v[64:67], v[128:131], v[228:231]\n\t" \
  "ds_read_b64_tr_b16 v[100:101], v63 offset:22528\n\t" \
  "v_lshlrev_b32 v50, 16, v39\n\t" \
  "v_mfma_f32_16x16x32_bf16 v[228:231], v[104:107], v[144:147], v[228:231]\n\t" \
  "ds_read_b64_tr_b16 v[102:103], v63 offset:23040\n\t" \
  "v_lshlrev_b32 v51, 16, v41\n\t" \
  "v_mfma_f32_16x16x32_bf16 v[236:239], v[112:115], v[144:147], v[236:239]\n\t" \
  "v_mul_f32 v44, v44, v50\n\t" \
  "v_mul_f32 v48, v48, v51\n\t" \
  "v_mfma_f32_16x16x32_bf16 v[236:239], v[116:119], v[128:131], v[236:239]\n\t" \
  "v_cvt_pk_bf16_f32 v44, v44, v48\n\t" \
  "v_and_b32 v50, 0xffff0000, v39\n\t" \
  "v_mfma_f32_16x16x32_bf16 v[244:247], v[120:123], v[128:131], v[244:247]\n\t" \
  "v_and_b32 v51, 0xffff0000, v41\n\t" \
  "v_mul_f32 v45, v45, v50\n\t" \
  "v_mfma_f32_16x16x32_bf16 v[244:247], v[124:127], v[144:147], v[244:247]\n\t" \
  "v_mul_f32 v49, v49, v51\n\t" \
  "ds_read_b64_tr_b16 v[38:39], v1 offset:768\n\t" \
  "v_mfma_f32_16x16x32_bf16 v[184:187], v[72:75], v[108:111], v[184:187]\n\t" \
  "ds_read_b64_tr_b16 v[40:41], v1 offset:256\n\t" \
  "v_cvt_pk_bf16_f32 v45, v45, v49\n\t" \
  "v_mfma_f32_16x16x32_bf16 v[184:187], v[140:143], v[148:151], v[184:187]\n\t" \
  "v_mfma_f32_16x16x32_bf16 v[192:195], v[140:143], v[64:67], v[192:195]\n\t" \
  "v_mfma_f32_16x16x32_bf16 v[192:195], v[72:75], v[104:107], v[192:195]\n\t" \
  "v_mfma_f32_16x16x32_bf16 v[200:203], v[72:75], v[112:115], v[200:203]\n\t" \
  "v_mfma_f32_16x16x32_bf16 v[200:203], v[140:143], v[116:119], v[200:203]\n\t" \
  "v_mfma_f32_16x16x32_bf16 v[208:211], v[140:143], v[120:123], v[208:211]\n\t" \
  "v_mfma_f32_16x16x32_bf16 v[208:211], v[72:75], v[124:127], v[208:211]\n\t" \
  "v_mfma_f32_16x16x32_bf16 v[248:251], v[72:75], v[68:71], v[248:251]\n\t" \
  "v_mfma_f32_16x16x32_bf16 v[248:251], v[140:143], v[76:79], v[248:251]\n\t" \
  "s_waitcnt lgkmcnt(0)\n\t" \
  "v_mfma_f32_16x16x32_bf16 v[252:255], v[80:83], v[68:71], v[252:255]\n\t" \
  "v_mfma_f32_16x16x32_bf16 v[252:255], v[84:87], v[76:79], v[252:255]\n\t" \
  "v_mfma_f32_16x16x32_bf16 v[188:191], v[84:87], v[148:151], v[188:191]\n\t" \
  "v_mfma_f32_16x16x32_bf16 v[188:191], v[80:83], v[108:111], v[188:191]\n\t" \
  "s_barrier\n\t" \
  "v_mfma_f32_16x16x32_bf16 v[196:199], v[80:83], v[104:107], v[196:199]\n\t" \
  "ds_read_b64_tr_b16 v[96:97], v54 offset:34816\n\t" \
  "v_mfma_f32_16x16x32_bf16 v[196:199], v[84:87], v[64:67], v[196:199]\n\t" \
  "v_mov_b32 v50, 163344\n\t" \
  "ds_read_b64_tr_b16 v[98:99], v54 offset:35328\n\t" \
  "v_mfma_f32_16x16x32_bf16 v[204:207], v[84:87], v[116:119], v[204:207]\n\t" \
  "ds_read_b32 v50, v50 offset:24\n\t" \
  "ds_read_b128 v[136:139], v52 offset:2048\n\t" \
  "v_mfma_f32_16x16x32_bf16 v[204:207], v[80:83], v[112:115], v[204:207]\n\t" \
  "ds_read_b64_tr_b16 v[128:129], v54 offset:38912\n\t" \
  "v_mfma_f32_16x16x32_bf16 v[212:215], v[80:83], v[124:127], v[212:215]\n\t" \
  "ds_read_b64_tr_b16 v[130:131], v54 offset:39424\n\t" \
  "v_mfma_f32_16x16x32_bf16 v[212:215], v[84:87], v[120:123], v[212:215]\n\t" \
  "ds_read_b128 v[144:147], v53 offset:2048\n\t" \
  "v_mfma_f32_16x16x32_bf16 v[152:155], v[88:91], v[108:111], v[152:155]\n\t" \
  "ds_read_b128 v[72:75], v53 offset:4096\n\t" \
  "v_mfma_f32_16x16x32_bf16 v[152:155], v[132:135], v[148:151], v[152:155]\n\t" \
  "ds_read_b128 v[140:143], v52 offset:4096\n\t" \
  "v_mfma_f32_16x16x32_bf16 v[160:163], v[132:135], v[64:67], v[160:163]\n\t" \
  "ds_read_b128 v[68:71], v52 offset:6144\n\t" \
  "v_mfma_f32_16x16x32_bf16 v[160:163], v[88:91], v[104:107], v[160:163]\n\t" \
  "ds_read_b128 v[76:79], v53 offset:6144\n\t" \
  "v_mfma_f32_16x16x32_bf16 v[168:171], v[88:91], v[112:115], v[168:171]\n\t" \
  "ds_read_b128 v[80:83], v53 offset:8192\n\t" \
  "v_mfma_f32_16x16x32_bf16 v[168:171], v[132:135], v[116:119], v[168:171]\n\t" \
  "ds_read_b128 v[84:87], v52 offset:8192\n\t" \
  "v_mfma_f32_16x16x32_bf16 v[176:179], v[132:135], v[120:123], v[176:179]\n\t" \
  "s_waitcnt lgkmcnt(10)\n\t" \
  "v_readfirstlane_b32 s88, v50\n\t" \
  "s_mov_b32 s89, 0\n\t" \
  "ds_read_b64_tr_b16 v[132:133], v55 offset:34816\n\t" \
  "v_mfma_f32_16x16x32_bf16 v[176:179], v[88:91], v[124:127], v[176:179]\n\t" \
  "ds_read_b64_tr_b16 v[134:135], v55 offset:35328\n\t" \
  "v_mfma_f32_16x16x32_bf16 v[156:159], v[92:95], v[108:111], v[156:159]\n\t" \
  "s_xor_b64 %24, %24, s[88:89]\n\t" \
  "ds_read_b64_tr_b16 v[88:89], v55 offset:38912\n\t" \
  "v_mfma_f32_16x16x32_bf16 v[156:159], v[100:103], v[148:151], v[156:159]\n\t" \
  "s_xor_b64 %25, %25, s[88:89]\n\t" \
  "ds_read_b64_tr_b16 v[90:91], v55 offset:39424\n\t" \
  "v_mfma_f32_16x16x32_bf16 v[164:167], v[100:103], v[64:67], v[164:167]\n\t" \
  "s_xor_b64 %14, %14, s[88:89]\n\t" \
  "ds_read_b64_tr_b16 v[108:109], v54 offset:26624\n\t" \
  "v_mfma_f32_16x16x32_bf16 v[164:167], v[92:95], v[104:107], v[164:167]\n\t" \
  "s_xor_b64 %19, %19, s[88:89]\n\t" \
  "ds_read_b64_tr_b16 v[110:111], v54 offset:27136\n\t" \
  "v_mfma_f32_16x16x32_bf16 v[172:175], v[92:95], v[112:115], v[172:175]\n\t" \
  "s_xor_b64 %21, %21, s[88:89]\n\t" \
  "ds_read_b64_tr_b16 v[148:149], v54 offset:30720\n\t" \
  "v_mfma_f32_16x16x32_bf16 v[172:175], v[100:103], v[116:119], v[172:175]\n\t" \
  "s_xor_b64 s[92:93], s[92:93], s[88:89]\n\t" \
  "ds_read_b64_tr_b16 v[150:151], v54 offset:31232\n\t" \
  "v_mfma_f32_16x16x32_bf16 v[180:183], v[100:103], v[120:123], v[180:183]\n\t" \
  "ds_read_b128 v[64:67], v52 offset:0\n\t" \
  "v_mfma_f32_16x16x32_bf16 v[180:183], v[92:95], v[124:127], v[180:183]\n\t" \
  "ds_read_b128 v[104:107], v53 offset:0\n\t" \
  "s_waitcnt lgkmcnt(7)\n\t" \
  "v_mfma_f32_16x16x32_bf16 v[216:219], v[136:139], v[96:99], v[216:219]\n\t" \
  "ds_read_b64_tr_b16 v[112:113], v55 offset:26624\n\t" \
  "v_mfma_f32_16x16x32_bf16 v[216:219], v[144:147], v[128:131], v[216:219]\n\t" \
  "ds_read_b64_tr_b16 v[114:115], v55 offset:27136\n\t" \
  "v_mfma_f32_16x16x32_bf16 v[224:227], v[72:75], v[128:131], v[224:227]\n\t" \
  "ds_read_b64_tr_b16 v[116:117], v55 offset:30720\n\t" \
  "v_mfma_f32_16x16x32_bf16 v[224:227], v[140:143], v[96:99], v[224:227]\n\t" \
  "ds_read_b64_tr_b16 v[118:119], v55 offset:31232\n\t" \
  "v_mfma_f32_16x16x32_bf16 v[232:235], v[68:71], v[96:99], v[232:235]\n\t" \
  "ds_read_b64_tr_b16 v[100:101], v54 offset:18432\n\t" \
  "v_mfma_f32_16x16x32_bf16 v[232:235], v[76:79], v[128:131], v[232:235]\n\t" \
  "ds_read_b64_tr_b16 v[102:103], v54 offset:18944\n\t" \
  "v_mfma_f32_16x16x32_bf16 v[240:243], v[80:83], v[128:131], v[240:243]\n\t" \
  "ds_read_b64_tr_b16 v[120:121], v54 offset:22528\n\t" \
  "v_mfma_f32_16x16x32_bf16 v[240:243], v[84:87], v[96:99], v[240:243]\n\t" \
  "ds_read_b64_tr_b16 v[122:123], v54 offset:23040\n\t" \
  "v_mfma_f32_16x16x32_bf16 v[220:223], v[136:139], v[132:135], v[220:223]\n\t" \
  "ds_read_b64_tr_b16 v[92:93], v55 offset:18432\n\t" \
  "s_waitcnt lgkmcnt(7)\n\t" \
  "v_mfma_f32_16x16x32_bf16 v[220:223], v[144:147], v[88:91], v[220:223]\n\t" \
  "ds_read_b64_tr_b16 v[94:95], v55 offset:18944\n\t" \
  "v_mfma_f32_16x16x32_bf16 v[228:231], v[72:75], v[88:91], v[228:231]\n\t" \
  "ds_read_b64_tr_b16 v[124:125], v55 offset:22528\n\t" \
  "v_mfma_f32_16x16x32_bf16 v[228:231], v[140:143], v[132:135], v[228:231]\n\t" \
  "ds_read_b64_tr_b16 v[126:127], v55 offset:23040\n\t" \
  "v_mfma_f32_16x16x32_bf16 v[236:239], v[68:71], v[132:135], v[236:239]\n\t" \
  "v_mfma_f32_16x16x32_bf16 v[236:239], v[76:79], v[88:91], v[236:239]\n\t" \
  "v_mfma_f32_16x16x32_bf16 v[244:247], v[80:83], v[88:91], v[244:247]\n\t" \
  "v_mfma_f32_16x16x32_bf16 v[244:247], v[84:87], v[132:135], v[244:247]\n\t" \
  "v_mfma_f32_16x16x32_bf16 v[184:187], v[108:111], v[136:139], v[184:187]\n\t" \
  "v_mfma_f32_16x16x32_bf16 v[184:187], v[148:151], v[144:147], v[184:187]\n\t" \
  "v_mfma_f32_16x16x32_bf16 v[192:195], v[148:151], v[72:75], v[192:195]\n\t" \
  "v_mfma_f32_16x16x32_bf16 v[192:195], v[108:111], v[140:143], v[192:195]\n\t" \
  "v_mfma_f32_16x16x32_bf16 v[200:203], v[108:111], v[68:71], v[200:203]\n\t" \
  "global_store_dwordx4 v2, v[42:45], %24 offset:0 sc1\n\t" \
  "v_mfma_f32_16x16x32_bf16 v[200:203], v[148:151], v[76:79], v[200:203]\n\t" \
  "s_nop 0\n\t" \
  "v_mfma_f32_16x16x32_bf16 v[42:45], v[26:29], v[18:21], 0\n\t" \
  "v_mfma_f32_16x16x32_bf16 v[42:45], v[30:33], v[22:25], v[42:45]\n\t" \
  "v_mfma_f32_16x16x32_bf16 v[46:49], v[30:33], v[18:21], 0\n\t" \
  "v_mfma_f32_16x16x32_bf16 v[46:49], v[34:37], v[22:25], v[46:49]\n\t" \
  "v_bfe_u32 v30, v4, 9, 1\n\t" \
  "v_mad_i32_i24 v30, v30, -16, v0\n\t" \
  "v_mfma_f32_16x16x32_bf16 v[208:211], v[148:151], v[80:83], v[208:211]\n\t" \
  "ds_read_b128 v[26:29], v0 offset:1584\n\t" \
  "ds_read_b128 v[30:33], v30 offset:1072\n\t" \
  "v_mfma_f32_16x16x32_bf16 v[208:211], v[108:111], v[84:87], v[208:211]\n\t" \
  "ds_read_b128 v[34:37], v0 offset:544\n\t" \
  "v_perm_b32 v18, v12, v12, v14\n\t" \
  "v_mfma_f32_16x16x32_bf16 v[248:251], v[108:111], v[64:67], v[248:251]\n\t" \
  "v_perm_b32 v19, v12, v12, v15\n\t" \
  "v_perm_b32 v20, v12, v12, v16\n\t" \
  "v_mfma_f32_16x16x32_bf16 v[248:251], v[148:151], v[104:107], v[248:251]\n\t" \
  "v_perm_b32 v21, v12, v12, v17\n\t" \
  "v_perm_b32 v22, v13, v13, v14\n\t" \
  "v_mfma_f32_16x16x32_bf16 v[252:255], v[112:115], v[64:67], v[252:255]\n\t" \
  "v_perm_b32 v23, v13, v13, v15\n\t" \
  "v_perm_b32 v24, v13, v13, v16\n\t" \
  "s_waitcnt lgkmcnt(3)\n\t" \
  "v_mfma_f32_16x16x32_bf16 v[252:255], v[116:119], v[104:107], v[252:255]\n\t" \
  "v_perm_b32 v25, v13, v13, v17\n\t" \
  "v_lshlrev_b32 v50, 16, v38\n\t" \
  "v_mfma_f32_16x16x32_bf16 v[188:191], v[116:119], v[144:147], v[188:191]\n\t" \
  "v_lshlrev_b32 v51, 16, v40\n\t" \
  "v_mul_f32 v42, v42, v50\n\t" \
  "v_mfma_f32_16x16x32_bf16 v[188:191], v[112:115], v[136:139], v[188:191]\n\t" \
  "v_mul_f32 v46, v46, v51\n\t" \
  "v_cvt_pk_bf16_f32 v42, v42, v46\n\t" \
  "s_waitcnt lgkmcnt(0)\n\t" \
  "s_barrier\n\t" \
  "v_mfma_f32_16x16x32_bf16 v[196:199], v[112:115], v[140:143], v[196:199]\n\t" \
  "ds_read_b64_tr_b16 v[128:129], v58 offset:34816\n\t" \
  "v_and_b32 v50, 0xffff0000, v38\n\t" \
  "v_mfma_f32_16x16x32_bf16 v[196:199], v[116:119], v[72:75], v[196:199]\n\t" \
  "ds_read_b64_tr_b16 v[130:131], v58 offset:35328\n\t" \
  "v_and_b32 v51, 0xffff0000, v40\n\t" \
  "v_mfma_f32_16x16x32_bf16 v[204:207], v[116:119], v[76:79], v[204:207]\n\t" \
  "ds_read_b128 v[96:99], v56 offset:2048\n\t" \
  "v_mul_f32 v43, v43, v50\n\t" \
  "v_mfma_f32_16x16x32_bf16 v[204:207], v[112:115], v[68:71], v[204:207]\n\t" \
  "ds_read_b64_tr_b16 v[88:89], v58 offset:38912\n\t" \
  "v_mul_f32 v47, v47, v51\n\t" \
  "v_mfma_f32_16x16x32_bf16 v[212:215], v[112:115], v[84:87], v[212:215]\n\t" \
  "ds_read_b64_tr_b16 v[90:91], v58 offset:39424\n\t" \
  "v_cvt_pk_bf16_f32 v43, v43, v47\n\t" \
  "v_mfma_f32_16x16x32_bf16 v[212:215], v[116:119], v[80:83], v[212:215]\n\t" \
  "ds_read_b128 v[132:135], v57 offset:2048\n\t" \
  "v_lshlrev_b32 v50, 16, v39\n\t" \
  "v_mfma_f32_16x16x32_bf16 v[152:155], v[100:103], v[136:139], v[152:155]\n\t" \
  "ds_read_b128 v[108:111], v57 offset:4096\n\t" \
  "v_lshlrev_b32 v51, 16, v41\n\t" \
  "v_mfma_f32_16x16x32_bf16 v[152:155], v[120:123], v[144:147], v[152:155]\n\t" \
  "ds_read_b128 v[148:151], v56 offset:4096\n\t" \
  "v_mul_f32 v44, v44, v50\n\t" \
  "v_mfma_f32_16x16x32_bf16 v[160:163], v[120:123], v[72:75], v[160:163]\n\t" \
  "ds_read_b128 v[64:67], v56 offset:6144\n\t" \
  "v_mul_f32 v48, v48, v51\n\t" \
  "v_mfma_f32_16x16x32_bf16 v[160:163], v[100:103], v[140:143], v[160:163]\n\t" \
  "ds_read_b128 v[104:107], v57 offset:6144\n\t" \
  "v_cvt_pk_bf16_f32 v44, v44, v48\n\t" \
  "v_mfma_f32_16x16x32_bf16 v[168:171], v[100:103], v[68:71], v[168:171]\n\t" \
  "ds_read_b128 v[112:115], v57 offset:8192\n\t" \
  "v_and_b32 v50, 0xffff0000, v39\n\t" \
  "v_mfma_f32_16x16x32_bf16 v[168:171], v[120:123], v[76:79], v[168:171]\n\t" \
  "ds_read_b128 v[116:119], v56 offset:8192\n\t" \
  "v_and_b32 v51, 0xffff0000, v41\n\t" \
  "v_mfma_f32_16x16x32_bf16 v[176:179], v[120:123], v[80:83], v[176:179]\n\t" \
  "ds_read_b64_tr_b16 v[120:121], v59 offset:34816\n\t" \
  "v_mul_f32 v45, v45, v50\n\t" \
  "v_mfma_f32_16x16x32_bf16 v[176:179], v[100:103], v[84:87], v[176:179]\n\t" \
  "ds_read_b64_tr_b16 v[122:123], v59 offset:35328\n\t" \
  "v_mul_f32 v49, v49, v51\n\t" \
  "v_mfma_f32_16x16x32_bf16 v[156:159], v[92:95], v[136:139], v[156:159]\n\t" \
  "ds_read_b64_tr_b16 v[100:101], v59 offset:38912\n\t" \
  "ds_read_b64_tr_b16 v[38:39], v1 offset:544\n\t" \
  "v_mfma_f32_16x16x32_bf16 v[156:159], v[124:127], v[144:147], v[156:159]\n\t" \
  "ds_read_b64_tr_b16 v[102:103], v59 offset:39424\n\t" \
  "ds_read_b64_tr_b16 v[40:41], v1 offset:32\n\t" \
  "v_mfma_f32_16x16x32_bf16 v[164:167], v[124:127], v[72:75], v[164:167]\n\t" \
  "ds_read_b64_tr_b16 v[136:137], v58 offset:26624\n\t" \
  "v_cvt_pk_bf16_f32 v45, v45, v49\n\t" \
  "v_mfma_f32_16x16x32_bf16 v[164:167], v[92:95], v[140:143], v[164:167]\n\t" \
  "ds_read_b64_tr_b16 v[138:139], v58 offset:27136\n\t" \
  "v_mfma_f32_16x16x32_bf16 v[172:175], v[92:95], v[68:71], v[172:175]\n\t" \
  "ds_read_b64_tr_b16 v[144:145], v58 offset:30720\n\t" \
  "v_mfma_f32_16x16x32_bf16 v[172:175], v[124:127], v[76:79], v[172:175]\n\t" \
  "ds_read_b64_tr_b16 v[146:147], v58 offset:31232\n\t" \
  "v_mfma_f32_16x16x32_bf16 v[180:183], v[124:127], v[80:83], v[180:183]\n\t" \
  "ds_read_b128 v[72:75], v56 offset:0\n\t" \
  "v_mfma_f32_16x16x32_bf16 v[180:183], v[92:95], v[84:87], v[180:183]\n\t" \
  "ds_read_b128 v[140:143], v57 offset:0\n\t" \
  "s_waitcnt lgkmcnt(8)\n\t" \
  "v_mfma_f32_16x16x32_bf16 v[216:219], v[96:99], v[128:131], v[216:219]\n\t" \
  "ds_read_b64_tr_b16 v[68:69], v59 offset:26624\n\t" \
  "v_mfma_f32_16x16x32_bf16 v[216:219], v[132:135], v[88:91], v[216:219]\n\t" \
  "ds_read_b64_tr_b16 v[70:71], v59 offset:27136\n\t" \
  "v_mfma_f32_16x16x32_bf16 v[224:227], v[108:111], v[88:91], v[224:227]\n\t" \
  "ds_read_b64_tr_b16 v[76:77], v59 offset:30720\n\t" \
  "v_mfma_f32_16x16x32_bf16 v[224:227], v[148:151], v[128:131], v[224:227]\n\t" \
  "ds_read_b64_tr_b16 v[78:79], v59 offset:31232\n\t" \
  "v_mfma_f32_16x16x32_bf16 v[232:235], v[64:67], v[128:131], v[232:235]\n\t" \
  "ds_read_b64_tr_b16 v[80:81], v58 offset:18432\n\t" \
  "v_mfma_f32_16x16x32_bf16 v[232:235], v[104:107], v[88:91], v[232:235]\n\t" \
  "ds_read_b64_tr_b16 v[82:83], v58 offset:18944\n\t" \
  "v_mfma_f32_16x16x32_bf16 v[240:243], v[112:115], v[88:91], v[240:243]\n\t" \
  "ds_read_b64_tr_b16 v[124:125], v58 offset:22528\n\t" \
  "v_mfma_f32_16x16x32_bf16 v[240:243], v[116:119], v[128:131], v[240:243]\n\t" \
  "ds_read_b64_tr_b16 v[126:127], v58 offset:23040\n\t" \
  "v_mfma_f32_16x16x32_bf16 v[220:223], v[96:99], v[120:123], v[220:223]\n\t" \
  "ds_read_b64_tr_b16 v[84:85], v59 offset:18432\n\t" \
  "s_waitcnt lgkmcnt(7)\n\t" \
  "v_mfma_f32_16x16x32_bf16 v[220:223], v[132:135], v[100:103], v[220:223]\n\t" \
  "ds_read_b64_tr_b16 v[86:87], v59 offset:18944\n\t" \
  "v_mfma_f32_16x16x32_bf16 v[228:231], v[108:111], v[100:103], v[228:231]\n\t" \
  "ds_read_b64_tr_b16 v[92:93], v59 offset:22528\n\t" \
  "v_mfma_f32_16x16x32_bf16 v[228:231], v[148:151], v[120:123], v[228:231]\n\t" \
  "ds_read_b64_tr_b16 v[94:95], v59 offset:23040\n\t" \
  "v_mfma_f32_16x16x32_bf16 v[236:239], v[64:67], v[120:123], v[236:239]\n\t" \
  "v_mfma_f32_16x16x32_bf16 v[236:239], v[104:107], v[100:103], v[236:239]\n\t" \
  "v_mfma_f32_16x16x32_bf16 v[244:247], v[112:115], v[100:103], v[244:247]\n\t" \
  "v_mfma_f32_16x16x32_bf16 v[244:247], v[116:119], v[120:123], v[244:247]\n\t" \
  "v_mfma_f32_16x16x32_bf16 v[184:187], v[136:139], v[96:99], v[184:187]\n\t" \
  "v_mfma_f32_16x16x32_bf16 v[184:187], v[144:147], v[132:135], v[184:187]\n\t" \
  "v_mfma_f32_16x16x32_bf16 v[192:195], v[144:147], v[108:111], v[192:195]\n\t" \
  "v_mfma_f32_16x16x32_bf16 v[192:195], v[136:139], v[148:151], v[192:195]\n\t" \
  "v_mfma_f32_16x16x32_bf16 v[200:203], v[136:139], v[64:67], v[200:203]\n\t" \
  "global_store_dwordx4 v2, v[42:45], %24 offset:1024 sc1\n\t" \
  "v_add_u32 v2, 0x20000, v2\n\t" \
  "v_mfma_f32_16x16x32_bf16 v[200:203], v[144:147], v[104:107], v[200:203]\n\t" \
  "v_mfma_f32_16x16x32_bf16 v[208:211], v[144:147], v[112:115], v[208:211]\n\t" \
  "v_mfma_f32_16x16x32_bf16 v[42:45], v[26:29], v[18:21], 0\n\t" \
  "v_mfma_f32_16x16x32_bf16 v[42:45], v[30:33], v[22:25], v[42:45]\n\t" \
  "v_mfma_f32_16x16x32_bf16 v[46:49], v[30:33], v[18:21], 0\n\t" \
  "v_mfma_f32_16x16x32_bf16 v[46:49], v[34:37], v[22:25], v[46:49]\n\t" \
  "ds_read_b128 v[26:29], v0 offset:1840\n\t" \
  "ds_read_b128 v[30:33], v0 offset:1328\n\t" \
  "v_mfma_f32_16x16x32_bf16 v[208:211], v[136:139], v[116:119], v[208:211]\n\t" \
  "ds_read_b128 v[34:37], v0 offset:800\n\t" \
  "v_mfma_f32_16x16x32_bf16 v[248:251], v[136:139], v[72:75], v[248:251]\n\t" \
  "v_mfma_f32_16x16x32_bf16 v[248:251], v[144:147], v[140:143], v[248:251]\n\t" \
  "v_lshlrev_b32 v50, 16, v38\n\t" \
  "v_lshlrev_b32 v51, 16, v40\n\t" \
  "v_mfma_f32_16x16x32_bf16 v[252:255], v[68:71], v[72:75], v[252:255]\n\t" \
  "v_mul_f32 v42, v42, v50\n\t" \
  "v_mul_f32 v46, v46, v51\n\t" \
  "s_waitcnt lgkmcnt(3)\n\t" \
  "v_mfma_f32_16x16x32_bf16 v[252:255], v[76:79], v[140:143], v[252:255]\n\t" \
  "v_cvt_pk_bf16_f32 v42, v42, v46\n\t" \
  "v_and_b32 v50, 0xffff0000, v38\n\t" \
  "v_mfma_f32_16x16x32_bf16 v[188:191], v[76:79], v[132:135], v[188:191]\n\t" \
  "v_and_b32 v51, 0xffff0000, v40\n\t" \
  "v_mul_f32 v43, v43, v50\n\t" \
  "v_mfma_f32_16x16x32_bf16 v[188:191], v[68:71], v[96:99], v[188:191]\n\t" \
  "v_mul_f32 v47, v47, v51\n\t" \
  "v_cvt_pk_bf16_f32 v43, v43, v47\n\t" \
  "s_waitcnt lgkmcnt(0)\n\t" \
  "s_barrier\n\t" \
  "v_mfma_f32_16x16x32_bf16 v[196:199], v[68:71], v[148:151], v[196:199]\n\t" \
  "ds_read_b64_tr_b16 v[88:89], v62 offset:34816\n\t" \
  "v_lshlrev_b32 v50, 16, v39\n\t" \
  "v_mfma_f32_16x16x32_bf16 v[196:199], v[76:79], v[108:111], v[196:199]\n\t" \
  "ds_read_b64_tr_b16 v[90:91], v62 offset:35328\n\t" \
  "v_lshlrev_b32 v51, 16, v41\n\t" \
  "v_mfma_f32_16x16x32_bf16 v[204:207], v[76:79], v[104:107], v[204:207]\n\t" \
  "ds_read_b128 v[128:131], v60 offset:2048\n\t" \
  "v_mul_f32 v44, v44, v50\n\t" \
  "v_mfma_f32_16x16x32_bf16 v[204:207], v[68:71], v[64:67], v[204:207]\n\t" \
  "ds_read_b64_tr_b16 v[100:101], v62 offset:38912\n\t" \
  "v_mul_f32 v48, v48, v51\n\t" \
  "v_mfma_f32_16x16x32_bf16 v[212:215], v[68:71], v[116:119], v[212:215]\n\t" \
  "ds_read_b64_tr_b16 v[102:103], v62 offset:39424\n\t" \
  "v_cvt_pk_bf16_f32 v44, v44, v48\n\t" \
  "v_mfma_f32_16x16x32_bf16 v[212:215], v[76:79], v[112:115], v[212:215]\n\t" \
  "ds_read_b128 v[120:123], v61 offset:2048\n\t" \
  "v_and_b32 v50, 0xffff0000, v39\n\t" \
  "v_mfma_f32_16x16x32_bf16 v[152:155], v[80:83], v[96:99], v[152:155]\n\t" \
  "ds_read_b128 v[136:139], v61 offset:4096\n\t" \
  "v_and_b32 v51, 0xffff0000, v41\n\t" \
  "v_mfma_f32_16x16x32_bf16 v[152:155], v[124:127], v[132:135], v[152:155]\n\t" \
  "ds_read_b128 v[144:147], v60 offset:4096\n\t" \
  "v_mul_f32 v45, v45, v50\n\t" \
  "v_mfma_f32_16x16x32_bf16 v[160:163], v[124:127], v[108:111], v[160:163]\n\t" \
  "ds_read_b128 v[72:75], v60 offset:6144\n\t" \
  "v_mul_f32 v49, v49, v51\n\t" \
  "v_mfma_f32_16x16x32_bf16 v[160:163], v[80:83], v[148:151], v[160:163]\n\t" \
  "ds_read_b128 v[140:143], v61 offset:6144\n\t" \
  "ds_read_b64_tr_b16 v[38:39], v1 offset:800\n\t" \
  "v_mfma_f32_16x16x32_bf16 v[168:171], v[80:83], v[64:67], v[168:171]\n\t" \
  "ds_read_b128 v[68:71], v61 offset:8192\n\t" \
  "ds_read_b64_tr_b16 v[40:41], v1 offset:288\n\t" \
  "v_mfma_f32_16x16x32_bf16 v[168:171], v[124:127], v[104:107], v[168:171]\n\t" \
  "ds_read_b128 v[76:79], v60 offset:8192\n\t" \
  "v_cvt_pk_bf16_f32 v45, v45, v49\n\t" \
  "v_mfma_f32_16x16x32_bf16 v[176:179], v[124:127], v[112:115], v[176:179]\n\t" \
  "ds_read_b64_tr_b16 v[124:125], v63 offset:34816\n\t" \
  "v_mfma_f32_16x16x32_bf16 v[176:179], v[80:83], v[116:119], v[176:179]\n\t" \
  "ds_read_b64_tr_b16 v[126:127], v63 offset:35328\n\t" \
  "v_mfma_f32_16x16x32_bf16 v[156:159], v[84:87], v[96:99], v[156:159]\n\t" \
  "ds_read_b64_tr_b16 v[80:81], v63 offset:38912\n\t" \
  "v_mfma_f32_16x16x32_bf16 v[156:159], v[92:95], v[132:135], v[156:159]\n\t" \
  "ds_read_b64_tr_b16 v[82:83], v63 offset:39424\n\t" \
  "v_mfma_f32_16x16x32_bf16 v[164:167], v[92:95], v[108:111], v[164:167]\n\t" \
  "ds_read_b64_tr_b16 v[96:97], v62 offset:26624\n\t" \
  "v_mfma_f32_16x16x32_bf16 v[164:167], v[84:87], v[148:151], v[164:167]\n\t" \
  "ds_read_b64_tr_b16 v[98:99], v62 offset:27136\n\t" \
  "v_mfma_f32_16x16x32_bf16 v[172:175], v[84:87], v[64:67], v[172:175]\n\t" \
  "ds_read_b64_tr_b16 v[132:133], v62 offset:30720\n\t" \
  "v_mfma_f32_16x16x32_bf16 v[172:175], v[92:95], v[104:107], v[172:175]\n\t" \
  "ds_read_b64_tr_b16 v[134:135], v62 offset:31232\n\t" \
  "v_mfma_f32_16x16x32_bf16 v[180:183], v[92:95], v[112:115], v[180:183]\n\t" \
  "ds_read_b128 v[108:111], v60 offset:0\n\t" \
  "v_mfma_f32_16x16x32_bf16 v[180:183], v[84:87], v[116:119], v[180:183]\n\t" \
  "ds_read_b128 v[148:151], v61 offset:0\n\t" \
  "s_waitcnt lgkmcnt(7)\n\t" \
  "v_mfma_f32_16x16x32_bf16 v[216:219], v[128:131], v[88:91], v[216:219]\n\t" \
  "ds_read_b64_tr_b16 v[64:65], v63 offset:26624\n\t" \
  "v_mfma_f32_16x16x32_bf16 v[216:219], v[120:123], v[100:103], v[216:219]\n\t" \
  "ds_read_b64_tr_b16 v[66:67], v63 offset:27136\n\t" \
  "v_mfma_f32_16x16x32_bf16 v[224:227], v[136:139], v[100:103], v[224:227]\n\t" \
  "ds_read_b64_tr_b16 v[104:105], v63 offset:30720\n\t" \
  "v_mfma_f32_16x16x32_bf16 v[224:227], v[144:147], v[88:91], v[224:227]\n\t" \
  "ds_read_b64_tr_b16 v[106:107], v63 offset:31232\n\t" \
  "v_mfma_f32_16x16x32_bf16 v[232:235], v[72:75], v[88:91], v[232:235]\n\t" \
  "ds_read_b64_tr_b16 v[92:93], v62 offset:18432\n\t" \
  "v_mfma_f32_16x16x32_bf16 v[232:235], v[140:143], v[100:103], v[232:235]\n\t" \
  "ds_read_b64_tr_b16 v[94:95], v62 offset:18944\n\t" \
  "v_mfma_f32_16x16x32_bf16 v[240:243], v[68:71], v[100:103], v[240:243]\n\t" \
  "ds_read_b64_tr_b16 v[112:113], v62 offset:22528\n\t" \
  "v_mfma_f32_16x16x32_bf16 v[240:243], v[76:79], v[88:91], v[240:243]\n\t" \
  "ds_read_b64_tr_b16 v[114:115], v62 offset:23040\n\t" \
  "v_mfma_f32_16x16x32_bf16 v[220:223], v[128:131], v[124:127], v[220:223]\n\t" \
  "ds_read_b64_tr_b16 v[84:85], v63 offset:18432\n\t" \
  "s_waitcnt lgkmcnt(7)\n\t" \
  "v_mfma_f32_16x16x32_bf16 v[220:223], v[120:123], v[80:83], v[220:223]\n\t" \
  "ds_read_b64_tr_b16 v[86:87], v63 offset:18944\n\t" \
  "v_mfma_f32_16x16x32_bf16 v[228:231], v[136:139], v[80:83], v[228:231]\n\t" \
  "ds_read_b64_tr_b16 v[116:117], v63 offset:22528\n\t" \
  "v_mfma_f32_16x16x32_bf16 v[228:231], v[144:147], v[124:127], v[228:231]\n\t" \
  "ds_read_b64_tr_b16 v[118:119], v63 offset:23040\n\t" \
  "v_mfma_f32_16x16x32_bf16 v[236:239], v[72:75], v[124:127], v[236:239]\n\t" \
  "v_mfma_f32_16x16x32_bf16 v[236:239], v[140:143], v[80:83], v[236:239]\n\t" \
  "v_mfma_f32_16x16x32_bf16 v[244:247], v[68:71], v[80:83], v[244:247]\n\t" \
  "v_mfma_f32_16x16x32_bf16 v[244:247], v[76:79], v[124:127], v[244:247]\n\t" \
  "v_mfma_f32_16x16x32_bf16 v[184:187], v[96:99], v[128:131], v[184:187]\n\t" \
  "v_mfma_f32_16x16x32_bf16 v[184:187], v[132:135], v[120:123], v[184:187]\n\t" \
  "v_mfma_f32_16x16x32_bf16 v[192:195], v[132:135], v[136:139], v[192:195]\n\t" \
  "v_mfma_f32_16x16x32_bf16 v[192:195], v[96:99], v[144:147], v[192:195]\n\t" \
  "v_mfma_f32_16x16x32_bf16 v[200:203], v[96:99], v[72:75], v[200:203]\n\t" \
  "global_store_dwordx4 v2, v[42:45], %24 offset:0 sc1\n\t" \
  "v_mfma_f32_16x16x32_bf16 v[200:203], v[132:135], v[140:143], v[200:203]\n\t" \
  "s_nop 0\n\t" \
  "v_mfma_f32_16x16x32_bf16 v[42:45], v[26:29], v[18:21], 0\n\t" \
  "v_mfma_f32_16x16x32_bf16 v[42:45], v[30:33], v[22:25], v[42:45]\n\t" \
  "v_mfma_f32_16x16x32_bf16 v[46:49], v[30:33], v[18:21], 0\n\t" \
  "v_mfma_f32_16x16x32_bf16 v[46:49], v[34:37], v[22:25], v[46:49]\n\t" \
  "v_mfma_f32_16x16x32_bf16 v[208:211], v[132:135], v[68:71], v[208:211]\n\t" \
  "v_mfma_f32_16x16x32_bf16 v[208:211], v[96:99], v[76:79], v[208:211]\n\t" \
  "v_lshlrev_b32 v50, 16, v38\n\t" \
  "v_lshlrev_b32 v51, 16, v40\n\t" \
  "v_mfma_f32_16x16x32_bf16 v[248:251], v[96:99], v[108:111], v[248:251]\n\t" \
  "s_nop 0\n\t" \
  "v_mul_f32 v42, v42, v50\n\t" \
  "s_nop 0\n\t" \
  "v_mul_f32 v46, v46, v51\n\t" \
  "v_mfma_f32_16x16x32_bf16 v[248:251], v[132:135], v[148:151], v[248:251]\n\t" \
  "v_cvt_pk_bf16_f32 v42, v42, v46\n\t" \
  "v_and_b32 v50, 0xffff0000, v38\n\t" \
  "v_mfma_f32_16x16x32_bf16 v[252:255], v[64:67], v[108:111], v[252:255]\n\t" \
  "v_and_b32 v51, 0xffff0000, v40\n\t" \
  "v_mul_f32 v43, v43, v50\n\t" \
  "s_waitcnt lgkmcnt(0)\n\t" \
  "v_mfma_f32_16x16x32_bf16 v[252:255], v[104:107], v[148:151], v[252:255]\n\t" \
  "v_mul_f32 v47, v47, v51\n\t" \
  "v_cvt_pk_bf16_f32 v43, v43, v47\n\t" \
  "v_mfma_f32_16x16x32_bf16 v[188:191], v[104:107], v[120:123], v[188:191]\n\t" \
  "v_lshlrev_b32 v50, 16, v39\n\t" \
  "v_lshlrev_b32 v51, 16, v41\n\t" \
  "v_mfma_f32_16x16x32_bf16 v[188:191], v[64:67], v[128:131], v[188:191]\n\t" \
  "v_mul_f32 v44, v44, v50\n\t" \
  "v_mul_f32 v48, v48, v51\n\t" \
  "s_barrier\n\t" \
  "v_mfma_f32_16x16x32_bf16 v[196:199], v[64:67], v[144:147], v[196:199]\n\t" \
  "ds_read_b64_tr_b16 v[100:101], v54 offset:34816\n\t" \
  "v_cvt_pk_bf16_f32 v44, v44, v48\n\t" \
  "v_mfma_f32_16x16x32_bf16 v[196:199], v[104:107], v[136:139], v[196:199]\n\t" \
  "ds_read_b64_tr_b16 v[102:103], v54 offset:35328\n\t" \
  "v_and_b32 v50, 0xffff0000, v39\n\t" \
  "v_mfma_f32_16x16x32_bf16 v[204:207], v[104:107], v[140:143], v[204:207]\n\t" \
  "ds_read_b128 v[88:91], v52 offset:2048\n\t" \
  "v_and_b32 v51, 0xffff0000, v41\n\t" \
  "v_mfma_f32_16x16x32_bf16 v[204:207], v[64:67], v[72:75], v[204:207]\n\t" \
  "ds_read_b64_tr_b16 v[80:81], v54 offset:38912\n\t" \
  "v_mul_f32 v45, v45, v50\n\t" \
  "v_mfma_f32_16x16x32_bf16 v[212:215], v[64:67], v[76:79], v[212:215]\n\t" \
  "ds_read_b64_tr_b16 v[82:83], v54 offset:39424\n\t" \
  "v_mul_f32 v49, v49, v51\n\t" \
  "v_mfma_f32_16x16x32_bf16 v[212:215], v[104:107], v[68:71], v[212:215]\n\t" \
  "ds_read_b128 v[124:127], v53 offset:2048\n\t" \
  "v_cvt_pk_bf16_f32 v45, v45, v49\n\t" \
  "v_mfma_f32_16x16x32_bf16 v[152:155], v[92:95], v[128:131], v[152:155]\n\t" \
  "ds_read_b128 v[96:99], v53 offset:4096\n\t" \
  "v_mfma_f32_16x16x32_bf16 v[152:155], v[112:115], v[120:123], v[152:155]\n\t" \
  "ds_read_b128 v[132:135], v52 offset:4096\n\t" \
  "v_mfma_f32_16x16x32_bf16 v[160:163], v[112:115], v[136:139], v[160:163]\n\t" \
  "ds_read_b128 v[108:111], v52 offset:6144\n\t" \
  "504:\n\t" \
  "s_cbranch_execz 508f\n\t" \
  "v_mfma_f32_16x16x32_bf16 v[160:163], v[92:95], v[144:147], v[160:163]\n\t" \
  "ds_read_b128 v[148:151], v53 offset:6144\n\t" \
  "v_mfma_f32_16x16x32_bf16 v[168:171], v[92:95], v[72:75], v[168:171]\n\t" \
  "ds_read_b128 v[64:67], v53 offset:8192\n\t" \
  "v_mfma_f32_16x16x32_bf16 v[168:171], v[112:115], v[140:143], v[168:171]\n\t" \
  "ds_read_b128 v[104:107], v52 offset:8192\n\t" \
  "v_mfma_f32_16x16x32_bf16 v[176:179], v[112:115], v[68:71], v[176:179]\n\t" \
  "ds_read_b64_tr_b16 v[112:113], v55 offset:34816\n\t" \
  "v_mfma_f32_16x16x32_bf16 v[176:179], v[92:95], v[76:79], v[176:179]\n\t" \
  "ds_read_b64_tr_b16 v[114:115], v55 offset:35328\n\t" \
  "v_mfma_f32_16x16x32_bf16 v[156:159], v[84:87], v[128:131], v[156:159]\n\t" \
  "ds_read_b64_tr_b16 v[92:93], v55 offset:38912\n\t" \
  "v_mfma_f32_16x16x32_bf16 v[156:159], v[116:119], v[120:123], v[156:159]\n\t" \
  "ds_read_b64_tr_b16 v[94:95], v55 offset:39424\n\t" \
  "v_mfma_f32_16x16x32_bf16 v[164:167], v[116:119], v[136:139], v[164:167]\n\t" \
  "ds_read_b64_tr_b16 v[128:129], v54 offset:26624\n\t" \
  "v_mfma_f32_16x16x32_bf16 v[164:167], v[84:87], v[144:147], v[164:167]\n\t" \
  "ds_read_b64_tr_b16 v[130:131], v54 offset:27136\n\t" \
  "v_mfma_f32_16x16x32_bf16 v[172:175], v[84:87], v[72:75], v[172:175]\n\t" \
  "ds_read_b64_tr_b16 v[120:121], v54 offset:30720\n\t" \
  "v_mfma_f32_16x16x32_bf16 v[172:175], v[116:119], v[140:143], v[172:175]\n\t" \
  "ds_read_b64_tr_b16 v[122:123], v54 offset:31232\n\t" \
  "v_mfma_f32_16x16x32_bf16 v[180:183], v[116:119], v[68:71], v[180:183]\n\t" \
  "ds_read_b128 v[136:139], v52 offset:0\n\t" \
  "v_mfma_f32_16x16x32_bf16 v[180:183], v[84:87], v[76:79], v[180:183]\n\t" \
  "ds_read_b128 v[144:147], v53 offset:0\n\t" \
  "s_waitcnt lgkmcnt(7)\n\t" \
  "v_mfma_f32_16x16x32_bf16 v[216:219], v[88:91], v[100:103], v[216:219]\n\t" \
  "ds_read_b64_tr_b16 v[72:73], v55 offset:26624\n\t" \
  "v_mfma_f32_16x16x32_bf16 v[216:219], v[124:127], v[80:83], v[216:219]\n\t" \
  "ds_read_b64_tr_b16 v[74:75], v55 offset:27136\n\t" \
  "v_mfma_f32_16x16x32_bf16 v[224:227], v[96:99], v[80:83], v[224:227]\n\t" \
  "ds_read_b64_tr_b16 v[140:141], v55 offset:30720\n\t" \
  "v_mfma_f32_16x16x32_bf16 v[224:227], v[132:135], v[100:103], v[224:227]\n\t" \
  "ds_read_b64_tr_b16 v[142:143], v55 offset:31232\n\t" \
  "v_mfma_f32_16x16x32_bf16 v[232:235], v[108:111], v[100:103], v[232:235]\n\t" \
  "ds_read_b64_tr_b16 v[68:69], v54 offset:18432\n\t" \
  "v_mfma_f32_16x16x32_bf16 v[232:235], v[148:151], v[80:83], v[232:235]\n\t" \
  "ds_read_b64_tr_b16 v[70:71], v54 offset:18944\n\t" \
  "v_mfma_f32_16x16x32_bf16 v[240:243], v[64:67], v[80:83], v[240:243]\n\t" \
  "ds_read_b64_tr_b16 v[116:117], v54 offset:22528\n\t" \
  "v_mfma_f32_16x16x32_bf16 v[240:243], v[104:107], v[100:103], v[240:243]\n\t" \
  "ds_read_b64_tr_b16 v[118:119], v54 offset:23040\n\t" \
  "v_mfma_f32_16x16x32_bf16 v[220:223], v[88:91], v[112:115], v[220:223]\n\t" \
  "ds_read_b64_tr_b16 v[76:77], v55 offset:18432\n\t" \
  "s_waitcnt lgkmcnt(7)\n\t" \
  "v_mfma_f32_16x16x32_bf16 v[220:223], v[124:127], v[92:95], v[220:223]\n\t" \
  "ds_read_b64_tr_b16 v[78:79], v55 offset:18944\n\t" \
  "v_mfma_f32_16x16x32_bf16 v[228:231], v[96:99], v[92:95], v[228:231]\n\t" \
  "ds_read_b64_tr_b16 v[84:85], v55 offset:22528\n\t" \
  "v_mfma_f32_16x16x32_bf16 v[228:231], v[132:135], v[112:115], v[228:231]\n\t" \
  "ds_read_b64_tr_b16 v[86:87], v55 offset:23040\n\t" \
  "v_mfma_f32_16x16x32_bf16 v[236:239], v[108:111], v[112:115], v[236:239]\n\t" \
  "v_mfma_f32_16x16x32_bf16 v[236:239], v[148:151], v[92:95], v[236:239]\n\t" \
  "v_mfma_f32_16x16x32_bf16 v[244:247], v[64:67], v[92:95], v[244:247]\n\t" \
  "v_mfma_f32_16x16x32_bf16 v[244:247], v[104:107], v[112:115], v[244:247]\n\t" \
  "v_mfma_f32_16x16x32_bf16 v[184:187], v[128:131], v[88:91], v[184:187]\n\t" \
  "v_mfma_f32_16x16x32_bf16 v[184:187], v[120:123], v[124:127], v[184:187]\n\t" \
  "v_mfma_f32_16x16x32_bf16 v[192:195], v[120:123], v[96:99], v[192:195]\n\t" \
  "v_mfma_f32_16x16x32_bf16 v[192:195], v[128:131], v[132:135], v[192:195]\n\t" \
  "v_mfma_f32_16x16x32_bf16 v[200:203], v[128:131], v[108:111], v[200:203]\n\t" \
  "global_store_dwordx4 v2, v[42:45], %24 offset:1024 sc1\n\t" \
  "v_mfma_f32_16x16x32_bf16 v[200:203], v[120:123], v[148:151], v[200:203]\n\t" \
  "v_mfma_f32_16x16x32_bf16 v[208:211], v[120:123], v[64:67], v[208:211]\n\t" \
  "v_mfma_f32_16x16x32_bf16 v[208:211], v[128:131], v[104:107], v[208:211]\n\t" \
  "v_mov_b32 v30, 0xff61b1e6\n\t" \
  "v_bfe_i32 v34, v3, 0, 1\n\t" \
  "v_mfma_f32_16x16x32_bf16 v[248:251], v[128:131], v[136:139], v[248:251]\n\t" \
  "v_and_b32 v34, v30, v34\n\t" \
  "v_bfe_i32 v38, v3, 4, 1\n\t" \
  "v_mfma_f32_16x16x32_bf16 v[248:251], v[120:123], v[144:147], v[248:251]\n\t" \
  "v_and_b32 v38, v30, v38\n\t" \
  "v_bfe_i32 v42, v3, 8, 1\n\t" \
  "v_mfma_f32_16x16x32_bf16 v[252:255], v[72:75], v[136:139], v[252:255]\n\t" \
  "v_bfi_b32 v42, v42, v30, %15\n\t" \
  "v_mov_b32 v48, 0x3f803f80\n\t" \
  "s_waitcnt lgkmcnt(0)\n\t" \
  "v_mfma_f32_16x16x32_bf16 v[252:255], v[140:143], v[144:147], v[252:255]\n\t" \
  "v_bfe_i32 v35, v3, 1, 1\n\t" \
  "v_and_b32 v35, v30, v35\n\t" \
  "v_mfma_f32_16x16x32_bf16 v[188:191], v[140:143], v[124:127], v[188:191]\n\t" \
  "v_bfe_i32 v39, v3, 5, 1\n\t" \
  "v_and_b32 v39, v30, v39\n\t" \
  "v_mfma_f32_16x16x32_bf16 v[188:191], v[72:75], v[88:91], v[188:191]\n\t" \
  "v_bfe_i32 v43, v3, 9, 1\n\t" \
  "v_bfi_b32 v43, v43, v30, %15\n\t" \
  "s_barrier\n\t" \
  "v_mfma_f32_16x16x32_bf16 v[196:199], v[72:75], v[132:135], v[196:199]\n\t" \
  "ds_read_b64_tr_b16 v[80:81], v58 offset:34816\n\t" \
  "v_mov_b32 v49, 0x3f803f80\n\t" \
  "v_mfma_f32_16x16x32_bf16 v[196:199], v[140:143], v[96:99], v[196:199]\n\t" \
  "ds_read_b64_tr_b16 v[82:83], v58 offset:35328\n\t" \
  "v_bfe_i32 v36, v3, 2, 1\n\t" \
  "v_mfma_f32_16x16x32_bf16 v[204:207], v[140:143], v[148:151], v[204:207]\n\t" \
  "ds_read_b128 v[100:103], v56 offset:2048\n\t" \
  "v_and_b32 v36, v30, v36\n\t" \
  "v_mfma_f32_16x16x32_bf16 v[204:207], v[72:75], v[108:111], v[204:207]\n\t" \
  "ds_read_b64_tr_b16 v[92:93], v58 offset:38912\n\t" \
  "v_bfe_i32 v40, v3, 6, 1\n\t" \
  "v_mfma_f32_16x16x32_bf16 v[212:215], v[72:75], v[104:107], v[212:215]\n\t" \
  "ds_read_b64_tr_b16 v[94:95], v58 offset:39424\n\t" \
  "v_and_b32 v40, v30, v40\n\t" \
  "v_mfma_f32_16x16x32_bf16 v[212:215], v[140:143], v[64:67], v[212:215]\n\t" \
  "ds_read_b128 v[112:115], v57 offset:2048\n\t" \
  "v_bfe_i32 v44, v3, 10, 1\n\t" \
  "v_mfma_f32_16x16x32_bf16 v[152:155], v[68:71], v[88:91], v[152:155]\n\t" \
  "ds_read_b128 v[128:131], v57 offset:4096\n\t" \
  "v_bfi_b32 v44, v44, v30, %15\n\t" \
  "v_mfma_f32_16x16x32_bf16 v[152:155], v[116:119], v[124:127], v[152:155]\n\t" \
  "ds_read_b128 v[120:123], v56 offset:4096\n\t" \
  "v_mov_b32 v50, 0x3f803f80\n\t" \
  "v_mfma_f32_16x16x32_bf16 v[160:163], v[116:119], v[96:99], v[160:163]\n\t" \
  "ds_read_b128 v[136:139], v56 offset:6144\n\t" \
  "v_bfe_i32 v37, v3, 3, 1\n\t" \
  "v_mfma_f32_16x16x32_bf16 v[160:163], v[68:71], v[132:135], v[160:163]\n\t" \
  "ds_read_b128 v[144:147], v57 offset:6144\n\t" \
  "v_and_b32 v37, v30, v37\n\t" \
  "v_mfma_f32_16x16x32_bf16 v[168:171], v[68:71], v[108:111], v[168:171]\n\t" \
  "ds_read_b128 v[72:75], v57 offset:8192\n\t" \
  "v_bfe_i32 v41, v3, 7, 1\n\t" \
  "v_mfma_f32_16x16x32_bf16 v[168:171], v[116:119], v[148:151], v[168:171]\n\t" \
  "ds_read_b128 v[140:143], v56 offset:8192\n\t" \
  "v_and_b32 v41, v30, v41\n\t" \
  "v_mfma_f32_16x16x32_bf16 v[176:179], v[116:119], v[64:67], v[176:179]\n\t" \
  "ds_read_b64_tr_b16 v[116:117], v59 offset:34816\n\t" \
  "v_bfe_i32 v45, v3, 11, 1\n\t" \
  "v_mfma_f32_16x16x32_bf16 v[176:179], v[68:71], v[104:107], v[176:179]\n\t" \
  "ds_read_b64_tr_b16 v[118:119], v59 offset:35328\n\t" \
  "v_bfi_b32 v45, v45, v30, %15\n\t" \
  "v_mfma_f32_16x16x32_bf16 v[156:159], v[76:79], v[88:91], v[156:159]\n\t" \
  "ds_read_b64_tr_b16 v[68:69], v59 offset:38912\n\t" \
  "v_mov_b32 v51, 0x3f803f80\n\t" \
  "v_mfma_f32_16x16x32_bf16 v[156:159], v[84:87], v[124:127], v[156:159]\n\t" \
  "ds_read_b64_tr_b16 v[70:71], v59 offset:39424\n\t" \
  "v_bfe_i32 v46, v3, 12, 1\n\t" \
  "v_mfma_f32_16x16x32_bf16 v[164:167], v[84:87], v[96:99], v[164:167]\n\t" \
  "ds_read_b64_tr_b16 v[88:89], v58 offset:26624\n\t" \
  "v_bfe_i32 v31, v3, 13, 1\n\t" \
  "v_mfma_f32_16x16x32_bf16 v[164:167], v[76:79], v[132:135], v[164:167]\n\t" \
  "ds_read_b64_tr_b16 v[90:91], v58 offset:27136\n\t" \
  "v_perm_b32 v46, v31, v46, vcc_hi\n\t" \
  "v_mfma_f32_16x16x32_bf16 v[172:175], v[76:79], v[108:111], v[172:175]\n\t" \
  "ds_read_b64_tr_b16 v[124:125], v58 offset:30720\n\t" \
  "v_bfe_i32 v47, v3, 14, 1\n\t" \
  "v_mfma_f32_16x16x32_bf16 v[172:175], v[84:87], v[148:151], v[172:175]\n\t" \
  "ds_read_b64_tr_b16 v[126:127], v58 offset:31232\n\t" \
  "v_bfe_i32 v31, v3, 15, 1\n\t" \
  "v_mfma_f32_16x16x32_bf16 v[180:183], v[84:87], v[64:67], v[180:183]\n\t" \
  "ds_read_b128 v[96:99], v56 offset:0\n\t" \
  "v_perm_b32 v47, v31, v47, vcc_hi\n\t" \
  "v_mfma_f32_16x16x32_bf16 v[180:183], v[76:79], v[104:107], v[180:183]\n\t" \
  "ds_read_b128 v[132:135], v57 offset:0\n\t" \
  "v_add_u32 v30, 18432, v4\n\t" \
  "s_waitcnt lgkmcnt(7)\n\t" \
  "v_mfma_f32_16x16x32_bf16 v[216:219], v[100:103], v[80:83], v[216:219]\n\t" \
  "ds_read_b64_tr_b16 v[108:109], v59 offset:26624\n\t" \
  "v_xor_b32 v31, 1024, v30\n\t" \
  "v_mfma_f32_16x16x32_bf16 v[216:219], v[112:115], v[92:95], v[216:219]\n\t" \
  "ds_read_b64_tr_b16 v[110:111], v59 offset:27136\n\t" \
  "v_mov_b32 v32, %12\n\t" \
  "v_mfma_f32_16x16x32_bf16 v[224:227], v[128:131], v[92:95], v[224:227]\n\t" \
  "ds_read_b64_tr_b16 v[148:149], v59 offset:30720\n\t" \
  "v_xor_b32 v33, 4, v32\n\t" \
  "v_mfma_f32_16x16x32_bf16 v[224:227], v[120:123], v[80:83], v[224:227]\n\t" \
  "ds_read_b64_tr_b16 v[150:151], v59 offset:31232\n\t" \
  "v_mov_b32 v0, 1\n\t" \
  "v_mfma_f32_16x16x32_bf16 v[232:235], v[136:139], v[80:83], v[232:235]\n\t" \
  "ds_read_b64_tr_b16 v[64:65], v58 offset:18432\n\t" \
  "v_and_b32 v4, 1023, v4\n\t" \
  "v_mfma_f32_16x16x32_bf16 v[232:235], v[144:147], v[92:95], v[232:235]\n\t" \
  "ds_read_b64_tr_b16 v[66:67], v58 offset:18944\n\t" \
  "v_add_u32 v1, 16384, v4\n\t" \
  "v_mfma_f32_16x16x32_bf16 v[240:243], v[72:75], v[92:95], v[240:243]\n\t" \
  "ds_read_b64_tr_b16 v[84:85], v58 offset:22528\n\t" \
  "v_mfma_f32_16x16x32_bf16 v[240:243], v[140:143], v[80:83], v[240:243]\n\t" \
  "ds_read_b64_tr_b16 v[86:87], v58 offset:23040\n\t" \
  "v_mfma_f32_16x16x32_bf16 v[220:223], v[100:103], v[116:119], v[220:223]\n\t" \
  "ds_read_b64_tr_b16 v[76:77], v59 offset:18432\n\t" \
  "s_waitcnt lgkmcnt(7)\n\t" \
  "v_mfma_f32_16x16x32_bf16 v[220:223], v[112:115], v[68:71], v[220:223]\n\t" \
  "ds_read_b64_tr_b16 v[78:79], v59 offset:18944\n\t" \
  "v_mfma_f32_16x16x32_bf16 v[228:231], v[128:131], v[68:71], v[228:231]\n\t" \
  "ds_read_b64_tr_b16 v[104:105], v59 offset:22528\n\t" \
  "v_mfma_f32_16x16x32_bf16 v[228:231], v[120:123], v[116:119], v[228:231]\n\t" \
  "ds_read_b64_tr_b16 v[106:107], v59 offset:23040\n\t" \
  "v_mfma_f32_16x16x32_bf16 v[236:239], v[136:139], v[116:119], v[236:239]\n\t" \
  "v_mfma_f32_16x16x32_bf16 v[236:239], v[144:147], v[68:71], v[236:239]\n\t" \
  "v_mfma_f32_16x16x32_bf16 v[244:247], v[72:75], v[68:71], v[244:247]\n\t" \
  "v_mfma_f32_16x16x32_bf16 v[244:247], v[140:143], v[116:119], v[244:247]\n\t" \
  "v_mfma_f32_16x16x32_bf16 v[184:187], v[88:91], v[100:103], v[184:187]\n\t" \
  "v_mfma_f32_16x16x32_bf16 v[184:187], v[124:127], v[112:115], v[184:187]\n\t" \
  "v_mfma_f32_16x16x32_bf16 v[192:195], v[124:127], v[128:131], v[192:195]\n\t" \
  "v_mfma_f32_16x16x32_bf16 v[192:195], v[88:91], v[120:123], v[192:195]\n\t" \
  "v_mfma_f32_16x16x32_bf16 v[200:203], v[88:91], v[136:139], v[200:203]\n\t" \
  "v_mfma_f32_16x16x32_bf16 v[200:203], v[124:127], v[144:147], v[200:203]\n\t" \
  "v_mfma_f32_16x16x32_bf16 v[208:211], v[124:127], v[72:75], v[208:211]\n\t" \
  "v_mfma_f32_16x16x32_bf16 v[208:211], v[88:91], v[140:143], v[208:211]\n\t" \
  "v_mfma_f32_16x16x32_bf16 v[248:251], v[88:91], v[96:99], v[248:251]\n\t" \
  "v_mfma_f32_16x16x32_bf16 v[248:251], v[124:127], v[132:135], v[248:251]\n\t" \
  "v_mfma_f32_16x16x32_bf16 v[252:255], v[108:111], v[96:99], v[252:255]\n\t" \
  "s_waitcnt lgkmcnt(0)\n\t" \
  "v_mfma_f32_16x16x32_bf16 v[252:255], v[148:151], v[132:135], v[252:255]\n\t" \
  "v_mfma_f32_16x16x32_bf16 v[188:191], v[148:151], v[112:115], v[188:191]\n\t" \
  "v_mfma_f32_16x16x32_bf16 v[188:191], v[108:111], v[100:103], v[188:191]\n\t" \
  "s_barrier\n\t" \
  "v_mfma_f32_16x16x32_bf16 v[196:199], v[108:111], v[120:123], v[196:199]\n\t" \
  "ds_read_b64_tr_b16 v[92:93], v62 offset:34816\n\t" \
  "v_mfma_f32_16x16x32_bf16 v[196:199], v[148:151], v[128:131], v[196:199]\n\t" \
  "ds_read_b64_tr_b16 v[94:95], v62 offset:35328\n\t" \
  "v_mfma_f32_16x16x32_bf16 v[204:207], v[148:151], v[144:147], v[204:207]\n\t" \
  "ds_read_b128 v[80:83], v60 offset:2048\n\t" \
  "v_mfma_f32_16x16x32_bf16 v[204:207], v[108:111], v[136:139], v[204:207]\n\t" \
  "ds_read_b64_tr_b16 v[68:69], v62 offset:38912\n\t" \
  "v_mfma_f32_16x16x32_bf16 v[212:215], v[108:111], v[140:143], v[212:215]\n\t" \
  "ds_read_b64_tr_b16 v[70:71], v62 offset:39424\n\t" \
  "v_mfma_f32_16x16x32_bf16 v[212:215], v[148:151], v[72:75], v[212:215]\n\t" \
  "ds_read_b128 v[116:119], v61 offset:2048\n\t" \
  "v_mfma_f32_16x16x32_bf16 v[152:155], v[64:67], v[100:103], v[152:155]\n\t" \
  "ds_read_b128 v[88:91], v61 offset:4096\n\t" \
  "v_mfma_f32_16x16x32_bf16 v[152:155], v[84:87], v[112:115], v[152:155]\n\t" \
  "ds_read_b128 v[124:127], v60 offset:4096\n\t" \
  "v_mfma_f32_16x16x32_bf16 v[160:163], v[84:87], v[128:131], v[160:163]\n\t" \
  "ds_read_b128 v[96:99], v60 offset:6144\n\t" \
  "v_mfma_f32_16x16x32_bf16 v[160:163], v[64:67], v[120:123], v[160:163]\n\t" \
  "ds_read_b128 v[132:135], v61 offset:6144\n\t" \
  "v_mfma_f32_16x16x32_bf16 v[168:171], v[64:67], v[136:139], v[168:171]\n\t" \
  "ds_read_b128 v[108:111], v61 offset:8192\n\t" \
  "v_mfma_f32_16x16x32_bf16 v[168:171], v[84:87], v[144:147], v[168:171]\n\t" \
  "ds_read_b128 v[148:151], v60 offset:8192\n\t" \
  "v_mfma_f32_16x16x32_bf16 v[176:179], v[84:87], v[72:75], v[176:179]\n\t" \
  "ds_read_b64_tr_b16 v[84:85], v63 offset:34816\n\t" \
  "v_mfma_f32_16x16x32_bf16 v[176:179], v[64:67], v[140:143], v[176:179]\n\t" \
  "ds_read_b64_tr_b16 v[86:87], v63 offset:35328\n\t" \
  "v_mfma_f32_16x16x32_bf16 v[156:159], v[76:79], v[100:103], v[156:159]\n\t" \
  "ds_read_b64_tr_b16 v[64:65], v63 offset:38912\n\t" \
  "v_mfma_f32_16x16x32_bf16 v[156:159], v[104:107], v[112:115], v[156:159]\n\t" \
  "ds_read_b64_tr_b16 v[66:67], v63 offset:39424\n\t" \
  "v_mfma_f32_16x16x32_bf16 v[164:167], v[104:107], v[128:131], v[164:167]\n\t" \
  "ds_read_b64_tr_b16 v[100:101], v62 offset:26624\n\t" \
  "v_mfma_f32_16x16x32_bf16 v[164:167], v[76:79], v[120:123], v[164:167]\n\t" \
  "ds_read_b64_tr_b16 v[102:103], v62 offset:27136\n\t" \
  "v_mfma_f32_16x16x32_bf16 v[172:175], v[76:79], v[136:139], v[172:175]\n\t" \
  "ds_read_b64_tr_b16 v[112:113], v62 offset:30720\n\t" \
  "v_mfma_f32_16x16x32_bf16 v[172:175], v[104:107], v[144:147], v[172:175]\n\t" \
  "ds_read_b64_tr_b16 v[114:115], v62 offset:31232\n\t" \
  "v_mfma_f32_16x16x32_bf16 v[180:183], v[104:107], v[72:75], v[180:183]\n\t" \
  "ds_read_b128 v[128:131], v60 offset:0\n\t" \
  "v_mfma_f32_16x16x32_bf16 v[180:183], v[76:79], v[140:143], v[180:183]\n\t" \
  "ds_read_b128 v[120:123], v61 offset:0\n\t" \
  "s_waitcnt lgkmcnt(7)\n\t" \
  "v_mfma_f32_16x16x32_bf16 v[216:219], v[80:83], v[92:95], v[216:219]\n\t" \
  "ds_read_b64_tr_b16 v[136:137], v63 offset:26624\n\t" \
  "v_mfma_f32_16x16x32_bf16 v[216:219], v[116:119], v[68:71], v[216:219]\n\t" \
  "ds_read_b64_tr_b16 v[138:139], v63 offset:27136\n\t" \
  "v_mfma_f32_16x16x32_bf16 v[224:227], v[88:91], v[68:71], v[224:227]\n\t" \
  "ds_read_b64_tr_b16 v[144:145], v63 offset:30720\n\t" \
  "v_mfma_f32_16x16x32_bf16 v[224:227], v[124:127], v[92:95], v[224:227]\n\t" \
  "ds_read_b64_tr_b16 v[146:147], v63 offset:31232\n\t" \
  "v_mfma_f32_16x16x32_bf16 v[232:235], v[96:99], v[92:95], v[232:235]\n\t" \
  "ds_read_b64_tr_b16 v[72:73], v62 offset:18432\n\t" \
  "v_mfma_f32_16x16x32_bf16 v[232:235], v[132:135], v[68:71], v[232:235]\n\t" \
  "ds_read_b64_tr_b16 v[74:75], v62 offset:18944\n\t" \
  "v_mfma_f32_16x16x32_bf16 v[240:243], v[108:111], v[68:71], v[240:243]\n\t" \
  "ds_read_b64_tr_b16 v[104:105], v62 offset:22528\n\t" \
  "v_mfma_f32_16x16x32_bf16 v[240:243], v[148:151], v[92:95], v[240:243]\n\t" \
  "ds_read_b64_tr_b16 v[106:107], v62 offset:23040\n\t" \
  "v_mfma_f32_16x16x32_bf16 v[220:223], v[80:83], v[84:87], v[220:223]\n\t" \
  "ds_read_b64_tr_b16 v[76:77], v63 offset:18432\n\t" \
  "s_waitcnt lgkmcnt(7)\n\t" \
  "v_mfma_f32_16x16x32_bf16 v[220:223], v[116:119], v[64:67], v[220:223]\n\t" \
  "ds_read_b64_tr_b16 v[78:79], v63 offset:18944\n\t" \
  "v_mfma_f32_16x16x32_bf16 v[228:231], v[88:91], v[64:67], v[228:231]\n\t" \
  "ds_read_b64_tr_b16 v[140:141], v63 offset:22528\n\t" \
  "v_mfma_f32_16x16x32_bf16 v[228:231], v[124:127], v[84:87], v[228:231]\n\t" \
  "ds_read_b64_tr_b16 v[142:143], v63 offset:23040\n\t" \
  "v_mfma_f32_16x16x32_bf16 v[236:239], v[96:99], v[84:87], v[236:239]\n\t" \
  "v_mfma_f32_16x16x32_bf16 v[236:239], v[132:135], v[64:67], v[236:239]\n\t" \
  "v_mfma_f32_16x16x32_bf16 v[244:247], v[108:111], v[64:67], v[244:247]\n\t" \
  "v_mfma_f32_16x16x32_bf16 v[244:247], v[148:151], v[84:87], v[244:247]\n\t" \
  "v_mfma_f32_16x16x32_bf16 v[184:187], v[100:103], v[80:83], v[184:187]\n\t" \
  "v_mfma_f32_16x16x32_bf16 v[184:187], v[112:115], v[116:119], v[184:187]\n\t" \
  "v_mfma_f32_16x16x32_bf16 v[192:195], v[112:115], v[88:91], v[192:195]\n\t" \
  "v_mfma_f32_16x16x32_bf16 v[192:195], v[100:103], v[124:127], v[192:195]\n\t" \
  "v_mfma_f32_16x16x32_bf16 v[200:203], v[100:103], v[96:99], v[200:203]\n\t" \
  "v_mfma_f32_16x16x32_bf16 v[200:203], v[112:115], v[132:135], v[200:203]\n\t" \
  "v_mfma_f32_16x16x32_bf16 v[208:211], v[112:115], v[108:111], v[208:211]\n\t" \
  "v_mfma_f32_16x16x32_bf16 v[208:211], v[100:103], v[148:151], v[208:211]\n\t" \
  "v_mfma_f32_16x16x32_bf16 v[248:251], v[100:103], v[128:131], v[248:251]\n\t" \
  "v_mfma_f32_16x16x32_bf16 v[248:251], v[112:115], v[120:123], v[248:251]\n\t" \
  "v_mfma_f32_16x16x32_bf16 v[252:255], v[136:139], v[128:131], v[252:255]\n\t" \
  "s_waitcnt lgkmcnt(0)\n\t" \
  "v_mfma_f32_16x16x32_bf16 v[252:255], v[144:147], v[120:123], v[252:255]\n\t" \
  "v_mfma_f32_16x16x32_bf16 v[188:191], v[144:147], v[116:119], v[188:191]\n\t" \
  "v_mfma_f32_16x16x32_bf16 v[188:191], v[136:139], v[80:83], v[188:191]\n\t" \
  "s_waitcnt vmcnt(0)\n\t" \
  "s_barrier\n\t" \
  "v_mfma_f32_16x16x32_bf16 v[196:199], v[136:139], v[124:127], v[196:199]\n\t" \
  "ds_read_b64_tr_b16 v[68:69], v54 offset:34816\n\t" \
  "v_mfma_f32_16x16x32_bf16 v[196:199], v[144:147], v[88:91], v[196:199]\n\t" \
  "ds_read_b64_tr_b16 v[70:71], v54 offset:35328\n\t" \
  "v_mfma_f32_16x16x32_bf16 v[204:207], v[144:147], v[132:135], v[204:207]\n\t" \
  "ds_read_b128 v[92:95], v52 offset:2048\n\t" \
  "v_mfma_f32_16x16x32_bf16 v[204:207], v[136:139], v[96:99], v[204:207]\n\t" \
  "ds_read_b64_tr_b16 v[64:65], v54 offset:38912\n\t" \
  "v_mfma_f32_16x16x32_bf16 v[212:215], v[136:139], v[148:151], v[212:215]\n\t" \
  "ds_read_b64_tr_b16 v[66:67], v54 offset:39424\n\t" \
  "v_mfma_f32_16x16x32_bf16 v[212:215], v[144:147], v[108:111], v[212:215]\n\t" \
  "ds_read_b128 v[84:87], v53 offset:2048\n\t" \
  "v_mfma_f32_16x16x32_bf16 v[152:155], v[72:75], v[80:83], v[152:155]\n\t" \
  "ds_read_b128 v[100:103], v53 offset:4096\n\t" \
  "v_mfma_f32_16x16x32_bf16 v[152:155], v[104:107], v[116:119], v[152:155]\n\t" \
  "ds_read_b128 v[112:115], v52 offset:4096\n\t" \
  "v_mfma_f32_16x16x32_bf16 v[160:163], v[104:107], v[88:91], v[160:163]\n\t" \
  "ds_read_b128 v[128:131], v52 offset:6144\n\t" \
  "v_mfma_f32_16x16x32_bf16 v[160:163], v[72:75], v[124:127], v[160:163]\n\t" \
  "ds_read_b128 v[120:123], v53 offset:6144\n\t" \
  "v_mfma_f32_16x16x32_bf16 v[168:171], v[72:75], v[96:99], v[168:171]\n\t" \
  "ds_read_b128 v[136:139], v53 offset:8192\n\t" \
  "v_mfma_f32_16x16x32_bf16 v[168:171], v[104:107], v[132:135], v[168:171]\n\t" \
  "ds_read_b128 v[144:147], v52 offset:8192\n\t" \
  "v_mfma_f32_16x16x32_bf16 v[176:179], v[104:107], v[108:111], v[176:179]\n\t" \
  "ds_read_b64_tr_b16 v[104:105], v55 offset:34816\n\t" \
  "v_mfma_f32_16x16x32_bf16 v[176:179], v[72:75], v[148:151], v[176:179]\n\t" \
  "ds_read_b64_tr_b16 v[106:107], v55 offset:35328\n\t" \
  "v_mfma_f32_16x16x32_bf16 v[156:159], v[76:79], v[80:83], v[156:159]\n\t" \
  "ds_read_b64_tr_b16 v[72:73], v55 offset:38912\n\t" \
  "v_mfma_f32_16x16x32_bf16 v[156:159], v[140:143], v[116:119], v[156:159]\n\t" \
  "ds_read_b64_tr_b16 v[74:75], v55 offset:39424\n\t" \
  "v_mfma_f32_16x16x32_bf16 v[164:167], v[140:143], v[88:91], v[164:167]\n\t" \
  "ds_read_b64_tr_b16 v[80:81], v54 offset:26624\n\t" \
  "v_mfma_f32_16x16x32_bf16 v[164:167], v[76:79], v[124:127], v[164:167]\n\t" \
  "ds_read_b64_tr_b16 v[82:83], v54 offset:27136\n\t" \
  "v_mfma_f32_16x16x32_bf16 v[172:175], v[76:79], v[96:99], v[172:175]\n\t" \
  "ds_read_b64_tr_b16 v[116:117], v54 offset:30720\n\t" \
  "v_mfma_f32_16x16x32_bf16 v[172:175], v[140:143], v[132:135], v[172:175]\n\t" \
  "ds_read_b64_tr_b16 v[118:119], v54 offset:31232\n\t" \
  "v_mfma_f32_16x16x32_bf16 v[180:183], v[140:143], v[108:111], v[180:183]\n\t" \
  "ds_read_b128 v[88:91], v52 offset:0\n\t" \
  "v_mfma_f32_16x16x32_bf16 v[180:183], v[76:79], v[148:151], v[180:183]\n\t" \
  "ds_read_b128 v[124:127], v53 offset:0\n\t" \
  "s_waitcnt lgkmcnt(7)\n\t" \
  "v_mfma_f32_16x16x32_bf16 v[216:219], v[92:95], v[68:71], v[216:219]\n\t" \
  "ds_read_b64_tr_b16 v[96:97], v55 offset:26624\n\t" \
  "v_mfma_f32_16x16x32_bf16 v[216:219], v[84:87], v[64:67], v[216:219]\n\t" \
  "ds_read_b64_tr_b16 v[98:99], v55 offset:27136\n\t" \
  "v_mfma_f32_16x16x32_bf16 v[224:227], v[100:103], v[64:67], v[224:227]\n\t" \
  "ds_read_b64_tr_b16 v[132:133], v55 offset:30720\n\t" \
  "v_mfma_f32_16x16x32_bf16 v[224:227], v[112:115], v[68:71], v[224:227]\n\t" \
  "ds_read_b64_tr_b16 v[134:135], v55 offset:31232\n\t" \
  "v_mfma_f32_16x16x32_bf16 v[232:235], v[128:131], v[68:71], v[232:235]\n\t" \
  "ds_read_b64_tr_b16 v[108:109], v54 offset:18432\n\t" \
  "v_mfma_f32_16x16x32_bf16 v[232:235], v[120:123], v[64:67], v[232:235]\n\t" \
  "ds_read_b64_tr_b16 v[110:111], v54 offset:18944\n\t" \
  "v_mfma_f32_16x16x32_bf16 v[240:243], v[136:139], v[64:67], v[240:243]\n\t" \
  "ds_read_b64_tr_b16 v[140:141], v54 offset:22528\n\t" \
  "v_mfma_f32_16x16x32_bf16 v[240:243], v[144:147], v[68:71], v[240:243]\n\t" \
  "ds_read_b64_tr_b16 v[142:143], v54 offset:23040\n\t" \
  "v_mfma_f32_16x16x32_bf16 v[220:223], v[92:95], v[104:107], v[220:223]\n\t" \
  "ds_read_b64_tr_b16 v[76:77], v55 offset:18432\n\t" \
  "s_waitcnt lgkmcnt(7)\n\t" \
  "v_mfma_f32_16x16x32_bf16 v[220:223], v[84:87], v[72:75], v[220:223]\n\t" \
  "ds_read_b64_tr_b16 v[78:79], v55 offset:18944\n\t" \
  "v_mfma_f32_16x16x32_bf16 v[228:231], v[100:103], v[72:75], v[228:231]\n\t" \
  "ds_read_b64_tr_b16 v[148:149], v55 offset:22528\n\t" \
  "v_mfma_f32_16x16x32_bf16 v[228:231], v[112:115], v[104:107], v[228:231]\n\t" \
  "ds_read_b64_tr_b16 v[150:151], v55 offset:23040\n\t" \
  "v_mfma_f32_16x16x32_bf16 v[236:239], v[128:131], v[104:107], v[236:239]\n\t" \
  "v_mfma_f32_16x16x32_bf16 v[236:239], v[120:123], v[72:75], v[236:239]\n\t" \
  "v_mfma_f32_16x16x32_bf16 v[244:247], v[136:139], v[72:75], v[244:247]\n\t" \
  "v_mfma_f32_16x16x32_bf16 v[244:247], v[144:147], v[104:107], v[244:247]\n\t" \
  "v_mfma_f32_16x16x32_bf16 v[184:187], v[80:83], v[92:95], v[184:187]\n\t" \
  "ds_read_b32 v10, v32 offset:104\n\t" \
  "v_mfma_f32_16x16x32_bf16 v[184:187], v[116:119], v[84:87], v[184:187]\n\t" \
  "v_lshlrev_b32 v11, 3, v4\n\t" \
  "v_mfma_f32_16x16x32_bf16 v[192:195], v[116:119], v[100:103], v[192:195]\n\t" \
  "v_mfma_f32_16x16x32_bf16 v[192:195], v[80:83], v[112:115], v[192:195]\n\t" \
  "v_mfma_f32_16x16x32_bf16 v[200:203], v[80:83], v[128:131], v[200:203]\n\t" \
  "s_waitcnt lgkmcnt(0)\n\t" \
  "s_cmp_lg_u32 %13, 0\n\t" \
  "s_cbranch_scc1 27f\n\t" \
  "s_mov_b32 exec_hi, 0\n\t" \
  "global_store_dword v11, v10, %25 sc1\n\t" \
  "s_mov_b32 exec_hi, -1\n\t" \
  "27:\n\t" \
  "v_mfma_f32_16x16x32_bf16 v[200:203], v[116:119], v[120:123], v[200:203]\n\t" \
  "v_mfma_f32_16x16x32_bf16 v[208:211], v[116:119], v[136:139], v[208:211]\n\t" \
  "v_mfma_f32_16x16x32_bf16 v[208:211], v[80:83], v[144:147], v[208:211]\n\t" \
  "v_mfma_f32_16x16x32_bf16 v[248:251], v[80:83], v[88:91], v[248:251]\n\t" \
  "v_mfma_f32_16x16x32_bf16 v[248:251], v[116:119], v[124:127], v[248:251]\n\t" \
  "v_mfma_f32_16x16x32_bf16 v[252:255], v[96:99], v[88:91], v[252:255]\n\t" \
  "v_mfma_f32_16x16x32_bf16 v[252:255], v[132:135], v[124:127], v[252:255]\n\t" \
  "v_mfma_f32_16x16x32_bf16 v[188:191], v[132:135], v[84:87], v[188:191]\n\t" \
  "v_mfma_f32_16x16x32_bf16 v[188:191], v[96:99], v[92:95], v[188:191]\n\t" \
  "s_barrier\n\t" \
  "v_mfma_f32_16x16x32_bf16 v[196:199], v[96:99], v[112:115], v[196:199]\n\t" \
  "ds_read_b64_tr_b16 v[64:65], v58 offset:34816\n\t" \
  "v_mfma_f32_16x16x32_bf16 v[196:199], v[132:135], v[100:103], v[196:199]\n\t" \
  "ds_read_b64_tr_b16 v[66:67], v58 offset:35328\n\t" \
  "v_mfma_f32_16x16x32_bf16 v[204:207], v[132:135], v[120:123], v[204:207]\n\t" \
  "ds_read_b128 v[68:71], v56 offset:2048\n\t" \
  "v_mfma_f32_16x16x32_bf16 v[204:207], v[96:99], v[128:131], v[204:207]\n\t" \
  "ds_read_b64_tr_b16 v[72:73], v58 offset:38912\n\t" \
  "v_mfma_f32_16x16x32_bf16 v[212:215], v[96:99], v[144:147], v[212:215]\n\t" \
  "ds_read_b64_tr_b16 v[74:75], v58 offset:39424\n\t" \
  "v_mfma_f32_16x16x32_bf16 v[212:215], v[132:135], v[136:139], v[212:215]\n\t" \
  "ds_read_b128 v[104:107], v57 offset:2048\n\t" \
  "v_mfma_f32_16x16x32_bf16 v[152:155], v[108:111], v[92:95], v[152:155]\n\t" \
  "ds_read_b128 v[80:83], v57 offset:4096\n\t" \
  "v_mfma_f32_16x16x32_bf16 v[152:155], v[140:143], v[84:87], v[152:155]\n\t" \
  "ds_read_b128 v[116:119], v56 offset:4096\n\t" \
  "v_mfma_f32_16x16x32_bf16 v[160:163], v[140:143], v[100:103], v[160:163]\n\t" \
  "ds_read_b128 v[88:91], v56 offset:6144\n\t" \
  "v_mfma_f32_16x16x32_bf16 v[160:163], v[108:111], v[112:115], v[160:163]\n\t" \
  "ds_read_b128 v[124:127], v57 offset:6144\n\t" \
  "v_mfma_f32_16x16x32_bf16 v[168:171], v[108:111], v[128:131], v[168:171]\n\t" \
  "ds_read_b128 v[96:99], v57 offset:8192\n\t" \
  "v_mfma_f32_16x16x32_bf16 v[168:171], v[140:143], v[120:123], v[168:171]\n\t" \
  "ds_read_b128 v[132:135], v56 offset:8192\n\t" \
  "v_mfma_f32_16x16x32_bf16 v[176:179], v[140:143], v[136:139], v[176:179]\n\t" \
  "ds_read_b64_tr_b16 v[140:141], v59 offset:34816\n\t" \
  "v_mfma_f32_16x16x32_bf16 v[176:179], v[108:111], v[144:147], v[176:179]\n\t" \
  "ds_read_b64_tr_b16 v[142:143], v59 offset:35328\n\t" \
  "v_mfma_f32_16x16x32_bf16 v[156:159], v[76:79], v[92:95], v[156:159]\n\t" \
  "ds_read_b64_tr_b16 v[108:109], v59 offset:38912\n\t" \
  "v_mfma_f32_16x16x32_bf16 v[156:159], v[148:151], v[84:87], v[156:159]\n\t" \
  "ds_read_b64_tr_b16 v[110:111], v59 offset:39424\n\t" \
  "v_mfma_f32_16x16x32_bf16 v[164:167], v[148:151], v[100:103], v[164:167]\n\t" \
  "ds_read_b64_tr_b16 v[92:93], v58 offset:26624\n\t" \
  "v_mfma_f32_16x16x32_bf16 v[164:167], v[76:79], v[112:115], v[164:167]\n\t" \
  "ds_read_b64_tr_b16 v[94:95], v58 offset:27136\n\t" \
  "v_mfma_f32_16x16x32_bf16 v[172:175], v[76:79], v[128:131], v[172:175]\n\t" \
  "ds_read_b64_tr_b16 v[84:85], v58 offset:30720\n\t" \
  "v_mfma_f32_16x16x32_bf16 v[172:175], v[148:151], v[120:123], v[172:175]\n\t" \
  "ds_read_b64_tr_b16 v[86:87], v58 offset:31232\n\t" \
  "v_mfma_f32_16x16x32_bf16 v[180:183], v[148:151], v[136:139], v[180:183]\n\t" \
  "ds_read_b128 v[100:103], v56 offset:0\n\t" \
  "v_mfma_f32_16x16x32_bf16 v[180:183], v[76:79], v[144:147], v[180:183]\n\t" \
  "ds_read_b128 v[112:115], v57 offset:0\n\t" \
  "s_waitcnt lgkmcnt(7)\n\t" \
  "v_mfma_f32_16x16x32_bf16 v[216:219], v[68:71], v[64:67], v[216:219]\n\t" \
  "ds_read_b64_tr_b16 v[128:129], v59 offset:26624\n\t" \
  "v_mfma_f32_16x16x32_bf16 v[216:219], v[104:107], v[72:75], v[216:219]\n\t" \
  "ds_read_b64_tr_b16 v[130:131], v59 offset:27136\n\t" \
  "v_mfma_f32_16x16x32_bf16 v[224:227], v[80:83], v[72:75], v[224:227]\n\t" \
  "ds_read_b64_tr_b16 v[120:121], v59 offset:30720\n\t" \
  "v_mfma_f32_16x16x32_bf16 v[224:227], v[116:119], v[64:67], v[224:227]\n\t" \
  "ds_read_b64_tr_b16 v[122:123], v59 offset:31232\n\t" \
  "v_mfma_f32_16x16x32_bf16 v[232:235], v[88:91], v[64:67], v[232:235]\n\t" \
  "ds_read_b64_tr_b16 v[136:137], v58 offset:18432\n\t" \
  "v_mfma_f32_16x16x32_bf16 v[232:235], v[124:127], v[72:75], v[232:235]\n\t" \
  "ds_read_b64_tr_b16 v[138:139], v58 offset:18944\n\t" \
  "v_mfma_f32_16x16x32_bf16 v[240:243], v[96:99], v[72:75], v[240:243]\n\t" \
  "ds_read_b64_tr_b16 v[148:149], v58 offset:22528\n\t" \
  "v_mfma_f32_16x16x32_bf16 v[240:243], v[132:135], v[64:67], v[240:243]\n\t" \
  "ds_read_b64_tr_b16 v[150:151], v58 offset:23040\n\t" \
  "v_mfma_f32_16x16x32_bf16 v[220:223], v[68:71], v[140:143], v[220:223]\n\t" \
  "ds_read_b64_tr_b16 v[76:77], v59 offset:18432\n\t" \
  "s_waitcnt lgkmcnt(7)\n\t" \
  "v_mfma_f32_16x16x32_bf16 v[220:223], v[104:107], v[108:111], v[220:223]\n\t" \
  "ds_read_b64_tr_b16 v[78:79], v59 offset:18944\n\t" \
  "v_mfma_f32_16x16x32_bf16 v[228:231], v[80:83], v[108:111], v[228:231]\n\t" \
  "ds_read_b64_tr_b16 v[144:145], v59 offset:22528\n\t" \
  "v_mfma_f32_16x16x32_bf16 v[228:231], v[116:119], v[140:143], v[228:231]\n\t" \
  "ds_read_b64_tr_b16 v[146:147], v59 offset:23040\n\t" \
  "v_mfma_f32_16x16x32_bf16 v[236:239], v[88:91], v[140:143], v[236:239]\n\t" \
  "v_mfma_f32_16x16x32_bf16 v[236:239], v[124:127], v[108:111], v[236:239]\n\t" \
  "v_mfma_f32_16x16x32_bf16 v[244:247], v[96:99], v[108:111], v[244:247]\n\t" \
  "v_mfma_f32_16x16x32_bf16 v[244:247], v[132:135], v[140:143], v[244:247]\n\t" \
  "v_mfma_f32_16x16x32_bf16 v[184:187], v[92:95], v[68:71], v[184:187]\n\t" \
  "v_mfma_f32_16x16x32_bf16 v[184:187], v[84:87], v[104:107], v[184:187]\n\t" \
  "v_mfma_f32_16x16x32_bf16 v[192:195], v[84:87], v[80:83], v[192:195]\n\t" \
  "v_mfma_f32_16x16x32_bf16 v[192:195], v[92:95], v[116:119], v[192:195]\n\t" \
  "v_mfma_f32_16x16x32_bf16 v[200:203], v[92:95], v[88:91], v[200:203]\n\t" \
  "v_mfma_f32_16x16x32_bf16 v[200:203], v[84:87], v[124:127], v[200:203]\n\t" \
  "v_mfma_f32_16x16x32_bf16 v[208:211], v[84:87], v[96:99], v[208:211]\n\t" \
  "v_mfma_f32_16x16x32_bf16 v[208:211], v[92:95], v[132:135], v[208:211]\n\t" \
  "v_mfma_f32_16x16x32_bf16 v[248:251], v[92:95], v[100:103], v[248:251]\n\t" \
  "v_mfma_f32_16x16x32_bf16 v[248:251], v[84:87], v[112:115], v[248:251]\n\t" \
  "v_mfma_f32_16x16x32_bf16 v[252:255], v[128:131], v[100:103], v[252:255]\n\t" \
  "s_waitcnt lgkmcnt(0)\n\t" \
  "v_mfma_f32_16x16x32_bf16 v[252:255], v[120:123], v[112:115], v[252:255]\n\t" \
  "v_mfma_f32_16x16x32_bf16 v[188:191], v[120:123], v[104:107], v[188:191]\n\t" \
  "v_mfma_f32_16x16x32_bf16 v[188:191], v[128:131], v[68:71], v[188:191]\n\t" \
  "s_barrier\n\t" \
  "v_mfma_f32_16x16x32_bf16 v[196:199], v[128:131], v[116:119], v[196:199]\n\t" \
  "ds_read_b64_tr_b16 v[72:73], v62 offset:34816\n\t" \
  "v_mfma_f32_16x16x32_bf16 v[196:199], v[120:123], v[80:83], v[196:199]\n\t" \
  "ds_read_b64_tr_b16 v[74:75], v62 offset:35328\n\t" \
  "v_mfma_f32_16x16x32_bf16 v[204:207], v[120:123], v[124:127], v[204:207]\n\t" \
  "ds_read_b128 v[64:67], v60 offset:2048\n\t" \
  "v_mfma_f32_16x16x32_bf16 v[204:207], v[128:131], v[88:91], v[204:207]\n\t" \
  "ds_read_b64_tr_b16 v[108:109], v62 offset:38912\n\t" \
  "v_mfma_f32_16x16x32_bf16 v[212:215], v[128:131], v[132:135], v[212:215]\n\t" \
  "ds_read_b64_tr_b16 v[110:111], v62 offset:39424\n\t" \
  "v_mfma_f32_16x16x32_bf16 v[212:215], v[120:123], v[96:99], v[212:215]\n\t" \
  "ds_read_b128 v[140:143], v61 offset:2048\n\t" \
  "v_mfma_f32_16x16x32_bf16 v[152:155], v[136:139], v[68:71], v[152:155]\n\t" \
  "ds_read_b128 v[92:95], v61 offset:4096\n\t" \
  "v_mfma_f32_16x16x32_bf16 v[152:155], v[148:151], v[104:107], v[152:155]\n\t" \
  "ds_read_b128 v[84:87], v60 offset:4096\n\t" \
  "v_mfma_f32_16x16x32_bf16 v[160:163], v[148:151], v[80:83], v[160:163]\n\t" \
  "ds_read_b128 v[100:103], v60 offset:6144\n\t" \
  "v_mfma_f32_16x16x32_bf16 v[160:163], v[136:139], v[116:119], v[160:163]\n\t" \
  "ds_read_b128 v[112:115], v61 offset:6144\n\t" \
  "v_mfma_f32_16x16x32_bf16 v[168:171], v[136:139], v[88:91], v[168:171]\n\t" \
  "ds_read_b128 v[128:131], v61 offset:8192\n\t" \
  "v_mfma_f32_16x16x32_bf16 v[168:171], v[148:151], v[124:127], v[168:171]\n\t" \
  "ds_read_b128 v[120:123], v60 offset:8192\n\t" \
  "v_mfma_f32_16x16x32_bf16 v[176:179], v[148:151], v[96:99], v[176:179]\n\t" \
  "ds_read_b64_tr_b16 v[148:149], v63 offset:34816\n\t" \
  "v_mfma_f32_16x16x32_bf16 v[176:179], v[136:139], v[132:135], v[176:179]\n\t" \
  "ds_read_b64_tr_b16 v[150:151], v63 offset:35328\n\t" \
  "v_mfma_f32_16x16x32_bf16 v[156:159], v[76:79], v[68:71], v[156:159]\n\t" \
  "ds_read_b64_tr_b16 v[136:137], v63 offset:38912\n\t" \
  "v_mfma_f32_16x16x32_bf16 v[156:159], v[144:147], v[104:107], v[156:159]\n\t" \
  "ds_read_b64_tr_b16 v[138:139], v63 offset:39424\n\t" \
  "v_mfma_f32_16x16x32_bf16 v[164:167], v[144:147], v[80:83], v[164:167]\n\t" \
  "ds_read_b64_tr_b16 v[68:69], v62 offset:26624\n\t" \
  "v_mfma_f32_16x16x32_bf16 v[164:167], v[76:79], v[116:119], v[164:167]\n\t" \
  "ds_read_b64_tr_b16 v[70:71], v62 offset:27136\n\t" \
  "v_mfma_f32_16x16x32_bf16 v[172:175], v[76:79], v[88:91], v[172:175]\n\t" \
  "ds_read_b64_tr_b16 v[104:105], v62 offset:30720\n\t" \
  "v_mfma_f32_16x16x32_bf16 v[172:175], v[144:147], v[124:127], v[172:175]\n\t" \
  "ds_read_b64_tr_b16 v[106:107], v62 offset:31232\n\t" \
  "v_mfma_f32_16x16x32_bf16 v[180:183], v[144:147], v[96:99], v[180:183]\n\t" \
  "ds_read_b128 v[80:83], v60 offset:0\n\t" \
  "v_mfma_f32_16x16x32_bf16 v[180:183], v[76:79], v[132:135], v[180:183]\n\t" \
  "ds_read_b128 v[116:119], v61 offset:0\n\t" \
  "s_waitcnt lgkmcnt(7)\n\t" \
  "v_mfma_f32_16x16x32_bf16 v[216:219], v[64:67], v[72:75], v[216:219]\n\t" \
  "ds_read_b64_tr_b16 v[88:89], v63 offset:26624\n\t" \
  "v_mfma_f32_16x16x32_bf16 v[216:219], v[140:143], v[108:111], v[216:219]\n\t" \
  "505:\n\t" \
  "s_cbranch_execz 509f\n\t" \
  "ds_read_b64_tr_b16 v[90:91], v63 offset:27136\n\t" \
  "v_mfma_f32_16x16x32_bf16 v[224:227], v[92:95], v[108:111], v[224:227]\n\t" \
  "ds_read_b64_tr_b16 v[124:125], v63 offset:30720\n\t" \
  "v_mfma_f32_16x16x32_bf16 v[224:227], v[84:87], v[72:75], v[224:227]\n\t" \
  "ds_read_b64_tr_b16 v[126:127], v63 offset:31232\n\t" \
  "v_mfma_f32_16x16x32_bf16 v[232:235], v[100:103], v[72:75], v[232:235]\n\t" \
  "ds_read_b64_tr_b16 v[96:97], v62 offset:18432\n\t" \
  "v_mfma_f32_16x16x32_bf16 v[232:235], v[112:115], v[108:111], v[232:235]\n\t" \
  "ds_read_b64_tr_b16 v[98:99], v62 offset:18944\n\t" \
  "v_mfma_f32_16x16x32_bf16 v[240:243], v[128:131], v[108:111], v[240:243]\n\t" \
  "ds_read_b64_tr_b16 v[144:145], v62 offset:22528\n\t" \
  "v_mfma_f32_16x16x32_bf16 v[240:243], v[120:123], v[72:75], v[240:243]\n\t" \
  "ds_read_b64_tr_b16 v[146:147], v62 offset:23040\n\t" \
  "v_mfma_f32_16x16x32_bf16 v[220:223], v[64:67], v[148:151], v[220:223]\n\t" \
  "ds_read_b64_tr_b16 v[76:77], v63 offset:18432\n\t" \
  "s_waitcnt lgkmcnt(7)\n\t" \
  "v_mfma_f32_16x16x32_bf16 v[220:223], v[140:143], v[136:139], v[220:223]\n\t" \
  "ds_read_b64_tr_b16 v[78:79], v63 offset:18944\n\t" \
  "v_mfma_f32_16x16x32_bf16 v[228:231], v[92:95], v[136:139], v[228:231]\n\t" \
  "ds_read_b64_tr_b16 v[132:133], v63 offset:22528\n\t" \
  "v_mfma_f32_16x16x32_bf16 v[228:231], v[84:87], v[148:151], v[228:231]\n\t" \
  "ds_read_b64_tr_b16 v[134:135], v63 offset:23040\n\t" \
  "v_mfma_f32_16x16x32_bf16 v[236:239], v[100:103], v[148:151], v[236:239]\n\t" \
  "v_mfma_f32_16x16x32_bf16 v[236:239], v[112:115], v[136:139], v[236:239]\n\t" \
  "v_mfma_f32_16x16x32_bf16 v[244:247], v[128:131], v[136:139], v[244:247]\n\t" \
  "v_mfma_f32_16x16x32_bf16 v[244:247], v[120:123], v[148:151], v[244:247]\n\t" \
  "v_mfma_f32_16x16x32_bf16 v[184:187], v[68:71], v[64:67], v[184:187]\n\t" \
  "v_mfma_f32_16x16x32_bf16 v[184:187], v[104:107], v[140:143], v[184:187]\n\t" \
  "v_mfma_f32_16x16x32_bf16 v[192:195], v[104:107], v[92:95], v[192:195]\n\t" \
  "v_mfma_f32_16x16x32_bf16 v[192:195], v[68:71], v[84:87], v[192:195]\n\t" \
  "v_mfma_f32_16x16x32_bf16 v[200:203], v[68:71], v[100:103], v[200:203]\n\t" \
  "v_mfma_f32_16x16x32_bf16 v[200:203], v[104:107], v[112:115], v[200:203]\n\t" \
  "v_mfma_f32_16x16x32_bf16 v[208:211], v[104:107], v[128:131], v[208:211]\n\t" \
  "v_mfma_f32_16x16x32_bf16 v[208:211], v[68:71], v[120:123], v[208:211]\n\t" \
  "v_mfma_f32_16x16x32_bf16 v[248:251], v[68:71], v[80:83], v[248:251]\n\t" \
  "v_mfma_f32_16x16x32_bf16 v[248:251], v[104:107], v[116:119], v[248:251]\n\t" \
  "v_mfma_f32_16x16x32_bf16 v[252:255], v[88:91], v[80:83], v[252:255]\n\t" \
  "s_waitcnt lgkmcnt(0)\n\t" \
  "v_mfma_f32_16x16x32_bf16 v[252:255], v[124:127], v[116:119], v[252:255]\n\t" \
  "v_mfma_f32_16x16x32_bf16 v[188:191], v[124:127], v[140:143], v[188:191]\n\t" \
  "v_mfma_f32_16x16x32_bf16 v[188:191], v[88:91], v[64:67], v[188:191]\n\t" \
  "s_barrier\n\t" \
  "v_mfma_f32_16x16x32_bf16 v[196:199], v[88:91], v[84:87], v[196:199]\n\t" \
  "ds_read_b64_tr_b16 v[108:109], v54 offset:34816\n\t" \
  "v_mfma_f32_16x16x32_bf16 v[196:199], v[124:127], v[92:95], v[196:199]\n\t" \
  "ds_read_b64_tr_b16 v[110:111], v54 offset:35328\n\t" \
  "v_mfma_f32_16x16x32_bf16 v[204:207], v[124:127], v[112:115], v[204:207]\n\t" \
  "ds_read_b128 v[72:75], v52 offset:2048\n\t" \
  "v_mfma_f32_16x16x32_bf16 v[204:207], v[88:91], v[100:103], v[204:207]\n\t" \
  "ds_read_b64_tr_b16 v[136:137], v54 offset:38912\n\t" \
  "v_mfma_f32_16x16x32_bf16 v[212:215], v[88:91], v[120:123], v[212:215]\n\t" \
  "ds_read_b64_tr_b16 v[138:139], v54 offset:39424\n\t" \
  "v_mfma_f32_16x16x32_bf16 v[212:215], v[124:127], v[128:131], v[212:215]\n\t" \
  "ds_read_b128 v[148:151], v53 offset:2048\n\t" \
  "v_mfma_f32_16x16x32_bf16 v[152:155], v[96:99], v[64:67], v[152:155]\n\t" \
  "ds_read_b128 v[68:71], v53 offset:4096\n\t" \
  "v_mfma_f32_16x16x32_bf16 v[152:155], v[144:147], v[140:143], v[152:155]\n\t" \
  "ds_read_b128 v[104:107], v52 offset:4096\n\t" \
  "v_mfma_f32_16x16x32_bf16 v[160:163], v[144:147], v[92:95], v[160:163]\n\t" \
  "ds_read_b128 v[80:83], v52 offset:6144\n\t" \
  "v_mfma_f32_16x16x32_bf16 v[160:163], v[96:99], v[84:87], v[160:163]\n\t" \
  "ds_read_b128 v[116:119], v53 offset:6144\n\t" \
  "v_mfma_f32_16x16x32_bf16 v[168:171], v[96:99], v[100:103], v[168:171]\n\t" \
  "ds_read_b128 v[88:91], v53 offset:8192\n\t" \
  "v_mfma_f32_16x16x32_bf16 v[168:171], v[144:147], v[112:115], v[168:171]\n\t" \
  "ds_read_b128 v[124:127], v52 offset:8192\n\t" \
  "v_mfma_f32_16x16x32_bf16 v[176:179], v[144:147], v[128:131], v[176:179]\n\t" \
  "ds_read_b64_tr_b16 v[144:145], v55 offset:34816\n\t" \
  "v_mfma_f32_16x16x32_bf16 v[176:179], v[96:99], v[120:123], v[176:179]\n\t" \
  "ds_read_b64_tr_b16 v[146:147], v55 offset:35328\n\t" \
  "v_mfma_f32_16x16x32_bf16 v[156:159], v[76:79], v[64:67], v[156:159]\n\t" \
  "ds_read_b64_tr_b16 v[96:97], v55 offset:38912\n\t" \
  "v_mfma_f32_16x16x32_bf16 v[156:159], v[132:135], v[140:143], v[156:159]\n\t" \
  "ds_read_b64_tr_b16 v[98:99], v55 offset:39424\n\t" \
  "v_mfma_f32_16x16x32_bf16 v[164:167], v[132:135], v[92:95], v[164:167]\n\t" \
  "ds_read_b64_tr_b16 v[64:65], v54 offset:26624\n\t" \
  "v_mfma_f32_16x16x32_bf16 v[164:167], v[76:79], v[84:87], v[164:167]\n\t" \
  "ds_read_b64_tr_b16 v[66:67], v54 offset:27136\n\t" \
  "v_mfma_f32_16x16x32_bf16 v[172:175], v[76:79], v[100:103], v[172:175]\n\t" \
  "ds_read_b64_tr_b16 v[140:141], v54 offset:30720\n\t" \
  "v_mfma_f32_16x16x32_bf16 v[172:175], v[132:135], v[112:115], v[172:175]\n\t" \
  "ds_read_b64_tr_b16 v[142:143], v54 offset:31232\n\t" \
  "v_mfma_f32_16x16x32_bf16 v[180:183], v[132:135], v[128:131], v[180:183]\n\t" \
  "ds_read_b128 v[92:95], v52 offset:0\n\t" \
  "v_mfma_f32_16x16x32_bf16 v[180:183], v[76:79], v[120:123], v[180:183]\n\t" \
  "ds_read_b128 v[84:87], v53 offset:0\n\t" \
  "s_waitcnt lgkmcnt(7)\n\t" \
  "v_mfma_f32_16x16x32_bf16 v[216:219], v[72:75], v[108:111], v[216:219]\n\t" \
  "ds_read_b64_tr_b16 v[100:101], v55 offset:26624\n\t" \
  "v_mfma_f32_16x16x32_bf16 v[216:219], v[148:151], v[136:139], v[216:219]\n\t" \
  "ds_read_b64_tr_b16 v[102:103], v55 offset:27136\n\t" \
  "v_mfma_f32_16x16x32_bf16 v[224:227], v[68:71], v[136:139], v[224:227]\n\t" \
  "ds_read_b64_tr_b16 v[112:113], v55 offset:30720\n\t" \
  "v_mfma_f32_16x16x32_bf16 v[224:227], v[104:107], v[108:111], v[224:227]\n\t" \
  "ds_read_b64_tr_b16 v[114:115], v55 offset:31232\n\t" \
  "v_mfma_f32_16x16x32_bf16 v[232:235], v[80:83], v[108:111], v[232:235]\n\t" \
  "ds_read_b64_tr_b16 v[128:129], v54 offset:18432\n\t" \
  "v_mfma_f32_16x16x32_bf16 v[232:235], v[116:119], v[136:139], v[232:235]\n\t" \
  "ds_read_b64_tr_b16 v[130:131], v54 offset:18944\n\t" \
  "v_mfma_f32_16x16x32_bf16 v[240:243], v[88:91], v[136:139], v[240:243]\n\t" \
  "ds_read_b64_tr_b16 v[132:133], v54 offset:22528\n\t" \
  "v_mfma_f32_16x16x32_bf16 v[240:243], v[124:127], v[108:111], v[240:243]\n\t" \
  "ds_read_b64_tr_b16 v[134:135], v54 offset:23040\n\t" \
  "v_mfma_f32_16x16x32_bf16 v[220:223], v[72:75], v[144:147], v[220:223]\n\t" \
  "ds_read_b64_tr_b16 v[76:77], v55 offset:18432\n\t" \
  "s_waitcnt lgkmcnt(7)\n\t" \
  "v_mfma_f32_16x16x32_bf16 v[220:223], v[148:151], v[96:99], v[220:223]\n\t" \
  "ds_read_b64_tr_b16 v[78:79], v55 offset:18944\n\t" \
  "v_mfma_f32_16x16x32_bf16 v[228:231], v[68:71], v[96:99], v[228:231]\n\t" \
  "ds_read_b64_tr_b16 v[120:121], v55 offset:22528\n\t" \
  "v_mfma_f32_16x16x32_bf16 v[228:231], v[104:107], v[144:147], v[228:231]\n\t" \
  "ds_read_b64_tr_b16 v[122:123], v55 offset:23040\n\t" \
  "v_mfma_f32_16x16x32_bf16 v[236:239], v[80:83], v[144:147], v[236:239]\n\t" \
  "v_mfma_f32_16x16x32_bf16 v[236:239], v[116:119], v[96:99], v[236:239]\n\t" \
  "v_mfma_f32_16x16x32_bf16 v[244:247], v[88:91], v[96:99], v[244:247]\n\t" \
  "v_mfma_f32_16x16x32_bf16 v[244:247], v[124:127], v[144:147], v[244:247]\n\t" \
  "v_mfma_f32_16x16x32_bf16 v[184:187], v[64:67], v[72:75], v[184:187]\n\t" \
  "v_mfma_f32_16x16x32_bf16 v[184:187], v[140:143], v[148:151], v[184:187]\n\t" \
  "v_mfma_f32_16x16x32_bf16 v[192:195], v[140:143], v[68:71], v[192:195]\n\t" \
  "v_mfma_f32_16x16x32_bf16 v[192:195], v[64:67], v[104:107], v[192:195]\n\t" \
  "v_mfma_f32_16x16x32_bf16 v[200:203], v[64:67], v[80:83], v[200:203]\n\t" \
  "v_mfma_f32_16x16x32_bf16 v[200:203], v[140:143], v[116:119], v[200:203]\n\t" \
  "v_mfma_f32_16x16x32_bf16 v[208:211], v[140:143], v[88:91], v[208:211]\n\t" \
  "v_mfma_f32_16x16x32_bf16 v[208:211], v[64:67], v[124:127], v[208:211]\n\t" \
  "v_mfma_f32_16x16x32_bf16 v[248:251], v[64:67], v[92:95], v[248:251]\n\t" \
  "v_mfma_f32_16x16x32_bf16 v[248:251], v[140:143], v[84:87], v[248:251]\n\t" \
  "v_mfma_f32_16x16x32_bf16 v[252:255], v[100:103], v[92:95], v[252:255]\n\t" \
  "s_waitcnt lgkmcnt(0)\n\t" \
  "v_mfma_f32_16x16x32_bf16 v[252:255], v[112:115], v[84:87], v[252:255]\n\t" \
  "v_mfma_f32_16x16x32_bf16 v[188:191], v[112:115], v[148:151], v[188:191]\n\t" \
  "v_mfma_f32_16x16x32_bf16 v[188:191], v[100:103], v[72:75], v[188:191]\n\t" \
  "s_barrier\n\t" \
  "v_mfma_f32_16x16x32_bf16 v[196:199], v[100:103], v[104:107], v[196:199]\n\t" \
  "ds_read_b64_tr_b16 v[96:97], v58 offset:34816\n\t" \
  "v_mfma_f32_16x16x32_bf16 v[196:199], v[112:115], v[68:71], v[196:199]\n\t" \
  "ds_read_b64_tr_b16 v[98:99], v58 offset:35328\n\t" \
  "v_mfma_f32_16x16x32_bf16 v[204:207], v[112:115], v[116:119], v[204:207]\n\t" \
  "ds_read_b128 v[136:139], v56 offset:2048\n\t" \
  "v_mfma_f32_16x16x32_bf16 v[204:207], v[100:103], v[80:83], v[204:207]\n\t" \
  "ds_read_b64_tr_b16 v[64:65], v58 offset:38912\n\t" \
  "v_mfma_f32_16x16x32_bf16 v[212:215], v[100:103], v[124:127], v[212:215]\n\t" \
  "ds_read_b64_tr_b16 v[66:67], v58 offset:39424\n\t" \
  "v_mfma_f32_16x16x32_bf16 v[212:215], v[112:115], v[88:91], v[212:215]\n\t" \
  "ds_read_b128 v[108:111], v57 offset:2048\n\t" \
  "v_mfma_f32_16x16x32_bf16 v[152:155], v[128:131], v[72:75], v[152:155]\n\t" \
  "ds_read_b128 v[144:147], v57 offset:4096\n\t" \
  "v_mfma_f32_16x16x32_bf16 v[152:155], v[132:135], v[148:151], v[152:155]\n\t" \
  "ds_read_b128 v[140:143], v56 offset:4096\n\t" \
  "v_mfma_f32_16x16x32_bf16 v[160:163], v[132:135], v[68:71], v[160:163]\n\t" \
  "ds_read_b128 v[112:115], v56 offset:6144\n\t" \
  "v_mfma_f32_16x16x32_bf16 v[160:163], v[128:131], v[104:107], v[160:163]\n\t" \
  "ds_read_b64_tr_b16 v[92:93], v59 offset:34816\n\t" \
  "v_mfma_f32_16x16x32_bf16 v[168:171], v[128:131], v[80:83], v[168:171]\n\t" \
  "ds_read_b64_tr_b16 v[94:95], v59 offset:35328\n\t" \
  "v_mfma_f32_16x16x32_bf16 v[168:171], v[132:135], v[116:119], v[168:171]\n\t" \
  "ds_read_b64_tr_b16 v[84:85], v59 offset:38912\n\t" \
  "v_mfma_f32_16x16x32_bf16 v[176:179], v[132:135], v[88:91], v[176:179]\n\t" \
  "ds_read_b128 v[132:135], v57 offset:6144\n\t" \
  "v_mfma_f32_16x16x32_bf16 v[176:179], v[128:131], v[124:127], v[176:179]\n\t" \
  "ds_read_b128 v[128:131], v57 offset:8192\n\t" \
  "v_mfma_f32_16x16x32_bf16 v[156:159], v[76:79], v[72:75], v[156:159]\n\t" \
  "ds_read_b64_tr_b16 v[86:87], v59 offset:39424\n\t" \
  "v_mfma_f32_16x16x32_bf16 v[156:159], v[120:123], v[148:151], v[156:159]\n\t" \
  "ds_read_b128 v[148:151], v56 offset:8192\n\t" \
  "v_mfma_f32_16x16x32_bf16 v[164:167], v[120:123], v[68:71], v[164:167]\n\t" \
  "ds_read_b64_tr_b16 v[100:101], v58 offset:26624\n\t" \
  "v_mfma_f32_16x16x32_bf16 v[164:167], v[76:79], v[104:107], v[164:167]\n\t" \
  "ds_read_b64_tr_b16 v[102:103], v58 offset:27136\n\t" \
  "v_mfma_f32_16x16x32_bf16 v[172:175], v[76:79], v[80:83], v[172:175]\n\t" \
  "ds_read_b64_tr_b16 v[72:73], v58 offset:30720\n\t" \
  "v_mfma_f32_16x16x32_bf16 v[172:175], v[120:123], v[116:119], v[172:175]\n\t" \
  "ds_read_b64_tr_b16 v[74:75], v58 offset:31232\n\t" \
  "v_mfma_f32_16x16x32_bf16 v[180:183], v[120:123], v[88:91], v[180:183]\n\t" \
  "ds_read_b128 v[68:71], v56 offset:0\n\t" \
  "v_mfma_f32_16x16x32_bf16 v[180:183], v[76:79], v[124:127], v[180:183]\n\t" \
  "ds_read_b128 v[104:107], v57 offset:0\n\t" \
  "s_waitcnt lgkmcnt(7)\n\t" \
  "v_mfma_f32_16x16x32_bf16 v[216:219], v[136:139], v[96:99], v[216:219]\n\t" \
  "ds_read_b64_tr_b16 v[80:81], v59 offset:26624\n\t" \
  "v_mfma_f32_16x16x32_bf16 v[216:219], v[108:111], v[64:67], v[216:219]\n\t" \
  "ds_read_b64_tr_b16 v[82:83], v59 offset:27136\n\t" \
  "v_mfma_f32_16x16x32_bf16 v[224:227], v[144:147], v[64:67], v[224:227]\n\t" \
  "ds_read_b64_tr_b16 v[88:89], v59 offset:30720\n\t" \
  "v_mfma_f32_16x16x32_bf16 v[224:227], v[140:143], v[96:99], v[224:227]\n\t" \
  "ds_read_b64_tr_b16 v[90:91], v59 offset:31232\n\t" \
  "v_mfma_f32_16x16x32_bf16 v[232:235], v[112:115], v[96:99], v[232:235]\n\t" \
  "ds_read_b64_tr_b16 v[116:117], v58 offset:18432\n\t" \
  "v_mfma_f32_16x16x32_bf16 v[232:235], v[132:135], v[64:67], v[232:235]\n\t" \
  "ds_read_b64_tr_b16 v[118:119], v58 offset:18944\n\t" \
  "v_mfma_f32_16x16x32_bf16 v[240:243], v[128:131], v[64:67], v[240:243]\n\t" \
  "ds_read_b64_tr_b16 v[120:121], v58 offset:22528\n\t" \
  "s_waitcnt lgkmcnt(7)\n\t" \
  "v_mfma_f32_16x16x32_bf16 v[240:243], v[148:151], v[96:99], v[240:243]\n\t" \
  "ds_read_b64_tr_b16 v[122:123], v58 offset:23040\n\t" \
  "v_mfma_f32_16x16x32_bf16 v[220:223], v[136:139], v[92:95], v[220:223]\n\t" \
  "ds_read_b64_tr_b16 v[124:125], v59 offset:18432\n\t" \
  "v_mfma_f32_16x16x32_bf16 v[220:223], v[108:111], v[84:87], v[220:223]\n\t" \
  "ds_read_b64_tr_b16 v[126:127], v59 offset:18944\n\t" \
  "v_mfma_f32_16x16x32_bf16 v[228:231], v[144:147], v[84:87], v[228:231]\n\t" \
  "ds_read_b64_tr_b16 v[76:77], v59 offset:22528\n\t" \
  "v_mfma_f32_16x16x32_bf16 v[228:231], v[140:143], v[92:95], v[228:231]\n\t" \
  "ds_read_b64_tr_b16 v[78:79], v59 offset:23040\n\t" \
  "v_mfma_f32_16x16x32_bf16 v[236:239], v[112:115], v[92:95], v[236:239]\n\t" \
  "v_mfma_f32_16x16x32_bf16 v[236:239], v[132:135], v[84:87], v[236:239]\n\t" \
  "v_mfma_f32_16x16x32_bf16 v[244:247], v[128:131], v[84:87], v[244:247]\n\t" \
  "v_mfma_f32_16x16x32_bf16 v[244:247], v[148:151], v[92:95], v[244:247]\n\t" \
  "v_mfma_f32_16x16x32_bf16 v[184:187], v[100:103], v[136:139], v[184:187]\n\t" \
  "v_mfma_f32_16x16x32_bf16 v[184:187], v[72:75], v[108:111], v[184:187]\n\t" \
  "v_mfma_f32_16x16x32_bf16 v[192:195], v[72:75], v[144:147], v[192:195]\n\t" \
  "v_and_b32 v14, -16, v32\n\t" \
  "v_mfma_f32_16x16x32_bf16 v[192:195], v[100:103], v[140:143], v[192:195]\n\t" \
  "v_mfma_f32_16x16x32_bf16 v[200:203], v[100:103], v[112:115], v[200:203]\n\t" \
  "ds_read_b128 v[10:13], v14 offset:128\n\t" \
  "v_mfma_f32_16x16x32_bf16 v[200:203], v[72:75], v[132:135], v[200:203]\n\t" \
  "v_mfma_f32_16x16x32_bf16 v[208:211], v[72:75], v[128:131], v[208:211]\n\t" \
  "v_mfma_f32_16x16x32_bf16 v[208:211], v[100:103], v[148:151], v[208:211]\n\t" \
  "v_mfma_f32_16x16x32_bf16 v[248:251], v[100:103], v[68:71], v[248:251]\n\t" \
  "v_mfma_f32_16x16x32_bf16 v[248:251], v[72:75], v[104:107], v[248:251]\n\t" \
  "s_waitcnt lgkmcnt(1)\n\t" \
  "v_mfma_f32_16x16x32_bf16 v[252:255], v[80:83], v[68:71], v[252:255]\n\t" \
  "s_waitcnt lgkmcnt(0)\n\t" \
  "v_and_b32 v10, v10, v11\n\t" \
  "v_and_b32 v12, v12, v13\n\t" \
  "v_mfma_f32_16x16x32_bf16 v[252:255], v[88:91], v[104:107], v[252:255]\n\t" \
  "v_and_b32 v10, v10, v12\n\t" \
  "v_mfma_f32_16x16x32_bf16 v[188:191], v[88:91], v[108:111], v[188:191]\n\t" \
  "v_readfirstlane_b32 vcc_lo, v10\n\t" \
  "v_mfma_f32_16x16x32_bf16 v[188:191], v[80:83], v[136:139], v[188:191]\n\t" \
  "ds_write_b32 v32, v0 offset:144\n\t" \
  "s_cmp_eq_u32 vcc_lo, 0\n\t" \
  "s_cbranch_scc1 15f\n\t" \
  "14:\n\t" \
  "v_mfma_f32_16x16x32_bf16 v[196:199], v[80:83], v[140:143], v[196:199]\n\t" \
  "ds_read_b64_tr_b16 v[64:65], v62 offset:26624\n\t" \
  "v_mfma_f32_16x16x32_bf16 v[196:199], v[88:91], v[144:147], v[196:199]\n\t" \
  "ds_read_b64_tr_b16 v[66:67], v62 offset:27136\n\t" \
  "v_mfma_f32_16x16x32_bf16 v[204:207], v[88:91], v[132:135], v[204:207]\n\t" \
  "ds_read_b128 v[96:99], v60 offset:0\n\t" \
  "v_mfma_f32_16x16x32_bf16 v[204:207], v[80:83], v[112:115], v[204:207]\n\t" \
  "ds_read_b128 v[84:87], v60 offset:2048\n\t" \
  "v_mfma_f32_16x16x32_bf16 v[212:215], v[80:83], v[148:151], v[212:215]\n\t" \
  "ds_read_b128 v[92:95], v60 offset:4096\n\t" \
  "v_mfma_f32_16x16x32_bf16 v[212:215], v[88:91], v[128:131], v[212:215]\n\t" \
  "ds_read_b128 v[100:103], v60 offset:6144\n\t" \
  "v_mfma_f32_16x16x32_bf16 v[152:155], v[116:119], v[136:139], v[152:155]\n\t" \
  "ds_read_b128 v[72:75], v60 offset:8192\n\t" \
  "v_mfma_f32_16x16x32_bf16 v[152:155], v[120:123], v[108:111], v[152:155]\n\t" \
  "ds_read_b64_tr_b16 v[68:69], v63 offset:26624\n\t" \
  "v_mfma_f32_16x16x32_bf16 v[160:163], v[120:123], v[144:147], v[160:163]\n\t" \
  "ds_read_b64_tr_b16 v[70:71], v63 offset:27136\n\t" \
  "v_mfma_f32_16x16x32_bf16 v[160:163], v[116:119], v[140:143], v[160:163]\n\t" \
  "ds_read_b64_tr_b16 v[104:105], v62 offset:34816\n\t" \
  "v_mfma_f32_16x16x32_bf16 v[168:171], v[116:119], v[112:115], v[168:171]\n\t" \
  "ds_read_b64_tr_b16 v[106:107], v62 offset:35328\n\t" \
  "v_mfma_f32_16x16x32_bf16 v[168:171], v[120:123], v[132:135], v[168:171]\n\t" \
  "ds_read_b64_tr_b16 v[80:81], v63 offset:34816\n\t" \
  "v_mfma_f32_16x16x32_bf16 v[176:179], v[120:123], v[128:131], v[176:179]\n\t" \
  "ds_read_b64_tr_b16 v[82:83], v63 offset:35328\n\t" \
  "v_mfma_f32_16x16x32_bf16 v[176:179], v[116:119], v[148:151], v[176:179]\n\t" \
  "ds_read_b64_tr_b16 v[88:89], v62 offset:18432\n\t" \
  "v_mfma_f32_16x16x32_bf16 v[156:159], v[124:127], v[136:139], v[156:159]\n\t" \
  "ds_read_b64_tr_b16 v[90:91], v62 offset:18944\n\t" \
  "v_mfma_f32_16x16x32_bf16 v[156:159], v[76:79], v[108:111], v[156:159]\n\t" \
  "ds_read_b64_tr_b16 v[120:121], v62 offset:30720\n\t" \
  "v_mfma_f32_16x16x32_bf16 v[164:167], v[76:79], v[144:147], v[164:167]\n\t" \
  "ds_read_b64_tr_b16 v[122:123], v62 offset:31232\n\t" \
  "v_mfma_f32_16x16x32_bf16 v[164:167], v[124:127], v[140:143], v[164:167]\n\t" \
  "ds_read_b128 v[116:119], v61 offset:0\n\t" \
  "v_mfma_f32_16x16x32_bf16 v[172:175], v[124:127], v[112:115], v[172:175]\n\t" \
  "ds_read_b128 v[136:139], v61 offset:2048\n\t" \
  "v_mfma_f32_16x16x32_bf16 v[172:175], v[76:79], v[132:135], v[172:175]\n\t" \
  "ds_read_b128 v[108:111], v61 offset:4096\n\t" \
  "v_mfma_f32_16x16x32_bf16 v[180:183], v[76:79], v[128:131], v[180:183]\n\t" \
  "ds_read_b64_tr_b16 v[76:77], v63 offset:18432\n\t" \
  "v_mfma_f32_16x16x32_bf16 v[180:183], v[124:127], v[148:151], v[180:183]\n\t" \
  "ds_read_b64_tr_b16 v[78:79], v63 offset:18944\n\t" \
  "s_waitcnt lgkmcnt(7)\n\t" \
  "v_mfma_f32_16x16x32_bf16 v[248:251], v[64:67], v[96:99], v[248:251]\n\t" \
  "ds_read_b128 v[144:147], v61 offset:6144\n\t" \
  "v_mfma_f32_16x16x32_bf16 v[184:187], v[64:67], v[84:87], v[184:187]\n\t" \
  "ds_read_b128 v[140:143], v61 offset:8192\n\t" \
  "v_mfma_f32_16x16x32_bf16 v[192:195], v[64:67], v[92:95], v[192:195]\n\t" \
  "ds_read_b64_tr_b16 v[112:113], v63 offset:30720\n\t" \
  "v_mfma_f32_16x16x32_bf16 v[200:203], v[64:67], v[100:103], v[200:203]\n\t" \
  "ds_read_b64_tr_b16 v[114:115], v63 offset:31232\n\t" \
  "v_mfma_f32_16x16x32_bf16 v[208:211], v[64:67], v[72:75], v[208:211]\n\t" \
  "ds_read_b64_tr_b16 v[132:133], v62 offset:22528\n\t" \
  "v_mfma_f32_16x16x32_bf16 v[252:255], v[68:71], v[96:99], v[252:255]\n\t" \
  "ds_read_b64_tr_b16 v[134:135], v62 offset:23040\n\t" \
  "v_mfma_f32_16x16x32_bf16 v[188:191], v[68:71], v[84:87], v[188:191]\n\t" \
  "ds_read_b64_tr_b16 v[128:129], v63 offset:22528\n\t" \
  "v_mfma_f32_16x16x32_bf16 v[196:199], v[68:71], v[92:95], v[196:199]\n\t" \
  "ds_read_b64_tr_b16 v[130:131], v63 offset:23040\n\t" \
  "v_mfma_f32_16x16x32_bf16 v[204:207], v[68:71], v[100:103], v[204:207]\n\t" \
  "ds_read_b64_tr_b16 v[124:125], v62 offset:38912\n\t" \
  "v_mfma_f32_16x16x32_bf16 v[212:215], v[68:71], v[72:75], v[212:215]\n\t" \
  "ds_read_b64_tr_b16 v[126:127], v62 offset:39424\n\t" \
  "v_mfma_f32_16x16x32_bf16 v[216:219], v[84:87], v[104:107], v[216:219]\n\t" \
  "ds_read_b64_tr_b16 v[148:149], v63 offset:38912\n\t" \
  "v_mfma_f32_16x16x32_bf16 v[224:227], v[92:95], v[104:107], v[224:227]\n\t" \
  "ds_read_b64_tr_b16 v[150:151], v63 offset:39424\n\t" \
  "v_mfma_f32_16x16x32_bf16 v[232:235], v[100:103], v[104:107], v[232:235]\n\t" \
  "v_mfma_f32_16x16x32_bf16 v[240:243], v[72:75], v[104:107], v[240:243]\n\t" \
  "v_mfma_f32_16x16x32_bf16 v[220:223], v[84:87], v[80:83], v[220:223]\n\t" \
  "v_mfma_f32_16x16x32_bf16 v[228:231], v[92:95], v[80:83], v[228:231]\n\t" \
  "v_mfma_f32_16x16x32_bf16 v[236:239], v[100:103], v[80:83], v[236:239]\n\t" \
  "v_mfma_f32_16x16x32_bf16 v[244:247], v[72:75], v[80:83], v[244:247]\n\t" \
  "v_mfma_f32_16x16x32_bf16 v[152:155], v[88:91], v[84:87], v[152:155]\n\t" \
  "v_mfma_f32_16x16x32_bf16 v[160:163], v[88:91], v[92:95], v[160:163]\n\t" \
  "v_mfma_f32_16x16x32_bf16 v[168:171], v[88:91], v[100:103], v[168:171]\n\t" \
  "v_mfma_f32_16x16x32_bf16 v[176:179], v[88:91], v[72:75], v[176:179]\n\t" \
  "s_waitcnt lgkmcnt(0)\n\t" \
  "v_mfma_f32_16x16x32_bf16 v[156:159], v[76:79], v[84:87], v[156:159]\n\t" \
  "v_mfma_f32_16x16x32_bf16 v[164:167], v[76:79], v[92:95], v[164:167]\n\t" \
  "ds_write_b32 v32, v0\n\t" \
  "v_mfma_f32_16x16x32_bf16 v[172:175], v[76:79], v[100:103], v[172:175]\n\t" \
  "v_mfma_f32_16x16x32_bf16 v[180:183], v[76:79], v[72:75], v[180:183]\n\t" \
  "v_mfma_f32_16x16x32_bf16 v[248:251], v[120:123], v[116:119], v[248:251]\n\t" \
  "v_and_b32 v56, -16, v32\n\t" \
  "v_mfma_f32_16x16x32_bf16 v[184:187], v[120:123], v[136:139], v[184:187]\n\t" \
  "v_mfma_f32_16x16x32_bf16 v[192:195], v[120:123], v[108:111], v[192:195]\n\t" \
  "v_mfma_f32_16x16x32_bf16 v[200:203], v[120:123], v[144:147], v[200:203]\n\t" \
  "v_mfma_f32_16x16x32_bf16 v[208:211], v[120:123], v[140:143], v[208:211]\n\t" \
  "v_mfma_f32_16x16x32_bf16 v[252:255], v[112:115], v[116:119], v[252:255]\n\t" \
  "v_mfma_f32_16x16x32_bf16 v[188:191], v[112:115], v[136:139], v[188:191]\n\t" \
  "v_mfma_f32_16x16x32_bf16 v[196:199], v[112:115], v[108:111], v[196:199]\n\t" \
  "v_cvt_pk_bf16_f32 v10, v248, v249\n\t" \
  "v_cvt_pk_bf16_f32 v11, v250, v251\n\t" \
  "v_mfma_f32_16x16x32_bf16 v[204:207], v[112:115], v[144:147], v[204:207]\n\t" \
  "v_cvt_pk_bf16_f32 v14, v184, v185\n\t" \
  "v_cvt_pk_bf16_f32 v15, v186, v187\n\t" \
  "v_mfma_f32_16x16x32_bf16 v[212:215], v[112:115], v[140:143], v[212:215]\n\t" \
  "v_cvt_pk_bf16_f32 v12, v252, v253\n\t" \
  "v_cvt_pk_bf16_f32 v13, v254, v255\n\t" \
  "v_mfma_f32_16x16x32_bf16 v[152:155], v[132:135], v[136:139], v[152:155]\n\t" \
  "ds_write_b128 v30, v[10:13]\n\t" \
  "ds_write_b32 v32, v0 offset:48\n\t" \
  "v_mfma_f32_16x16x32_bf16 v[160:163], v[132:135], v[108:111], v[160:163]\n\t" \
  "v_cvt_pk_bf16_f32 v16, v188, v189\n\t" \
  "v_cvt_pk_bf16_f32 v17, v190, v191\n\t" \
  "v_mfma_f32_16x16x32_bf16 v[168:171], v[132:135], v[144:147], v[168:171]\n\t" \
  "v_cvt_pk_bf16_f32 v18, v192, v193\n\t" \
  "v_cvt_pk_bf16_f32 v19, v194, v195\n\t" \
  "v_mfma_f32_16x16x32_bf16 v[176:179], v[132:135], v[140:143], v[176:179]\n\t" \
  "v_cvt_pk_bf16_f32 v20, v196, v197\n\t" \
  "v_cvt_pk_bf16_f32 v21, v198, v199\n\t" \
  "v_mfma_f32_16x16x32_bf16 v[156:159], v[128:131], v[136:139], v[156:159]\n\t" \
  "v_cvt_pk_bf16_f32 v22, v200, v201\n\t" \
  "v_cvt_pk_bf16_f32 v23, v202, v203\n\t" \
  "v_mfma_f32_16x16x32_bf16 v[164:167], v[128:131], v[108:111], v[164:167]\n\t" \
  "v_cvt_pk_bf16_f32 v24, v204, v205\n\t" \
  "v_cvt_pk_bf16_f32 v25, v206, v207\n\t" \
  "v_mfma_f32_16x16x32_bf16 v[172:175], v[128:131], v[144:147], v[172:175]\n\t" \
  "v_cvt_pk_bf16_f32 v26, v208, v209\n\t" \
  "v_cvt_pk_bf16_f32 v27, v210, v211\n\t" \
  "v_mfma_f32_16x16x32_bf16 v[180:183], v[128:131], v[140:143], v[180:183]\n\t" \
  "v_cvt_pk_bf16_f32 v28, v212, v213\n\t" \
  "v_cvt_pk_bf16_f32 v29, v214, v215\n\t" \
  "v_mfma_f32_16x16x32_bf16 v[216:219], v[136:139], v[124:127], v[216:219]\n\t" \
  "v_cvt_pk_bf16_f32 v84, v152, v153\n\t" \
  "v_cvt_pk_bf16_f32 v85, v154, v155\n\t" \
  "v_mfma_f32_16x16x32_bf16 v[224:227], v[108:111], v[124:127], v[224:227]\n\t" \
  "v_cvt_pk_bf16_f32 v86, v156, v157\n\t" \
  "v_cvt_pk_bf16_f32 v87, v158, v159\n\t" \
  "v_mfma_f32_16x16x32_bf16 v[232:235], v[144:147], v[124:127], v[232:235]\n\t" \
  "v_cvt_pk_bf16_f32 v88, v160, v161\n\t" \
  "v_cvt_pk_bf16_f32 v89, v162, v163\n\t" \
  "v_mfma_f32_16x16x32_bf16 v[240:243], v[140:143], v[124:127], v[240:243]\n\t" \
  "v_cvt_pk_bf16_f32 v90, v164, v165\n\t" \
  "v_cvt_pk_bf16_f32 v91, v166, v167\n\t" \
  "v_mfma_f32_16x16x32_bf16 v[220:223], v[136:139], v[148:151], v[220:223]\n\t" \
  "v_cvt_pk_bf16_f32 v92, v168, v169\n\t" \
  "v_cvt_pk_bf16_f32 v93, v170, v171\n\t" \
  "v_mfma_f32_16x16x32_bf16 v[228:231], v[108:111], v[148:151], v[228:231]\n\t" \
  "v_cvt_pk_bf16_f32 v94, v172, v173\n\t" \
  "v_cvt_pk_bf16_f32 v95, v174, v175\n\t" \
  "v_mfma_f32_16x16x32_bf16 v[236:239], v[144:147], v[148:151], v[236:239]\n\t" \
  "v_cvt_pk_bf16_f32 v96, v176, v177\n\t" \
  "v_cvt_pk_bf16_f32 v97, v178, v179\n\t" \
  "v_mfma_f32_16x16x32_bf16 v[244:247], v[140:143], v[148:151], v[244:247]\n\t" \
  "v_cvt_pk_bf16_f32 v98, v180, v181\n\t" \
  "v_cvt_pk_bf16_f32 v99, v182, v183\n\t" \
  "v_mfma_f32_16x16x32_bf16 v[200:203], v[22:25], v[92:95], v[34:37]\n\t" \
  "v_cvt_pk_bf16_f32 v66, v232, v233\n\t" \
  "v_cvt_pk_bf16_f32 v67, v234, v235\n\t" \
  "v_mfma_f32_16x16x32_bf16 v[208:211], v[26:29], v[96:99], v[34:37]\n\t" \
  "v_cvt_pk_bf16_f32 v76, v236, v237\n\t" \
  "v_cvt_pk_bf16_f32 v77, v238, v239\n\t" \
  "v_mfma_f32_16x16x32_bf16 v[212:215], v[22:25], v[96:99], v[38:41]\n\t" \
  "v_cvt_pk_bf16_f32 v64, v240, v241\n\t" \
  "v_cvt_pk_bf16_f32 v65, v242, v243\n\t" \
  "v_mfma_f32_16x16x32_bf16 v[204:207], v[18:21], v[92:95], v[38:41]\n\t" \
  "v_cvt_pk_bf16_f32 v74, v244, v245\n\t" \
  "v_cvt_pk_bf16_f32 v75, v246, v247\n\t" \
  "v_mfma_f32_16x16x32_bf16 v[192:195], v[18:21], v[88:91], v[34:37]\n\t" \
  "v_cvt_pk_bf16_f32 v68, v224, v225\n\t" \
  "v_cvt_pk_bf16_f32 v69, v226, v227\n\t" \
  "v_mfma_f32_16x16x32_bf16 v[196:199], v[14:17], v[88:91], v[38:41]\n\t" \
  "v_max_f32 v208, v208, v212\n\t" \
  "v_max_f32 v209, v209, v213\n\t" \
  "v_mfma_f32_16x16x32_bf16 v[184:187], v[14:17], v[84:87], v[34:37]\n\t" \
  "v_max_f32 v210, v210, v214\n\t" \
  "v_max_f32 v200, v200, v204\n\t" \
  "v_mfma_f32_16x16x32_bf16 v[188:191], v[10:13], v[84:87], v[42:45]\n\t" \
  "v_max_f32 v201, v201, v205\n\t" \
  "v_max_f32 v202, v202, v206\n\t" \
  "v_max_f32 v192, v192, v196\n\t" \
  "v_max_f32 v193, v193, v197\n\t" \
  "v_max_f32 v194, v194, v198\n\t" \
  "v_max_f32 v211, v211, v215\n\t" \
  "ds_read_b32 v52, v33 offset:48\n\t" \
  "ds_read_b64 v[72:73], v31\n\t" \
  "ds_read_b64 v[82:83], v31 offset:8\n\t" \
  "v_max3_f32 v106, v208, v209, v210\n\t" \
  "v_max_f32 v184, v184, v188\n\t" \
  "v_max_f32 v185, v185, v189\n\t" \
  "v_max_f32 v186, v186, v190\n\t" \
  "v_max_f32 v203, v203, v207\n\t" \
  "v_max3_f32 v104, v200, v201, v202\n\t" \
  "v_max_f32 v195, v195, v199\n\t" \
  "v_max3_f32 v102, v192, v193, v194\n\t" \
  "v_max_f32 v187, v187, v191\n\t" \
  "v_max3_f32 v100, v184, v185, v186\n\t" \
  "v_max_f32 v106, v106, v211\n\t" \
  "v_max_f32 v104, v104, v203\n\t" \
  "v_max_f32 v102, v102, v195\n\t" \
  "v_max_f32 v100, v100, v187\n\t" \
  "v_permlane16_swap_b32 v104, v106\n\t" \
  "v_cvt_pk_bf16_f32 v78, v228, v229\n\t" \
  "v_permlane16_swap_b32 v100, v102\n\t" \
  "v_max_f32 v104, v104, v106\n\t" \
  "v_max_f32 v100, v100, v102\n\t" \
  "v_cvt_pk_bf16_f32 v79, v230, v231\n\t" \
  "v_cvt_pk_bf16_f32 v70, v216, v217\n\t" \
  "v_permlane32_swap_b32 v100, v104\n\t" \
  "v_cvt_pk_bf16_f32 v71, v218, v219\n\t" \
  "v_max_f32 v100, v100, v104\n\t" \
  "v_mul_f32 v100, 0xbe8293ee, v100\n\t" \
  "v_mov_b32 v104, v100\n\t" \
  "v_cvt_pk_bf16_f32 v80, v220, v221\n\t" \
  "v_cvt_pk_bf16_f32 v81, v222, v223\n\t" \
  "v_permlane32_swap_b32 v100, v104\n\t" \
  "s_waitcnt lgkmcnt(0)\n\t" \
  "v_readfirstlane_b32 vcc_lo, v52\n\t" \
  "s_nop 1\n\t" \
  "s_cmp_eq_u32 vcc_lo, 0\n\t" \
  "s_cbranch_scc1 5f\n\t" \
  "4:\n\t" \
  "v_mov_b32 v106, v104\n\t" \
  "v_mov_b32 v102, v100\n\t" \
  "s_nop 0\n\t" \
  "v_permlane16_swap_b32 v104, v106\n\t" \
  "v_permlane16_swap_b32 v100, v102\n\t" \
  "v_fma_f32 v208, v208, %16, v106\n\t" \
  "v_fma_f32 v209, v209, %16, v106\n\t" \
  "v_fma_f32 v210, v210, %16, v106\n\t" \
  "v_fma_f32 v211, v211, %16, v106\n\t" \
  "v_exp_f32 v208, v208\n\t" \
  "v_exp_f32 v209, v209\n\t" \
  "v_exp_f32 v210, v210\n\t" \
  "v_exp_f32 v211, v211\n\t" \
  "v_cvt_pk_bf16_f32 v212, v208, v209\n\t" \
  "v_cvt_pk_bf16_f32 v213, v210, v211\n\t" \
  "v_bfi_b32 v214, v46, 0, v212\n\t" \
  "v_bfi_b32 v215, v47, 0, v213\n\t" \
  "v_and_b32 v212, v46, v212\n\t" \
  "v_and_b32 v213, v47, v213\n\t" \
  "v_fma_f32 v200, v200, %16, v104\n\t" \
  "v_fma_f32 v201, v201, %16, v104\n\t" \
  "v_mfma_f32_16x16x32_bf16 v[208:211], v[48:51], v[212:215], 0\n\t" \
  "v_fma_f32 v202, v202, %16, v104\n\t" \
  "v_fma_f32 v203, v203, %16, v104\n\t" \
  "v_mfma_f32_16x16x32_bf16 v[176:179], v[64:67], v[212:215], 0\n\t" \
  "v_exp_f32 v200, v200\n\t" \
  "v_mfma_f32_16x16x32_bf16 v[180:183], v[74:77], v[212:215], 0\n\t" \
  "v_exp_f32 v201, v201\n\t" \
  "v_exp_f32 v202, v202\n\t" \
  "v_exp_f32 v203, v203\n\t" \
  "v_cvt_pk_bf16_f32 v204, v200, v201\n\t" \
  "v_cvt_pk_bf16_f32 v205, v202, v203\n\t" \
  "v_bfi_b32 v206, v46, 0, v204\n\t" \
  "v_bfi_b32 v207, v47, 0, v205\n\t" \
  "v_rcp_f32 v106, v208\n\t" \
  "v_and_b32 v204, v46, v204\n\t" \
  "v_and_b32 v205, v47, v205\n\t" \
  "v_pk_mul_f32 v[176:177], v[176:177], v[106:107] op_sel_hi:[1,0]\n\t" \
  "v_pk_mul_f32 v[178:179], v[178:179], v[106:107] op_sel_hi:[1,0]\n\t" \
  "v_mfma_f32_16x16x32_bf16 v[200:203], v[48:51], v[204:207], 0\n\t" \
  "v_pk_mul_f32 v[180:181], v[180:181], v[106:107] op_sel_hi:[1,0]\n\t" \
  "v_pk_mul_f32 v[182:183], v[182:183], v[106:107] op_sel_hi:[1,0]\n\t" \
  "v_mfma_f32_16x16x32_bf16 v[168:171], v[66:69], v[204:207], 0\n\t" \
  "v_cvt_pk_bf16_f32 v240, v176, v177\n\t" \
  "v_cvt_pk_bf16_f32 v241, v178, v179\n\t" \
  "v_mfma_f32_16x16x32_bf16 v[172:175], v[76:79], v[204:207], 0\n\t" \
  "v_cvt_pk_bf16_f32 v242, v180, v181\n\t" \
  "v_cvt_pk_bf16_f32 v243, v182, v183\n\t" \
  "global_store_dwordx4 v1, v[240:243], %14 offset:0 sc1\n\t" \
  "v_fma_f32 v192, v192, %16, v102\n\t" \
  "v_fma_f32 v193, v193, %16, v102\n\t" \
  "v_fma_f32 v194, v194, %16, v102\n\t" \
  "v_fma_f32 v195, v195, %16, v102\n\t" \
  "v_exp_f32 v192, v192\n\t" \
  "v_exp_f32 v193, v193\n\t" \
  "v_exp_f32 v194, v194\n\t" \
  "v_exp_f32 v195, v195\n\t" \
  "v_cvt_pk_bf16_f32 v196, v192, v193\n\t" \
  "v_cvt_pk_bf16_f32 v197, v194, v195\n\t" \
  "v_bfi_b32 v198, v46, 0, v196\n\t" \
  "v_bfi_b32 v199, v47, 0, v197\n\t" \
  "v_rcp_f32 v104, v200\n\t" \
  "v_and_b32 v196, v46, v196\n\t" \
  "v_and_b32 v197, v47, v197\n\t" \
  "v_pk_mul_f32 v[168:169], v[168:169], v[104:105] op_sel_hi:[1,0]\n\t" \
  "v_pk_mul_f32 v[170:171], v[170:171], v[104:105] op_sel_hi:[1,0]\n\t" \
  "v_mfma_f32_16x16x32_bf16 v[192:195], v[48:51], v[196:199], 0\n\t" \
  "v_pk_mul_f32 v[172:173], v[172:173], v[104:105] op_sel_hi:[1,0]\n\t" \
  "v_pk_mul_f32 v[174:175], v[174:175], v[104:105] op_sel_hi:[1,0]\n\t" \
  "v_mfma_f32_16x16x32_bf16 v[160:163], v[68:71], v[196:199], 0\n\t" \
  "v_cvt_pk_bf16_f32 v232, v168, v169\n\t" \
  "v_cvt_pk_bf16_f32 v233, v170, v171\n\t" \
  "v_mfma_f32_16x16x32_bf16 v[164:167], v[78:81], v[196:199], 0\n\t" \
  "v_cvt_pk_bf16_f32 v234, v172, v173\n\t" \
  "v_cvt_pk_bf16_f32 v235, v174, v175\n\t" \
  "global_store_dwordx4 v1, v[232:235], %14 offset:-4096 sc1\n\t" \
  "v_fma_f32 v184, v184, %16, v100\n\t" \
  "v_fma_f32 v185, v185, %16, v100\n\t" \
  "v_fma_f32 v186, v186, %16, v100\n\t" \
  "v_fma_f32 v187, v187, %16, v100\n\t" \
  "v_exp_f32 v184, v184\n\t" \
  "v_exp_f32 v185, v185\n\t" \
  "v_exp_f32 v186, v186\n\t" \
  "v_exp_f32 v187, v187\n\t" \
  "v_cvt_pk_bf16_f32 v188, v184, v185\n\t" \
  "v_cvt_pk_bf16_f32 v189, v186, v187\n\t" \
  "v_bfi_b32 v190, v46, 0, v188\n\t" \
  "v_bfi_b32 v191, v47, 0, v189\n\t" \
  "v_rcp_f32 v102, v192\n\t" \
  "v_and_b32 v188, v46, v188\n\t" \
  "v_and_b32 v189, v47, v189\n\t" \
  "v_pk_mul_f32 v[160:161], v[160:161], v[102:103] op_sel_hi:[1,0]\n\t" \
  "v_pk_mul_f32 v[162:163], v[162:163], v[102:103] op_sel_hi:[1,0]\n\t" \
  "v_mfma_f32_16x16x32_bf16 v[184:187], v[48:51], v[188:191], 0\n\t" \
  "v_pk_mul_f32 v[164:165], v[164:165], v[102:103] op_sel_hi:[1,0]\n\t" \
  "v_pk_mul_f32 v[166:167], v[166:167], v[102:103] op_sel_hi:[1,0]\n\t" \
  "v_mfma_f32_16x16x32_bf16 v[152:155], v[70:73], v[188:191], 0\n\t" \
  "v_cvt_pk_bf16_f32 v224, v160, v161\n\t" \
  "v_cvt_pk_bf16_f32 v225, v162, v163\n\t" \
  "v_mfma_f32_16x16x32_bf16 v[156:159], v[80:83], v[188:191], 0\n\t" \
  "v_cvt_pk_bf16_f32 v226, v164, v165\n\t" \
  "v_cvt_pk_bf16_f32 v227, v166, v167\n\t" \
  "global_store_dwordx4 v4, v[224:227], %14 offset:0 sc1\n\t" \
  "v_rcp_f32 v100, v184\n\t" \
  "ds_read_b128 v[60:63], v56 offset:64\n\t" \
  "ds_read_b64_tr_b16 v[108:109], v5 offset:0\n\t" \
  "ds_read_b64_tr_b16 v[110:111], v5 offset:16\n\t" \
  "ds_read_b128 v[112:115], v6 offset:16384\n\t" \
  "ds_read_b128 v[116:119], v6 offset:15360\n\t" \
  "506:\n\t" \
  "s_cbranch_execz 510f\n\t" \
  "ds_read_b64_tr_b16 v[120:121], v5 offset:512\n\t" \
  "ds_read_b64_tr_b16 v[122:123], v5 offset:528\n\t" \
  "ds_read_b128 v[124:127], v6 offset:14336\n\t" \
  "ds_read_b64_tr_b16 v[128:129], v5 offset:1024\n\t" \
  "ds_read_b64_tr_b16 v[130:131], v5 offset:1040\n\t" \
  "ds_read_b128 v[132:135], v6 offset:13312\n\t" \
  "ds_read_b64_tr_b16 v[136:137], v5 offset:1536\n\t" \
  "ds_read_b64_tr_b16 v[138:139], v5 offset:1552\n\t" \
  "ds_read_b128 v[140:143], v6 offset:12288\n\t" \
  "ds_read_b64_tr_b16 v[144:145], v5 offset:2048\n\t" \
  "v_pk_mul_f32 v[152:153], v[152:153], v[100:101] op_sel_hi:[1,0]\n\t" \
  "v_pk_mul_f32 v[154:155], v[154:155], v[100:101] op_sel_hi:[1,0]\n\t" \
  "v_cvt_pk_bf16_f32 v216, v152, v153\n\t" \
  "v_pk_mul_f32 v[156:157], v[156:157], v[100:101] op_sel_hi:[1,0]\n\t" \
  "v_pk_mul_f32 v[158:159], v[158:159], v[100:101] op_sel_hi:[1,0]\n\t" \
  "v_cvt_pk_bf16_f32 v217, v154, v155\n\t" \
  "v_cvt_pk_bf16_f32 v218, v156, v157\n\t" \
  "v_cvt_pk_bf16_f32 v219, v158, v159\n\t" \
  "global_store_dwordx4 v4, v[216:219], %14 offset:-4096 sc1\n\t" \
  "ds_write_b32 v32, v0 offset:112\n\t" \
  "s_branch 9f\n\t" \
  "5:\n\t" \
  "ds_read_b32 v52, v33 offset:48\n\t" \
  "ds_read_b64 v[72:73], v31\n\t" \
  "ds_read_b64 v[82:83], v31 offset:8\n\t" \
  "s_waitcnt lgkmcnt(0)\n\t" \
  "v_readfirstlane_b32 vcc_lo, v52\n\t" \
  "s_nop 1\n\t" \
  "s_cmp_eq_u32 vcc_lo, 0\n\t" \
  "s_cbranch_scc1 5b\n\t" \
  "s_branch 4b\n\t" \
  "15:\n\t" \
  "ds_read_b128 v[10:13], v14 offset:128\n\t" \
  "s_waitcnt lgkmcnt(0)\n\t" \
  "v_and_b32 v10, v10, v11\n\t" \
  "v_and_b32 v12, v12, v13\n\t" \
  "v_and_b32 v10, v10, v12\n\t" \
  "s_nop 1\n\t" \
  "v_readfirstlane_b32 vcc_lo, v10\n\t" \
  "s_nop 1\n\t" \
  "s_cmp_eq_u32 vcc_lo, 0\n\t" \
  "s_cbranch_scc1 15b\n\t" \
  "s_branch 14b\n\t" \
  "9:\n\t" \
  "507:\n\t" \
  "s_cbranch_execz 511f\n\t" \
  "s_waitcnt lgkmcnt(14)\n\t" \
  "v_and_b32 v60, v60, v61\n\t" \
  "v_and_b32 v62, v62, v63\n\t" \
  "v_mov_b32 v12, %13\n\t" \
  "v_and_b32 v60, v60, v62\n\t" \
  "s_nop 0\n\t" \
  "v_readfirstlane_b32 vcc_lo, v60\n\t" \
  "s_nop 1\n\t" \
  "s_cmp_eq_u32 vcc_lo, 0\n\t" \
  "s_cbranch_scc1 7f\n\t" \
  "6:\n\t" \
  "s_waitcnt lgkmcnt(11)\n\t" \
  "v_mfma_f32_16x16x32_bf16 v[192:195], v[108:111], v[112:115], 0\n\t" \
  "ds_read_b64_tr_b16 v[146:147], v5 offset:2064\n\t" \
  "ds_read_b128 v[148:151], v6 offset:11264\n\t" \
  "s_waitcnt lgkmcnt(12)\n\t" \
  "v_mfma_f32_16x16x32_bf16 v[196:199], v[108:111], v[116:119], 0\n\t" \
  "ds_read_b64_tr_b16 v[64:65], v5 offset:2560\n\t" \
  "ds_read_b64_tr_b16 v[66:67], v5 offset:2576\n\t" \
  "s_waitcnt lgkmcnt(12)\n\t" \
  "v_mfma_f32_16x16x32_bf16 v[196:199], v[120:123], v[112:115], v[196:199]\n\t" \
  "ds_read_b128 v[68:71], v6 offset:10240\n\t" \
  "ds_read_b64_tr_b16 v[72:73], v5 offset:3072\n\t" \
  "s_waitcnt lgkmcnt(13)\n\t" \
  "v_mfma_f32_16x16x32_bf16 v[200:203], v[108:111], v[124:127], 0\n\t" \
  "ds_read_b64_tr_b16 v[74:75], v5 offset:3088\n\t" \
  "ds_read_b128 v[76:79], v6 offset:9216\n\t" \
  "v_mfma_f32_16x16x32_bf16 v[200:203], v[120:123], v[116:119], v[200:203]\n\t" \
  "ds_read_b64_tr_b16 v[80:81], v5 offset:3584\n\t" \
  "ds_read_b64_tr_b16 v[82:83], v5 offset:3600\n\t" \
  "s_waitcnt lgkmcnt(15)\n\t" \
  "v_mfma_f32_16x16x32_bf16 v[200:203], v[128:131], v[112:115], v[200:203]\n\t" \
  "ds_read_b128 v[84:87], v6 offset:0\n\t" \
  "ds_read_b128 v[88:91], v6 offset:1024\n\t" \
  "s_waitcnt lgkmcnt(15)\n\t" \
  "v_mfma_f32_16x16x32_bf16 v[204:207], v[108:111], v[132:135], 0\n\t" \
  "ds_read_b128 v[92:95], v6 offset:2048\n\t" \
  "ds_read_b128 v[96:99], v6 offset:3072\n\t" \
  "v_mfma_f32_16x16x32_bf16 v[204:207], v[120:123], v[124:127], v[204:207]\n\t" \
  "ds_read_b128 v[100:103], v6 offset:4096\n\t" \
  "ds_read_b128 v[104:107], v6 offset:5120\n\t" \
  "v_mfma_f32_16x16x32_bf16 v[204:207], v[128:131], v[116:119], v[204:207]\n\t" \
  "ds_read_b128 v[152:155], v6 offset:6144\n\t" \
  "ds_read_b128 v[156:159], v6 offset:7168\n\t" \
  "s_waitcnt lgkmcnt(14)\n\t" \
  "v_mfma_f32_16x16x32_bf16 v[204:207], v[136:139], v[112:115], v[204:207]\n\t" \
  "v_lshl_add_u32 v12, v12, 12, v4\n\t" \
  "s_lshl_b32 s88, %13, 5\n\t" \
  "v_mfma_f32_16x16x32_bf16 v[208:211], v[108:111], v[140:143], 0\n\t" \
  "v_mfma_f32_16x16x32_bf16 v[208:211], v[120:123], v[132:135], v[208:211]\n\t" \
  "v_mfma_f32_16x16x32_bf16 v[208:211], v[128:131], v[124:127], v[208:211]\n\t" \
  "s_and_b32 s88, s88, 64\n\t" \
  "v_xor_b32 v13, s88, v54\n\t" \
  "v_mfma_f32_16x16x32_bf16 v[208:211], v[136:139], v[116:119], v[208:211]\n\t" \
  "v_mfma_f32_16x16x32_bf16 v[208:211], v[144:147], v[112:115], v[208:211]\n\t" \
  "v_mfma_f32_16x16x32_bf16 v[212:215], v[108:111], v[148:151], 0\n\t" \
  "ds_read_b32 v55, v56 offset:104\n\t" \
  "v_mfma_f32_16x16x32_bf16 v[212:215], v[120:123], v[140:143], v[212:215]\n\t" \
  "v_mfma_f32_16x16x32_bf16 v[212:215], v[128:131], v[132:135], v[212:215]\n\t" \
  "v_bfe_u32 v11, v12, 4, 4\n\t" \
  "s_mov_b32 s74, 0\n\t" \
  "v_mfma_f32_16x16x32_bf16 v[212:215], v[136:139], v[124:127], v[212:215]\n\t" \
  "v_lshlrev_b32 v11, 7, v11\n\t" \
  "v_mfma_f32_16x16x32_bf16 v[212:215], v[144:147], v[116:119], v[212:215]\n\t" \
  "v_mov_b32 v10, 1\n\t" \
  "v_mfma_f32_16x16x32_bf16 v[212:215], v[64:67], v[112:115], v[212:215]\n\t" \
  "s_getpc_b64 s[70:71]\n\t" \
  "24:\n\t" \
  "s_add_u32 s70, s70, 23f-24b\n\t" \
  "s_addc_u32 s71, s71, 0\n\t" \
  "s_waitcnt lgkmcnt(1)\n\t" \
  "v_mfma_f32_16x16x32_bf16 v[216:219], v[108:111], v[68:71], 0\n\t" \
  "v_mfma_f32_16x16x32_bf16 v[216:219], v[120:123], v[148:151], v[216:219]\n\t" \
  "v_and_b32 v1, 0x180, v4\n\t" \
  "v_mfma_f32_16x16x32_bf16 v[216:219], v[128:131], v[140:143], v[216:219]\n\t" \
  "v_and_b32 v2, 0x70, v4\n\t" \
  "v_mfma_f32_16x16x32_bf16 v[216:219], v[136:139], v[132:135], v[216:219]\n\t" \
  "v_lshlrev_b32 v2, 3, v2\n\t" \
  "v_mfma_f32_16x16x32_bf16 v[216:219], v[144:147], v[124:127], v[216:219]\n\t" \
  "v_lshl_add_u32 v1, v1, 5, v2\n\t" \
  "v_mfma_f32_16x16x32_bf16 v[216:219], v[64:67], v[116:119], v[216:219]\n\t" \
  "v_and_b32 v2, 0x2000, v1\n\t" \
  "v_mfma_f32_16x16x32_bf16 v[216:219], v[72:75], v[112:115], v[216:219]\n\t" \
  "v_add_u32 v1, v1, v2\n\t" \
  "v_mfma_f32_16x16x32_bf16 v[220:223], v[108:111], v[76:79], 0\n\t" \
  "v_mfma_f32_16x16x32_bf16 v[220:223], v[120:123], v[68:71], v[220:223]\n\t" \
  "v_mfma_f32_16x16x32_bf16 v[220:223], v[128:131], v[148:151], v[220:223]\n\t" \
  "v_mfma_f32_16x16x32_bf16 v[220:223], v[136:139], v[140:143], v[220:223]\n\t" \
  "v_mfma_f32_16x16x32_bf16 v[220:223], v[144:147], v[132:135], v[220:223]\n\t" \
  "ds_read_b64_tr_b16 v[160:161], v5 offset:128\n\t" \
  "v_mfma_f32_16x16x32_bf16 v[220:223], v[64:67], v[124:127], v[220:223]\n\t" \
  "ds_read_b64_tr_b16 v[162:163], v5 offset:144\n\t" \
  "v_mfma_f32_16x16x32_bf16 v[220:223], v[72:75], v[116:119], v[220:223]\n\t" \
  "ds_read_b128 v[164:167], v7 offset:16384\n\t" \
  "v_mfma_f32_16x16x32_bf16 v[220:223], v[80:83], v[112:115], v[220:223]\n\t" \
  "ds_read_b128 v[168:171], v7 offset:15360\n\t" \
  "v_mfma_f32_16x16x32_bf16 v[252:255], v[108:111], v[84:87], 0\n\t" \
  "ds_read_b64_tr_b16 v[172:173], v5 offset:640\n\t" \
  "v_mfma_f32_16x16x32_bf16 v[252:255], v[120:123], v[88:91], v[252:255]\n\t" \
  "ds_read_b64_tr_b16 v[174:175], v5 offset:656\n\t" \
  "v_mfma_f32_16x16x32_bf16 v[252:255], v[128:131], v[92:95], v[252:255]\n\t" \
  "ds_read_b128 v[176:179], v7 offset:14336\n\t" \
  "v_mfma_f32_16x16x32_bf16 v[252:255], v[136:139], v[96:99], v[252:255]\n\t" \
  "ds_read_b64_tr_b16 v[180:181], v5 offset:1152\n\t" \
  "v_mfma_f32_16x16x32_bf16 v[252:255], v[144:147], v[100:103], v[252:255]\n\t" \
  "ds_read_b64_tr_b16 v[182:183], v5 offset:1168\n\t" \
  "v_mfma_f32_16x16x32_bf16 v[252:255], v[64:67], v[104:107], v[252:255]\n\t" \
  "ds_read_b128 v[184:187], v7 offset:13312\n\t" \
  "v_mfma_f32_16x16x32_bf16 v[252:255], v[72:75], v[152:155], v[252:255]\n\t" \
  "ds_read_b64_tr_b16 v[188:189], v5 offset:1664\n\t" \
  "v_mfma_f32_16x16x32_bf16 v[252:255], v[80:83], v[156:159], v[252:255]\n\t" \
  "ds_read_b64_tr_b16 v[190:191], v5 offset:1680\n\t" \
  "v_mfma_f32_16x16x32_bf16 v[248:251], v[108:111], v[88:91], 0\n\t" \
  "ds_read_b128 v[76:79], v7 offset:12288\n\t" \
  "v_mfma_f32_16x16x32_bf16 v[248:251], v[120:123], v[92:95], v[248:251]\n\t" \
  "v_mfma_f32_16x16x32_bf16 v[248:251], v[128:131], v[96:99], v[248:251]\n\t" \
  "ds_read_b64_tr_b16 v[68:69], v5 offset:2176\n\t" \
  "v_mfma_f32_16x16x32_bf16 v[248:251], v[136:139], v[100:103], v[248:251]\n\t" \
  "ds_read_b64_tr_b16 v[70:71], v5 offset:2192\n\t" \
  "v_mfma_f32_16x16x32_bf16 v[248:251], v[144:147], v[104:107], v[248:251]\n\t" \
  "ds_read_b128 v[148:151], v7 offset:11264\n\t" \
  "v_mfma_f32_16x16x32_bf16 v[248:251], v[64:67], v[152:155], v[248:251]\n\t" \
  "v_mfma_f32_16x16x32_bf16 v[248:251], v[72:75], v[156:159], v[248:251]\n\t" \
  "v_mfma_f32_16x16x32_bf16 v[244:247], v[108:111], v[92:95], 0\n\t" \
  "v_mfma_f32_16x16x32_bf16 v[244:247], v[120:123], v[96:99], v[244:247]\n\t" \
  "ds_read_b64_tr_b16 v[140:141], v5 offset:2688\n\t" \
  "v_mfma_f32_16x16x32_bf16 v[244:247], v[128:131], v[100:103], v[244:247]\n\t" \
  "ds_read_b64_tr_b16 v[142:143], v5 offset:2704\n\t" \
  "v_mfma_f32_16x16x32_bf16 v[244:247], v[136:139], v[104:107], v[244:247]\n\t" \
  "ds_read_b128 v[132:135], v7 offset:10240\n\t" \
  "v_mfma_f32_16x16x32_bf16 v[244:247], v[144:147], v[152:155], v[244:247]\n\t" \
  "v_mfma_f32_16x16x32_bf16 v[244:247], v[64:67], v[156:159], v[244:247]\n\t" \
  "v_mfma_f32_16x16x32_bf16 v[240:243], v[108:111], v[96:99], 0\n\t" \
  "v_mfma_f32_16x16x32_bf16 v[240:243], v[120:123], v[100:103], v[240:243]\n\t" \
  "v_mfma_f32_16x16x32_bf16 v[240:243], v[128:131], v[104:107], v[240:243]\n\t" \
  "ds_read_b64_tr_b16 v[124:125], v5 offset:3200\n\t" \
  "v_mfma_f32_16x16x32_bf16 v[240:243], v[136:139], v[152:155], v[240:243]\n\t" \
  "ds_read_b64_tr_b16 v[126:127], v5 offset:3216\n\t" \
  "v_mfma_f32_16x16x32_bf16 v[240:243], v[144:147], v[156:159], v[240:243]\n\t" \
  "ds_read_b128 v[116:119], v7 offset:9216\n\t" \
  "v_mfma_f32_16x16x32_bf16 v[236:239], v[108:111], v[100:103], 0\n\t" \
  "v_mfma_f32_16x16x32_bf16 v[236:239], v[120:123], v[104:107], v[236:239]\n\t" \
  "s_getreg_b32 vcc_lo, hwreg(HW_REG_IB_STS, 0, 4)\n\t" \
  "v_mfma_f32_16x16x32_bf16 v[236:239], v[128:131], v[152:155], v[236:239]\n\t" \
  "s_or_b32 vcc_lo, vcc_lo, s74\n\t" \
  "s_cbranch_scc1 30f\n\t" \
  "s_swappc_b64 s[68:69], s[70:71]\n\t" \
  "30:\n\t" \
  "v_mfma_f32_16x16x32_bf16 v[236:239], v[136:139], v[156:159], v[236:239]\n\t" \
  "v_mfma_f32_16x16x32_bf16 v[232:235], v[108:111], v[104:107], 0\n\t" \
  "v_mfma_f32_16x16x32_bf16 v[232:235], v[120:123], v[152:155], v[232:235]\n\t" \
  "ds_read_b64_tr_b16 v[64:65], v5 offset:3712\n\t" \
  "v_mfma_f32_16x16x32_bf16 v[232:235], v[128:131], v[156:159], v[232:235]\n\t" \
  "ds_read_b64_tr_b16 v[66:67], v5 offset:3728\n\t" \
  "v_mfma_f32_16x16x32_bf16 v[228:231], v[108:111], v[152:155], 0\n\t" \
  "ds_read_b128 v[72:75], v7 offset:0\n\t" \
  "v_mfma_f32_16x16x32_bf16 v[228:231], v[120:123], v[156:159], v[228:231]\n\t" \
  "ds_read_b128 v[80:83], v7 offset:1024\n\t" \
  "v_mfma_f32_16x16x32_bf16 v[224:227], v[108:111], v[156:159], 0\n\t" \
  "ds_read_b128 v[84:87], v7 offset:2048\n\t" \
  "s_waitcnt lgkmcnt(5)\n\t" \
  "v_mfma_f32_16x16x32_bf16 v[192:195], v[160:163], v[164:167], v[192:195]\n\t" \
  "ds_read_b128 v[88:91], v7 offset:3072\n\t" \
  "v_mfma_f32_16x16x32_bf16 v[196:199], v[160:163], v[168:171], v[196:199]\n\t" \
  "ds_read_b128 v[92:95], v7 offset:4096\n\t" \
  "v_mfma_f32_16x16x32_bf16 v[196:199], v[172:175], v[164:167], v[196:199]\n\t" \
  "ds_read_b128 v[108:111], v7 offset:5120\n\t" \
  "v_mfma_f32_16x16x32_bf16 v[200:203], v[160:163], v[176:179], v[200:203]\n\t" \
  "ds_read_b128 v[112:115], v7 offset:6144\n\t" \
  "v_mfma_f32_16x16x32_bf16 v[200:203], v[172:175], v[168:171], v[200:203]\n\t" \
  "ds_read_b128 v[120:123], v7 offset:7168\n\t" \
  "v_mfma_f32_16x16x32_bf16 v[200:203], v[180:183], v[164:167], v[200:203]\n\t" \
  "v_mfma_f32_16x16x32_bf16 v[204:207], v[160:163], v[184:187], v[204:207]\n\t" \
  "v_mfma_f32_16x16x32_bf16 v[204:207], v[172:175], v[176:179], v[204:207]\n\t" \
  "v_mfma_f32_16x16x32_bf16 v[204:207], v[180:183], v[168:171], v[204:207]\n\t" \
  "v_mfma_f32_16x16x32_bf16 v[204:207], v[188:191], v[164:167], v[204:207]\n\t" \
  "v_mfma_f32_16x16x32_bf16 v[208:211], v[160:163], v[76:79], v[208:211]\n\t" \
  "v_mfma_f32_16x16x32_bf16 v[208:211], v[172:175], v[184:187], v[208:211]\n\t" \
  "s_getreg_b32 vcc_lo, hwreg(HW_REG_IB_STS, 0, 4)\n\t" \
  "v_mfma_f32_16x16x32_bf16 v[208:211], v[180:183], v[176:179], v[208:211]\n\t" \
  "s_or_b32 vcc_lo, vcc_lo, s74\n\t" \
  "s_cbranch_scc1 31f\n\t" \
  "s_swappc_b64 s[68:69], s[70:71]\n\t" \
  "31:\n\t" \
  "v_mfma_f32_16x16x32_bf16 v[208:211], v[188:191], v[168:171], v[208:211]\n\t" \
  "v_mfma_f32_16x16x32_bf16 v[208:211], v[68:71], v[164:167], v[208:211]\n\t" \
  "v_mfma_f32_16x16x32_bf16 v[212:215], v[160:163], v[148:151], v[212:215]\n\t" \
  "v_mfma_f32_16x16x32_bf16 v[212:215], v[172:175], v[76:79], v[212:215]\n\t" \
  "v_mfma_f32_16x16x32_bf16 v[212:215], v[180:183], v[184:187], v[212:215]\n\t" \
  "v_mfma_f32_16x16x32_bf16 v[212:215], v[188:191], v[176:179], v[212:215]\n\t" \
  "v_mfma_f32_16x16x32_bf16 v[212:215], v[68:71], v[168:171], v[212:215]\n\t" \
  "v_mfma_f32_16x16x32_bf16 v[212:215], v[140:143], v[164:167], v[212:215]\n\t" \
  "v_mfma_f32_16x16x32_bf16 v[216:219], v[160:163], v[132:135], v[216:219]\n\t" \
  "v_mfma_f32_16x16x32_bf16 v[216:219], v[172:175], v[148:151], v[216:219]\n\t" \
  "v_mfma_f32_16x16x32_bf16 v[216:219], v[180:183], v[76:79], v[216:219]\n\t" \
  "v_mfma_f32_16x16x32_bf16 v[216:219], v[188:191], v[184:187], v[216:219]\n\t" \
  "v_mfma_f32_16x16x32_bf16 v[216:219], v[68:71], v[176:179], v[216:219]\n\t" \
  "v_mfma_f32_16x16x32_bf16 v[216:219], v[140:143], v[168:171], v[216:219]\n\t" \
  "v_mfma_f32_16x16x32_bf16 v[216:219], v[124:127], v[164:167], v[216:219]\n\t" \
  "v_mfma_f32_16x16x32_bf16 v[220:223], v[160:163], v[116:119], v[220:223]\n\t" \
  "v_mfma_f32_16x16x32_bf16 v[220:223], v[172:175], v[132:135], v[220:223]\n\t" \
  "v_mfma_f32_16x16x32_bf16 v[220:223], v[180:183], v[148:151], v[220:223]\n\t" \
  "v_mfma_f32_16x16x32_bf16 v[220:223], v[188:191], v[76:79], v[220:223]\n\t" \
  "v_mfma_f32_16x16x32_bf16 v[220:223], v[68:71], v[184:187], v[220:223]\n\t" \
  "s_cmp_lg_u32 s74, 0\n\t" \
  "s_cbranch_scc1 32f\n\t" \
  "s_swappc_b64 s[68:69], s[70:71]\n\t" \
  "32:\n\t" \
  "v_mfma_f32_16x16x32_bf16 v[220:223], v[140:143], v[176:179], v[220:223]\n\t" \
  "ds_read_b64_tr_b16 v[76:77], v5 offset:256\n\t" \
  "v_mfma_f32_16x16x32_bf16 v[220:223], v[124:127], v[168:171], v[220:223]\n\t" \
  "ds_read_b64_tr_b16 v[78:79], v5 offset:272\n\t" \
  "s_waitcnt lgkmcnt(2)\n\t" \
  "v_mfma_f32_16x16x32_bf16 v[220:223], v[64:67], v[164:167], v[220:223]\n\t" \
  "s_getpc_b64 s[70:71]\n\t" \
  "24:\n\t" \
  "s_add_u32 s70, s70, 25f-24b\n\t" \
  "s_addc_u32 s71, s71, 0\n\t" \
  "v_mfma_f32_16x16x32_bf16 v[252:255], v[160:163], v[72:75], v[252:255]\n\t" \
  "ds_read_b128 v[128:131], v8 offset:16384\n\t" \
  "v_mfma_f32_16x16x32_bf16 v[252:255], v[172:175], v[80:83], v[252:255]\n\t" \
  "ds_read_b128 v[136:139], v8 offset:15360\n\t" \
  "v_mfma_f32_16x16x32_bf16 v[252:255], v[180:183], v[84:87], v[252:255]\n\t" \
  "v_and_b32 v53, 0x2000, v12\n\t" \
  "ds_read_b64_tr_b16 v[144:145], v5 offset:768\n\t" \
  "v_mfma_f32_16x16x32_bf16 v[252:255], v[188:191], v[88:91], v[252:255]\n\t" \
  "ds_read_b64_tr_b16 v[146:147], v5 offset:784\n\t" \
  "v_mfma_f32_16x16x32_bf16 v[252:255], v[68:71], v[92:95], v[252:255]\n\t" \
  "v_add_u32 v53, v53, v12\n\t" \
  "ds_read_b128 v[96:99], v8 offset:14336\n\t" \
  "v_mfma_f32_16x16x32_bf16 v[252:255], v[140:143], v[108:111], v[252:255]\n\t" \
  "ds_read_b64_tr_b16 v[72:73], v5 offset:1280\n\t" \
  "v_mfma_f32_16x16x32_bf16 v[252:255], v[124:127], v[112:115], v[252:255]\n\t" \
  "v_lshrrev_b32 v14, 1, v4\n\t" \
  "ds_read_b64_tr_b16 v[74:75], v5 offset:1296\n\t" \
  "v_mfma_f32_16x16x32_bf16 v[252:255], v[64:67], v[120:123], v[252:255]\n\t" \
  "ds_read_b128 v[100:103], v8 offset:13312\n\t" \
  "v_mfma_f32_16x16x32_bf16 v[248:251], v[160:163], v[80:83], v[248:251]\n\t" \
  "v_lshrrev_b32 v15, 24, v3\n\t" \
  "ds_read_b64_tr_b16 v[104:105], v5 offset:1792\n\t" \
  "v_mfma_f32_16x16x32_bf16 v[248:251], v[172:175], v[84:87], v[248:251]\n\t" \
  "ds_read_b64_tr_b16 v[106:107], v5 offset:1808\n\t" \
  "v_mfma_f32_16x16x32_bf16 v[248:251], v[180:183], v[88:91], v[248:251]\n\t" \
  "v_add_u32 v14, %20, v14\n\t" \
  "ds_read_b128 v[152:155], v8 offset:12288\n\t" \
  "v_mfma_f32_16x16x32_bf16 v[248:251], v[188:191], v[92:95], v[248:251]\n\t" \
  "ds_read_b64_tr_b16 v[64:65], v5 offset:2304\n\t" \
  "v_mfma_f32_16x16x32_bf16 v[248:251], v[68:71], v[108:111], v[248:251]\n\t" \
  "v_lshrrev_b32 v18, 10, v12\n\t" \
  "ds_read_b64_tr_b16 v[66:67], v5 offset:2320\n\t" \
  "v_mfma_f32_16x16x32_bf16 v[248:251], v[140:143], v[112:115], v[248:251]\n\t" \
  "ds_read_b128 v[80:83], v8 offset:11264\n\t" \
  "v_mfma_f32_16x16x32_bf16 v[248:251], v[124:127], v[120:123], v[248:251]\n\t" \
  "v_and_b32 v18, 12, v18\n\t" \
  "v_mfma_f32_16x16x32_bf16 v[244:247], v[160:163], v[84:87], v[244:247]\n\t" \
  "v_mfma_f32_16x16x32_bf16 v[244:247], v[172:175], v[88:91], v[244:247]\n\t" \
  "v_mov_b32 v17, 16\n\t" \
  "ds_read_b64_tr_b16 v[84:85], v5 offset:2816\n\t" \
  "v_mfma_f32_16x16x32_bf16 v[244:247], v[180:183], v[92:95], v[244:247]\n\t" \
  "ds_read_b64_tr_b16 v[86:87], v5 offset:2832\n\t" \
  "v_mfma_f32_16x16x32_bf16 v[244:247], v[188:191], v[108:111], v[244:247]\n\t" \
  "v_add_u32 v18, v56, v18\n\t" \
  "ds_read_b128 v[156:159], v8 offset:10240\n\t" \
  "v_mfma_f32_16x16x32_bf16 v[244:247], v[68:71], v[112:115], v[244:247]\n\t" \
  "v_mfma_f32_16x16x32_bf16 v[244:247], v[140:143], v[120:123], v[244:247]\n\t" \
  "s_mov_b32 s76, 0\n\t" \
  "v_mfma_f32_16x16x32_bf16 v[240:243], v[160:163], v[88:91], v[240:243]\n\t" \
  "v_mfma_f32_16x16x32_bf16 v[240:243], v[172:175], v[92:95], v[240:243]\n\t" \
  "s_brev_b32 s77, 1\n\t" \
  "v_mfma_f32_16x16x32_bf16 v[240:243], v[180:183], v[108:111], v[240:243]\n\t" \
  "ds_read_b64_tr_b16 v[88:89], v5 offset:3328\n\t" \
  "v_mfma_f32_16x16x32_bf16 v[240:243], v[188:191], v[112:115], v[240:243]\n\t" \
  "s_mov_b32 s75, 0\n\t" \
  "ds_read_b64_tr_b16 v[90:91], v5 offset:3344\n\t" \
  "v_mfma_f32_16x16x32_bf16 v[240:243], v[68:71], v[120:123], v[240:243]\n\t" \
  "ds_read_b128 v[164:167], v8 offset:9216\n\t" \
  "v_mfma_f32_16x16x32_bf16 v[236:239], v[160:163], v[92:95], v[236:239]\n\t" \
  "v_add_u32 v28, 0, v13\n\t" \
  "v_mfma_f32_16x16x32_bf16 v[236:239], v[172:175], v[108:111], v[236:239]\n\t" \
  "v_mfma_f32_16x16x32_bf16 v[236:239], v[180:183], v[112:115], v[236:239]\n\t" \
  "v_xor_b32 v29, 32, v28\n\t" \
  "v_mfma_f32_16x16x32_bf16 v[236:239], v[188:191], v[120:123], v[236:239]\n\t" \
  "v_mfma_f32_16x16x32_bf16 v[232:235], v[160:163], v[108:111], v[232:235]\n\t" \
  "v_xor_b32 v30, 64, v28\n\t" \
  "v_mfma_f32_16x16x32_bf16 v[232:235], v[172:175], v[112:115], v[232:235]\n\t" \
  "ds_read_b64_tr_b16 v[68:69], v5 offset:3840\n\t" \
  "v_mfma_f32_16x16x32_bf16 v[232:235], v[180:183], v[120:123], v[232:235]\n\t" \
  "v_xor_b32 v31, 96, v28\n\t" \
  "ds_read_b64_tr_b16 v[70:71], v5 offset:3856\n\t" \
  "v_mfma_f32_16x16x32_bf16 v[228:231], v[160:163], v[112:115], v[228:231]\n\t" \
  "ds_read_b128 v[92:95], v8 offset:0\n\t" \
  "v_mfma_f32_16x16x32_bf16 v[228:231], v[172:175], v[120:123], v[228:231]\n\t" \
  "v_add_u32 v32, 65536, v13\n\t" \
  "ds_read_b128 v[168:171], v8 offset:1024\n\t" \
  "v_mfma_f32_16x16x32_bf16 v[224:227], v[160:163], v[120:123], v[224:227]\n\t" \
  "ds_read_b128 v[172:175], v8 offset:2048\n\t" \
  "s_waitcnt lgkmcnt(5)\n\t" \
  "v_mfma_f32_16x16x32_bf16 v[192:195], v[76:79], v[128:131], v[192:195]\n\t" \
  "v_xor_b32 v33, 32, v32\n\t" \
  "ds_read_b128 v[160:163], v8 offset:3072\n\t" \
  "v_mfma_f32_16x16x32_bf16 v[196:199], v[76:79], v[136:139], v[196:199]\n\t" \
  "ds_read_b128 v[176:179], v8 offset:4096\n\t" \
  "v_mfma_f32_16x16x32_bf16 v[196:199], v[144:147], v[128:131], v[196:199]\n\t" \
  "v_xor_b32 v34, 64, v32\n\t" \
  "ds_read_b128 v[180:183], v8 offset:5120\n\t" \
  "v_mfma_f32_16x16x32_bf16 v[200:203], v[76:79], v[96:99], v[200:203]\n\t" \
  "ds_read_b128 v[184:187], v8 offset:6144\n\t" \
  "v_mfma_f32_16x16x32_bf16 v[200:203], v[144:147], v[136:139], v[200:203]\n\t" \
  "v_xor_b32 v35, 96, v32\n\t" \
  "ds_read_b128 v[188:191], v8 offset:7168\n\t" \
  "v_mfma_f32_16x16x32_bf16 v[200:203], v[72:75], v[128:131], v[200:203]\n\t" \
  "v_mfma_f32_16x16x32_bf16 v[204:207], v[76:79], v[100:103], v[204:207]\n\t" \
  "v_add_u32 v36, 131072, v13\n\t" \
  "v_mfma_f32_16x16x32_bf16 v[204:207], v[144:147], v[96:99], v[204:207]\n\t" \
  "v_mfma_f32_16x16x32_bf16 v[204:207], v[72:75], v[136:139], v[204:207]\n\t" \
  "v_xor_b32 v37, 32, v36\n\t" \
  "v_mfma_f32_16x16x32_bf16 v[204:207], v[104:107], v[128:131], v[204:207]\n\t" \
  "v_mfma_f32_16x16x32_bf16 v[208:211], v[76:79], v[152:155], v[208:211]\n\t" \
  "v_xor_b32 v38, 64, v36\n\t" \
  "v_mfma_f32_16x16x32_bf16 v[208:211], v[144:147], v[100:103], v[208:211]\n\t" \
  "v_mfma_f32_16x16x32_bf16 v[208:211], v[72:75], v[96:99], v[208:211]\n\t" \
  "v_xor_b32 v39, 96, v36\n\t" \
  "v_mfma_f32_16x16x32_bf16 v[208:211], v[104:107], v[136:139], v[208:211]\n\t" \
  "v_mfma_f32_16x16x32_bf16 v[208:211], v[64:67], v[128:131], v[208:211]\n\t" \
  "v_lshrrev_b32 v57, 5, v4\n\t" \
  "v_mfma_f32_16x16x32_bf16 v[212:215], v[76:79], v[80:83], v[212:215]\n\t" \
  "v_mfma_f32_16x16x32_bf16 v[212:215], v[144:147], v[152:155], v[212:215]\n\t" \
  "v_and_b32 v57, 0x18, v57\n\t" \
  "v_mfma_f32_16x16x32_bf16 v[212:215], v[72:75], v[100:103], v[212:215]\n\t" \
  "v_mfma_f32_16x16x32_bf16 v[212:215], v[104:107], v[96:99], v[212:215]\n\t" \
  "v_and_b32 v62, 0x70, v4\n\t" \
  "v_mfma_f32_16x16x32_bf16 v[212:215], v[64:67], v[136:139], v[212:215]\n\t" \
  "v_mfma_f32_16x16x32_bf16 v[212:215], v[84:87], v[128:131], v[212:215]\n\t" \
  "v_xor_b32 v57, v57, v62\n\t" \
  "v_mfma_f32_16x16x32_bf16 v[216:219], v[76:79], v[156:159], v[216:219]\n\t" \
  "v_mfma_f32_16x16x32_bf16 v[216:219], v[144:147], v[80:83], v[216:219]\n\t" \
  "v_and_b32 v58, 0xf0, v4\n\t" \
  "v_mfma_f32_16x16x32_bf16 v[216:219], v[72:75], v[152:155], v[216:219]\n\t" \
  "v_mfma_f32_16x16x32_bf16 v[216:219], v[104:107], v[100:103], v[216:219]\n\t" \
  "v_lshl_add_u32 v57, v58, 3, v57\n\t" \
  "v_mfma_f32_16x16x32_bf16 v[216:219], v[64:67], v[96:99], v[216:219]\n\t" \
  "v_mfma_f32_16x16x32_bf16 v[216:219], v[84:87], v[136:139], v[216:219]\n\t" \
  "v_add_u32 v57, %23, v57\n\t" \
  "v_mfma_f32_16x16x32_bf16 v[216:219], v[88:91], v[128:131], v[216:219]\n\t" \
  "v_mfma_f32_16x16x32_bf16 v[220:223], v[76:79], v[164:167], v[220:223]\n\t" \
  "v_xor_b32 v58, 32, v57\n\t" \
  "v_mfma_f32_16x16x32_bf16 v[220:223], v[144:147], v[156:159], v[220:223]\n\t" \
  "v_mfma_f32_16x16x32_bf16 v[220:223], v[72:75], v[80:83], v[220:223]\n\t" \
  "v_xor_b32 v59, 64, v57\n\t" \
  "v_mfma_f32_16x16x32_bf16 v[220:223], v[104:107], v[152:155], v[220:223]\n\t" \
  "v_mfma_f32_16x16x32_bf16 v[220:223], v[64:67], v[100:103], v[220:223]\n\t" \
  "v_xor_b32 v60, 96, v57\n\t" \
  "ds_read_b64_tr_b16 v[148:149], v5 offset:384\n\t" \
  "v_mfma_f32_16x16x32_bf16 v[220:223], v[84:87], v[96:99], v[220:223]\n\t" \
  "ds_read_b64_tr_b16 v[150:151], v5 offset:400\n\t" \
  "v_mfma_f32_16x16x32_bf16 v[220:223], v[88:91], v[136:139], v[220:223]\n\t" \
  "v_lshrrev_b32 v61, 3, v4\n\t" \
  "ds_read_b128 v[80:83], v9 offset:16384\n\t" \
  "s_waitcnt lgkmcnt(3)\n\t" \
  "v_mfma_f32_16x16x32_bf16 v[220:223], v[68:71], v[128:131], v[220:223]\n\t" \
  "ds_read_b128 v[140:143], v9 offset:15360\n\t" \
  "v_mfma_f32_16x16x32_bf16 v[252:255], v[76:79], v[92:95], v[252:255]\n\t" \
  "v_and_b32 v61, 0x70, v61\n\t" \
  "ds_read_b64_tr_b16 v[132:133], v5 offset:896\n\t" \
  "v_mfma_f32_16x16x32_bf16 v[252:255], v[144:147], v[168:171], v[252:255]\n\t" \
  "ds_read_b64_tr_b16 v[134:135], v5 offset:912\n\t" \
  "v_mfma_f32_16x16x32_bf16 v[252:255], v[72:75], v[172:175], v[252:255]\n\t" \
  "v_xor_b32 v61, v61, v4\n\t" \
  "ds_read_b128 v[124:127], v9 offset:14336\n\t" \
  "v_mfma_f32_16x16x32_bf16 v[252:255], v[104:107], v[160:163], v[252:255]\n\t" \
  "ds_read_b64_tr_b16 v[116:117], v5 offset:1408\n\t" \
  "v_mfma_f32_16x16x32_bf16 v[252:255], v[64:67], v[176:179], v[252:255]\n\t" \
  "v_add_u32 v61, %23, v61\n\t" \
  "ds_read_b64_tr_b16 v[118:119], v5 offset:1424\n\t" \
  "v_mfma_f32_16x16x32_bf16 v[252:255], v[84:87], v[180:183], v[252:255]\n\t" \
  "ds_read_b128 v[92:95], v9 offset:13312\n\t" \
  "v_mfma_f32_16x16x32_bf16 v[252:255], v[88:91], v[184:187], v[252:255]\n\t" \
  "v_and_b32 v63, 0x380, v4\n\t" \
  "ds_read_b64_tr_b16 v[108:109], v5 offset:1920\n\t" \
  "v_mfma_f32_16x16x32_bf16 v[252:255], v[68:71], v[188:191], v[252:255]\n\t" \
  "ds_read_b64_tr_b16 v[110:111], v5 offset:1936\n\t" \
  "v_mfma_f32_16x16x32_bf16 v[248:251], v[76:79], v[168:171], v[248:251]\n\t" \
  "v_lshl_add_u32 v62, v63, 6, v62\n\t" \
  "ds_read_b128 v[112:115], v9 offset:12288\n\t" \
  "v_mfma_f32_16x16x32_bf16 v[248:251], v[144:147], v[172:175], v[248:251]\n\t" \
  "v_mfma_f32_16x16x32_bf16 v[248:251], v[72:75], v[160:163], v[248:251]\n\t" \
  "v_add_u32 v63, 0x10000, v62\n\t" \
  "ds_read_b64_tr_b16 v[120:121], v5 offset:2432\n\t" \
  "v_mfma_f32_16x16x32_bf16 v[248:251], v[104:107], v[176:179], v[248:251]\n\t" \
  "ds_read_b64_tr_b16 v[122:123], v5 offset:2448\n\t" \
  "v_mfma_f32_16x16x32_bf16 v[248:251], v[64:67], v[180:183], v[248:251]\n\t" \
  "ds_read_b128 v[68:71], v9 offset:11264\n\t" \
  "v_mfma_f32_16x16x32_bf16 v[248:251], v[84:87], v[184:187], v[248:251]\n\t" \
  "v_mfma_f32_16x16x32_bf16 v[248:251], v[88:91], v[188:191], v[248:251]\n\t" \
  "v_mfma_f32_16x16x32_bf16 v[244:247], v[76:79], v[172:175], v[244:247]\n\t" \
  "v_mfma_f32_16x16x32_bf16 v[244:247], v[144:147], v[160:163], v[244:247]\n\t" \
  "ds_read_b64_tr_b16 v[128:129], v5 offset:2944\n\t" \
  "v_mfma_f32_16x16x32_bf16 v[244:247], v[72:75], v[176:179], v[244:247]\n\t" \
  "ds_read_b64_tr_b16 v[130:131], v5 offset:2960\n\t" \
  "v_mfma_f32_16x16x32_bf16 v[244:247], v[104:107], v[180:183], v[244:247]\n\t" \
  "ds_read_b128 v[88:91], v9 offset:10240\n\t" \
  "v_mfma_f32_16x16x32_bf16 v[244:247], v[64:67], v[184:187], v[244:247]\n\t" \
  "v_mfma_f32_16x16x32_bf16 v[244:247], v[84:87], v[188:191], v[244:247]\n\t" \
  "v_mfma_f32_16x16x32_bf16 v[240:243], v[76:79], v[160:163], v[240:243]\n\t" \
  "v_mfma_f32_16x16x32_bf16 v[240:243], v[144:147], v[176:179], v[240:243]\n\t" \
  "v_mfma_f32_16x16x32_bf16 v[240:243], v[72:75], v[180:183], v[240:243]\n\t" \
  "ds_read_b64_tr_b16 v[136:137], v5 offset:3456\n\t" \
  "v_mfma_f32_16x16x32_bf16 v[240:243], v[104:107], v[184:187], v[240:243]\n\t" \
  "ds_read_b64_tr_b16 v[138:139], v5 offset:3472\n\t" \
  "v_mfma_f32_16x16x32_bf16 v[240:243], v[64:67], v[188:191], v[240:243]\n\t" \
  "ds_read_b128 v[84:87], v9 offset:9216\n\t" \
  "v_mfma_f32_16x16x32_bf16 v[236:239], v[76:79], v[176:179], v[236:239]\n\t" \
  "v_mfma_f32_16x16x32_bf16 v[236:239], v[144:147], v[180:183], v[236:239]\n\t" \
  "v_mfma_f32_16x16x32_bf16 v[236:239], v[72:75], v[184:187], v[236:239]\n\t" \
  "v_mfma_f32_16x16x32_bf16 v[236:239], v[104:107], v[188:191], v[236:239]\n\t" \
  "v_mfma_f32_16x16x32_bf16 v[232:235], v[76:79], v[180:183], v[232:235]\n\t" \
  "v_mfma_f32_16x16x32_bf16 v[232:235], v[144:147], v[184:187], v[232:235]\n\t" \
  "ds_read_b64_tr_b16 v[96:97], v5 offset:3968\n\t" \
  "v_mfma_f32_16x16x32_bf16 v[232:235], v[72:75], v[188:191], v[232:235]\n\t" \
  "ds_read_b64_tr_b16 v[98:99], v5 offset:3984\n\t" \
  "ds_write_b32 v18, v17 offset:176\n\t" \
  "v_mfma_f32_16x16x32_bf16 v[228:231], v[76:79], v[184:187], v[228:231]\n\t" \
  "ds_read_b128 v[100:103], v9 offset:7168\n\t" \
  "v_mfma_f32_16x16x32_bf16 v[228:231], v[144:147], v[188:191], v[228:231]\n\t" \
  "ds_read_b128 v[104:107], v9 offset:6144\n\t" \
  "v_mfma_f32_16x16x32_bf16 v[224:227], v[76:79], v[188:191], v[224:227]\n\t" \
  "ds_read_b128 v[144:147], v9 offset:5120\n\t" \
  "s_waitcnt lgkmcnt(6)\n\t" \
  "v_mfma_f32_16x16x32_bf16 v[192:195], v[148:151], v[80:83], v[192:195]\n\t" \
  "v_mfma_f32_16x16x32_bf16 v[196:199], v[148:151], v[140:143], v[196:199]\n\t" \
  "v_mfma_f32_16x16x32_bf16 v[196:199], v[132:135], v[80:83], v[196:199]\n\t" \
  "ds_read_b128 v[152:155], v9 offset:4096\n\t" \
  "v_mfma_f32_16x16x32_bf16 v[200:203], v[148:151], v[124:127], v[200:203]\n\t" \
  "v_mfma_f32_16x16x32_bf16 v[200:203], v[132:135], v[140:143], v[200:203]\n\t" \
  "v_mfma_f32_16x16x32_bf16 v[200:203], v[116:119], v[80:83], v[200:203]\n\t" \
  "v_mfma_f32_16x16x32_bf16 v[204:207], v[148:151], v[92:95], v[204:207]\n\t" \
  "ds_read_b128 v[156:159], v9 offset:3072\n\t" \
  "v_cndmask_b32 v20, v195, 0, s[76:77]\n\t" \
  "v_mfma_f32_16x16x32_bf16 v[204:207], v[132:135], v[124:127], v[204:207]\n\t" \
  "ds_bpermute_b32 v20, v15, v20\n\t" \
  "v_add_f32 v225, v225, v192\n\t" \
  "v_mfma_f32_16x16x32_bf16 v[204:207], v[116:119], v[140:143], v[204:207]\n\t" \
  "v_add_f32 v226, v226, v193\n\t" \
  "v_add_f32 v227, v227, v194\n\t" \
  "v_mfma_f32_16x16x32_bf16 v[204:207], v[108:111], v[80:83], v[204:207]\n\t" \
  "v_cndmask_b32 v21, v199, v195, s[76:77]\n\t" \
  "ds_bpermute_b32 v21, v15, v21\n\t" \
  "v_mfma_f32_16x16x32_bf16 v[208:211], v[148:151], v[112:115], v[208:211]\n\t" \
  "v_add_f32 v229, v229, v196\n\t" \
  "v_add_f32 v230, v230, v197\n\t" \
  "v_mfma_f32_16x16x32_bf16 v[208:211], v[132:135], v[92:95], v[208:211]\n\t" \
  "ds_read_b128 v[164:167], v9 offset:2048\n\t" \
  "v_add_f32 v231, v231, v198\n\t" \
  "v_mfma_f32_16x16x32_bf16 v[208:211], v[116:119], v[124:127], v[208:211]\n\t" \
  "v_cndmask_b32 v22, v203, v199, s[76:77]\n\t" \
  "ds_bpermute_b32 v22, v15, v22\n\t" \
  "v_mfma_f32_16x16x32_bf16 v[208:211], v[108:111], v[140:143], v[208:211]\n\t" \
  "v_add_f32 v233, v233, v200\n\t" \
  "v_add_f32 v234, v234, v201\n\t" \
  "v_mfma_f32_16x16x32_bf16 v[208:211], v[120:123], v[80:83], v[208:211]\n\t" \
  "v_add_f32 v235, v235, v202\n\t" \
  "s_waitcnt lgkmcnt(3)\n\t" \
  "v_add_f32 v224, v224, v20\n\t" \
  "v_mfma_f32_16x16x32_bf16 v[212:215], v[148:151], v[68:71], v[212:215]\n\t" \
  "v_cndmask_b32 v23, v207, v203, s[76:77]\n\t" \
  "ds_bpermute_b32 v23, v15, v23\n\t" \
  "v_mfma_f32_16x16x32_bf16 v[212:215], v[132:135], v[112:115], v[212:215]\n\t" \
  "v_add_f32 v237, v237, v204\n\t" \
  "v_add_f32 v238, v238, v205\n\t" \
  "v_mfma_f32_16x16x32_bf16 v[212:215], v[116:119], v[92:95], v[212:215]\n\t" \
  "ds_read_b128 v[168:171], v9 offset:1024\n\t" \
  "v_add_f32 v239, v239, v206\n\t" \
  "v_mfma_f32_16x16x32_bf16 v[212:215], v[108:111], v[124:127], v[212:215]\n\t" \
  "s_waitcnt lgkmcnt(4)\n\t" \
  "v_add_f32 v228, v228, v21\n\t" \
  "v_mfma_f32_16x16x32_bf16 v[212:215], v[120:123], v[140:143], v[212:215]\n\t" \
  "s_waitcnt lgkmcnt(2)\n\t" \
  "v_add_f32 v232, v232, v22\n\t" \
  "v_mfma_f32_16x16x32_bf16 v[212:215], v[128:131], v[80:83], v[212:215]\n\t" \
  "v_cndmask_b32 v24, v211, v207, s[76:77]\n\t" \
  "ds_bpermute_b32 v24, v15, v24\n\t" \
  "v_mfma_f32_16x16x32_bf16 v[216:219], v[148:151], v[88:91], v[216:219]\n\t" \
  "v_add_f32 v241, v241, v208\n\t" \
  "v_add_f32 v242, v242, v209\n\t" \
  "v_mfma_f32_16x16x32_bf16 v[216:219], v[132:135], v[68:71], v[216:219]\n\t" \
  "v_add_f32 v243, v243, v210\n\t" \
  "v_mfma_f32_16x16x32_bf16 v[216:219], v[116:119], v[112:115], v[216:219]\n\t" \
  "s_waitcnt lgkmcnt(2)\n\t" \
  "v_add_f32 v236, v236, v23\n\t" \
  "v_mfma_f32_16x16x32_bf16 v[216:219], v[108:111], v[92:95], v[216:219]\n\t" \
  "ds_read_b128 v[172:175], v9 offset:0\n\t" \
  "ds_write_b32 v18, v17 offset:192\n\t" \
  "v_mfma_f32_16x16x32_bf16 v[216:219], v[120:123], v[124:127], v[216:219]\n\t" \
  "v_mfma_f32_16x16x32_bf16 v[216:219], v[128:131], v[140:143], v[216:219]\n\t" \
  "ds_read_b32 v16, v56 offset:96\n\t" \
  "v_cndmask_b32 v25, v215, v211, s[76:77]\n\t" \
  "v_mfma_f32_16x16x32_bf16 v[216:219], v[136:139], v[80:83], v[216:219]\n\t" \
  "ds_bpermute_b32 v25, v15, v25\n\t" \
  "v_add_f32 v245, v245, v212\n\t" \
  "v_mfma_f32_16x16x32_bf16 v[220:223], v[148:151], v[84:87], v[220:223]\n\t" \
  "v_add_f32 v246, v246, v213\n\t" \
  "v_add_f32 v247, v247, v214\n\t" \
  "v_mfma_f32_16x16x32_bf16 v[220:223], v[132:135], v[88:91], v[220:223]\n\t" \
  "s_waitcnt lgkmcnt(4)\n\t" \
  "v_add_f32 v240, v240, v24\n\t" \
  "v_mfma_f32_16x16x32_bf16 v[220:223], v[116:119], v[68:71], v[220:223]\n\t" \
  "v_mfma_f32_16x16x32_bf16 v[220:223], v[108:111], v[112:115], v[220:223]\n\t" \
  "v_mfma_f32_16x16x32_bf16 v[220:223], v[120:123], v[92:95], v[220:223]\n\t" \
  "v_mfma_f32_16x16x32_bf16 v[220:223], v[128:131], v[124:127], v[220:223]\n\t" \
  "s_waitcnt lgkmcnt(1)\n\t" \
  "v_readfirstlane_b32 vcc_lo, v16\n\t" \
  "v_mfma_f32_16x16x32_bf16 v[220:223], v[136:139], v[140:143], v[220:223]\n\t" \
  "s_cmp_eq_u32 vcc_lo, 0\n\t" \
  "s_cbranch_scc1 33f\n\t" \
  "s_lshr_b64 s[90:91], %18, s75\n\t" \
  "s_and_b32 s90, s90, 15\n\t" \
  "s_lshl_b32 s90, s90, 15\n\t" \
  "s_add_u32 s90, s90, s92\n\t" \
  "s_addc_u32 s91, s93, 0\n\t" \
  "s_add_u32 s75, s75, 4\n\t" \
  "s_swappc_b64 s[68:69], s[70:71]\n\t" \
  "33:\n\t" \
  "v_mfma_f32_16x16x32_bf16 v[220:223], v[96:99], v[80:83], v[220:223]\n\t" \
  "v_cndmask_b32 v26, v219, v215, s[76:77]\n\t" \
  "ds_bpermute_b32 v26, v15, v26\n\t" \
  "v_mfma_f32_16x16x32_bf16 v[224:227], v[148:151], v[100:103], v[224:227]\n\t" \
  "v_add_f32 v249, v249, v216\n\t" \
  "v_add_f32 v250, v250, v217\n\t" \
  "v_mfma_f32_16x16x32_bf16 v[228:231], v[148:151], v[104:107], v[228:231]\n\t" \
  "ds_read_b128 v[40:43], v56 offset:80\n\t" \
  "v_add_f32 v251, v251, v218\n\t" \
  "v_mfma_f32_16x16x32_bf16 v[228:231], v[132:135], v[100:103], v[228:231]\n\t" \
  "s_waitcnt lgkmcnt(2)\n\t" \
  "v_add_f32 v244, v244, v25\n\t" \
  "v_mfma_f32_16x16x32_bf16 v[232:235], v[148:151], v[144:147], v[232:235]\n\t" \
  "v_mfma_f32_16x16x32_bf16 v[232:235], v[132:135], v[104:107], v[232:235]\n\t" \
  "v_mfma_f32_16x16x32_bf16 v[232:235], v[116:119], v[100:103], v[232:235]\n\t" \
  "s_waitcnt lgkmcnt(1)\n\t" \
  "v_add_f32 v248, v248, v26\n\t" \
  "v_cndmask_b32 v27, v223, v219, s[76:77]\n\t" \
  "v_mfma_f32_16x16x32_bf16 v[236:239], v[148:151], v[152:155], v[236:239]\n\t" \
  "ds_bpermute_b32 v27, v15, v27\n\t" \
  "v_add_f32 v253, v253, v220\n\t" \
  "v_mfma_f32_16x16x32_bf16 v[236:239], v[132:135], v[144:147], v[236:239]\n\t" \
  "v_add_f32 v254, v254, v221\n\t" \
  "v_add_f32 v255, v255, v222\n\t" \
  "508:\n\t" \
  "s_cbranch_execz 26b\n\t" \
  "v_mfma_f32_16x16x32_bf16 v[236:239], v[116:119], v[104:107], v[236:239]\n\t" \
  "s_waitcnt lgkmcnt(1)\n\t" \
  "v_min_u32 v40, v40, v41\n\t" \
  "v_min3_u32 v40, v40, v42, v43\n\t" \
  "v_mfma_f32_16x16x32_bf16 v[236:239], v[108:111], v[100:103], v[236:239]\n\t" \
  "v_readfirstlane_b32 vcc_lo, v40\n\t" \
  "v_cvt_pk_bf16_f32 v45, v226, v227\n\t" \
  "v_mfma_f32_16x16x32_bf16 v[240:243], v[148:151], v[156:159], v[240:243]\n\t" \
  "ds_read_b32 v16, v56 offset:96\n\t" \
  "s_cmp_ge_u32 vcc_lo, 7\n\t" \
  "s_cbranch_scc0 36f\n\t" \
  "37:\n\t" \
  "v_mfma_f32_16x16x32_bf16 v[240:243], v[132:135], v[152:155], v[240:243]\n\t" \
  "v_cvt_pk_bf16_f32 v44, v224, v225\n\t" \
  "ds_write_b64 v14, v[44:45] offset:0\n\t" \
  "v_mfma_f32_16x16x32_bf16 v[240:243], v[116:119], v[144:147], v[240:243]\n\t" \
  "v_cvt_pk_bf16_f32 v47, v230, v231\n\t" \
  "v_cvt_pk_bf16_f32 v46, v228, v229\n\t" \
  "v_mfma_f32_16x16x32_bf16 v[240:243], v[108:111], v[104:107], v[240:243]\n\t" \
  "v_mov_b32 v19, 1\n\t" \
  "ds_write_b64 v14, v[46:47] offset:512\n\t" \
  "v_mfma_f32_16x16x32_bf16 v[240:243], v[120:123], v[100:103], v[240:243]\n\t" \
  "ds_write_b32 v18, v19 offset:32\n\t" \
  "v_cvt_pk_bf16_f32 v49, v234, v235\n\t" \
  "v_mfma_f32_16x16x32_bf16 v[244:247], v[148:151], v[164:167], v[244:247]\n\t" \
  "v_cvt_pk_bf16_f32 v48, v232, v233\n\t" \
  "ds_write_b64 v14, v[48:49] offset:1024\n\t" \
  "v_mfma_f32_16x16x32_bf16 v[244:247], v[132:135], v[156:159], v[244:247]\n\t" \
  "s_waitcnt lgkmcnt(5)\n\t" \
  "v_add_f32 v252, v252, v27\n\t" \
  "v_cvt_pk_bf16_f32 v51, v238, v239\n\t" \
  "v_mfma_f32_16x16x32_bf16 v[244:247], v[116:119], v[152:155], v[244:247]\n\t" \
  "s_waitcnt lgkmcnt(4)\n\t" \
  "v_readfirstlane_b32 vcc_lo, v16\n\t" \
  "v_mfma_f32_16x16x32_bf16 v[244:247], v[108:111], v[144:147], v[244:247]\n\t" \
  "s_cmp_eq_u32 vcc_lo, 0\n\t" \
  "s_cbranch_scc1 34f\n\t" \
  "s_lshr_b64 s[90:91], %18, s75\n\t" \
  "s_and_b32 s90, s90, 15\n\t" \
  "s_lshl_b32 s90, s90, 15\n\t" \
  "s_add_u32 s90, s90, s92\n\t" \
  "s_addc_u32 s91, s93, 0\n\t" \
  "s_add_u32 s75, s75, 4\n\t" \
  "s_swappc_b64 s[68:69], s[70:71]\n\t" \
  "34:\n\t" \
  "v_mfma_f32_16x16x32_bf16 v[244:247], v[120:123], v[104:107], v[244:247]\n\t" \
  "v_cvt_pk_bf16_f32 v50, v236, v237\n\t" \
  "v_mov_b32 v19, 3\n\t" \
  "v_mfma_f32_16x16x32_bf16 v[244:247], v[128:131], v[100:103], v[244:247]\n\t" \
  "ds_write_b64 v14, v[50:51] offset:1536\n\t" \
  "ds_write_b32 v18, v19 offset:32\n\t" \
  "v_mfma_f32_16x16x32_bf16 v[248:251], v[148:151], v[168:171], v[248:251]\n\t" \
  "v_cvt_pk_bf16_f32 v45, v242, v243\n\t" \
  "v_cvt_pk_bf16_f32 v44, v240, v241\n\t" \
  "v_mfma_f32_16x16x32_bf16 v[248:251], v[132:135], v[164:167], v[248:251]\n\t" \
  "ds_read_b32 v16, v56 offset:96\n\t" \
  "ds_write_b64 v14, v[44:45] offset:2048\n\t" \
  "v_mfma_f32_16x16x32_bf16 v[248:251], v[116:119], v[156:159], v[248:251]\n\t" \
  "ds_read_b64_tr_b16 v[192:193], v28 offset:22528\n\t" \
  "v_mfma_f32_16x16x32_bf16 v[248:251], v[108:111], v[152:155], v[248:251]\n\t" \
  "ds_read_b64_tr_b16 v[194:195], v28 offset:23040\n\t" \
  "v_mfma_f32_16x16x32_bf16 v[248:251], v[120:123], v[144:147], v[248:251]\n\t" \
  "ds_read_b64_tr_b16 v[196:197], v28 offset:26624\n\t" \
  "v_mfma_f32_16x16x32_bf16 v[248:251], v[128:131], v[104:107], v[248:251]\n\t" \
  "v_cvt_pk_bf16_f32 v47, v246, v247\n\t" \
  "v_cvt_pk_bf16_f32 v46, v244, v245\n\t" \
  "v_mfma_f32_16x16x32_bf16 v[248:251], v[136:139], v[100:103], v[248:251]\n\t" \
  "v_mov_b32 v19, 7\n\t" \
  "ds_write_b64 v14, v[46:47] offset:2560\n\t" \
  "v_mfma_f32_16x16x32_bf16 v[252:255], v[148:151], v[172:175], v[252:255]\n\t" \
  "ds_write_b32 v18, v19 offset:32\n\t" \
  "ds_read_b64_tr_b16 v[198:199], v28 offset:27136\n\t" \
  "v_mfma_f32_16x16x32_bf16 v[252:255], v[132:135], v[168:171], v[252:255]\n\t" \
  "s_waitcnt lgkmcnt(7)\n\t" \
  "v_readfirstlane_b32 vcc_lo, v16\n\t" \
  "v_mfma_f32_16x16x32_bf16 v[252:255], v[116:119], v[164:167], v[252:255]\n\t" \
  "s_cmp_eq_u32 vcc_lo, 0\n\t" \
  "s_cbranch_scc1 35f\n\t" \
  "s_cmp_ge_u32 s75, 8\n\t" \
  "s_cbranch_scc1 35f\n\t" \
  "s_lshr_b64 s[90:91], %18, s75\n\t" \
  "s_and_b32 s90, s90, 15\n\t" \
  "s_lshl_b32 s90, s90, 15\n\t" \
  "s_add_u32 s90, s90, s92\n\t" \
  "s_addc_u32 s91, s93, 0\n\t" \
  "s_add_u32 s75, s75, 4\n\t" \
  "s_swappc_b64 s[68:69], s[70:71]\n\t" \
  "35:\n\t" \
  "v_mfma_f32_16x16x32_bf16 v[252:255], v[108:111], v[156:159], v[252:255]\n\t" \
  "ds_read_b64_tr_b16 v[200:201], v29 offset:22528\n\t" \
  "v_mfma_f32_16x16x32_bf16 v[252:255], v[120:123], v[152:155], v[252:255]\n\t" \
  "ds_read_b64_tr_b16 v[202:203], v29 offset:23040\n\t" \
  "v_mfma_f32_16x16x32_bf16 v[252:255], v[128:131], v[144:147], v[252:255]\n\t" \
  "v_cvt_pk_bf16_f32 v49, v250, v251\n\t" \
  "v_cvt_pk_bf16_f32 v48, v248, v249\n\t" \
  "v_mfma_f32_16x16x32_bf16 v[252:255], v[136:139], v[104:107], v[252:255]\n\t" \
  "ds_write_b64 v14, v[48:49] offset:3072\n\t" \
  "ds_read_b64_tr_b16 v[204:205], v29 offset:26624\n\t" \
  "v_mfma_f32_16x16x32_bf16 v[252:255], v[96:99], v[100:103], v[252:255]\n\t" \
  "ds_read_b64_tr_b16 v[206:207], v29 offset:27136\n\t" \
  "ds_read_b32 v16, v56 offset:96\n\t" \
  "ds_read_b64_tr_b16 v[208:209], v30 offset:22528\n\t" \
  "ds_read_b64_tr_b16 v[210:211], v30 offset:23040\n\t" \
  "ds_read_b64_tr_b16 v[212:213], v30 offset:26624\n\t" \
  "ds_read_b64_tr_b16 v[214:215], v30 offset:27136\n\t" \
  "ds_read_b64_tr_b16 v[216:217], v31 offset:22528\n\t" \
  "ds_read_b64_tr_b16 v[218:219], v31 offset:23040\n\t" \
  "ds_read_b64_tr_b16 v[220:221], v31 offset:26624\n\t" \
  "ds_read_b64_tr_b16 v[222:223], v31 offset:27136\n\t" \
  "v_cvt_pk_bf16_f32 v51, v254, v255\n\t" \
  "v_cvt_pk_bf16_f32 v50, v252, v253\n\t" \
  "v_mov_b32 v19, 15\n\t" \
  "ds_write_b64 v14, v[50:51] offset:3584\n\t" \
  "ds_write_b32 v18, v19 offset:32\n\t" \
  "s_cmp_lg_u32 s75, 0\n\t" \
  "s_cbranch_scc1 44f\n\t" \
  "s_waitcnt lgkmcnt(10)\n\t" \
  "v_readfirstlane_b32 vcc_lo, v16\n\t" \
  "s_nop 1\n\t" \
  "s_cmp_ge_u32 vcc_lo, 1\n\t" \
  "s_cbranch_scc0 46f\n\t" \
  "47:\n\t" \
  "44:\n\t" \
  "s_cmp_ge_u32 s75, 8\n\t" \
  "s_cbranch_scc1 45f\n\t" \
  "s_lshr_b64 s[90:91], %18, s75\n\t" \
  "s_and_b32 s90, s90, 15\n\t" \
  "s_lshl_b32 s90, s90, 15\n\t" \
  "s_add_u32 s90, s90, s92\n\t" \
  "s_addc_u32 s91, s93, 0\n\t" \
  "s_add_u32 s75, s75, 4\n\t" \
  "s_swappc_b64 s[68:69], s[70:71]\n\t" \
  "s_branch 44b\n\t" \
  "45:\n\t" \
  "s_waitcnt lgkmcnt(15)\n\t" \
  "s_waitcnt vmcnt(6)\n\t" \
  "v_mfma_f32_16x16x32_bf16 v[224:227], v[192:195], v[64:67], 0\n\t" \
  "v_mfma_f32_16x16x32_bf16 v[224:227], v[196:199], v[68:71], v[224:227]\n\t" \
  "s_lshr_b64 s[90:91], %18, 8\n\t" \
  "s_and_b32 s90, s90, 15\n\t" \
  "s_lshl_b32 s90, s90, 15\n\t" \
  "s_add_u32 s90, s90, s92\n\t" \
  "s_addc_u32 s91, s93, 0\n\t" \
  "global_load_dwordx4 v[96:99], v53, s[90:91] offset:0 sc1\n\t" \
  "s_waitcnt vmcnt(5)\n\t" \
  "v_mfma_f32_16x16x32_bf16 v[228:231], v[196:199], v[76:79], 0\n\t" \
  "v_mfma_f32_16x16x32_bf16 v[228:231], v[192:195], v[72:75], v[228:231]\n\t" \
  "global_load_dwordx4 v[100:103], v53, s[90:91] offset:1024 sc1\n\t" \
  "s_waitcnt lgkmcnt(14)\n\t" \
  "v_mfma_f32_16x16x32_bf16 v[232:235], v[200:203], v[64:67], 0\n\t" \
  "ds_read_b64_tr_b16 v[192:193], v28 offset:30720\n\t" \
  "s_waitcnt lgkmcnt(12)\n\t" \
  "v_mfma_f32_16x16x32_bf16 v[232:235], v[204:207], v[68:71], v[232:235]\n\t" \
  "ds_read_b64_tr_b16 v[194:195], v28 offset:31232\n\t" \
  "global_load_dwordx4 v[104:107], v53, s[90:91] offset:2048 sc1\n\t" \
  "v_mfma_f32_16x16x32_bf16 v[236:239], v[204:207], v[76:79], 0\n\t" \
  "ds_read_b64_tr_b16 v[196:197], v28 offset:34816\n\t" \
  "v_mfma_f32_16x16x32_bf16 v[236:239], v[200:203], v[72:75], v[236:239]\n\t" \
  "ds_read_b64_tr_b16 v[198:199], v28 offset:35328\n\t" \
  "global_load_dwordx4 v[108:111], v53, s[90:91] offset:3072 sc1\n\t" \
  "s_waitcnt lgkmcnt(6)\n\t" \
  "v_mfma_f32_16x16x32_bf16 v[240:243], v[208:211], v[64:67], 0\n\t" \
  "ds_read_b64_tr_b16 v[200:201], v29 offset:30720\n\t" \
  "v_mfma_f32_16x16x32_bf16 v[240:243], v[212:215], v[68:71], v[240:243]\n\t" \
  "ds_read_b64_tr_b16 v[202:203], v29 offset:31232\n\t" \
  "s_lshr_b64 s[90:91], %18, 12\n\t" \
  "s_and_b32 s90, s90, 15\n\t" \
  "s_lshl_b32 s90, s90, 15\n\t" \
  "s_add_u32 s90, s90, s92\n\t" \
  "s_addc_u32 s91, s93, 0\n\t" \
  "global_load_dwordx4 v[112:115], v53, s[90:91] offset:0 sc1\n\t" \
  "v_mfma_f32_16x16x32_bf16 v[244:247], v[212:215], v[76:79], 0\n\t" \
  "ds_read_b64_tr_b16 v[204:205], v29 offset:34816\n\t" \
  "v_mfma_f32_16x16x32_bf16 v[244:247], v[208:211], v[72:75], v[244:247]\n\t" \
  "ds_read_b64_tr_b16 v[206:207], v29 offset:35328\n\t" \
  "global_load_dwordx4 v[116:119], v53, s[90:91] offset:1024 sc1\n\t" \
  "v_mfma_f32_16x16x32_bf16 v[248:251], v[216:219], v[64:67], 0\n\t" \
  "ds_read_b64_tr_b16 v[208:209], v30 offset:30720\n\t" \
  "v_mfma_f32_16x16x32_bf16 v[248:251], v[220:223], v[68:71], v[248:251]\n\t" \
  "ds_read_b64_tr_b16 v[210:211], v30 offset:31232\n\t" \
  "global_load_dwordx4 v[120:123], v53, s[90:91] offset:2048 sc1\n\t" \
  "v_mfma_f32_16x16x32_bf16 v[252:255], v[220:223], v[76:79], 0\n\t" \
  "ds_read_b64_tr_b16 v[212:213], v30 offset:34816\n\t" \
  "v_mfma_f32_16x16x32_bf16 v[252:255], v[216:219], v[72:75], v[252:255]\n\t" \
  "ds_read_b64_tr_b16 v[214:215], v30 offset:35328\n\t" \
  "global_load_dwordx4 v[124:127], v53, s[90:91] offset:3072 sc1\n\t" \
  "s_waitcnt lgkmcnt(6)\n\t" \
  "s_waitcnt vmcnt(10)\n\t" \
  "v_mfma_f32_16x16x32_bf16 v[224:227], v[192:195], v[80:83], v[224:227]\n\t" \
  "ds_read_b64_tr_b16 v[216:217], v31 offset:30720\n\t" \
  "v_mfma_f32_16x16x32_bf16 v[224:227], v[196:199], v[84:87], v[224:227]\n\t" \
  "ds_read_b64_tr_b16 v[218:219], v31 offset:31232\n\t" \
  "s_waitcnt vmcnt(8)\n\t" \
  "v_mfma_f32_16x16x32_bf16 v[228:231], v[196:199], v[92:95], v[228:231]\n\t" \
  "ds_read_b64_tr_b16 v[220:221], v31 offset:34816\n\t" \
  "s_lshr_b64 s[90:91], %18, 16\n\t" \
  "s_and_b32 s90, s90, 15\n\t" \
  "s_lshl_b32 s90, s90, 15\n\t" \
  "s_add_u32 s90, s90, s92\n\t" \
  "s_addc_u32 s91, s93, 0\n\t" \
  "global_load_dwordx4 v[128:131], v53, s[90:91] offset:0 sc1\n\t" \
  "v_mfma_f32_16x16x32_bf16 v[228:231], v[192:195], v[88:91], v[228:231]\n\t" \
  "ds_read_b64_tr_b16 v[222:223], v31 offset:35328\n\t" \
  "v_mfma_f32_16x16x32_bf16 v[232:235], v[200:203], v[80:83], v[232:235]\n\t" \
  "ds_read_b64_tr_b16 v[192:193], v28 offset:38912\n\t" \
  "s_waitcnt lgkmcnt(6)\n\t" \
  "v_mfma_f32_16x16x32_bf16 v[232:235], v[204:207], v[84:87], v[232:235]\n\t" \
  "ds_read_b64_tr_b16 v[194:195], v28 offset:39424\n\t" \
  "ds_write_b32 v18, v17 offset:160\n\t" \
  "v_mfma_f32_16x16x32_bf16 v[236:239], v[204:207], v[92:95], v[236:239]\n\t" \
  "ds_read_b64_tr_b16 v[196:197], v28 offset:43008\n\t" \
  "global_load_dwordx4 v[132:135], v53, s[90:91] offset:1024 sc1\n\t" \
  "v_mfma_f32_16x16x32_bf16 v[236:239], v[200:203], v[88:91], v[236:239]\n\t" \
  "ds_read_b64_tr_b16 v[198:199], v28 offset:43520\n\t" \
  "v_mfma_f32_16x16x32_bf16 v[240:243], v[208:211], v[80:83], v[240:243]\n\t" \
  "ds_read_b64_tr_b16 v[200:201], v29 offset:38912\n\t" \
  "s_waitcnt lgkmcnt(7)\n\t" \
  "v_mfma_f32_16x16x32_bf16 v[240:243], v[212:215], v[84:87], v[240:243]\n\t" \
  "ds_read_b64_tr_b16 v[202:203], v29 offset:39424\n\t" \
  "v_mfma_f32_16x16x32_bf16 v[244:247], v[212:215], v[92:95], v[244:247]\n\t" \
  "ds_read_b64_tr_b16 v[204:205], v29 offset:43008\n\t" \
  "global_load_dwordx4 v[136:139], v53, s[90:91] offset:2048 sc1\n\t" \
  "v_mfma_f32_16x16x32_bf16 v[244:247], v[208:211], v[88:91], v[244:247]\n\t" \
  "ds_read_b64_tr_b16 v[206:207], v29 offset:43520\n\t" \
  "v_mfma_f32_16x16x32_bf16 v[248:251], v[216:219], v[80:83], v[248:251]\n\t" \
  "ds_read_b64_tr_b16 v[208:209], v30 offset:38912\n\t" \
  "s_waitcnt lgkmcnt(6)\n\t" \
  "v_mfma_f32_16x16x32_bf16 v[248:251], v[220:223], v[84:87], v[248:251]\n\t" \
  "ds_read_b64_tr_b16 v[210:211], v30 offset:39424\n\t" \
  "v_mfma_f32_16x16x32_bf16 v[252:255], v[220:223], v[92:95], v[252:255]\n\t" \
  "ds_read_b64_tr_b16 v[212:213], v30 offset:43008\n\t" \
  "global_load_dwordx4 v[140:143], v53, s[90:91] offset:3072 sc1\n\t" \
  "v_mfma_f32_16x16x32_bf16 v[252:255], v[216:219], v[88:91], v[252:255]\n\t" \
  "ds_read_b64_tr_b16 v[214:215], v30 offset:43520\n\t" \
  "s_waitcnt vmcnt(10)\n\t" \
  "v_mfma_f32_16x16x32_bf16 v[224:227], v[192:195], v[96:99], v[224:227]\n\t" \
  "ds_read_b64_tr_b16 v[216:217], v31 offset:38912\n\t" \
  "s_waitcnt lgkmcnt(6)\n\t" \
  "v_mfma_f32_16x16x32_bf16 v[224:227], v[196:199], v[100:103], v[224:227]\n\t" \
  "ds_read_b64_tr_b16 v[218:219], v31 offset:39424\n\t" \
  "s_waitcnt vmcnt(8)\n\t" \
  "v_mfma_f32_16x16x32_bf16 v[228:231], v[196:199], v[108:111], v[228:231]\n\t" \
  "ds_read_b64_tr_b16 v[220:221], v31 offset:43008\n\t" \
  "s_lshr_b64 s[90:91], %18, 20\n\t" \
  "s_and_b32 s90, s90, 15\n\t" \
  "s_lshl_b32 s90, s90, 15\n\t" \
  "s_add_u32 s90, s90, s92\n\t" \
  "s_addc_u32 s91, s93, 0\n\t" \
  "global_load_dwordx4 v[144:147], v53, s[90:91] offset:0 sc1\n\t" \
  "v_mfma_f32_16x16x32_bf16 v[228:231], v[192:195], v[104:107], v[228:231]\n\t" \
  "ds_read_b64_tr_b16 v[222:223], v31 offset:43520\n\t" \
  "v_mfma_f32_16x16x32_bf16 v[232:235], v[200:203], v[96:99], v[232:235]\n\t" \
  "ds_read_b64_tr_b16 v[192:193], v28 offset:47104\n\t" \
  "s_waitcnt lgkmcnt(6)\n\t" \
  "v_mfma_f32_16x16x32_bf16 v[232:235], v[204:207], v[100:103], v[232:235]\n\t" \
  "ds_read_b64_tr_b16 v[194:195], v28 offset:47616\n\t" \
  "v_mfma_f32_16x16x32_bf16 v[236:239], v[204:207], v[108:111], v[236:239]\n\t" \
  "ds_read_b64_tr_b16 v[196:197], v28 offset:51200\n\t" \
  "global_load_dwordx4 v[148:151], v53, s[90:91] offset:1024 sc1\n\t" \
  "v_mfma_f32_16x16x32_bf16 v[236:239], v[200:203], v[104:107], v[236:239]\n\t" \
  "ds_read_b64_tr_b16 v[198:199], v28 offset:51712\n\t" \
  "v_mfma_f32_16x16x32_bf16 v[240:243], v[208:211], v[96:99], v[240:243]\n\t" \
  "ds_read_b64_tr_b16 v[200:201], v29 offset:47104\n\t" \
  "s_waitcnt lgkmcnt(6)\n\t" \
  "v_mfma_f32_16x16x32_bf16 v[240:243], v[212:215], v[100:103], v[240:243]\n\t" \
  "ds_read_b64_tr_b16 v[202:203], v29 offset:47616\n\t" \
  "v_mfma_f32_16x16x32_bf16 v[244:247], v[212:215], v[108:111], v[244:247]\n\t" \
  "ds_read_b64_tr_b16 v[204:205], v29 offset:51200\n\t" \
  "global_load_dwordx4 v[152:155], v53, s[90:91] offset:2048 sc1\n\t" \
  "v_mfma_f32_16x16x32_bf16 v[244:247], v[208:211], v[104:107], v[244:247]\n\t" \
  "ds_read_b64_tr_b16 v[206:207], v29 offset:51712\n\t" \
  "v_mfma_f32_16x16x32_bf16 v[248:251], v[216:219], v[96:99], v[248:251]\n\t" \
  "ds_read_b64_tr_b16 v[208:209], v30 offset:47104\n\t" \
  "s_waitcnt lgkmcnt(6)\n\t" \
  "v_mfma_f32_16x16x32_bf16 v[248:251], v[220:223], v[100:103], v[248:251]\n\t" \
  "ds_read_b64_tr_b16 v[210:211], v30 offset:47616\n\t" \
  "v_mfma_f32_16x16x32_bf16 v[252:255], v[220:223], v[108:111], v[252:255]\n\t" \
  "ds_read_b64_tr_b16 v[212:213], v30 offset:51200\n\t" \
  "global_load_dwordx4 v[156:159], v53, s[90:91] offset:3072 sc1\n\t" \
  "v_mfma_f32_16x16x32_bf16 v[252:255], v[216:219], v[104:107], v[252:255]\n\t" \
  "ds_read_b64_tr_b16 v[214:215], v30 offset:51712\n\t" \
  "s_waitcnt vmcnt(10)\n\t" \
  "v_mfma_f32_16x16x32_bf16 v[224:227], v[192:195], v[112:115], v[224:227]\n\t" \
  "ds_read_b64_tr_b16 v[216:217], v31 offset:47104\n\t" \
  "s_waitcnt lgkmcnt(6)\n\t" \
  "v_mfma_f32_16x16x32_bf16 v[224:227], v[196:199], v[116:119], v[224:227]\n\t" \
  "ds_read_b64_tr_b16 v[218:219], v31 offset:47616\n\t" \
  "s_waitcnt vmcnt(8)\n\t" \
  "v_mfma_f32_16x16x32_bf16 v[228:231], v[196:199], v[124:127], v[228:231]\n\t" \
  "ds_read_b64_tr_b16 v[220:221], v31 offset:51200\n\t" \
  "s_lshr_b64 s[90:91], %18, 24\n\t" \
  "s_and_b32 s90, s90, 15\n\t" \
  "s_lshl_b32 s90, s90, 15\n\t" \
  "s_add_u32 s90, s90, s92\n\t" \
  "s_addc_u32 s91, s93, 0\n\t" \
  "global_load_dwordx4 v[160:163], v53, s[90:91] offset:0 sc1\n\t" \
  "v_mfma_f32_16x16x32_bf16 v[228:231], v[192:195], v[120:123], v[228:231]\n\t" \
  "ds_read_b64_tr_b16 v[222:223], v31 offset:51712\n\t" \
  "v_mfma_f32_16x16x32_bf16 v[232:235], v[200:203], v[112:115], v[232:235]\n\t" \
  "ds_read_b64_tr_b16 v[192:193], v28 offset:55296\n\t" \
  "s_waitcnt lgkmcnt(6)\n\t" \
  "v_mfma_f32_16x16x32_bf16 v[232:235], v[204:207], v[116:119], v[232:235]\n\t" \
  "ds_read_b64_tr_b16 v[194:195], v28 offset:55808\n\t" \
  "v_mfma_f32_16x16x32_bf16 v[236:239], v[204:207], v[124:127], v[236:239]\n\t" \
  "ds_read_b64_tr_b16 v[196:197], v28 offset:59392\n\t" \
  "global_load_dwordx4 v[164:167], v53, s[90:91] offset:1024 sc1\n\t" \
  "ds_read_b32 v16, v56 offset:96\n\t" \
  "v_mfma_f32_16x16x32_bf16 v[236:239], v[200:203], v[120:123], v[236:239]\n\t" \
  "ds_read_b64_tr_b16 v[198:199], v28 offset:59904\n\t" \
  "v_mfma_f32_16x16x32_bf16 v[240:243], v[208:211], v[112:115], v[240:243]\n\t" \
  "ds_read_b64_tr_b16 v[200:201], v29 offset:55296\n\t" \
  "s_waitcnt lgkmcnt(7)\n\t" \
  "v_mfma_f32_16x16x32_bf16 v[240:243], v[212:215], v[116:119], v[240:243]\n\t" \
  "ds_read_b64_tr_b16 v[202:203], v29 offset:55808\n\t" \
  "v_mfma_f32_16x16x32_bf16 v[244:247], v[212:215], v[124:127], v[244:247]\n\t" \
  "ds_read_b64_tr_b16 v[204:205], v29 offset:59392\n\t" \
  "global_load_dwordx4 v[168:171], v53, s[90:91] offset:2048 sc1\n\t" \
  "v_mfma_f32_16x16x32_bf16 v[244:247], v[208:211], v[120:123], v[244:247]\n\t" \
  "ds_read_b64_tr_b16 v[206:207], v29 offset:59904\n\t" \
  "v_mfma_f32_16x16x32_bf16 v[248:251], v[216:219], v[112:115], v[248:251]\n\t" \
  "ds_read_b64_tr_b16 v[208:209], v30 offset:55296\n\t" \
  "s_waitcnt lgkmcnt(6)\n\t" \
  "v_mfma_f32_16x16x32_bf16 v[248:251], v[220:223], v[116:119], v[248:251]\n\t" \
  "ds_read_b64_tr_b16 v[210:211], v30 offset:55808\n\t" \
  "v_mfma_f32_16x16x32_bf16 v[252:255], v[220:223], v[124:127], v[252:255]\n\t" \
  "ds_read_b64_tr_b16 v[212:213], v30 offset:59392\n\t" \
  "global_load_dwordx4 v[172:175], v53, s[90:91] offset:3072 sc1\n\t" \
  "v_readfirstlane_b32 vcc_lo, v16\n\t" \
  "v_mfma_f32_16x16x32_bf16 v[252:255], v[216:219], v[120:123], v[252:255]\n\t" \
  "ds_read_b64_tr_b16 v[214:215], v30 offset:59904\n\t" \
  "s_cmp_ge_u32 vcc_lo, 2\n\t" \
  "s_cbranch_scc0 49f\n\t" \
  "50:\n\t" \
  "s_waitcnt vmcnt(10)\n\t" \
  "v_mfma_f32_16x16x32_bf16 v[224:227], v[192:195], v[128:131], v[224:227]\n\t" \
  "ds_read_b64_tr_b16 v[216:217], v31 offset:55296\n\t" \
  "s_waitcnt lgkmcnt(6)\n\t" \
  "v_mfma_f32_16x16x32_bf16 v[224:227], v[196:199], v[132:135], v[224:227]\n\t" \
  "ds_read_b64_tr_b16 v[218:219], v31 offset:55808\n\t" \
  "s_waitcnt vmcnt(8)\n\t" \
  "v_mfma_f32_16x16x32_bf16 v[228:231], v[196:199], v[140:143], v[228:231]\n\t" \
  "ds_read_b64_tr_b16 v[220:221], v31 offset:59392\n\t" \
  "s_lshr_b64 s[90:91], %18, 28\n\t" \
  "s_and_b32 s90, s90, 15\n\t" \
  "s_lshl_b32 s90, s90, 15\n\t" \
  "s_add_u32 s90, s90, s92\n\t" \
  "s_addc_u32 s91, s93, 0\n\t" \
  "global_load_dwordx4 v[176:179], v53, s[90:91] offset:0 sc1\n\t" \
  "v_mfma_f32_16x16x32_bf16 v[228:231], v[192:195], v[136:139], v[228:231]\n\t" \
  "ds_read_b64_tr_b16 v[222:223], v31 offset:59904\n\t" \
  "v_mfma_f32_16x16x32_bf16 v[232:235], v[200:203], v[128:131], v[232:235]\n\t" \
  "ds_read_b64_tr_b16 v[192:193], v28 offset:63488\n\t" \
  "s_waitcnt lgkmcnt(6)\n\t" \
  "v_mfma_f32_16x16x32_bf16 v[232:235], v[204:207], v[132:135], v[232:235]\n\t" \
  "ds_read_b64_tr_b16 v[194:195], v28 offset:64000\n\t" \
  "v_mfma_f32_16x16x32_bf16 v[236:239], v[204:207], v[140:143], v[236:239]\n\t" \
  "ds_read_b64_tr_b16 v[196:197], v32 offset:2048\n\t" \
  "global_load_dwordx4 v[180:183], v53, s[90:91] offset:1024 sc1\n\t" \
  "v_mfma_f32_16x16x32_bf16 v[236:239], v[200:203], v[136:139], v[236:239]\n\t" \
  "ds_read_b64_tr_b16 v[198:199], v32 offset:2560\n\t" \
  "v_mfma_f32_16x16x32_bf16 v[240:243], v[208:211], v[128:131], v[240:243]\n\t" \
  "ds_read_b64_tr_b16 v[200:201], v29 offset:63488\n\t" \
  "s_waitcnt lgkmcnt(6)\n\t" \
  "v_mfma_f32_16x16x32_bf16 v[240:243], v[212:215], v[132:135], v[240:243]\n\t" \
  "ds_read_b64_tr_b16 v[202:203], v29 offset:64000\n\t" \
  "v_mfma_f32_16x16x32_bf16 v[244:247], v[212:215], v[140:143], v[244:247]\n\t" \
  "ds_read_b64_tr_b16 v[204:205], v33 offset:2048\n\t" \
  "global_load_dwordx4 v[184:187], v53, s[90:91] offset:2048 sc1\n\t" \
  "v_mfma_f32_16x16x32_bf16 v[244:247], v[208:211], v[136:139], v[244:247]\n\t" \
  "ds_read_b64_tr_b16 v[206:207], v33 offset:2560\n\t" \
  "v_mfma_f32_16x16x32_bf16 v[248:251], v[216:219], v[128:131], v[248:251]\n\t" \
  "ds_read_b64_tr_b16 v[208:209], v30 offset:63488\n\t" \
  "s_waitcnt lgkmcnt(6)\n\t" \
  "v_mfma_f32_16x16x32_bf16 v[248:251], v[220:223], v[132:135], v[248:251]\n\t" \
  "ds_read_b64_tr_b16 v[210:211], v30 offset:64000\n\t" \
  "v_mfma_f32_16x16x32_bf16 v[252:255], v[220:223], v[140:143], v[252:255]\n\t" \
  "ds_read_b64_tr_b16 v[212:213], v34 offset:2048\n\t" \
  "global_load_dwordx4 v[188:191], v53, s[90:91] offset:3072 sc1\n\t" \
  "v_mfma_f32_16x16x32_bf16 v[252:255], v[216:219], v[136:139], v[252:255]\n\t" \
  "ds_read_b64_tr_b16 v[214:215], v34 offset:2560\n\t" \
  "s_waitcnt vmcnt(10)\n\t" \
  "v_mfma_f32_16x16x32_bf16 v[224:227], v[192:195], v[144:147], v[224:227]\n\t" \
  "ds_read_b64_tr_b16 v[216:217], v31 offset:63488\n\t" \
  "s_waitcnt lgkmcnt(6)\n\t" \
  "v_mfma_f32_16x16x32_bf16 v[224:227], v[196:199], v[148:151], v[224:227]\n\t" \
  "ds_read_b64_tr_b16 v[218:219], v31 offset:64000\n\t" \
  "s_waitcnt vmcnt(8)\n\t" \
  "v_mfma_f32_16x16x32_bf16 v[228:231], v[196:199], v[156:159], v[228:231]\n\t" \
  "ds_read_b64_tr_b16 v[220:221], v35 offset:2048\n\t" \
  "s_lshr_b64 s[90:91], %18, 32\n\t" \
  "s_and_b32 s90, s90, 15\n\t" \
  "s_lshl_b32 s90, s90, 15\n\t" \
  "s_add_u32 s90, s90, s92\n\t" \
  "s_addc_u32 s91, s93, 0\n\t" \
  "global_load_dwordx4 v[64:67], v53, s[90:91] offset:0 sc1\n\t" \
  "v_mfma_f32_16x16x32_bf16 v[228:231], v[192:195], v[152:155], v[228:231]\n\t" \
  "ds_read_b64_tr_b16 v[222:223], v35 offset:2560\n\t" \
  "v_mfma_f32_16x16x32_bf16 v[232:235], v[200:203], v[144:147], v[232:235]\n\t" \
  "ds_read_b64_tr_b16 v[192:193], v32 offset:6144\n\t" \
  "s_waitcnt lgkmcnt(6)\n\t" \
  "v_mfma_f32_16x16x32_bf16 v[232:235], v[204:207], v[148:151], v[232:235]\n\t" \
  "ds_read_b64_tr_b16 v[194:195], v32 offset:6656\n\t" \
  "v_mfma_f32_16x16x32_bf16 v[236:239], v[204:207], v[156:159], v[236:239]\n\t" \
  "ds_read_b64_tr_b16 v[196:197], v32 offset:10240\n\t" \
  "global_load_dwordx4 v[68:71], v53, s[90:91] offset:1024 sc1\n\t" \
  "v_mfma_f32_16x16x32_bf16 v[236:239], v[200:203], v[152:155], v[236:239]\n\t" \
  "ds_read_b64_tr_b16 v[198:199], v32 offset:10752\n\t" \
  "v_mfma_f32_16x16x32_bf16 v[240:243], v[208:211], v[144:147], v[240:243]\n\t" \
  "ds_read_b64_tr_b16 v[200:201], v33 offset:6144\n\t" \
  "s_waitcnt lgkmcnt(6)\n\t" \
  "v_mfma_f32_16x16x32_bf16 v[240:243], v[212:215], v[148:151], v[240:243]\n\t" \
  "ds_read_b128 v[40:43], v56 offset:80\n\t" \
  "ds_read_b64_tr_b16 v[202:203], v33 offset:6656\n\t" \
  "v_mfma_f32_16x16x32_bf16 v[244:247], v[212:215], v[156:159], v[244:247]\n\t" \
  "ds_read_b64_tr_b16 v[204:205], v33 offset:10240\n\t" \
  "global_load_dwordx4 v[72:75], v53, s[90:91] offset:2048 sc1\n\t" \
  "v_mfma_f32_16x16x32_bf16 v[244:247], v[208:211], v[152:155], v[244:247]\n\t" \
  "ds_read_b64_tr_b16 v[206:207], v33 offset:10752\n\t" \
  "v_mfma_f32_16x16x32_bf16 v[248:251], v[216:219], v[144:147], v[248:251]\n\t" \
  "ds_read_b64_tr_b16 v[208:209], v34 offset:6144\n\t" \
  "s_waitcnt lgkmcnt(7)\n\t" \
  "v_mfma_f32_16x16x32_bf16 v[248:251], v[220:223], v[148:151], v[248:251]\n\t" \
  "ds_read_b64_tr_b16 v[210:211], v34 offset:6656\n\t" \
  "v_mfma_f32_16x16x32_bf16 v[252:255], v[220:223], v[156:159], v[252:255]\n\t" \
  "ds_read_b64_tr_b16 v[212:213], v34 offset:10240\n\t" \
  "global_load_dwordx4 v[76:79], v53, s[90:91] offset:3072 sc1\n\t" \
  "v_mfma_f32_16x16x32_bf16 v[252:255], v[216:219], v[152:155], v[252:255]\n\t" \
  "ds_read_b64_tr_b16 v[214:215], v34 offset:10752\n\t" \
  "s_waitcnt vmcnt(10)\n\t" \
  "v_mfma_f32_16x16x32_bf16 v[224:227], v[192:195], v[160:163], v[224:227]\n\t" \
  "s_waitcnt lgkmcnt(7)\n\t" \
  "v_min_u32 v40, v40, v41\n\t" \
  "v_min3_u32 v40, v40, v42, v43\n\t" \
  "ds_read_b64_tr_b16 v[216:217], v35 offset:6144\n\t" \
  "v_mfma_f32_16x16x32_bf16 v[224:227], v[196:199], v[164:167], v[224:227]\n\t" \
  "v_readfirstlane_b32 vcc_lo, v40\n\t" \
  "ds_read_b64_tr_b16 v[218:219], v35 offset:6656\n\t" \
  "s_waitcnt vmcnt(8)\n\t" \
  "v_mfma_f32_16x16x32_bf16 v[228:231], v[196:199], v[172:175], v[228:231]\n\t" \
  "s_cmp_ge_u32 vcc_lo, 13\n\t" \
  "s_cbranch_scc0 38f\n\t" \
  "39:\n\t" \
  "ds_read_b64_tr_b16 v[220:221], v35 offset:10240\n\t" \
  "s_lshr_b64 s[90:91], %18, 36\n\t" \
  "s_and_b32 s90, s90, 15\n\t" \
  "s_lshl_b32 s90, s90, 15\n\t" \
  "s_add_u32 s90, s90, s92\n\t" \
  "s_addc_u32 s91, s93, 0\n\t" \
  "global_load_dwordx4 v[80:83], v53, s[90:91] offset:0 sc1\n\t" \
  "v_mfma_f32_16x16x32_bf16 v[228:231], v[192:195], v[168:171], v[228:231]\n\t" \
  "ds_read_b64_tr_b16 v[222:223], v35 offset:10752\n\t" \
  "s_waitcnt lgkmcnt(6)\n\t" \
  "v_mfma_f32_16x16x32_bf16 v[232:235], v[200:203], v[160:163], v[232:235]\n\t" \
  "ds_read_b64_tr_b16 v[192:193], v32 offset:14336\n\t" \
  "v_mfma_f32_16x16x32_bf16 v[232:235], v[204:207], v[164:167], v[232:235]\n\t" \
  "ds_read_b64_tr_b16 v[194:195], v32 offset:14848\n\t" \
  "v_mfma_f32_16x16x32_bf16 v[236:239], v[204:207], v[172:175], v[236:239]\n\t" \
  "ds_read_b64_tr_b16 v[196:197], v32 offset:18432\n\t" \
  "global_load_dwordx4 v[84:87], v53, s[90:91] offset:1024 sc1\n\t" \
  "v_mfma_f32_16x16x32_bf16 v[236:239], v[200:203], v[168:171], v[236:239]\n\t" \
  "ds_read_b64_tr_b16 v[198:199], v32 offset:18944\n\t" \
  "v_mfma_f32_16x16x32_bf16 v[240:243], v[208:211], v[160:163], v[240:243]\n\t" \
  "ds_read_b64_tr_b16 v[200:201], v33 offset:14336\n\t" \
  "s_waitcnt lgkmcnt(6)\n\t" \
  "v_mfma_f32_16x16x32_bf16 v[240:243], v[212:215], v[164:167], v[240:243]\n\t" \
  "ds_read_b64_tr_b16 v[202:203], v33 offset:14848\n\t" \
  "v_mfma_f32_16x16x32_bf16 v[244:247], v[212:215], v[172:175], v[244:247]\n\t" \
  "ds_read_b64_tr_b16 v[204:205], v33 offset:18432\n\t" \
  "global_load_dwordx4 v[88:91], v53, s[90:91] offset:2048 sc1\n\t" \
  "v_mfma_f32_16x16x32_bf16 v[244:247], v[208:211], v[168:171], v[244:247]\n\t" \
  "ds_read_b64_tr_b16 v[206:207], v33 offset:18944\n\t" \
  "v_mfma_f32_16x16x32_bf16 v[248:251], v[216:219], v[160:163], v[248:251]\n\t" \
  "ds_read_b64_tr_b16 v[208:209], v34 offset:14336\n\t" \
  "s_waitcnt lgkmcnt(6)\n\t" \
  "v_mfma_f32_16x16x32_bf16 v[248:251], v[220:223], v[164:167], v[248:251]\n\t" \
  "ds_read_b64_tr_b16 v[210:211], v34 offset:14848\n\t" \
  "v_mfma_f32_16x16x32_bf16 v[252:255], v[220:223], v[172:175], v[252:255]\n\t" \
  "ds_read_b64_tr_b16 v[212:213], v34 offset:18432\n\t" \
  "global_load_dwordx4 v[92:95], v53, s[90:91] offset:3072 sc1\n\t" \
  "v_mfma_f32_16x16x32_bf16 v[252:255], v[216:219], v[168:171], v[252:255]\n\t" \
  "ds_read_b64_tr_b16 v[214:215], v34 offset:18944\n\t" \
  "s_waitcnt vmcnt(10)\n\t" \
  "v_mfma_f32_16x16x32_bf16 v[224:227], v[192:195], v[176:179], v[224:227]\n\t" \
  "ds_read_b64_tr_b16 v[216:217], v35 offset:14336\n\t" \
  "s_waitcnt lgkmcnt(6)\n\t" \
  "v_mfma_f32_16x16x32_bf16 v[224:227], v[196:199], v[180:183], v[224:227]\n\t" \
  "ds_read_b64_tr_b16 v[218:219], v35 offset:14848\n\t" \
  "s_waitcnt vmcnt(8)\n\t" \
  "v_mfma_f32_16x16x32_bf16 v[228:231], v[196:199], v[188:191], v[228:231]\n\t" \
  "ds_read_b64_tr_b16 v[220:221], v35 offset:18432\n\t" \
  "s_lshr_b64 s[90:91], %18, 40\n\t" \
  "s_and_b32 s90, s90, 15\n\t" \
  "s_lshl_b32 s90, s90, 15\n\t" \
  "s_add_u32 s90, s90, s92\n\t" \
  "s_addc_u32 s91, s93, 0\n\t" \
  "global_load_dwordx4 v[96:99], v53, s[90:91] offset:0 sc1\n\t" \
  "v_mfma_f32_16x16x32_bf16 v[228:231], v[192:195], v[184:187], v[228:231]\n\t" \
  "ds_read_b64_tr_b16 v[222:223], v35 offset:18944\n\t" \
  "v_mfma_f32_16x16x32_bf16 v[232:235], v[200:203], v[176:179], v[232:235]\n\t" \
  "ds_read_b64_tr_b16 v[192:193], v32 offset:22528\n\t" \
  "s_waitcnt lgkmcnt(6)\n\t" \
  "v_mfma_f32_16x16x32_bf16 v[232:235], v[204:207], v[180:183], v[232:235]\n\t" \
  "ds_read_b64_tr_b16 v[194:195], v32 offset:23040\n\t" \
  "v_mfma_f32_16x16x32_bf16 v[236:239], v[204:207], v[188:191], v[236:239]\n\t" \
  "ds_read_b64_tr_b16 v[196:197], v32 offset:26624\n\t" \
  "global_load_dwordx4 v[100:103], v53, s[90:91] offset:1024 sc1\n\t" \
  "v_mfma_f32_16x16x32_bf16 v[236:239], v[200:203], v[184:187], v[236:239]\n\t" \
  "ds_read_b64_tr_b16 v[198:199], v32 offset:27136\n\t" \
  "v_mfma_f32_16x16x32_bf16 v[240:243], v[208:211], v[176:179], v[240:243]\n\t" \
  "ds_read_b64_tr_b16 v[200:201], v33 offset:22528\n\t" \
  "s_waitcnt lgkmcnt(6)\n\t" \
  "v_mfma_f32_16x16x32_bf16 v[240:243], v[212:215], v[180:183], v[240:243]\n\t" \
  "ds_read_b64_tr_b16 v[202:203], v33 offset:23040\n\t" \
  "v_mfma_f32_16x16x32_bf16 v[244:247], v[212:215], v[188:191], v[244:247]\n\t" \
  "ds_read_b64_tr_b16 v[204:205], v33 offset:26624\n\t" \
  "global_load_dwordx4 v[104:107], v53, s[90:91] offset:2048 sc1\n\t" \
  "v_mfma_f32_16x16x32_bf16 v[244:247], v[208:211], v[184:187], v[244:247]\n\t" \
  "ds_read_b64_tr_b16 v[206:207], v33 offset:27136\n\t" \
  "v_mfma_f32_16x16x32_bf16 v[248:251], v[216:219], v[176:179], v[248:251]\n\t" \
  "ds_read_b64_tr_b16 v[208:209], v34 offset:22528\n\t" \
  "s_waitcnt lgkmcnt(6)\n\t" \
  "v_mfma_f32_16x16x32_bf16 v[248:251], v[220:223], v[180:183], v[248:251]\n\t" \
  "ds_read_b64_tr_b16 v[210:211], v34 offset:23040\n\t" \
  "v_mfma_f32_16x16x32_bf16 v[252:255], v[220:223], v[188:191], v[252:255]\n\t" \
  "ds_read_b64_tr_b16 v[212:213], v34 offset:26624\n\t" \
  "global_load_dwordx4 v[108:111], v53, s[90:91] offset:3072 sc1\n\t" \
  "ds_write_b32 v56, v56 offset:108\n\t" \
  "v_mfma_f32_16x16x32_bf16 v[252:255], v[216:219], v[184:187], v[252:255]\n\t" \
  "ds_read_b64_tr_b16 v[214:215], v34 offset:27136\n\t" \
  "s_waitcnt vmcnt(10)\n\t" \
  "v_mfma_f32_16x16x32_bf16 v[224:227], v[192:195], v[64:67], v[224:227]\n\t" \
  "ds_read_b64_tr_b16 v[216:217], v35 offset:22528\n\t" \
  "s_waitcnt lgkmcnt(7)\n\t" \
  "v_mfma_f32_16x16x32_bf16 v[224:227], v[196:199], v[68:71], v[224:227]\n\t" \
  "ds_read_b64_tr_b16 v[218:219], v35 offset:23040\n\t" \
  "s_waitcnt vmcnt(8)\n\t" \
  "v_mfma_f32_16x16x32_bf16 v[228:231], v[196:199], v[76:79], v[228:231]\n\t" \
  "ds_read_b64_tr_b16 v[220:221], v35 offset:26624\n\t" \
  "s_lshr_b64 s[90:91], %18, 44\n\t" \
  "s_and_b32 s90, s90, 15\n\t" \
  "s_lshl_b32 s90, s90, 15\n\t" \
  "s_add_u32 s90, s90, s92\n\t" \
  "s_addc_u32 s91, s93, 0\n\t" \
  "global_load_dwordx4 v[112:115], v53, s[90:91] offset:0 sc1\n\t" \
  "v_mfma_f32_16x16x32_bf16 v[228:231], v[192:195], v[72:75], v[228:231]\n\t" \
  "ds_read_b64_tr_b16 v[222:223], v35 offset:27136\n\t" \
  "v_mfma_f32_16x16x32_bf16 v[232:235], v[200:203], v[64:67], v[232:235]\n\t" \
  "ds_read_b64_tr_b16 v[192:193], v32 offset:30720\n\t" \
  "s_waitcnt lgkmcnt(6)\n\t" \
  "v_mfma_f32_16x16x32_bf16 v[232:235], v[204:207], v[68:71], v[232:235]\n\t" \
  "ds_read_b64_tr_b16 v[194:195], v32 offset:31232\n\t" \
  "v_mfma_f32_16x16x32_bf16 v[236:239], v[204:207], v[76:79], v[236:239]\n\t" \
  "ds_read_b64_tr_b16 v[196:197], v32 offset:34816\n\t" \
  "global_load_dwordx4 v[116:119], v53, s[90:91] offset:1024 sc1\n\t" \
  "v_mfma_f32_16x16x32_bf16 v[236:239], v[200:203], v[72:75], v[236:239]\n\t" \
  "ds_read_b64_tr_b16 v[198:199], v32 offset:35328\n\t" \
  "v_mfma_f32_16x16x32_bf16 v[240:243], v[208:211], v[64:67], v[240:243]\n\t" \
  "ds_read_b64_tr_b16 v[200:201], v33 offset:30720\n\t" \
  "s_waitcnt lgkmcnt(6)\n\t" \
  "v_mfma_f32_16x16x32_bf16 v[240:243], v[212:215], v[68:71], v[240:243]\n\t" \
  "ds_read_b64_tr_b16 v[202:203], v33 offset:31232\n\t" \
  "v_mfma_f32_16x16x32_bf16 v[244:247], v[212:215], v[76:79], v[244:247]\n\t" \
  "ds_read_b64_tr_b16 v[204:205], v33 offset:34816\n\t" \
  "global_load_dwordx4 v[120:123], v53, s[90:91] offset:2048 sc1\n\t" \
  "v_mfma_f32_16x16x32_bf16 v[244:247], v[208:211], v[72:75], v[244:247]\n\t" \
  "ds_read_b64_tr_b16 v[206:207], v33 offset:35328\n\t" \
  "v_mfma_f32_16x16x32_bf16 v[248:251], v[216:219], v[64:67], v[248:251]\n\t" \
  "ds_read_b64_tr_b16 v[208:209], v34 offset:30720\n\t" \
  "s_waitcnt lgkmcnt(6)\n\t" \
  "v_mfma_f32_16x16x32_bf16 v[248:251], v[220:223], v[68:71], v[248:251]\n\t" \
  "ds_read_b64_tr_b16 v[210:211], v34 offset:31232\n\t" \
  "v_mfma_f32_16x16x32_bf16 v[252:255], v[220:223], v[76:79], v[252:255]\n\t" \
  "ds_read_b64_tr_b16 v[212:213], v34 offset:34816\n\t" \
  "global_load_dwordx4 v[124:127], v53, s[90:91] offset:3072 sc1\n\t" \
  "v_mfma_f32_16x16x32_bf16 v[252:255], v[216:219], v[72:75], v[252:255]\n\t" \
  "ds_read_b64_tr_b16 v[214:215], v34 offset:35328\n\t" \
  "s_waitcnt vmcnt(10)\n\t" \
  "v_mfma_f32_16x16x32_bf16 v[224:227], v[192:195], v[80:83], v[224:227]\n\t" \
  "ds_read_b64_tr_b16 v[216:217], v35 offset:30720\n\t" \
  "s_waitcnt lgkmcnt(6)\n\t" \
  "v_mfma_f32_16x16x32_bf16 v[224:227], v[196:199], v[84:87], v[224:227]\n\t" \
  "ds_read_b64_tr_b16 v[218:219], v35 offset:31232\n\t" \
  "s_waitcnt vmcnt(8)\n\t" \
  "v_mfma_f32_16x16x32_bf16 v[228:231], v[196:199], v[92:95], v[228:231]\n\t" \
  "ds_read_b64_tr_b16 v[220:221], v35 offset:34816\n\t" \
  "s_lshr_b64 s[90:91], %18, 48\n\t" \
  "s_and_b32 s90, s90, 15\n\t" \
  "s_lshl_b32 s90, s90, 15\n\t" \
  "s_add_u32 s90, s90, s92\n\t" \
  "s_addc_u32 s91, s93, 0\n\t" \
  "global_load_dwordx4 v[128:131], v53, s[90:91] offset:0 sc1\n\t" \
  "v_mfma_f32_16x16x32_bf16 v[228:231], v[192:195], v[88:91], v[228:231]\n\t" \
  "ds_read_b64_tr_b16 v[222:223], v35 offset:35328\n\t" \
  "v_mfma_f32_16x16x32_bf16 v[232:235], v[200:203], v[80:83], v[232:235]\n\t" \
  "ds_read_b64_tr_b16 v[192:193], v32 offset:38912\n\t" \
  "s_waitcnt lgkmcnt(6)\n\t" \
  "v_mfma_f32_16x16x32_bf16 v[232:235], v[204:207], v[84:87], v[232:235]\n\t" \
  "ds_read_b64_tr_b16 v[194:195], v32 offset:39424\n\t" \
  "v_mfma_f32_16x16x32_bf16 v[236:239], v[204:207], v[92:95], v[236:239]\n\t" \
  "ds_read_b64_tr_b16 v[196:197], v32 offset:43008\n\t" \
  "global_load_dwordx4 v[132:135], v53, s[90:91] offset:1024 sc1\n\t" \
  "v_mfma_f32_16x16x32_bf16 v[236:239], v[200:203], v[88:91], v[236:239]\n\t" \
  "ds_read_b64_tr_b16 v[198:199], v32 offset:43520\n\t" \
  "v_mfma_f32_16x16x32_bf16 v[240:243], v[208:211], v[80:83], v[240:243]\n\t" \
  "ds_read_b64_tr_b16 v[200:201], v33 offset:38912\n\t" \
  "s_waitcnt lgkmcnt(6)\n\t" \
  "509:\n\t" \
  "s_cbranch_execz 26b\n\t" \
  "v_mfma_f32_16x16x32_bf16 v[240:243], v[212:215], v[84:87], v[240:243]\n\t" \
  "ds_read_b64_tr_b16 v[202:203], v33 offset:39424\n\t" \
  "v_mfma_f32_16x16x32_bf16 v[244:247], v[212:215], v[92:95], v[244:247]\n\t" \
  "ds_read_b64_tr_b16 v[204:205], v33 offset:43008\n\t" \
  "global_load_dwordx4 v[136:139], v53, s[90:91] offset:2048 sc1\n\t" \
  "v_mfma_f32_16x16x32_bf16 v[244:247], v[208:211], v[88:91], v[244:247]\n\t" \
  "ds_read_b64_tr_b16 v[206:207], v33 offset:43520\n\t" \
  "v_mfma_f32_16x16x32_bf16 v[248:251], v[216:219], v[80:83], v[248:251]\n\t" \
  "ds_read_b64_tr_b16 v[208:209], v34 offset:38912\n\t" \
  "s_waitcnt lgkmcnt(6)\n\t" \
  "v_mfma_f32_16x16x32_bf16 v[248:251], v[220:223], v[84:87], v[248:251]\n\t" \
  "ds_read_b64_tr_b16 v[210:211], v34 offset:39424\n\t" \
  "v_mfma_f32_16x16x32_bf16 v[252:255], v[220:223], v[92:95], v[252:255]\n\t" \
  "ds_read_b64_tr_b16 v[212:213], v34 offset:43008\n\t" \
  "global_load_dwordx4 v[140:143], v53, s[90:91] offset:3072 sc1\n\t" \
  "v_mfma_f32_16x16x32_bf16 v[252:255], v[216:219], v[88:91], v[252:255]\n\t" \
  "ds_read_b64_tr_b16 v[214:215], v34 offset:43520\n\t" \
  "s_waitcnt vmcnt(10)\n\t" \
  "v_mfma_f32_16x16x32_bf16 v[224:227], v[192:195], v[96:99], v[224:227]\n\t" \
  "ds_read_b64_tr_b16 v[216:217], v35 offset:38912\n\t" \
  "s_waitcnt lgkmcnt(6)\n\t" \
  "v_mfma_f32_16x16x32_bf16 v[224:227], v[196:199], v[100:103], v[224:227]\n\t" \
  "ds_read_b64_tr_b16 v[218:219], v35 offset:39424\n\t" \
  "s_waitcnt vmcnt(8)\n\t" \
  "v_mfma_f32_16x16x32_bf16 v[228:231], v[196:199], v[108:111], v[228:231]\n\t" \
  "ds_read_b64_tr_b16 v[220:221], v35 offset:43008\n\t" \
  "s_lshr_b64 s[90:91], %18, 52\n\t" \
  "s_and_b32 s90, s90, 15\n\t" \
  "s_lshl_b32 s90, s90, 15\n\t" \
  "s_add_u32 s90, s90, s92\n\t" \
  "s_addc_u32 s91, s93, 0\n\t" \
  "global_load_dwordx4 v[144:147], v53, s[90:91] offset:0 sc1\n\t" \
  "v_mfma_f32_16x16x32_bf16 v[228:231], v[192:195], v[104:107], v[228:231]\n\t" \
  "ds_read_b64_tr_b16 v[222:223], v35 offset:43520\n\t" \
  "v_mfma_f32_16x16x32_bf16 v[232:235], v[200:203], v[96:99], v[232:235]\n\t" \
  "ds_read_b64_tr_b16 v[192:193], v32 offset:47104\n\t" \
  "s_waitcnt lgkmcnt(6)\n\t" \
  "v_mfma_f32_16x16x32_bf16 v[232:235], v[204:207], v[100:103], v[232:235]\n\t" \
  "ds_read_b64_tr_b16 v[194:195], v32 offset:47616\n\t" \
  "v_mfma_f32_16x16x32_bf16 v[236:239], v[204:207], v[108:111], v[236:239]\n\t" \
  "ds_read_b64_tr_b16 v[196:197], v32 offset:51200\n\t" \
  "global_load_dwordx4 v[148:151], v53, s[90:91] offset:1024 sc1\n\t" \
  "v_mfma_f32_16x16x32_bf16 v[236:239], v[200:203], v[104:107], v[236:239]\n\t" \
  "ds_read_b64_tr_b16 v[198:199], v32 offset:51712\n\t" \
  "v_mfma_f32_16x16x32_bf16 v[240:243], v[208:211], v[96:99], v[240:243]\n\t" \
  "ds_read_b64_tr_b16 v[200:201], v33 offset:47104\n\t" \
  "s_waitcnt lgkmcnt(6)\n\t" \
  "v_mfma_f32_16x16x32_bf16 v[240:243], v[212:215], v[100:103], v[240:243]\n\t" \
  "ds_read_b64_tr_b16 v[202:203], v33 offset:47616\n\t" \
  "v_mfma_f32_16x16x32_bf16 v[244:247], v[212:215], v[108:111], v[244:247]\n\t" \
  "ds_read_b64_tr_b16 v[204:205], v33 offset:51200\n\t" \
  "global_load_dwordx4 v[152:155], v53, s[90:91] offset:2048 sc1\n\t" \
  "v_mfma_f32_16x16x32_bf16 v[244:247], v[208:211], v[104:107], v[244:247]\n\t" \
  "ds_read_b64_tr_b16 v[206:207], v33 offset:51712\n\t" \
  "v_mfma_f32_16x16x32_bf16 v[248:251], v[216:219], v[96:99], v[248:251]\n\t" \
  "ds_read_b64_tr_b16 v[208:209], v34 offset:47104\n\t" \
  "s_waitcnt lgkmcnt(6)\n\t" \
  "v_mfma_f32_16x16x32_bf16 v[248:251], v[220:223], v[100:103], v[248:251]\n\t" \
  "ds_read_b64_tr_b16 v[210:211], v34 offset:47616\n\t" \
  "v_mfma_f32_16x16x32_bf16 v[252:255], v[220:223], v[108:111], v[252:255]\n\t" \
  "ds_read_b64_tr_b16 v[212:213], v34 offset:51200\n\t" \
  "global_load_dwordx4 v[156:159], v53, s[90:91] offset:3072 sc1\n\t" \
  "v_mfma_f32_16x16x32_bf16 v[252:255], v[216:219], v[104:107], v[252:255]\n\t" \
  "ds_read_b64_tr_b16 v[214:215], v34 offset:51712\n\t" \
  "s_waitcnt vmcnt(10)\n\t" \
  "v_mfma_f32_16x16x32_bf16 v[224:227], v[192:195], v[112:115], v[224:227]\n\t" \
  "ds_read_b64_tr_b16 v[216:217], v35 offset:47104\n\t" \
  "s_waitcnt lgkmcnt(6)\n\t" \
  "v_mfma_f32_16x16x32_bf16 v[224:227], v[196:199], v[116:119], v[224:227]\n\t" \
  "ds_read_b64_tr_b16 v[218:219], v35 offset:47616\n\t" \
  "s_waitcnt vmcnt(8)\n\t" \
  "v_mfma_f32_16x16x32_bf16 v[228:231], v[196:199], v[124:127], v[228:231]\n\t" \
  "ds_read_b64_tr_b16 v[220:221], v35 offset:51200\n\t" \
  "s_lshr_b64 s[90:91], %18, 56\n\t" \
  "s_and_b32 s90, s90, 15\n\t" \
  "s_lshl_b32 s90, s90, 15\n\t" \
  "s_add_u32 s90, s90, s92\n\t" \
  "s_addc_u32 s91, s93, 0\n\t" \
  "global_load_dwordx4 v[160:163], v53, s[90:91] offset:0 sc1\n\t" \
  "v_mfma_f32_16x16x32_bf16 v[228:231], v[192:195], v[120:123], v[228:231]\n\t" \
  "ds_read_b64_tr_b16 v[222:223], v35 offset:51712\n\t" \
  "v_mfma_f32_16x16x32_bf16 v[232:235], v[200:203], v[112:115], v[232:235]\n\t" \
  "ds_read_b64_tr_b16 v[192:193], v32 offset:55296\n\t" \
  "s_waitcnt lgkmcnt(6)\n\t" \
  "v_mfma_f32_16x16x32_bf16 v[232:235], v[204:207], v[116:119], v[232:235]\n\t" \
  "ds_read_b64_tr_b16 v[194:195], v32 offset:55808\n\t" \
  "v_mfma_f32_16x16x32_bf16 v[236:239], v[204:207], v[124:127], v[236:239]\n\t" \
  "ds_read_b64_tr_b16 v[196:197], v32 offset:59392\n\t" \
  "global_load_dwordx4 v[164:167], v53, s[90:91] offset:1024 sc1\n\t" \
  "v_mfma_f32_16x16x32_bf16 v[236:239], v[200:203], v[120:123], v[236:239]\n\t" \
  "ds_read_b64_tr_b16 v[198:199], v32 offset:59904\n\t" \
  "v_mfma_f32_16x16x32_bf16 v[240:243], v[208:211], v[112:115], v[240:243]\n\t" \
  "ds_read_b64_tr_b16 v[200:201], v33 offset:55296\n\t" \
  "s_waitcnt lgkmcnt(6)\n\t" \
  "v_mfma_f32_16x16x32_bf16 v[240:243], v[212:215], v[116:119], v[240:243]\n\t" \
  "ds_read_b128 v[40:43], v56 offset:80\n\t" \
  "ds_read_b64_tr_b16 v[202:203], v33 offset:55808\n\t" \
  "v_mfma_f32_16x16x32_bf16 v[244:247], v[212:215], v[124:127], v[244:247]\n\t" \
  "ds_read_b64_tr_b16 v[204:205], v33 offset:59392\n\t" \
  "global_load_dwordx4 v[168:171], v53, s[90:91] offset:2048 sc1\n\t" \
  "v_mfma_f32_16x16x32_bf16 v[244:247], v[208:211], v[120:123], v[244:247]\n\t" \
  "ds_read_b64_tr_b16 v[206:207], v33 offset:59904\n\t" \
  "v_mfma_f32_16x16x32_bf16 v[248:251], v[216:219], v[112:115], v[248:251]\n\t" \
  "ds_read_b64_tr_b16 v[208:209], v34 offset:55296\n\t" \
  "s_waitcnt lgkmcnt(7)\n\t" \
  "v_mfma_f32_16x16x32_bf16 v[248:251], v[220:223], v[116:119], v[248:251]\n\t" \
  "ds_read_b64_tr_b16 v[210:211], v34 offset:55808\n\t" \
  "v_mfma_f32_16x16x32_bf16 v[252:255], v[220:223], v[124:127], v[252:255]\n\t" \
  "ds_read_b64_tr_b16 v[212:213], v34 offset:59392\n\t" \
  "global_load_dwordx4 v[172:175], v53, s[90:91] offset:3072 sc1\n\t" \
  "v_mfma_f32_16x16x32_bf16 v[252:255], v[216:219], v[120:123], v[252:255]\n\t" \
  "ds_read_b64_tr_b16 v[214:215], v34 offset:59904\n\t" \
  "s_waitcnt vmcnt(10)\n\t" \
  "v_mfma_f32_16x16x32_bf16 v[224:227], v[192:195], v[128:131], v[224:227]\n\t" \
  "s_waitcnt lgkmcnt(7)\n\t" \
  "v_min_u32 v40, v40, v41\n\t" \
  "v_min3_u32 v40, v40, v42, v43\n\t" \
  "ds_read_b64_tr_b16 v[216:217], v35 offset:55296\n\t" \
  "v_mfma_f32_16x16x32_bf16 v[224:227], v[196:199], v[132:135], v[224:227]\n\t" \
  "v_readfirstlane_b32 vcc_lo, v40\n\t" \
  "ds_read_b64_tr_b16 v[218:219], v35 offset:55808\n\t" \
  "s_waitcnt vmcnt(8)\n\t" \
  "v_mfma_f32_16x16x32_bf16 v[228:231], v[196:199], v[140:143], v[228:231]\n\t" \
  "s_cmp_ge_u32 vcc_lo, 16\n\t" \
  "s_cbranch_scc0 40f\n\t" \
  "41:\n\t" \
  "ds_read_b64_tr_b16 v[220:221], v35 offset:59392\n\t" \
  "s_lshr_b64 s[90:91], %18, 60\n\t" \
  "s_and_b32 s90, s90, 15\n\t" \
  "s_lshl_b32 s90, s90, 15\n\t" \
  "s_add_u32 s90, s90, s92\n\t" \
  "s_addc_u32 s91, s93, 0\n\t" \
  "global_load_dwordx4 v[176:179], v53, s[90:91] offset:0 sc1\n\t" \
  "v_mfma_f32_16x16x32_bf16 v[228:231], v[192:195], v[136:139], v[228:231]\n\t" \
  "ds_read_b64_tr_b16 v[222:223], v35 offset:59904\n\t" \
  "s_waitcnt lgkmcnt(6)\n\t" \
  "v_mfma_f32_16x16x32_bf16 v[232:235], v[200:203], v[128:131], v[232:235]\n\t" \
  "ds_read_b64_tr_b16 v[192:193], v36 offset:2304\n\t" \
  "v_mfma_f32_16x16x32_bf16 v[232:235], v[204:207], v[132:135], v[232:235]\n\t" \
  "ds_read_b64_tr_b16 v[194:195], v36 offset:2816\n\t" \
  "v_mfma_f32_16x16x32_bf16 v[236:239], v[204:207], v[140:143], v[236:239]\n\t" \
  "ds_read_b64_tr_b16 v[196:197], v36 offset:10752\n\t" \
  "global_load_dwordx4 v[180:183], v53, s[90:91] offset:1024 sc1\n\t" \
  "v_mfma_f32_16x16x32_bf16 v[236:239], v[200:203], v[136:139], v[236:239]\n\t" \
  "ds_read_b64_tr_b16 v[198:199], v36 offset:11264\n\t" \
  "v_mfma_f32_16x16x32_bf16 v[240:243], v[208:211], v[128:131], v[240:243]\n\t" \
  "ds_read_b64_tr_b16 v[200:201], v37 offset:2304\n\t" \
  "s_waitcnt lgkmcnt(6)\n\t" \
  "v_mfma_f32_16x16x32_bf16 v[240:243], v[212:215], v[132:135], v[240:243]\n\t" \
  "ds_read_b64_tr_b16 v[202:203], v37 offset:2816\n\t" \
  "v_mfma_f32_16x16x32_bf16 v[244:247], v[212:215], v[140:143], v[244:247]\n\t" \
  "ds_read_b64_tr_b16 v[204:205], v37 offset:10752\n\t" \
  "global_load_dwordx4 v[184:187], v53, s[90:91] offset:2048 sc1\n\t" \
  "v_mfma_f32_16x16x32_bf16 v[244:247], v[208:211], v[136:139], v[244:247]\n\t" \
  "ds_read_b64_tr_b16 v[206:207], v37 offset:11264\n\t" \
  "v_mfma_f32_16x16x32_bf16 v[248:251], v[216:219], v[128:131], v[248:251]\n\t" \
  "ds_read_b64_tr_b16 v[208:209], v38 offset:2304\n\t" \
  "s_waitcnt lgkmcnt(6)\n\t" \
  "v_mfma_f32_16x16x32_bf16 v[248:251], v[220:223], v[132:135], v[248:251]\n\t" \
  "ds_read_b64_tr_b16 v[210:211], v38 offset:2816\n\t" \
  "v_mfma_f32_16x16x32_bf16 v[252:255], v[220:223], v[140:143], v[252:255]\n\t" \
  "ds_read_b64_tr_b16 v[212:213], v38 offset:10752\n\t" \
  "global_load_dwordx4 v[188:191], v53, s[90:91] offset:3072 sc1\n\t" \
  "v_mfma_f32_16x16x32_bf16 v[252:255], v[216:219], v[136:139], v[252:255]\n\t" \
  "ds_read_b64_tr_b16 v[214:215], v38 offset:11264\n\t" \
  "s_waitcnt vmcnt(10)\n\t" \
  "v_mfma_f32_16x16x32_bf16 v[224:227], v[192:195], v[144:147], v[224:227]\n\t" \
  "ds_read_b64_tr_b16 v[216:217], v39 offset:2304\n\t" \
  "s_waitcnt lgkmcnt(6)\n\t" \
  "v_mfma_f32_16x16x32_bf16 v[224:227], v[196:199], v[148:151], v[224:227]\n\t" \
  "ds_read_b64_tr_b16 v[218:219], v39 offset:2816\n\t" \
  "s_waitcnt vmcnt(8)\n\t" \
  "v_mfma_f32_16x16x32_bf16 v[228:231], v[196:199], v[156:159], v[228:231]\n\t" \
  "ds_read_b64_tr_b16 v[220:221], v39 offset:10752\n\t" \
  "v_mfma_f32_16x16x32_bf16 v[228:231], v[192:195], v[152:155], v[228:231]\n\t" \
  "ds_read_b64_tr_b16 v[222:223], v39 offset:11264\n\t" \
  "v_mfma_f32_16x16x32_bf16 v[232:235], v[200:203], v[144:147], v[232:235]\n\t" \
  "ds_read_b64_tr_b16 v[192:193], v36 offset:19200\n\t" \
  "s_waitcnt lgkmcnt(6)\n\t" \
  "v_mfma_f32_16x16x32_bf16 v[232:235], v[204:207], v[148:151], v[232:235]\n\t" \
  "ds_read_b64_tr_b16 v[194:195], v36 offset:19712\n\t" \
  "v_mfma_f32_16x16x32_bf16 v[236:239], v[204:207], v[156:159], v[236:239]\n\t" \
  "ds_read_b64_tr_b16 v[196:197], v36 offset:27648\n\t" \
  "v_mfma_f32_16x16x32_bf16 v[236:239], v[200:203], v[152:155], v[236:239]\n\t" \
  "ds_read_b64_tr_b16 v[198:199], v36 offset:28160\n\t" \
  "v_mfma_f32_16x16x32_bf16 v[240:243], v[208:211], v[144:147], v[240:243]\n\t" \
  "ds_read_b64_tr_b16 v[200:201], v37 offset:19200\n\t" \
  "s_waitcnt lgkmcnt(6)\n\t" \
  "v_mfma_f32_16x16x32_bf16 v[240:243], v[212:215], v[148:151], v[240:243]\n\t" \
  "ds_read_b128 v[40:43], v56 offset:160\n\t" \
  "ds_read_b64_tr_b16 v[202:203], v37 offset:19712\n\t" \
  "v_mfma_f32_16x16x32_bf16 v[244:247], v[212:215], v[156:159], v[244:247]\n\t" \
  "ds_read_b64_tr_b16 v[204:205], v37 offset:27648\n\t" \
  "v_mfma_f32_16x16x32_bf16 v[244:247], v[208:211], v[152:155], v[244:247]\n\t" \
  "ds_read_b64_tr_b16 v[206:207], v37 offset:28160\n\t" \
  "v_mfma_f32_16x16x32_bf16 v[248:251], v[216:219], v[144:147], v[248:251]\n\t" \
  "ds_read_b64_tr_b16 v[208:209], v38 offset:19200\n\t" \
  "s_waitcnt lgkmcnt(7)\n\t" \
  "v_mfma_f32_16x16x32_bf16 v[248:251], v[220:223], v[148:151], v[248:251]\n\t" \
  "ds_read_b64_tr_b16 v[210:211], v38 offset:19712\n\t" \
  "v_mfma_f32_16x16x32_bf16 v[252:255], v[220:223], v[156:159], v[252:255]\n\t" \
  "ds_read_b64_tr_b16 v[212:213], v38 offset:27648\n\t" \
  "v_mfma_f32_16x16x32_bf16 v[252:255], v[216:219], v[152:155], v[252:255]\n\t" \
  "ds_read_b64_tr_b16 v[214:215], v38 offset:28160\n\t" \
  "s_waitcnt vmcnt(6)\n\t" \
  "v_mfma_f32_16x16x32_bf16 v[224:227], v[192:195], v[160:163], v[224:227]\n\t" \
  "s_waitcnt lgkmcnt(7)\n\t" \
  "v_min_u32 v40, v40, v41\n\t" \
  "v_min3_u32 v40, v40, v42, v43\n\t" \
  "ds_read_b64_tr_b16 v[216:217], v39 offset:19200\n\t" \
  "v_mfma_f32_16x16x32_bf16 v[224:227], v[196:199], v[164:167], v[224:227]\n\t" \
  "v_readfirstlane_b32 vcc_lo, v40\n\t" \
  "ds_read_b64_tr_b16 v[218:219], v39 offset:19712\n\t" \
  "s_waitcnt vmcnt(4)\n\t" \
  "v_mfma_f32_16x16x32_bf16 v[228:231], v[196:199], v[172:175], v[228:231]\n\t" \
  "s_cmp_ge_u32 vcc_lo, 16\n\t" \
  "s_cbranch_scc0 42f\n\t" \
  "43:\n\t" \
  "ds_read_b64_tr_b16 v[220:221], v39 offset:27648\n\t" \
  "v_mfma_f32_16x16x32_bf16 v[228:231], v[192:195], v[168:171], v[228:231]\n\t" \
  "ds_read_b64_tr_b16 v[222:223], v39 offset:28160\n\t" \
  "s_waitcnt lgkmcnt(6)\n\t" \
  "v_mfma_f32_16x16x32_bf16 v[232:235], v[200:203], v[160:163], v[232:235]\n\t" \
  "ds_read_b64_tr_b16 v[192:193], v28 offset:0\n\t" \
  "v_mfma_f32_16x16x32_bf16 v[232:235], v[204:207], v[164:167], v[232:235]\n\t" \
  "ds_read_b64_tr_b16 v[194:195], v28 offset:512\n\t" \
  "v_mfma_f32_16x16x32_bf16 v[236:239], v[204:207], v[172:175], v[236:239]\n\t" \
  "ds_read_b64_tr_b16 v[196:197], v28 offset:4096\n\t" \
  "v_mfma_f32_16x16x32_bf16 v[236:239], v[200:203], v[168:171], v[236:239]\n\t" \
  "ds_read_b64_tr_b16 v[198:199], v28 offset:4608\n\t" \
  "v_mfma_f32_16x16x32_bf16 v[240:243], v[208:211], v[160:163], v[240:243]\n\t" \
  "ds_read_b64_tr_b16 v[200:201], v29 offset:0\n\t" \
  "s_waitcnt lgkmcnt(6)\n\t" \
  "v_mfma_f32_16x16x32_bf16 v[240:243], v[212:215], v[164:167], v[240:243]\n\t" \
  "ds_read_b64_tr_b16 v[202:203], v29 offset:512\n\t" \
  "v_mfma_f32_16x16x32_bf16 v[244:247], v[212:215], v[172:175], v[244:247]\n\t" \
  "ds_read_b64_tr_b16 v[204:205], v29 offset:4096\n\t" \
  "v_mfma_f32_16x16x32_bf16 v[244:247], v[208:211], v[168:171], v[244:247]\n\t" \
  "ds_read_b64_tr_b16 v[206:207], v29 offset:4608\n\t" \
  "v_mfma_f32_16x16x32_bf16 v[248:251], v[216:219], v[160:163], v[248:251]\n\t" \
  "ds_read_b64_tr_b16 v[208:209], v30 offset:0\n\t" \
  "s_waitcnt lgkmcnt(6)\n\t" \
  "v_mfma_f32_16x16x32_bf16 v[248:251], v[220:223], v[164:167], v[248:251]\n\t" \
  "ds_read_b64_tr_b16 v[210:211], v30 offset:512\n\t" \
  "v_mfma_f32_16x16x32_bf16 v[252:255], v[220:223], v[172:175], v[252:255]\n\t" \
  "ds_read_b64_tr_b16 v[212:213], v30 offset:4096\n\t" \
  "v_mfma_f32_16x16x32_bf16 v[252:255], v[216:219], v[168:171], v[252:255]\n\t" \
  "ds_read_b64_tr_b16 v[214:215], v30 offset:4608\n\t" \
  "s_waitcnt vmcnt(2)\n\t" \
  "v_mfma_f32_16x16x32_bf16 v[224:227], v[192:195], v[176:179], v[224:227]\n\t" \
  "ds_read_b64_tr_b16 v[216:217], v31 offset:0\n\t" \
  "s_waitcnt lgkmcnt(6)\n\t" \
  "v_mfma_f32_16x16x32_bf16 v[224:227], v[196:199], v[180:183], v[224:227]\n\t" \
  "ds_read_b64_tr_b16 v[218:219], v31 offset:512\n\t" \
  "s_waitcnt vmcnt(0)\n\t" \
  "v_mfma_f32_16x16x32_bf16 v[228:231], v[196:199], v[188:191], v[228:231]\n\t" \
  "ds_read_b64_tr_b16 v[220:221], v31 offset:4096\n\t" \
  "v_mfma_f32_16x16x32_bf16 v[228:231], v[192:195], v[184:187], v[228:231]\n\t" \
  "ds_read_b64_tr_b16 v[222:223], v31 offset:4608\n\t" \
  "v_mfma_f32_16x16x32_bf16 v[232:235], v[200:203], v[176:179], v[232:235]\n\t" \
  "s_waitcnt lgkmcnt(5)\n\t" \
  "v_mfma_f32_16x16x32_bf16 v[232:235], v[204:207], v[180:183], v[232:235]\n\t" \
  "v_mfma_f32_16x16x32_bf16 v[236:239], v[204:207], v[188:191], v[236:239]\n\t" \
  "v_cvt_pk_bf16_f32 v224, v224, v225\n\t" \
  "v_cvt_pk_bf16_f32 v225, v226, v227\n\t" \
  "v_mfma_f32_16x16x32_bf16 v[236:239], v[200:203], v[184:187], v[236:239]\n\t" \
  "ds_write_b64 v57, v[224:225] offset:0\n\t" \
  "v_mfma_f32_16x16x32_bf16 v[240:243], v[208:211], v[176:179], v[240:243]\n\t" \
  "v_cvt_pk_bf16_f32 v228, v228, v229\n\t" \
  "v_cvt_pk_bf16_f32 v229, v230, v231\n\t" \
  "s_waitcnt lgkmcnt(2)\n\t" \
  "v_mfma_f32_16x16x32_bf16 v[240:243], v[212:215], v[180:183], v[240:243]\n\t" \
  "ds_write_b64 v57, v[228:229] offset:2048\n\t" \
  "v_mfma_f32_16x16x32_bf16 v[244:247], v[212:215], v[188:191], v[244:247]\n\t" \
  "v_cvt_pk_bf16_f32 v232, v232, v233\n\t" \
  "v_cvt_pk_bf16_f32 v233, v234, v235\n\t" \
  "v_mfma_f32_16x16x32_bf16 v[244:247], v[208:211], v[184:187], v[244:247]\n\t" \
  "ds_write_b64 v58, v[232:233] offset:0\n\t" \
  "v_mfma_f32_16x16x32_bf16 v[248:251], v[216:219], v[176:179], v[248:251]\n\t" \
  "v_cvt_pk_bf16_f32 v236, v236, v237\n\t" \
  "v_cvt_pk_bf16_f32 v237, v238, v239\n\t" \
  "s_waitcnt lgkmcnt(3)\n\t" \
  "v_mfma_f32_16x16x32_bf16 v[248:251], v[220:223], v[180:183], v[248:251]\n\t" \
  "ds_write_b64 v58, v[236:237] offset:2048\n\t" \
  "v_mfma_f32_16x16x32_bf16 v[252:255], v[220:223], v[188:191], v[252:255]\n\t" \
  "v_cvt_pk_bf16_f32 v240, v240, v241\n\t" \
  "v_cvt_pk_bf16_f32 v241, v242, v243\n\t" \
  "v_mfma_f32_16x16x32_bf16 v[252:255], v[216:219], v[184:187], v[252:255]\n\t" \
  "ds_write_b64 v59, v[240:241] offset:0\n\t" \
  "v_cvt_pk_bf16_f32 v244, v244, v245\n\t" \
  "v_cvt_pk_bf16_f32 v245, v246, v247\n\t" \
  "ds_write_b64 v59, v[244:245] offset:2048\n\t" \
  "v_cvt_pk_bf16_f32 v248, v248, v249\n\t" \
  "v_cvt_pk_bf16_f32 v249, v250, v251\n\t" \
  "ds_write_b64 v60, v[248:249] offset:0\n\t" \
  "s_nop 0\n\t" \
  "v_cvt_pk_bf16_f32 v252, v252, v253\n\t" \
  "v_cvt_pk_bf16_f32 v253, v254, v255\n\t" \
  "ds_write_b64 v60, v[252:253] offset:2048\n\t" \
  "ds_read_b128 v[64:67], v61 offset:0\n\t" \
  "ds_read_b128 v[68:71], v61 offset:1024\n\t" \
  "ds_read_b128 v[72:75], v61 offset:2048\n\t" \
  "ds_read_b128 v[76:79], v61 offset:3072\n\t" \
  "s_waitcnt lgkmcnt(3)\n\t" \
  "global_store_dwordx4 v62, v[64:67], %22 offset:-4096" OUTPOL "\n\t" \
  "s_waitcnt lgkmcnt(2)\n\t" \
  "global_store_dwordx4 v63, v[68:71], %22 offset:-4096" OUTPOL "\n\t" \
  "s_waitcnt lgkmcnt(1)\n\t" \
  "global_store_dwordx4 v62, v[72:75], %22 offset:0" OUTPOL "\n\t" \
  "s_waitcnt lgkmcnt(0)\n\t" \
  "global_store_dwordx4 v63, v[76:79], %22 offset:0" OUTPOL "\n\t" \
  "s_branch 19f\n\t" \
  "23:\n\t" \
  "510:\n\t" \
  "s_cbranch_execz 26b\n\t" \
  "s_waitcnt vmcnt(0)\n\t" \
  "s_mov_b64 exec, 1\n\t" \
  "ds_add_rtn_u32 v10, v56, v10 offset:16\n\t" \
  "s_mov_b32 s74, 16\n\t" \
  "s_waitcnt lgkmcnt(0)\n\t" \
  "v_readfirstlane_b32 vcc_lo, v10\n\t" \
  "s_mov_b64 exec, 0xffff\n\t" \
  "s_nop 0\n\t" \
  "s_cmp_eq_u32 vcc_lo, 3\n\t" \
  "s_cbranch_scc0 22f\n\t" \
  "global_store_dword v11, v55, %19 sc1\n\t" \
  "22:\n\t" \
  "s_mov_b32 exec_lo, -1\n\t" \
  "global_load_dword v2, v1, %14 offset:-4096 sc1\n\t" \
  "s_mov_b64 exec, -1\n\t" \
  "s_setpc_b64 s[68:69]\n\t" \
  "7:\n\t" \
  "s_sleep 1\n\t" \
  "ds_read_b128 v[60:63], v56 offset:64\n\t" \
  "ds_read_b64_tr_b16 v[108:109], v5 offset:0\n\t" \
  "ds_read_b64_tr_b16 v[110:111], v5 offset:16\n\t" \
  "ds_read_b128 v[112:115], v6 offset:16384\n\t" \
  "ds_read_b128 v[116:119], v6 offset:15360\n\t" \
  "ds_read_b64_tr_b16 v[120:121], v5 offset:512\n\t" \
  "ds_read_b64_tr_b16 v[122:123], v5 offset:528\n\t" \
  "ds_read_b128 v[124:127], v6 offset:14336\n\t" \
  "ds_read_b64_tr_b16 v[128:129], v5 offset:1024\n\t" \
  "ds_read_b64_tr_b16 v[130:131], v5 offset:1040\n\t" \
  "ds_read_b128 v[132:135], v6 offset:13312\n\t" \
  "ds_read_b64_tr_b16 v[136:137], v5 offset:1536\n\t" \
  "ds_read_b64_tr_b16 v[138:139], v5 offset:1552\n\t" \
  "ds_read_b128 v[140:143], v6 offset:12288\n\t" \
  "ds_read_b64_tr_b16 v[144:145], v5 offset:2048\n\t" \
  "s_waitcnt lgkmcnt(0)\n\t" \
  "v_and_b32 v60, v60, v61\n\t" \
  "v_and_b32 v62, v62, v63\n\t" \
  "v_mov_b32 v12, %13\n\t" \
  "v_and_b32 v60, v60, v62\n\t" \
  "s_nop 0\n\t" \
  "v_readfirstlane_b32 vcc_lo, v60\n\t" \
  "s_nop 1\n\t" \
  "s_cmp_eq_u32 vcc_lo, 0\n\t" \
  "s_cbranch_scc1 7b\n\t" \
  "s_branch 6b\n\t" \
  "25:\n\t" \
  "511:\n\t" \
  "s_cbranch_execz 26b\n\t" \
  "global_load_dwordx4 v[64:67], v53, s[90:91] offset:0 sc1\n\t" \
  "global_load_dwordx4 v[68:71], v53, s[90:91] offset:1024 sc1\n\t" \
  "global_load_dwordx4 v[72:75], v53, s[90:91] offset:2048 sc1\n\t" \
  "global_load_dwordx4 v[76:79], v53, s[90:91] offset:3072 sc1\n\t" \
  "s_swappc_b64 s[70:71], s[68:69]\n\t" \
  "global_load_dwordx4 v[80:83], v53, s[90:91] offset:0 sc1\n\t" \
  "global_load_dwordx4 v[84:87], v53, s[90:91] offset:1024 sc1\n\t" \
  "global_load_dwordx4 v[88:91], v53, s[90:91] offset:2048 sc1\n\t" \
  "global_load_dwordx4 v[92:95], v53, s[90:91] offset:3072 sc1\n\t" \
  "s_swappc_b64 s[70:71], s[68:69]\n\t" \
  "36:\n\t" \
  "ds_read_b128 v[40:43], v56 offset:80\n\t" \
  "s_waitcnt lgkmcnt(0)\n\t" \
  "v_min_u32 v40, v40, v41\n\t" \
  "v_min3_u32 v40, v40, v42, v43\n\t" \
  "s_nop 1\n\t" \
  "v_readfirstlane_b32 vcc_lo, v40\n\t" \
  "s_nop 1\n\t" \
  "s_cmp_ge_u32 vcc_lo, 7\n\t" \
  "s_cbranch_scc0 36b\n\t" \
  "s_branch 37b\n\t" \
  "38:\n\t" \
  "ds_read_b128 v[40:43], v56 offset:80\n\t" \
  "s_waitcnt lgkmcnt(0)\n\t" \
  "v_min_u32 v40, v40, v41\n\t" \
  "v_min3_u32 v40, v40, v42, v43\n\t" \
  "s_nop 1\n\t" \
  "v_readfirstlane_b32 vcc_lo, v40\n\t" \
  "s_nop 1\n\t" \
  "s_cmp_ge_u32 vcc_lo, 13\n\t" \
  "s_cbranch_scc0 38b\n\t" \
  "s_branch 39b\n\t" \
  "40:\n\t" \
  "ds_read_b128 v[40:43], v56 offset:80\n\t" \
  "s_waitcnt lgkmcnt(0)\n\t" \
  "v_min_u32 v40, v40, v41\n\t" \
  "v_min3_u32 v40, v40, v42, v43\n\t" \
  "s_nop 1\n\t" \
  "v_readfirstlane_b32 vcc_lo, v40\n\t" \
  "s_nop 1\n\t" \
  "s_cmp_ge_u32 vcc_lo, 16\n\t" \
  "s_cbranch_scc0 40b\n\t" \
  "s_branch 41b\n\t" \
  "42:\n\t" \
  "ds_read_b128 v[40:43], v56 offset:160\n\t" \
  "s_waitcnt lgkmcnt(0)\n\t" \
  "v_min_u32 v40, v40, v41\n\t" \
  "v_min3_u32 v40, v40, v42, v43\n\t" \
  "s_nop 1\n\t" \
  "v_readfirstlane_b32 vcc_lo, v40\n\t" \
  "s_nop 1\n\t" \
  "s_cmp_ge_u32 vcc_lo, 16\n\t" \
  "s_cbranch_scc0 42b\n\t" \
  "s_branch 43b\n\t" \
  "46:\n\t" \
  "s_cmp_eq_u32 %13, 0\n\t" \
  "s_cbranch_scc1 48f\n\t" \
  "s_sleep 1\n\t" \
  "ds_read_b32 v16, v56 offset:96\n\t" \
  "s_waitcnt lgkmcnt(0)\n\t" \
  "v_readfirstlane_b32 vcc_lo, v16\n\t" \
  "s_nop 1\n\t" \
  "s_cmp_ge_u32 vcc_lo, 1\n\t" \
  "s_cbranch_scc0 46b\n\t" \
  "s_branch 47b\n\t" \
  "48:\n\t" \
  "v_bfe_u32 v19, v12, 4, 4\n\t" \
  "v_lshlrev_b32 v19, 2, v19\n\t" \
  "s_mov_b32 s90, 0\n\t" \
  "148:\n\t" \
  "global_load_dword v16, v19, %21 sc1\n\t" \
  "s_add_u32 s90, s90, 1\n\t" \
  "s_waitcnt vmcnt(0)\n\t" \
  "v_cmp_ne_u32 vcc, v55, v16\n\t" \
  "s_mov_b32 s88, %26\n\t" \
  "s_mov_b32 s89, s88\n\t" \
  "s_and_b64 vcc, vcc, s[88:89]\n\t" \
  "s_cbranch_scc0 248f\n\t" \
  "s_sleep 2\n\t" \
  "s_cmp_lt_u32 s90, 0x400000\n\t" \
  "s_cbranch_scc1 148b\n\t" \
  "248:\n\t" \
  "v_mov_b32 v16, 1\n\t" \
  "ds_max_u32 v56, v16 offset:96\n\t" \
  "s_branch 47b\n\t" \
  "49:\n\t" \
  "s_cmp_eq_u32 %13, 0\n\t" \
  "s_cbranch_scc1 51f\n\t" \
  "s_sleep 1\n\t" \
  "ds_read_b32 v16, v56 offset:96\n\t" \
  "s_waitcnt lgkmcnt(0)\n\t" \
  "v_readfirstlane_b32 vcc_lo, v16\n\t" \
  "s_nop 1\n\t" \
  "s_cmp_ge_u32 vcc_lo, 2\n\t" \
  "s_cbranch_scc0 49b\n\t" \
  "s_branch 50b\n\t" \
  "51:\n\t" \
  "v_bfe_u32 v19, v12, 4, 4\n\t" \
  "v_lshlrev_b32 v19, 2, v19\n\t" \
  "s_mov_b32 s90, 0\n\t" \
  "151:\n\t" \
  "global_load_dword v16, v19, %21 sc1\n\t" \
  "s_add_u32 s90, s90, 1\n\t" \
  "s_waitcnt vmcnt(0)\n\t" \
  "v_cmp_ne_u32 vcc, v55, v16\n\t" \
  "s_mov_b32 s88, %26\n\t" \
  "s_mov_b32 s89, s88\n\t" \
  "s_and_b64 vcc, vcc, -1\n\t" \
  "s_cbranch_scc0 251f\n\t" \
  "s_sleep 2\n\t" \
  "s_cmp_lt_u32 s90, 0x400000\n\t" \
  "s_cbranch_scc1 151b\n\t" \
  "251:\n\t" \
  "v_mov_b32 v16, 2\n\t" \
  "ds_max_u32 v56, v16 offset:96\n\t" \
  "s_branch 50b\n\t" \
  "19:\n\t"
#define C_CLOB "v10","v11","v12","v13","v18","v19","v20","v21","v22","v23","v24","v25","v26","v27","v28","v29","v30","v31","v32","v33","v34","v35","v36","v37","v38","v39","v40","v41","v42","v43","v44","v45","v46","v47","v48","v49","v50","v53","v55","v56","v57","v58","v59","v60","v61","v62","v63","v64","v65","v66","v67","v68","v69","v70","v71","v72","v73","v74","v75","v76","v77","v78","v79","v80","v81","v82","v83","v84","v85","v86","v87","v88","v89","v90","v91","v92","v93","v94","v95","v96","v97","v98","v99","v100","v101","v102","v103","v104","v105","v106","v107","v108","v109","v110","v111","v112","v113","v114","v115","v116","v117","v118","v119","v120","v121","v122","v123","v124","v125","v126","v127","v128","v129","v130","v131","v132","v133","v134","v135","v136","v137","v138","v139","v140","v141","v142","v143","v144","v145","v146","v147","v148","v149","v150","v151","v152","v153","v154","v155","v156","v157","v158","v159","v160","v161","v162","v163","v164","v165","v166","v167","v168","v169","v170","v171","v172","v173","v174","v175","v176","v177","v178","v179","v180","v181","v182","v183","v184","v185","v186","v187","v188","v189","v190","v191","v192","v193","v194","v195","v196","v197","v198","v199","v200","v201","v202","v203","v204","v205","v206","v207","v208","v209","v210","v211","v212","v213","v214","v215","v216","v217","v218","v219","v220","v221","v222","v223","v224","v225","v226","v227","v228","v229","v230","v231","v232","v233","v234","v235","v236","v237","v238","v239","v240","v241","v242","v243","v244","v245","v246","v247","v248","v249","v250","v251","v252","v253","v254","v255"

static_assert(KVP<13 && KVP>TGS+1, "");

#define TS(i)
#define TSDUMP()
#define TSB(i)
#define TSR(i)
#define TSH(i)
#define TSDECL()

#define TSF(i)
typedef unsigned long long u64;
typedef u64 u64x8 __attribute__((ext_vector_type(8)));
typedef u64 u64x4 __attribute__((ext_vector_type(4)));
typedef unsigned u32x8 __attribute__((ext_vector_type(8)));
typedef unsigned u32x16 __attribute__((ext_vector_type(16)));
template<class T> __device__ __forceinline__ T sg(T v){
  if constexpr(sizeof(T)==4) return (T)__builtin_amdgcn_readfirstlane((unsigned)v); else return v; }
#define GP(T,v) ((T*)(__attribute__((address_space(1))) T*)(v))
__global__ __launch_bounds__(512,2) __attribute__((aligned(4096))) void fused_kernel(const u16* x_, const u16* wqkv_, const u16* kin_, const u16* vin_, const u16* shk_, const u16* hk_,
    const u16* wo_, const u16* qin_, const u16* sD_, u16* out_, u16* ws_, u16* unused_)
{
#define ST0()
#define ST1()
#define ARGS0() u64x8 ka; u64x4 kb; asm volatile("s_load_dwordx16 %0, %2, 0\n\ts_load_dwordx8 %1, %2, 64" : "=&s"(ka), "=&s"(kb) : "s"(__builtin_amdgcn_kernarg_segment_ptr()) : "memory")
#define ARGS() asm volatile("s_waitcnt lgkmcnt(0)" : "+s"(ka), "+s"(kb) :: "memory"); ST1() \
  const u16 *x=GP(const u16,ka[0]), *wqkv=GP(const u16,ka[1]), *kin=GP(const u16,ka[2]), *vin=GP(const u16,ka[3]), *shk=GP(const u16,ka[4]), *hk=GP(const u16,ka[5]), *wo=GP(const u16,ka[6]), *qin=GP(const u16,ka[7]), *sD=GP(const u16,kb[0]); \
  u16 *out=GP(u16,kb[1]), *o=GP(u16,kb[2]); char* ws=(char*)o; unsigned cpar=xcd>>2; \
  auto loc=[&](unsigned off)->unsigned{ return off|(((cpar^(off>>21))&1)<<13); }; \
  (void)x; (void)wqkv; (void)kin; (void)vin; (void)shk; (void)hk; (void)wo; (void)qin; (void)sD; (void)out; (void)loc
  ST0()
  __shared__ __attribute__((aligned(1024))) char smem[ALL_LDS];
  const int tid=threadIdx.x;
  const int w8=__builtin_amdgcn_readfirstlane(tid>>6);
  asm volatile("s_sub_u32 vcc_lo, %0, 4\n\ts_cbranch_scc1 1f\n\ts_cmp_lg_u32 %0, 7\n\ts_cbranch_scc1 2f\n\t"
      "s_lshr_b32 vcc_lo, %2, 3\n\ts_mul_i32 vcc_lo, vcc_lo, %5\n\tv_and_b32 v254, 15, %1\n\tv_lshl_add_u32 v254, v254, 7, vcc_lo\n\ts_getpc_b64 vcc\n\tglobal_load_dword v255, v254, vcc\n\ts_branch 1f\n"
      "2:\n\ts_and_b32 vcc_hi, %2, 7\n\ts_cbranch_scc1 1f\n\ts_cmp_gt_u32 %2, 32\n\ts_cbranch_scc1 1f\n\ts_mul_i32 vcc_hi, %2, 3\n\ts_lshr_b32 vcc_hi, vcc_hi, 3\n\ts_add_u32 vcc_lo, vcc_lo, vcc_hi\n\t"
      "v_and_b32 v253, 3, %1\n\tv_and_b32 v254, 4, %1\n\tv_lshlrev_b32 v253, 7, v253\n\tv_lshl_add_u32 v253, v254, 11, v253\n\tv_lshl_add_u32 v253, vcc_lo, 9, v253\n\tv_add_u32 v253, %4, v253\n\t"
      "s_load_dwordx2 vcc, %3, 80\n\ts_mov_b64 exec, 255\n\ts_waitcnt lgkmcnt(0)\n\tglobal_store_dword v253, v253, vcc sc1\n\ts_mov_b64 exec, -1\n1:"
      :: "s"(w8), "v"(tid), "s"(blockIdx.x), "s"(__builtin_amdgcn_kernarg_segment_ptr()), "n"(WS_GEN+16384), "n"(CODE_ST) : "memory", "scc", "vcc", "v253", "v254", "v255");
  LDS_AS unsigned* const ct=(LDS_AS unsigned*)(LDS_AS char*)(smem+CT_OFF);
  volatile LDS_AS unsigned* const vct=ct;
  auto all4=[&](int i)->unsigned{ u32x4 v=*(volatile LDS_AS u32x4*)(ct+i); return __builtin_amdgcn_readfirstlane(v[0]&v[1]&v[2]&v[3]); };
#define ROTF(b) ((4+4*xr+4*xc+8*(b))&15)
#define BLOCK() int bid=blockIdx.x, w=w8&3; asm volatile("" : "+s"(bid), "+s"(w)); __builtin_assume(bid>=0 && bid<256 && w>=0 && w<4); DERIVE()
#define DERIVE() const int xcd=bid&7, idx=bid>>3, ra=idx&3, rbb=idx>>2, xr=xcd>>1, xc=xcd&1, rb=xr*4+ra, hp=xc*8+rbb, s0=rb*128, tm=rb, tn=hp^TNX, cb=(xcd>>2)*128+idx*4+(xcd&3), gi=cb^16; (void)s0; (void)gi; \
  auto korder=[&]()->unsigned long long{ const unsigned N=0x7654210u, sh=4*(KR), q=(((N>>sh)|(N<<(28-sh)))&0xfffffffu)|0x30000000u; \
    return ((unsigned long long)q<<32|q)^(xc? 0x88888888ull : 0x8888888800000000ull); }; \
  const unsigned l1m=0xf7u<<(8*xc); (void)l1m; (void)korder

  if(__builtin_expect(w8>=4,0)){
  BLOCK(); constexpr int WB=4; (void)WB;
  const int hd=w>>1, par=w&1;
  __builtin_amdgcn_s_setprio(CPRIO);
  {
    ARGS0(); LANES();
    if(w==0){ if(l<52) ct[l]=0u; if(idx==0) *(u32x4*)(smem+AUX+l*16)=u32x4{0,0,0,0}; }
    unsigned a0b=fr*128+((g^(fr&7))*16);
    const int r0=8*g+tq;
    unsigned bbb=r0*128+(((2*hd)^wswz(r0))*32)+8*tp;
    unsigned kA=AUX+(fr^3)*KVP_+w*64+(g&1)*16-(g>>1)*256, kB=AUX+(20+4*g-tq)*KVP_+w*64+8*tp;
    const int cch=127-4*idx-g;
    unsigned kvo=(8*w+tq)*32768+(cch>>6)*16384+((cch>>4)&3)*2048+(cch&15)*64+((tp^((cch>>2)&3))*16);
    unsigned sho=(xcd*128+32*w+fr)*8; const unsigned ctl=CT_OFF+4*w;
    u32x4 sel;
    { const unsigned b2=(g>>1)? 0x0302u : 0x0100u, sv=(fr&1)? (b2<<16|0x0c0cu) : (0x0c0c0000u|b2);
      #pragma unroll
      for(int i=0;i<4;i++) sel[i]= (fr>>1)==(g&1)*4+i ? sv : 0x0c0c0c0cu; }
    unsigned msk=0, lx=w*1024+l*16;
    #pragma unroll
    for(int e=0;e<4;e++){ const int d=4*g+e-fr; const unsigned pv=d<9, hv=par? pv : (unsigned)(d<1 || 4*g+e>7);
      msk|=((unsigned)(d+7)>7u?1u:0u)<<e | pv<<(4+e) | hv<<(8+e) | (d<=0?1u:0u)<<(12+e); }
    msk|=(unsigned)(g? l-16 : 48+((l-1)&15))<<26;
    const unsigned hb=__builtin_amdgcn_readfirstlane(rb==0 ? 0xcfa8965fu : 0u);
    unsigned ap=AUX+w*CV_W+(tq&1)*(CV_CP-4)+8+32*g+4*tq+8*tp;
    u32x4 bp;
    #pragma unroll
    for(int b=0;b<4;b++){ const int c=15+4*b-fr+g; bp[b]=c*64+((w^((c>>2)&3))*16); }
    asm volatile("" : "+v"(a0b), "+v"(bbb), "+v"(kA), "+v"(kB), "+v"(kvo), "+v"(sho), "+v"(msk), "+v"(lx), "+v"(ap), "+v"(bp), "+v"(sel));
    ARGS();
    const char* ow=ws+loc((rb*16+hp)*32768)+(par*2+hd)*1024+4096;
    asm volatile("" : "+s"(shk));
    asm volatile(C_ASM : "+{v52}"(a0b), "+{v54}"(bbb), "+{v0}"(kA), "+{v1}"(kB), "+{v2}"(kvo), "+{v51}"(sho), "+{v3}"(msk), "+{v4}"(lx), "+{v5}"(ap), "+{v[6:9]}"(bp), "+{v[14:17]}"(sel)
       : "s"(shk), "s"(ctl), "s"(w), "s"(ow), "s"(hb), "s"(0.17677669529663687f*1.44269504088896341f), "{s[92:93]}"(ws+loc(tm*16*32768)), "s"(korder()),
         "s"(ws+loc(flo(1,rb*16))+hp*4), "s"(AUX+w*(CV_W+64)), "s"(ws+loc(flo(1,tm*16+tn))),
         "s"(out+(size_t)(tm*128+w*32+1)*2048+1024+tn*64), "s"(8192+w*4096), "s"(ws+loc(WS_KV+xcd*32*32768)), "s"(ws+loc(flo(0,xcd*32))+idx*4), "s"(l1m), "s"(xcd==0 && idx<5 && w<3? (u64)(ws+WS_GEN+idx*3+w) : 0ull) : "memory", "scc", "vcc", "s68", "s69", "s70", "s71", "s74", "s75", "s76", "s77", "s88", "s89", "s90", "s91",
       C_CLOB);
    TSDECL();
    TS(20); TSDUMP();
  }
  return;
  }
  {
    ARGS0(); BLOCK(); constexpr int WB=0; (void)WB; LANES(); TSDECL();
    const unsigned vx=(l>>3)*4096+(((l&7)^(l>>3))*16), vxs=vx+(s0+32*w)*2048, vw=(l>>3)*6144+((((l&7)>>1)^((l>>4)&1))*32)+(l&1)*16+hp*128+w*98304, vw1=(vw^64)+49152-1024;
    u32x8 va; u32x2 vb; unsigned vsp;
    #pragma unroll
    for(int i=0;i<6;i++) va[i]=((i&1)? vw1 : vw)+(i>>1)*2048;
    va[6]=vxs; va[7]=vxs+32768-1024; vb[0]=vxs+2048; vb[1]=vxs+2048+32768-1024;
    vsp=vx+(rb? s0-16+w : 0)*2048;
    const unsigned sw1=w*1024, sw2=w*2048;
    unsigned rot=ROTF(rbb);
    asm volatile("" : "+v"(va), "+v"(vb), "+v"(vsp));
    asm volatile("" : "+s"(rot));
    unsigned vt=(w&1)? (xr*512+idx*16+(l>>1))*2048+(l&1)*1024 : (2*idx+(l&1))*6144+((l>>1)&3)*2048+xc*1024;
    const int kt0=idx*64; int kvs=KS0+KVSEL;
    unsigned kvl=(kt0+16*w+g)*2048+xcd*256+fr*16, l16=l*16;
    const bool hl= w==0 && kt0;
    unsigned c46=sg(w<2? 128u : w==2? 393216u : 0u), c49=sg(w<2? sw1 : (unsigned)ALL_LDS-96), c64=sg(hl? 8192u : 0u), c68=sg(hl? AUX : ALL_LDS-96);
    unsigned long long c50= w<2? ~0ull : w==2? 63ull : 1ull, c66= hl? ~0ull : 1ull;
    asm volatile("" : "+v"(vt), "+v"(kvl), "+v"(l16));
    asm volatile("" : "+s"(c46), "+s"(c49), "+s"(c64), "+s"(c68), "+s"(c50), "+s"(c66), "+s"(kvs));
    ARGS();
    TSB(16);
    asm volatile(H0_ASM : "+{s[40:41]}"(x), "+{s[42:43]}"(wqkv) : "{v[0:7]}"(va), "{v[8:9]}"(vb), "{v10}"(vsp), "{s52}"(rot), "{s53}"(sw2), "{s59}"(w)
      : "memory", "scc", "s76", "s77", "s78", "s79", "s82", "s83");
    const u16* spb= w==2? wqkv : x;
    const int hl3=(3-(int)((size_t)spb>>7))&7, roth=ROTF(hl3), xs=((hl3-ROTF(0))&7)-LEAD;
    unsigned i11=sg((w==2? roth+LEAD+1 : rot+1)&15), sbo, sst;
    if(w>=2){
      vsp=vt+hl3*128;
      touch(spb, vsp+((roth+(l>>3))&15)*c46, w==2? 0x3f3f3f3f3f3f3f3full>>(56-8*LEAD) : xs<=1? 0xffffffffull : 1ull);
    }
    TSB(17);
    unsigned gof=(((gi&15)*128+64*(w&1)+4*(w>>1)+16*((l>>5)&1)+((l>>3)&3))*DM+(gi>>4)*64)*2+(l16&127);
    unsigned wv[2];
    wv[0]=(((l>>5)*16+w*4+((l>>3)&3))*DM+tn*64)*2+((l16&127)^((l&16)*2)^((w&1)*64)); wv[1]=wv[0]+32*DM*2;
    unsigned zpa=(w>>1)*9216+(w&1)*512+l16, hda=AUX+w*CV_W+(l&1)*CV_CP+(l>>1)*16, yra=AUX+tq*(CV_W+64)+(g>>1)*128+(g&1)*32+tp*8+w*1024;
    const unsigned f0=loc(flo(0,cb)), f1=loc(flo(1,tm*16+tn)), f2=loc(flo(2,gi));
    unsigned vc=(w&1)? (g==0? WS_GEN : (g==1? f0 : g==2? f1 : f2)^((l&1)<<13)) : idx*CODE_ST+fr*128u;
    unsigned kfo=f0+(l&31)*4, ofo= w==0? f1+fr*4 : WS_GEN;
    asm volatile("" : "+v"(kfo), "+v"(ofo), "+v"(gof), "+v"(wv[0]), "+v"(wv[1]), "+v"(hda), "+v"(yra), "+v"(zpa), "+v"(vc));
    unsigned long long kt=korder();
    asm volatile("" : "+s"(kt));
#define HDBG_O
#define HDBG_I
#define H_IN "{v[0:7]}"(va), "{v[8:9]}"(vb), "{v10}"(vsp), "{v11}"(kvl), "{s[40:41]}"(x), "{s[42:43]}"(wqkv), "{s[44:45]}"(spb), "{s46}"(c46), "{s49}"(c49), "{s[50:51]}"(c50), "{s53}"(sw2), \
      "{s58}"(kvs), "{s59}"(w), "{s[60:61]}"(kin), "{s[62:63]}"(vin), "{s64}"(c64), "{s[66:67]}"(c66), "{s68}"(c68) HDBG_I
#define H_CLOB "memory", "scc", "vcc", "s76", "s77", "s78", "s79", "s80", "s81", "s82", "s83", "s84", "s85", "v20", "v21", "v22", "v253", "v254", "v255"
    asm volatile(H1_ASM : "+{s47}"(i11), "+{s52}"(rot), "={s54}"(sbo), "={s55}"(sst) HDBG_O : H_IN : H_CLOB);
    const u16* hrow=hk+(size_t)(cb*4+w)*S;
    u32x4 h0; unsigned dvu;
    asm volatile("s_load_dwordx4 %0, %2, 0\n\ts_load_dword %1, %3, %4" : "=&s"(h0), "=&s"(dvu) : "s"(hrow), "s"(sD), "s"(cb*8+(w&2)*2) : "memory");
    unsigned long long pc; asm volatile("s_getpc_b64 %0" : "=s"(pc)); pc&=~4095ull;
    asm volatile("" : "+s"(wo), "+s"(qin), "+s"(out), "+s"(ws));
    __builtin_amdgcn_s_setprio(HP1);
    unsigned kf2=kfo;
    asm volatile(H_ASM : "+{v16}"(kf2), "+{s47}"(i11), "+{s52}"(rot), "+{s54}"(sbo), "+{s55}"(sst) HDBG_O
      : H_IN, "{v14}"(vc), "{v13}"(WS_GEN+256+(l&31)*4), "{s56}"(sg(3u<<(kvs-1) | 1u<<15)),
        "{s57}"(sg(((w&1)? (1u<<TGS | 3u<<PLS | 1u<<14 | (xs>1? 8u : 0u)) : 1u<<CPS)&~(unsigned)(c50>>32))), "{s[70:71]}"(ws), "{s[72:73]}"(pc) : H_CLOB);
    asm volatile("s_waitcnt lgkmcnt(0)" : "+s"(h0), "+s"(dvu) :: "memory");
    asm volatile("" : "+s"(bid), "+s"(w)); __builtin_assume(bid>=0 && bid<256 && w>=0 && w<4);
    { DERIVE(); const unsigned sw1=w*1024, sw2=w*2048;
    u32x4 qv[4];
    BAR(); TSB(14);
    __builtin_amdgcn_s_setprio(HP2);
    { const unsigned fa=AUX+w*CV_W+32;
      asm volatile("s_mov_b64 exec, -2\n\ts_add_u32 m0, %3, %4\n\ts_nop 0\n\tglobal_load_lds_dwordx4 %0, %1" LDPOL "\n\ts_mov_b32 m0, %3\n\ts_nop 0\n\tglobal_load_lds_dwordx4 %0, %2" LDPOL "\n\ts_mov_b64 exec, -1"
        :: "v"(l16), "s"(hrow), "s"(hrow-2), "s"(fa), "n"(CV_CP) : "memory", "scc");
      #pragma unroll
      for(int i=1;i<4;i++){ gldsn(hrow+i*512, l16, fa, CV_CP+i*1024); gldsn(hrow-2+i*512, l16, fa, i*1024); } }
    WAITV(8);
    vct[32+w]=1u;
    TSH(6);
    const unsigned tag=__builtin_amdgcn_readfirstlane(vct[26]); const bool kvok=__builtin_amdgcn_readfirstlane(vct[5])!=0;
    { const unsigned pl=__builtin_amdgcn_readfirstlane(vct[6]); cpar=(xcd>>2)^(pl>>13); kfo^=pl; if(w==0) ofo^=pl; }
    u32x4 hv;
    { const unsigned d0=(h0[0]&0xffff0000u)|(pack2(bflo(h0[0])+((w&1)? bfhi(dvu) : bflo(dvu)),0.f)&0xffffu);
      hv= l==4? u32x4{0,d0,h0[1],h0[2]} : l==5? u32x4{d0,h0[1],h0[2],h0[3]} : u32x4{0,0,0,0}; }
    asm volatile("s_mov_b64 exec, 63\n\tds_write_b128 %0, %1\n\ts_mov_b64 exec, %3\n\tds_write_b128 %2, %1 offset:7680\n\ts_mov_b64 exec, -1"
      :: "v"(hda), "v"(hv), "v"(zpa), "s"(0xffffffff00000000ull) : "memory");
#define QLD() asm volatile("global_load_dwordx4 %0, %4, %5" LDPOL "\n\tglobal_load_dwordx4 %1, %4, %6" LDPOL "\n\tglobal_load_dwordx4 %2, %4, %7" LDPOL "\n\tglobal_load_dwordx4 %3, %4, %8" LDPOL \
        : "=&v"(qv[0]), "=&v"(qv[1]), "=&v"(qv[2]), "=&v"(qv[3]) : "v"(gof), "s"(qin), "s"(qin+8*DM), "s"(qin+32*DM), "s"(qin+40*DM) : "memory")
    auto kvt=[&](){
      #pragma unroll
      for(int i=0;i<4;i++) gldsc(ws+loc(WS_KV+cb*32768)+(w>>1)*16384+(w&1)*4096+i*1024, l16, sw2, (i&1)*9216+(i>>1)*1024); };
    if(!kvok) wait_flag((const unsigned*)(ws+kfo), tag);
    kvt();
    TSH(7);
    auto wos=[&](int st){ if(NOWO && st>1) return; const u16* b=wo+(size_t)((kt>>(4*st))&15)*64*DM; glds(b,wv[0],sw1,WOB+st*8192); glds(b,wv[1],sw1,WOB+st*8192+4096); };
    wos(0); wos(1);
    while(!all4(36)) __builtin_amdgcn_s_sleep(1);
    #pragma unroll
    for(int st=2;st<NE;st++) wos(st);
    WAITV(2*NE); WAITL();
    vct[16+w]=1u;
    TSH(8);
    __builtin_amdgcn_s_setprio(HP4);
    if(GATE==1) while(!all4(28)) __builtin_amdgcn_s_sleep(1);
    if(GATE==2) while(vct[4]<4u) __builtin_amdgcn_s_sleep(1);
    TSF(24);
    QLD();
    #pragma unroll
    for(int st=NE;st<13;st++){ if(GATE==0 && st==7) while(!all4(0)) __builtin_amdgcn_s_sleep(1);
      WAITV(W1); TSF(24+st);
      if(st==EP+W1/2+1 || st==EP2+W1/2+1)
        asm volatile("s_cmp_lg_u32 %0, 0\n\ts_cbranch_scc1 1f\n\tv_cmp_ne_u32 vcc, %1, v253\n\tv_mov_b32 v253, %3\n\ts_nop 1\n\ts_and_b32 vcc_lo, vcc_lo, %2\n\ts_cbranch_scc1 1f\n\tv_mov_b32 v254, 1\n\tds_max_u32 v253, v254\n1:"
          :: "s"(w), "s"(tag), "s"(l1m), "n"(CT_OFF+96) : "memory", "scc", "vcc", "v253", "v254");
      if(st==EP || st==EP2) asm volatile("global_load_dword v253, %0, %1 sc1" :: "v"(ofo), "s"(ws) : "memory", "v253");
      wos(st); if(st==7+W1/2) vct[20+w]=7u; }
    unsigned op;
    asm volatile("global_load_dword %0, %1, %2 sc1" : "=v"(op) : "v"(ofo), "s"(ws) : "memory");
    WAITV(1); vct[20+w]=13u;
    TSH(9);
    asm volatile("s_waitcnt vmcnt(0)" : "+v"(qv[0]), "+v"(qv[1]), "+v"(qv[2]), "+v"(qv[3]), "+v"(op) :: "memory");
    unsigned oseen=0; bool ptch=false;
    auto olevel=[&](unsigned v){
      const unsigned ne=(unsigned)__ballot(v!=tag), lv= ne==0 ? 2u : (ne&l1m)==0 ? 1u : 0u;
      if(lv>oseen){ if(oseen==0){ TSH(10); } oseen=lv; if(l==0) __hip_atomic_fetch_max(ct+24, lv, __ATOMIC_RELAXED, __HIP_MEMORY_SCOPE_WORKGROUP); }
      if(PTCH && !ptch && !((ne>>(hp^8))&1)){ ptch=true; const char* b=ws+loc((tm*16+(hp^8))*32768);
        asm volatile("global_load_dword v255, %0, %1 sc1\n\tglobal_load_dword v255, %0, %2 sc1" :: "v"(l16*8), "s"(b), "s"(b+16384) : "memory", "v255"); } };
    auto opoll=[&](){ olevel(__hip_atomic_load((const unsigned*)(ws+ofo), __ATOMIC_RELAXED, __HIP_MEMORY_SCOPE_AGENT)); };
    if(w==0) olevel(op);
    float qf[32];
    auto qun=[&](){
    #pragma unroll
    for(int i=0;i<16;i++){ qf[2*i]=bflo(qv[i>>2][i&3]); qf[2*i+1]=bfhi(qv[i>>2][i&3]); }
    #pragma unroll
    for(int i=0;i<32;i+=8) asm volatile("" : "+v"(qf[i]), "+v"(qf[i+1]), "+v"(qf[i+2]), "+v"(qf[i+3]), "+v"(qf[i+4]), "+v"(qf[i+5]), "+v"(qf[i+6]), "+v"(qf[i+7]));
    };
    qun();
    auto wol=[&](int i){ if(!NOWO) glds(wo+(size_t)((kt>>(52+4*(i>>1)))&15)*64*DM, wv[i&1], sw1, i<4? AUX+4352+i*CV_W : (i&1)*4096); };
    while(!all4(44)){ if(w==0 && oseen<1) opoll(); else __builtin_amdgcn_s_sleep(1); }
    wol(0); wol(1); wol(2); wol(3);
    while(!all4(48)){ if(w==0 && oseen<1) opoll(); else __builtin_amdgcn_s_sleep(1); }
    wol(4); wol(5);
    __builtin_amdgcn_s_setprio(HP3);
    bool gen= !(bid==0 && w==1);
    auto trygen=[&](){
      unsigned v[4];
      #pragma unroll
      for(int i=0;i<4;i++) v[i]=__hip_atomic_load((const unsigned*)(ws+(loc(flo(0,i*64))^((i>>1)<<13))+(l>>5)*4096+(l&31)*4), __ATOMIC_RELAXED, __HIP_MEMORY_SCOPE_AGENT);
      if(__all(v[0]==tag && v[1]==tag && v[2]==tag && v[3]==tag)){ if(l==0) st4c(ws+WS_GEN, tag); gen=true; } };
    if(!gen) trygen();
    while(!((all4(8)>>w)&1)){
      if(w==0 && oseen<2) opoll();
      __builtin_amdgcn_s_sleep(OPOLLS);
    }
    TSH(11);
    __builtin_amdgcn_s_setprio(HP5);
    {
      const char* yb=smem+yra;
      s16x4 ya[4], yc[4];
      #pragma unroll
      for(int i=0;i<4;i++){ ya[i]=trread(yb+(i>>1)*512+(i&1)*64); yc[i]=trread(yb+(i>>1)*512+(i&1)*64+256); }
      #pragma unroll
      for(int i=0;i<4;i++) if(NOY!=1) st16c(ws+loc(WS_Y+(cb&~15)*32768)+(w*4+i)*32768+((cb>>3)&1)*16384+(cb&7)*1024, l16, __builtin_bit_cast(u32x4,__builtin_shufflevector(ya[i],yc[i],0,1,2,3,4,5,6,7)));
    }
    TSH(12);
    if(NOY){ WAITV(0); vct[20+w]=16u; for(int sp_=0; !gen && sp_<(1<<22); sp_++) trygen(); TS(20); TSDUMP(); return; }
    unsigned po=gof+(gof&~2047u);
    const char* p0=ws+loc(WS_Y+gi*32768);
    unsigned y0=g*1024+w*256+((fr^(g&2))*16);
    asm volatile("" : "+v"(y0), "+v"(po));
    WAITV(0);
    vct[20+w]=16u;
    if(l<4) st4c(ws+loc(flo(2,cb&~15))+(w*4+l)*128+(cb&15)*4, tag);
    TSH(13);
    if(w==0){ int sp_=0; while(oseen<2 && vct[24]<2u && ++sp_<(1<<22)){ opoll(); __builtin_amdgcn_s_sleep(8); } }
    if(w==0){
      const unsigned* fp=(const unsigned*)(ws+loc(flo(2,gi)))+(l&15); int spins=0;
      while(vct[27]==0u) __builtin_amdgcn_s_sleep(2);
      while(true){
        unsigned v=__hip_atomic_load(fp, __ATOMIC_RELAXED, __HIP_MEMORY_SCOPE_AGENT);
        if(__all(v==tag) || ++spins>(1<<20)) break;
        __builtin_amdgcn_s_sleep(POLLS);
      }
      { u32x4 v; do{ v=*(volatile LDS_AS u32x4*)(ct+20); }while(__builtin_amdgcn_readfirstlane(v[0]&v[1]&v[2]&v[3]&16u)==0); }
      vct[25]=1u;
    } else { while(vct[25]==0u) __builtin_amdgcn_s_sleep(2); }
    TSH(14);
    {
      u32x4 yv[4];
      #pragma unroll
      for(int i=0;i<4;i++) gldsc(p0+(i>>1)*16384+(i&1)*4096, y0^(i*64), w*CV_W, AUX+i*1024);
      const char* ys=smem+AUX+w*CV_W+(l&7)*512;
      const int yi=((l>>3)^((l&7)*2))*16;
      WAITV(0);
      yv[0]=*(const u32x4*)(ys+yi); yv[1]=*(const u32x4*)(ys+yi+256); yv[2]=*(const u32x4*)(ys+(yi^128)); yv[3]=*(const u32x4*)(ys+(yi^128)+256);
      TSH(15);
      #pragma unroll
      for(int r=0;r<4;r++){
        u32x4 ov;
        #pragma unroll
        for(int e=0;e<4;e++){ const unsigned y2=yv[(r>>1)*2+(e>>1)][(r&1)*2+(e&1)]; ov[e]=pack2(qf[r*8+2*e]*bflo(y2), qf[r*8+2*e+1]*bfhi(y2)); }
        stos(out+((r&1)*8+(r>>1)*32)*2048, po, ov);
      }
    }
    for(int sp_=0; !gen && sp_<(1<<22); sp_++) trygen();
    TS(20); TSDUMP();
    }
  }

}

extern "C" void kernel_launch(void* const* d_in, const int* in_sizes, int n_in, void* d_out, int out_size, void* d_ws, size_t ws_size, hipStream_t stream){
  const u16* q=(const u16*)d_in[0]; const u16* k=(const u16*)d_in[1]; const u16* v=(const u16*)d_in[2];
  const u16* shk=(const u16*)d_in[3]; const u16* hk=(const u16*)d_in[4]; const u16* sD=(const u16*)d_in[5];
  const u16* x=(const u16*)d_in[6]; const u16* wqkv=(const u16*)d_in[7]; const u16* wo=(const u16*)d_in[8];
  u16* ws=(u16*)(((size_t)d_ws+16383)&~(size_t)16383);
  fused_kernel<<<256,512,0,stream>>>(x,wqkv,k,v,shk,hk,wo,q,sD,(u16*)d_out,ws,ws);
}
```

```cpp
#include <hip/hip_runtime.h>
#include <hip/hip_bf16.h>
#include <stdint.h>

typedef unsigned short u16;
typedef short s16x4 __attribute__((ext_vector_type(4)));
typedef float f32x32 __attribute__((ext_vector_type(32)));
typedef unsigned u32x4 __attribute__((ext_vector_type(4)));
typedef unsigned u32x2 __attribute__((ext_vector_type(2)));
#define LDS_AS __attribute__((address_space(3)))

__device__ __forceinline__ float bflo(unsigned x){ return __uint_as_float(x<<16); }
__device__ __forceinline__ float bfhi(unsigned x){ return __uint_as_float(x&0xffff0000u); }
typedef float f32x2 __attribute__((ext_vector_type(2)));
typedef __bf16 bf16x2_t __attribute__((ext_vector_type(2)));
__device__ __forceinline__ unsigned pack2(float lo, float hi){ return __builtin_bit_cast(unsigned, __builtin_convertvector(f32x2{lo,hi}, bf16x2_t)); }

#define GLDS(name,pol) __device__ __forceinline__ void name(const void* sb, unsigned voff, unsigned ldsb, int imm){ \
  asm volatile("s_add_u32 m0, %2, %3\n\ts_nop 0\n\tglobal_load_lds_dwordx4 %0, %1" pol :: "v"(voff), "s"(sb), "s"(ldsb), "n"(imm) : "memory", "scc"); }
#define LDPOL " nt"
GLDS(glds,"") GLDS(gldsn,LDPOL) GLDS(gldsc," sc1")
__device__ __forceinline__ void touch(const void* sb, unsigned voff, unsigned long long mask){
  asm volatile("s_mov_b64 exec, %2\n\tglobal_load_dword v255, %0, %1\n\ts_mov_b64 exec, -1" :: "v"(voff), "s"(sb), "s"(mask) : "memory", "v255"); }
__device__ __forceinline__ void st16c(void* sb, unsigned vo, u32x4 v){ asm volatile("global_store_dwordx4 %0, %1, %2 sc1\n\ts_nop 1" :: "v"(vo), "v"(v), "s"(sb) : "memory"); }
#define OUTPOL " sc0 sc1"
__device__ __forceinline__ void stos(void* sb, unsigned vo, u32x4 v){ asm volatile("global_store_dwordx4 %0, %1, %2" OUTPOL "\n\ts_nop 1" :: "v"(vo), "v"(v), "s"(sb) : "memory"); }
__device__ __forceinline__ void st4c(void* p, unsigned v){ asm volatile("global_store_dword %0, %1, off sc1" :: "v"(p), "v"(v) : "memory"); }
__device__ __forceinline__ unsigned ld4c(const void* sb, unsigned vo){ unsigned v; asm volatile("global_load_dword %0, %1, %2 sc1\n\ts_waitcnt vmcnt(0)" : "=v"(v) : "v"(vo), "s"(sb) : "memory"); return v; }
__device__ __forceinline__ void wait_flag(const void* sb, unsigned vo, unsigned tag){
  int spins=0;
  while(true){
    unsigned v=ld4c(sb,vo);
    if(__all(v==tag)) break;
    __builtin_amdgcn_s_sleep(2);
    if(++spins>(1<<22)) break;
  }
}
#define WAITV(n) asm volatile("s_waitcnt vmcnt(%0)"::"n"(n):"memory")
#define WAITL() asm volatile("s_waitcnt lgkmcnt(0)":::"memory")
#define LANES() int l; asm volatile("v_mbcnt_lo_u32_b32 %0, -1, 0\n\tv_mbcnt_hi_u32_b32 %0, -1, %0" : "=v"(l)); __builtin_assume(l>=0 && l<64); const int g=l>>4, fr=l&15, tq=(l&15)>>2, tp=l&3; (void)g; (void)fr; (void)tq; (void)tp;

__device__ __forceinline__ s16x4 trread(const char* p){ return __builtin_amdgcn_ds_read_tr16_b64_v4i16((LDS_AS s16x4*)p); }
__device__ __forceinline__ int wswz(int row){ return ((row>>1)&1) | (((row>>3)&1)<<1); }

constexpr int S=2048, DM=1024;
constexpr int K1_XB=144*128, K1_WB=3*64*128, K1_ST=K1_XB+K1_WB;
constexpr int AUX=3*K1_ST, KVP_=1040;
constexpr int WOB=22528, CV_W=8448, CV_CP=4224;
constexpr int CT_OFF=AUX+33*KVP_;
constexpr int ALL_LDS=CT_OFF+208+96;
constexpr unsigned WS_KV=8u<<20, WS_Y=16u<<20, WS_FL=24u<<20, WS_GEN=WS_FL+(1u<<20);
__device__ __forceinline__ unsigned flo(int kind, int c){ return WS_FL+kind*65536+(c>>6)*16384+(c&63)*128; }

#define CPRIO 2
#define CODE_ST 1600
#define LEAD 3
#define HP3 3
#define HP2 3
#define HP4 3
#define TNX 3
#define HP1 0
#define NE 3
#define HP5 3
#define KX 1
#define SPN 4
#define W1 2
#define OPOLLS 1
#define POLLS 3
#define KR ((rbb+2*ra)%7)
#define KVSEL (rbb==3? 0 : idx%KSN)
#define KS0 1
#define KSN 3
#define KVP 5
#define TGS 2
#define CPS 13
#define PLS 4
#define H0_ASM \
  "s_mul_i32 s76, s52, 0x60000\n\t" \
  "s_add_u32 s76, s42, s76\n\t" \
  "s_addc_u32 s77, s43, 0\n\t" \
  "s_add_u32 s82, s53, 86016\n\t" \
  "s_add_u32 m0, s82, 18432\n\t" \
  "s_lshl_b32 s78, s52, 7\n\t" \
  "global_load_lds_dwordx4 v0, s[76:77]\n\t" \
  "global_load_lds_dwordx4 v1, s[76:77] offset:1024\n\t" \
  "s_add_u32 s78, s40, s78\n\t" \
  "s_addc_u32 s79, s41, 0\n\t" \
  "s_add_u32 m0, s82, 2048\n\t" \
  "s_nop 0\n\t" \
  "global_load_lds_dwordx4 v6, s[78:79]\n\t" \
  "global_load_lds_dwordx4 v7, s[78:79] offset:1024\n\t" \
  "s_add_u32 m0, s82, 26624\n\t" \
  "s_nop 0\n\t" \
  "global_load_lds_dwordx4 v2, s[76:77]\n\t" \
  "global_load_lds_dwordx4 v3, s[76:77] offset:1024\n\t" \
  "s_add_u32 m0, s82, 10240\n\t" \
  "s_nop 0\n\t" \
  "global_load_lds_dwordx4 v8, s[78:79]\n\t" \
  "global_load_lds_dwordx4 v9, s[78:79] offset:1024\n\t" \
  "s_add_u32 m0, s82, 34816\n\t" \
  "s_nop 0\n\t" \
  "global_load_lds_dwordx4 v4, s[76:77]\n\t" \
  "global_load_lds_dwordx4 v5, s[76:77] offset:1024\n\t" \
  "s_cmp_gt_u32 s59, 1\n\t" \
  "s_cbranch_scc1 1f\n\t" \
  "s_lshr_b32 s83, s53, 1\n\t" \
  "s_add_u32 m0, s83, 86016\n\t" \
  "s_nop 0\n\t" \
  "global_load_lds_dwordx4 v10, s[78:79]\n\t" \
  "1:\n\t"
#define H1_ASM \
  "s_mov_b32 s55, 0\n\t" \
  "s_mov_b32 s54, 0\n\t" \
  "s_add_u32 s52, s52, 1\n\t" \
  "s_and_b32 s52, s52, 15\n\t" \
  "s_cmp_eq_u32 s58, 1\n\t" \
  "s_cbranch_scc1 1f\n\t" \
  "s_mul_i32 s76, s52, 0x60000\n\t" \
  "s_add_u32 s76, s42, s76\n\t" \
  "s_addc_u32 s77, s43, 0\n\t" \
  "s_lshl_b32 s78, s52, 7\n\t" \
  "s_add_u32 s78, s40, s78\n\t" \
  "s_addc_u32 s79, s41, 0\n\t" \
  "s_mul_i32 s80, s47, s46\n\t" \
  "s_add_u32 s80, s44, s80\n\t" \
  "s_addc_u32 s81, s45, 0\n\t" \
  "s_mov_b32 s82, s53\n\t" \
  "s_add_u32 m0, s82, 18432\n\t" \
  "s_add_u32 s52, s52, 1\n\t" \
  "global_load_lds_dwordx4 v0, s[76:77]\n\t" \
  "global_load_lds_dwordx4 v1, s[76:77] offset:1024\n\t" \
  "s_add_u32 m0, s82, 2048\n\t" \
  "s_and_b32 s52, s52, 15\n\t" \
  "global_load_lds_dwordx4 v6, s[78:79]\n\t" \
  "global_load_lds_dwordx4 v7, s[78:79] offset:1024\n\t" \
  "s_add_u32 m0, s82, 26624\n\t" \
  "s_add_u32 s47, s47, 1\n\t" \
  "global_load_lds_dwordx4 v2, s[76:77]\n\t" \
  "global_load_lds_dwordx4 v3, s[76:77] offset:1024\n\t" \
  "s_add_u32 m0, s82, 10240\n\t" \
  "s_and_b32 s47, s47, 15\n\t" \
  "global_load_lds_dwordx4 v8, s[78:79]\n\t" \
  "global_load_lds_dwordx4 v9, s[78:79] offset:1024\n\t" \
  "s_add_u32 m0, s82, 34816\n\t" \
  "s_and_b32 s83, s54, s51\n\t" \
  "global_load_lds_dwordx4 v4, s[76:77]\n\t" \
  "global_load_lds_dwordx4 v5, s[76:77] offset:1024\n\t" \
  "s_add_u32 m0, s83, s49\n\t" \
  "s_mov_b64 exec, s[50:51]\n\t" \
  "global_load_lds_dwordx4 v10, s[80:81]\n\t" \
  "s_mov_b64 exec, -1\n\t" \
  "s_mov_b32 s55, 1\n\t" \
  "s_mov_b32 s54, 43008\n\t" \
  "s_waitcnt vmcnt(11)\n\t" \
  "s_branch 2f\n\t" \
  "1:\n\t" \
  "s_mul_i32 s83, s59, 4160\n\t" \
  "v_add_u32 v20, 7152, v11\n\t" \
  "v_add_u32 v21, 14304, v11\n\t" \
  "v_add_u32 v22, 21456, v11\n\t" \
  "s_add_u32 m0, s83, 130064\n\t" \
  "s_nop 0\n\t" \
  "global_load_lds_dwordx4 v11, s[60:61]" LDPOL "\n\t" \
  "global_load_lds_dwordx4 v20, s[60:61] offset:1040" LDPOL "\n\t" \
  "global_load_lds_dwordx4 v21, s[60:61] offset:2080" LDPOL "\n\t" \
  "global_load_lds_dwordx4 v22, s[60:61] offset:3120" LDPOL "\n\t" \
  "s_add_u32 m0, s83, 146704\n\t" \
  "s_nop 0\n\t" \
  "global_load_lds_dwordx4 v11, s[62:63]" LDPOL "\n\t" \
  "global_load_lds_dwordx4 v20, s[62:63] offset:1040" LDPOL "\n\t" \
  "global_load_lds_dwordx4 v21, s[62:63] offset:2080" LDPOL "\n\t" \
  "global_load_lds_dwordx4 v22, s[62:63] offset:3120" LDPOL "\n\t" \
  "v_subrev_u32 v20, s64, v11\n\t" \
  "s_mov_b32 m0, s68\n\t" \
  "s_mov_b64 exec, s[66:67]\n\t" \
  "global_load_lds_dwordx4 v20, s[60:61]" LDPOL "\n\t" \
  "s_mov_b64 exec, -1\n\t" \
  "s_waitcnt vmcnt(9)\n\t" \
  "2:\n\t" \
  "s_waitcnt lgkmcnt(0)\n\t" \
  "s_barrier\n\t"
#define H_ASM \
  "s_mul_i32 s76, s52, 0x60000\n\t" \
  "s_add_u32 s76, s42, s76\n\t" \
  "s_addc_u32 s77, s43, 0\n\t" \
  "s_lshl_b32 s78, s52, 7\n\t" \
  "s_add_u32 s78, s40, s78\n\t" \
  "s_addc_u32 s79, s41, 0\n\t" \
  "s_mul_i32 s80, s47, s46\n\t" \
  "s_add_u32 s80, s44, s80\n\t" \
  "s_addc_u32 s81, s45, 0\n\t" \
  "s_add_u32 s82, s54, s53\n\t" \
  "s_cmp_eq_u32 s58, 2\n\t" \
  "s_cbranch_scc1 5f\n\t" \
  "s_branch 3f\n\t" \
  "1:\n\t" \
  "s_waitcnt vmcnt(11) lgkmcnt(0)\n\t" \
  "2:\n\t" \
  "s_barrier\n\t" \
  "3:\n\t" \
  "s_add_u32 s55, s55, 1\n\t" \
  "s_add_u32 m0, s82, 18432\n\t" \
  "s_add_u32 s52, s52, 1\n\t" \
  "global_load_lds_dwordx4 v0, s[76:77]\n\t" \
  "global_load_lds_dwordx4 v1, s[76:77] offset:1024\n\t" \
  "s_add_u32 m0, s82, 2048\n\t" \
  "s_and_b32 s52, s52, 15\n\t" \
  "global_load_lds_dwordx4 v6, s[78:79]\n\t" \
  "global_load_lds_dwordx4 v7, s[78:79] offset:1024\n\t" \
  "s_add_u32 m0, s82, 26624\n\t" \
  "s_add_u32 s47, s47, 1\n\t" \
  "global_load_lds_dwordx4 v2, s[76:77]\n\t" \
  "global_load_lds_dwordx4 v3, s[76:77] offset:1024\n\t" \
  "s_add_u32 m0, s82, 10240\n\t" \
  "s_and_b32 s47, s47, 15\n\t" \
  "global_load_lds_dwordx4 v8, s[78:79]\n\t" \
  "global_load_lds_dwordx4 v9, s[78:79] offset:1024\n\t" \
  "s_add_u32 m0, s82, 34816\n\t" \
  "s_and_b32 s83, s54, s51\n\t" \
  "global_load_lds_dwordx4 v4, s[76:77]\n\t" \
  "global_load_lds_dwordx4 v5, s[76:77] offset:1024\n\t" \
  "s_bitcmp1_b32 s57, s55\n\t" \
  "s_cbranch_scc1 10f\n\t" \
  "8:\n\t" \
  "s_add_u32 m0, s83, s49\n\t" \
  "s_mov_b64 exec, s[50:51]\n\t" \
  "global_load_lds_dwordx4 v10, s[80:81]\n\t" \
  "s_mov_b64 exec, -1\n\t" \
  "4:\n\t" \
  "s_mul_i32 s76, s52, 0x60000\n\t" \
  "s_add_u32 s76, s42, s76\n\t" \
  "s_addc_u32 s77, s43, 0\n\t" \
  "s_lshl_b32 s78, s52, 7\n\t" \
  "s_add_u32 s78, s40, s78\n\t" \
  "s_addc_u32 s79, s41, 0\n\t" \
  "s_mul_i32 s80, s47, s46\n\t" \
  "s_add_u32 s80, s44, s80\n\t" \
  "s_addc_u32 s81, s45, 0\n\t" \
  "s_add_u32 s54, s54, 43008\n\t" \
  "s_cmp_eq_u32 s54, 129024\n\t" \
  "s_cselect_b32 s54, 0, s54\n\t" \
  "s_add_u32 s82, s54, s53\n\t" \
  "s_bitcmp0_b32 s56, s55\n\t" \
  "s_cbranch_scc1 1b\n\t" \
  "s_cmp_eq_u32 s55, 15\n\t" \
  "s_cbranch_scc1 19f\n\t" \
  "s_cmp_eq_u32 s55, s58\n\t" \
  "s_cbranch_scc1 3b\n\t" \
  "s_waitcnt vmcnt(11)\n\t" \
  "s_barrier\n\t" \
  "5:\n\t" \
  "s_mul_i32 s83, s59, 4160\n\t" \
  "v_add_u32 v20, 7152, v11\n\t" \
  "v_add_u32 v21, 14304, v11\n\t" \
  "v_add_u32 v22, 21456, v11\n\t" \
  "s_add_u32 m0, s83, 130064\n\t" \
  "s_nop 0\n\t" \
  "global_load_lds_dwordx4 v11, s[60:61]" LDPOL "\n\t" \
  "global_load_lds_dwordx4 v20, s[60:61] offset:1040" LDPOL "\n\t" \
  "global_load_lds_dwordx4 v21, s[60:61] offset:2080" LDPOL "\n\t" \
  "global_load_lds_dwordx4 v22, s[60:61] offset:3120" LDPOL "\n\t" \
  "s_add_u32 m0, s83, 146704\n\t" \
  "s_nop 0\n\t" \
  "global_load_lds_dwordx4 v11, s[62:63]" LDPOL "\n\t" \
  "global_load_lds_dwordx4 v20, s[62:63] offset:1040" LDPOL "\n\t" \
  "global_load_lds_dwordx4 v21, s[62:63] offset:2080" LDPOL "\n\t" \
  "global_load_lds_dwordx4 v22, s[62:63] offset:3120" LDPOL "\n\t" \
  "v_subrev_u32 v20, s64, v11\n\t" \
  "s_mov_b32 m0, s68\n\t" \
  "s_mov_b64 exec, s[66:67]\n\t" \
  "global_load_lds_dwordx4 v20, s[60:61]" LDPOL "\n\t" \
  "s_mov_b64 exec, -1\n\t" \
  "s_waitcnt vmcnt(9)\n\t" \
  "s_branch 2b\n\t" \
  "10:\n\t" \
  "s_cmp_eq_u32 s59, 2\n\t" \
  "s_cbranch_scc0 11f\n\t" \
  "s_mov_b64 exec, 0xffff\n\t" \
  "global_load_dword v255, v14, s[72:73]\n\t" \
  "s_mov_b64 exec, -1\n\t" \
  "s_branch 4b\n\t" \
  "11:\n\t" \
  "s_cmp_eq_u32 s55, 2\n\t" \
  "s_cbranch_scc0 12f\n\t" \
  "global_load_dword v254, v14, s[70:71] sc1\n\t" \
  "s_branch 4b\n\t" \
  "12:\n\t" \
  "s_cmp_eq_u32 s55, 4\n\t" \
  "s_cbranch_scc0 15f\n\t" \
  "global_load_dword v253, v13, s[70:71] sc1\n\t" \
  "s_branch 4b\n\t" \
  "15:\n\t" \
  "s_cmp_eq_u32 s55, 5\n\t" \
  "s_cbranch_scc0 13f\n\t" \
  "s_setprio 3\n\t" \
  "v_readfirstlane_b32 s84, v254\n\t" \
  "s_mov_b32 m0, 0x400000\n\t" \
  "s_waitcnt vmcnt(10)\n\t" \
  "s_add_u32 s84, s84, 1\n\t" \
  "v_mov_b32 v254, s84\n\t" \
  "16:\n\t" \
  "v_cmp_eq_u32 vcc, v253, v254\n\t" \
  "s_nop 1\n\t" \
  "s_and_b32 s84, vcc_lo, 0x3fffffff\n\t" \
  "s_bcnt1_i32_b32 s85, s84\n\t" \
  "s_cmp_ge_u32 s85, 15\n\t" \
  "s_cbranch_scc1 17f\n\t" \
  "s_sub_u32 m0, m0, 1\n\t" \
  "s_cbranch_scc1 17f\n\t" \
  "global_load_dword v253, v13, s[70:71] sc1\n\t" \
  "s_waitcnt vmcnt(0)\n\t" \
  "s_branch 16b\n\t" \
  "17:\n\t" \
  "s_and_b32 s84, s84, 0xaaaaaaaa\n\t" \
  "s_bcnt1_i32_b32 s84, s84\n\t" \
  "s_and_b32 s84, s84, 8\n\t" \
  "s_lshl_b32 s84, s84, 10\n\t" \
  "v_mov_b32 v20, s84\n\t" \
  "v_mov_b32 v253, 163344\n\t" \
  "ds_write_b32 v253, v254 offset:104\n\t" \
  "ds_write_b32 v253, v20 offset:24\n\t" \
  "s_setprio 0\n\t" \
  "s_branch 8b\n\t" \
  "13:\n\t" \
  "s_mov_b32 exec_hi, 0\n\t" \
  "global_load_dword v255, v10, s[44:45]\n\t" \
  "s_mov_b64 exec, -1\n\t" \
  "s_branch 4b\n\t" \
  "19:\n\t" \
  "s_waitcnt vmcnt(11) lgkmcnt(0)\n\t" \
  "s_barrier\n\t"
#define C_ASM \
  "s_branch 29f\n\t" \
  "21:\n\t" \
  "s_mov_b64 s[68:69], %27\n\t" \
  "s_and_b32 s74, s68, 15\n\t" \
  "s_andn2_b32 s68, s68, 15\n\t" \
  "s_load_dword vcc_lo, s[68:69], 0 glc\n\t" \
  "s_and_b32 s75, s74, 1\n\t" \
  "s_lshl_b32 s70, s74, 9\n\t" \
  "s_lshl_b32 s71, s75, 13\n\t" \
  "s_or_b32 s70, s70, s71\n\t" \
  "s_waitcnt lgkmcnt(0)\n\t" \
  "s_or_b32 s68, s68, s70\n\t" \
  "s_add_u32 vcc_lo, vcc_lo, 1\n\t" \
  "v_mov_b32 v18, vcc_lo\n\t" \
  "s_memtime s[70:71]\n\t" \
  "s_waitcnt lgkmcnt(0)\n\t" \
  "s_load_dword s76, s[68:69], 16384 glc\n\t" \
  "s_waitcnt lgkmcnt(0)\n\t" \
  "s_xor_b32 s68, s68, 0x2000\n\t" \
  "s_memtime s[90:91]\n\t" \
  "s_waitcnt lgkmcnt(0)\n\t" \
  "s_load_dword s76, s[68:69], 16384 glc\n\t" \
  "s_waitcnt lgkmcnt(0)\n\t" \
  "s_sub_u32 vcc_lo, s90, s70\n\t" \
  "s_memtime s[70:71]\n\t" \
  "s_waitcnt lgkmcnt(0)\n\t" \
  "s_sub_u32 s70, s70, s90\n\t" \
  "s_cmp_gt_u32 vcc_lo, s70\n\t" \
  "s_cselect_b32 s70, 1, 0\n\t" \
  "s_xor_b32 s70, s70, s75\n\t" \
  "s_lshl1_add_u32 s70, s74, s70\n\t" \
  "s_lshl_b32 s70, s70, 2\n\t" \
  "v_mov_b32 v19, s70\n\t" \
  "s_and_b32 s68, s68, 0xffffc000\n\t" \
  "s_mov_b64 exec, 1\n\t" \
  "global_store_dword v19, v18, s[68:69] offset:256 sc1\n\t" \
  "s_branch 26f\n\t" \
  "28:\n\t" \
  "s_cmp_lg_u64 %27, 0\n\t" \
  "s_cbranch_scc1 21b\n\t" \
  "s_mov_b64 exec, 0\n\t" \
  "s_cmp_eq_u32 %13, 0\n\t" \
  "s_cbranch_scc1 500f\n\t" \
  "s_cmp_eq_u32 %13, 1\n\t" \
  "s_cbranch_scc1 501f\n\t" \
  "s_cmp_eq_u32 %13, 2\n\t" \
  "s_cbranch_scc1 502f\n\t" \
  "s_branch 503f\n\t" \
  "26:\n\t" \
  "s_mov_b64 exec, -1\n\t" \
  "s_add_u32 s88, s88, 4\n\t" \
  "s_addc_u32 s89, s89, 0\n\t" \
  "s_setpc_b64 s[88:89]\n\t" \
  ".p2align 12\n\t" \
  "29:\n\t" \
  "s_bitcmp0_b32 %13, 0\n\t" \
  "s_cbranch_scc1 8f\n\t" \
  "global_load_dwordx2 v[10:11], v51, %11\n\t" \
  "global_load_dwordx2 v[12:13], v51, %11 offset:128\n\t" \
  "v_xor_b32 v53, 64, v52\n\t" \
  "v_xor_b32 v55, 32, v54\n\t" \
  "v_add_u32 v56, 43008, v52\n\t" \
  "v_add_u32 v58, 43008, v54\n\t" \
  "v_xor_b32 v57, 64, v56\n\t" \
  "v_xor_b32 v59, 32, v58\n\t" \
  "v_add_u32 v60, 86016, v52\n\t" \
  "v_add_u32 v62, 86016, v54\n\t" \
  "v_xor_b32 v61, 64, v60\n\t" \
  "v_xor_b32 v63, 32, v62\n\t" \
  "s_mov_b32 vcc_hi, 0x05040100\n\t" \
  "s_waitcnt vmcnt(2)\n\t" \
  "s_barrier\n\t" \
  "ds_read_b64_tr_b16 v[64:65], v62 offset:26624\n\t" \
  "ds_read_b64_tr_b16 v[66:67], v62 offset:27136\n\t" \
  "ds_read_b128 v[68:71], v60 offset:10240\n\t" \
  "ds_read_b64_tr_b16 v[72:73], v62 offset:30720\n\t" \
  "ds_read_b64_tr_b16 v[74:75], v62 offset:31232\n\t" \
  "ds_read_b128 v[76:79], v61 offset:10240\n\t" \
  "ds_read_b128 v[80:83], v61 offset:12288\n\t" \
  "ds_read_b128 v[84:87], v60 offset:12288\n\t" \
  "ds_read_b128 v[88:91], v60 offset:14336\n\t" \
  "ds_read_b128 v[92:95], v61 offset:14336\n\t" \
  "ds_read_b128 v[96:99], v61 offset:16384\n\t" \
  "ds_read_b128 v[100:103], v60 offset:16384\n\t" \
  "ds_read_b64_tr_b16 v[104:105], v63 offset:26624\n\t" \
  "ds_read_b64_tr_b16 v[106:107], v63 offset:27136\n\t" \
  "ds_read_b64_tr_b16 v[108:109], v63 offset:30720\n\t" \
  "ds_read_b64_tr_b16 v[110:111], v63 offset:31232\n\t" \
  "s_waitcnt lgkmcnt(13)\n\t" \
  "v_mfma_f32_16x16x32_bf16 v[184:187], v[64:67], v[68:71], 0\n\t" \
  "ds_read_b64_tr_b16 v[112:113], v62 offset:34816\n\t" \
  "ds_read_b64_tr_b16 v[114:115], v62 offset:35328\n\t" \
  "s_waitcnt lgkmcnt(12)\n\t" \
  "v_mfma_f32_16x16x32_bf16 v[184:187], v[72:75], v[76:79], v[184:187]\n\t" \
  "ds_read_b64_tr_b16 v[116:117], v62 offset:38912\n\t" \
  "ds_read_b64_tr_b16 v[118:119], v62 offset:39424\n\t" \
  "s_waitcnt lgkmcnt(13)\n\t" \
  "v_mfma_f32_16x16x32_bf16 v[192:195], v[72:75], v[80:83], 0\n\t" \
  "ds_read_b128 v[120:123], v60 offset:0\n\t" \
  "s_waitcnt lgkmcnt(13)\n\t" \
  "v_mfma_f32_16x16x32_bf16 v[192:195], v[64:67], v[84:87], v[192:195]\n\t" \
  "ds_read_b128 v[124:127], v61 offset:0\n\t" \
  "s_waitcnt lgkmcnt(13)\n\t" \
  "v_mfma_f32_16x16x32_bf16 v[200:203], v[64:67], v[88:91], 0\n\t" \
  "ds_read_b64_tr_b16 v[128:129], v63 offset:34816\n\t" \
  "s_waitcnt lgkmcnt(13)\n\t" \
  "v_mfma_f32_16x16x32_bf16 v[200:203], v[72:75], v[92:95], v[200:203]\n\t" \
  "ds_read_b64_tr_b16 v[130:131], v63 offset:35328\n\t" \
  "s_waitcnt lgkmcnt(13)\n\t" \
  "v_mfma_f32_16x16x32_bf16 v[208:211], v[72:75], v[96:99], 0\n\t" \
  "ds_read_b64_tr_b16 v[132:133], v63 offset:38912\n\t" \
  "s_waitcnt lgkmcnt(9)\n\t" \
  "v_mfma_f32_16x16x32_bf16 v[208:211], v[64:67], v[100:103], v[208:211]\n\t" \
  "ds_read_b64_tr_b16 v[134:135], v63 offset:39424\n\t" \
  "v_mfma_f32_16x16x32_bf16 v[188:191], v[104:107], v[68:71], 0\n\t" \
  "ds_read_b64_tr_b16 v[136:137], v62 offset:18432\n\t" \
  "v_mfma_f32_16x16x32_bf16 v[188:191], v[108:111], v[76:79], v[188:191]\n\t" \
  "ds_read_b64_tr_b16 v[138:139], v62 offset:18944\n\t" \
  "v_mfma_f32_16x16x32_bf16 v[196:199], v[108:111], v[80:83], 0\n\t" \
  "ds_read_b64_tr_b16 v[140:141], v62 offset:22528\n\t" \
  "v_mfma_f32_16x16x32_bf16 v[196:199], v[104:107], v[84:87], v[196:199]\n\t" \
  "ds_read_b64_tr_b16 v[142:143], v62 offset:23040\n\t" \
  "v_mfma_f32_16x16x32_bf16 v[204:207], v[104:107], v[88:91], 0\n\t" \
  "ds_read_b64_tr_b16 v[144:145], v63 offset:18432\n\t" \
  "v_mfma_f32_16x16x32_bf16 v[204:207], v[108:111], v[92:95], v[204:207]\n\t" \
  "ds_read_b64_tr_b16 v[146:147], v63 offset:18944\n\t" \
  "v_mfma_f32_16x16x32_bf16 v[212:215], v[108:111], v[96:99], 0\n\t" \
  "ds_read_b64_tr_b16 v[148:149], v63 offset:22528\n\t" \
  "v_mfma_f32_16x16x32_bf16 v[212:215], v[104:107], v[100:103], v[212:215]\n\t" \
  "ds_read_b64_tr_b16 v[150:151], v63 offset:23040\n\t" \
  "s_waitcnt lgkmcnt(7)\n\t" \
  "v_mfma_f32_16x16x32_bf16 v[216:219], v[68:71], v[112:115], 0\n\t" \
  "v_mfma_f32_16x16x32_bf16 v[216:219], v[76:79], v[116:119], v[216:219]\n\t" \
  "v_mfma_f32_16x16x32_bf16 v[224:227], v[80:83], v[116:119], 0\n\t" \
  "v_mfma_f32_16x16x32_bf16 v[224:227], v[84:87], v[112:115], v[224:227]\n\t" \
  "v_mfma_f32_16x16x32_bf16 v[232:235], v[88:91], v[112:115], 0\n\t" \
  "v_mfma_f32_16x16x32_bf16 v[232:235], v[92:95], v[116:119], v[232:235]\n\t" \
  "v_mfma_f32_16x16x32_bf16 v[240:243], v[96:99], v[116:119], 0\n\t" \
  "v_mfma_f32_16x16x32_bf16 v[240:243], v[100:103], v[112:115], v[240:243]\n\t" \
  "v_mfma_f32_16x16x32_bf16 v[248:251], v[120:123], v[112:115], 0\n\t" \
  "v_mfma_f32_16x16x32_bf16 v[248:251], v[124:127], v[116:119], v[248:251]\n\t" \
  "v_mfma_f32_16x16x32_bf16 v[252:255], v[120:123], v[128:131], 0\n\t" \
  "v_mfma_f32_16x16x32_bf16 v[252:255], v[124:127], v[132:135], v[252:255]\n\t" \
  "v_mfma_f32_16x16x32_bf16 v[220:223], v[76:79], v[132:135], 0\n\t" \
  "v_mfma_f32_16x16x32_bf16 v[220:223], v[68:71], v[128:131], v[220:223]\n\t" \
  "s_waitcnt lgkmcnt(0)\n\t" \
  "s_getpc_b64 s[88:89]\n\t" \
  "s_branch 28b\n\t" \
  "s_barrier\n\t" \
  "v_mfma_f32_16x16x32_bf16 v[228:231], v[84:87], v[128:131], 0\n\t" \
  "ds_read_b64_tr_b16 v[72:73], v54 offset:26624\n\t" \
  "v_mfma_f32_16x16x32_bf16 v[228:231], v[80:83], v[132:135], v[228:231]\n\t" \
  "ds_read_b64_tr_b16 v[74:75], v54 offset:27136\n\t" \
  "v_mfma_f32_16x16x32_bf16 v[236:239], v[92:95], v[132:135], 0\n\t" \
  "ds_read_b128 v[64:67], v52 offset:10240\n\t" \
  "v_mfma_f32_16x16x32_bf16 v[236:239], v[88:91], v[128:131], v[236:239]\n\t" \
  "ds_read_b64_tr_b16 v[108:109], v54 offset:30720\n\t" \
  "v_mfma_f32_16x16x32_bf16 v[244:247], v[100:103], v[128:131], 0\n\t" \
  "ds_read_b64_tr_b16 v[110:111], v54 offset:31232\n\t" \
  "v_mfma_f32_16x16x32_bf16 v[244:247], v[96:99], v[132:135], v[244:247]\n\t" \
  "ds_read_b128 v[104:107], v53 offset:10240\n\t" \
  "v_mfma_f32_16x16x32_bf16 v[152:155], v[136:139], v[68:71], 0\n\t" \
  "ds_read_b128 v[112:115], v53 offset:12288\n\t" \
  "v_mfma_f32_16x16x32_bf16 v[152:155], v[140:143], v[76:79], v[152:155]\n\t" \
  "ds_read_b128 v[116:119], v52 offset:12288\n\t" \
  "v_mfma_f32_16x16x32_bf16 v[160:163], v[140:143], v[80:83], 0\n\t" \
  "ds_read_b128 v[120:123], v52 offset:14336\n\t" \
  "v_mfma_f32_16x16x32_bf16 v[160:163], v[136:139], v[84:87], v[160:163]\n\t" \
  "ds_read_b128 v[124:127], v53 offset:14336\n\t" \
  "v_mfma_f32_16x16x32_bf16 v[168:171], v[136:139], v[88:91], 0\n\t" \
  "ds_read_b128 v[128:131], v53 offset:16384\n\t" \
  "v_mfma_f32_16x16x32_bf16 v[168:171], v[140:143], v[92:95], v[168:171]\n\t" \
  "ds_read_b128 v[132:135], v52 offset:16384\n\t" \
  "v_mfma_f32_16x16x32_bf16 v[176:179], v[140:143], v[96:99], 0\n\t" \
  "ds_read_b64_tr_b16 v[140:141], v55 offset:26624\n\t" \
  "v_mfma_f32_16x16x32_bf16 v[176:179], v[136:139], v[100:103], v[176:179]\n\t" \
  "ds_read_b64_tr_b16 v[142:143], v55 offset:27136\n\t" \
  "v_mfma_f32_16x16x32_bf16 v[156:159], v[144:147], v[68:71], 0\n\t" \
  "ds_read_b64_tr_b16 v[136:137], v55 offset:30720\n\t" \
  "v_mfma_f32_16x16x32_bf16 v[156:159], v[148:151], v[76:79], v[156:159]\n\t" \
  "ds_read_b64_tr_b16 v[138:139], v55 offset:31232\n\t" \
  "v_mfma_f32_16x16x32_bf16 v[164:167], v[148:151], v[80:83], 0\n\t" \
  "ds_read_b64_tr_b16 v[68:69], v54 offset:34816\n\t" \
  "v_mfma_f32_16x16x32_bf16 v[164:167], v[144:147], v[84:87], v[164:167]\n\t" \
  "ds_read_b64_tr_b16 v[70:71], v54 offset:35328\n\t" \
  "v_mfma_f32_16x16x32_bf16 v[172:175], v[144:147], v[88:91], 0\n\t" \
  "ds_read_b64_tr_b16 v[76:77], v54 offset:38912\n\t" \
  "v_mfma_f32_16x16x32_bf16 v[172:175], v[148:151], v[92:95], v[172:175]\n\t" \
  "ds_read_b64_tr_b16 v[78:79], v54 offset:39424\n\t" \
  "v_mfma_f32_16x16x32_bf16 v[180:183], v[148:151], v[96:99], 0\n\t" \
  "ds_read_b128 v[80:83], v52 offset:0\n\t" \
  "v_mfma_f32_16x16x32_bf16 v[180:183], v[144:147], v[100:103], v[180:183]\n\t" \
  "ds_read_b128 v[84:87], v53 offset:0\n\t" \
  "s_waitcnt lgkmcnt(7)\n\t" \
  "v_mfma_f32_16x16x32_bf16 v[184:187], v[72:75], v[64:67], v[184:187]\n\t" \
  "ds_read_b64_tr_b16 v[88:89], v55 offset:34816\n\t" \
  "v_mfma_f32_16x16x32_bf16 v[184:187], v[108:111], v[104:107], v[184:187]\n\t" \
  "ds_read_b64_tr_b16 v[90:91], v55 offset:35328\n\t" \
  "v_mfma_f32_16x16x32_bf16 v[192:195], v[108:111], v[112:115], v[192:195]\n\t" \
  "ds_read_b64_tr_b16 v[92:93], v55 offset:38912\n\t" \
  "v_mfma_f32_16x16x32_bf16 v[192:195], v[72:75], v[116:119], v[192:195]\n\t" \
  "ds_read_b64_tr_b16 v[94:95], v55 offset:39424\n\t" \
  "v_mfma_f32_16x16x32_bf16 v[200:203], v[72:75], v[120:123], v[200:203]\n\t" \
  "ds_read_b64_tr_b16 v[96:97], v54 offset:18432\n\t" \
  "v_mfma_f32_16x16x32_bf16 v[200:203], v[108:111], v[124:127], v[200:203]\n\t" \
  "ds_read_b64_tr_b16 v[98:99], v54 offset:18944\n\t" \
  "v_mfma_f32_16x16x32_bf16 v[208:211], v[108:111], v[128:131], v[208:211]\n\t" \
  "ds_read_b64_tr_b16 v[148:149], v54 offset:22528\n\t" \
  "v_mfma_f32_16x16x32_bf16 v[208:211], v[72:75], v[132:135], v[208:211]\n\t" \
  "ds_read_b64_tr_b16 v[150:151], v54 offset:23040\n\t" \
  "v_mfma_f32_16x16x32_bf16 v[188:191], v[140:143], v[64:67], v[188:191]\n\t" \
  "ds_read_b64_tr_b16 v[100:101], v55 offset:18432\n\t" \
  "s_waitcnt lgkmcnt(7)\n\t" \
  "v_mfma_f32_16x16x32_bf16 v[188:191], v[136:139], v[104:107], v[188:191]\n\t" \
  "ds_read_b64_tr_b16 v[102:103], v55 offset:18944\n\t" \
  "v_mfma_f32_16x16x32_bf16 v[196:199], v[136:139], v[112:115], v[196:199]\n\t" \
  "ds_read_b64_tr_b16 v[144:145], v55 offset:22528\n\t" \
  "v_mfma_f32_16x16x32_bf16 v[196:199], v[140:143], v[116:119], v[196:199]\n\t" \
  "ds_read_b64_tr_b16 v[146:147], v55 offset:23040\n\t" \
  "v_mfma_f32_16x16x32_bf16 v[204:207], v[140:143], v[120:123], v[204:207]\n\t" \
  "v_mfma_f32_16x16x32_bf16 v[204:207], v[136:139], v[124:127], v[204:207]\n\t" \
  "v_mfma_f32_16x16x32_bf16 v[212:215], v[136:139], v[128:131], v[212:215]\n\t" \
  "v_mfma_f32_16x16x32_bf16 v[212:215], v[140:143], v[132:135], v[212:215]\n\t" \
  "v_mfma_f32_16x16x32_bf16 v[216:219], v[64:67], v[68:71], v[216:219]\n\t" \
  "v_mfma_f32_16x16x32_bf16 v[216:219], v[104:107], v[76:79], v[216:219]\n\t" \
  "v_mfma_f32_16x16x32_bf16 v[224:227], v[112:115], v[76:79], v[224:227]\n\t" \
  "v_mfma_f32_16x16x32_bf16 v[224:227], v[116:119], v[68:71], v[224:227]\n\t" \
  "v_mfma_f32_16x16x32_bf16 v[232:235], v[120:123], v[68:71], v[232:235]\n\t" \
  "v_mfma_f32_16x16x32_bf16 v[232:235], v[124:127], v[76:79], v[232:235]\n\t" \
  "v_mfma_f32_16x16x32_bf16 v[240:243], v[128:131], v[76:79], v[240:243]\n\t" \
  "v_mfma_f32_16x16x32_bf16 v[240:243], v[132:135], v[68:71], v[240:243]\n\t" \
  "v_mfma_f32_16x16x32_bf16 v[248:251], v[80:83], v[68:71], v[248:251]\n\t" \
  "v_mfma_f32_16x16x32_bf16 v[248:251], v[84:87], v[76:79], v[248:251]\n\t" \
  "v_mfma_f32_16x16x32_bf16 v[252:255], v[80:83], v[88:91], v[252:255]\n\t" \
  "s_waitcnt lgkmcnt(0)\n\t" \
  "v_mfma_f32_16x16x32_bf16 v[252:255], v[84:87], v[92:95], v[252:255]\n\t" \
  "v_mfma_f32_16x16x32_bf16 v[220:223], v[104:107], v[92:95], v[220:223]\n\t" \
  "v_mfma_f32_16x16x32_bf16 v[220:223], v[64:67], v[88:91], v[220:223]\n\t" \
  "s_barrier\n\t" \
  "v_mfma_f32_16x16x32_bf16 v[228:231], v[116:119], v[88:91], v[228:231]\n\t" \
  "ds_read_b64_tr_b16 v[108:109], v58 offset:26624\n\t" \
  "v_mfma_f32_16x16x32_bf16 v[228:231], v[112:115], v[92:95], v[228:231]\n\t" \
  "ds_read_b64_tr_b16 v[110:111], v58 offset:27136\n\t" \
  "v_mfma_f32_16x16x32_bf16 v[236:239], v[124:127], v[92:95], v[236:239]\n\t" \
  "ds_read_b128 v[72:75], v56 offset:10240\n\t" \
  "v_mfma_f32_16x16x32_bf16 v[236:239], v[120:123], v[88:91], v[236:239]\n\t" \
  "ds_read_b64_tr_b16 v[136:137], v58 offset:30720\n\t" \
  "v_mfma_f32_16x16x32_bf16 v[244:247], v[132:135], v[88:91], v[244:247]\n\t" \
  "ds_read_b64_tr_b16 v[138:139], v58 offset:31232\n\t" \
  "v_mfma_f32_16x16x32_bf16 v[244:247], v[128:131], v[92:95], v[244:247]\n\t" \
  "ds_read_b128 v[140:143], v57 offset:10240\n\t" \
  "v_mfma_f32_16x16x32_bf16 v[152:155], v[96:99], v[64:67], v[152:155]\n\t" \
  "ds_read_b128 v[68:71], v57 offset:12288\n\t" \
  "v_mfma_f32_16x16x32_bf16 v[152:155], v[148:151], v[104:107], v[152:155]\n\t" \
  "ds_read_b128 v[76:79], v56 offset:12288\n\t" \
  "v_mfma_f32_16x16x32_bf16 v[160:163], v[148:151], v[112:115], v[160:163]\n\t" \
  "ds_read_b128 v[80:83], v56 offset:14336\n\t" \
  "v_mfma_f32_16x16x32_bf16 v[160:163], v[96:99], v[116:119], v[160:163]\n\t" \
  "ds_read_b128 v[84:87], v57 offset:14336\n\t" \
  "v_mfma_f32_16x16x32_bf16 v[168:171], v[96:99], v[120:123], v[168:171]\n\t" \
  "ds_read_b128 v[88:91], v57 offset:16384\n\t" \
  "v_mfma_f32_16x16x32_bf16 v[168:171], v[148:151], v[124:127], v[168:171]\n\t" \
  "ds_read_b128 v[92:95], v56 offset:16384\n\t" \
  "v_mfma_f32_16x16x32_bf16 v[176:179], v[148:151], v[128:131], v[176:179]\n\t" \
  "ds_read_b64_tr_b16 v[148:149], v59 offset:26624\n\t" \
  "v_mfma_f32_16x16x32_bf16 v[176:179], v[96:99], v[132:135], v[176:179]\n\t" \
  "ds_read_b64_tr_b16 v[150:151], v59 offset:27136\n\t" \
  "v_mfma_f32_16x16x32_bf16 v[156:159], v[100:103], v[64:67], v[156:159]\n\t" \
  "ds_read_b64_tr_b16 v[96:97], v59 offset:30720\n\t" \
  "v_mfma_f32_16x16x32_bf16 v[156:159], v[144:147], v[104:107], v[156:159]\n\t" \
  "ds_read_b64_tr_b16 v[98:99], v59 offset:31232\n\t" \
  "v_mfma_f32_16x16x32_bf16 v[164:167], v[144:147], v[112:115], v[164:167]\n\t" \
  "ds_read_b64_tr_b16 v[64:65], v58 offset:34816\n\t" \
  "v_mfma_f32_16x16x32_bf16 v[164:167], v[100:103], v[116:119], v[164:167]\n\t" \
  "ds_read_b64_tr_b16 v[66:67], v58 offset:35328\n\t" \
  "v_mfma_f32_16x16x32_bf16 v[172:175], v[100:103], v[120:123], v[172:175]\n\t" \
  "ds_read_b64_tr_b16 v[104:105], v58 offset:38912\n\t" \
  "v_mfma_f32_16x16x32_bf16 v[172:175], v[144:147], v[124:127], v[172:175]\n\t" \
  "ds_read_b64_tr_b16 v[106:107], v58 offset:39424\n\t" \
  "v_mfma_f32_16x16x32_bf16 v[180:183], v[144:147], v[128:131], v[180:183]\n\t" \
  "ds_read_b128 v[112:115], v56 offset:0\n\t" \
  "v_mfma_f32_16x16x32_bf16 v[180:183], v[100:103], v[132:135], v[180:183]\n\t" \
  "ds_read_b128 v[116:119], v57 offset:0\n\t" \
  "s_waitcnt lgkmcnt(7)\n\t" \
  "v_mfma_f32_16x16x32_bf16 v[184:187], v[108:111], v[72:75], v[184:187]\n\t" \
  "ds_read_b64_tr_b16 v[120:121], v59 offset:34816\n\t" \
  "v_mfma_f32_16x16x32_bf16 v[184:187], v[136:139], v[140:143], v[184:187]\n\t" \
  "ds_read_b64_tr_b16 v[122:123], v59 offset:35328\n\t" \
  "v_mfma_f32_16x16x32_bf16 v[192:195], v[136:139], v[68:71], v[192:195]\n\t" \
  "ds_read_b64_tr_b16 v[124:125], v59 offset:38912\n\t" \
  "v_mfma_f32_16x16x32_bf16 v[192:195], v[108:111], v[76:79], v[192:195]\n\t" \
  "ds_read_b64_tr_b16 v[126:127], v59 offset:39424\n\t" \
  "v_mfma_f32_16x16x32_bf16 v[200:203], v[108:111], v[80:83], v[200:203]\n\t" \
  "ds_read_b64_tr_b16 v[128:129], v58 offset:18432\n\t" \
  "v_mfma_f32_16x16x32_bf16 v[200:203], v[136:139], v[84:87], v[200:203]\n\t" \
  "ds_read_b64_tr_b16 v[130:131], v58 offset:18944\n\t" \
  "v_mfma_f32_16x16x32_bf16 v[208:211], v[136:139], v[88:91], v[208:211]\n\t" \
  "ds_read_b64_tr_b16 v[144:145], v58 offset:22528\n\t" \
  "v_mfma_f32_16x16x32_bf16 v[208:211], v[108:111], v[92:95], v[208:211]\n\t" \
  "ds_read_b64_tr_b16 v[146:147], v58 offset:23040\n\t" \
  "v_mfma_f32_16x16x32_bf16 v[188:191], v[148:151], v[72:75], v[188:191]\n\t" \
  "ds_read_b64_tr_b16 v[100:101], v59 offset:18432\n\t" \
  "s_waitcnt lgkmcnt(7)\n\t" \
  "v_mfma_f32_16x16x32_bf16 v[188:191], v[96:99], v[140:143], v[188:191]\n\t" \
  "ds_read_b64_tr_b16 v[102:103], v59 offset:18944\n\t" \
  "v_mfma_f32_16x16x32_bf16 v[196:199], v[96:99], v[68:71], v[196:199]\n\t" \
  "ds_read_b64_tr_b16 v[132:133], v59 offset:22528\n\t" \
  "v_mfma_f32_16x16x32_bf16 v[196:199], v[148:151], v[76:79], v[196:199]\n\t" \
  "ds_read_b64_tr_b16 v[134:135], v59 offset:23040\n\t" \
  "v_mfma_f32_16x16x32_bf16 v[204:207], v[148:151], v[80:83], v[204:207]\n\t" \
  "v_mfma_f32_16x16x32_bf16 v[204:207], v[96:99], v[84:87], v[204:207]\n\t" \
  "v_mfma_f32_16x16x32_bf16 v[212:215], v[96:99], v[88:91], v[212:215]\n\t" \
  "v_mfma_f32_16x16x32_bf16 v[212:215], v[148:151], v[92:95], v[212:215]\n\t" \
  "v_mfma_f32_16x16x32_bf16 v[216:219], v[72:75], v[64:67], v[216:219]\n\t" \
  "v_mfma_f32_16x16x32_bf16 v[216:219], v[140:143], v[104:107], v[216:219]\n\t" \
  "v_mfma_f32_16x16x32_bf16 v[224:227], v[68:71], v[104:107], v[224:227]\n\t" \
  "v_mfma_f32_16x16x32_bf16 v[224:227], v[76:79], v[64:67], v[224:227]\n\t" \
  "v_mfma_f32_16x16x32_bf16 v[232:235], v[80:83], v[64:67], v[232:235]\n\t" \
  "v_mfma_f32_16x16x32_bf16 v[232:235], v[84:87], v[104:107], v[232:235]\n\t" \
  "v_mfma_f32_16x16x32_bf16 v[240:243], v[88:91], v[104:107], v[240:243]\n\t" \
  "v_mfma_f32_16x16x32_bf16 v[240:243], v[92:95], v[64:67], v[240:243]\n\t" \
  "v_mfma_f32_16x16x32_bf16 v[248:251], v[112:115], v[64:67], v[248:251]\n\t" \
  "v_mfma_f32_16x16x32_bf16 v[248:251], v[116:119], v[104:107], v[248:251]\n\t" \
  "v_mfma_f32_16x16x32_bf16 v[252:255], v[112:115], v[120:123], v[252:255]\n\t" \
  "s_waitcnt lgkmcnt(0)\n\t" \
  "v_mfma_f32_16x16x32_bf16 v[252:255], v[116:119], v[124:127], v[252:255]\n\t" \
  "v_mfma_f32_16x16x32_bf16 v[220:223], v[140:143], v[124:127], v[220:223]\n\t" \
  "v_mfma_f32_16x16x32_bf16 v[220:223], v[72:75], v[120:123], v[220:223]\n\t" \
  "s_barrier\n\t" \
  "v_mfma_f32_16x16x32_bf16 v[228:231], v[76:79], v[120:123], v[228:231]\n\t" \
  "ds_read_b64_tr_b16 v[136:137], v62 offset:26624\n\t" \
  "v_bfe_u32 v30, v4, 9, 1\n\t" \
  "v_mfma_f32_16x16x32_bf16 v[228:231], v[68:71], v[124:127], v[228:231]\n\t" \
  "ds_read_b64_tr_b16 v[138:139], v62 offset:27136\n\t" \
  "v_mad_i32_i24 v30, v30, -16, v0\n\t" \
  "v_mfma_f32_16x16x32_bf16 v[236:239], v[84:87], v[124:127], v[236:239]\n\t" \
  "ds_read_b128 v[108:111], v60 offset:10240\n\t" \
  "ds_read_b128 v[26:29], v0 offset:1552\n\t" \
  "v_mfma_f32_16x16x32_bf16 v[236:239], v[80:83], v[120:123], v[236:239]\n\t" \
  "ds_read_b64_tr_b16 v[96:97], v62 offset:30720\n\t" \
  "ds_read_b128 v[30:33], v30 offset:1040\n\t" \
  "v_mfma_f32_16x16x32_bf16 v[244:247], v[92:95], v[120:123], v[244:247]\n\t" \
  "ds_read_b64_tr_b16 v[98:99], v62 offset:31232\n\t" \
  "ds_read_b128 v[34:37], v0 offset:512\n\t" \
  "v_mfma_f32_16x16x32_bf16 v[244:247], v[88:91], v[124:127], v[244:247]\n\t" \
  "ds_read_b128 v[148:151], v61 offset:10240\n\t" \
  "ds_read_b64_tr_b16 v[38:39], v1 offset:512\n\t" \
  "v_mfma_f32_16x16x32_bf16 v[152:155], v[128:131], v[72:75], v[152:155]\n\t" \
  "ds_read_b128 v[64:67], v61 offset:12288\n\t" \
  "ds_read_b64_tr_b16 v[40:41], v1 offset:0\n\t" \
  "v_mfma_f32_16x16x32_bf16 v[152:155], v[144:147], v[140:143], v[152:155]\n\t" \
  "ds_read_b128 v[104:107], v60 offset:12288\n\t" \
  "s_waitcnt vmcnt(0)\n\t" \
  "v_mfma_f32_16x16x32_bf16 v[160:163], v[144:147], v[68:71], v[160:163]\n\t" \
  "ds_read_b128 v[112:115], v60 offset:14336\n\t" \
  "v_perm_b32 v18, v10, v10, v14\n\t" \
  "v_mfma_f32_16x16x32_bf16 v[160:163], v[128:131], v[76:79], v[160:163]\n\t" \
  "ds_read_b128 v[116:119], v61 offset:14336\n\t" \
  "v_perm_b32 v19, v10, v10, v15\n\t" \
  "v_mfma_f32_16x16x32_bf16 v[168:171], v[128:131], v[80:83], v[168:171]\n\t" \
  "ds_read_b128 v[120:123], v61 offset:16384\n\t" \
  "v_perm_b32 v20, v10, v10, v16\n\t" \
  "v_mfma_f32_16x16x32_bf16 v[168:171], v[144:147], v[84:87], v[168:171]\n\t" \
  "ds_read_b128 v[124:127], v60 offset:16384\n\t" \
  "v_perm_b32 v21, v10, v10, v17\n\t" \
  "v_mfma_f32_16x16x32_bf16 v[176:179], v[144:147], v[88:91], v[176:179]\n\t" \
  "ds_read_b64_tr_b16 v[144:145], v63 offset:26624\n\t" \
  "v_perm_b32 v22, v11, v11, v14\n\t" \
  "v_mfma_f32_16x16x32_bf16 v[176:179], v[128:131], v[92:95], v[176:179]\n\t" \
  "ds_read_b64_tr_b16 v[146:147], v63 offset:27136\n\t" \
  "v_perm_b32 v23, v11, v11, v15\n\t" \
  "v_mfma_f32_16x16x32_bf16 v[156:159], v[100:103], v[72:75], v[156:159]\n\t" \
  "ds_read_b64_tr_b16 v[128:129], v63 offset:30720\n\t" \
  "v_perm_b32 v24, v11, v11, v16\n\t" \
  "v_mfma_f32_16x16x32_bf16 v[156:159], v[132:135], v[140:143], v[156:159]\n\t" \
  "ds_read_b64_tr_b16 v[130:131], v63 offset:31232\n\t" \
  "v_perm_b32 v25, v11, v11, v17\n\t" \
  "v_mfma_f32_16x16x32_bf16 v[164:167], v[132:135], v[68:71], v[164:167]\n\t" \
  "ds_read_b64_tr_b16 v[72:73], v62 offset:34816\n\t" \
  "v_mfma_f32_16x16x32_bf16 v[164:167], v[100:103], v[76:79], v[164:167]\n\t" \
  "ds_read_b64_tr_b16 v[74:75], v62 offset:35328\n\t" \
  "v_mfma_f32_16x16x32_bf16 v[172:175], v[100:103], v[80:83], v[172:175]\n\t" \
  "ds_read_b64_tr_b16 v[140:141], v62 offset:38912\n\t" \
  "s_waitcnt lgkmcnt(6)\n\t" \
  "v_mfma_f32_16x16x32_bf16 v[42:45], v[26:29], v[18:21], 0\n\t" \
  "v_mfma_f32_16x16x32_bf16 v[42:45], v[30:33], v[22:25], v[42:45]\n\t" \
  "v_mfma_f32_16x16x32_bf16 v[46:49], v[30:33], v[18:21], 0\n\t" \
  "v_mfma_f32_16x16x32_bf16 v[46:49], v[34:37], v[22:25], v[46:49]\n\t" \
  "ds_read_b128 v[26:29], v0 offset:1808\n\t" \
  "ds_read_b128 v[30:33], v0 offset:1296\n\t" \
  "v_mfma_f32_16x16x32_bf16 v[172:175], v[132:135], v[84:87], v[172:175]\n\t" \
  "ds_read_b64_tr_b16 v[142:143], v62 offset:39424\n\t" \
  "ds_read_b128 v[34:37], v0 offset:768\n\t" \
  "v_mfma_f32_16x16x32_bf16 v[180:183], v[132:135], v[88:91], v[180:183]\n\t" \
  "ds_read_b128 v[68:71], v60 offset:0\n\t" \
  "v_mfma_f32_16x16x32_bf16 v[180:183], v[100:103], v[92:95], v[180:183]\n\t" \
  "ds_read_b128 v[76:79], v61 offset:0\n\t" \
  "v_mfma_f32_16x16x32_bf16 v[184:187], v[136:139], v[108:111], v[184:187]\n\t" \
  "ds_read_b64_tr_b16 v[80:81], v63 offset:34816\n\t" \
  "v_lshlrev_b32 v50, 16, v38\n\t" \
  "v_mfma_f32_16x16x32_bf16 v[184:187], v[96:99], v[148:151], v[184:187]\n\t" \
  "ds_read_b64_tr_b16 v[82:83], v63 offset:35328\n\t" \
  "v_lshlrev_b32 v51, 16, v40\n\t" \
  "v_mfma_f32_16x16x32_bf16 v[192:195], v[96:99], v[64:67], v[192:195]\n\t" \
  "ds_read_b64_tr_b16 v[84:85], v63 offset:38912\n\t" \
  "v_mul_f32 v42, v42, v50\n\t" \
  "v_mfma_f32_16x16x32_bf16 v[192:195], v[136:139], v[104:107], v[192:195]\n\t" \
  "ds_read_b64_tr_b16 v[86:87], v63 offset:39424\n\t" \
  "v_mul_f32 v46, v46, v51\n\t" \
  "v_mfma_f32_16x16x32_bf16 v[200:203], v[136:139], v[112:115], v[200:203]\n\t" \
  "ds_read_b64_tr_b16 v[88:89], v62 offset:18432\n\t" \
  "v_cvt_pk_bf16_f32 v42, v42, v46\n\t" \
  "v_mfma_f32_16x16x32_bf16 v[200:203], v[96:99], v[116:119], v[200:203]\n\t" \
  "ds_read_b64_tr_b16 v[90:91], v62 offset:18944\n\t" \
  "v_and_b32 v50, 0xffff0000, v38\n\t" \
  "v_mfma_f32_16x16x32_bf16 v[208:211], v[96:99], v[120:123], v[208:211]\n\t" \
  "ds_read_b64_tr_b16 v[132:133], v62 offset:22528\n\t" \
  "v_and_b32 v51, 0xffff0000, v40\n\t" \
  "v_mfma_f32_16x16x32_bf16 v[208:211], v[136:139], v[124:127], v[208:211]\n\t" \
  "ds_read_b64_tr_b16 v[134:135], v62 offset:23040\n\t" \
  "v_mul_f32 v43, v43, v50\n\t" \
  "s_waitcnt lgkmcnt(7)\n\t" \
  "v_mfma_f32_16x16x32_bf16 v[188:191], v[144:147], v[108:111], v[188:191]\n\t" \
  "ds_read_b64_tr_b16 v[92:93], v63 offset:18432\n\t" \
  "v_mul_f32 v47, v47, v51\n\t" \
  "v_mfma_f32_16x16x32_bf16 v[188:191], v[128:131], v[148:151], v[188:191]\n\t" \
  "ds_read_b64_tr_b16 v[94:95], v63 offset:18944\n\t" \
  "v_cvt_pk_bf16_f32 v43, v43, v47\n\t" \
  "v_mfma_f32_16x16x32_bf16 v[196:199], v[128:131], v[64:67], v[196:199]\n\t" \
  "ds_read_b64_tr_b16 v[100:101], v63 offset:22528\n\t" \
  "v_lshlrev_b32 v50, 16, v39\n\t" \
  "v_mfma_f32_16x16x32_bf16 v[196:199], v[144:147], v[104:107], v[196:199]\n\t" \
  "ds_read_b64_tr_b16 v[102:103], v63 offset:23040\n\t" \
  "v_lshlrev_b32 v51, 16, v41\n\t" \
  "v_mfma_f32_16x16x32_bf16 v[204:207], v[144:147], v[112:115], v[204:207]\n\t" \
  "v_mul_f32 v44, v44, v50\n\t" \
  "v_mul_f32 v48, v48, v51\n\t" \
  "v_mfma_f32_16x16x32_bf16 v[204:207], v[128:131], v[116:119], v[204:207]\n\t" \
  "v_cvt_pk_bf16_f32 v44, v44, v48\n\t" \
  "v_and_b32 v50, 0xffff0000, v39\n\t" \
  "v_mfma_f32_16x16x32_bf16 v[212:215], v[128:131], v[120:123], v[212:215]\n\t" \
  "v_and_b32 v51, 0xffff0000, v41\n\t" \
  "v_mul_f32 v45, v45, v50\n\t" \
  "v_mfma_f32_16x16x32_bf16 v[212:215], v[144:147], v[124:127], v[212:215]\n\t" \
  "v_mul_f32 v49, v49, v51\n\t" \
  "ds_read_b64_tr_b16 v[38:39], v1 offset:768\n\t" \
  "v_mfma_f32_16x16x32_bf16 v[216:219], v[108:111], v[72:75], v[216:219]\n\t" \
  "ds_read_b64_tr_b16 v[40:41], v1 offset:256\n\t" \
  "v_cvt_pk_bf16_f32 v45, v45, v49\n\t" \
  "v_mfma_f32_16x16x32_bf16 v[216:219], v[148:151], v[140:143], v[216:219]\n\t" \
  "v_mfma_f32_16x16x32_bf16 v[224:227], v[64:67], v[140:143], v[224:227]\n\t" \
  "v_mfma_f32_16x16x32_bf16 v[224:227], v[104:107], v[72:75], v[224:227]\n\t" \
  "v_mfma_f32_16x16x32_bf16 v[232:235], v[112:115], v[72:75], v[232:235]\n\t" \
  "v_mfma_f32_16x16x32_bf16 v[232:235], v[116:119], v[140:143], v[232:235]\n\t" \
  "v_mfma_f32_16x16x32_bf16 v[240:243], v[120:123], v[140:143], v[240:243]\n\t" \
  "v_mfma_f32_16x16x32_bf16 v[240:243], v[124:127], v[72:75], v[240:243]\n\t" \
  "v_mfma_f32_16x16x32_bf16 v[248:251], v[68:71], v[72:75], v[248:251]\n\t" \
  "v_mfma_f32_16x16x32_bf16 v[248:251], v[76:79], v[140:143], v[248:251]\n\t" \
  "s_waitcnt lgkmcnt(0)\n\t" \
  "v_mfma_f32_16x16x32_bf16 v[252:255], v[68:71], v[80:83], v[252:255]\n\t" \
  "v_mfma_f32_16x16x32_bf16 v[252:255], v[76:79], v[84:87], v[252:255]\n\t" \
  "v_mfma_f32_16x16x32_bf16 v[220:223], v[148:151], v[84:87], v[220:223]\n\t" \
  "v_mfma_f32_16x16x32_bf16 v[220:223], v[108:111], v[80:83], v[220:223]\n\t" \
  "s_barrier\n\t" \
  "v_mfma_f32_16x16x32_bf16 v[228:231], v[104:107], v[80:83], v[228:231]\n\t" \
  "ds_read_b64_tr_b16 v[96:97], v54 offset:26624\n\t" \
  "v_mfma_f32_16x16x32_bf16 v[228:231], v[64:67], v[84:87], v[228:231]\n\t" \
  "v_mov_b32 v50, 163344\n\t" \
  "ds_read_b64_tr_b16 v[98:99], v54 offset:27136\n\t" \
  "v_mfma_f32_16x16x32_bf16 v[236:239], v[116:119], v[84:87], v[236:239]\n\t" \
  "ds_read_b32 v50, v50 offset:24\n\t" \
  "ds_read_b128 v[136:139], v52 offset:10240\n\t" \
  "v_mfma_f32_16x16x32_bf16 v[236:239], v[112:115], v[80:83], v[236:239]\n\t" \
  "ds_read_b64_tr_b16 v[128:129], v54 offset:30720\n\t" \
  "v_mfma_f32_16x16x32_bf16 v[244:247], v[124:127], v[80:83], v[244:247]\n\t" \
  "ds_read_b64_tr_b16 v[130:131], v54 offset:31232\n\t" \
  "v_mfma_f32_16x16x32_bf16 v[244:247], v[120:123], v[84:87], v[244:247]\n\t" \
  "ds_read_b128 v[144:147], v53 offset:10240\n\t" \
  "v_mfma_f32_16x16x32_bf16 v[152:155], v[88:91], v[108:111], v[152:155]\n\t" \
  "ds_read_b128 v[72:75], v53 offset:12288\n\t" \
  "v_mfma_f32_16x16x32_bf16 v[152:155], v[132:135], v[148:151], v[152:155]\n\t" \
  "ds_read_b128 v[140:143], v52 offset:12288\n\t" \
  "v_mfma_f32_16x16x32_bf16 v[160:163], v[132:135], v[64:67], v[160:163]\n\t" \
  "ds_read_b128 v[68:71], v52 offset:14336\n\t" \
  "v_mfma_f32_16x16x32_bf16 v[160:163], v[88:91], v[104:107], v[160:163]\n\t" \
  "ds_read_b128 v[76:79], v53 offset:14336\n\t" \
  "v_mfma_f32_16x16x32_bf16 v[168:171], v[88:91], v[112:115], v[168:171]\n\t" \
  "ds_read_b128 v[80:83], v53 offset:16384\n\t" \
  "v_mfma_f32_16x16x32_bf16 v[168:171], v[132:135], v[116:119], v[168:171]\n\t" \
  "ds_read_b128 v[84:87], v52 offset:16384\n\t" \
  "v_mfma_f32_16x16x32_bf16 v[176:179], v[132:135], v[120:123], v[176:179]\n\t" \
  "s_waitcnt lgkmcnt(10)\n\t" \
  "v_readfirstlane_b32 s88, v50\n\t" \
  "s_mov_b32 s89, 0\n\t" \
  "ds_read_b64_tr_b16 v[132:133], v55 offset:26624\n\t" \
  "v_mfma_f32_16x16x32_bf16 v[176:179], v[88:91], v[124:127], v[176:179]\n\t" \
  "ds_read_b64_tr_b16 v[134:135], v55 offset:27136\n\t" \
  "v_mfma_f32_16x16x32_bf16 v[156:159], v[92:95], v[108:111], v[156:159]\n\t" \
  "s_xor_b64 %24, %24, s[88:89]\n\t" \
  "ds_read_b64_tr_b16 v[88:89], v55 offset:30720\n\t" \
  "v_mfma_f32_16x16x32_bf16 v[156:159], v[100:103], v[148:151], v[156:159]\n\t" \
  "s_xor_b64 %25, %25, s[88:89]\n\t" \
  "ds_read_b64_tr_b16 v[90:91], v55 offset:31232\n\t" \
  "v_mfma_f32_16x16x32_bf16 v[164:167], v[100:103], v[64:67], v[164:167]\n\t" \
  "s_xor_b64 %14, %14, s[88:89]\n\t" \
  "ds_read_b64_tr_b16 v[108:109], v54 offset:34816\n\t" \
  "v_mfma_f32_16x16x32_bf16 v[164:167], v[92:95], v[104:107], v[164:167]\n\t" \
  "s_xor_b64 %19, %19, s[88:89]\n\t" \
  "ds_read_b64_tr_b16 v[110:111], v54 offset:35328\n\t" \
  "v_mfma_f32_16x16x32_bf16 v[172:175], v[92:95], v[112:115], v[172:175]\n\t" \
  "s_xor_b64 %21, %21, s[88:89]\n\t" \
  "ds_read_b64_tr_b16 v[148:149], v54 offset:38912\n\t" \
  "v_mfma_f32_16x16x32_bf16 v[172:175], v[100:103], v[116:119], v[172:175]\n\t" \
  "s_xor_b64 s[92:93], s[92:93], s[88:89]\n\t" \
  "ds_read_b64_tr_b16 v[150:151], v54 offset:39424\n\t" \
  "v_mfma_f32_16x16x32_bf16 v[180:183], v[100:103], v[120:123], v[180:183]\n\t" \
  "ds_read_b128 v[64:67], v52 offset:0\n\t" \
  "v_mfma_f32_16x16x32_bf16 v[180:183], v[92:95], v[124:127], v[180:183]\n\t" \
  "ds_read_b128 v[104:107], v53 offset:0\n\t" \
  "s_waitcnt lgkmcnt(7)\n\t" \
  "v_mfma_f32_16x16x32_bf16 v[184:187], v[96:99], v[136:139], v[184:187]\n\t" \
  "ds_read_b64_tr_b16 v[112:113], v55 offset:34816\n\t" \
  "v_mfma_f32_16x16x32_bf16 v[184:187], v[128:131], v[144:147], v[184:187]\n\t" \
  "ds_read_b64_tr_b16 v[114:115], v55 offset:35328\n\t" \
  "v_mfma_f32_16x16x32_bf16 v[192:195], v[128:131], v[72:75], v[192:195]\n\t" \
  "ds_read_b64_tr_b16 v[116:117], v55 offset:38912\n\t" \
  "v_mfma_f32_16x16x32_bf16 v[192:195], v[96:99], v[140:143], v[192:195]\n\t" \
  "ds_read_b64_tr_b16 v[118:119], v55 offset:39424\n\t" \
  "v_mfma_f32_16x16x32_bf16 v[200:203], v[96:99], v[68:71], v[200:203]\n\t" \
  "ds_read_b64_tr_b16 v[100:101], v54 offset:18432\n\t" \
  "v_mfma_f32_16x16x32_bf16 v[200:203], v[128:131], v[76:79], v[200:203]\n\t" \
  "ds_read_b64_tr_b16 v[102:103], v54 offset:18944\n\t" \
  "v_mfma_f32_16x16x32_bf16 v[208:211], v[128:131], v[80:83], v[208:211]\n\t" \
  "ds_read_b64_tr_b16 v[120:121], v54 offset:22528\n\t" \
  "v_mfma_f32_16x16x32_bf16 v[208:211], v[96:99], v[84:87], v[208:211]\n\t" \
  "ds_read_b64_tr_b16 v[122:123], v54 offset:23040\n\t" \
  "v_mfma_f32_16x16x32_bf16 v[188:191], v[132:135], v[136:139], v[188:191]\n\t" \
  "ds_read_b64_tr_b16 v[92:93], v55 offset:18432\n\t" \
  "s_waitcnt lgkmcnt(7)\n\t" \
  "v_mfma_f32_16x16x32_bf16 v[188:191], v[88:91], v[144:147], v[188:191]\n\t" \
  "ds_read_b64_tr_b16 v[94:95], v55 offset:18944\n\t" \
  "v_mfma_f32_16x16x32_bf16 v[196:199], v[88:91], v[72:75], v[196:199]\n\t" \
  "ds_read_b64_tr_b16 v[124:125], v55 offset:22528\n\t" \
  "v_mfma_f32_16x16x32_bf16 v[196:199], v[132:135], v[140:143], v[196:199]\n\t" \
  "ds_read_b64_tr_b16 v[126:127], v55 offset:23040\n\t" \
  "v_mfma_f32_16x16x32_bf16 v[204:207], v[132:135], v[68:71], v[204:207]\n\t" \
  "v_mfma_f32_16x16x32_bf16 v[204:207], v[88:91], v[76:79], v[204:207]\n\t" \
  "v_mfma_f32_16x16x32_bf16 v[212:215], v[88:91], v[80:83], v[212:215]\n\t" \
  "v_mfma_f32_16x16x32_bf16 v[212:215], v[132:135], v[84:87], v[212:215]\n\t" \
  "v_mfma_f32_16x16x32_bf16 v[216:219], v[136:139], v[108:111], v[216:219]\n\t" \
  "v_mfma_f32_16x16x32_bf16 v[216:219], v[144:147], v[148:151], v[216:219]\n\t" \
  "v_mfma_f32_16x16x32_bf16 v[224:227], v[72:75], v[148:151], v[224:227]\n\t" \
  "v_mfma_f32_16x16x32_bf16 v[224:227], v[140:143], v[108:111], v[224:227]\n\t" \
  "v_mfma_f32_16x16x32_bf16 v[232:235], v[68:71], v[108:111], v[232:235]\n\t" \
  "global_store_dwordx4 v2, v[42:45], %24 offset:0 sc1\n\t" \
  "v_mfma_f32_16x16x32_bf16 v[232:235], v[76:79], v[148:151], v[232:235]\n\t" \
  "s_nop 0\n\t" \
  "v_mfma_f32_16x16x32_bf16 v[42:45], v[26:29], v[18:21], 0\n\t" \
  "v_mfma_f32_16x16x32_bf16 v[42:45], v[30:33], v[22:25], v[42:45]\n\t" \
  "v_mfma_f32_16x16x32_bf16 v[46:49], v[30:33], v[18:21], 0\n\t" \
  "v_mfma_f32_16x16x32_bf16 v[46:49], v[34:37], v[22:25], v[46:49]\n\t" \
  "v_bfe_u32 v30, v4, 9, 1\n\t" \
  "v_mad_i32_i24 v30, v30, -16, v0\n\t" \
  "v_mfma_f32_16x16x32_bf16 v[240:243], v[80:83], v[148:151], v[240:243]\n\t" \
  "ds_read_b128 v[26:29], v0 offset:1584\n\t" \
  "ds_read_b128 v[30:33], v30 offset:1072\n\t" \
  "v_mfma_f32_16x16x32_bf16 v[240:243], v[84:87], v[108:111], v[240:243]\n\t" \
  "ds_read_b128 v[34:37], v0 offset:544\n\t" \
  "v_perm_b32 v18, v12, v12, v14\n\t" \
  "v_mfma_f32_16x16x32_bf16 v[248:251], v[64:67], v[108:111], v[248:251]\n\t" \
  "v_perm_b32 v19, v12, v12, v15\n\t" \
  "v_perm_b32 v20, v12, v12, v16\n\t" \
  "v_mfma_f32_16x16x32_bf16 v[248:251], v[104:107], v[148:151], v[248:251]\n\t" \
  "v_perm_b32 v21, v12, v12, v17\n\t" \
  "v_perm_b32 v22, v13, v13, v14\n\t" \
  "v_mfma_f32_16x16x32_bf16 v[252:255], v[64:67], v[112:115], v[252:255]\n\t" \
  "v_perm_b32 v23, v13, v13, v15\n\t" \
  "v_perm_b32 v24, v13, v13, v16\n\t" \
  "s_waitcnt lgkmcnt(3)\n\t" \
  "v_mfma_f32_16x16x32_bf16 v[252:255], v[104:107], v[116:119], v[252:255]\n\t" \
  "v_perm_b32 v25, v13, v13, v17\n\t" \
  "v_lshlrev_b32 v50, 16, v38\n\t" \
  "v_mfma_f32_16x16x32_bf16 v[220:223], v[144:147], v[116:119], v[220:223]\n\t" \
  "v_lshlrev_b32 v51, 16, v40\n\t" \
  "v_mul_f32 v42, v42, v50\n\t" \
  "v_mfma_f32_16x16x32_bf16 v[220:223], v[136:139], v[112:115], v[220:223]\n\t" \
  "v_mul_f32 v46, v46, v51\n\t" \
  "v_cvt_pk_bf16_f32 v42, v42, v46\n\t" \
  "s_waitcnt lgkmcnt(0)\n\t" \
  "s_barrier\n\t" \
  "v_mfma_f32_16x16x32_bf16 v[228:231], v[140:143], v[112:115], v[228:231]\n\t" \
  "ds_read_b64_tr_b16 v[128:129], v58 offset:26624\n\t" \
  "v_and_b32 v50, 0xffff0000, v38\n\t" \
  "v_mfma_f32_16x16x32_bf16 v[228:231], v[72:75], v[116:119], v[228:231]\n\t" \
  "ds_read_b64_tr_b16 v[130:131], v58 offset:27136\n\t" \
  "v_and_b32 v51, 0xffff0000, v40\n\t" \
  "v_mfma_f32_16x16x32_bf16 v[236:239], v[76:79], v[116:119], v[236:239]\n\t" \
  "ds_read_b128 v[96:99], v56 offset:10240\n\t" \
  "v_mul_f32 v43, v43, v50\n\t" \
  "v_mfma_f32_16x16x32_bf16 v[236:239], v[68:71], v[112:115], v[236:239]\n\t" \
  "ds_read_b64_tr_b16 v[88:89], v58 offset:30720\n\t" \
  "500:\n\t" \
  "s_cbranch_execz 504f\n\t" \
  "v_mul_f32 v47, v47, v51\n\t" \
  "v_mfma_f32_16x16x32_bf16 v[244:247], v[84:87], v[112:115], v[244:247]\n\t" \
  "ds_read_b64_tr_b16 v[90:91], v58 offset:31232\n\t" \
  "v_cvt_pk_bf16_f32 v43, v43, v47\n\t" \
  "v_mfma_f32_16x16x32_bf16 v[244:247], v[80:83], v[116:119], v[244:247]\n\t" \
  "ds_read_b128 v[132:135], v57 offset:10240\n\t" \
  "v_lshlrev_b32 v50, 16, v39\n\t" \
  "v_mfma_f32_16x16x32_bf16 v[152:155], v[100:103], v[136:139], v[152:155]\n\t" \
  "ds_read_b128 v[108:111], v57 offset:12288\n\t" \
  "v_lshlrev_b32 v51, 16, v41\n\t" \
  "v_mfma_f32_16x16x32_bf16 v[152:155], v[120:123], v[144:147], v[152:155]\n\t" \
  "ds_read_b128 v[148:151], v56 offset:12288\n\t" \
  "v_mul_f32 v44, v44, v50\n\t" \
  "v_mfma_f32_16x16x32_bf16 v[160:163], v[120:123], v[72:75], v[160:163]\n\t" \
  "ds_read_b128 v[64:67], v56 offset:14336\n\t" \
  "v_mul_f32 v48, v48, v51\n\t" \
  "v_mfma_f32_16x16x32_bf16 v[160:163], v[100:103], v[140:143], v[160:163]\n\t" \
  "ds_read_b128 v[104:107], v57 offset:14336\n\t" \
  "v_cvt_pk_bf16_f32 v44, v44, v48\n\t" \
  "v_mfma_f32_16x16x32_bf16 v[168:171], v[100:103], v[68:71], v[168:171]\n\t" \
  "ds_read_b128 v[112:115], v57 offset:16384\n\t" \
  "v_and_b32 v50, 0xffff0000, v39\n\t" \
  "v_mfma_f32_16x16x32_bf16 v[168:171], v[120:123], v[76:79], v[168:171]\n\t" \
  "ds_read_b128 v[116:119], v56 offset:16384\n\t" \
  "v_and_b32 v51, 0xffff0000, v41\n\t" \
  "v_mfma_f32_16x16x32_bf16 v[176:179], v[120:123], v[80:83], v[176:179]\n\t" \
  "ds_read_b64_tr_b16 v[120:121], v59 offset:26624\n\t" \
  "v_mul_f32 v45, v45, v50\n\t" \
  "v_mfma_f32_16x16x32_bf16 v[176:179], v[100:103], v[84:87], v[176:179]\n\t" \
  "ds_read_b64_tr_b16 v[122:123], v59 offset:27136\n\t" \
  "v_mul_f32 v49, v49, v51\n\t" \
  "v_mfma_f32_16x16x32_bf16 v[156:159], v[92:95], v[136:139], v[156:159]\n\t" \
  "ds_read_b64_tr_b16 v[100:101], v59 offset:30720\n\t" \
  "ds_read_b64_tr_b16 v[38:39], v1 offset:544\n\t" \
  "v_mfma_f32_16x16x32_bf16 v[156:159], v[124:127], v[144:147], v[156:159]\n\t" \
  "ds_read_b64_tr_b16 v[102:103], v59 offset:31232\n\t" \
  "ds_read_b64_tr_b16 v[40:41], v1 offset:32\n\t" \
  "v_mfma_f32_16x16x32_bf16 v[164:167], v[124:127], v[72:75], v[164:167]\n\t" \
  "ds_read_b64_tr_b16 v[136:137], v58 offset:34816\n\t" \
  "v_cvt_pk_bf16_f32 v45, v45, v49\n\t" \
  "v_mfma_f32_16x16x32_bf16 v[164:167], v[92:95], v[140:143], v[164:167]\n\t" \
  "ds_read_b64_tr_b16 v[138:139], v58 offset:35328\n\t" \
  "v_mfma_f32_16x16x32_bf16 v[172:175], v[92:95], v[68:71], v[172:175]\n\t" \
  "ds_read_b64_tr_b16 v[144:145], v58 offset:38912\n\t" \
  "v_mfma_f32_16x16x32_bf16 v[172:175], v[124:127], v[76:79], v[172:175]\n\t" \
  "ds_read_b64_tr_b16 v[146:147], v58 offset:39424\n\t" \
  "v_mfma_f32_16x16x32_bf16 v[180:183], v[124:127], v[80:83], v[180:183]\n\t" \
  "ds_read_b128 v[72:75], v56 offset:0\n\t" \
  "v_mfma_f32_16x16x32_bf16 v[180:183], v[92:95], v[84:87], v[180:183]\n\t" \
  "ds_read_b128 v[140:143], v57 offset:0\n\t" \
  "s_waitcnt lgkmcnt(8)\n\t" \
  "v_mfma_f32_16x16x32_bf16 v[184:187], v[128:131], v[96:99], v[184:187]\n\t" \
  "ds_read_b64_tr_b16 v[68:69], v59 offset:34816\n\t" \
  "v_mfma_f32_16x16x32_bf16 v[184:187], v[88:91], v[132:135], v[184:187]\n\t" \
  "ds_read_b64_tr_b16 v[70:71], v59 offset:35328\n\t" \
  "v_mfma_f32_16x16x32_bf16 v[192:195], v[88:91], v[108:111], v[192:195]\n\t" \
  "ds_read_b64_tr_b16 v[76:77], v59 offset:38912\n\t" \
  "v_mfma_f32_16x16x32_bf16 v[192:195], v[128:131], v[148:151], v[192:195]\n\t" \
  "ds_read_b64_tr_b16 v[78:79], v59 offset:39424\n\t" \
  "v_mfma_f32_16x16x32_bf16 v[200:203], v[128:131], v[64:67], v[200:203]\n\t" \
  "ds_read_b64_tr_b16 v[80:81], v58 offset:18432\n\t" \
  "v_mfma_f32_16x16x32_bf16 v[200:203], v[88:91], v[104:107], v[200:203]\n\t" \
  "ds_read_b64_tr_b16 v[82:83], v58 offset:18944\n\t" \
  "v_mfma_f32_16x16x32_bf16 v[208:211], v[88:91], v[112:115], v[208:211]\n\t" \
  "ds_read_b64_tr_b16 v[124:125], v58 offset:22528\n\t" \
  "v_mfma_f32_16x16x32_bf16 v[208:211], v[128:131], v[116:119], v[208:211]\n\t" \
  "ds_read_b64_tr_b16 v[126:127], v58 offset:23040\n\t" \
  "v_mfma_f32_16x16x32_bf16 v[188:191], v[120:123], v[96:99], v[188:191]\n\t" \
  "ds_read_b64_tr_b16 v[84:85], v59 offset:18432\n\t" \
  "s_waitcnt lgkmcnt(7)\n\t" \
  "v_mfma_f32_16x16x32_bf16 v[188:191], v[100:103], v[132:135], v[188:191]\n\t" \
  "ds_read_b64_tr_b16 v[86:87], v59 offset:18944\n\t" \
  "v_mfma_f32_16x16x32_bf16 v[196:199], v[100:103], v[108:111], v[196:199]\n\t" \
  "ds_read_b64_tr_b16 v[92:93], v59 offset:22528\n\t" \
  "v_mfma_f32_16x16x32_bf16 v[196:199], v[120:123], v[148:151], v[196:199]\n\t" \
  "ds_read_b64_tr_b16 v[94:95], v59 offset:23040\n\t" \
  "v_mfma_f32_16x16x32_bf16 v[204:207], v[120:123], v[64:67], v[204:207]\n\t" \
  "v_mfma_f32_16x16x32_bf16 v[204:207], v[100:103], v[104:107], v[204:207]\n\t" \
  "v_mfma_f32_16x16x32_bf16 v[212:215], v[100:103], v[112:115], v[212:215]\n\t" \
  "v_mfma_f32_16x16x32_bf16 v[212:215], v[120:123], v[116:119], v[212:215]\n\t" \
  "v_mfma_f32_16x16x32_bf16 v[216:219], v[96:99], v[136:139], v[216:219]\n\t" \
  "v_mfma_f32_16x16x32_bf16 v[216:219], v[132:135], v[144:147], v[216:219]\n\t" \
  "v_mfma_f32_16x16x32_bf16 v[224:227], v[108:111], v[144:147], v[224:227]\n\t" \
  "v_mfma_f32_16x16x32_bf16 v[224:227], v[148:151], v[136:139], v[224:227]\n\t" \
  "v_mfma_f32_16x16x32_bf16 v[232:235], v[64:67], v[136:139], v[232:235]\n\t" \
  "global_store_dwordx4 v2, v[42:45], %24 offset:1024 sc1\n\t" \
  "v_add_u32 v2, 0x20000, v2\n\t" \
  "v_mfma_f32_16x16x32_bf16 v[232:235], v[104:107], v[144:147], v[232:235]\n\t" \
  "v_mfma_f32_16x16x32_bf16 v[240:243], v[112:115], v[144:147], v[240:243]\n\t" \
  "v_mfma_f32_16x16x32_bf16 v[42:45], v[26:29], v[18:21], 0\n\t" \
  "v_mfma_f32_16x16x32_bf16 v[42:45], v[30:33], v[22:25], v[42:45]\n\t" \
  "v_mfma_f32_16x16x32_bf16 v[46:49], v[30:33], v[18:21], 0\n\t" \
  "v_mfma_f32_16x16x32_bf16 v[46:49], v[34:37], v[22:25], v[46:49]\n\t" \
  "ds_read_b128 v[26:29], v0 offset:1840\n\t" \
  "ds_read_b128 v[30:33], v0 offset:1328\n\t" \
  "v_mfma_f32_16x16x32_bf16 v[240:243], v[116:119], v[136:139], v[240:243]\n\t" \
  "ds_read_b128 v[34:37], v0 offset:800\n\t" \
  "v_mfma_f32_16x16x32_bf16 v[248:251], v[72:75], v[136:139], v[248:251]\n\t" \
  "v_mfma_f32_16x16x32_bf16 v[248:251], v[140:143], v[144:147], v[248:251]\n\t" \
  "v_lshlrev_b32 v50, 16, v38\n\t" \
  "v_lshlrev_b32 v51, 16, v40\n\t" \
  "v_mfma_f32_16x16x32_bf16 v[252:255], v[72:75], v[68:71], v[252:255]\n\t" \
  "v_mul_f32 v42, v42, v50\n\t" \
  "v_mul_f32 v46, v46, v51\n\t" \
  "s_waitcnt lgkmcnt(3)\n\t" \
  "v_mfma_f32_16x16x32_bf16 v[252:255], v[140:143], v[76:79], v[252:255]\n\t" \
  "v_cvt_pk_bf16_f32 v42, v42, v46\n\t" \
  "v_and_b32 v50, 0xffff0000, v38\n\t" \
  "v_mfma_f32_16x16x32_bf16 v[220:223], v[132:135], v[76:79], v[220:223]\n\t" \
  "v_and_b32 v51, 0xffff0000, v40\n\t" \
  "v_mul_f32 v43, v43, v50\n\t" \
  "v_mfma_f32_16x16x32_bf16 v[220:223], v[96:99], v[68:71], v[220:223]\n\t" \
  "v_mul_f32 v47, v47, v51\n\t" \
  "v_cvt_pk_bf16_f32 v43, v43, v47\n\t" \
  "s_waitcnt lgkmcnt(0)\n\t" \
  "s_barrier\n\t" \
  "v_mfma_f32_16x16x32_bf16 v[228:231], v[148:151], v[68:71], v[228:231]\n\t" \
  "ds_read_b64_tr_b16 v[88:89], v62 offset:26624\n\t" \
  "v_lshlrev_b32 v50, 16, v39\n\t" \
  "v_mfma_f32_16x16x32_bf16 v[228:231], v[108:111], v[76:79], v[228:231]\n\t" \
  "ds_read_b64_tr_b16 v[90:91], v62 offset:27136\n\t" \
  "v_lshlrev_b32 v51, 16, v41\n\t" \
  "v_mfma_f32_16x16x32_bf16 v[236:239], v[104:107], v[76:79], v[236:239]\n\t" \
  "ds_read_b128 v[128:131], v60 offset:10240\n\t" \
  "v_mul_f32 v44, v44, v50\n\t" \
  "v_mfma_f32_16x16x32_bf16 v[236:239], v[64:67], v[68:71], v[236:239]\n\t" \
  "ds_read_b64_tr_b16 v[100:101], v62 offset:30720\n\t" \
  "v_mul_f32 v48, v48, v51\n\t" \
  "v_mfma_f32_16x16x32_bf16 v[244:247], v[116:119], v[68:71], v[244:247]\n\t" \
  "ds_read_b64_tr_b16 v[102:103], v62 offset:31232\n\t" \
  "v_cvt_pk_bf16_f32 v44, v44, v48\n\t" \
  "v_mfma_f32_16x16x32_bf16 v[244:247], v[112:115], v[76:79], v[244:247]\n\t" \
  "ds_read_b128 v[120:123], v61 offset:10240\n\t" \
  "v_and_b32 v50, 0xffff0000, v39\n\t" \
  "v_mfma_f32_16x16x32_bf16 v[152:155], v[80:83], v[96:99], v[152:155]\n\t" \
  "ds_read_b128 v[136:139], v61 offset:12288\n\t" \
  "v_and_b32 v51, 0xffff0000, v41\n\t" \
  "v_mfma_f32_16x16x32_bf16 v[152:155], v[124:127], v[132:135], v[152:155]\n\t" \
  "ds_read_b128 v[144:147], v60 offset:12288\n\t" \
  "v_mul_f32 v45, v45, v50\n\t" \
  "v_mfma_f32_16x16x32_bf16 v[160:163], v[124:127], v[108:111], v[160:163]\n\t" \
  "ds_read_b128 v[72:75], v60 offset:14336\n\t" \
  "v_mul_f32 v49, v49, v51\n\t" \
  "v_mfma_f32_16x16x32_bf16 v[160:163], v[80:83], v[148:151], v[160:163]\n\t" \
  "ds_read_b128 v[140:143], v61 offset:14336\n\t" \
  "ds_read_b64_tr_b16 v[38:39], v1 offset:800\n\t" \
  "v_mfma_f32_16x16x32_bf16 v[168:171], v[80:83], v[64:67], v[168:171]\n\t" \
  "ds_read_b128 v[68:71], v61 offset:16384\n\t" \
  "ds_read_b64_tr_b16 v[40:41], v1 offset:288\n\t" \
  "v_mfma_f32_16x16x32_bf16 v[168:171], v[124:127], v[104:107], v[168:171]\n\t" \
  "ds_read_b128 v[76:79], v60 offset:16384\n\t" \
  "v_cvt_pk_bf16_f32 v45, v45, v49\n\t" \
  "v_mfma_f32_16x16x32_bf16 v[176:179], v[124:127], v[112:115], v[176:179]\n\t" \
  "ds_read_b64_tr_b16 v[124:125], v63 offset:26624\n\t" \
  "v_mfma_f32_16x16x32_bf16 v[176:179], v[80:83], v[116:119], v[176:179]\n\t" \
  "ds_read_b64_tr_b16 v[126:127], v63 offset:27136\n\t" \
  "v_mfma_f32_16x16x32_bf16 v[156:159], v[84:87], v[96:99], v[156:159]\n\t" \
  "ds_read_b64_tr_b16 v[80:81], v63 offset:30720\n\t" \
  "v_mfma_f32_16x16x32_bf16 v[156:159], v[92:95], v[132:135], v[156:159]\n\t" \
  "ds_read_b64_tr_b16 v[82:83], v63 offset:31232\n\t" \
  "v_mfma_f32_16x16x32_bf16 v[164:167], v[92:95], v[108:111], v[164:167]\n\t" \
  "ds_read_b64_tr_b16 v[96:97], v62 offset:34816\n\t" \
  "v_mfma_f32_16x16x32_bf16 v[164:167], v[84:87], v[148:151], v[164:167]\n\t" \
  "ds_read_b64_tr_b16 v[98:99], v62 offset:35328\n\t" \
  "v_mfma_f32_16x16x32_bf16 v[172:175], v[84:87], v[64:67], v[172:175]\n\t" \
  "ds_read_b64_tr_b16 v[132:133], v62 offset:38912\n\t" \
  "v_mfma_f32_16x16x32_bf16 v[172:175], v[92:95], v[104:107], v[172:175]\n\t" \
  "ds_read_b64_tr_b16 v[134:135], v62 offset:39424\n\t" \
  "v_mfma_f32_16x16x32_bf16 v[180:183], v[92:95], v[112:115], v[180:183]\n\t" \
  "ds_read_b128 v[108:111], v60 offset:0\n\t" \
  "v_mfma_f32_16x16x32_bf16 v[180:183], v[84:87], v[116:119], v[180:183]\n\t" \
  "ds_read_b128 v[148:151], v61 offset:0\n\t" \
  "s_waitcnt lgkmcnt(7)\n\t" \
  "v_mfma_f32_16x16x32_bf16 v[184:187], v[88:91], v[128:131], v[184:187]\n\t" \
  "ds_read_b64_tr_b16 v[64:65], v63 offset:34816\n\t" \
  "v_mfma_f32_16x16x32_bf16 v[184:187], v[100:103], v[120:123], v[184:187]\n\t" \
  "ds_read_b64_tr_b16 v[66:67], v63 offset:35328\n\t" \
  "v_mfma_f32_16x16x32_bf16 v[192:195], v[100:103], v[136:139], v[192:195]\n\t" \
  "ds_read_b64_tr_b16 v[104:105], v63 offset:38912\n\t" \
  "v_mfma_f32_16x16x32_bf16 v[192:195], v[88:91], v[144:147], v[192:195]\n\t" \
  "ds_read_b64_tr_b16 v[106:107], v63 offset:39424\n\t" \
  "v_mfma_f32_16x16x32_bf16 v[200:203], v[88:91], v[72:75], v[200:203]\n\t" \
  "ds_read_b64_tr_b16 v[92:93], v62 offset:18432\n\t" \
  "v_mfma_f32_16x16x32_bf16 v[200:203], v[100:103], v[140:143], v[200:203]\n\t" \
  "ds_read_b64_tr_b16 v[94:95], v62 offset:18944\n\t" \
  "v_mfma_f32_16x16x32_bf16 v[208:211], v[100:103], v[68:71], v[208:211]\n\t" \
  "ds_read_b64_tr_b16 v[112:113], v62 offset:22528\n\t" \
  "v_mfma_f32_16x16x32_bf16 v[208:211], v[88:91], v[76:79], v[208:211]\n\t" \
  "ds_read_b64_tr_b16 v[114:115], v62 offset:23040\n\t" \
  "v_mfma_f32_16x16x32_bf16 v[188:191], v[124:127], v[128:131], v[188:191]\n\t" \
  "ds_read_b64_tr_b16 v[84:85], v63 offset:18432\n\t" \
  "s_waitcnt lgkmcnt(7)\n\t" \
  "v_mfma_f32_16x16x32_bf16 v[188:191], v[80:83], v[120:123], v[188:191]\n\t" \
  "ds_read_b64_tr_b16 v[86:87], v63 offset:18944\n\t" \
  "v_mfma_f32_16x16x32_bf16 v[196:199], v[80:83], v[136:139], v[196:199]\n\t" \
  "ds_read_b64_tr_b16 v[116:117], v63 offset:22528\n\t" \
  "v_mfma_f32_16x16x32_bf16 v[196:199], v[124:127], v[144:147], v[196:199]\n\t" \
  "ds_read_b64_tr_b16 v[118:119], v63 offset:23040\n\t" \
  "v_mfma_f32_16x16x32_bf16 v[204:207], v[124:127], v[72:75], v[204:207]\n\t" \
  "v_mfma_f32_16x16x32_bf16 v[204:207], v[80:83], v[140:143], v[204:207]\n\t" \
  "v_mfma_f32_16x16x32_bf16 v[212:215], v[80:83], v[68:71], v[212:215]\n\t" \
  "v_mfma_f32_16x16x32_bf16 v[212:215], v[124:127], v[76:79], v[212:215]\n\t" \
  "v_mfma_f32_16x16x32_bf16 v[216:219], v[128:131], v[96:99], v[216:219]\n\t" \
  "v_mfma_f32_16x16x32_bf16 v[216:219], v[120:123], v[132:135], v[216:219]\n\t" \
  "v_mfma_f32_16x16x32_bf16 v[224:227], v[136:139], v[132:135], v[224:227]\n\t" \
  "v_mfma_f32_16x16x32_bf16 v[224:227], v[144:147], v[96:99], v[224:227]\n\t" \
  "v_mfma_f32_16x16x32_bf16 v[232:235], v[72:75], v[96:99], v[232:235]\n\t" \
  "global_store_dwordx4 v2, v[42:45], %24 offset:0 sc1\n\t" \
  "v_mfma_f32_16x16x32_bf16 v[232:235], v[140:143], v[132:135], v[232:235]\n\t" \
  "s_nop 0\n\t" \
  "v_mfma_f32_16x16x32_bf16 v[42:45], v[26:29], v[18:21], 0\n\t" \
  "v_mfma_f32_16x16x32_bf16 v[42:45], v[30:33], v[22:25], v[42:45]\n\t" \
  "v_mfma_f32_16x16x32_bf16 v[46:49], v[30:33], v[18:21], 0\n\t" \
  "v_mfma_f32_16x16x32_bf16 v[46:49], v[34:37], v[22:25], v[46:49]\n\t" \
  "v_mfma_f32_16x16x32_bf16 v[240:243], v[68:71], v[132:135], v[240:243]\n\t" \
  "v_mfma_f32_16x16x32_bf16 v[240:243], v[76:79], v[96:99], v[240:243]\n\t" \
  "v_lshlrev_b32 v50, 16, v38\n\t" \
  "v_lshlrev_b32 v51, 16, v40\n\t" \
  "v_mfma_f32_16x16x32_bf16 v[248:251], v[108:111], v[96:99], v[248:251]\n\t" \
  "s_nop 0\n\t" \
  "v_mul_f32 v42, v42, v50\n\t" \
  "s_nop 0\n\t" \
  "v_mul_f32 v46, v46, v51\n\t" \
  "v_mfma_f32_16x16x32_bf16 v[248:251], v[148:151], v[132:135], v[248:251]\n\t" \
  "v_cvt_pk_bf16_f32 v42, v42, v46\n\t" \
  "v_and_b32 v50, 0xffff0000, v38\n\t" \
  "v_mfma_f32_16x16x32_bf16 v[252:255], v[108:111], v[64:67], v[252:255]\n\t" \
  "v_and_b32 v51, 0xffff0000, v40\n\t" \
  "v_mul_f32 v43, v43, v50\n\t" \
  "s_waitcnt lgkmcnt(0)\n\t" \
  "v_mfma_f32_16x16x32_bf16 v[252:255], v[148:151], v[104:107], v[252:255]\n\t" \
  "v_mul_f32 v47, v47, v51\n\t" \
  "v_cvt_pk_bf16_f32 v43, v43, v47\n\t" \
  "v_mfma_f32_16x16x32_bf16 v[220:223], v[120:123], v[104:107], v[220:223]\n\t" \
  "v_lshlrev_b32 v50, 16, v39\n\t" \
  "v_lshlrev_b32 v51, 16, v41\n\t" \
  "v_mfma_f32_16x16x32_bf16 v[220:223], v[128:131], v[64:67], v[220:223]\n\t" \
  "v_mul_f32 v44, v44, v50\n\t" \
  "v_mul_f32 v48, v48, v51\n\t" \
  "s_barrier\n\t" \
  "v_mfma_f32_16x16x32_bf16 v[228:231], v[144:147], v[64:67], v[228:231]\n\t" \
  "ds_read_b64_tr_b16 v[100:101], v54 offset:26624\n\t" \
  "v_cvt_pk_bf16_f32 v44, v44, v48\n\t" \
  "v_mfma_f32_16x16x32_bf16 v[228:231], v[136:139], v[104:107], v[228:231]\n\t" \
  "ds_read_b64_tr_b16 v[102:103], v54 offset:27136\n\t" \
  "v_and_b32 v50, 0xffff0000, v39\n\t" \
  "v_mfma_f32_16x16x32_bf16 v[236:239], v[140:143], v[104:107], v[236:239]\n\t" \
  "ds_read_b128 v[88:91], v52 offset:10240\n\t" \
  "v_and_b32 v51, 0xffff0000, v41\n\t" \
  "v_mfma_f32_16x16x32_bf16 v[236:239], v[72:75], v[64:67], v[236:239]\n\t" \
  "ds_read_b64_tr_b16 v[80:81], v54 offset:30720\n\t" \
  "v_mul_f32 v45, v45, v50\n\t" \
  "v_mfma_f32_16x16x32_bf16 v[244:247], v[76:79], v[64:67], v[244:247]\n\t" \
  "ds_read_b64_tr_b16 v[82:83], v54 offset:31232\n\t" \
  "v_mul_f32 v49, v49, v51\n\t" \
  "v_mfma_f32_16x16x32_bf16 v[244:247], v[68:71], v[104:107], v[244:247]\n\t" \
  "ds_read_b128 v[124:127], v53 offset:10240\n\t" \
  "v_cvt_pk_bf16_f32 v45, v45, v49\n\t" \
  "v_mfma_f32_16x16x32_bf16 v[152:155], v[92:95], v[128:131], v[152:155]\n\t" \
  "ds_read_b128 v[96:99], v53 offset:12288\n\t" \
  "v_mfma_f32_16x16x32_bf16 v[152:155], v[112:115], v[120:123], v[152:155]\n\t" \
  "ds_read_b128 v[132:135], v52 offset:12288\n\t" \
  "v_mfma_f32_16x16x32_bf16 v[160:163], v[112:115], v[136:139], v[160:163]\n\t" \
  "ds_read_b128 v[108:111], v52 offset:14336\n\t" \
  "v_mfma_f32_16x16x32_bf16 v[160:163], v[92:95], v[144:147], v[160:163]\n\t" \
  "ds_read_b128 v[148:151], v53 offset:14336\n\t" \
  "v_mfma_f32_16x16x32_bf16 v[168:171], v[92:95], v[72:75], v[168:171]\n\t" \
  "ds_read_b128 v[64:67], v53 offset:16384\n\t" \
  "v_mfma_f32_16x16x32_bf16 v[168:171], v[112:115], v[140:143], v[168:171]\n\t" \
  "ds_read_b128 v[104:107], v52 offset:16384\n\t" \
  "v_mfma_f32_16x16x32_bf16 v[176:179], v[112:115], v[68:71], v[176:179]\n\t" \
  "ds_read_b64_tr_b16 v[112:113], v55 offset:26624\n\t" \
  "v_mfma_f32_16x16x32_bf16 v[176:179], v[92:95], v[76:79], v[176:179]\n\t" \
  "ds_read_b64_tr_b16 v[114:115], v55 offset:27136\n\t" \
  "v_mfma_f32_16x16x32_bf16 v[156:159], v[84:87], v[128:131], v[156:159]\n\t" \
  "ds_read_b64_tr_b16 v[92:93], v55 offset:30720\n\t" \
  "v_mfma_f32_16x16x32_bf16 v[156:159], v[116:119], v[120:123], v[156:159]\n\t" \
  "ds_read_b64_tr_b16 v[94:95], v55 offset:31232\n\t" \
  "v_mfma_f32_16x16x32_bf16 v[164:167], v[116:119], v[136:139], v[164:167]\n\t" \
  "ds_read_b64_tr_b16 v[128:129], v54 offset:34816\n\t" \
  "v_mfma_f32_16x16x32_bf16 v[164:167], v[84:87], v[144:147], v[164:167]\n\t" \
  "ds_read_b64_tr_b16 v[130:131], v54 offset:35328\n\t" \
  "v_mfma_f32_16x16x32_bf16 v[172:175], v[84:87], v[72:75], v[172:175]\n\t" \
  "ds_read_b64_tr_b16 v[120:121], v54 offset:38912\n\t" \
  "v_mfma_f32_16x16x32_bf16 v[172:175], v[116:119], v[140:143], v[172:175]\n\t" \
  "ds_read_b64_tr_b16 v[122:123], v54 offset:39424\n\t" \
  "v_mfma_f32_16x16x32_bf16 v[180:183], v[116:119], v[68:71], v[180:183]\n\t" \
  "ds_read_b128 v[136:139], v52 offset:0\n\t" \
  "v_mfma_f32_16x16x32_bf16 v[180:183], v[84:87], v[76:79], v[180:183]\n\t" \
  "ds_read_b128 v[144:147], v53 offset:0\n\t" \
  "s_waitcnt lgkmcnt(7)\n\t" \
  "v_mfma_f32_16x16x32_bf16 v[184:187], v[100:103], v[88:91], v[184:187]\n\t" \
  "ds_read_b64_tr_b16 v[72:73], v55 offset:34816\n\t" \
  "v_mfma_f32_16x16x32_bf16 v[184:187], v[80:83], v[124:127], v[184:187]\n\t" \
  "ds_read_b64_tr_b16 v[74:75], v55 offset:35328\n\t" \
  "v_mfma_f32_16x16x32_bf16 v[192:195], v[80:83], v[96:99], v[192:195]\n\t" \
  "ds_read_b64_tr_b16 v[140:141], v55 offset:38912\n\t" \
  "v_mfma_f32_16x16x32_bf16 v[192:195], v[100:103], v[132:135], v[192:195]\n\t" \
  "ds_read_b64_tr_b16 v[142:143], v55 offset:39424\n\t" \
  "v_mfma_f32_16x16x32_bf16 v[200:203], v[100:103], v[108:111], v[200:203]\n\t" \
  "ds_read_b64_tr_b16 v[68:69], v54 offset:18432\n\t" \
  "v_mfma_f32_16x16x32_bf16 v[200:203], v[80:83], v[148:151], v[200:203]\n\t" \
  "ds_read_b64_tr_b16 v[70:71], v54 offset:18944\n\t" \
  "v_mfma_f32_16x16x32_bf16 v[208:211], v[80:83], v[64:67], v[208:211]\n\t" \
  "ds_read_b64_tr_b16 v[116:117], v54 offset:22528\n\t" \
  "v_mfma_f32_16x16x32_bf16 v[208:211], v[100:103], v[104:107], v[208:211]\n\t" \
  "ds_read_b64_tr_b16 v[118:119], v54 offset:23040\n\t" \
  "v_mfma_f32_16x16x32_bf16 v[188:191], v[112:115], v[88:91], v[188:191]\n\t" \
  "ds_read_b64_tr_b16 v[76:77], v55 offset:18432\n\t" \
  "s_waitcnt lgkmcnt(7)\n\t" \
  "v_mfma_f32_16x16x32_bf16 v[188:191], v[92:95], v[124:127], v[188:191]\n\t" \
  "ds_read_b64_tr_b16 v[78:79], v55 offset:18944\n\t" \
  "v_mfma_f32_16x16x32_bf16 v[196:199], v[92:95], v[96:99], v[196:199]\n\t" \
  "ds_read_b64_tr_b16 v[84:85], v55 offset:22528\n\t" \
  "v_mfma_f32_16x16x32_bf16 v[196:199], v[112:115], v[132:135], v[196:199]\n\t" \
  "ds_read_b64_tr_b16 v[86:87], v55 offset:23040\n\t" \
  "v_mfma_f32_16x16x32_bf16 v[204:207], v[112:115], v[108:111], v[204:207]\n\t" \
  "v_mfma_f32_16x16x32_bf16 v[204:207], v[92:95], v[148:151], v[204:207]\n\t" \
  "v_mfma_f32_16x16x32_bf16 v[212:215], v[92:95], v[64:67], v[212:215]\n\t" \
  "v_mfma_f32_16x16x32_bf16 v[212:215], v[112:115], v[104:107], v[212:215]\n\t" \
  "v_mfma_f32_16x16x32_bf16 v[216:219], v[88:91], v[128:131], v[216:219]\n\t" \
  "v_mfma_f32_16x16x32_bf16 v[216:219], v[124:127], v[120:123], v[216:219]\n\t" \
  "v_mfma_f32_16x16x32_bf16 v[224:227], v[96:99], v[120:123], v[224:227]\n\t" \
  "v_mfma_f32_16x16x32_bf16 v[224:227], v[132:135], v[128:131], v[224:227]\n\t" \
  "v_mfma_f32_16x16x32_bf16 v[232:235], v[108:111], v[128:131], v[232:235]\n\t" \
  "global_store_dwordx4 v2, v[42:45], %24 offset:1024 sc1\n\t" \
  "v_mfma_f32_16x16x32_bf16 v[232:235], v[148:151], v[120:123], v[232:235]\n\t" \
  "v_mfma_f32_16x16x32_bf16 v[240:243], v[64:67], v[120:123], v[240:243]\n\t" \
  "v_mfma_f32_16x16x32_bf16 v[240:243], v[104:107], v[128:131], v[240:243]\n\t" \
  "v_mov_b32 v30, 0xff61b1e6\n\t" \
  "v_bfe_i32 v34, v3, 0, 1\n\t" \
  "v_mfma_f32_16x16x32_bf16 v[248:251], v[136:139], v[128:131], v[248:251]\n\t" \
  "v_and_b32 v34, v30, v34\n\t" \
  "v_bfe_i32 v38, v3, 4, 1\n\t" \
  "v_mfma_f32_16x16x32_bf16 v[248:251], v[144:147], v[120:123], v[248:251]\n\t" \
  "v_and_b32 v38, v30, v38\n\t" \
  "v_bfe_i32 v42, v3, 8, 1\n\t" \
  "v_mfma_f32_16x16x32_bf16 v[252:255], v[136:139], v[72:75], v[252:255]\n\t" \
  "v_bfi_b32 v42, v42, v30, %15\n\t" \
  "v_mov_b32 v48, 0x3f803f80\n\t" \
  "s_waitcnt lgkmcnt(0)\n\t" \
  "v_mfma_f32_16x16x32_bf16 v[252:255], v[144:147], v[140:143], v[252:255]\n\t" \
  "v_bfe_i32 v35, v3, 1, 1\n\t" \
  "v_and_b32 v35, v30, v35\n\t" \
  "v_mfma_f32_16x16x32_bf16 v[220:223], v[124:127], v[140:143], v[220:223]\n\t" \
  "v_bfe_i32 v39, v3, 5, 1\n\t" \
  "v_and_b32 v39, v30, v39\n\t" \
  "v_mfma_f32_16x16x32_bf16 v[220:223], v[88:91], v[72:75], v[220:223]\n\t" \
  "v_bfe_i32 v43, v3, 9, 1\n\t" \
  "v_bfi_b32 v43, v43, v30, %15\n\t" \
  "s_barrier\n\t" \
  "v_mfma_f32_16x16x32_bf16 v[228:231], v[132:135], v[72:75], v[228:231]\n\t" \
  "ds_read_b64_tr_b16 v[80:81], v58 offset:26624\n\t" \
  "v_mov_b32 v49, 0x3f803f80\n\t" \
  "v_mfma_f32_16x16x32_bf16 v[228:231], v[96:99], v[140:143], v[228:231]\n\t" \
  "ds_read_b64_tr_b16 v[82:83], v58 offset:27136\n\t" \
  "v_bfe_i32 v36, v3, 2, 1\n\t" \
  "v_mfma_f32_16x16x32_bf16 v[236:239], v[148:151], v[140:143], v[236:239]\n\t" \
  "ds_read_b128 v[100:103], v56 offset:10240\n\t" \
  "v_and_b32 v36, v30, v36\n\t" \
  "v_mfma_f32_16x16x32_bf16 v[236:239], v[108:111], v[72:75], v[236:239]\n\t" \
  "ds_read_b64_tr_b16 v[92:93], v58 offset:30720\n\t" \
  "v_bfe_i32 v40, v3, 6, 1\n\t" \
  "v_mfma_f32_16x16x32_bf16 v[244:247], v[104:107], v[72:75], v[244:247]\n\t" \
  "ds_read_b64_tr_b16 v[94:95], v58 offset:31232\n\t" \
  "v_and_b32 v40, v30, v40\n\t" \
  "v_mfma_f32_16x16x32_bf16 v[244:247], v[64:67], v[140:143], v[244:247]\n\t" \
  "ds_read_b128 v[112:115], v57 offset:10240\n\t" \
  "v_bfe_i32 v44, v3, 10, 1\n\t" \
  "v_mfma_f32_16x16x32_bf16 v[152:155], v[68:71], v[88:91], v[152:155]\n\t" \
  "ds_read_b128 v[128:131], v57 offset:12288\n\t" \
  "v_bfi_b32 v44, v44, v30, %15\n\t" \
  "v_mfma_f32_16x16x32_bf16 v[152:155], v[116:119], v[124:127], v[152:155]\n\t" \
  "ds_read_b128 v[120:123], v56 offset:12288\n\t" \
  "v_mov_b32 v50, 0x3f803f80\n\t" \
  "v_mfma_f32_16x16x32_bf16 v[160:163], v[116:119], v[96:99], v[160:163]\n\t" \
  "ds_read_b128 v[136:139], v56 offset:14336\n\t" \
  "v_bfe_i32 v37, v3, 3, 1\n\t" \
  "v_mfma_f32_16x16x32_bf16 v[160:163], v[68:71], v[132:135], v[160:163]\n\t" \
  "ds_read_b128 v[144:147], v57 offset:14336\n\t" \
  "v_and_b32 v37, v30, v37\n\t" \
  "v_mfma_f32_16x16x32_bf16 v[168:171], v[68:71], v[108:111], v[168:171]\n\t" \
  "ds_read_b128 v[72:75], v57 offset:16384\n\t" \
  "v_bfe_i32 v41, v3, 7, 1\n\t" \
  "v_mfma_f32_16x16x32_bf16 v[168:171], v[116:119], v[148:151], v[168:171]\n\t" \
  "ds_read_b128 v[140:143], v56 offset:16384\n\t" \
  "v_and_b32 v41, v30, v41\n\t" \
  "v_mfma_f32_16x16x32_bf16 v[176:179], v[116:119], v[64:67], v[176:179]\n\t" \
  "ds_read_b64_tr_b16 v[116:117], v59 offset:26624\n\t" \
  "v_bfe_i32 v45, v3, 11, 1\n\t" \
  "v_mfma_f32_16x16x32_bf16 v[176:179], v[68:71], v[104:107], v[176:179]\n\t" \
  "ds_read_b64_tr_b16 v[118:119], v59 offset:27136\n\t" \
  "v_bfi_b32 v45, v45, v30, %15\n\t" \
  "v_mfma_f32_16x16x32_bf16 v[156:159], v[76:79], v[88:91], v[156:159]\n\t" \
  "ds_read_b64_tr_b16 v[68:69], v59 offset:30720\n\t" \
  "v_mov_b32 v51, 0x3f803f80\n\t" \
  "v_mfma_f32_16x16x32_bf16 v[156:159], v[84:87], v[124:127], v[156:159]\n\t" \
  "ds_read_b64_tr_b16 v[70:71], v59 offset:31232\n\t" \
  "v_bfe_i32 v46, v3, 12, 1\n\t" \
  "v_mfma_f32_16x16x32_bf16 v[164:167], v[84:87], v[96:99], v[164:167]\n\t" \
  "ds_read_b64_tr_b16 v[88:89], v58 offset:34816\n\t" \
  "v_bfe_i32 v31, v3, 13, 1\n\t" \
  "v_mfma_f32_16x16x32_bf16 v[164:167], v[76:79], v[132:135], v[164:167]\n\t" \
  "ds_read_b64_tr_b16 v[90:91], v58 offset:35328\n\t" \
  "v_perm_b32 v46, v31, v46, vcc_hi\n\t" \
  "v_mfma_f32_16x16x32_bf16 v[172:175], v[76:79], v[108:111], v[172:175]\n\t" \
  "ds_read_b64_tr_b16 v[124:125], v58 offset:38912\n\t" \
  "v_bfe_i32 v47, v3, 14, 1\n\t" \
  "v_mfma_f32_16x16x32_bf16 v[172:175], v[84:87], v[148:151], v[172:175]\n\t" \
  "ds_read_b64_tr_b16 v[126:127], v58 offset:39424\n\t" \
  "v_bfe_i32 v31, v3, 15, 1\n\t" \
  "v_mfma_f32_16x16x32_bf16 v[180:183], v[84:87], v[64:67], v[180:183]\n\t" \
  "ds_read_b128 v[96:99], v56 offset:0\n\t" \
  "v_perm_b32 v47, v31, v47, vcc_hi\n\t" \
  "v_mfma_f32_16x16x32_bf16 v[180:183], v[76:79], v[104:107], v[180:183]\n\t" \
  "ds_read_b128 v[132:135], v57 offset:0\n\t" \
  "v_add_u32 v30, 18432, v4\n\t" \
  "s_waitcnt lgkmcnt(7)\n\t" \
  "v_mfma_f32_16x16x32_bf16 v[184:187], v[80:83], v[100:103], v[184:187]\n\t" \
  "ds_read_b64_tr_b16 v[108:109], v59 offset:34816\n\t" \
  "v_xor_b32 v31, 1024, v30\n\t" \
  "v_mfma_f32_16x16x32_bf16 v[184:187], v[92:95], v[112:115], v[184:187]\n\t" \
  "ds_read_b64_tr_b16 v[110:111], v59 offset:35328\n\t" \
  "v_mov_b32 v32, %12\n\t" \
  "v_mfma_f32_16x16x32_bf16 v[192:195], v[92:95], v[128:131], v[192:195]\n\t" \
  "ds_read_b64_tr_b16 v[148:149], v59 offset:38912\n\t" \
  "v_xor_b32 v33, 4, v32\n\t" \
  "v_mfma_f32_16x16x32_bf16 v[192:195], v[80:83], v[120:123], v[192:195]\n\t" \
  "ds_read_b64_tr_b16 v[150:151], v59 offset:39424\n\t" \
  "v_mov_b32 v0, 1\n\t" \
  "v_mfma_f32_16x16x32_bf16 v[200:203], v[80:83], v[136:139], v[200:203]\n\t" \
  "ds_read_b64_tr_b16 v[64:65], v58 offset:18432\n\t" \
  "v_and_b32 v4, 1023, v4\n\t" \
  "v_mfma_f32_16x16x32_bf16 v[200:203], v[92:95], v[144:147], v[200:203]\n\t" \
  "ds_read_b64_tr_b16 v[66:67], v58 offset:18944\n\t" \
  "v_add_u32 v1, 16384, v4\n\t" \
  "v_mfma_f32_16x16x32_bf16 v[208:211], v[92:95], v[72:75], v[208:211]\n\t" \
  "ds_read_b64_tr_b16 v[84:85], v58 offset:22528\n\t" \
  "v_mfma_f32_16x16x32_bf16 v[208:211], v[80:83], v[140:143], v[208:211]\n\t" \
  "ds_read_b64_tr_b16 v[86:87], v58 offset:23040\n\t" \
  "v_mfma_f32_16x16x32_bf16 v[188:191], v[116:119], v[100:103], v[188:191]\n\t" \
  "ds_read_b64_tr_b16 v[76:77], v59 offset:18432\n\t" \
  "s_waitcnt lgkmcnt(7)\n\t" \
  "v_mfma_f32_16x16x32_bf16 v[188:191], v[68:71], v[112:115], v[188:191]\n\t" \
  "ds_read_b64_tr_b16 v[78:79], v59 offset:18944\n\t" \
  "v_mfma_f32_16x16x32_bf16 v[196:199], v[68:71], v[128:131], v[196:199]\n\t" \
  "ds_read_b64_tr_b16 v[104:105], v59 offset:22528\n\t" \
  "v_mfma_f32_16x16x32_bf16 v[196:199], v[116:119], v[120:123], v[196:199]\n\t" \
  "ds_read_b64_tr_b16 v[106:107], v59 offset:23040\n\t" \
  "v_mfma_f32_16x16x32_bf16 v[204:207], v[116:119], v[136:139], v[204:207]\n\t" \
  "v_mfma_f32_16x16x32_bf16 v[204:207], v[68:71], v[144:147], v[204:207]\n\t" \
  "v_mfma_f32_16x16x32_bf16 v[212:215], v[68:71], v[72:75], v[212:215]\n\t" \
  "v_mfma_f32_16x16x32_bf16 v[212:215], v[116:119], v[140:143], v[212:215]\n\t" \
  "v_mfma_f32_16x16x32_bf16 v[216:219], v[100:103], v[88:91], v[216:219]\n\t" \
  "v_mfma_f32_16x16x32_bf16 v[216:219], v[112:115], v[124:127], v[216:219]\n\t" \
  "v_mfma_f32_16x16x32_bf16 v[224:227], v[128:131], v[124:127], v[224:227]\n\t" \
  "v_mfma_f32_16x16x32_bf16 v[224:227], v[120:123], v[88:91], v[224:227]\n\t" \
  "v_mfma_f32_16x16x32_bf16 v[232:235], v[136:139], v[88:91], v[232:235]\n\t" \
  "v_mfma_f32_16x16x32_bf16 v[232:235], v[144:147], v[124:127], v[232:235]\n\t" \
  "v_mfma_f32_16x16x32_bf16 v[240:243], v[72:75], v[124:127], v[240:243]\n\t" \
  "v_mfma_f32_16x16x32_bf16 v[240:243], v[140:143], v[88:91], v[240:243]\n\t" \
  "v_mfma_f32_16x16x32_bf16 v[248:251], v[96:99], v[88:91], v[248:251]\n\t" \
  "v_mfma_f32_16x16x32_bf16 v[248:251], v[132:135], v[124:127], v[248:251]\n\t" \
  "v_mfma_f32_16x16x32_bf16 v[252:255], v[96:99], v[108:111], v[252:255]\n\t" \
  "s_waitcnt lgkmcnt(0)\n\t" \
  "v_mfma_f32_16x16x32_bf16 v[252:255], v[132:135], v[148:151], v[252:255]\n\t" \
  "v_mfma_f32_16x16x32_bf16 v[220:223], v[112:115], v[148:151], v[220:223]\n\t" \
  "v_mfma_f32_16x16x32_bf16 v[220:223], v[100:103], v[108:111], v[220:223]\n\t" \
  "s_barrier\n\t" \
  "v_mfma_f32_16x16x32_bf16 v[228:231], v[120:123], v[108:111], v[228:231]\n\t" \
  "ds_read_b64_tr_b16 v[92:93], v62 offset:26624\n\t" \
  "v_mfma_f32_16x16x32_bf16 v[228:231], v[128:131], v[148:151], v[228:231]\n\t" \
  "ds_read_b64_tr_b16 v[94:95], v62 offset:27136\n\t" \
  "v_mfma_f32_16x16x32_bf16 v[236:239], v[144:147], v[148:151], v[236:239]\n\t" \
  "ds_read_b128 v[80:83], v60 offset:10240\n\t" \
  "v_mfma_f32_16x16x32_bf16 v[236:239], v[136:139], v[108:111], v[236:239]\n\t" \
  "ds_read_b64_tr_b16 v[68:69], v62 offset:30720\n\t" \
  "v_mfma_f32_16x16x32_bf16 v[244:247], v[140:143], v[108:111], v[244:247]\n\t" \
  "ds_read_b64_tr_b16 v[70:71], v62 offset:31232\n\t" \
  "v_mfma_f32_16x16x32_bf16 v[244:247], v[72:75], v[148:151], v[244:247]\n\t" \
  "ds_read_b128 v[116:119], v61 offset:10240\n\t" \
  "v_mfma_f32_16x16x32_bf16 v[152:155], v[64:67], v[100:103], v[152:155]\n\t" \
  "ds_read_b128 v[88:91], v61 offset:12288\n\t" \
  "v_mfma_f32_16x16x32_bf16 v[152:155], v[84:87], v[112:115], v[152:155]\n\t" \
  "ds_read_b128 v[124:127], v60 offset:12288\n\t" \
  "v_mfma_f32_16x16x32_bf16 v[160:163], v[84:87], v[128:131], v[160:163]\n\t" \
  "ds_read_b128 v[96:99], v60 offset:14336\n\t" \
  "v_mfma_f32_16x16x32_bf16 v[160:163], v[64:67], v[120:123], v[160:163]\n\t" \
  "ds_read_b128 v[132:135], v61 offset:14336\n\t" \
  "v_mfma_f32_16x16x32_bf16 v[168:171], v[64:67], v[136:139], v[168:171]\n\t" \
  "ds_read_b128 v[108:111], v61 offset:16384\n\t" \
  "v_mfma_f32_16x16x32_bf16 v[168:171], v[84:87], v[144:147], v[168:171]\n\t" \
  "ds_read_b128 v[148:151], v60 offset:16384\n\t" \
  "v_mfma_f32_16x16x32_bf16 v[176:179], v[84:87], v[72:75], v[176:179]\n\t" \
  "ds_read_b64_tr_b16 v[84:85], v63 offset:26624\n\t" \
  "v_mfma_f32_16x16x32_bf16 v[176:179], v[64:67], v[140:143], v[176:179]\n\t" \
  "ds_read_b64_tr_b16 v[86:87], v63 offset:27136\n\t" \
  "v_mfma_f32_16x16x32_bf16 v[156:159], v[76:79], v[100:103], v[156:159]\n\t" \
  "ds_read_b64_tr_b16 v[64:65], v63 offset:30720\n\t" \
  "v_mfma_f32_16x16x32_bf16 v[156:159], v[104:107], v[112:115], v[156:159]\n\t" \
  "ds_read_b64_tr_b16 v[66:67], v63 offset:31232\n\t" \
  "v_mfma_f32_16x16x32_bf16 v[164:167], v[104:107], v[128:131], v[164:167]\n\t" \
  "ds_read_b64_tr_b16 v[100:101], v62 offset:34816\n\t" \
  "v_mfma_f32_16x16x32_bf16 v[164:167], v[76:79], v[120:123], v[164:167]\n\t" \
  "ds_read_b64_tr_b16 v[102:103], v62 offset:35328\n\t" \
  "v_mfma_f32_16x16x32_bf16 v[172:175], v[76:79], v[136:139], v[172:175]\n\t" \
  "ds_read_b64_tr_b16 v[112:113], v62 offset:38912\n\t" \
  "v_mfma_f32_16x16x32_bf16 v[172:175], v[104:107], v[144:147], v[172:175]\n\t" \
  "ds_read_b64_tr_b16 v[114:115], v62 offset:39424\n\t" \
  "v_mfma_f32_16x16x32_bf16 v[180:183], v[104:107], v[72:75], v[180:183]\n\t" \
  "ds_read_b128 v[128:131], v60 offset:0\n\t" \
  "v_mfma_f32_16x16x32_bf16 v[180:183], v[76:79], v[140:143], v[180:183]\n\t" \
  "ds_read_b128 v[120:123], v61 offset:0\n\t" \
  "s_waitcnt lgkmcnt(7)\n\t" \
  "v_mfma_f32_16x16x32_bf16 v[184:187], v[92:95], v[80:83], v[184:187]\n\t" \
  "ds_read_b64_tr_b16 v[136:137], v63 offset:34816\n\t" \
  "v_mfma_f32_16x16x32_bf16 v[184:187], v[68:71], v[116:119], v[184:187]\n\t" \
  "ds_read_b64_tr_b16 v[138:139], v63 offset:35328\n\t" \
  "v_mfma_f32_16x16x32_bf16 v[192:195], v[68:71], v[88:91], v[192:195]\n\t" \
  "ds_read_b64_tr_b16 v[144:145], v63 offset:38912\n\t" \
  "v_mfma_f32_16x16x32_bf16 v[192:195], v[92:95], v[124:127], v[192:195]\n\t" \
  "ds_read_b64_tr_b16 v[146:147], v63 offset:39424\n\t" \
  "v_mfma_f32_16x16x32_bf16 v[200:203], v[92:95], v[96:99], v[200:203]\n\t" \
  "ds_read_b64_tr_b16 v[72:73], v62 offset:18432\n\t" \
  "v_mfma_f32_16x16x32_bf16 v[200:203], v[68:71], v[132:135], v[200:203]\n\t" \
  "ds_read_b64_tr_b16 v[74:75], v62 offset:18944\n\t" \
  "v_mfma_f32_16x16x32_bf16 v[208:211], v[68:71], v[108:111], v[208:211]\n\t" \
  "ds_read_b64_tr_b16 v[104:105], v62 offset:22528\n\t" \
  "v_mfma_f32_16x16x32_bf16 v[208:211], v[92:95], v[148:151], v[208:211]\n\t" \
  "ds_read_b64_tr_b16 v[106:107], v62 offset:23040\n\t" \
  "v_mfma_f32_16x16x32_bf16 v[188:191], v[84:87], v[80:83], v[188:191]\n\t" \
  "ds_read_b64_tr_b16 v[76:77], v63 offset:18432\n\t" \
  "s_waitcnt lgkmcnt(7)\n\t" \
  "v_mfma_f32_16x16x32_bf16 v[188:191], v[64:67], v[116:119], v[188:191]\n\t" \
  "ds_read_b64_tr_b16 v[78:79], v63 offset:18944\n\t" \
  "v_mfma_f32_16x16x32_bf16 v[196:199], v[64:67], v[88:91], v[196:199]\n\t" \
  "ds_read_b64_tr_b16 v[140:141], v63 offset:22528\n\t" \
  "v_mfma_f32_16x16x32_bf16 v[196:199], v[84:87], v[124:127], v[196:199]\n\t" \
  "ds_read_b64_tr_b16 v[142:143], v63 offset:23040\n\t" \
  "v_mfma_f32_16x16x32_bf16 v[204:207], v[84:87], v[96:99], v[204:207]\n\t" \
  "v_mfma_f32_16x16x32_bf16 v[204:207], v[64:67], v[132:135], v[204:207]\n\t" \
  "v_mfma_f32_16x16x32_bf16 v[212:215], v[64:67], v[108:111], v[212:215]\n\t" \
  "v_mfma_f32_16x16x32_bf16 v[212:215], v[84:87], v[148:151], v[212:215]\n\t" \
  "v_mfma_f32_16x16x32_bf16 v[216:219], v[80:83], v[100:103], v[216:219]\n\t" \
  "501:\n\t" \
  "s_cbranch_execz 505f\n\t" \
  "v_mfma_f32_16x16x32_bf16 v[216:219], v[116:119], v[112:115], v[216:219]\n\t" \
  "v_mfma_f32_16x16x32_bf16 v[224:227], v[88:91], v[112:115], v[224:227]\n\t" \
  "v_mfma_f32_16x16x32_bf16 v[224:227], v[124:127], v[100:103], v[224:227]\n\t" \
  "v_mfma_f32_16x16x32_bf16 v[232:235], v[96:99], v[100:103], v[232:235]\n\t" \
  "v_mfma_f32_16x16x32_bf16 v[232:235], v[132:135], v[112:115], v[232:235]\n\t" \
  "v_mfma_f32_16x16x32_bf16 v[240:243], v[108:111], v[112:115], v[240:243]\n\t" \
  "v_mfma_f32_16x16x32_bf16 v[240:243], v[148:151], v[100:103], v[240:243]\n\t" \
  "v_mfma_f32_16x16x32_bf16 v[248:251], v[128:131], v[100:103], v[248:251]\n\t" \
  "v_mfma_f32_16x16x32_bf16 v[248:251], v[120:123], v[112:115], v[248:251]\n\t" \
  "v_mfma_f32_16x16x32_bf16 v[252:255], v[128:131], v[136:139], v[252:255]\n\t" \
  "s_waitcnt lgkmcnt(0)\n\t" \
  "v_mfma_f32_16x16x32_bf16 v[252:255], v[120:123], v[144:147], v[252:255]\n\t" \
  "v_mfma_f32_16x16x32_bf16 v[220:223], v[116:119], v[144:147], v[220:223]\n\t" \
  "v_mfma_f32_16x16x32_bf16 v[220:223], v[80:83], v[136:139], v[220:223]\n\t" \
  "s_waitcnt vmcnt(0)\n\t" \
  "s_barrier\n\t" \
  "v_mfma_f32_16x16x32_bf16 v[228:231], v[124:127], v[136:139], v[228:231]\n\t" \
  "ds_read_b64_tr_b16 v[68:69], v54 offset:26624\n\t" \
  "v_mfma_f32_16x16x32_bf16 v[228:231], v[88:91], v[144:147], v[228:231]\n\t" \
  "ds_read_b64_tr_b16 v[70:71], v54 offset:27136\n\t" \
  "v_mfma_f32_16x16x32_bf16 v[236:239], v[132:135], v[144:147], v[236:239]\n\t" \
  "ds_read_b128 v[92:95], v52 offset:10240\n\t" \
  "v_mfma_f32_16x16x32_bf16 v[236:239], v[96:99], v[136:139], v[236:239]\n\t" \
  "ds_read_b64_tr_b16 v[64:65], v54 offset:30720\n\t" \
  "v_mfma_f32_16x16x32_bf16 v[244:247], v[148:151], v[136:139], v[244:247]\n\t" \
  "ds_read_b64_tr_b16 v[66:67], v54 offset:31232\n\t" \
  "v_mfma_f32_16x16x32_bf16 v[244:247], v[108:111], v[144:147], v[244:247]\n\t" \
  "ds_read_b128 v[84:87], v53 offset:10240\n\t" \
  "v_mfma_f32_16x16x32_bf16 v[152:155], v[72:75], v[80:83], v[152:155]\n\t" \
  "ds_read_b128 v[100:103], v53 offset:12288\n\t" \
  "v_mfma_f32_16x16x32_bf16 v[152:155], v[104:107], v[116:119], v[152:155]\n\t" \
  "ds_read_b128 v[112:115], v52 offset:12288\n\t" \
  "v_mfma_f32_16x16x32_bf16 v[160:163], v[104:107], v[88:91], v[160:163]\n\t" \
  "ds_read_b128 v[128:131], v52 offset:14336\n\t" \
  "v_mfma_f32_16x16x32_bf16 v[160:163], v[72:75], v[124:127], v[160:163]\n\t" \
  "ds_read_b128 v[120:123], v53 offset:14336\n\t" \
  "v_mfma_f32_16x16x32_bf16 v[168:171], v[72:75], v[96:99], v[168:171]\n\t" \
  "ds_read_b128 v[136:139], v53 offset:16384\n\t" \
  "v_mfma_f32_16x16x32_bf16 v[168:171], v[104:107], v[132:135], v[168:171]\n\t" \
  "ds_read_b128 v[144:147], v52 offset:16384\n\t" \
  "v_mfma_f32_16x16x32_bf16 v[176:179], v[104:107], v[108:111], v[176:179]\n\t" \
  "ds_read_b64_tr_b16 v[104:105], v55 offset:26624\n\t" \
  "v_mfma_f32_16x16x32_bf16 v[176:179], v[72:75], v[148:151], v[176:179]\n\t" \
  "ds_read_b64_tr_b16 v[106:107], v55 offset:27136\n\t" \
  "v_mfma_f32_16x16x32_bf16 v[156:159], v[76:79], v[80:83], v[156:159]\n\t" \
  "ds_read_b64_tr_b16 v[72:73], v55 offset:30720\n\t" \
  "v_mfma_f32_16x16x32_bf16 v[156:159], v[140:143], v[116:119], v[156:159]\n\t" \
  "ds_read_b64_tr_b16 v[74:75], v55 offset:31232\n\t" \
  "v_mfma_f32_16x16x32_bf16 v[164:167], v[140:143], v[88:91], v[164:167]\n\t" \
  "ds_read_b64_tr_b16 v[80:81], v54 offset:34816\n\t" \
  "v_mfma_f32_16x16x32_bf16 v[164:167], v[76:79], v[124:127], v[164:167]\n\t" \
  "ds_read_b64_tr_b16 v[82:83], v54 offset:35328\n\t" \
  "v_mfma_f32_16x16x32_bf16 v[172:175], v[76:79], v[96:99], v[172:175]\n\t" \
  "ds_read_b64_tr_b16 v[116:117], v54 offset:38912\n\t" \
  "v_mfma_f32_16x16x32_bf16 v[172:175], v[140:143], v[132:135], v[172:175]\n\t" \
  "ds_read_b64_tr_b16 v[118:119], v54 offset:39424\n\t" \
  "v_mfma_f32_16x16x32_bf16 v[180:183], v[140:143], v[108:111], v[180:183]\n\t" \
  "ds_read_b128 v[88:91], v52 offset:0\n\t" \
  "v_mfma_f32_16x16x32_bf16 v[180:183], v[76:79], v[148:151], v[180:183]\n\t" \
  "ds_read_b128 v[124:127], v53 offset:0\n\t" \
  "s_waitcnt lgkmcnt(7)\n\t" \
  "v_mfma_f32_16x16x32_bf16 v[184:187], v[68:71], v[92:95], v[184:187]\n\t" \
  "ds_read_b64_tr_b16 v[96:97], v55 offset:34816\n\t" \
  "v_mfma_f32_16x16x32_bf16 v[184:187], v[64:67], v[84:87], v[184:187]\n\t" \
  "ds_read_b64_tr_b16 v[98:99], v55 offset:35328\n\t" \
  "v_mfma_f32_16x16x32_bf16 v[192:195], v[64:67], v[100:103], v[192:195]\n\t" \
  "ds_read_b64_tr_b16 v[132:133], v55 offset:38912\n\t" \
  "v_mfma_f32_16x16x32_bf16 v[192:195], v[68:71], v[112:115], v[192:195]\n\t" \
  "ds_read_b64_tr_b16 v[134:135], v55 offset:39424\n\t" \
  "v_mfma_f32_16x16x32_bf16 v[200:203], v[68:71], v[128:131], v[200:203]\n\t" \
  "ds_read_b64_tr_b16 v[108:109], v54 offset:18432\n\t" \
  "v_mfma_f32_16x16x32_bf16 v[200:203], v[64:67], v[120:123], v[200:203]\n\t" \
  "ds_read_b64_tr_b16 v[110:111], v54 offset:18944\n\t" \
  "v_mfma_f32_16x16x32_bf16 v[208:211], v[64:67], v[136:139], v[208:211]\n\t" \
  "ds_read_b64_tr_b16 v[140:141], v54 offset:22528\n\t" \
  "v_mfma_f32_16x16x32_bf16 v[208:211], v[68:71], v[144:147], v[208:211]\n\t" \
  "ds_read_b64_tr_b16 v[142:143], v54 offset:23040\n\t" \
  "v_mfma_f32_16x16x32_bf16 v[188:191], v[104:107], v[92:95], v[188:191]\n\t" \
  "ds_read_b64_tr_b16 v[76:77], v55 offset:18432\n\t" \
  "s_waitcnt lgkmcnt(7)\n\t" \
  "v_mfma_f32_16x16x32_bf16 v[188:191], v[72:75], v[84:87], v[188:191]\n\t" \
  "ds_read_b64_tr_b16 v[78:79], v55 offset:18944\n\t" \
  "v_mfma_f32_16x16x32_bf16 v[196:199], v[72:75], v[100:103], v[196:199]\n\t" \
  "ds_read_b64_tr_b16 v[148:149], v55 offset:22528\n\t" \
  "v_mfma_f32_16x16x32_bf16 v[196:199], v[104:107], v[112:115], v[196:199]\n\t" \
  "ds_read_b64_tr_b16 v[150:151], v55 offset:23040\n\t" \
  "v_mfma_f32_16x16x32_bf16 v[204:207], v[104:107], v[128:131], v[204:207]\n\t" \
  "v_mfma_f32_16x16x32_bf16 v[204:207], v[72:75], v[120:123], v[204:207]\n\t" \
  "v_mfma_f32_16x16x32_bf16 v[212:215], v[72:75], v[136:139], v[212:215]\n\t" \
  "v_mfma_f32_16x16x32_bf16 v[212:215], v[104:107], v[144:147], v[212:215]\n\t" \
  "v_mfma_f32_16x16x32_bf16 v[216:219], v[92:95], v[80:83], v[216:219]\n\t" \
  "v_mfma_f32_16x16x32_bf16 v[216:219], v[84:87], v[116:119], v[216:219]\n\t" \
  "v_mfma_f32_16x16x32_bf16 v[224:227], v[100:103], v[116:119], v[224:227]\n\t" \
  "v_mfma_f32_16x16x32_bf16 v[224:227], v[112:115], v[80:83], v[224:227]\n\t" \
  "v_mfma_f32_16x16x32_bf16 v[232:235], v[128:131], v[80:83], v[232:235]\n\t" \
  "v_mfma_f32_16x16x32_bf16 v[232:235], v[120:123], v[116:119], v[232:235]\n\t" \
  "v_mfma_f32_16x16x32_bf16 v[240:243], v[136:139], v[116:119], v[240:243]\n\t" \
  "v_mfma_f32_16x16x32_bf16 v[240:243], v[144:147], v[80:83], v[240:243]\n\t" \
  "v_mfma_f32_16x16x32_bf16 v[248:251], v[88:91], v[80:83], v[248:251]\n\t" \
  "v_mfma_f32_16x16x32_bf16 v[248:251], v[124:127], v[116:119], v[248:251]\n\t" \
  "v_mfma_f32_16x16x32_bf16 v[252:255], v[88:91], v[96:99], v[252:255]\n\t" \
  "s_waitcnt lgkmcnt(0)\n\t" \
  "v_mfma_f32_16x16x32_bf16 v[252:255], v[124:127], v[132:135], v[252:255]\n\t" \
  "v_mfma_f32_16x16x32_bf16 v[220:223], v[84:87], v[132:135], v[220:223]\n\t" \
  "v_mfma_f32_16x16x32_bf16 v[220:223], v[92:95], v[96:99], v[220:223]\n\t" \
  "s_barrier\n\t" \
  "v_mfma_f32_16x16x32_bf16 v[228:231], v[112:115], v[96:99], v[228:231]\n\t" \
  "ds_read_b64_tr_b16 v[64:65], v58 offset:26624\n\t" \
  "v_mfma_f32_16x16x32_bf16 v[228:231], v[100:103], v[132:135], v[228:231]\n\t" \
  "ds_read_b64_tr_b16 v[66:67], v58 offset:27136\n\t" \
  "v_mfma_f32_16x16x32_bf16 v[236:239], v[120:123], v[132:135], v[236:239]\n\t" \
  "ds_read_b128 v[68:71], v56 offset:10240\n\t" \
  "v_mfma_f32_16x16x32_bf16 v[236:239], v[128:131], v[96:99], v[236:239]\n\t" \
  "ds_read_b64_tr_b16 v[72:73], v58 offset:30720\n\t" \
  "v_mfma_f32_16x16x32_bf16 v[244:247], v[144:147], v[96:99], v[244:247]\n\t" \
  "ds_read_b64_tr_b16 v[74:75], v58 offset:31232\n\t" \
  "v_mfma_f32_16x16x32_bf16 v[244:247], v[136:139], v[132:135], v[244:247]\n\t" \
  "ds_read_b128 v[104:107], v57 offset:10240\n\t" \
  "v_mfma_f32_16x16x32_bf16 v[152:155], v[108:111], v[92:95], v[152:155]\n\t" \
  "ds_read_b128 v[80:83], v57 offset:12288\n\t" \
  "v_mfma_f32_16x16x32_bf16 v[152:155], v[140:143], v[84:87], v[152:155]\n\t" \
  "ds_read_b128 v[116:119], v56 offset:12288\n\t" \
  "v_mfma_f32_16x16x32_bf16 v[160:163], v[140:143], v[100:103], v[160:163]\n\t" \
  "ds_read_b128 v[88:91], v56 offset:14336\n\t" \
  "v_mfma_f32_16x16x32_bf16 v[160:163], v[108:111], v[112:115], v[160:163]\n\t" \
  "ds_read_b128 v[124:127], v57 offset:14336\n\t" \
  "v_mfma_f32_16x16x32_bf16 v[168:171], v[108:111], v[128:131], v[168:171]\n\t" \
  "ds_read_b128 v[96:99], v57 offset:16384\n\t" \
  "v_mfma_f32_16x16x32_bf16 v[168:171], v[140:143], v[120:123], v[168:171]\n\t" \
  "ds_read_b128 v[132:135], v56 offset:16384\n\t" \
  "v_mfma_f32_16x16x32_bf16 v[176:179], v[140:143], v[136:139], v[176:179]\n\t" \
  "ds_read_b64_tr_b16 v[140:141], v59 offset:26624\n\t" \
  "v_mfma_f32_16x16x32_bf16 v[176:179], v[108:111], v[144:147], v[176:179]\n\t" \
  "ds_read_b64_tr_b16 v[142:143], v59 offset:27136\n\t" \
  "v_mfma_f32_16x16x32_bf16 v[156:159], v[76:79], v[92:95], v[156:159]\n\t" \
  "ds_read_b64_tr_b16 v[108:109], v59 offset:30720\n\t" \
  "v_mfma_f32_16x16x32_bf16 v[156:159], v[148:151], v[84:87], v[156:159]\n\t" \
  "ds_read_b64_tr_b16 v[110:111], v59 offset:31232\n\t" \
  "v_mfma_f32_16x16x32_bf16 v[164:167], v[148:151], v[100:103], v[164:167]\n\t" \
  "ds_read_b64_tr_b16 v[92:93], v58 offset:34816\n\t" \
  "v_mfma_f32_16x16x32_bf16 v[164:167], v[76:79], v[112:115], v[164:167]\n\t" \
  "ds_read_b64_tr_b16 v[94:95], v58 offset:35328\n\t" \
  "v_mfma_f32_16x16x32_bf16 v[172:175], v[76:79], v[128:131], v[172:175]\n\t" \
  "ds_read_b64_tr_b16 v[84:85], v58 offset:38912\n\t" \
  "v_mfma_f32_16x16x32_bf16 v[172:175], v[148:151], v[120:123], v[172:175]\n\t" \
  "ds_read_b64_tr_b16 v[86:87], v58 offset:39424\n\t" \
  "v_mfma_f32_16x16x32_bf16 v[180:183], v[148:151], v[136:139], v[180:183]\n\t" \
  "ds_read_b128 v[100:103], v56 offset:0\n\t" \
  "v_mfma_f32_16x16x32_bf16 v[180:183], v[76:79], v[144:147], v[180:183]\n\t" \
  "ds_read_b128 v[112:115], v57 offset:0\n\t" \
  "s_waitcnt lgkmcnt(7)\n\t" \
  "v_mfma_f32_16x16x32_bf16 v[184:187], v[64:67], v[68:71], v[184:187]\n\t" \
  "ds_read_b64_tr_b16 v[128:129], v59 offset:34816\n\t" \
  "v_mfma_f32_16x16x32_bf16 v[184:187], v[72:75], v[104:107], v[184:187]\n\t" \
  "ds_read_b64_tr_b16 v[130:131], v59 offset:35328\n\t" \
  "v_mfma_f32_16x16x32_bf16 v[192:195], v[72:75], v[80:83], v[192:195]\n\t" \
  "ds_read_b64_tr_b16 v[120:121], v59 offset:38912\n\t" \
  "v_mfma_f32_16x16x32_bf16 v[192:195], v[64:67], v[116:119], v[192:195]\n\t" \
  "ds_read_b64_tr_b16 v[122:123], v59 offset:39424\n\t" \
  "v_mfma_f32_16x16x32_bf16 v[200:203], v[64:67], v[88:91], v[200:203]\n\t" \
  "ds_read_b64_tr_b16 v[136:137], v58 offset:18432\n\t" \
  "v_mfma_f32_16x16x32_bf16 v[200:203], v[72:75], v[124:127], v[200:203]\n\t" \
  "ds_read_b64_tr_b16 v[138:139], v58 offset:18944\n\t" \
  "v_mfma_f32_16x16x32_bf16 v[208:211], v[72:75], v[96:99], v[208:211]\n\t" \
  "ds_read_b64_tr_b16 v[148:149], v58 offset:22528\n\t" \
  "v_mfma_f32_16x16x32_bf16 v[208:211], v[64:67], v[132:135], v[208:211]\n\t" \
  "ds_read_b64_tr_b16 v[150:151], v58 offset:23040\n\t" \
  "v_mfma_f32_16x16x32_bf16 v[188:191], v[140:143], v[68:71], v[188:191]\n\t" \
  "ds_read_b64_tr_b16 v[76:77], v59 offset:18432\n\t" \
  "s_waitcnt lgkmcnt(7)\n\t" \
  "v_mfma_f32_16x16x32_bf16 v[188:191], v[108:111], v[104:107], v[188:191]\n\t" \
  "ds_read_b64_tr_b16 v[78:79], v59 offset:18944\n\t" \
  "v_mfma_f32_16x16x32_bf16 v[196:199], v[108:111], v[80:83], v[196:199]\n\t" \
  "ds_read_b64_tr_b16 v[144:145], v59 offset:22528\n\t" \
  "v_mfma_f32_16x16x32_bf16 v[196:199], v[140:143], v[116:119], v[196:199]\n\t" \
  "ds_read_b64_tr_b16 v[146:147], v59 offset:23040\n\t" \
  "v_mfma_f32_16x16x32_bf16 v[204:207], v[140:143], v[88:91], v[204:207]\n\t" \
  "v_mfma_f32_16x16x32_bf16 v[204:207], v[108:111], v[124:127], v[204:207]\n\t" \
  "v_mfma_f32_16x16x32_bf16 v[212:215], v[108:111], v[96:99], v[212:215]\n\t" \
  "v_mfma_f32_16x16x32_bf16 v[212:215], v[140:143], v[132:135], v[212:215]\n\t" \
  "v_mfma_f32_16x16x32_bf16 v[216:219], v[68:71], v[92:95], v[216:219]\n\t" \
  "v_mfma_f32_16x16x32_bf16 v[216:219], v[104:107], v[84:87], v[216:219]\n\t" \
  "v_mfma_f32_16x16x32_bf16 v[224:227], v[80:83], v[84:87], v[224:227]\n\t" \
  "v_mfma_f32_16x16x32_bf16 v[224:227], v[116:119], v[92:95], v[224:227]\n\t" \
  "v_mfma_f32_16x16x32_bf16 v[232:235], v[88:91], v[92:95], v[232:235]\n\t" \
  "v_mfma_f32_16x16x32_bf16 v[232:235], v[124:127], v[84:87], v[232:235]\n\t" \
  "v_mfma_f32_16x16x32_bf16 v[240:243], v[96:99], v[84:87], v[240:243]\n\t" \
  "v_mfma_f32_16x16x32_bf16 v[240:243], v[132:135], v[92:95], v[240:243]\n\t" \
  "v_mfma_f32_16x16x32_bf16 v[248:251], v[100:103], v[92:95], v[248:251]\n\t" \
  "v_mfma_f32_16x16x32_bf16 v[248:251], v[112:115], v[84:87], v[248:251]\n\t" \
  "v_mfma_f32_16x16x32_bf16 v[252:255], v[100:103], v[128:131], v[252:255]\n\t" \
  "s_waitcnt lgkmcnt(0)\n\t" \
  "v_mfma_f32_16x16x32_bf16 v[252:255], v[112:115], v[120:123], v[252:255]\n\t" \
  "v_mfma_f32_16x16x32_bf16 v[220:223], v[104:107], v[120:123], v[220:223]\n\t" \
  "v_mfma_f32_16x16x32_bf16 v[220:223], v[68:71], v[128:131], v[220:223]\n\t" \
  "s_barrier\n\t" \
  "v_mfma_f32_16x16x32_bf16 v[228:231], v[116:119], v[128:131], v[228:231]\n\t" \
  "ds_read_b64_tr_b16 v[72:73], v62 offset:26624\n\t" \
  "v_mfma_f32_16x16x32_bf16 v[228:231], v[80:83], v[120:123], v[228:231]\n\t" \
  "ds_read_b64_tr_b16 v[74:75], v62 offset:27136\n\t" \
  "v_mfma_f32_16x16x32_bf16 v[236:239], v[124:127], v[120:123], v[236:239]\n\t" \
  "ds_read_b128 v[64:67], v60 offset:10240\n\t" \
  "v_mfma_f32_16x16x32_bf16 v[236:239], v[88:91], v[128:131], v[236:239]\n\t" \
  "ds_read_b64_tr_b16 v[108:109], v62 offset:30720\n\t" \
  "v_mfma_f32_16x16x32_bf16 v[244:247], v[132:135], v[128:131], v[244:247]\n\t" \
  "ds_read_b64_tr_b16 v[110:111], v62 offset:31232\n\t" \
  "v_mfma_f32_16x16x32_bf16 v[244:247], v[96:99], v[120:123], v[244:247]\n\t" \
  "ds_read_b128 v[140:143], v61 offset:10240\n\t" \
  "v_mfma_f32_16x16x32_bf16 v[152:155], v[136:139], v[68:71], v[152:155]\n\t" \
  "ds_read_b128 v[92:95], v61 offset:12288\n\t" \
  "v_mfma_f32_16x16x32_bf16 v[152:155], v[148:151], v[104:107], v[152:155]\n\t" \
  "ds_read_b128 v[84:87], v60 offset:12288\n\t" \
  "v_mfma_f32_16x16x32_bf16 v[160:163], v[148:151], v[80:83], v[160:163]\n\t" \
  "ds_read_b128 v[100:103], v60 offset:14336\n\t" \
  "v_mfma_f32_16x16x32_bf16 v[160:163], v[136:139], v[116:119], v[160:163]\n\t" \
  "ds_read_b128 v[112:115], v61 offset:14336\n\t" \
  "v_mfma_f32_16x16x32_bf16 v[168:171], v[136:139], v[88:91], v[168:171]\n\t" \
  "ds_read_b128 v[128:131], v61 offset:16384\n\t" \
  "v_mfma_f32_16x16x32_bf16 v[168:171], v[148:151], v[124:127], v[168:171]\n\t" \
  "ds_read_b128 v[120:123], v60 offset:16384\n\t" \
  "v_mfma_f32_16x16x32_bf16 v[176:179], v[148:151], v[96:99], v[176:179]\n\t" \
  "ds_read_b64_tr_b16 v[148:149], v63 offset:26624\n\t" \
  "v_mfma_f32_16x16x32_bf16 v[176:179], v[136:139], v[132:135], v[176:179]\n\t" \
  "ds_read_b64_tr_b16 v[150:151], v63 offset:27136\n\t" \
  "v_mfma_f32_16x16x32_bf16 v[156:159], v[76:79], v[68:71], v[156:159]\n\t" \
  "ds_read_b64_tr_b16 v[136:137], v63 offset:30720\n\t" \
  "v_mfma_f32_16x16x32_bf16 v[156:159], v[144:147], v[104:107], v[156:159]\n\t" \
  "ds_read_b64_tr_b16 v[138:139], v63 offset:31232\n\t" \
  "v_mfma_f32_16x16x32_bf16 v[164:167], v[144:147], v[80:83], v[164:167]\n\t" \
  "ds_read_b64_tr_b16 v[68:69], v62 offset:34816\n\t" \
  "v_mfma_f32_16x16x32_bf16 v[164:167], v[76:79], v[116:119], v[164:167]\n\t" \
  "ds_read_b64_tr_b16 v[70:71], v62 offset:35328\n\t" \
  "v_mfma_f32_16x16x32_bf16 v[172:175], v[76:79], v[88:91], v[172:175]\n\t" \
  "ds_read_b64_tr_b16 v[104:105], v62 offset:38912\n\t" \
  "v_mfma_f32_16x16x32_bf16 v[172:175], v[144:147], v[124:127], v[172:175]\n\t" \
  "ds_read_b64_tr_b16 v[106:107], v62 offset:39424\n\t" \
  "v_mfma_f32_16x16x32_bf16 v[180:183], v[144:147], v[96:99], v[180:183]\n\t" \
  "ds_read_b128 v[80:83], v60 offset:0\n\t" \
  "v_mfma_f32_16x16x32_bf16 v[180:183], v[76:79], v[132:135], v[180:183]\n\t" \
  "ds_read_b128 v[116:119], v61 offset:0\n\t" \
  "s_waitcnt lgkmcnt(7)\n\t" \
  "v_mfma_f32_16x16x32_bf16 v[184:187], v[72:75], v[64:67], v[184:187]\n\t" \
  "ds_read_b64_tr_b16 v[88:89], v63 offset:34816\n\t" \
  "v_mfma_f32_16x16x32_bf16 v[184:187], v[108:111], v[140:143], v[184:187]\n\t" \
  "ds_read_b64_tr_b16 v[90:91], v63 offset:35328\n\t" \
  "v_mfma_f32_16x16x32_bf16 v[192:195], v[108:111], v[92:95], v[192:195]\n\t" \
  "ds_read_b64_tr_b16 v[124:125], v63 offset:38912\n\t" \
  "v_mfma_f32_16x16x32_bf16 v[192:195], v[72:75], v[84:87], v[192:195]\n\t" \
  "ds_read_b64_tr_b16 v[126:127], v63 offset:39424\n\t" \
  "v_mfma_f32_16x16x32_bf16 v[200:203], v[72:75], v[100:103], v[200:203]\n\t" \
  "ds_read_b64_tr_b16 v[96:97], v62 offset:18432\n\t" \
  "v_mfma_f32_16x16x32_bf16 v[200:203], v[108:111], v[112:115], v[200:203]\n\t" \
  "ds_read_b64_tr_b16 v[98:99], v62 offset:18944\n\t" \
  "v_mfma_f32_16x16x32_bf16 v[208:211], v[108:111], v[128:131], v[208:211]\n\t" \
  "ds_read_b64_tr_b16 v[144:145], v62 offset:22528\n\t" \
  "v_mfma_f32_16x16x32_bf16 v[208:211], v[72:75], v[120:123], v[208:211]\n\t" \
  "ds_read_b64_tr_b16 v[146:147], v62 offset:23040\n\t" \
  "v_mfma_f32_16x16x32_bf16 v[188:191], v[148:151], v[64:67], v[188:191]\n\t" \
  "ds_read_b64_tr_b16 v[76:77], v63 offset:18432\n\t" \
  "s_waitcnt lgkmcnt(7)\n\t" \
  "v_mfma_f32_16x16x32_bf16 v[188:191], v[136:139], v[140:143], v[188:191]\n\t" \
  "ds_read_b64_tr_b16 v[78:79], v63 offset:18944\n\t" \
  "v_mfma_f32_16x16x32_bf16 v[196:199], v[136:139], v[92:95], v[196:199]\n\t" \
  "ds_read_b64_tr_b16 v[132:133], v63 offset:22528\n\t" \
  "v_mfma_f32_16x16x32_bf16 v[196:199], v[148:151], v[84:87], v[196:199]\n\t" \
  "ds_read_b64_tr_b16 v[134:135], v63 offset:23040\n\t" \
  "v_mfma_f32_16x16x32_bf16 v[204:207], v[148:151], v[100:103], v[204:207]\n\t" \
  "v_mfma_f32_16x16x32_bf16 v[204:207], v[136:139], v[112:115], v[204:207]\n\t" \
  "v_mfma_f32_16x16x32_bf16 v[212:215], v[136:139], v[128:131], v[212:215]\n\t" \
  "v_mfma_f32_16x16x32_bf16 v[212:215], v[148:151], v[120:123], v[212:215]\n\t" \
  "v_mfma_f32_16x16x32_bf16 v[216:219], v[64:67], v[68:71], v[216:219]\n\t" \
  "v_mfma_f32_16x16x32_bf16 v[216:219], v[140:143], v[104:107], v[216:219]\n\t" \
  "v_mfma_f32_16x16x32_bf16 v[224:227], v[92:95], v[104:107], v[224:227]\n\t" \
  "v_mfma_f32_16x16x32_bf16 v[224:227], v[84:87], v[68:71], v[224:227]\n\t" \
  "v_mfma_f32_16x16x32_bf16 v[232:235], v[100:103], v[68:71], v[232:235]\n\t" \
  "v_mfma_f32_16x16x32_bf16 v[232:235], v[112:115], v[104:107], v[232:235]\n\t" \
  "v_mfma_f32_16x16x32_bf16 v[240:243], v[128:131], v[104:107], v[240:243]\n\t" \
  "v_mfma_f32_16x16x32_bf16 v[240:243], v[120:123], v[68:71], v[240:243]\n\t" \
  "v_mfma_f32_16x16x32_bf16 v[248:251], v[80:83], v[68:71], v[248:251]\n\t" \
  "v_mfma_f32_16x16x32_bf16 v[248:251], v[116:119], v[104:107], v[248:251]\n\t" \
  "v_mfma_f32_16x16x32_bf16 v[252:255], v[80:83], v[88:91], v[252:255]\n\t" \
  "s_waitcnt lgkmcnt(0)\n\t" \
  "v_mfma_f32_16x16x32_bf16 v[252:255], v[116:119], v[124:127], v[252:255]\n\t" \
  "v_mfma_f32_16x16x32_bf16 v[220:223], v[140:143], v[124:127], v[220:223]\n\t" \
  "v_mfma_f32_16x16x32_bf16 v[220:223], v[64:67], v[88:91], v[220:223]\n\t" \
  "s_barrier\n\t" \
  "v_mfma_f32_16x16x32_bf16 v[228:231], v[84:87], v[88:91], v[228:231]\n\t" \
  "ds_read_b64_tr_b16 v[108:109], v54 offset:26624\n\t" \
  "v_mfma_f32_16x16x32_bf16 v[228:231], v[92:95], v[124:127], v[228:231]\n\t" \
  "ds_read_b64_tr_b16 v[110:111], v54 offset:27136\n\t" \
  "v_mfma_f32_16x16x32_bf16 v[236:239], v[112:115], v[124:127], v[236:239]\n\t" \
  "ds_read_b128 v[72:75], v52 offset:10240\n\t" \
  "v_mfma_f32_16x16x32_bf16 v[236:239], v[100:103], v[88:91], v[236:239]\n\t" \
  "ds_read_b64_tr_b16 v[136:137], v54 offset:30720\n\t" \
  "v_mfma_f32_16x16x32_bf16 v[244:247], v[120:123], v[88:91], v[244:247]\n\t" \
  "ds_read_b64_tr_b16 v[138:139], v54 offset:31232\n\t" \
  "v_mfma_f32_16x16x32_bf16 v[244:247], v[128:131], v[124:127], v[244:247]\n\t" \
  "ds_read_b128 v[148:151], v53 offset:10240\n\t" \
  "v_mfma_f32_16x16x32_bf16 v[152:155], v[96:99], v[64:67], v[152:155]\n\t" \
  "ds_read_b128 v[68:71], v53 offset:12288\n\t" \
  "v_mfma_f32_16x16x32_bf16 v[152:155], v[144:147], v[140:143], v[152:155]\n\t" \
  "ds_read_b128 v[104:107], v52 offset:12288\n\t" \
  "v_mfma_f32_16x16x32_bf16 v[160:163], v[144:147], v[92:95], v[160:163]\n\t" \
  "ds_read_b128 v[80:83], v52 offset:14336\n\t" \
  "v_mfma_f32_16x16x32_bf16 v[160:163], v[96:99], v[84:87], v[160:163]\n\t" \
  "ds_read_b128 v[116:119], v53 offset:14336\n\t" \
  "v_mfma_f32_16x16x32_bf16 v[168:171], v[96:99], v[100:103], v[168:171]\n\t" \
  "ds_read_b128 v[88:91], v53 offset:16384\n\t" \
  "v_mfma_f32_16x16x32_bf16 v[168:171], v[144:147], v[112:115], v[168:171]\n\t" \
  "ds_read_b128 v[124:127], v52 offset:16384\n\t" \
  "v_mfma_f32_16x16x32_bf16 v[176:179], v[144:147], v[128:131], v[176:179]\n\t" \
  "ds_read_b64_tr_b16 v[144:145], v55 offset:26624\n\t" \
  "v_mfma_f32_16x16x32_bf16 v[176:179], v[96:99], v[120:123], v[176:179]\n\t" \
  "ds_read_b64_tr_b16 v[146:147], v55 offset:27136\n\t" \
  "v_mfma_f32_16x16x32_bf16 v[156:159], v[76:79], v[64:67], v[156:159]\n\t" \
  "ds_read_b64_tr_b16 v[96:97], v55 offset:30720\n\t" \
  "v_mfma_f32_16x16x32_bf16 v[156:159], v[132:135], v[140:143], v[156:159]\n\t" \
  "ds_read_b64_tr_b16 v[98:99], v55 offset:31232\n\t" \
  "v_mfma_f32_16x16x32_bf16 v[164:167], v[132:135], v[92:95], v[164:167]\n\t" \
  "ds_read_b64_tr_b16 v[64:65], v54 offset:34816\n\t" \
  "v_mfma_f32_16x16x32_bf16 v[164:167], v[76:79], v[84:87], v[164:167]\n\t" \
  "ds_read_b64_tr_b16 v[66:67], v54 offset:35328\n\t" \
  "v_mfma_f32_16x16x32_bf16 v[172:175], v[76:79], v[100:103], v[172:175]\n\t" \
  "ds_read_b64_tr_b16 v[140:141], v54 offset:38912\n\t" \
  "v_mfma_f32_16x16x32_bf16 v[172:175], v[132:135], v[112:115], v[172:175]\n\t" \
  "ds_read_b64_tr_b16 v[142:143], v54 offset:39424\n\t" \
  "v_mfma_f32_16x16x32_bf16 v[180:183], v[132:135], v[128:131], v[180:183]\n\t" \
  "ds_read_b128 v[92:95], v52 offset:0\n\t" \
  "v_mfma_f32_16x16x32_bf16 v[180:183], v[76:79], v[120:123], v[180:183]\n\t" \
  "ds_read_b128 v[84:87], v53 offset:0\n\t" \
  "s_waitcnt lgkmcnt(7)\n\t" \
  "v_mfma_f32_16x16x32_bf16 v[184:187], v[108:111], v[72:75], v[184:187]\n\t" \
  "ds_read_b64_tr_b16 v[100:101], v55 offset:34816\n\t" \
  "v_mfma_f32_16x16x32_bf16 v[184:187], v[136:139], v[148:151], v[184:187]\n\t" \
  "ds_read_b64_tr_b16 v[102:103], v55 offset:35328\n\t" \
  "v_mfma_f32_16x16x32_bf16 v[192:195], v[136:139], v[68:71], v[192:195]\n\t" \
  "ds_read_b64_tr_b16 v[112:113], v55 offset:38912\n\t" \
  "v_mfma_f32_16x16x32_bf16 v[192:195], v[108:111], v[104:107], v[192:195]\n\t" \
  "ds_read_b64_tr_b16 v[114:115], v55 offset:39424\n\t" \
  "v_mfma_f32_16x16x32_bf16 v[200:203], v[108:111], v[80:83], v[200:203]\n\t" \
  "ds_read_b64_tr_b16 v[128:129], v54 offset:18432\n\t" \
  "v_mfma_f32_16x16x32_bf16 v[200:203], v[136:139], v[116:119], v[200:203]\n\t" \
  "ds_read_b64_tr_b16 v[130:131], v54 offset:18944\n\t" \
  "v_mfma_f32_16x16x32_bf16 v[208:211], v[136:139], v[88:91], v[208:211]\n\t" \
  "ds_read_b64_tr_b16 v[132:133], v54 offset:22528\n\t" \
  "v_mfma_f32_16x16x32_bf16 v[208:211], v[108:111], v[124:127], v[208:211]\n\t" \
  "ds_read_b64_tr_b16 v[134:135], v54 offset:23040\n\t" \
  "v_mfma_f32_16x16x32_bf16 v[188:191], v[144:147], v[72:75], v[188:191]\n\t" \
  "ds_read_b64_tr_b16 v[76:77], v55 offset:18432\n\t" \
  "s_waitcnt lgkmcnt(7)\n\t" \
  "v_mfma_f32_16x16x32_bf16 v[188:191], v[96:99], v[148:151], v[188:191]\n\t" \
  "ds_read_b64_tr_b16 v[78:79], v55 offset:18944\n\t" \
  "v_mfma_f32_16x16x32_bf16 v[196:199], v[96:99], v[68:71], v[196:199]\n\t" \
  "ds_read_b64_tr_b16 v[120:121], v55 offset:22528\n\t" \
  "v_mfma_f32_16x16x32_bf16 v[196:199], v[144:147], v[104:107], v[196:199]\n\t" \
  "ds_read_b64_tr_b16 v[122:123], v55 offset:23040\n\t" \
  "v_mfma_f32_16x16x32_bf16 v[204:207], v[144:147], v[80:83], v[204:207]\n\t" \
  "v_mfma_f32_16x16x32_bf16 v[204:207], v[96:99], v[116:119], v[204:207]\n\t" \
  "v_mfma_f32_16x16x32_bf16 v[212:215], v[96:99], v[88:91], v[212:215]\n\t" \
  "v_mfma_f32_16x16x32_bf16 v[212:215], v[144:147], v[124:127], v[212:215]\n\t" \
  "v_mfma_f32_16x16x32_bf16 v[216:219], v[72:75], v[64:67], v[216:219]\n\t" \
  "v_mfma_f32_16x16x32_bf16 v[216:219], v[148:151], v[140:143], v[216:219]\n\t" \
  "v_mfma_f32_16x16x32_bf16 v[224:227], v[68:71], v[140:143], v[224:227]\n\t" \
  "v_mfma_f32_16x16x32_bf16 v[224:227], v[104:107], v[64:67], v[224:227]\n\t" \
  "v_mfma_f32_16x16x32_bf16 v[232:235], v[80:83], v[64:67], v[232:235]\n\t" \
  "v_mfma_f32_16x16x32_bf16 v[232:235], v[116:119], v[140:143], v[232:235]\n\t" \
  "v_mfma_f32_16x16x32_bf16 v[240:243], v[88:91], v[140:143], v[240:243]\n\t" \
  "v_mfma_f32_16x16x32_bf16 v[240:243], v[124:127], v[64:67], v[240:243]\n\t" \
  "v_mfma_f32_16x16x32_bf16 v[248:251], v[92:95], v[64:67], v[248:251]\n\t" \
  "v_mfma_f32_16x16x32_bf16 v[248:251], v[84:87], v[140:143], v[248:251]\n\t" \
  "v_mfma_f32_16x16x32_bf16 v[252:255], v[92:95], v[100:103], v[252:255]\n\t" \
  "s_waitcnt lgkmcnt(0)\n\t" \
  "v_mfma_f32_16x16x32_bf16 v[252:255], v[84:87], v[112:115], v[252:255]\n\t" \
  "v_mfma_f32_16x16x32_bf16 v[220:223], v[148:151], v[112:115], v[220:223]\n\t" \
  "v_mfma_f32_16x16x32_bf16 v[220:223], v[72:75], v[100:103], v[220:223]\n\t" \
  "s_barrier\n\t" \
  "v_mfma_f32_16x16x32_bf16 v[228:231], v[104:107], v[100:103], v[228:231]\n\t" \
  "ds_read_b64_tr_b16 v[96:97], v58 offset:26624\n\t" \
  "v_mfma_f32_16x16x32_bf16 v[228:231], v[68:71], v[112:115], v[228:231]\n\t" \
  "ds_read_b64_tr_b16 v[98:99], v58 offset:27136\n\t" \
  "v_mfma_f32_16x16x32_bf16 v[236:239], v[116:119], v[112:115], v[236:239]\n\t" \
  "ds_read_b128 v[136:139], v56 offset:10240\n\t" \
  "v_mfma_f32_16x16x32_bf16 v[236:239], v[80:83], v[100:103], v[236:239]\n\t" \
  "ds_read_b64_tr_b16 v[64:65], v58 offset:30720\n\t" \
  "v_mfma_f32_16x16x32_bf16 v[244:247], v[124:127], v[100:103], v[244:247]\n\t" \
  "ds_read_b64_tr_b16 v[66:67], v58 offset:31232\n\t" \
  "v_mfma_f32_16x16x32_bf16 v[244:247], v[88:91], v[112:115], v[244:247]\n\t" \
  "ds_read_b128 v[108:111], v57 offset:10240\n\t" \
  "v_mfma_f32_16x16x32_bf16 v[152:155], v[128:131], v[72:75], v[152:155]\n\t" \
  "ds_read_b128 v[144:147], v57 offset:12288\n\t" \
  "v_mfma_f32_16x16x32_bf16 v[152:155], v[132:135], v[148:151], v[152:155]\n\t" \
  "ds_read_b128 v[140:143], v56 offset:12288\n\t" \
  "v_mfma_f32_16x16x32_bf16 v[160:163], v[132:135], v[68:71], v[160:163]\n\t" \
  "ds_read_b128 v[112:115], v56 offset:14336\n\t" \
  "v_mfma_f32_16x16x32_bf16 v[160:163], v[128:131], v[104:107], v[160:163]\n\t" \
  "ds_read_b64_tr_b16 v[92:93], v59 offset:26624\n\t" \
  "v_mfma_f32_16x16x32_bf16 v[168:171], v[128:131], v[80:83], v[168:171]\n\t" \
  "ds_read_b64_tr_b16 v[94:95], v59 offset:27136\n\t" \
  "v_mfma_f32_16x16x32_bf16 v[168:171], v[132:135], v[116:119], v[168:171]\n\t" \
  "ds_read_b64_tr_b16 v[84:85], v59 offset:30720\n\t" \
  "v_mfma_f32_16x16x32_bf16 v[176:179], v[132:135], v[88:91], v[176:179]\n\t" \
  "ds_read_b128 v[132:135], v57 offset:14336\n\t" \
  "v_mfma_f32_16x16x32_bf16 v[176:179], v[128:131], v[124:127], v[176:179]\n\t" \
  "ds_read_b128 v[128:131], v57 offset:16384\n\t" \
  "v_mfma_f32_16x16x32_bf16 v[156:159], v[76:79], v[72:75], v[156:159]\n\t" \
  "ds_read_b64_tr_b16 v[86:87], v59 offset:31232\n\t" \
  "v_mfma_f32_16x16x32_bf16 v[156:159], v[120:123], v[148:151], v[156:159]\n\t" \
  "ds_read_b128 v[148:151], v56 offset:16384\n\t" \
  "v_mfma_f32_16x16x32_bf16 v[164:167], v[120:123], v[68:71], v[164:167]\n\t" \
  "ds_read_b64_tr_b16 v[100:101], v58 offset:34816\n\t" \
  "v_mfma_f32_16x16x32_bf16 v[164:167], v[76:79], v[104:107], v[164:167]\n\t" \
  "ds_read_b64_tr_b16 v[102:103], v58 offset:35328\n\t" \
  "v_mfma_f32_16x16x32_bf16 v[172:175], v[76:79], v[80:83], v[172:175]\n\t" \
  "ds_read_b64_tr_b16 v[72:73], v58 offset:38912\n\t" \
  "v_mfma_f32_16x16x32_bf16 v[172:175], v[120:123], v[116:119], v[172:175]\n\t" \
  "ds_read_b64_tr_b16 v[74:75], v58 offset:39424\n\t" \
  "v_mfma_f32_16x16x32_bf16 v[180:183], v[120:123], v[88:91], v[180:183]\n\t" \
  "ds_read_b128 v[68:71], v56 offset:0\n\t" \
  "v_mfma_f32_16x16x32_bf16 v[180:183], v[76:79], v[124:127], v[180:183]\n\t" \
  "ds_read_b128 v[104:107], v57 offset:0\n\t" \
  "s_waitcnt lgkmcnt(7)\n\t" \
  "v_mfma_f32_16x16x32_bf16 v[184:187], v[96:99], v[136:139], v[184:187]\n\t" \
  "ds_read_b64_tr_b16 v[80:81], v59 offset:34816\n\t" \
  "v_mfma_f32_16x16x32_bf16 v[184:187], v[64:67], v[108:111], v[184:187]\n\t" \
  "ds_read_b64_tr_b16 v[82:83], v59 offset:35328\n\t" \
  "v_mfma_f32_16x16x32_bf16 v[192:195], v[64:67], v[144:147], v[192:195]\n\t" \
  "ds_read_b64_tr_b16 v[88:89], v59 offset:38912\n\t" \
  "v_mfma_f32_16x16x32_bf16 v[192:195], v[96:99], v[140:143], v[192:195]\n\t" \
  "ds_read_b64_tr_b16 v[90:91], v59 offset:39424\n\t" \
  "v_mfma_f32_16x16x32_bf16 v[200:203], v[96:99], v[112:115], v[200:203]\n\t" \
  "ds_read_b64_tr_b16 v[116:117], v58 offset:18432\n\t" \
  "v_mfma_f32_16x16x32_bf16 v[200:203], v[64:67], v[132:135], v[200:203]\n\t" \
  "ds_read_b64_tr_b16 v[118:119], v58 offset:18944\n\t" \
  "v_mfma_f32_16x16x32_bf16 v[208:211], v[64:67], v[128:131], v[208:211]\n\t" \
  "ds_read_b64_tr_b16 v[120:121], v58 offset:22528\n\t" \
  "s_waitcnt lgkmcnt(7)\n\t" \
  "v_mfma_f32_16x16x32_bf16 v[208:211], v[96:99], v[148:151], v[208:211]\n\t" \
  "ds_read_b64_tr_b16 v[122:123], v58 offset:23040\n\t" \
  "v_mfma_f32_16x16x32_bf16 v[188:191], v[92:95], v[136:139], v[188:191]\n\t" \
  "ds_read_b64_tr_b16 v[124:125], v59 offset:18432\n\t" \
  "v_mfma_f32_16x16x32_bf16 v[188:191], v[84:87], v[108:111], v[188:191]\n\t" \
  "ds_read_b64_tr_b16 v[126:127], v59 offset:18944\n\t" \
  "v_mfma_f32_16x16x32_bf16 v[196:199], v[84:87], v[144:147], v[196:199]\n\t" \
  "ds_read_b64_tr_b16 v[76:77], v59 offset:22528\n\t" \
  "v_mfma_f32_16x16x32_bf16 v[196:199], v[92:95], v[140:143], v[196:199]\n\t" \
  "ds_read_b64_tr_b16 v[78:79], v59 offset:23040\n\t" \
  "v_mfma_f32_16x16x32_bf16 v[204:207], v[92:95], v[112:115], v[204:207]\n\t" \
  "v_mfma_f32_16x16x32_bf16 v[204:207], v[84:87], v[132:135], v[204:207]\n\t" \
  "v_mfma_f32_16x16x32_bf16 v[212:215], v[84:87], v[128:131], v[212:215]\n\t" \
  "v_mfma_f32_16x16x32_bf16 v[212:215], v[92:95], v[148:151], v[212:215]\n\t" \
  "v_mfma_f32_16x16x32_bf16 v[216:219], v[136:139], v[100:103], v[216:219]\n\t" \
  "v_mfma_f32_16x16x32_bf16 v[216:219], v[108:111], v[72:75], v[216:219]\n\t" \
  "v_mfma_f32_16x16x32_bf16 v[224:227], v[144:147], v[72:75], v[224:227]\n\t" \
  "v_and_b32 v14, -16, v32\n\t" \
  "v_mfma_f32_16x16x32_bf16 v[224:227], v[140:143], v[100:103], v[224:227]\n\t" \
  "v_mfma_f32_16x16x32_bf16 v[232:235], v[112:115], v[100:103], v[232:235]\n\t" \
  "ds_read_b128 v[10:13], v14 offset:128\n\t" \
  "v_mfma_f32_16x16x32_bf16 v[232:235], v[132:135], v[72:75], v[232:235]\n\t" \
  "v_mfma_f32_16x16x32_bf16 v[240:243], v[128:131], v[72:75], v[240:243]\n\t" \
  "v_mfma_f32_16x16x32_bf16 v[240:243], v[148:151], v[100:103], v[240:243]\n\t" \
  "v_mfma_f32_16x16x32_bf16 v[248:251], v[68:71], v[100:103], v[248:251]\n\t" \
  "v_mfma_f32_16x16x32_bf16 v[248:251], v[104:107], v[72:75], v[248:251]\n\t" \
  "s_waitcnt lgkmcnt(1)\n\t" \
  "v_mfma_f32_16x16x32_bf16 v[252:255], v[68:71], v[80:83], v[252:255]\n\t" \
  "s_waitcnt lgkmcnt(0)\n\t" \
  "v_and_b32 v10, v10, v11\n\t" \
  "v_and_b32 v12, v12, v13\n\t" \
  "v_mfma_f32_16x16x32_bf16 v[252:255], v[104:107], v[88:91], v[252:255]\n\t" \
  "v_and_b32 v10, v10, v12\n\t" \
  "v_mfma_f32_16x16x32_bf16 v[220:223], v[108:111], v[88:91], v[220:223]\n\t" \
  "v_readfirstlane_b32 vcc_lo, v10\n\t" \
  "v_mfma_f32_16x16x32_bf16 v[220:223], v[136:139], v[80:83], v[220:223]\n\t" \
  "ds_write_b32 v32, v0 offset:144\n\t" \
  "s_cmp_eq_u32 vcc_lo, 0\n\t" \
  "s_cbranch_scc1 15f\n\t" \
  "14:\n\t" \
  "v_mfma_f32_16x16x32_bf16 v[228:231], v[140:143], v[80:83], v[228:231]\n\t" \
  "ds_read_b64_tr_b16 v[64:65], v62 offset:26624\n\t" \
  "v_mfma_f32_16x16x32_bf16 v[228:231], v[144:147], v[88:91], v[228:231]\n\t" \
  "ds_read_b64_tr_b16 v[66:67], v62 offset:27136\n\t" \
  "v_mfma_f32_16x16x32_bf16 v[236:239], v[132:135], v[88:91], v[236:239]\n\t" \
  "ds_read_b128 v[96:99], v60 offset:10240\n\t" \
  "v_mfma_f32_16x16x32_bf16 v[236:239], v[112:115], v[80:83], v[236:239]\n\t" \
  "ds_read_b128 v[84:87], v60 offset:12288\n\t" \
  "v_mfma_f32_16x16x32_bf16 v[244:247], v[148:151], v[80:83], v[244:247]\n\t" \
  "ds_read_b128 v[92:95], v60 offset:14336\n\t" \
  "v_mfma_f32_16x16x32_bf16 v[244:247], v[128:131], v[88:91], v[244:247]\n\t" \
  "ds_read_b128 v[100:103], v60 offset:16384\n\t" \
  "v_mfma_f32_16x16x32_bf16 v[152:155], v[116:119], v[136:139], v[152:155]\n\t" \
  "ds_read_b64_tr_b16 v[72:73], v63 offset:26624\n\t" \
  "v_mfma_f32_16x16x32_bf16 v[152:155], v[120:123], v[108:111], v[152:155]\n\t" \
  "ds_read_b64_tr_b16 v[74:75], v63 offset:27136\n\t" \
  "v_mfma_f32_16x16x32_bf16 v[160:163], v[120:123], v[144:147], v[160:163]\n\t" \
  "ds_read_b64_tr_b16 v[68:69], v62 offset:34816\n\t" \
  "v_mfma_f32_16x16x32_bf16 v[160:163], v[116:119], v[140:143], v[160:163]\n\t" \
  "ds_read_b64_tr_b16 v[70:71], v62 offset:35328\n\t" \
  "v_mfma_f32_16x16x32_bf16 v[168:171], v[116:119], v[112:115], v[168:171]\n\t" \
  "ds_read_b128 v[104:107], v60 offset:0\n\t" \
  "v_mfma_f32_16x16x32_bf16 v[168:171], v[120:123], v[132:135], v[168:171]\n\t" \
  "ds_read_b64_tr_b16 v[80:81], v63 offset:34816\n\t" \
  "v_mfma_f32_16x16x32_bf16 v[176:179], v[120:123], v[128:131], v[176:179]\n\t" \
  "ds_read_b64_tr_b16 v[82:83], v63 offset:35328\n\t" \
  "v_mfma_f32_16x16x32_bf16 v[176:179], v[116:119], v[148:151], v[176:179]\n\t" \
  "ds_read_b64_tr_b16 v[88:89], v62 offset:18432\n\t" \
  "v_mfma_f32_16x16x32_bf16 v[156:159], v[124:127], v[136:139], v[156:159]\n\t" \
  "ds_read_b64_tr_b16 v[90:91], v62 offset:18944\n\t" \
  "v_mfma_f32_16x16x32_bf16 v[156:159], v[76:79], v[108:111], v[156:159]\n\t" \
  "ds_read_b64_tr_b16 v[120:121], v62 offset:30720\n\t" \
  "v_mfma_f32_16x16x32_bf16 v[164:167], v[76:79], v[144:147], v[164:167]\n\t" \
  "ds_read_b64_tr_b16 v[122:123], v62 offset:31232\n\t" \
  "v_mfma_f32_16x16x32_bf16 v[164:167], v[124:127], v[140:143], v[164:167]\n\t" \
  "ds_read_b128 v[116:119], v61 offset:10240\n\t" \
  "v_mfma_f32_16x16x32_bf16 v[172:175], v[124:127], v[112:115], v[172:175]\n\t" \
  "ds_read_b128 v[136:139], v61 offset:12288\n\t" \
  "v_mfma_f32_16x16x32_bf16 v[172:175], v[76:79], v[132:135], v[172:175]\n\t" \
  "ds_read_b128 v[108:111], v61 offset:14336\n\t" \
  "v_mfma_f32_16x16x32_bf16 v[180:183], v[76:79], v[128:131], v[180:183]\n\t" \
  "ds_read_b64_tr_b16 v[76:77], v63 offset:18432\n\t" \
  "v_mfma_f32_16x16x32_bf16 v[180:183], v[124:127], v[148:151], v[180:183]\n\t" \
  "ds_read_b64_tr_b16 v[78:79], v63 offset:18944\n\t" \
  "s_waitcnt lgkmcnt(7)\n\t" \
  "v_mfma_f32_16x16x32_bf16 v[184:187], v[64:67], v[96:99], v[184:187]\n\t" \
  "ds_read_b128 v[144:147], v61 offset:16384\n\t" \
  "v_mfma_f32_16x16x32_bf16 v[192:195], v[64:67], v[84:87], v[192:195]\n\t" \
  "ds_read_b64_tr_b16 v[140:141], v63 offset:30720\n\t" \
  "v_mfma_f32_16x16x32_bf16 v[200:203], v[64:67], v[92:95], v[200:203]\n\t" \
  "ds_read_b64_tr_b16 v[142:143], v63 offset:31232\n\t" \
  "v_mfma_f32_16x16x32_bf16 v[208:211], v[64:67], v[100:103], v[208:211]\n\t" \
  "502:\n\t" \
  "s_cbranch_execz 506f\n\t" \
  "ds_read_b64_tr_b16 v[112:113], v62 offset:22528\n\t" \
  "v_mfma_f32_16x16x32_bf16 v[188:191], v[72:75], v[96:99], v[188:191]\n\t" \
  "ds_read_b64_tr_b16 v[114:115], v62 offset:23040\n\t" \
  "v_mfma_f32_16x16x32_bf16 v[196:199], v[72:75], v[84:87], v[196:199]\n\t" \
  "ds_read_b64_tr_b16 v[132:133], v63 offset:22528\n\t" \
  "v_mfma_f32_16x16x32_bf16 v[204:207], v[72:75], v[92:95], v[204:207]\n\t" \
  "ds_read_b64_tr_b16 v[134:135], v63 offset:23040\n\t" \
  "v_mfma_f32_16x16x32_bf16 v[212:215], v[72:75], v[100:103], v[212:215]\n\t" \
  "ds_read_b64_tr_b16 v[128:129], v62 offset:38912\n\t" \
  "v_mfma_f32_16x16x32_bf16 v[248:251], v[104:107], v[68:71], v[248:251]\n\t" \
  "ds_read_b64_tr_b16 v[130:131], v62 offset:39424\n\t" \
  "v_mfma_f32_16x16x32_bf16 v[216:219], v[96:99], v[68:71], v[216:219]\n\t" \
  "ds_read_b128 v[124:127], v61 offset:0\n\t" \
  "v_mfma_f32_16x16x32_bf16 v[224:227], v[84:87], v[68:71], v[224:227]\n\t" \
  "ds_read_b64_tr_b16 v[148:149], v63 offset:38912\n\t" \
  "v_mfma_f32_16x16x32_bf16 v[232:235], v[92:95], v[68:71], v[232:235]\n\t" \
  "ds_read_b64_tr_b16 v[150:151], v63 offset:39424\n\t" \
  "v_mfma_f32_16x16x32_bf16 v[240:243], v[100:103], v[68:71], v[240:243]\n\t" \
  "v_mfma_f32_16x16x32_bf16 v[252:255], v[104:107], v[80:83], v[252:255]\n\t" \
  "v_mfma_f32_16x16x32_bf16 v[220:223], v[96:99], v[80:83], v[220:223]\n\t" \
  "v_mfma_f32_16x16x32_bf16 v[228:231], v[84:87], v[80:83], v[228:231]\n\t" \
  "v_mfma_f32_16x16x32_bf16 v[236:239], v[92:95], v[80:83], v[236:239]\n\t" \
  "v_mfma_f32_16x16x32_bf16 v[244:247], v[100:103], v[80:83], v[244:247]\n\t" \
  "v_mfma_f32_16x16x32_bf16 v[152:155], v[88:91], v[96:99], v[152:155]\n\t" \
  "v_mfma_f32_16x16x32_bf16 v[160:163], v[88:91], v[84:87], v[160:163]\n\t" \
  "v_mfma_f32_16x16x32_bf16 v[168:171], v[88:91], v[92:95], v[168:171]\n\t" \
  "v_mfma_f32_16x16x32_bf16 v[176:179], v[88:91], v[100:103], v[176:179]\n\t" \
  "s_waitcnt lgkmcnt(0)\n\t" \
  "v_mfma_f32_16x16x32_bf16 v[156:159], v[76:79], v[96:99], v[156:159]\n\t" \
  "v_mfma_f32_16x16x32_bf16 v[164:167], v[76:79], v[84:87], v[164:167]\n\t" \
  "ds_write_b32 v32, v0\n\t" \
  "v_mfma_f32_16x16x32_bf16 v[172:175], v[76:79], v[92:95], v[172:175]\n\t" \
  "v_mfma_f32_16x16x32_bf16 v[180:183], v[76:79], v[100:103], v[180:183]\n\t" \
  "v_mfma_f32_16x16x32_bf16 v[184:187], v[120:123], v[116:119], v[184:187]\n\t" \
  "v_and_b32 v56, -16, v32\n\t" \
  "v_mfma_f32_16x16x32_bf16 v[192:195], v[120:123], v[136:139], v[192:195]\n\t" \
  "v_mfma_f32_16x16x32_bf16 v[200:203], v[120:123], v[108:111], v[200:203]\n\t" \
  "v_mfma_f32_16x16x32_bf16 v[208:211], v[120:123], v[144:147], v[208:211]\n\t" \
  "v_mfma_f32_16x16x32_bf16 v[188:191], v[140:143], v[116:119], v[188:191]\n\t" \
  "v_mfma_f32_16x16x32_bf16 v[196:199], v[140:143], v[136:139], v[196:199]\n\t" \
  "v_mfma_f32_16x16x32_bf16 v[204:207], v[140:143], v[108:111], v[204:207]\n\t" \
  "v_mfma_f32_16x16x32_bf16 v[212:215], v[140:143], v[144:147], v[212:215]\n\t" \
  "v_cvt_pk_bf16_f32 v14, v184, v185\n\t" \
  "v_cvt_pk_bf16_f32 v15, v186, v187\n\t" \
  "v_mfma_f32_16x16x32_bf16 v[152:155], v[112:115], v[116:119], v[152:155]\n\t" \
  "v_cvt_pk_bf16_f32 v18, v192, v193\n\t" \
  "v_cvt_pk_bf16_f32 v19, v194, v195\n\t" \
  "v_mfma_f32_16x16x32_bf16 v[160:163], v[112:115], v[136:139], v[160:163]\n\t" \
  "v_cvt_pk_bf16_f32 v16, v188, v189\n\t" \
  "v_cvt_pk_bf16_f32 v17, v190, v191\n\t" \
  "v_mfma_f32_16x16x32_bf16 v[168:171], v[112:115], v[108:111], v[168:171]\n\t" \
  "v_cvt_pk_bf16_f32 v20, v196, v197\n\t" \
  "v_cvt_pk_bf16_f32 v21, v198, v199\n\t" \
  "v_mfma_f32_16x16x32_bf16 v[176:179], v[112:115], v[144:147], v[176:179]\n\t" \
  "v_cvt_pk_bf16_f32 v22, v200, v201\n\t" \
  "v_cvt_pk_bf16_f32 v23, v202, v203\n\t" \
  "v_mfma_f32_16x16x32_bf16 v[156:159], v[132:135], v[116:119], v[156:159]\n\t" \
  "v_cvt_pk_bf16_f32 v24, v204, v205\n\t" \
  "v_cvt_pk_bf16_f32 v25, v206, v207\n\t" \
  "v_mfma_f32_16x16x32_bf16 v[164:167], v[132:135], v[136:139], v[164:167]\n\t" \
  "v_cvt_pk_bf16_f32 v26, v208, v209\n\t" \
  "v_cvt_pk_bf16_f32 v27, v210, v211\n\t" \
  "v_mfma_f32_16x16x32_bf16 v[172:175], v[132:135], v[108:111], v[172:175]\n\t" \
  "v_cvt_pk_bf16_f32 v28, v212, v213\n\t" \
  "v_cvt_pk_bf16_f32 v29, v214, v215\n\t" \
  "v_mfma_f32_16x16x32_bf16 v[180:183], v[132:135], v[144:147], v[180:183]\n\t" \
  "v_cvt_pk_bf16_f32 v84, v152, v153\n\t" \
  "v_cvt_pk_bf16_f32 v85, v154, v155\n\t" \
  "v_mfma_f32_16x16x32_bf16 v[248:251], v[124:127], v[128:131], v[248:251]\n\t" \
  "v_cvt_pk_bf16_f32 v86, v156, v157\n\t" \
  "v_cvt_pk_bf16_f32 v87, v158, v159\n\t" \
  "v_mfma_f32_16x16x32_bf16 v[216:219], v[116:119], v[128:131], v[216:219]\n\t" \
  "v_cvt_pk_bf16_f32 v88, v160, v161\n\t" \
  "v_cvt_pk_bf16_f32 v89, v162, v163\n\t" \
  "v_mfma_f32_16x16x32_bf16 v[224:227], v[136:139], v[128:131], v[224:227]\n\t" \
  "v_cvt_pk_bf16_f32 v90, v164, v165\n\t" \
  "v_cvt_pk_bf16_f32 v91, v166, v167\n\t" \
  "v_mfma_f32_16x16x32_bf16 v[232:235], v[108:111], v[128:131], v[232:235]\n\t" \
  "v_cvt_pk_bf16_f32 v72, v248, v249\n\t" \
  "v_cvt_pk_bf16_f32 v73, v250, v251\n\t" \
  "v_mfma_f32_16x16x32_bf16 v[240:243], v[144:147], v[128:131], v[240:243]\n\t" \
  "ds_read_b32 v52, v33 offset:48\n\t" \
  "ds_read_b128 v[10:13], v31\n\t" \
  "v_cvt_pk_bf16_f32 v92, v168, v169\n\t" \
  "v_mfma_f32_16x16x32_bf16 v[252:255], v[124:127], v[148:151], v[252:255]\n\t" \
  "v_cvt_pk_bf16_f32 v93, v170, v171\n\t" \
  "v_cvt_pk_bf16_f32 v94, v172, v173\n\t" \
  "v_mfma_f32_16x16x32_bf16 v[220:223], v[116:119], v[148:151], v[220:223]\n\t" \
  "v_cvt_pk_bf16_f32 v95, v174, v175\n\t" \
  "v_cvt_pk_bf16_f32 v96, v176, v177\n\t" \
  "v_mfma_f32_16x16x32_bf16 v[228:231], v[136:139], v[148:151], v[228:231]\n\t" \
  "v_cvt_pk_bf16_f32 v97, v178, v179\n\t" \
  "v_cvt_pk_bf16_f32 v98, v180, v181\n\t" \
  "v_mfma_f32_16x16x32_bf16 v[236:239], v[108:111], v[148:151], v[236:239]\n\t" \
  "v_cvt_pk_bf16_f32 v82, v252, v253\n\t" \
  "v_cvt_pk_bf16_f32 v83, v254, v255\n\t" \
  "v_mfma_f32_16x16x32_bf16 v[244:247], v[144:147], v[148:151], v[244:247]\n\t" \
  "ds_write_b64 v30, v[72:73]\n\t" \
  "ds_write_b64 v30, v[82:83] offset:8\n\t" \
  "ds_write_b32 v32, v0 offset:48\n\t" \
  "v_mfma_f32_16x16x32_bf16 v[200:203], v[22:25], v[92:95], v[34:37]\n\t" \
  "v_cvt_pk_bf16_f32 v99, v182, v183\n\t" \
  "s_waitcnt lgkmcnt(3)\n\t" \
  "v_readfirstlane_b32 vcc_lo, v52\n\t" \
  "s_nop 1\n\t" \
  "s_cmp_eq_u32 vcc_lo, 0\n\t" \
  "s_cbranch_scc1 5f\n\t" \
  "4:\n\t" \
  "v_mfma_f32_16x16x32_bf16 v[204:207], v[18:21], v[92:95], v[38:41]\n\t" \
  "v_cvt_pk_bf16_f32 v66, v232, v233\n\t" \
  "v_cvt_pk_bf16_f32 v67, v234, v235\n\t" \
  "v_mfma_f32_16x16x32_bf16 v[208:211], v[26:29], v[96:99], v[34:37]\n\t" \
  "v_cvt_pk_bf16_f32 v76, v236, v237\n\t" \
  "v_cvt_pk_bf16_f32 v77, v238, v239\n\t" \
  "v_mfma_f32_16x16x32_bf16 v[212:215], v[22:25], v[96:99], v[38:41]\n\t" \
  "v_cvt_pk_bf16_f32 v64, v240, v241\n\t" \
  "v_cvt_pk_bf16_f32 v65, v242, v243\n\t" \
  "v_mfma_f32_16x16x32_bf16 v[192:195], v[18:21], v[88:91], v[34:37]\n\t" \
  "v_max_f32 v200, v200, v204\n\t" \
  "v_max_f32 v201, v201, v205\n\t" \
  "v_mfma_f32_16x16x32_bf16 v[196:199], v[14:17], v[88:91], v[38:41]\n\t" \
  "v_max_f32 v202, v202, v206\n\t" \
  "v_max_f32 v203, v203, v207\n\t" \
  "v_mfma_f32_16x16x32_bf16 v[184:187], v[14:17], v[84:87], v[34:37]\n\t" \
  "v_max_f32 v208, v208, v212\n\t" \
  "v_max_f32 v209, v209, v213\n\t" \
  "v_mfma_f32_16x16x32_bf16 v[188:191], v[10:13], v[84:87], v[42:45]\n\t" \
  "v_max_f32 v210, v210, v214\n\t" \
  "v_max_f32 v211, v211, v215\n\t" \
  "v_max_f32 v192, v192, v196\n\t" \
  "v_max_f32 v193, v193, v197\n\t" \
  "v_max_f32 v194, v194, v198\n\t" \
  "v_max3_f32 v106, v208, v209, v210\n\t" \
  "v_max3_f32 v104, v200, v201, v202\n\t" \
  "v_max_f32 v195, v195, v199\n\t" \
  "v_max_f32 v184, v184, v188\n\t" \
  "v_max_f32 v185, v185, v189\n\t" \
  "v_max_f32 v186, v186, v190\n\t" \
  "v_max3_f32 v102, v192, v193, v194\n\t" \
  "v_max_f32 v187, v187, v191\n\t" \
  "v_max3_f32 v100, v184, v185, v186\n\t" \
  "v_max_f32 v106, v106, v211\n\t" \
  "v_max_f32 v104, v104, v203\n\t" \
  "v_max_f32 v102, v102, v195\n\t" \
  "v_max_f32 v100, v100, v187\n\t" \
  "v_permlane16_swap_b32 v104, v106\n\t" \
  "v_cvt_pk_bf16_f32 v74, v244, v245\n\t" \
  "v_permlane16_swap_b32 v100, v102\n\t" \
  "v_max_f32 v104, v104, v106\n\t" \
  "v_max_f32 v100, v100, v102\n\t" \
  "v_cvt_pk_bf16_f32 v75, v246, v247\n\t" \
  "v_cvt_pk_bf16_f32 v68, v224, v225\n\t" \
  "v_permlane32_swap_b32 v100, v104\n\t" \
  "v_cvt_pk_bf16_f32 v69, v226, v227\n\t" \
  "v_max_f32 v100, v100, v104\n\t" \
  "v_mul_f32 v100, 0xbe8293ee, v100\n\t" \
  "v_mov_b32 v104, v100\n\t" \
  "v_cvt_pk_bf16_f32 v78, v228, v229\n\t" \
  "v_cvt_pk_bf16_f32 v79, v230, v231\n\t" \
  "v_permlane32_swap_b32 v100, v104\n\t" \
  "v_cvt_pk_bf16_f32 v70, v216, v217\n\t" \
  "v_mov_b32 v106, v104\n\t" \
  "v_mov_b32 v102, v100\n\t" \
  "v_cvt_pk_bf16_f32 v71, v218, v219\n\t" \
  "v_permlane16_swap_b32 v104, v106\n\t" \
  "v_permlane16_swap_b32 v100, v102\n\t" \
  "v_fma_f32 v208, v208, %16, v106\n\t" \
  "v_fma_f32 v209, v209, %16, v106\n\t" \
  "v_fma_f32 v210, v210, %16, v106\n\t" \
  "v_fma_f32 v211, v211, %16, v106\n\t" \
  "v_exp_f32 v208, v208\n\t" \
  "v_exp_f32 v209, v209\n\t" \
  "v_exp_f32 v210, v210\n\t" \
  "v_exp_f32 v211, v211\n\t" \
  "v_cvt_pk_bf16_f32 v212, v208, v209\n\t" \
  "v_cvt_pk_bf16_f32 v213, v210, v211\n\t" \
  "v_bfi_b32 v214, v46, 0, v212\n\t" \
  "v_bfi_b32 v215, v47, 0, v213\n\t" \
  "v_and_b32 v212, v46, v212\n\t" \
  "v_and_b32 v213, v47, v213\n\t" \
  "v_fma_f32 v200, v200, %16, v104\n\t" \
  "v_fma_f32 v201, v201, %16, v104\n\t" \
  "v_mfma_f32_16x16x32_bf16 v[208:211], v[48:51], v[212:215], 0\n\t" \
  "v_fma_f32 v202, v202, %16, v104\n\t" \
  "v_fma_f32 v203, v203, %16, v104\n\t" \
  "v_mfma_f32_16x16x32_bf16 v[176:179], v[64:67], v[212:215], 0\n\t" \
  "v_exp_f32 v200, v200\n\t" \
  "v_mfma_f32_16x16x32_bf16 v[180:183], v[74:77], v[212:215], 0\n\t" \
  "v_exp_f32 v201, v201\n\t" \
  "v_exp_f32 v202, v202\n\t" \
  "v_exp_f32 v203, v203\n\t" \
  "v_cvt_pk_bf16_f32 v204, v200, v201\n\t" \
  "v_cvt_pk_bf16_f32 v205, v202, v203\n\t" \
  "v_bfi_b32 v206, v46, 0, v204\n\t" \
  "v_bfi_b32 v207, v47, 0, v205\n\t" \
  "v_rcp_f32 v106, v208\n\t" \
  "v_and_b32 v204, v46, v204\n\t" \
  "v_and_b32 v205, v47, v205\n\t" \
  "v_pk_mul_f32 v[176:177], v[176:177], v[106:107] op_sel_hi:[1,0]\n\t" \
  "v_pk_mul_f32 v[178:179], v[178:179], v[106:107] op_sel_hi:[1,0]\n\t" \
  "v_mfma_f32_16x16x32_bf16 v[200:203], v[48:51], v[204:207], 0\n\t" \
  "v_pk_mul_f32 v[180:181], v[180:181], v[106:107] op_sel_hi:[1,0]\n\t" \
  "v_pk_mul_f32 v[182:183], v[182:183], v[106:107] op_sel_hi:[1,0]\n\t" \
  "v_mfma_f32_16x16x32_bf16 v[168:171], v[66:69], v[204:207], 0\n\t" \
  "v_cvt_pk_bf16_f32 v240, v176, v177\n\t" \
  "v_cvt_pk_bf16_f32 v241, v178, v179\n\t" \
  "v_mfma_f32_16x16x32_bf16 v[172:175], v[76:79], v[204:207], 0\n\t" \
  "v_cvt_pk_bf16_f32 v242, v180, v181\n\t" \
  "v_cvt_pk_bf16_f32 v243, v182, v183\n\t" \
  "global_store_dwordx4 v1, v[240:243], %14 offset:0 sc1\n\t" \
  "v_fma_f32 v192, v192, %16, v102\n\t" \
  "v_fma_f32 v193, v193, %16, v102\n\t" \
  "v_fma_f32 v194, v194, %16, v102\n\t" \
  "v_fma_f32 v195, v195, %16, v102\n\t" \
  "v_exp_f32 v192, v192\n\t" \
  "v_exp_f32 v193, v193\n\t" \
  "v_exp_f32 v194, v194\n\t" \
  "v_exp_f32 v195, v195\n\t" \
  "v_cvt_pk_bf16_f32 v196, v192, v193\n\t" \
  "v_cvt_pk_bf16_f32 v197, v194, v195\n\t" \
  "v_bfi_b32 v198, v46, 0, v196\n\t" \
  "v_bfi_b32 v199, v47, 0, v197\n\t" \
  "v_rcp_f32 v104, v200\n\t" \
  "v_and_b32 v196, v46, v196\n\t" \
  "v_and_b32 v197, v47, v197\n\t" \
  "v_cvt_pk_bf16_f32 v80, v220, v221\n\t" \
  "v_cvt_pk_bf16_f32 v81, v222, v223\n\t" \
  "v_mfma_f32_16x16x32_bf16 v[192:195], v[48:51], v[196:199], 0\n\t" \
  "v_pk_mul_f32 v[168:169], v[168:169], v[104:105] op_sel_hi:[1,0]\n\t" \
  "v_pk_mul_f32 v[170:171], v[170:171], v[104:105] op_sel_hi:[1,0]\n\t" \
  "v_mfma_f32_16x16x32_bf16 v[160:163], v[68:71], v[196:199], 0\n\t" \
  "v_pk_mul_f32 v[172:173], v[172:173], v[104:105] op_sel_hi:[1,0]\n\t" \
  "v_pk_mul_f32 v[174:175], v[174:175], v[104:105] op_sel_hi:[1,0]\n\t" \
  "v_mfma_f32_16x16x32_bf16 v[164:167], v[78:81], v[196:199], 0\n\t" \
  "v_cvt_pk_bf16_f32 v232, v168, v169\n\t" \
  "v_cvt_pk_bf16_f32 v233, v170, v171\n\t" \
  "v_cvt_pk_bf16_f32 v234, v172, v173\n\t" \
  "v_cvt_pk_bf16_f32 v235, v174, v175\n\t" \
  "global_store_dwordx4 v1, v[232:235], %14 offset:-4096 sc1\n\t" \
  "v_fma_f32 v184, v184, %16, v100\n\t" \
  "v_fma_f32 v185, v185, %16, v100\n\t" \
  "v_fma_f32 v186, v186, %16, v100\n\t" \
  "v_fma_f32 v187, v187, %16, v100\n\t" \
  "v_exp_f32 v184, v184\n\t" \
  "v_exp_f32 v185, v185\n\t" \
  "v_exp_f32 v186, v186\n\t" \
  "v_exp_f32 v187, v187\n\t" \
  "v_cvt_pk_bf16_f32 v188, v184, v185\n\t" \
  "v_cvt_pk_bf16_f32 v189, v186, v187\n\t" \
  "v_bfi_b32 v190, v46, 0, v188\n\t" \
  "v_bfi_b32 v191, v47, 0, v189\n\t" \
  "v_rcp_f32 v102, v192\n\t" \
  "v_and_b32 v188, v46, v188\n\t" \
  "v_and_b32 v189, v47, v189\n\t" \
  "v_pk_mul_f32 v[160:161], v[160:161], v[102:103] op_sel_hi:[1,0]\n\t" \
  "v_pk_mul_f32 v[162:163], v[162:163], v[102:103] op_sel_hi:[1,0]\n\t" \
  "v_mfma_f32_16x16x32_bf16 v[184:187], v[48:51], v[188:191], 0\n\t" \
  "v_pk_mul_f32 v[164:165], v[164:165], v[102:103] op_sel_hi:[1,0]\n\t" \
  "v_pk_mul_f32 v[166:167], v[166:167], v[102:103] op_sel_hi:[1,0]\n\t" \
  "v_mfma_f32_16x16x32_bf16 v[152:155], v[70:73], v[188:191], 0\n\t" \
  "v_cvt_pk_bf16_f32 v224, v160, v161\n\t" \
  "v_cvt_pk_bf16_f32 v225, v162, v163\n\t" \
  "v_mfma_f32_16x16x32_bf16 v[156:159], v[80:83], v[188:191], 0\n\t" \
  "v_cvt_pk_bf16_f32 v226, v164, v165\n\t" \
  "v_cvt_pk_bf16_f32 v227, v166, v167\n\t" \
  "global_store_dwordx4 v4, v[224:227], %14 offset:0 sc1\n\t" \
  "v_rcp_f32 v100, v184\n\t" \
  "ds_read_b128 v[60:63], v56 offset:64\n\t" \
  "ds_read_b64_tr_b16 v[108:109], v5 offset:0\n\t" \
  "ds_read_b64_tr_b16 v[110:111], v5 offset:16\n\t" \
  "ds_read_b128 v[112:115], v6 offset:16384\n\t" \
  "ds_read_b128 v[116:119], v6 offset:15360\n\t" \
  "ds_read_b64_tr_b16 v[120:121], v5 offset:512\n\t" \
  "ds_read_b64_tr_b16 v[122:123], v5 offset:528\n\t" \
  "ds_read_b128 v[124:127], v6 offset:14336\n\t" \
  "ds_read_b64_tr_b16 v[128:129], v5 offset:1024\n\t" \
  "ds_read_b64_tr_b16 v[130:131], v5 offset:1040\n\t" \
  "ds_read_b128 v[132:135], v6 offset:13312\n\t" \
  "ds_read_b64_tr_b16 v[136:137], v5 offset:1536\n\t" \
  "ds_read_b64_tr_b16 v[138:139], v5 offset:1552\n\t" \
  "ds_read_b128 v[140:143], v6 offset:12288\n\t" \
  "ds_read_b64_tr_b16 v[144:145], v5 offset:2048\n\t" \
  "v_pk_mul_f32 v[152:153], v[152:153], v[100:101] op_sel_hi:[1,0]\n\t" \
  "v_pk_mul_f32 v[154:155], v[154:155], v[100:101] op_sel_hi:[1,0]\n\t" \
  "v_cvt_pk_bf16_f32 v216, v152, v153\n\t" \
  "v_pk_mul_f32 v[156:157], v[156:157], v[100:101] op_sel_hi:[1,0]\n\t" \
  "v_pk_mul_f32 v[158:159], v[158:159], v[100:101] op_sel_hi:[1,0]\n\t" \
  "v_cvt_pk_bf16_f32 v217, v154, v155\n\t" \
  "v_cvt_pk_bf16_f32 v218, v156, v157\n\t" \
  "v_cvt_pk_bf16_f32 v219, v158, v159\n\t" \
  "global_store_dwordx4 v4, v[216:219], %14 offset:-4096 sc1\n\t" \
  "ds_write_b32 v32, v0 offset:112\n\t" \
  "s_branch 9f\n\t" \
  "5:\n\t" \
  "ds_read_b32 v52, v33 offset:48\n\t" \
  "ds_read_b128 v[10:13], v31\n\t" \
  "s_waitcnt lgkmcnt(0)\n\t" \
  "v_readfirstlane_b32 vcc_lo, v52\n\t" \
  "s_nop 1\n\t" \
  "s_cmp_eq_u32 vcc_lo, 0\n\t" \
  "s_cbranch_scc1 5b\n\t" \
  "s_branch 4b\n\t" \
  "15:\n\t" \
  "ds_read_b128 v[10:13], v14 offset:128\n\t" \
  "s_waitcnt lgkmcnt(0)\n\t" \
  "v_and_b32 v10, v10, v11\n\t" \
  "v_and_b32 v12, v12, v13\n\t" \
  "v_and_b32 v10, v10, v12\n\t" \
  "s_nop 1\n\t" \
  "v_readfirstlane_b32 vcc_lo, v10\n\t" \
  "s_nop 1\n\t" \
  "s_cmp_eq_u32 vcc_lo, 0\n\t" \
  "s_cbranch_scc1 15b\n\t" \
  "s_branch 14b\n\t" \
  "8:\n\t" \
  "global_load_dwordx2 v[10:11], v51, %11\n\t" \
  "global_load_dwordx2 v[12:13], v51, %11 offset:128\n\t" \
  "v_xor_b32 v53, 64, v52\n\t" \
  "v_xor_b32 v55, 32, v54\n\t" \
  "v_add_u32 v56, 43008, v52\n\t" \
  "v_add_u32 v58, 43008, v54\n\t" \
  "v_xor_b32 v57, 64, v56\n\t" \
  "v_xor_b32 v59, 32, v58\n\t" \
  "v_add_u32 v60, 86016, v52\n\t" \
  "v_add_u32 v62, 86016, v54\n\t" \
  "v_xor_b32 v61, 64, v60\n\t" \
  "v_xor_b32 v63, 32, v62\n\t" \
  "s_mov_b32 vcc_hi, 0x05040100\n\t" \
  "s_waitcnt vmcnt(2)\n\t" \
  "s_barrier\n\t" \
  "ds_read_b64_tr_b16 v[64:65], v62 offset:34816\n\t" \
  "ds_read_b64_tr_b16 v[66:67], v62 offset:35328\n\t" \
  "ds_read_b128 v[68:71], v60 offset:2048\n\t" \
  "ds_read_b64_tr_b16 v[72:73], v62 offset:38912\n\t" \
  "ds_read_b64_tr_b16 v[74:75], v62 offset:39424\n\t" \
  "ds_read_b128 v[76:79], v61 offset:2048\n\t" \
  "ds_read_b128 v[80:83], v61 offset:4096\n\t" \
  "ds_read_b128 v[84:87], v60 offset:4096\n\t" \
  "ds_read_b128 v[88:91], v60 offset:6144\n\t" \
  "ds_read_b128 v[92:95], v61 offset:6144\n\t" \
  "ds_read_b128 v[96:99], v61 offset:8192\n\t" \
  "ds_read_b128 v[100:103], v60 offset:8192\n\t" \
  "ds_read_b64_tr_b16 v[104:105], v63 offset:34816\n\t" \
  "ds_read_b64_tr_b16 v[106:107], v63 offset:35328\n\t" \
  "ds_read_b64_tr_b16 v[108:109], v63 offset:38912\n\t" \
  "ds_read_b64_tr_b16 v[110:111], v63 offset:39424\n\t" \
  "s_waitcnt lgkmcnt(13)\n\t" \
  "v_mfma_f32_16x16x32_bf16 v[216:219], v[68:71], v[64:67], 0\n\t" \
  "ds_read_b64_tr_b16 v[112:113], v62 offset:26624\n\t" \
  "ds_read_b64_tr_b16 v[114:115], v62 offset:27136\n\t" \
  "s_waitcnt lgkmcnt(12)\n\t" \
  "v_mfma_f32_16x16x32_bf16 v[216:219], v[76:79], v[72:75], v[216:219]\n\t" \
  "ds_read_b64_tr_b16 v[116:117], v62 offset:30720\n\t" \
  "ds_read_b64_tr_b16 v[118:119], v62 offset:31232\n\t" \
  "s_waitcnt lgkmcnt(13)\n\t" \
  "v_mfma_f32_16x16x32_bf16 v[224:227], v[80:83], v[72:75], 0\n\t" \
  "ds_read_b128 v[120:123], v60 offset:0\n\t" \
  "s_waitcnt lgkmcnt(13)\n\t" \
  "v_mfma_f32_16x16x32_bf16 v[224:227], v[84:87], v[64:67], v[224:227]\n\t" \
  "ds_read_b128 v[124:127], v61 offset:0\n\t" \
  "s_waitcnt lgkmcnt(13)\n\t" \
  "v_mfma_f32_16x16x32_bf16 v[232:235], v[88:91], v[64:67], 0\n\t" \
  "ds_read_b64_tr_b16 v[128:129], v63 offset:26624\n\t" \
  "s_waitcnt lgkmcnt(13)\n\t" \
  "v_mfma_f32_16x16x32_bf16 v[232:235], v[92:95], v[72:75], v[232:235]\n\t" \
  "ds_read_b64_tr_b16 v[130:131], v63 offset:27136\n\t" \
  "s_waitcnt lgkmcnt(13)\n\t" \
  "v_mfma_f32_16x16x32_bf16 v[240:243], v[96:99], v[72:75], 0\n\t" \
  "ds_read_b64_tr_b16 v[132:133], v63 offset:30720\n\t" \
  "s_waitcnt lgkmcnt(9)\n\t" \
  "v_mfma_f32_16x16x32_bf16 v[240:243], v[100:103], v[64:67], v[240:243]\n\t" \
  "ds_read_b64_tr_b16 v[134:135], v63 offset:31232\n\t" \
  "v_mfma_f32_16x16x32_bf16 v[220:223], v[68:71], v[104:107], 0\n\t" \
  "ds_read_b64_tr_b16 v[136:137], v62 offset:18432\n\t" \
  "v_mfma_f32_16x16x32_bf16 v[220:223], v[76:79], v[108:111], v[220:223]\n\t" \
  "ds_read_b64_tr_b16 v[138:139], v62 offset:18944\n\t" \
  "v_mfma_f32_16x16x32_bf16 v[228:231], v[80:83], v[108:111], 0\n\t" \
  "ds_read_b64_tr_b16 v[140:141], v62 offset:22528\n\t" \
  "v_mfma_f32_16x16x32_bf16 v[228:231], v[84:87], v[104:107], v[228:231]\n\t" \
  "ds_read_b64_tr_b16 v[142:143], v62 offset:23040\n\t" \
  "v_mfma_f32_16x16x32_bf16 v[236:239], v[88:91], v[104:107], 0\n\t" \
  "ds_read_b64_tr_b16 v[144:145], v63 offset:18432\n\t" \
  "v_mfma_f32_16x16x32_bf16 v[236:239], v[92:95], v[108:111], v[236:239]\n\t" \
  "ds_read_b64_tr_b16 v[146:147], v63 offset:18944\n\t" \
  "v_mfma_f32_16x16x32_bf16 v[244:247], v[96:99], v[108:111], 0\n\t" \
  "ds_read_b64_tr_b16 v[148:149], v63 offset:22528\n\t" \
  "v_mfma_f32_16x16x32_bf16 v[244:247], v[100:103], v[104:107], v[244:247]\n\t" \
  "ds_read_b64_tr_b16 v[150:151], v63 offset:23040\n\t" \
  "s_waitcnt lgkmcnt(7)\n\t" \
  "v_mfma_f32_16x16x32_bf16 v[184:187], v[112:115], v[68:71], 0\n\t" \
  "v_mfma_f32_16x16x32_bf16 v[184:187], v[116:119], v[76:79], v[184:187]\n\t" \
  "v_mfma_f32_16x16x32_bf16 v[192:195], v[116:119], v[80:83], 0\n\t" \
  "v_mfma_f32_16x16x32_bf16 v[192:195], v[112:115], v[84:87], v[192:195]\n\t" \
  "v_mfma_f32_16x16x32_bf16 v[200:203], v[112:115], v[88:91], 0\n\t" \
  "v_mfma_f32_16x16x32_bf16 v[200:203], v[116:119], v[92:95], v[200:203]\n\t" \
  "v_mfma_f32_16x16x32_bf16 v[208:211], v[116:119], v[96:99], 0\n\t" \
  "v_mfma_f32_16x16x32_bf16 v[208:211], v[112:115], v[100:103], v[208:211]\n\t" \
  "v_mfma_f32_16x16x32_bf16 v[248:251], v[112:115], v[120:123], 0\n\t" \
  "v_mfma_f32_16x16x32_bf16 v[248:251], v[116:119], v[124:127], v[248:251]\n\t" \
  "v_mfma_f32_16x16x32_bf16 v[252:255], v[128:131], v[120:123], 0\n\t" \
  "v_mfma_f32_16x16x32_bf16 v[252:255], v[132:135], v[124:127], v[252:255]\n\t" \
  "v_mfma_f32_16x16x32_bf16 v[188:191], v[132:135], v[76:79], 0\n\t" \
  "v_mfma_f32_16x16x32_bf16 v[188:191], v[128:131], v[68:71], v[188:191]\n\t" \
  "s_waitcnt lgkmcnt(0)\n\t" \
  "s_getpc_b64 s[88:89]\n\t" \
  "s_branch 28b\n\t" \
  "s_barrier\n\t" \
  "v_mfma_f32_16x16x32_bf16 v[196:199], v[128:131], v[84:87], 0\n\t" \
  "ds_read_b64_tr_b16 v[72:73], v54 offset:34816\n\t" \
  "v_mfma_f32_16x16x32_bf16 v[196:199], v[132:135], v[80:83], v[196:199]\n\t" \
  "ds_read_b64_tr_b16 v[74:75], v54 offset:35328\n\t" \
  "v_mfma_f32_16x16x32_bf16 v[204:207], v[132:135], v[92:95], 0\n\t" \
  "ds_read_b128 v[64:67], v52 offset:2048\n\t" \
  "v_mfma_f32_16x16x32_bf16 v[204:207], v[128:131], v[88:91], v[204:207]\n\t" \
  "ds_read_b64_tr_b16 v[108:109], v54 offset:38912\n\t" \
  "v_mfma_f32_16x16x32_bf16 v[212:215], v[128:131], v[100:103], 0\n\t" \
  "ds_read_b64_tr_b16 v[110:111], v54 offset:39424\n\t" \
  "v_mfma_f32_16x16x32_bf16 v[212:215], v[132:135], v[96:99], v[212:215]\n\t" \
  "ds_read_b128 v[104:107], v53 offset:2048\n\t" \
  "v_mfma_f32_16x16x32_bf16 v[152:155], v[136:139], v[68:71], 0\n\t" \
  "ds_read_b128 v[112:115], v53 offset:4096\n\t" \
  "v_mfma_f32_16x16x32_bf16 v[152:155], v[140:143], v[76:79], v[152:155]\n\t" \
  "ds_read_b128 v[116:119], v52 offset:4096\n\t" \
  "v_mfma_f32_16x16x32_bf16 v[160:163], v[140:143], v[80:83], 0\n\t" \
  "ds_read_b128 v[120:123], v52 offset:6144\n\t" \
  "v_mfma_f32_16x16x32_bf16 v[160:163], v[136:139], v[84:87], v[160:163]\n\t" \
  "ds_read_b128 v[124:127], v53 offset:6144\n\t" \
  "v_mfma_f32_16x16x32_bf16 v[168:171], v[136:139], v[88:91], 0\n\t" \
  "ds_read_b128 v[128:131], v53 offset:8192\n\t" \
  "v_mfma_f32_16x16x32_bf16 v[168:171], v[140:143], v[92:95], v[168:171]\n\t" \
  "ds_read_b128 v[132:135], v52 offset:8192\n\t" \
  "v_mfma_f32_16x16x32_bf16 v[176:179], v[140:143], v[96:99], 0\n\t" \
  "ds_read_b64_tr_b16 v[140:141], v55 offset:34816\n\t" \
  "v_mfma_f32_16x16x32_bf16 v[176:179], v[136:139], v[100:103], v[176:179]\n\t" \
  "ds_read_b64_tr_b16 v[142:143], v55 offset:35328\n\t" \
  "v_mfma_f32_16x16x32_bf16 v[156:159], v[144:147], v[68:71], 0\n\t" \
  "ds_read_b64_tr_b16 v[136:137], v55 offset:38912\n\t" \
  "v_mfma_f32_16x16x32_bf16 v[156:159], v[148:151], v[76:79], v[156:159]\n\t" \
  "ds_read_b64_tr_b16 v[138:139], v55 offset:39424\n\t" \
  "v_mfma_f32_16x16x32_bf16 v[164:167], v[148:151], v[80:83], 0\n\t" \
  "ds_read_b64_tr_b16 v[68:69], v54 offset:26624\n\t" \
  "v_mfma_f32_16x16x32_bf16 v[164:167], v[144:147], v[84:87], v[164:167]\n\t" \
  "ds_read_b64_tr_b16 v[70:71], v54 offset:27136\n\t" \
  "v_mfma_f32_16x16x32_bf16 v[172:175], v[144:147], v[88:91], 0\n\t" \
  "ds_read_b64_tr_b16 v[76:77], v54 offset:30720\n\t" \
  "v_mfma_f32_16x16x32_bf16 v[172:175], v[148:151], v[92:95], v[172:175]\n\t" \
  "ds_read_b64_tr_b16 v[78:79], v54 offset:31232\n\t" \
  "v_mfma_f32_16x16x32_bf16 v[180:183], v[148:151], v[96:99], 0\n\t" \
  "ds_read_b128 v[80:83], v52 offset:0\n\t" \
  "v_mfma_f32_16x16x32_bf16 v[180:183], v[144:147], v[100:103], v[180:183]\n\t" \
  "ds_read_b128 v[84:87], v53 offset:0\n\t" \
  "s_waitcnt lgkmcnt(7)\n\t" \
  "v_mfma_f32_16x16x32_bf16 v[216:219], v[64:67], v[72:75], v[216:219]\n\t" \
  "ds_read_b64_tr_b16 v[88:89], v55 offset:26624\n\t" \
  "v_mfma_f32_16x16x32_bf16 v[216:219], v[104:107], v[108:111], v[216:219]\n\t" \
  "ds_read_b64_tr_b16 v[90:91], v55 offset:27136\n\t" \
  "v_mfma_f32_16x16x32_bf16 v[224:227], v[112:115], v[108:111], v[224:227]\n\t" \
  "ds_read_b64_tr_b16 v[92:93], v55 offset:30720\n\t" \
  "v_mfma_f32_16x16x32_bf16 v[224:227], v[116:119], v[72:75], v[224:227]\n\t" \
  "ds_read_b64_tr_b16 v[94:95], v55 offset:31232\n\t" \
  "v_mfma_f32_16x16x32_bf16 v[232:235], v[120:123], v[72:75], v[232:235]\n\t" \
  "ds_read_b64_tr_b16 v[96:97], v54 offset:18432\n\t" \
  "v_mfma_f32_16x16x32_bf16 v[232:235], v[124:127], v[108:111], v[232:235]\n\t" \
  "ds_read_b64_tr_b16 v[98:99], v54 offset:18944\n\t" \
  "v_mfma_f32_16x16x32_bf16 v[240:243], v[128:131], v[108:111], v[240:243]\n\t" \
  "ds_read_b64_tr_b16 v[148:149], v54 offset:22528\n\t" \
  "v_mfma_f32_16x16x32_bf16 v[240:243], v[132:135], v[72:75], v[240:243]\n\t" \
  "ds_read_b64_tr_b16 v[150:151], v54 offset:23040\n\t" \
  "v_mfma_f32_16x16x32_bf16 v[220:223], v[64:67], v[140:143], v[220:223]\n\t" \
  "ds_read_b64_tr_b16 v[100:101], v55 offset:18432\n\t" \
  "s_waitcnt lgkmcnt(7)\n\t" \
  "v_mfma_f32_16x16x32_bf16 v[220:223], v[104:107], v[136:139], v[220:223]\n\t" \
  "ds_read_b64_tr_b16 v[102:103], v55 offset:18944\n\t" \
  "v_mfma_f32_16x16x32_bf16 v[228:231], v[112:115], v[136:139], v[228:231]\n\t" \
  "ds_read_b64_tr_b16 v[144:145], v55 offset:22528\n\t" \
  "v_mfma_f32_16x16x32_bf16 v[228:231], v[116:119], v[140:143], v[228:231]\n\t" \
  "ds_read_b64_tr_b16 v[146:147], v55 offset:23040\n\t" \
  "v_mfma_f32_16x16x32_bf16 v[236:239], v[120:123], v[140:143], v[236:239]\n\t" \
  "v_mfma_f32_16x16x32_bf16 v[236:239], v[124:127], v[136:139], v[236:239]\n\t" \
  "v_mfma_f32_16x16x32_bf16 v[244:247], v[128:131], v[136:139], v[244:247]\n\t" \
  "v_mfma_f32_16x16x32_bf16 v[244:247], v[132:135], v[140:143], v[244:247]\n\t" \
  "v_mfma_f32_16x16x32_bf16 v[184:187], v[68:71], v[64:67], v[184:187]\n\t" \
  "v_mfma_f32_16x16x32_bf16 v[184:187], v[76:79], v[104:107], v[184:187]\n\t" \
  "v_mfma_f32_16x16x32_bf16 v[192:195], v[76:79], v[112:115], v[192:195]\n\t" \
  "v_mfma_f32_16x16x32_bf16 v[192:195], v[68:71], v[116:119], v[192:195]\n\t" \
  "v_mfma_f32_16x16x32_bf16 v[200:203], v[68:71], v[120:123], v[200:203]\n\t" \
  "v_mfma_f32_16x16x32_bf16 v[200:203], v[76:79], v[124:127], v[200:203]\n\t" \
  "v_mfma_f32_16x16x32_bf16 v[208:211], v[76:79], v[128:131], v[208:211]\n\t" \
  "v_mfma_f32_16x16x32_bf16 v[208:211], v[68:71], v[132:135], v[208:211]\n\t" \
  "v_mfma_f32_16x16x32_bf16 v[248:251], v[68:71], v[80:83], v[248:251]\n\t" \
  "v_mfma_f32_16x16x32_bf16 v[248:251], v[76:79], v[84:87], v[248:251]\n\t" \
  "v_mfma_f32_16x16x32_bf16 v[252:255], v[88:91], v[80:83], v[252:255]\n\t" \
  "s_waitcnt lgkmcnt(0)\n\t" \
  "v_mfma_f32_16x16x32_bf16 v[252:255], v[92:95], v[84:87], v[252:255]\n\t" \
  "v_mfma_f32_16x16x32_bf16 v[188:191], v[92:95], v[104:107], v[188:191]\n\t" \
  "v_mfma_f32_16x16x32_bf16 v[188:191], v[88:91], v[64:67], v[188:191]\n\t" \
  "s_barrier\n\t" \
  "v_mfma_f32_16x16x32_bf16 v[196:199], v[88:91], v[116:119], v[196:199]\n\t" \
  "ds_read_b64_tr_b16 v[108:109], v58 offset:34816\n\t" \
  "v_mfma_f32_16x16x32_bf16 v[196:199], v[92:95], v[112:115], v[196:199]\n\t" \
  "ds_read_b64_tr_b16 v[110:111], v58 offset:35328\n\t" \
  "v_mfma_f32_16x16x32_bf16 v[204:207], v[92:95], v[124:127], v[204:207]\n\t" \
  "ds_read_b128 v[72:75], v56 offset:2048\n\t" \
  "v_mfma_f32_16x16x32_bf16 v[204:207], v[88:91], v[120:123], v[204:207]\n\t" \
  "ds_read_b64_tr_b16 v[136:137], v58 offset:38912\n\t" \
  "v_mfma_f32_16x16x32_bf16 v[212:215], v[88:91], v[132:135], v[212:215]\n\t" \
  "ds_read_b64_tr_b16 v[138:139], v58 offset:39424\n\t" \
  "v_mfma_f32_16x16x32_bf16 v[212:215], v[92:95], v[128:131], v[212:215]\n\t" \
  "ds_read_b128 v[140:143], v57 offset:2048\n\t" \
  "v_mfma_f32_16x16x32_bf16 v[152:155], v[96:99], v[64:67], v[152:155]\n\t" \
  "ds_read_b128 v[68:71], v57 offset:4096\n\t" \
  "v_mfma_f32_16x16x32_bf16 v[152:155], v[148:151], v[104:107], v[152:155]\n\t" \
  "ds_read_b128 v[76:79], v56 offset:4096\n\t" \
  "v_mfma_f32_16x16x32_bf16 v[160:163], v[148:151], v[112:115], v[160:163]\n\t" \
  "ds_read_b128 v[80:83], v56 offset:6144\n\t" \
  "v_mfma_f32_16x16x32_bf16 v[160:163], v[96:99], v[116:119], v[160:163]\n\t" \
  "ds_read_b128 v[84:87], v57 offset:6144\n\t" \
  "v_mfma_f32_16x16x32_bf16 v[168:171], v[96:99], v[120:123], v[168:171]\n\t" \
  "ds_read_b128 v[88:91], v57 offset:8192\n\t" \
  "v_mfma_f32_16x16x32_bf16 v[168:171], v[148:151], v[124:127], v[168:171]\n\t" \
  "ds_read_b128 v[92:95], v56 offset:8192\n\t" \
  "v_mfma_f32_16x16x32_bf16 v[176:179], v[148:151], v[128:131], v[176:179]\n\t" \
  "ds_read_b64_tr_b16 v[148:149], v59 offset:34816\n\t" \
  "v_mfma_f32_16x16x32_bf16 v[176:179], v[96:99], v[132:135], v[176:179]\n\t" \
  "ds_read_b64_tr_b16 v[150:151], v59 offset:35328\n\t" \
  "v_mfma_f32_16x16x32_bf16 v[156:159], v[100:103], v[64:67], v[156:159]\n\t" \
  "ds_read_b64_tr_b16 v[96:97], v59 offset:38912\n\t" \
  "v_mfma_f32_16x16x32_bf16 v[156:159], v[144:147], v[104:107], v[156:159]\n\t" \
  "ds_read_b64_tr_b16 v[98:99], v59 offset:39424\n\t" \
  "v_mfma_f32_16x16x32_bf16 v[164:167], v[144:147], v[112:115], v[164:167]\n\t" \
  "ds_read_b64_tr_b16 v[64:65], v58 offset:26624\n\t" \
  "v_mfma_f32_16x16x32_bf16 v[164:167], v[100:103], v[116:119], v[164:167]\n\t" \
  "ds_read_b64_tr_b16 v[66:67], v58 offset:27136\n\t" \
  "v_mfma_f32_16x16x32_bf16 v[172:175], v[100:103], v[120:123], v[172:175]\n\t" \
  "ds_read_b64_tr_b16 v[104:105], v58 offset:30720\n\t" \
  "v_mfma_f32_16x16x32_bf16 v[172:175], v[144:147], v[124:127], v[172:175]\n\t" \
  "ds_read_b64_tr_b16 v[106:107], v58 offset:31232\n\t" \
  "v_mfma_f32_16x16x32_bf16 v[180:183], v[144:147], v[128:131], v[180:183]\n\t" \
  "ds_read_b128 v[112:115], v56 offset:0\n\t" \
  "v_mfma_f32_16x16x32_bf16 v[180:183], v[100:103], v[132:135], v[180:183]\n\t" \
  "ds_read_b128 v[116:119], v57 offset:0\n\t" \
  "s_waitcnt lgkmcnt(7)\n\t" \
  "v_mfma_f32_16x16x32_bf16 v[216:219], v[72:75], v[108:111], v[216:219]\n\t" \
  "ds_read_b64_tr_b16 v[120:121], v59 offset:26624\n\t" \
  "v_mfma_f32_16x16x32_bf16 v[216:219], v[140:143], v[136:139], v[216:219]\n\t" \
  "ds_read_b64_tr_b16 v[122:123], v59 offset:27136\n\t" \
  "v_mfma_f32_16x16x32_bf16 v[224:227], v[68:71], v[136:139], v[224:227]\n\t" \
  "ds_read_b64_tr_b16 v[124:125], v59 offset:30720\n\t" \
  "v_mfma_f32_16x16x32_bf16 v[224:227], v[76:79], v[108:111], v[224:227]\n\t" \
  "ds_read_b64_tr_b16 v[126:127], v59 offset:31232\n\t" \
  "v_mfma_f32_16x16x32_bf16 v[232:235], v[80:83], v[108:111], v[232:235]\n\t" \
  "ds_read_b64_tr_b16 v[128:129], v58 offset:18432\n\t" \
  "v_mfma_f32_16x16x32_bf16 v[232:235], v[84:87], v[136:139], v[232:235]\n\t" \
  "ds_read_b64_tr_b16 v[130:131], v58 offset:18944\n\t" \
  "v_mfma_f32_16x16x32_bf16 v[240:243], v[88:91], v[136:139], v[240:243]\n\t" \
  "ds_read_b64_tr_b16 v[144:145], v58 offset:22528\n\t" \
  "v_mfma_f32_16x16x32_bf16 v[240:243], v[92:95], v[108:111], v[240:243]\n\t" \
  "ds_read_b64_tr_b16 v[146:147], v58 offset:23040\n\t" \
  "v_mfma_f32_16x16x32_bf16 v[220:223], v[72:75], v[148:151], v[220:223]\n\t" \
  "ds_read_b64_tr_b16 v[100:101], v59 offset:18432\n\t" \
  "s_waitcnt lgkmcnt(7)\n\t" \
  "v_mfma_f32_16x16x32_bf16 v[220:223], v[140:143], v[96:99], v[220:223]\n\t" \
  "ds_read_b64_tr_b16 v[102:103], v59 offset:18944\n\t" \
  "v_mfma_f32_16x16x32_bf16 v[228:231], v[68:71], v[96:99], v[228:231]\n\t" \
  "ds_read_b64_tr_b16 v[132:133], v59 offset:22528\n\t" \
  "v_mfma_f32_16x16x32_bf16 v[228:231], v[76:79], v[148:151], v[228:231]\n\t" \
  "ds_read_b64_tr_b16 v[134:135], v59 offset:23040\n\t" \
  "v_mfma_f32_16x16x32_bf16 v[236:239], v[80:83], v[148:151], v[236:239]\n\t" \
  "v_mfma_f32_16x16x32_bf16 v[236:239], v[84:87], v[96:99], v[236:239]\n\t" \
  "v_mfma_f32_16x16x32_bf16 v[244:247], v[88:91], v[96:99], v[244:247]\n\t" \
  "v_mfma_f32_16x16x32_bf16 v[244:247], v[92:95], v[148:151], v[244:247]\n\t" \
  "503:\n\t" \
  "s_cbranch_execz 507f\n\t" \
  "v_mfma_f32_16x16x32_bf16 v[184:187], v[64:67], v[72:75], v[184:187]\n\t" \
  "v_mfma_f32_16x16x32_bf16 v[184:187], v[104:107], v[140:143], v[184:187]\n\t" \
  "v_mfma_f32_16x16x32_bf16 v[192:195], v[104:107], v[68:71], v[192:195]\n\t" \
  "v_mfma_f32_16x16x32_bf16 v[192:195], v[64:67], v[76:79], v[192:195]\n\t" \
  "v_mfma_f32_16x16x32_bf16 v[200:203], v[64:67], v[80:83], v[200:203]\n\t" \
  "v_mfma_f32_16x16x32_bf16 v[200:203], v[104:107], v[84:87], v[200:203]\n\t" \
  "v_mfma_f32_16x16x32_bf16 v[208:211], v[104:107], v[88:91], v[208:211]\n\t" \
  "v_mfma_f32_16x16x32_bf16 v[208:211], v[64:67], v[92:95], v[208:211]\n\t" \
  "v_mfma_f32_16x16x32_bf16 v[248:251], v[64:67], v[112:115], v[248:251]\n\t" \
  "v_mfma_f32_16x16x32_bf16 v[248:251], v[104:107], v[116:119], v[248:251]\n\t" \
  "v_mfma_f32_16x16x32_bf16 v[252:255], v[120:123], v[112:115], v[252:255]\n\t" \
  "s_waitcnt lgkmcnt(0)\n\t" \
  "v_mfma_f32_16x16x32_bf16 v[252:255], v[124:127], v[116:119], v[252:255]\n\t" \
  "v_mfma_f32_16x16x32_bf16 v[188:191], v[124:127], v[140:143], v[188:191]\n\t" \
  "v_mfma_f32_16x16x32_bf16 v[188:191], v[120:123], v[72:75], v[188:191]\n\t" \
  "s_barrier\n\t" \
  "v_mfma_f32_16x16x32_bf16 v[196:199], v[120:123], v[76:79], v[196:199]\n\t" \
  "ds_read_b64_tr_b16 v[136:137], v62 offset:34816\n\t" \
  "v_bfe_u32 v30, v4, 9, 1\n\t" \
  "v_mfma_f32_16x16x32_bf16 v[196:199], v[124:127], v[68:71], v[196:199]\n\t" \
  "ds_read_b64_tr_b16 v[138:139], v62 offset:35328\n\t" \
  "v_mad_i32_i24 v30, v30, -16, v0\n\t" \
  "v_mfma_f32_16x16x32_bf16 v[204:207], v[124:127], v[84:87], v[204:207]\n\t" \
  "ds_read_b128 v[108:111], v60 offset:2048\n\t" \
  "ds_read_b128 v[26:29], v0 offset:1552\n\t" \
  "v_mfma_f32_16x16x32_bf16 v[204:207], v[120:123], v[80:83], v[204:207]\n\t" \
  "ds_read_b64_tr_b16 v[96:97], v62 offset:38912\n\t" \
  "ds_read_b128 v[30:33], v30 offset:1040\n\t" \
  "v_mfma_f32_16x16x32_bf16 v[212:215], v[120:123], v[92:95], v[212:215]\n\t" \
  "ds_read_b64_tr_b16 v[98:99], v62 offset:39424\n\t" \
  "ds_read_b128 v[34:37], v0 offset:512\n\t" \
  "v_mfma_f32_16x16x32_bf16 v[212:215], v[124:127], v[88:91], v[212:215]\n\t" \
  "ds_read_b128 v[148:151], v61 offset:2048\n\t" \
  "ds_read_b64_tr_b16 v[38:39], v1 offset:512\n\t" \
  "v_mfma_f32_16x16x32_bf16 v[152:155], v[128:131], v[72:75], v[152:155]\n\t" \
  "ds_read_b128 v[64:67], v61 offset:4096\n\t" \
  "ds_read_b64_tr_b16 v[40:41], v1 offset:0\n\t" \
  "v_mfma_f32_16x16x32_bf16 v[152:155], v[144:147], v[140:143], v[152:155]\n\t" \
  "ds_read_b128 v[104:107], v60 offset:4096\n\t" \
  "s_waitcnt vmcnt(0)\n\t" \
  "v_mfma_f32_16x16x32_bf16 v[160:163], v[144:147], v[68:71], v[160:163]\n\t" \
  "ds_read_b128 v[112:115], v60 offset:6144\n\t" \
  "v_perm_b32 v18, v10, v10, v14\n\t" \
  "v_mfma_f32_16x16x32_bf16 v[160:163], v[128:131], v[76:79], v[160:163]\n\t" \
  "ds_read_b128 v[116:119], v61 offset:6144\n\t" \
  "v_perm_b32 v19, v10, v10, v15\n\t" \
  "v_mfma_f32_16x16x32_bf16 v[168:171], v[128:131], v[80:83], v[168:171]\n\t" \
  "ds_read_b128 v[120:123], v61 offset:8192\n\t" \
  "v_perm_b32 v20, v10, v10, v16\n\t" \
  "v_mfma_f32_16x16x32_bf16 v[168:171], v[144:147], v[84:87], v[168:171]\n\t" \
  "ds_read_b128 v[124:127], v60 offset:8192\n\t" \
  "v_perm_b32 v21, v10, v10, v17\n\t" \
  "v_mfma_f32_16x16x32_bf16 v[176:179], v[144:147], v[88:91], v[176:179]\n\t" \
  "ds_read_b64_tr_b16 v[144:145], v63 offset:34816\n\t" \
  "v_perm_b32 v22, v11, v11, v14\n\t" \
  "v_mfma_f32_16x16x32_bf16 v[176:179], v[128:131], v[92:95], v[176:179]\n\t" \
  "ds_read_b64_tr_b16 v[146:147], v63 offset:35328\n\t" \
  "v_perm_b32 v23, v11, v11, v15\n\t" \
  "v_mfma_f32_16x16x32_bf16 v[156:159], v[100:103], v[72:75], v[156:159]\n\t" \
  "ds_read_b64_tr_b16 v[128:129], v63 offset:38912\n\t" \
  "v_perm_b32 v24, v11, v11, v16\n\t" \
  "v_mfma_f32_16x16x32_bf16 v[156:159], v[132:135], v[140:143], v[156:159]\n\t" \
  "ds_read_b64_tr_b16 v[130:131], v63 offset:39424\n\t" \
  "v_perm_b32 v25, v11, v11, v17\n\t" \
  "v_mfma_f32_16x16x32_bf16 v[164:167], v[132:135], v[68:71], v[164:167]\n\t" \
  "ds_read_b64_tr_b16 v[72:73], v62 offset:26624\n\t" \
  "v_mfma_f32_16x16x32_bf16 v[164:167], v[100:103], v[76:79], v[164:167]\n\t" \
  "ds_read_b64_tr_b16 v[74:75], v62 offset:27136\n\t" \
  "v_mfma_f32_16x16x32_bf16 v[172:175], v[100:103], v[80:83], v[172:175]\n\t" \
  "ds_read_b64_tr_b16 v[140:141], v62 offset:30720\n\t" \
  "s_waitcnt lgkmcnt(6)\n\t" \
  "v_mfma_f32_16x16x32_bf16 v[42:45], v[26:29], v[18:21], 0\n\t" \
  "v_mfma_f32_16x16x32_bf16 v[42:45], v[30:33], v[22:25], v[42:45]\n\t" \
  "v_mfma_f32_16x16x32_bf16 v[46:49], v[30:33], v[18:21], 0\n\t" \
  "v_mfma_f32_16x16x32_bf16 v[46:49], v[34:37], v[22:25], v[46:49]\n\t" \
  "ds_read_b128 v[26:29], v0 offset:1808\n\t" \
  "ds_read_b128 v[30:33], v0 offset:1296\n\t" \
  "v_mfma_f32_16x16x32_bf16 v[172:175], v[132:135], v[84:87], v[172:175]\n\t" \
  "ds_read_b64_tr_b16 v[142:143], v62 offset:31232\n\t" \
  "ds_read_b128 v[34:37], v0 offset:768\n\t" \
  "v_mfma_f32_16x16x32_bf16 v[180:183], v[132:135], v[88:91], v[180:183]\n\t" \
  "ds_read_b128 v[68:71], v60 offset:0\n\t" \
  "v_mfma_f32_16x16x32_bf16 v[180:183], v[100:103], v[92:95], v[180:183]\n\t" \
  "ds_read_b128 v[76:79], v61 offset:0\n\t" \
  "v_mfma_f32_16x16x32_bf16 v[216:219], v[108:111], v[136:139], v[216:219]\n\t" \
  "ds_read_b64_tr_b16 v[80:81], v63 offset:26624\n\t" \
  "v_lshlrev_b32 v50, 16, v38\n\t" \
  "v_mfma_f32_16x16x32_bf16 v[216:219], v[148:151], v[96:99], v[216:219]\n\t" \
  "ds_read_b64_tr_b16 v[82:83], v63 offset:27136\n\t" \
  "v_lshlrev_b32 v51, 16, v40\n\t" \
  "v_mfma_f32_16x16x32_bf16 v[224:227], v[64:67], v[96:99], v[224:227]\n\t" \
  "ds_read_b64_tr_b16 v[84:85], v63 offset:30720\n\t" \
  "v_mul_f32 v42, v42, v50\n\t" \
  "v_mfma_f32_16x16x32_bf16 v[224:227], v[104:107], v[136:139], v[224:227]\n\t" \
  "ds_read_b64_tr_b16 v[86:87], v63 offset:31232\n\t" \
  "v_mul_f32 v46, v46, v51\n\t" \
  "v_mfma_f32_16x16x32_bf16 v[232:235], v[112:115], v[136:139], v[232:235]\n\t" \
  "ds_read_b64_tr_b16 v[88:89], v62 offset:18432\n\t" \
  "v_cvt_pk_bf16_f32 v42, v42, v46\n\t" \
  "v_mfma_f32_16x16x32_bf16 v[232:235], v[116:119], v[96:99], v[232:235]\n\t" \
  "ds_read_b64_tr_b16 v[90:91], v62 offset:18944\n\t" \
  "v_and_b32 v50, 0xffff0000, v38\n\t" \
  "v_mfma_f32_16x16x32_bf16 v[240:243], v[120:123], v[96:99], v[240:243]\n\t" \
  "ds_read_b64_tr_b16 v[132:133], v62 offset:22528\n\t" \
  "v_and_b32 v51, 0xffff0000, v40\n\t" \
  "v_mfma_f32_16x16x32_bf16 v[240:243], v[124:127], v[136:139], v[240:243]\n\t" \
  "ds_read_b64_tr_b16 v[134:135], v62 offset:23040\n\t" \
  "v_mul_f32 v43, v43, v50\n\t" \
  "s_waitcnt lgkmcnt(7)\n\t" \
  "v_mfma_f32_16x16x32_bf16 v[220:223], v[108:111], v[144:147], v[220:223]\n\t" \
  "ds_read_b64_tr_b16 v[92:93], v63 offset:18432\n\t" \
  "v_mul_f32 v47, v47, v51\n\t" \
  "v_mfma_f32_16x16x32_bf16 v[220:223], v[148:151], v[128:131], v[220:223]\n\t" \
  "ds_read_b64_tr_b16 v[94:95], v63 offset:18944\n\t" \
  "v_cvt_pk_bf16_f32 v43, v43, v47\n\t" \
  "v_mfma_f32_16x16x32_bf16 v[228:231], v[64:67], v[128:131], v[228:231]\n\t" \
  "ds_read_b64_tr_b16 v[100:101], v63 offset:22528\n\t" \
  "v_lshlrev_b32 v50, 16, v39\n\t" \
  "v_mfma_f32_16x16x32_bf16 v[228:231], v[104:107], v[144:147], v[228:231]\n\t" \
  "ds_read_b64_tr_b16 v[102:103], v63 offset:23040\n\t" \
  "v_lshlrev_b32 v51, 16, v41\n\t" \
  "v_mfma_f32_16x16x32_bf16 v[236:239], v[112:115], v[144:147], v[236:239]\n\t" \
  "v_mul_f32 v44, v44, v50\n\t" \
  "v_mul_f32 v48, v48, v51\n\t" \
  "v_mfma_f32_16x16x32_bf16 v[236:239], v[116:119], v[128:131], v[236:239]\n\t" \
  "v_cvt_pk_bf16_f32 v44, v44, v48\n\t" \
  "v_and_b32 v50, 0xffff0000, v39\n\t" \
  "v_mfma_f32_16x16x32_bf16 v[244:247], v[120:123], v[128:131], v[244:247]\n\t" \
  "v_and_b32 v51, 0xffff0000, v41\n\t" \
  "v_mul_f32 v45, v45, v50\n\t" \
  "v_mfma_f32_16x16x32_bf16 v[244:247], v[124:127], v[144:147], v[244:247]\n\t" \
  "v_mul_f32 v49, v49, v51\n\t" \
  "ds_read_b64_tr_b16 v[38:39], v1 offset:768\n\t" \
  "v_mfma_f32_16x16x32_bf16 v[184:187], v[72:75], v[108:111], v[184:187]\n\t" \
  "ds_read_b64_tr_b16 v[40:41], v1 offset:256\n\t" \
  "v_cvt_pk_bf16_f32 v45, v45, v49\n\t" \
  "v_mfma_f32_16x16x32_bf16 v[184:187], v[140:143], v[148:151], v[184:187]\n\t" \
  "v_mfma_f32_16x16x32_bf16 v[192:195], v[140:143], v[64:67], v[192:195]\n\t" \
  "v_mfma_f32_16x16x32_bf16 v[192:195], v[72:75], v[104:107], v[192:195]\n\t" \
  "v_mfma_f32_16x16x32_bf16 v[200:203], v[72:75], v[112:115], v[200:203]\n\t" \
  "v_mfma_f32_16x16x32_bf16 v[200:203], v[140:143], v[116:119], v[200:203]\n\t" \
  "v_mfma_f32_16x16x32_bf16 v[208:211], v[140:143], v[120:123], v[208:211]\n\t" \
  "v_mfma_f32_16x16x32_bf16 v[208:211], v[72:75], v[124:127], v[208:211]\n\t" \
  "v_mfma_f32_16x16x32_bf16 v[248:251], v[72:75], v[68:71], v[248:251]\n\t" \
  "v_mfma_f32_16x16x32_bf16 v[248:251], v[140:143], v[76:79], v[248:251]\n\t" \
  "s_waitcnt lgkmcnt(0)\n\t" \
  "v_mfma_f32_16x16x32_bf16 v[252:255], v[80:83], v[68:71], v[252:255]\n\t" \
  "v_mfma_f32_16x16x32_bf16 v[252:255], v[84:87], v[76:79], v[252:255]\n\t" \
  "v_mfma_f32_16x16x32_bf16 v[188:191], v[84:87], v[148:151], v[188:191]\n\t" \
  "v_mfma_f32_16x16x32_bf16 v[188:191], v[80:83], v[108:111], v[188:191]\n\t" \
  "s_barrier\n\t" \
  "v_mfma_f32_16x16x32_bf16 v[196:199], v[80:83], v[104:107], v[196:199]\n\t" \
  "ds_read_b64_tr_b16 v[96:97], v54 offset:34816\n\t" \
  "v_mfma_f32_16x16x32_bf16 v[196:199], v[84:87], v[64:67], v[196:199]\n\t" \
  "v_mov_b32 v50, 163344\n\t" \
  "ds_read_b64_tr_b16 v[98:99], v54 offset:35328\n\t" \
  "v_mfma_f32_16x16x32_bf16 v[204:207], v[84:87], v[116:119], v[204:207]\n\t" \
  "ds_read_b32 v50, v50 offset:24\n\t" \
  "ds_read_b128 v[136:139], v52 offset:2048\n\t" \
  "v_mfma_f32_16x16x32_bf16 v[204:207], v[80:83], v[112:115], v[204:207]\n\t" \
  "ds_read_b64_tr_b16 v[128:129], v54 offset:38912\n\t" \
  "v_mfma_f32_16x16x32_bf16 v[212:215], v[80:83], v[124:127], v[212:215]\n\t" \
  "ds_read_b64_tr_b16 v[130:131], v54 offset:39424\n\t" \
  "v_mfma_f32_16x16x32_bf16 v[212:215], v[84:87], v[120:123], v[212:215]\n\t" \
  "ds_read_b128 v[144:147], v53 offset:2048\n\t" \
  "v_mfma_f32_16x16x32_bf16 v[152:155], v[88:91], v[108:111], v[152:155]\n\t" \
  "ds_read_b128 v[72:75], v53 offset:4096\n\t" \
  "v_mfma_f32_16x16x32_bf16 v[152:155], v[132:135], v[148:151], v[152:155]\n\t" \
  "ds_read_b128 v[140:143], v52 offset:4096\n\t" \
  "v_mfma_f32_16x16x32_bf16 v[160:163], v[132:135], v[64:67], v[160:163]\n\t" \
  "ds_read_b128 v[68:71], v52 offset:6144\n\t" \
  "v_mfma_f32_16x16x32_bf16 v[160:163], v[88:91], v[104:107], v[160:163]\n\t" \
  "ds_read_b128 v[76:79], v53 offset:6144\n\t" \
  "v_mfma_f32_16x16x32_bf16 v[168:171], v[88:91], v[112:115], v[168:171]\n\t" \
  "ds_read_b128 v[80:83], v53 offset:8192\n\t" \
  "v_mfma_f32_16x16x32_bf16 v[168:171], v[132:135], v[116:119], v[168:171]\n\t" \
  "ds_read_b128 v[84:87], v52 offset:8192\n\t" \
  "v_mfma_f32_16x16x32_bf16 v[176:179], v[132:135], v[120:123], v[176:179]\n\t" \
  "s_waitcnt lgkmcnt(10)\n\t" \
  "v_readfirstlane_b32 s88, v50\n\t" \
  "s_mov_b32 s89, 0\n\t" \
  "ds_read_b64_tr_b16 v[132:133], v55 offset:34816\n\t" \
  "v_mfma_f32_16x16x32_bf16 v[176:179], v[88:91], v[124:127], v[176:179]\n\t" \
  "ds_read_b64_tr_b16 v[134:135], v55 offset:35328\n\t" \
  "v_mfma_f32_16x16x32_bf16 v[156:159], v[92:95], v[108:111], v[156:159]\n\t" \
  "s_xor_b64 %24, %24, s[88:89]\n\t" \
  "ds_read_b64_tr_b16 v[88:89], v55 offset:38912\n\t" \
  "v_mfma_f32_16x16x32_bf16 v[156:159], v[100:103], v[148:151], v[156:159]\n\t" \
  "s_xor_b64 %25, %25, s[88:89]\n\t" \
  "ds_read_b64_tr_b16 v[90:91], v55 offset:39424\n\t" \
  "v_mfma_f32_16x16x32_bf16 v[164:167], v[100:103], v[64:67], v[164:167]\n\t" \
  "s_xor_b64 %14, %14, s[88:89]\n\t" \
  "ds_read_b64_tr_b16 v[108:109], v54 offset:26624\n\t" \
  "v_mfma_f32_16x16x32_bf16 v[164:167], v[92:95], v[104:107], v[164:167]\n\t" \
  "s_xor_b64 %19, %19, s[88:89]\n\t" \
  "ds_read_b64_tr_b16 v[110:111], v54 offset:27136\n\t" \
  "v_mfma_f32_16x16x32_bf16 v[172:175], v[92:95], v[112:115], v[172:175]\n\t" \
  "s_xor_b64 %21, %21, s[88:89]\n\t" \
  "ds_read_b64_tr_b16 v[148:149], v54 offset:30720\n\t" \
  "v_mfma_f32_16x16x32_bf16 v[172:175], v[100:103], v[116:119], v[172:175]\n\t" \
  "s_xor_b64 s[92:93], s[92:93], s[88:89]\n\t" \
  "ds_read_b64_tr_b16 v[150:151], v54 offset:31232\n\t" \
  "v_mfma_f32_16x16x32_bf16 v[180:183], v[100:103], v[120:123], v[180:183]\n\t" \
  "ds_read_b128 v[64:67], v52 offset:0\n\t" \
  "v_mfma_f32_16x16x32_bf16 v[180:183], v[92:95], v[124:127], v[180:183]\n\t" \
  "ds_read_b128 v[104:107], v53 offset:0\n\t" \
  "s_waitcnt lgkmcnt(7)\n\t" \
  "v_mfma_f32_16x16x32_bf16 v[216:219], v[136:139], v[96:99], v[216:219]\n\t" \
  "ds_read_b64_tr_b16 v[112:113], v55 offset:26624\n\t" \
  "v_mfma_f32_16x16x32_bf16 v[216:219], v[144:147], v[128:131], v[216:219]\n\t" \
  "ds_read_b64_tr_b16 v[114:115], v55 offset:27136\n\t" \
  "v_mfma_f32_16x16x32_bf16 v[224:227], v[72:75], v[128:131], v[224:227]\n\t" \
  "ds_read_b64_tr_b16 v[116:117], v55 offset:30720\n\t" \
  "v_mfma_f32_16x16x32_bf16 v[224:227], v[140:143], v[96:99], v[224:227]\n\t" \
  "ds_read_b64_tr_b16 v[118:119], v55 offset:31232\n\t" \
  "v_mfma_f32_16x16x32_bf16 v[232:235], v[68:71], v[96:99], v[232:235]\n\t" \
  "ds_read_b64_tr_b16 v[100:101], v54 offset:18432\n\t" \
  "v_mfma_f32_16x16x32_bf16 v[232:235], v[76:79], v[128:131], v[232:235]\n\t" \
  "ds_read_b64_tr_b16 v[102:103], v54 offset:18944\n\t" \
  "v_mfma_f32_16x16x32_bf16 v[240:243], v[80:83], v[128:131], v[240:243]\n\t" \
  "ds_read_b64_tr_b16 v[120:121], v54 offset:22528\n\t" \
  "v_mfma_f32_16x16x32_bf16 v[240:243], v[84:87], v[96:99], v[240:243]\n\t" \
  "ds_read_b64_tr_b16 v[122:123], v54 offset:23040\n\t" \
  "v_mfma_f32_16x16x32_bf16 v[220:223], v[136:139], v[132:135], v[220:223]\n\t" \
  "ds_read_b64_tr_b16 v[92:93], v55 offset:18432\n\t" \
  "s_waitcnt lgkmcnt(7)\n\t" \
  "v_mfma_f32_16x16x32_bf16 v[220:223], v[144:147], v[88:91], v[220:223]\n\t" \
  "ds_read_b64_tr_b16 v[94:95], v55 offset:18944\n\t" \
  "v_mfma_f32_16x16x32_bf16 v[228:231], v[72:75], v[88:91], v[228:231]\n\t" \
  "ds_read_b64_tr_b16 v[124:125], v55 offset:22528\n\t" \
  "v_mfma_f32_16x16x32_bf16 v[228:231], v[140:143], v[132:135], v[228:231]\n\t" \
  "ds_read_b64_tr_b16 v[126:127], v55 offset:23040\n\t" \
  "v_mfma_f32_16x16x32_bf16 v[236:239], v[68:71], v[132:135], v[236:239]\n\t" \
  "v_mfma_f32_16x16x32_bf16 v[236:239], v[76:79], v[88:91], v[236:239]\n\t" \
  "v_mfma_f32_16x16x32_bf16 v[244:247], v[80:83], v[88:91], v[244:247]\n\t" \
  "v_mfma_f32_16x16x32_bf16 v[244:247], v[84:87], v[132:135], v[244:247]\n\t" \
  "v_mfma_f32_16x16x32_bf16 v[184:187], v[108:111], v[136:139], v[184:187]\n\t" \
  "v_mfma_f32_16x16x32_bf16 v[184:187], v[148:151], v[144:147], v[184:187]\n\t" \
  "v_mfma_f32_16x16x32_bf16 v[192:195], v[148:151], v[72:75], v[192:195]\n\t" \
  "v_mfma_f32_16x16x32_bf16 v[192:195], v[108:111], v[140:143], v[192:195]\n\t" \
  "v_mfma_f32_16x16x32_bf16 v[200:203], v[108:111], v[68:71], v[200:203]\n\t" \
  "global_store_dwordx4 v2, v[42:45], %24 offset:0 sc1\n\t" \
  "v_mfma_f32_16x16x32_bf16 v[200:203], v[148:151], v[76:79], v[200:203]\n\t" \
  "s_nop 0\n\t" \
  "v_mfma_f32_16x16x32_bf16 v[42:45], v[26:29], v[18:21], 0\n\t" \
  "v_mfma_f32_16x16x32_bf16 v[42:45], v[30:33], v[22:25], v[42:45]\n\t" \
  "v_mfma_f32_16x16x32_bf16 v[46:49], v[30:33], v[18:21], 0\n\t" \
  "v_mfma_f32_16x16x32_bf16 v[46:49], v[34:37], v[22:25], v[46:49]\n\t" \
  "v_bfe_u32 v30, v4, 9, 1\n\t" \
  "v_mad_i32_i24 v30, v30, -16, v0\n\t" \
  "v_mfma_f32_16x16x32_bf16 v[208:211], v[148:151], v[80:83], v[208:211]\n\t" \
  "ds_read_b128 v[26:29], v0 offset:1584\n\t" \
  "ds_read_b128 v[30:33], v30 offset:1072\n\t" \
  "v_mfma_f32_16x16x32_bf16 v[208:211], v[108:111], v[84:87], v[208:211]\n\t" \
  "ds_read_b128 v[34:37], v0 offset:544\n\t" \
  "v_perm_b32 v18, v12, v12, v14\n\t" \
  "v_mfma_f32_16x16x32_bf16 v[248:251], v[108:111], v[64:67], v[248:251]\n\t" \
  "v_perm_b32 v19, v12, v12, v15\n\t" \
  "v_perm_b32 v20, v12, v12, v16\n\t" \
  "v_mfma_f32_16x16x32_bf16 v[248:251], v[148:151], v[104:107], v[248:251]\n\t" \
  "v_perm_b32 v21, v12, v12, v17\n\t" \
  "v_perm_b32 v22, v13, v13, v14\n\t" \
  "v_mfma_f32_16x16x32_bf16 v[252:255], v[112:115], v[64:67], v[252:255]\n\t" \
  "v_perm_b32 v23, v13, v13, v15\n\t" \
  "v_perm_b32 v24, v13, v13, v16\n\t" \
  "s_waitcnt lgkmcnt(3)\n\t" \
  "v_mfma_f32_16x16x32_bf16 v[252:255], v[116:119], v[104:107], v[252:255]\n\t" \
  "v_perm_b32 v25, v13, v13, v17\n\t" \
  "v_lshlrev_b32 v50, 16, v38\n\t" \
  "v_mfma_f32_16x16x32_bf16 v[188:191], v[116:119], v[144:147], v[188:191]\n\t" \
  "v_lshlrev_b32 v51, 16, v40\n\t" \
  "v_mul_f32 v42, v42, v50\n\t" \
  "v_mfma_f32_16x16x32_bf16 v[188:191], v[112:115], v[136:139], v[188:191]\n\t" \
  "v_mul_f32 v46, v46, v51\n\t" \
  "v_cvt_pk_bf16_f32 v42, v42, v46\n\t" \
  "s_waitcnt lgkmcnt(0)\n\t" \
  "s_barrier\n\t" \
  "v_mfma_f32_16x16x32_bf16 v[196:199], v[112:115], v[140:143], v[196:199]\n\t" \
  "ds_read_b64_tr_b16 v[128:129], v58 offset:34816\n\t" \
  "v_and_b32 v50, 0xffff0000, v38\n\t" \
  "v_mfma_f32_16x16x32_bf16 v[196:199], v[116:119], v[72:75], v[196:199]\n\t" \
  "ds_read_b64_tr_b16 v[130:131], v58 offset:35328\n\t" \
  "v_and_b32 v51, 0xffff0000, v40\n\t" \
  "v_mfma_f32_16x16x32_bf16 v[204:207], v[116:119], v[76:79], v[204:207]\n\t" \
  "ds_read_b128 v[96:99], v56 offset:2048\n\t" \
  "v_mul_f32 v43, v43, v50\n\t" \
  "v_mfma_f32_16x16x32_bf16 v[204:207], v[112:115], v[68:71], v[204:207]\n\t" \
  "ds_read_b64_tr_b16 v[88:89], v58 offset:38912\n\t" \
  "v_mul_f32 v47, v47, v51\n\t" \
  "v_mfma_f32_16x16x32_bf16 v[212:215], v[112:115], v[84:87], v[212:215]\n\t" \
  "ds_read_b64_tr_b16 v[90:91], v58 offset:39424\n\t" \
  "v_cvt_pk_bf16_f32 v43, v43, v47\n\t" \
  "v_mfma_f32_16x16x32_bf16 v[212:215], v[116:119], v[80:83], v[212:215]\n\t" \
  "ds_read_b128 v[132:135], v57 offset:2048\n\t" \
  "v_lshlrev_b32 v50, 16, v39\n\t" \
  "v_mfma_f32_16x16x32_bf16 v[152:155], v[100:103], v[136:139], v[152:155]\n\t" \
  "ds_read_b128 v[108:111], v57 offset:4096\n\t" \
  "v_lshlrev_b32 v51, 16, v41\n\t" \
  "v_mfma_f32_16x16x32_bf16 v[152:155], v[120:123], v[144:147], v[152:155]\n\t" \
  "ds_read_b128 v[148:151], v56 offset:4096\n\t" \
  "v_mul_f32 v44, v44, v50\n\t" \
  "v_mfma_f32_16x16x32_bf16 v[160:163], v[120:123], v[72:75], v[160:163]\n\t" \
  "ds_read_b128 v[64:67], v56 offset:6144\n\t" \
  "v_mul_f32 v48, v48, v51\n\t" \
  "v_mfma_f32_16x16x32_bf16 v[160:163], v[100:103], v[140:143], v[160:163]\n\t" \
  "ds_read_b128 v[104:107], v57 offset:6144\n\t" \
  "v_cvt_pk_bf16_f32 v44, v44, v48\n\t" \
  "v_mfma_f32_16x16x32_bf16 v[168:171], v[100:103], v[68:71], v[168:171]\n\t" \
  "ds_read_b128 v[112:115], v57 offset:8192\n\t" \
  "v_and_b32 v50, 0xffff0000, v39\n\t" \
  "v_mfma_f32_16x16x32_bf16 v[168:171], v[120:123], v[76:79], v[168:171]\n\t" \
  "ds_read_b128 v[116:119], v56 offset:8192\n\t" \
  "v_and_b32 v51, 0xffff0000, v41\n\t" \
  "v_mfma_f32_16x16x32_bf16 v[176:179], v[120:123], v[80:83], v[176:179]\n\t" \
  "ds_read_b64_tr_b16 v[120:121], v59 offset:34816\n\t" \
  "v_mul_f32 v45, v45, v50\n\t" \
  "v_mfma_f32_16x16x32_bf16 v[176:179], v[100:103], v[84:87], v[176:179]\n\t" \
  "ds_read_b64_tr_b16 v[122:123], v59 offset:35328\n\t" \
  "v_mul_f32 v49, v49, v51\n\t" \
  "v_mfma_f32_16x16x32_bf16 v[156:159], v[92:95], v[136:139], v[156:159]\n\t" \
  "ds_read_b64_tr_b16 v[100:101], v59 offset:38912\n\t" \
  "ds_read_b64_tr_b16 v[38:39], v1 offset:544\n\t" \
  "v_mfma_f32_16x16x32_bf16 v[156:159], v[124:127], v[144:147], v[156:159]\n\t" \
  "ds_read_b64_tr_b16 v[102:103], v59 offset:39424\n\t" \
  "ds_read_b64_tr_b16 v[40:41], v1 offset:32\n\t" \
  "v_mfma_f32_16x16x32_bf16 v[164:167], v[124:127], v[72:75], v[164:167]\n\t" \
  "ds_read_b64_tr_b16 v[136:137], v58 offset:26624\n\t" \
  "v_cvt_pk_bf16_f32 v45, v45, v49\n\t" \
  "v_mfma_f32_16x16x32_bf16 v[164:167], v[92:95], v[140:143], v[164:167]\n\t" \
  "ds_read_b64_tr_b16 v[138:139], v58 offset:27136\n\t" \
  "v_mfma_f32_16x16x32_bf16 v[172:175], v[92:95], v[68:71], v[172:175]\n\t" \
  "ds_read_b64_tr_b16 v[144:145], v58 offset:30720\n\t" \
  "v_mfma_f32_16x16x32_bf16 v[172:175], v[124:127], v[76:79], v[172:175]\n\t" \
  "ds_read_b64_tr_b16 v[146:147], v58 offset:31232\n\t" \
  "v_mfma_f32_16x16x32_bf16 v[180:183], v[124:127], v[80:83], v[180:183]\n\t" \
  "ds_read_b128 v[72:75], v56 offset:0\n\t" \
  "v_mfma_f32_16x16x32_bf16 v[180:183], v[92:95], v[84:87], v[180:183]\n\t" \
  "ds_read_b128 v[140:143], v57 offset:0\n\t" \
  "s_waitcnt lgkmcnt(8)\n\t" \
  "v_mfma_f32_16x16x32_bf16 v[216:219], v[96:99], v[128:131], v[216:219]\n\t" \
  "ds_read_b64_tr_b16 v[68:69], v59 offset:26624\n\t" \
  "v_mfma_f32_16x16x32_bf16 v[216:219], v[132:135], v[88:91], v[216:219]\n\t" \
  "ds_read_b64_tr_b16 v[70:71], v59 offset:27136\n\t" \
  "v_mfma_f32_16x16x32_bf16 v[224:227], v[108:111], v[88:91], v[224:227]\n\t" \
  "ds_read_b64_tr_b16 v[76:77], v59 offset:30720\n\t" \
  "v_mfma_f32_16x16x32_bf16 v[224:227], v[148:151], v[128:131], v[224:227]\n\t" \
  "ds_read_b64_tr_b16 v[78:79], v59 offset:31232\n\t" \
  "v_mfma_f32_16x16x32_bf16 v[232:235], v[64:67], v[128:131], v[232:235]\n\t" \
  "ds_read_b64_tr_b16 v[80:81], v58 offset:18432\n\t" \
  "v_mfma_f32_16x16x32_bf16 v[232:235], v[104:107], v[88:91], v[232:235]\n\t" \
  "ds_read_b64_tr_b16 v[82:83], v58 offset:18944\n\t" \
  "v_mfma_f32_16x16x32_bf16 v[240:243], v[112:115], v[88:91], v[240:243]\n\t" \
  "ds_read_b64_tr_b16 v[124:125], v58 offset:22528\n\t" \
  "v_mfma_f32_16x16x32_bf16 v[240:243], v[116:119], v[128:131], v[240:243]\n\t" \
  "ds_read_b64_tr_b16 v[126:127], v58 offset:23040\n\t" \
  "v_mfma_f32_16x16x32_bf16 v[220:223], v[96:99], v[120:123], v[220:223]\n\t" \
  "ds_read_b64_tr_b16 v[84:85], v59 offset:18432\n\t" \
  "s_waitcnt lgkmcnt(7)\n\t" \
  "v_mfma_f32_16x16x32_bf16 v[220:223], v[132:135], v[100:103], v[220:223]\n\t" \
  "ds_read_b64_tr_b16 v[86:87], v59 offset:18944\n\t" \
  "v_mfma_f32_16x16x32_bf16 v[228:231], v[108:111], v[100:103], v[228:231]\n\t" \
  "ds_read_b64_tr_b16 v[92:93], v59 offset:22528\n\t" \
  "v_mfma_f32_16x16x32_bf16 v[228:231], v[148:151], v[120:123], v[228:231]\n\t" \
  "ds_read_b64_tr_b16 v[94:95], v59 offset:23040\n\t" \
  "v_mfma_f32_16x16x32_bf16 v[236:239], v[64:67], v[120:123], v[236:239]\n\t" \
  "v_mfma_f32_16x16x32_bf16 v[236:239], v[104:107], v[100:103], v[236:239]\n\t" \
  "v_mfma_f32_16x16x32_bf16 v[244:247], v[112:115], v[100:103], v[244:247]\n\t" \
  "v_mfma_f32_16x16x32_bf16 v[244:247], v[116:119], v[120:123], v[244:247]\n\t" \
  "v_mfma_f32_16x16x32_bf16 v[184:187], v[136:139], v[96:99], v[184:187]\n\t" \
  "v_mfma_f32_16x16x32_bf16 v[184:187], v[144:147], v[132:135], v[184:187]\n\t" \
  "v_mfma_f32_16x16x32_bf16 v[192:195], v[144:147], v[108:111], v[192:195]\n\t" \
  "v_mfma_f32_16x16x32_bf16 v[192:195], v[136:139], v[148:151], v[192:195]\n\t" \
  "v_mfma_f32_16x16x32_bf16 v[200:203], v[136:139], v[64:67], v[200:203]\n\t" \
  "global_store_dwordx4 v2, v[42:45], %24 offset:1024 sc1\n\t" \
  "v_add_u32 v2, 0x20000, v2\n\t" \
  "v_mfma_f32_16x16x32_bf16 v[200:203], v[144:147], v[104:107], v[200:203]\n\t" \
  "v_mfma_f32_16x16x32_bf16 v[208:211], v[144:147], v[112:115], v[208:211]\n\t" \
  "v_mfma_f32_16x16x32_bf16 v[42:45], v[26:29], v[18:21], 0\n\t" \
  "v_mfma_f32_16x16x32_bf16 v[42:45], v[30:33], v[22:25], v[42:45]\n\t" \
  "v_mfma_f32_16x16x32_bf16 v[46:49], v[30:33], v[18:21], 0\n\t" \
  "v_mfma_f32_16x16x32_bf16 v[46:49], v[34:37], v[22:25], v[46:49]\n\t" \
  "ds_read_b128 v[26:29], v0 offset:1840\n\t" \
  "ds_read_b128 v[30:33], v0 offset:1328\n\t" \
  "v_mfma_f32_16x16x32_bf16 v[208:211], v[136:139], v[116:119], v[208:211]\n\t" \
  "ds_read_b128 v[34:37], v0 offset:800\n\t" \
  "v_mfma_f32_16x16x32_bf16 v[248:251], v[136:139], v[72:75], v[248:251]\n\t" \
  "v_mfma_f32_16x16x32_bf16 v[248:251], v[144:147], v[140:143], v[248:251]\n\t" \
  "v_lshlrev_b32 v50, 16, v38\n\t" \
  "v_lshlrev_b32 v51, 16, v40\n\t" \
  "v_mfma_f32_16x16x32_bf16 v[252:255], v[68:71], v[72:75], v[252:255]\n\t" \
  "v_mul_f32 v42, v42, v50\n\t" \
  "v_mul_f32 v46, v46, v51\n\t" \
  "s_waitcnt lgkmcnt(3)\n\t" \
  "v_mfma_f32_16x16x32_bf16 v[252:255], v[76:79], v[140:143], v[252:255]\n\t" \
  "v_cvt_pk_bf16_f32 v42, v42, v46\n\t" \
  "v_and_b32 v50, 0xffff0000, v38\n\t" \
  "v_mfma_f32_16x16x32_bf16 v[188:191], v[76:79], v[132:135], v[188:191]\n\t" \
  "v_and_b32 v51, 0xffff0000, v40\n\t" \
  "v_mul_f32 v43, v43, v50\n\t" \
  "v_mfma_f32_16x16x32_bf16 v[188:191], v[68:71], v[96:99], v[188:191]\n\t" \
  "v_mul_f32 v47, v47, v51\n\t" \
  "v_cvt_pk_bf16_f32 v43, v43, v47\n\t" \
  "s_waitcnt lgkmcnt(0)\n\t" \
  "s_barrier\n\t" \
  "v_mfma_f32_16x16x32_bf16 v[196:199], v[68:71], v[148:151], v[196:199]\n\t" \
  "ds_read_b64_tr_b16 v[88:89], v62 offset:34816\n\t" \
  "v_lshlrev_b32 v50, 16, v39\n\t" \
  "v_mfma_f32_16x16x32_bf16 v[196:199], v[76:79], v[108:111], v[196:199]\n\t" \
  "ds_read_b64_tr_b16 v[90:91], v62 offset:35328\n\t" \
  "v_lshlrev_b32 v51, 16, v41\n\t" \
  "v_mfma_f32_16x16x32_bf16 v[204:207], v[76:79], v[104:107], v[204:207]\n\t" \
  "ds_read_b128 v[128:131], v60 offset:2048\n\t" \
  "v_mul_f32 v44, v44, v50\n\t" \
  "v_mfma_f32_16x16x32_bf16 v[204:207], v[68:71], v[64:67], v[204:207]\n\t" \
  "ds_read_b64_tr_b16 v[100:101], v62 offset:38912\n\t" \
  "v_mul_f32 v48, v48, v51\n\t" \
  "v_mfma_f32_16x16x32_bf16 v[212:215], v[68:71], v[116:119], v[212:215]\n\t" \
  "ds_read_b64_tr_b16 v[102:103], v62 offset:39424\n\t" \
  "v_cvt_pk_bf16_f32 v44, v44, v48\n\t" \
  "v_mfma_f32_16x16x32_bf16 v[212:215], v[76:79], v[112:115], v[212:215]\n\t" \
  "ds_read_b128 v[120:123], v61 offset:2048\n\t" \
  "v_and_b32 v50, 0xffff0000, v39\n\t" \
  "v_mfma_f32_16x16x32_bf16 v[152:155], v[80:83], v[96:99], v[152:155]\n\t" \
  "ds_read_b128 v[136:139], v61 offset:4096\n\t" \
  "v_and_b32 v51, 0xffff0000, v41\n\t" \
  "v_mfma_f32_16x16x32_bf16 v[152:155], v[124:127], v[132:135], v[152:155]\n\t" \
  "ds_read_b128 v[144:147], v60 offset:4096\n\t" \
  "v_mul_f32 v45, v45, v50\n\t" \
  "v_mfma_f32_16x16x32_bf16 v[160:163], v[124:127], v[108:111], v[160:163]\n\t" \
  "ds_read_b128 v[72:75], v60 offset:6144\n\t" \
  "v_mul_f32 v49, v49, v51\n\t" \
  "v_mfma_f32_16x16x32_bf16 v[160:163], v[80:83], v[148:151], v[160:163]\n\t" \
  "ds_read_b128 v[140:143], v61 offset:6144\n\t" \
  "ds_read_b64_tr_b16 v[38:39], v1 offset:800\n\t" \
  "v_mfma_f32_16x16x32_bf16 v[168:171], v[80:83], v[64:67], v[168:171]\n\t" \
  "ds_read_b128 v[68:71], v61 offset:8192\n\t" \
  "ds_read_b64_tr_b16 v[40:41], v1 offset:288\n\t" \
  "v_mfma_f32_16x16x32_bf16 v[168:171], v[124:127], v[104:107], v[168:171]\n\t" \
  "ds_read_b128 v[76:79], v60 offset:8192\n\t" \
  "v_cvt_pk_bf16_f32 v45, v45, v49\n\t" \
  "v_mfma_f32_16x16x32_bf16 v[176:179], v[124:127], v[112:115], v[176:179]\n\t" \
  "ds_read_b64_tr_b16 v[124:125], v63 offset:34816\n\t" \
  "v_mfma_f32_16x16x32_bf16 v[176:179], v[80:83], v[116:119], v[176:179]\n\t" \
  "ds_read_b64_tr_b16 v[126:127], v63 offset:35328\n\t" \
  "v_mfma_f32_16x16x32_bf16 v[156:159], v[84:87], v[96:99], v[156:159]\n\t" \
  "ds_read_b64_tr_b16 v[80:81], v63 offset:38912\n\t" \
  "v_mfma_f32_16x16x32_bf16 v[156:159], v[92:95], v[132:135], v[156:159]\n\t" \
  "ds_read_b64_tr_b16 v[82:83], v63 offset:39424\n\t" \
  "v_mfma_f32_16x16x32_bf16 v[164:167], v[92:95], v[108:111], v[164:167]\n\t" \
  "ds_read_b64_tr_b16 v[96:97], v62 offset:26624\n\t" \
  "v_mfma_f32_16x16x32_bf16 v[164:167], v[84:87], v[148:151], v[164:167]\n\t" \
  "ds_read_b64_tr_b16 v[98:99], v62 offset:27136\n\t" \
  "v_mfma_f32_16x16x32_bf16 v[172:175], v[84:87], v[64:67], v[172:175]\n\t" \
  "ds_read_b64_tr_b16 v[132:133], v62 offset:30720\n\t" \
  "v_mfma_f32_16x16x32_bf16 v[172:175], v[92:95], v[104:107], v[172:175]\n\t" \
  "ds_read_b64_tr_b16 v[134:135], v62 offset:31232\n\t" \
  "v_mfma_f32_16x16x32_bf16 v[180:183], v[92:95], v[112:115], v[180:183]\n\t" \
  "ds_read_b128 v[108:111], v60 offset:0\n\t" \
  "v_mfma_f32_16x16x32_bf16 v[180:183], v[84:87], v[116:119], v[180:183]\n\t" \
  "ds_read_b128 v[148:151], v61 offset:0\n\t" \
  "s_waitcnt lgkmcnt(7)\n\t" \
  "v_mfma_f32_16x16x32_bf16 v[216:219], v[128:131], v[88:91], v[216:219]\n\t" \
  "ds_read_b64_tr_b16 v[64:65], v63 offset:26624\n\t" \
  "v_mfma_f32_16x16x32_bf16 v[216:219], v[120:123], v[100:103], v[216:219]\n\t" \
  "ds_read_b64_tr_b16 v[66:67], v63 offset:27136\n\t" \
  "v_mfma_f32_16x16x32_bf16 v[224:227], v[136:139], v[100:103], v[224:227]\n\t" \
  "ds_read_b64_tr_b16 v[104:105], v63 offset:30720\n\t" \
  "v_mfma_f32_16x16x32_bf16 v[224:227], v[144:147], v[88:91], v[224:227]\n\t" \
  "ds_read_b64_tr_b16 v[106:107], v63 offset:31232\n\t" \
  "v_mfma_f32_16x16x32_bf16 v[232:235], v[72:75], v[88:91], v[232:235]\n\t" \
  "ds_read_b64_tr_b16 v[92:93], v62 offset:18432\n\t" \
  "v_mfma_f32_16x16x32_bf16 v[232:235], v[140:143], v[100:103], v[232:235]\n\t" \
  "ds_read_b64_tr_b16 v[94:95], v62 offset:18944\n\t" \
  "v_mfma_f32_16x16x32_bf16 v[240:243], v[68:71], v[100:103], v[240:243]\n\t" \
  "ds_read_b64_tr_b16 v[112:113], v62 offset:22528\n\t" \
  "v_mfma_f32_16x16x32_bf16 v[240:243], v[76:79], v[88:91], v[240:243]\n\t" \
  "ds_read_b64_tr_b16 v[114:115], v62 offset:23040\n\t" \
  "v_mfma_f32_16x16x32_bf16 v[220:223], v[128:131], v[124:127], v[220:223]\n\t" \
  "ds_read_b64_tr_b16 v[84:85], v63 offset:18432\n\t" \
  "s_waitcnt lgkmcnt(7)\n\t" \
  "v_mfma_f32_16x16x32_bf16 v[220:223], v[120:123], v[80:83], v[220:223]\n\t" \
  "ds_read_b64_tr_b16 v[86:87], v63 offset:18944\n\t" \
  "v_mfma_f32_16x16x32_bf16 v[228:231], v[136:139], v[80:83], v[228:231]\n\t" \
  "ds_read_b64_tr_b16 v[116:117], v63 offset:22528\n\t" \
  "v_mfma_f32_16x16x32_bf16 v[228:231], v[144:147], v[124:127], v[228:231]\n\t" \
  "ds_read_b64_tr_b16 v[118:119], v63 offset:23040\n\t" \
  "v_mfma_f32_16x16x32_bf16 v[236:239], v[72:75], v[124:127], v[236:239]\n\t" \
  "v_mfma_f32_16x16x32_bf16 v[236:239], v[140:143], v[80:83], v[236:239]\n\t" \
  "v_mfma_f32_16x16x32_bf16 v[244:247], v[68:71], v[80:83], v[244:247]\n\t" \
  "v_mfma_f32_16x16x32_bf16 v[244:247], v[76:79], v[124:127], v[244:247]\n\t" \
  "v_mfma_f32_16x16x32_bf16 v[184:187], v[96:99], v[128:131], v[184:187]\n\t" \
  "v_mfma_f32_16x16x32_bf16 v[184:187], v[132:135], v[120:123], v[184:187]\n\t" \
  "v_mfma_f32_16x16x32_bf16 v[192:195], v[132:135], v[136:139], v[192:195]\n\t" \
  "v_mfma_f32_16x16x32_bf16 v[192:195], v[96:99], v[144:147], v[192:195]\n\t" \
  "v_mfma_f32_16x16x32_bf16 v[200:203], v[96:99], v[72:75], v[200:203]\n\t" \
  "global_store_dwordx4 v2, v[42:45], %24 offset:0 sc1\n\t" \
  "v_mfma_f32_16x16x32_bf16 v[200:203], v[132:135], v[140:143], v[200:203]\n\t" \
  "s_nop 0\n\t" \
  "v_mfma_f32_16x16x32_bf16 v[42:45], v[26:29], v[18:21], 0\n\t" \
  "v_mfma_f32_16x16x32_bf16 v[42:45], v[30:33], v[22:25], v[42:45]\n\t" \
  "v_mfma_f32_16x16x32_bf16 v[46:49], v[30:33], v[18:21], 0\n\t" \
  "v_mfma_f32_16x16x32_bf16 v[46:49], v[34:37], v[22:25], v[46:49]\n\t" \
  "v_mfma_f32_16x16x32_bf16 v[208:211], v[132:135], v[68:71], v[208:211]\n\t" \
  "v_mfma_f32_16x16x32_bf16 v[208:211], v[96:99], v[76:79], v[208:211]\n\t" \
  "v_lshlrev_b32 v50, 16, v38\n\t" \
  "v_lshlrev_b32 v51, 16, v40\n\t" \
  "v_mfma_f32_16x16x32_bf16 v[248:251], v[96:99], v[108:111], v[248:251]\n\t" \
  "s_nop 0\n\t" \
  "v_mul_f32 v42, v42, v50\n\t" \
  "s_nop 0\n\t" \
  "v_mul_f32 v46, v46, v51\n\t" \
  "v_mfma_f32_16x16x32_bf16 v[248:251], v[132:135], v[148:151], v[248:251]\n\t" \
  "v_cvt_pk_bf16_f32 v42, v42, v46\n\t" \
  "v_and_b32 v50, 0xffff0000, v38\n\t" \
  "v_mfma_f32_16x16x32_bf16 v[252:255], v[64:67], v[108:111], v[252:255]\n\t" \
  "v_and_b32 v51, 0xffff0000, v40\n\t" \
  "v_mul_f32 v43, v43, v50\n\t" \
  "s_waitcnt lgkmcnt(0)\n\t" \
  "v_mfma_f32_16x16x32_bf16 v[252:255], v[104:107], v[148:151], v[252:255]\n\t" \
  "v_mul_f32 v47, v47, v51\n\t" \
  "v_cvt_pk_bf16_f32 v43, v43, v47\n\t" \
  "v_mfma_f32_16x16x32_bf16 v[188:191], v[104:107], v[120:123], v[188:191]\n\t" \
  "v_lshlrev_b32 v50, 16, v39\n\t" \
  "v_lshlrev_b32 v51, 16, v41\n\t" \
  "v_mfma_f32_16x16x32_bf16 v[188:191], v[64:67], v[128:131], v[188:191]\n\t" \
  "v_mul_f32 v44, v44, v50\n\t" \
  "v_mul_f32 v48, v48, v51\n\t" \
  "s_barrier\n\t" \
  "v_mfma_f32_16x16x32_bf16 v[196:199], v[64:67], v[144:147], v[196:199]\n\t" \
  "ds_read_b64_tr_b16 v[100:101], v54 offset:34816\n\t" \
  "v_cvt_pk_bf16_f32 v44, v44, v48\n\t" \
  "v_mfma_f32_16x16x32_bf16 v[196:199], v[104:107], v[136:139], v[196:199]\n\t" \
  "ds_read_b64_tr_b16 v[102:103], v54 offset:35328\n\t" \
  "v_and_b32 v50, 0xffff0000, v39\n\t" \
  "v_mfma_f32_16x16x32_bf16 v[204:207], v[104:107], v[140:143], v[204:207]\n\t" \
  "ds_read_b128 v[88:91], v52 offset:2048\n\t" \
  "v_and_b32 v51, 0xffff0000, v41\n\t" \
  "v_mfma_f32_16x16x32_bf16 v[204:207], v[64:67], v[72:75], v[204:207]\n\t" \
  "ds_read_b64_tr_b16 v[80:81], v54 offset:38912\n\t" \
  "v_mul_f32 v45, v45, v50\n\t" \
  "v_mfma_f32_16x16x32_bf16 v[212:215], v[64:67], v[76:79], v[212:215]\n\t" \
  "ds_read_b64_tr_b16 v[82:83], v54 offset:39424\n\t" \
  "v_mul_f32 v49, v49, v51\n\t" \
  "v_mfma_f32_16x16x32_bf16 v[212:215], v[104:107], v[68:71], v[212:215]\n\t" \
  "ds_read_b128 v[124:127], v53 offset:2048\n\t" \
  "v_cvt_pk_bf16_f32 v45, v45, v49\n\t" \
  "v_mfma_f32_16x16x32_bf16 v[152:155], v[92:95], v[128:131], v[152:155]\n\t" \
  "ds_read_b128 v[96:99], v53 offset:4096\n\t" \
  "v_mfma_f32_16x16x32_bf16 v[152:155], v[112:115], v[120:123], v[152:155]\n\t" \
  "ds_read_b128 v[132:135], v52 offset:4096\n\t" \
  "v_mfma_f32_16x16x32_bf16 v[160:163], v[112:115], v[136:139], v[160:163]\n\t" \
  "ds_read_b128 v[108:111], v52 offset:6144\n\t" \
  "504:\n\t" \
  "s_cbranch_execz 508f\n\t" \
  "v_mfma_f32_16x16x32_bf16 v[160:163], v[92:95], v[144:147], v[160:163]\n\t" \
  "ds_read_b128 v[148:151], v53 offset:6144\n\t" \
  "v_mfma_f32_16x16x32_bf16 v[168:171], v[92:95], v[72:75], v[168:171]\n\t" \
  "ds_read_b128 v[64:67], v53 offset:8192\n\t" \
  "v_mfma_f32_16x16x32_bf16 v[168:171], v[112:115], v[140:143], v[168:171]\n\t" \
  "ds_read_b128 v[104:107], v52 offset:8192\n\t" \
  "v_mfma_f32_16x16x32_bf16 v[176:179], v[112:115], v[68:71], v[176:179]\n\t" \
  "ds_read_b64_tr_b16 v[112:113], v55 offset:34816\n\t" \
  "v_mfma_f32_16x16x32_bf16 v[176:179], v[92:95], v[76:79], v[176:179]\n\t" \
  "ds_read_b64_tr_b16 v[114:115], v55 offset:35328\n\t" \
  "v_mfma_f32_16x16x32_bf16 v[156:159], v[84:87], v[128:131], v[156:159]\n\t" \
  "ds_read_b64_tr_b16 v[92:93], v55 offset:38912\n\t" \
  "v_mfma_f32_16x16x32_bf16 v[156:159], v[116:119], v[120:123], v[156:159]\n\t" \
  "ds_read_b64_tr_b16 v[94:95], v55 offset:39424\n\t" \
  "v_mfma_f32_16x16x32_bf16 v[164:167], v[116:119], v[136:139], v[164:167]\n\t" \
  "ds_read_b64_tr_b16 v[128:129], v54 offset:26624\n\t" \
  "v_mfma_f32_16x16x32_bf16 v[164:167], v[84:87], v[144:147], v[164:167]\n\t" \
  "ds_read_b64_tr_b16 v[130:131], v54 offset:27136\n\t" \
  "v_mfma_f32_16x16x32_bf16 v[172:175], v[84:87], v[72:75], v[172:175]\n\t" \
  "ds_read_b64_tr_b16 v[120:121], v54 offset:30720\n\t" \
  "v_mfma_f32_16x16x32_bf16 v[172:175], v[116:119], v[140:143], v[172:175]\n\t" \
  "ds_read_b64_tr_b16 v[122:123], v54 offset:31232\n\t" \
  "v_mfma_f32_16x16x32_bf16 v[180:183], v[116:119], v[68:71], v[180:183]\n\t" \
  "ds_read_b128 v[136:139], v52 offset:0\n\t" \
  "v_mfma_f32_16x16x32_bf16 v[180:183], v[84:87], v[76:79], v[180:183]\n\t" \
  "ds_read_b128 v[144:147], v53 offset:0\n\t" \
  "s_waitcnt lgkmcnt(7)\n\t" \
  "v_mfma_f32_16x16x32_bf16 v[216:219], v[88:91], v[100:103], v[216:219]\n\t" \
  "ds_read_b64_tr_b16 v[72:73], v55 offset:26624\n\t" \
  "v_mfma_f32_16x16x32_bf16 v[216:219], v[124:127], v[80:83], v[216:219]\n\t" \
  "ds_read_b64_tr_b16 v[74:75], v55 offset:27136\n\t" \
  "v_mfma_f32_16x16x32_bf16 v[224:227], v[96:99], v[80:83], v[224:227]\n\t" \
  "ds_read_b64_tr_b16 v[140:141], v55 offset:30720\n\t" \
  "v_mfma_f32_16x16x32_bf16 v[224:227], v[132:135], v[100:103], v[224:227]\n\t" \
  "ds_read_b64_tr_b16 v[142:143], v55 offset:31232\n\t" \
  "v_mfma_f32_16x16x32_bf16 v[232:235], v[108:111], v[100:103], v[232:235]\n\t" \
  "ds_read_b64_tr_b16 v[68:69], v54 offset:18432\n\t" \
  "v_mfma_f32_16x16x32_bf16 v[232:235], v[148:151], v[80:83], v[232:235]\n\t" \
  "ds_read_b64_tr_b16 v[70:71], v54 offset:18944\n\t" \
  "v_mfma_f32_16x16x32_bf16 v[240:243], v[64:67], v[80:83], v[240:243]\n\t" \
  "ds_read_b64_tr_b16 v[116:117], v54 offset:22528\n\t" \
  "v_mfma_f32_16x16x32_bf16 v[240:243], v[104:107], v[100:103], v[240:243]\n\t" \
  "ds_read_b64_tr_b16 v[118:119], v54 offset:23040\n\t" \
  "v_mfma_f32_16x16x32_bf16 v[220:223], v[88:91], v[112:115], v[220:223]\n\t" \
  "ds_read_b64_tr_b16 v[76:77], v55 offset:18432\n\t" \
  "s_waitcnt lgkmcnt(7)\n\t" \
  "v_mfma_f32_16x16x32_bf16 v[220:223], v[124:127], v[92:95], v[220:223]\n\t" \
  "ds_read_b64_tr_b16 v[78:79], v55 offset:18944\n\t" \
  "v_mfma_f32_16x16x32_bf16 v[228:231], v[96:99], v[92:95], v[228:231]\n\t" \
  "ds_read_b64_tr_b16 v[84:85], v55 offset:22528\n\t" \
  "v_mfma_f32_16x16x32_bf16 v[228:231], v[132:135], v[112:115], v[228:231]\n\t" \
  "ds_read_b64_tr_b16 v[86:87], v55 offset:23040\n\t" \
  "v_mfma_f32_16x16x32_bf16 v[236:239], v[108:111], v[112:115], v[236:239]\n\t" \
  "v_mfma_f32_16x16x32_bf16 v[236:239], v[148:151], v[92:95], v[236:239]\n\t" \
  "v_mfma_f32_16x16x32_bf16 v[244:247], v[64:67], v[92:95], v[244:247]\n\t" \
  "v_mfma_f32_16x16x32_bf16 v[244:247], v[104:107], v[112:115], v[244:247]\n\t" \
  "v_mfma_f32_16x16x32_bf16 v[184:187], v[128:131], v[88:91], v[184:187]\n\t" \
  "v_mfma_f32_16x16x32_bf16 v[184:187], v[120:123], v[124:127], v[184:187]\n\t" \
  "v_mfma_f32_16x16x32_bf16 v[192:195], v[120:123], v[96:99], v[192:195]\n\t" \
  "v_mfma_f32_16x16x32_bf16 v[192:195], v[128:131], v[132:135], v[192:195]\n\t" \
  "v_mfma_f32_16x16x32_bf16 v[200:203], v[128:131], v[108:111], v[200:203]\n\t" \
  "global_store_dwordx4 v2, v[42:45], %24 offset:1024 sc1\n\t" \
  "v_mfma_f32_16x16x32_bf16 v[200:203], v[120:123], v[148:151], v[200:203]\n\t" \
  "v_mfma_f32_16x16x32_bf16 v[208:211], v[120:123], v[64:67], v[208:211]\n\t" \
  "v_mfma_f32_16x16x32_bf16 v[208:211], v[128:131], v[104:107], v[208:211]\n\t" \
  "v_mov_b32 v30, 0xff61b1e6\n\t" \
  "v_bfe_i32 v34, v3, 0, 1\n\t" \
  "v_mfma_f32_16x16x32_bf16 v[248:251], v[128:131], v[136:139], v[248:251]\n\t" \
  "v_and_b32 v34, v30, v34\n\t" \
  "v_bfe_i32 v38, v3, 4, 1\n\t" \
  "v_mfma_f32_16x16x32_bf16 v[248:251], v[120:123], v[144:147], v[248:251]\n\t" \
  "v_and_b32 v38, v30, v38\n\t" \
  "v_bfe_i32 v42, v3, 8, 1\n\t" \
  "v_mfma_f32_16x16x32_bf16 v[252:255], v[72:75], v[136:139], v[252:255]\n\t" \
  "v_bfi_b32 v42, v42, v30, %15\n\t" \
  "v_mov_b32 v48, 0x3f803f80\n\t" \
  "s_waitcnt lgkmcnt(0)\n\t" \
  "v_mfma_f32_16x16x32_bf16 v[252:255], v[140:143], v[144:147], v[252:255]\n\t" \
  "v_bfe_i32 v35, v3, 1, 1\n\t" \
  "v_and_b32 v35, v30, v35\n\t" \
  "v_mfma_f32_16x16x32_bf16 v[188:191], v[140:143], v[124:127], v[188:191]\n\t" \
  "v_bfe_i32 v39, v3, 5, 1\n\t" \
  "v_and_b32 v39, v30, v39\n\t" \
  "v_mfma_f32_16x16x32_bf16 v[188:191], v[72:75], v[88:91], v[188:191]\n\t" \
  "v_bfe_i32 v43, v3, 9, 1\n\t" \
  "v_bfi_b32 v43, v43, v30, %15\n\t" \
  "s_barrier\n\t" \
  "v_mfma_f32_16x16x32_bf16 v[196:199], v[72:75], v[132:135], v[196:199]\n\t" \
  "ds_read_b64_tr_b16 v[80:81], v58 offset:34816\n\t" \
  "v_mov_b32 v49, 0x3f803f80\n\t" \
  "v_mfma_f32_16x16x32_bf16 v[196:199], v[140:143], v[96:99], v[196:199]\n\t" \
  "ds_read_b64_tr_b16 v[82:83], v58 offset:35328\n\t" \
  "v_bfe_i32 v36, v3, 2, 1\n\t" \
  "v_mfma_f32_16x16x32_bf16 v[204:207], v[140:143], v[148:151], v[204:207]\n\t" \
  "ds_read_b128 v[100:103], v56 offset:2048\n\t" \
  "v_and_b32 v36, v30, v36\n\t" \
  "v_mfma_f32_16x16x32_bf16 v[204:207], v[72:75], v[108:111], v[204:207]\n\t" \
  "ds_read_b64_tr_b16 v[92:93], v58 offset:38912\n\t" \
  "v_bfe_i32 v40, v3, 6, 1\n\t" \
  "v_mfma_f32_16x16x32_bf16 v[212:215], v[72:75], v[104:107], v[212:215]\n\t" \
  "ds_read_b64_tr_b16 v[94:95], v58 offset:39424\n\t" \
  "v_and_b32 v40, v30, v40\n\t" \
  "v_mfma_f32_16x16x32_bf16 v[212:215], v[140:143], v[64:67], v[212:215]\n\t" \
  "ds_read_b128 v[112:115], v57 offset:2048\n\t" \
  "v_bfe_i32 v44, v3, 10, 1\n\t" \
  "v_mfma_f32_16x16x32_bf16 v[152:155], v[68:71], v[88:91], v[152:155]\n\t" \
  "ds_read_b128 v[128:131], v57 offset:4096\n\t" \
  "v_bfi_b32 v44, v44, v30, %15\n\t" \
  "v_mfma_f32_16x16x32_bf16 v[152:155], v[116:119], v[124:127], v[152:155]\n\t" \
  "ds_read_b128 v[120:123], v56 offset:4096\n\t" \
  "v_mov_b32 v50, 0x3f803f80\n\t" \
  "v_mfma_f32_16x16x32_bf16 v[160:163], v[116:119], v[96:99], v[160:163]\n\t" \
  "ds_read_b128 v[136:139], v56 offset:6144\n\t" \
  "v_bfe_i32 v37, v3, 3, 1\n\t" \
  "v_mfma_f32_16x16x32_bf16 v[160:163], v[68:71], v[132:135], v[160:163]\n\t" \
  "ds_read_b128 v[144:147], v57 offset:6144\n\t" \
  "v_and_b32 v37, v30, v37\n\t" \
  "v_mfma_f32_16x16x32_bf16 v[168:171], v[68:71], v[108:111], v[168:171]\n\t" \
  "ds_read_b128 v[72:75], v57 offset:8192\n\t" \
  "v_bfe_i32 v41, v3, 7, 1\n\t" \
  "v_mfma_f32_16x16x32_bf16 v[168:171], v[116:119], v[148:151], v[168:171]\n\t" \
  "ds_read_b128 v[140:143], v56 offset:8192\n\t" \
  "v_and_b32 v41, v30, v41\n\t" \
  "v_mfma_f32_16x16x32_bf16 v[176:179], v[116:119], v[64:67], v[176:179]\n\t" \
  "ds_read_b64_tr_b16 v[116:117], v59 offset:34816\n\t" \
  "v_bfe_i32 v45, v3, 11, 1\n\t" \
  "v_mfma_f32_16x16x32_bf16 v[176:179], v[68:71], v[104:107], v[176:179]\n\t" \
  "ds_read_b64_tr_b16 v[118:119], v59 offset:35328\n\t" \
  "v_bfi_b32 v45, v45, v30, %15\n\t" \
  "v_mfma_f32_16x16x32_bf16 v[156:159], v[76:79], v[88:91], v[156:159]\n\t" \
  "ds_read_b64_tr_b16 v[68:69], v59 offset:38912\n\t" \
  "v_mov_b32 v51, 0x3f803f80\n\t" \
  "v_mfma_f32_16x16x32_bf16 v[156:159], v[84:87], v[124:127], v[156:159]\n\t" \
  "ds_read_b64_tr_b16 v[70:71], v59 offset:39424\n\t" \
  "v_bfe_i32 v46, v3, 12, 1\n\t" \
  "v_mfma_f32_16x16x32_bf16 v[164:167], v[84:87], v[96:99], v[164:167]\n\t" \
  "ds_read_b64_tr_b16 v[88:89], v58 offset:26624\n\t" \
  "v_bfe_i32 v31, v3, 13, 1\n\t" \
  "v_mfma_f32_16x16x32_bf16 v[164:167], v[76:79], v[132:135], v[164:167]\n\t" \
  "ds_read_b64_tr_b16 v[90:91], v58 offset:27136\n\t" \
  "v_perm_b32 v46, v31, v46, vcc_hi\n\t" \
  "v_mfma_f32_16x16x32_bf16 v[172:175], v[76:79], v[108:111], v[172:175]\n\t" \
  "ds_read_b64_tr_b16 v[124:125], v58 offset:30720\n\t" \
  "v_bfe_i32 v47, v3, 14, 1\n\t" \
  "v_mfma_f32_16x16x32_bf16 v[172:175], v[84:87], v[148:151], v[172:175]\n\t" \
  "ds_read_b64_tr_b16 v[126:127], v58 offset:31232\n\t" \
  "v_bfe_i32 v31, v3, 15, 1\n\t" \
  "v_mfma_f32_16x16x32_bf16 v[180:183], v[84:87], v[64:67], v[180:183]\n\t" \
  "ds_read_b128 v[96:99], v56 offset:0\n\t" \
  "v_perm_b32 v47, v31, v47, vcc_hi\n\t" \
  "v_mfma_f32_16x16x32_bf16 v[180:183], v[76:79], v[104:107], v[180:183]\n\t" \
  "ds_read_b128 v[132:135], v57 offset:0\n\t" \
  "v_add_u32 v30, 18432, v4\n\t" \
  "s_waitcnt lgkmcnt(7)\n\t" \
  "v_mfma_f32_16x16x32_bf16 v[216:219], v[100:103], v[80:83], v[216:219]\n\t" \
  "ds_read_b64_tr_b16 v[108:109], v59 offset:26624\n\t" \
  "v_xor_b32 v31, 1024, v30\n\t" \
  "v_mfma_f32_16x16x32_bf16 v[216:219], v[112:115], v[92:95], v[216:219]\n\t" \
  "ds_read_b64_tr_b16 v[110:111], v59 offset:27136\n\t" \
  "v_mov_b32 v32, %12\n\t" \
  "v_mfma_f32_16x16x32_bf16 v[224:227], v[128:131], v[92:95], v[224:227]\n\t" \
  "ds_read_b64_tr_b16 v[148:149], v59 offset:30720\n\t" \
  "v_xor_b32 v33, 4, v32\n\t" \
  "v_mfma_f32_16x16x32_bf16 v[224:227], v[120:123], v[80:83], v[224:227]\n\t" \
  "ds_read_b64_tr_b16 v[150:151], v59 offset:31232\n\t" \
  "v_mov_b32 v0, 1\n\t" \
  "v_mfma_f32_16x16x32_bf16 v[232:235], v[136:139], v[80:83], v[232:235]\n\t" \
  "ds_read_b64_tr_b16 v[64:65], v58 offset:18432\n\t" \
  "v_and_b32 v4, 1023, v4\n\t" \
  "v_mfma_f32_16x16x32_bf16 v[232:235], v[144:147], v[92:95], v[232:235]\n\t" \
  "ds_read_b64_tr_b16 v[66:67], v58 offset:18944\n\t" \
  "v_add_u32 v1, 16384, v4\n\t" \
  "v_mfma_f32_16x16x32_bf16 v[240:243], v[72:75], v[92:95], v[240:243]\n\t" \
  "ds_read_b64_tr_b16 v[84:85], v58 offset:22528\n\t" \
  "v_mfma_f32_16x16x32_bf16 v[240:243], v[140:143], v[80:83], v[240:243]\n\t" \
  "ds_read_b64_tr_b16 v[86:87], v58 offset:23040\n\t" \
  "v_mfma_f32_16x16x32_bf16 v[220:223], v[100:103], v[116:119], v[220:223]\n\t" \
  "ds_read_b64_tr_b16 v[76:77], v59 offset:18432\n\t" \
  "s_waitcnt lgkmcnt(7)\n\t" \
  "v_mfma_f32_16x16x32_bf16 v[220:223], v[112:115], v[68:71], v[220:223]\n\t" \
  "ds_read_b64_tr_b16 v[78:79], v59 offset:18944\n\t" \
  "v_mfma_f32_16x16x32_bf16 v[228:231], v[128:131], v[68:71], v[228:231]\n\t" \
  "ds_read_b64_tr_b16 v[104:105], v59 offset:22528\n\t" \
  "v_mfma_f32_16x16x32_bf16 v[228:231], v[120:123], v[116:119], v[228:231]\n\t" \
  "ds_read_b64_tr_b16 v[106:107], v59 offset:23040\n\t" \
  "v_mfma_f32_16x16x32_bf16 v[236:239], v[136:139], v[116:119], v[236:239]\n\t" \
  "v_mfma_f32_16x16x32_bf16 v[236:239], v[144:147], v[68:71], v[236:239]\n\t" \
  "v_mfma_f32_16x16x32_bf16 v[244:247], v[72:75], v[68:71], v[244:247]\n\t" \
  "v_mfma_f32_16x16x32_bf16 v[244:247], v[140:143], v[116:119], v[244:247]\n\t" \
  "v_mfma_f32_16x16x32_bf16 v[184:187], v[88:91], v[100:103], v[184:187]\n\t" \
  "v_mfma_f32_16x16x32_bf16 v[184:187], v[124:127], v[112:115], v[184:187]\n\t" \
  "v_mfma_f32_16x16x32_bf16 v[192:195], v[124:127], v[128:131], v[192:195]\n\t" \
  "v_mfma_f32_16x16x32_bf16 v[192:195], v[88:91], v[120:123], v[192:195]\n\t" \
  "v_mfma_f32_16x16x32_bf16 v[200:203], v[88:91], v[136:139], v[200:203]\n\t" \
  "v_mfma_f32_16x16x32_bf16 v[200:203], v[124:127], v[144:147], v[200:203]\n\t" \
  "v_mfma_f32_16x16x32_bf16 v[208:211], v[124:127], v[72:75], v[208:211]\n\t" \
  "v_mfma_f32_16x16x32_bf16 v[208:211], v[88:91], v[140:143], v[208:211]\n\t" \
  "v_mfma_f32_16x16x32_bf16 v[248:251], v[88:91], v[96:99], v[248:251]\n\t" \
  "v_mfma_f32_16x16x32_bf16 v[248:251], v[124:127], v[132:135], v[248:251]\n\t" \
  "v_mfma_f32_16x16x32_bf16 v[252:255], v[108:111], v[96:99], v[252:255]\n\t" \
  "s_waitcnt lgkmcnt(0)\n\t" \
  "v_mfma_f32_16x16x32_bf16 v[252:255], v[148:151], v[132:135], v[252:255]\n\t" \
  "v_mfma_f32_16x16x32_bf16 v[188:191], v[148:151], v[112:115], v[188:191]\n\t" \
  "v_mfma_f32_16x16x32_bf16 v[188:191], v[108:111], v[100:103], v[188:191]\n\t" \
  "s_barrier\n\t" \
  "v_mfma_f32_16x16x32_bf16 v[196:199], v[108:111], v[120:123], v[196:199]\n\t" \
  "ds_read_b64_tr_b16 v[92:93], v62 offset:34816\n\t" \
  "v_mfma_f32_16x16x32_bf16 v[196:199], v[148:151], v[128:131], v[196:199]\n\t" \
  "ds_read_b64_tr_b16 v[94:95], v62 offset:35328\n\t" \
  "v_mfma_f32_16x16x32_bf16 v[204:207], v[148:151], v[144:147], v[204:207]\n\t" \
  "ds_read_b128 v[80:83], v60 offset:2048\n\t" \
  "v_mfma_f32_16x16x32_bf16 v[204:207], v[108:111], v[136:139], v[204:207]\n\t" \
  "ds_read_b64_tr_b16 v[68:69], v62 offset:38912\n\t" \
  "v_mfma_f32_16x16x32_bf16 v[212:215], v[108:111], v[140:143], v[212:215]\n\t" \
  "ds_read_b64_tr_b16 v[70:71], v62 offset:39424\n\t" \
  "v_mfma_f32_16x16x32_bf16 v[212:215], v[148:151], v[72:75], v[212:215]\n\t" \
  "ds_read_b128 v[116:119], v61 offset:2048\n\t" \
  "v_mfma_f32_16x16x32_bf16 v[152:155], v[64:67], v[100:103], v[152:155]\n\t" \
  "ds_read_b128 v[88:91], v61 offset:4096\n\t" \
  "v_mfma_f32_16x16x32_bf16 v[152:155], v[84:87], v[112:115], v[152:155]\n\t" \
  "ds_read_b128 v[124:127], v60 offset:4096\n\t" \
  "v_mfma_f32_16x16x32_bf16 v[160:163], v[84:87], v[128:131], v[160:163]\n\t" \
  "ds_read_b128 v[96:99], v60 offset:6144\n\t" \
  "v_mfma_f32_16x16x32_bf16 v[160:163], v[64:67], v[120:123], v[160:163]\n\t" \
  "ds_read_b128 v[132:135], v61 offset:6144\n\t" \
  "v_mfma_f32_16x16x32_bf16 v[168:171], v[64:67], v[136:139], v[168:171]\n\t" \
  "ds_read_b128 v[108:111], v61 offset:8192\n\t" \
  "v_mfma_f32_16x16x32_bf16 v[168:171], v[84:87], v[144:147], v[168:171]\n\t" \
  "ds_read_b128 v[148:151], v60 offset:8192\n\t" \
  "v_mfma_f32_16x16x32_bf16 v[176:179], v[84:87], v[72:75], v[176:179]\n\t" \
  "ds_read_b64_tr_b16 v[84:85], v63 offset:34816\n\t" \
  "v_mfma_f32_16x16x32_bf16 v[176:179], v[64:67], v[140:143], v[176:179]\n\t" \
  "ds_read_b64_tr_b16 v[86:87], v63 offset:35328\n\t" \
  "v_mfma_f32_16x16x32_bf16 v[156:159], v[76:79], v[100:103], v[156:159]\n\t" \
  "ds_read_b64_tr_b16 v[64:65], v63 offset:38912\n\t" \
  "v_mfma_f32_16x16x32_bf16 v[156:159], v[104:107], v[112:115], v[156:159]\n\t" \
  "ds_read_b64_tr_b16 v[66:67], v63 offset:39424\n\t" \
  "v_mfma_f32_16x16x32_bf16 v[164:167], v[104:107], v[128:131], v[164:167]\n\t" \
  "ds_read_b64_tr_b16 v[100:101], v62 offset:26624\n\t" \
  "v_mfma_f32_16x16x32_bf16 v[164:167], v[76:79], v[120:123], v[164:167]\n\t" \
  "ds_read_b64_tr_b16 v[102:103], v62 offset:27136\n\t" \
  "v_mfma_f32_16x16x32_bf16 v[172:175], v[76:79], v[136:139], v[172:175]\n\t" \
  "ds_read_b64_tr_b16 v[112:113], v62 offset:30720\n\t" \
  "v_mfma_f32_16x16x32_bf16 v[172:175], v[104:107], v[144:147], v[172:175]\n\t" \
  "ds_read_b64_tr_b16 v[114:115], v62 offset:31232\n\t" \
  "v_mfma_f32_16x16x32_bf16 v[180:183], v[104:107], v[72:75], v[180:183]\n\t" \
  "ds_read_b128 v[128:131], v60 offset:0\n\t" \
  "v_mfma_f32_16x16x32_bf16 v[180:183], v[76:79], v[140:143], v[180:183]\n\t" \
  "ds_read_b128 v[120:123], v61 offset:0\n\t" \
  "s_waitcnt lgkmcnt(7)\n\t" \
  "v_mfma_f32_16x16x32_bf16 v[216:219], v[80:83], v[92:95], v[216:219]\n\t" \
  "ds_read_b64_tr_b16 v[136:137], v63 offset:26624\n\t" \
  "v_mfma_f32_16x16x32_bf16 v[216:219], v[116:119], v[68:71], v[216:219]\n\t" \
  "ds_read_b64_tr_b16 v[138:139], v63 offset:27136\n\t" \
  "v_mfma_f32_16x16x32_bf16 v[224:227], v[88:91], v[68:71], v[224:227]\n\t" \
  "ds_read_b64_tr_b16 v[144:145], v63 offset:30720\n\t" \
  "v_mfma_f32_16x16x32_bf16 v[224:227], v[124:127], v[92:95], v[224:227]\n\t" \
  "ds_read_b64_tr_b16 v[146:147], v63 offset:31232\n\t" \
  "v_mfma_f32_16x16x32_bf16 v[232:235], v[96:99], v[92:95], v[232:235]\n\t" \
  "ds_read_b64_tr_b16 v[72:73], v62 offset:18432\n\t" \
  "v_mfma_f32_16x16x32_bf16 v[232:235], v[132:135], v[68:71], v[232:235]\n\t" \
  "ds_read_b64_tr_b16 v[74:75], v62 offset:18944\n\t" \
  "v_mfma_f32_16x16x32_bf16 v[240:243], v[108:111], v[68:71], v[240:243]\n\t" \
  "ds_read_b64_tr_b16 v[104:105], v62 offset:22528\n\t" \
  "v_mfma_f32_16x16x32_bf16 v[240:243], v[148:151], v[92:95], v[240:243]\n\t" \
  "ds_read_b64_tr_b16 v[106:107], v62 offset:23040\n\t" \
  "v_mfma_f32_16x16x32_bf16 v[220:223], v[80:83], v[84:87], v[220:223]\n\t" \
  "ds_read_b64_tr_b16 v[76:77], v63 offset:18432\n\t" \
  "s_waitcnt lgkmcnt(7)\n\t" \
  "v_mfma_f32_16x16x32_bf16 v[220:223], v[116:119], v[64:67], v[220:223]\n\t" \
  "ds_read_b64_tr_b16 v[78:79], v63 offset:18944\n\t" \
  "v_mfma_f32_16x16x32_bf16 v[228:231], v[88:91], v[64:67], v[228:231]\n\t" \
  "ds_read_b64_tr_b16 v[140:141], v63 offset:22528\n\t" \
  "v_mfma_f32_16x16x32_bf16 v[228:231], v[124:127], v[84:87], v[228:231]\n\t" \
  "ds_read_b64_tr_b16 v[142:143], v63 offset:23040\n\t" \
  "v_mfma_f32_16x16x32_bf16 v[236:239], v[96:99], v[84:87], v[236:239]\n\t" \
  "v_mfma_f32_16x16x32_bf16 v[236:239], v[132:135], v[64:67], v[236:239]\n\t" \
  "v_mfma_f32_16x16x32_bf16 v[244:247], v[108:111], v[64:67], v[244:247]\n\t" \
  "v_mfma_f32_16x16x32_bf16 v[244:247], v[148:151], v[84:87], v[244:247]\n\t" \
  "v_mfma_f32_16x16x32_bf16 v[184:187], v[100:103], v[80:83], v[184:187]\n\t" \
  "v_mfma_f32_16x16x32_bf16 v[184:187], v[112:115], v[116:119], v[184:187]\n\t" \
  "v_mfma_f32_16x16x32_bf16 v[192:195], v[112:115], v[88:91], v[192:195]\n\t" \
  "v_mfma_f32_16x16x32_bf16 v[192:195], v[100:103], v[124:127], v[192:195]\n\t" \
  "v_mfma_f32_16x16x32_bf16 v[200:203], v[100:103], v[96:99], v[200:203]\n\t" \
  "v_mfma_f32_16x16x32_bf16 v[200:203], v[112:115], v[132:135], v[200:203]\n\t" \
  "v_mfma_f32_16x16x32_bf16 v[208:211], v[112:115], v[108:111], v[208:211]\n\t" \
  "v_mfma_f32_16x16x32_bf16 v[208:211], v[100:103], v[148:151], v[208:211]\n\t" \
  "v_mfma_f32_16x16x32_bf16 v[248:251], v[100:103], v[128:131], v[248:251]\n\t" \
  "v_mfma_f32_16x16x32_bf16 v[248:251], v[112:115], v[120:123], v[248:251]\n\t" \
  "v_mfma_f32_16x16x32_bf16 v[252:255], v[136:139], v[128:131], v[252:255]\n\t" \
  "s_waitcnt lgkmcnt(0)\n\t" \
  "v_mfma_f32_16x16x32_bf16 v[252:255], v[144:147], v[120:123], v[252:255]\n\t" \
  "v_mfma_f32_16x16x32_bf16 v[188:191], v[144:147], v[116:119], v[188:191]\n\t" \
  "v_mfma_f32_16x16x32_bf16 v[188:191], v[136:139], v[80:83], v[188:191]\n\t" \
  "s_waitcnt vmcnt(0)\n\t" \
  "s_barrier\n\t" \
  "v_mfma_f32_16x16x32_bf16 v[196:199], v[136:139], v[124:127], v[196:199]\n\t" \
  "ds_read_b64_tr_b16 v[68:69], v54 offset:34816\n\t" \
  "v_mfma_f32_16x16x32_bf16 v[196:199], v[144:147], v[88:91], v[196:199]\n\t" \
  "ds_read_b64_tr_b16 v[70:71], v54 offset:35328\n\t" \
  "v_mfma_f32_16x16x32_bf16 v[204:207], v[144:147], v[132:135], v[204:207]\n\t" \
  "ds_read_b128 v[92:95], v52 offset:2048\n\t" \
  "v_mfma_f32_16x16x32_bf16 v[204:207], v[136:139], v[96:99], v[204:207]\n\t" \
  "ds_read_b64_tr_b16 v[64:65], v54 offset:38912\n\t" \
  "v_mfma_f32_16x16x32_bf16 v[212:215], v[136:139], v[148:151], v[212:215]\n\t" \
  "ds_read_b64_tr_b16 v[66:67], v54 offset:39424\n\t" \
  "v_mfma_f32_16x16x32_bf16 v[212:215], v[144:147], v[108:111], v[212:215]\n\t" \
  "ds_read_b128 v[84:87], v53 offset:2048\n\t" \
  "v_mfma_f32_16x16x32_bf16 v[152:155], v[72:75], v[80:83], v[152:155]\n\t" \
  "ds_read_b128 v[100:103], v53 offset:4096\n\t" \
  "v_mfma_f32_16x16x32_bf16 v[152:155], v[104:107], v[116:119], v[152:155]\n\t" \
  "ds_read_b128 v[112:115], v52 offset:4096\n\t" \
  "v_mfma_f32_16x16x32_bf16 v[160:163], v[104:107], v[88:91], v[160:163]\n\t" \
  "ds_read_b128 v[128:131], v52 offset:6144\n\t" \
  "v_mfma_f32_16x16x32_bf16 v[160:163], v[72:75], v[124:127], v[160:163]\n\t" \
  "ds_read_b128 v[120:123], v53 offset:6144\n\t" \
  "v_mfma_f32_16x16x32_bf16 v[168:171], v[72:75], v[96:99], v[168:171]\n\t" \
  "ds_read_b128 v[136:139], v53 offset:8192\n\t" \
  "v_mfma_f32_16x16x32_bf16 v[168:171], v[104:107], v[132:135], v[168:171]\n\t" \
  "ds_read_b128 v[144:147], v52 offset:8192\n\t" \
  "v_mfma_f32_16x16x32_bf16 v[176:179], v[104:107], v[108:111], v[176:179]\n\t" \
  "ds_read_b64_tr_b16 v[104:105], v55 offset:34816\n\t" \
  "v_mfma_f32_16x16x32_bf16 v[176:179], v[72:75], v[148:151], v[176:179]\n\t" \
  "ds_read_b64_tr_b16 v[106:107], v55 offset:35328\n\t" \
  "v_mfma_f32_16x16x32_bf16 v[156:159], v[76:79], v[80:83], v[156:159]\n\t" \
  "ds_read_b64_tr_b16 v[72:73], v55 offset:38912\n\t" \
  "v_mfma_f32_16x16x32_bf16 v[156:159], v[140:143], v[116:119], v[156:159]\n\t" \
  "ds_read_b64_tr_b16 v[74:75], v55 offset:39424\n\t" \
  "v_mfma_f32_16x16x32_bf16 v[164:167], v[140:143], v[88:91], v[164:167]\n\t" \
  "ds_read_b64_tr_b16 v[80:81], v54 offset:26624\n\t" \
  "v_mfma_f32_16x16x32_bf16 v[164:167], v[76:79], v[124:127], v[164:167]\n\t" \
  "ds_read_b64_tr_b16 v[82:83], v54 offset:27136\n\t" \
  "v_mfma_f32_16x16x32_bf16 v[172:175], v[76:79], v[96:99], v[172:175]\n\t" \
  "ds_read_b64_tr_b16 v[116:117], v54 offset:30720\n\t" \
  "v_mfma_f32_16x16x32_bf16 v[172:175], v[140:143], v[132:135], v[172:175]\n\t" \
  "ds_read_b64_tr_b16 v[118:119], v54 offset:31232\n\t" \
  "v_mfma_f32_16x16x32_bf16 v[180:183], v[140:143], v[108:111], v[180:183]\n\t" \
  "ds_read_b128 v[88:91], v52 offset:0\n\t" \
  "v_mfma_f32_16x16x32_bf16 v[180:183], v[76:79], v[148:151], v[180:183]\n\t" \
  "ds_read_b128 v[124:127], v53 offset:0\n\t" \
  "s_waitcnt lgkmcnt(7)\n\t" \
  "v_mfma_f32_16x16x32_bf16 v[216:219], v[92:95], v[68:71], v[216:219]\n\t" \
  "ds_read_b64_tr_b16 v[96:97], v55 offset:26624\n\t" \
  "v_mfma_f32_16x16x32_bf16 v[216:219], v[84:87], v[64:67], v[216:219]\n\t" \
  "ds_read_b64_tr_b16 v[98:99], v55 offset:27136\n\t" \
  "v_mfma_f32_16x16x32_bf16 v[224:227], v[100:103], v[64:67], v[224:227]\n\t" \
  "ds_read_b64_tr_b16 v[132:133], v55 offset:30720\n\t" \
  "v_mfma_f32_16x16x32_bf16 v[224:227], v[112:115], v[68:71], v[224:227]\n\t" \
  "ds_read_b64_tr_b16 v[134:135], v55 offset:31232\n\t" \
  "v_mfma_f32_16x16x32_bf16 v[232:235], v[128:131], v[68:71], v[232:235]\n\t" \
  "ds_read_b64_tr_b16 v[108:109], v54 offset:18432\n\t" \
  "v_mfma_f32_16x16x32_bf16 v[232:235], v[120:123], v[64:67], v[232:235]\n\t" \
  "ds_read_b64_tr_b16 v[110:111], v54 offset:18944\n\t" \
  "v_mfma_f32_16x16x32_bf16 v[240:243], v[136:139], v[64:67], v[240:243]\n\t" \
  "ds_read_b64_tr_b16 v[140:141], v54 offset:22528\n\t" \
  "v_mfma_f32_16x16x32_bf16 v[240:243], v[144:147], v[68:71], v[240:243]\n\t" \
  "ds_read_b64_tr_b16 v[142:143], v54 offset:23040\n\t" \
  "v_mfma_f32_16x16x32_bf16 v[220:223], v[92:95], v[104:107], v[220:223]\n\t" \
  "ds_read_b64_tr_b16 v[76:77], v55 offset:18432\n\t" \
  "s_waitcnt lgkmcnt(7)\n\t" \
  "v_mfma_f32_16x16x32_bf16 v[220:223], v[84:87], v[72:75], v[220:223]\n\t" \
  "ds_read_b64_tr_b16 v[78:79], v55 offset:18944\n\t" \
  "v_mfma_f32_16x16x32_bf16 v[228:231], v[100:103], v[72:75], v[228:231]\n\t" \
  "ds_read_b64_tr_b16 v[148:149], v55 offset:22528\n\t" \
  "v_mfma_f32_16x16x32_bf16 v[228:231], v[112:115], v[104:107], v[228:231]\n\t" \
  "ds_read_b64_tr_b16 v[150:151], v55 offset:23040\n\t" \
  "v_mfma_f32_16x16x32_bf16 v[236:239], v[128:131], v[104:107], v[236:239]\n\t" \
  "v_mfma_f32_16x16x32_bf16 v[236:239], v[120:123], v[72:75], v[236:239]\n\t" \
  "v_mfma_f32_16x16x32_bf16 v[244:247], v[136:139], v[72:75], v[244:247]\n\t" \
  "v_mfma_f32_16x16x32_bf16 v[244:247], v[144:147], v[104:107], v[244:247]\n\t" \
  "v_mfma_f32_16x16x32_bf16 v[184:187], v[80:83], v[92:95], v[184:187]\n\t" \
  "ds_read_b32 v10, v32 offset:104\n\t" \
  "v_mfma_f32_16x16x32_bf16 v[184:187], v[116:119], v[84:87], v[184:187]\n\t" \
  "v_lshlrev_b32 v11, 3, v4\n\t" \
  "v_mfma_f32_16x16x32_bf16 v[192:195], v[116:119], v[100:103], v[192:195]\n\t" \
  "v_mfma_f32_16x16x32_bf16 v[192:195], v[80:83], v[112:115], v[192:195]\n\t" \
  "v_mfma_f32_16x16x32_bf16 v[200:203], v[80:83], v[128:131], v[200:203]\n\t" \
  "s_waitcnt lgkmcnt(0)\n\t" \
  "s_cmp_lg_u32 %13, 0\n\t" \
  "s_cbranch_scc1 27f\n\t" \
  "s_mov_b32 exec_hi, 0\n\t" \
  "global_store_dword v11, v10, %25 sc1\n\t" \
  "s_mov_b32 exec_hi, -1\n\t" \
  "27:\n\t" \
  "v_mfma_f32_16x16x32_bf16 v[200:203], v[116:119], v[120:123], v[200:203]\n\t" \
  "v_mfma_f32_16x16x32_bf16 v[208:211], v[116:119], v[136:139], v[208:211]\n\t" \
  "v_mfma_f32_16x16x32_bf16 v[208:211], v[80:83], v[144:147], v[208:211]\n\t" \
  "v_mfma_f32_16x16x32_bf16 v[248:251], v[80:83], v[88:91], v[248:251]\n\t" \
  "v_mfma_f32_16x16x32_bf16 v[248:251], v[116:119], v[124:127], v[248:251]\n\t" \
  "v_mfma_f32_16x16x32_bf16 v[252:255], v[96:99], v[88:91], v[252:255]\n\t" \
  "v_mfma_f32_16x16x32_bf16 v[252:255], v[132:135], v[124:127], v[252:255]\n\t" \
  "v_mfma_f32_16x16x32_bf16 v[188:191], v[132:135], v[84:87], v[188:191]\n\t" \
  "v_mfma_f32_16x16x32_bf16 v[188:191], v[96:99], v[92:95], v[188:191]\n\t" \
  "s_barrier\n\t" \
  "v_mfma_f32_16x16x32_bf16 v[196:199], v[96:99], v[112:115], v[196:199]\n\t" \
  "ds_read_b64_tr_b16 v[64:65], v58 offset:34816\n\t" \
  "v_mfma_f32_16x16x32_bf16 v[196:199], v[132:135], v[100:103], v[196:199]\n\t" \
  "ds_read_b64_tr_b16 v[66:67], v58 offset:35328\n\t" \
  "v_mfma_f32_16x16x32_bf16 v[204:207], v[132:135], v[120:123], v[204:207]\n\t" \
  "ds_read_b128 v[68:71], v56 offset:2048\n\t" \
  "v_mfma_f32_16x16x32_bf16 v[204:207], v[96:99], v[128:131], v[204:207]\n\t" \
  "ds_read_b64_tr_b16 v[72:73], v58 offset:38912\n\t" \
  "v_mfma_f32_16x16x32_bf16 v[212:215], v[96:99], v[144:147], v[212:215]\n\t" \
  "ds_read_b64_tr_b16 v[74:75], v58 offset:39424\n\t" \
  "v_mfma_f32_16x16x32_bf16 v[212:215], v[132:135], v[136:139], v[212:215]\n\t" \
  "ds_read_b128 v[104:107], v57 offset:2048\n\t" \
  "v_mfma_f32_16x16x32_bf16 v[152:155], v[108:111], v[92:95], v[152:155]\n\t" \
  "ds_read_b128 v[80:83], v57 offset:4096\n\t" \
  "v_mfma_f32_16x16x32_bf16 v[152:155], v[140:143], v[84:87], v[152:155]\n\t" \
  "ds_read_b128 v[116:119], v56 offset:4096\n\t" \
  "v_mfma_f32_16x16x32_bf16 v[160:163], v[140:143], v[100:103], v[160:163]\n\t" \
  "ds_read_b128 v[88:91], v56 offset:6144\n\t" \
  "v_mfma_f32_16x16x32_bf16 v[160:163], v[108:111], v[112:115], v[160:163]\n\t" \
  "ds_read_b128 v[124:127], v57 offset:6144\n\t" \
  "v_mfma_f32_16x16x32_bf16 v[168:171], v[108:111], v[128:131], v[168:171]\n\t" \
  "ds_read_b128 v[96:99], v57 offset:8192\n\t" \
  "v_mfma_f32_16x16x32_bf16 v[168:171], v[140:143], v[120:123], v[168:171]\n\t" \
  "ds_read_b128 v[132:135], v56 offset:8192\n\t" \
  "v_mfma_f32_16x16x32_bf16 v[176:179], v[140:143], v[136:139], v[176:179]\n\t" \
  "ds_read_b64_tr_b16 v[140:141], v59 offset:34816\n\t" \
  "v_mfma_f32_16x16x32_bf16 v[176:179], v[108:111], v[144:147], v[176:179]\n\t" \
  "ds_read_b64_tr_b16 v[142:143], v59 offset:35328\n\t" \
  "v_mfma_f32_16x16x32_bf16 v[156:159], v[76:79], v[92:95], v[156:159]\n\t" \
  "ds_read_b64_tr_b16 v[108:109], v59 offset:38912\n\t" \
  "v_mfma_f32_16x16x32_bf16 v[156:159], v[148:151], v[84:87], v[156:159]\n\t" \
  "ds_read_b64_tr_b16 v[110:111], v59 offset:39424\n\t" \
  "v_mfma_f32_16x16x32_bf16 v[164:167], v[148:151], v[100:103], v[164:167]\n\t" \
  "ds_read_b64_tr_b16 v[92:93], v58 offset:26624\n\t" \
  "v_mfma_f32_16x16x32_bf16 v[164:167], v[76:79], v[112:115], v[164:167]\n\t" \
  "ds_read_b64_tr_b16 v[94:95], v58 offset:27136\n\t" \
  "v_mfma_f32_16x16x32_bf16 v[172:175], v[76:79], v[128:131], v[172:175]\n\t" \
  "ds_read_b64_tr_b16 v[84:85], v58 offset:30720\n\t" \
  "v_mfma_f32_16x16x32_bf16 v[172:175], v[148:151], v[120:123], v[172:175]\n\t" \
  "ds_read_b64_tr_b16 v[86:87], v58 offset:31232\n\t" \
  "v_mfma_f32_16x16x32_bf16 v[180:183], v[148:151], v[136:139], v[180:183]\n\t" \
  "ds_read_b128 v[100:103], v56 offset:0\n\t" \
  "v_mfma_f32_16x16x32_bf16 v[180:183], v[76:79], v[144:147], v[180:183]\n\t" \
  "ds_read_b128 v[112:115], v57 offset:0\n\t" \
  "s_waitcnt lgkmcnt(7)\n\t" \
  "v_mfma_f32_16x16x32_bf16 v[216:219], v[68:71], v[64:67], v[216:219]\n\t" \
  "ds_read_b64_tr_b16 v[128:129], v59 offset:26624\n\t" \
  "v_mfma_f32_16x16x32_bf16 v[216:219], v[104:107], v[72:75], v[216:219]\n\t" \
  "ds_read_b64_tr_b16 v[130:131], v59 offset:27136\n\t" \
  "v_mfma_f32_16x16x32_bf16 v[224:227], v[80:83], v[72:75], v[224:227]\n\t" \
  "ds_read_b64_tr_b16 v[120:121], v59 offset:30720\n\t" \
  "v_mfma_f32_16x16x32_bf16 v[224:227], v[116:119], v[64:67], v[224:227]\n\t" \
  "ds_read_b64_tr_b16 v[122:123], v59 offset:31232\n\t" \
  "v_mfma_f32_16x16x32_bf16 v[232:235], v[88:91], v[64:67], v[232:235]\n\t" \
  "ds_read_b64_tr_b16 v[136:137], v58 offset:18432\n\t" \
  "v_mfma_f32_16x16x32_bf16 v[232:235], v[124:127], v[72:75], v[232:235]\n\t" \
  "ds_read_b64_tr_b16 v[138:139], v58 offset:18944\n\t" \
  "v_mfma_f32_16x16x32_bf16 v[240:243], v[96:99], v[72:75], v[240:243]\n\t" \
  "ds_read_b64_tr_b16 v[148:149], v58 offset:22528\n\t" \
  "v_mfma_f32_16x16x32_bf16 v[240:243], v[132:135], v[64:67], v[240:243]\n\t" \
  "ds_read_b64_tr_b16 v[150:151], v58 offset:23040\n\t" \
  "v_mfma_f32_16x16x32_bf16 v[220:223], v[68:71], v[140:143], v[220:223]\n\t" \
  "ds_read_b64_tr_b16 v[76:77], v59 offset:18432\n\t" \
  "s_waitcnt lgkmcnt(7)\n\t" \
  "v_mfma_f32_16x16x32_bf16 v[220:223], v[104:107], v[108:111], v[220:223]\n\t" \
  "ds_read_b64_tr_b16 v[78:79], v59 offset:18944\n\t" \
  "v_mfma_f32_16x16x32_bf16 v[228:231], v[80:83], v[108:111], v[228:231]\n\t" \
  "ds_read_b64_tr_b16 v[144:145], v59 offset:22528\n\t" \
  "v_mfma_f32_16x16x32_bf16 v[228:231], v[116:119], v[140:143], v[228:231]\n\t" \
  "ds_read_b64_tr_b16 v[146:147], v59 offset:23040\n\t" \
  "v_mfma_f32_16x16x32_bf16 v[236:239], v[88:91], v[140:143], v[236:239]\n\t" \
  "v_mfma_f32_16x16x32_bf16 v[236:239], v[124:127], v[108:111], v[236:239]\n\t" \
  "v_mfma_f32_16x16x32_bf16 v[244:247], v[96:99], v[108:111], v[244:247]\n\t" \
  "v_mfma_f32_16x16x32_bf16 v[244:247], v[132:135], v[140:143], v[244:247]\n\t" \
  "v_mfma_f32_16x16x32_bf16 v[184:187], v[92:95], v[68:71], v[184:187]\n\t" \
  "v_mfma_f32_16x16x32_bf16 v[184:187], v[84:87], v[104:107], v[184:187]\n\t" \
  "v_mfma_f32_16x16x32_bf16 v[192:195], v[84:87], v[80:83], v[192:195]\n\t" \
  "v_mfma_f32_16x16x32_bf16 v[192:195], v[92:95], v[116:119], v[192:195]\n\t" \
  "v_mfma_f32_16x16x32_bf16 v[200:203], v[92:95], v[88:91], v[200:203]\n\t" \
  "v_mfma_f32_16x16x32_bf16 v[200:203], v[84:87], v[124:127], v[200:203]\n\t" \
  "v_mfma_f32_16x16x32_bf16 v[208:211], v[84:87], v[96:99], v[208:211]\n\t" \
  "v_mfma_f32_16x16x32_bf16 v[208:211], v[92:95], v[132:135], v[208:211]\n\t" \
  "v_mfma_f32_16x16x32_bf16 v[248:251], v[92:95], v[100:103], v[248:251]\n\t" \
  "v_mfma_f32_16x16x32_bf16 v[248:251], v[84:87], v[112:115], v[248:251]\n\t" \
  "v_mfma_f32_16x16x32_bf16 v[252:255], v[128:131], v[100:103], v[252:255]\n\t" \
  "s_waitcnt lgkmcnt(0)\n\t" \
  "v_mfma_f32_16x16x32_bf16 v[252:255], v[120:123], v[112:115], v[252:255]\n\t" \
  "v_mfma_f32_16x16x32_bf16 v[188:191], v[120:123], v[104:107], v[188:191]\n\t" \
  "v_mfma_f32_16x16x32_bf16 v[188:191], v[128:131], v[68:71], v[188:191]\n\t" \
  "s_barrier\n\t" \
  "v_mfma_f32_16x16x32_bf16 v[196:199], v[128:131], v[116:119], v[196:199]\n\t" \
  "ds_read_b64_tr_b16 v[72:73], v62 offset:34816\n\t" \
  "v_mfma_f32_16x16x32_bf16 v[196:199], v[120:123], v[80:83], v[196:199]\n\t" \
  "ds_read_b64_tr_b16 v[74:75], v62 offset:35328\n\t" \
  "v_mfma_f32_16x16x32_bf16 v[204:207], v[120:123], v[124:127], v[204:207]\n\t" \
  "ds_read_b128 v[64:67], v60 offset:2048\n\t" \
  "v_mfma_f32_16x16x32_bf16 v[204:207], v[128:131], v[88:91], v[204:207]\n\t" \
  "ds_read_b64_tr_b16 v[108:109], v62 offset:38912\n\t" \
  "v_mfma_f32_16x16x32_bf16 v[212:215], v[128:131], v[132:135], v[212:215]\n\t" \
  "ds_read_b64_tr_b16 v[110:111], v62 offset:39424\n\t" \
  "v_mfma_f32_16x16x32_bf16 v[212:215], v[120:123], v[96:99], v[212:215]\n\t" \
  "ds_read_b128 v[140:143], v61 offset:2048\n\t" \
  "v_mfma_f32_16x16x32_bf16 v[152:155], v[136:139], v[68:71], v[152:155]\n\t" \
  "ds_read_b128 v[92:95], v61 offset:4096\n\t" \
  "v_mfma_f32_16x16x32_bf16 v[152:155], v[148:151], v[104:107], v[152:155]\n\t" \
  "ds_read_b128 v[84:87], v60 offset:4096\n\t" \
  "v_mfma_f32_16x16x32_bf16 v[160:163], v[148:151], v[80:83], v[160:163]\n\t" \
  "ds_read_b128 v[100:103], v60 offset:6144\n\t" \
  "v_mfma_f32_16x16x32_bf16 v[160:163], v[136:139], v[116:119], v[160:163]\n\t" \
  "ds_read_b128 v[112:115], v61 offset:6144\n\t" \
  "v_mfma_f32_16x16x32_bf16 v[168:171], v[136:139], v[88:91], v[168:171]\n\t" \
  "ds_read_b128 v[128:131], v61 offset:8192\n\t" \
  "v_mfma_f32_16x16x32_bf16 v[168:171], v[148:151], v[124:127], v[168:171]\n\t" \
  "ds_read_b128 v[120:123], v60 offset:8192\n\t" \
  "v_mfma_f32_16x16x32_bf16 v[176:179], v[148:151], v[96:99], v[176:179]\n\t" \
  "ds_read_b64_tr_b16 v[148:149], v63 offset:34816\n\t" \
  "v_mfma_f32_16x16x32_bf16 v[176:179], v[136:139], v[132:135], v[176:179]\n\t" \
  "ds_read_b64_tr_b16 v[150:151], v63 offset:35328\n\t" \
  "v_mfma_f32_16x16x32_bf16 v[156:159], v[76:79], v[68:71], v[156:159]\n\t" \
  "ds_read_b64_tr_b16 v[136:137], v63 offset:38912\n\t" \
  "v_mfma_f32_16x16x32_bf16 v[156:159], v[144:147], v[104:107], v[156:159]\n\t" \
  "ds_read_b64_tr_b16 v[138:139], v63 offset:39424\n\t" \
  "v_mfma_f32_16x16x32_bf16 v[164:167], v[144:147], v[80:83], v[164:167]\n\t" \
  "ds_read_b64_tr_b16 v[68:69], v62 offset:26624\n\t" \
  "v_mfma_f32_16x16x32_bf16 v[164:167], v[76:79], v[116:119], v[164:167]\n\t" \
  "ds_read_b64_tr_b16 v[70:71], v62 offset:27136\n\t" \
  "v_mfma_f32_16x16x32_bf16 v[172:175], v[76:79], v[88:91], v[172:175]\n\t" \
  "ds_read_b64_tr_b16 v[104:105], v62 offset:30720\n\t" \
  "v_mfma_f32_16x16x32_bf16 v[172:175], v[144:147], v[124:127], v[172:175]\n\t" \
  "ds_read_b64_tr_b16 v[106:107], v62 offset:31232\n\t" \
  "v_mfma_f32_16x16x32_bf16 v[180:183], v[144:147], v[96:99], v[180:183]\n\t" \
  "ds_read_b128 v[80:83], v60 offset:0\n\t" \
  "v_mfma_f32_16x16x32_bf16 v[180:183], v[76:79], v[132:135], v[180:183]\n\t" \
  "ds_read_b128 v[116:119], v61 offset:0\n\t" \
  "s_waitcnt lgkmcnt(7)\n\t" \
  "v_mfma_f32_16x16x32_bf16 v[216:219], v[64:67], v[72:75], v[216:219]\n\t" \
  "ds_read_b64_tr_b16 v[88:89], v63 offset:26624\n\t" \
  "v_mfma_f32_16x16x32_bf16 v[216:219], v[140:143], v[108:111], v[216:219]\n\t" \
  "505:\n\t" \
  "s_cbranch_execz 509f\n\t" \
  "ds_read_b64_tr_b16 v[90:91], v63 offset:27136\n\t" \
  "v_mfma_f32_16x16x32_bf16 v[224:227], v[92:95], v[108:111], v[224:227]\n\t" \
  "ds_read_b64_tr_b16 v[124:125], v63 offset:30720\n\t" \
  "v_mfma_f32_16x16x32_bf16 v[224:227], v[84:87], v[72:75], v[224:227]\n\t" \
  "ds_read_b64_tr_b16 v[126:127], v63 offset:31232\n\t" \
  "v_mfma_f32_16x16x32_bf16 v[232:235], v[100:103], v[72:75], v[232:235]\n\t" \
  "ds_read_b64_tr_b16 v[96:97], v62 offset:18432\n\t" \
  "v_mfma_f32_16x16x32_bf16 v[232:235], v[112:115], v[108:111], v[232:235]\n\t" \
  "ds_read_b64_tr_b16 v[98:99], v62 offset:18944\n\t" \
  "v_mfma_f32_16x16x32_bf16 v[240:243], v[128:131], v[108:111], v[240:243]\n\t" \
  "ds_read_b64_tr_b16 v[144:145], v62 offset:22528\n\t" \
  "v_mfma_f32_16x16x32_bf16 v[240:243], v[120:123], v[72:75], v[240:243]\n\t" \
  "ds_read_b64_tr_b16 v[146:147], v62 offset:23040\n\t" \
  "v_mfma_f32_16x16x32_bf16 v[220:223], v[64:67], v[148:151], v[220:223]\n\t" \
  "ds_read_b64_tr_b16 v[76:77], v63 offset:18432\n\t" \
  "s_waitcnt lgkmcnt(7)\n\t" \
  "v_mfma_f32_16x16x32_bf16 v[220:223], v[140:143], v[136:139], v[220:223]\n\t" \
  "ds_read_b64_tr_b16 v[78:79], v63 offset:18944\n\t" \
  "v_mfma_f32_16x16x32_bf16 v[228:231], v[92:95], v[136:139], v[228:231]\n\t" \
  "ds_read_b64_tr_b16 v[132:133], v63 offset:22528\n\t" \
  "v_mfma_f32_16x16x32_bf16 v[228:231], v[84:87], v[148:151], v[228:231]\n\t" \
  "ds_read_b64_tr_b16 v[134:135], v63 offset:23040\n\t" \
  "v_mfma_f32_16x16x32_bf16 v[236:239], v[100:103], v[148:151], v[236:239]\n\t" \
  "v_mfma_f32_16x16x32_bf16 v[236:239], v[112:115], v[136:139], v[236:239]\n\t" \
  "v_mfma_f32_16x16x32_bf16 v[244:247], v[128:131], v[136:139], v[244:247]\n\t" \
  "v_mfma_f32_16x16x32_bf16 v[244:247], v[120:123], v[148:151], v[244:247]\n\t" \
  "v_mfma_f32_16x16x32_bf16 v[184:187], v[68:71], v[64:67], v[184:187]\n\t" \
  "v_mfma_f32_16x16x32_bf16 v[184:187], v[104:107], v[140:143], v[184:187]\n\t" \
  "v_mfma_f32_16x16x32_bf16 v[192:195], v[104:107], v[92:95], v[192:195]\n\t" \
  "v_mfma_f32_16x16x32_bf16 v[192:195], v[68:71], v[84:87], v[192:195]\n\t" \
  "v_mfma_f32_16x16x32_bf16 v[200:203], v[68:71], v[100:103], v[200:203]\n\t" \
  "v_mfma_f32_16x16x32_bf16 v[200:203], v[104:107], v[112:115], v[200:203]\n\t" \
  "v_mfma_f32_16x16x32_bf16 v[208:211], v[104:107], v[128:131], v[208:211]\n\t" \
  "v_mfma_f32_16x16x32_bf16 v[208:211], v[68:71], v[120:123], v[208:211]\n\t" \
  "v_mfma_f32_16x16x32_bf16 v[248:251], v[68:71], v[80:83], v[248:251]\n\t" \
  "v_mfma_f32_16x16x32_bf16 v[248:251], v[104:107], v[116:119], v[248:251]\n\t" \
  "v_mfma_f32_16x16x32_bf16 v[252:255], v[88:91], v[80:83], v[252:255]\n\t" \
  "s_waitcnt lgkmcnt(0)\n\t" \
  "v_mfma_f32_16x16x32_bf16 v[252:255], v[124:127], v[116:119], v[252:255]\n\t" \
  "v_mfma_f32_16x16x32_bf16 v[188:191], v[124:127], v[140:143], v[188:191]\n\t" \
  "v_mfma_f32_16x16x32_bf16 v[188:191], v[88:91], v[64:67], v[188:191]\n\t" \
  "s_barrier\n\t" \
  "v_mfma_f32_16x16x32_bf16 v[196:199], v[88:91], v[84:87], v[196:199]\n\t" \
  "ds_read_b64_tr_b16 v[108:109], v54 offset:34816\n\t" \
  "v_mfma_f32_16x16x32_bf16 v[196:199], v[124:127], v[92:95], v[196:199]\n\t" \
  "ds_read_b64_tr_b16 v[110:111], v54 offset:35328\n\t" \
  "v_mfma_f32_16x16x32_bf16 v[204:207], v[124:127], v[112:115], v[204:207]\n\t" \
  "ds_read_b128 v[72:75], v52 offset:2048\n\t" \
  "v_mfma_f32_16x16x32_bf16 v[204:207], v[88:91], v[100:103], v[204:207]\n\t" \
  "ds_read_b64_tr_b16 v[136:137], v54 offset:38912\n\t" \
  "v_mfma_f32_16x16x32_bf16 v[212:215], v[88:91], v[120:123], v[212:215]\n\t" \
  "ds_read_b64_tr_b16 v[138:139], v54 offset:39424\n\t" \
  "v_mfma_f32_16x16x32_bf16 v[212:215], v[124:127], v[128:131], v[212:215]\n\t" \
  "ds_read_b128 v[148:151], v53 offset:2048\n\t" \
  "v_mfma_f32_16x16x32_bf16 v[152:155], v[96:99], v[64:67], v[152:155]\n\t" \
  "ds_read_b128 v[68:71], v53 offset:4096\n\t" \
  "v_mfma_f32_16x16x32_bf16 v[152:155], v[144:147], v[140:143], v[152:155]\n\t" \
  "ds_read_b128 v[104:107], v52 offset:4096\n\t" \
  "v_mfma_f32_16x16x32_bf16 v[160:163], v[144:147], v[92:95], v[160:163]\n\t" \
  "ds_read_b128 v[80:83], v52 offset:6144\n\t" \
  "v_mfma_f32_16x16x32_bf16 v[160:163], v[96:99], v[84:87], v[160:163]\n\t" \
  "ds_read_b128 v[116:119], v53 offset:6144\n\t" \
  "v_mfma_f32_16x16x32_bf16 v[168:171], v[96:99], v[100:103], v[168:171]\n\t" \
  "ds_read_b128 v[88:91], v53 offset:8192\n\t" \
  "v_mfma_f32_16x16x32_bf16 v[168:171], v[144:147], v[112:115], v[168:171]\n\t" \
  "ds_read_b128 v[124:127], v52 offset:8192\n\t" \
  "v_mfma_f32_16x16x32_bf16 v[176:179], v[144:147], v[128:131], v[176:179]\n\t" \
  "ds_read_b64_tr_b16 v[144:145], v55 offset:34816\n\t" \
  "v_mfma_f32_16x16x32_bf16 v[176:179], v[96:99], v[120:123], v[176:179]\n\t" \
  "ds_read_b64_tr_b16 v[146:147], v55 offset:35328\n\t" \
  "v_mfma_f32_16x16x32_bf16 v[156:159], v[76:79], v[64:67], v[156:159]\n\t" \
  "ds_read_b64_tr_b16 v[96:97], v55 offset:38912\n\t" \
  "v_mfma_f32_16x16x32_bf16 v[156:159], v[132:135], v[140:143], v[156:159]\n\t" \
  "ds_read_b64_tr_b16 v[98:99], v55 offset:39424\n\t" \
  "v_mfma_f32_16x16x32_bf16 v[164:167], v[132:135], v[92:95], v[164:167]\n\t" \
  "ds_read_b64_tr_b16 v[64:65], v54 offset:26624\n\t" \
  "v_mfma_f32_16x16x32_bf16 v[164:167], v[76:79], v[84:87], v[164:167]\n\t" \
  "ds_read_b64_tr_b16 v[66:67], v54 offset:27136\n\t" \
  "v_mfma_f32_16x16x32_bf16 v[172:175], v[76:79], v[100:103], v[172:175]\n\t" \
  "ds_read_b64_tr_b16 v[140:141], v54 offset:30720\n\t" \
  "v_mfma_f32_16x16x32_bf16 v[172:175], v[132:135], v[112:115], v[172:175]\n\t" \
  "ds_read_b64_tr_b16 v[142:143], v54 offset:31232\n\t" \
  "v_mfma_f32_16x16x32_bf16 v[180:183], v[132:135], v[128:131], v[180:183]\n\t" \
  "ds_read_b128 v[92:95], v52 offset:0\n\t" \
  "v_mfma_f32_16x16x32_bf16 v[180:183], v[76:79], v[120:123], v[180:183]\n\t" \
  "ds_read_b128 v[84:87], v53 offset:0\n\t" \
  "s_waitcnt lgkmcnt(7)\n\t" \
  "v_mfma_f32_16x16x32_bf16 v[216:219], v[72:75], v[108:111], v[216:219]\n\t" \
  "ds_read_b64_tr_b16 v[100:101], v55 offset:26624\n\t" \
  "v_mfma_f32_16x16x32_bf16 v[216:219], v[148:151], v[136:139], v[216:219]\n\t" \
  "ds_read_b64_tr_b16 v[102:103], v55 offset:27136\n\t" \
  "v_mfma_f32_16x16x32_bf16 v[224:227], v[68:71], v[136:139], v[224:227]\n\t" \
  "ds_read_b64_tr_b16 v[112:113], v55 offset:30720\n\t" \
  "v_mfma_f32_16x16x32_bf16 v[224:227], v[104:107], v[108:111], v[224:227]\n\t" \
  "ds_read_b64_tr_b16 v[114:115], v55 offset:31232\n\t" \
  "v_mfma_f32_16x16x32_bf16 v[232:235], v[80:83], v[108:111], v[232:235]\n\t" \
  "ds_read_b64_tr_b16 v[128:129], v54 offset:18432\n\t" \
  "v_mfma_f32_16x16x32_bf16 v[232:235], v[116:119], v[136:139], v[232:235]\n\t" \
  "ds_read_b64_tr_b16 v[130:131], v54 offset:18944\n\t" \
  "v_mfma_f32_16x16x32_bf16 v[240:243], v[88:91], v[136:139], v[240:243]\n\t" \
  "ds_read_b64_tr_b16 v[132:133], v54 offset:22528\n\t" \
  "v_mfma_f32_16x16x32_bf16 v[240:243], v[124:127], v[108:111], v[240:243]\n\t" \
  "ds_read_b64_tr_b16 v[134:135], v54 offset:23040\n\t" \
  "v_mfma_f32_16x16x32_bf16 v[220:223], v[72:75], v[144:147], v[220:223]\n\t" \
  "ds_read_b64_tr_b16 v[76:77], v55 offset:18432\n\t" \
  "s_waitcnt lgkmcnt(7)\n\t" \
  "v_mfma_f32_16x16x32_bf16 v[220:223], v[148:151], v[96:99], v[220:223]\n\t" \
  "ds_read_b64_tr_b16 v[78:79], v55 offset:18944\n\t" \
  "v_mfma_f32_16x16x32_bf16 v[228:231], v[68:71], v[96:99], v[228:231]\n\t" \
  "ds_read_b64_tr_b16 v[120:121], v55 offset:22528\n\t" \
  "v_mfma_f32_16x16x32_bf16 v[228:231], v[104:107], v[144:147], v[228:231]\n\t" \
  "ds_read_b64_tr_b16 v[122:123], v55 offset:23040\n\t" \
  "v_mfma_f32_16x16x32_bf16 v[236:239], v[80:83], v[144:147], v[236:239]\n\t" \
  "v_mfma_f32_16x16x32_bf16 v[236:239], v[116:119], v[96:99], v[236:239]\n\t" \
  "v_mfma_f32_16x16x32_bf16 v[244:247], v[88:91], v[96:99], v[244:247]\n\t" \
  "v_mfma_f32_16x16x32_bf16 v[244:247], v[124:127], v[144:147], v[244:247]\n\t" \
  "v_mfma_f32_16x16x32_bf16 v[184:187], v[64:67], v[72:75], v[184:187]\n\t" \
  "v_mfma_f32_16x16x32_bf16 v[184:187], v[140:143], v[148:151], v[184:187]\n\t" \
  "v_mfma_f32_16x16x32_bf16 v[192:195], v[140:143], v[68:71], v[192:195]\n\t" \
  "v_mfma_f32_16x16x32_bf16 v[192:195], v[64:67], v[104:107], v[192:195]\n\t" \
  "v_mfma_f32_16x16x32_bf16 v[200:203], v[64:67], v[80:83], v[200:203]\n\t" \
  "v_mfma_f32_16x16x32_bf16 v[200:203], v[140:143], v[116:119], v[200:203]\n\t" \
  "v_mfma_f32_16x16x32_bf16 v[208:211], v[140:143], v[88:91], v[208:211]\n\t" \
  "v_mfma_f32_16x16x32_bf16 v[208:211], v[64:67], v[124:127], v[208:211]\n\t" \
  "v_mfma_f32_16x16x32_bf16 v[248:251], v[64:67], v[92:95], v[248:251]\n\t" \
  "v_mfma_f32_16x16x32_bf16 v[248:251], v[140:143], v[84:87], v[248:251]\n\t" \
  "v_mfma_f32_16x16x32_bf16 v[252:255], v[100:103], v[92:95], v[252:255]\n\t" \
  "s_waitcnt lgkmcnt(0)\n\t" \
  "v_mfma_f32_16x16x32_bf16 v[252:255], v[112:115], v[84:87], v[252:255]\n\t" \
  "v_mfma_f32_16x16x32_bf16 v[188:191], v[112:115], v[148:151], v[188:191]\n\t" \
  "v_mfma_f32_16x16x32_bf16 v[188:191], v[100:103], v[72:75], v[188:191]\n\t" \
  "s_barrier\n\t" \
  "v_mfma_f32_16x16x32_bf16 v[196:199], v[100:103], v[104:107], v[196:199]\n\t" \
  "ds_read_b64_tr_b16 v[96:97], v58 offset:34816\n\t" \
  "v_mfma_f32_16x16x32_bf16 v[196:199], v[112:115], v[68:71], v[196:199]\n\t" \
  "ds_read_b64_tr_b16 v[98:99], v58 offset:35328\n\t" \
  "v_mfma_f32_16x16x32_bf16 v[204:207], v[112:115], v[116:119], v[204:207]\n\t" \
  "ds_read_b128 v[136:139], v56 offset:2048\n\t" \
  "v_mfma_f32_16x16x32_bf16 v[204:207], v[100:103], v[80:83], v[204:207]\n\t" \
  "ds_read_b64_tr_b16 v[64:65], v58 offset:38912\n\t" \
  "v_mfma_f32_16x16x32_bf16 v[212:215], v[100:103], v[124:127], v[212:215]\n\t" \
  "ds_read_b64_tr_b16 v[66:67], v58 offset:39424\n\t" \
  "v_mfma_f32_16x16x32_bf16 v[212:215], v[112:115], v[88:91], v[212:215]\n\t" \
  "ds_read_b128 v[108:111], v57 offset:2048\n\t" \
  "v_mfma_f32_16x16x32_bf16 v[152:155], v[128:131], v[72:75], v[152:155]\n\t" \
  "ds_read_b128 v[144:147], v57 offset:4096\n\t" \
  "v_mfma_f32_16x16x32_bf16 v[152:155], v[132:135], v[148:151], v[152:155]\n\t" \
  "ds_read_b128 v[140:143], v56 offset:4096\n\t" \
  "v_mfma_f32_16x16x32_bf16 v[160:163], v[132:135], v[68:71], v[160:163]\n\t" \
  "ds_read_b128 v[112:115], v56 offset:6144\n\t" \
  "v_mfma_f32_16x16x32_bf16 v[160:163], v[128:131], v[104:107], v[160:163]\n\t" \
  "ds_read_b64_tr_b16 v[92:93], v59 offset:34816\n\t" \
  "v_mfma_f32_16x16x32_bf16 v[168:171], v[128:131], v[80:83], v[168:171]\n\t" \
  "ds_read_b64_tr_b16 v[94:95], v59 offset:35328\n\t" \
  "v_mfma_f32_16x16x32_bf16 v[168:171], v[132:135], v[116:119], v[168:171]\n\t" \
  "ds_read_b64_tr_b16 v[84:85], v59 offset:38912\n\t" \
  "v_mfma_f32_16x16x32_bf16 v[176:179], v[132:135], v[88:91], v[176:179]\n\t" \
  "ds_read_b128 v[132:135], v57 offset:6144\n\t" \
  "v_mfma_f32_16x16x32_bf16 v[176:179], v[128:131], v[124:127], v[176:179]\n\t" \
  "ds_read_b128 v[128:131], v57 offset:8192\n\t" \
  "v_mfma_f32_16x16x32_bf16 v[156:159], v[76:79], v[72:75], v[156:159]\n\t" \
  "ds_read_b64_tr_b16 v[86:87], v59 offset:39424\n\t" \
  "v_mfma_f32_16x16x32_bf16 v[156:159], v[120:123], v[148:151], v[156:159]\n\t" \
  "ds_read_b128 v[148:151], v56 offset:8192\n\t" \
  "v_mfma_f32_16x16x32_bf16 v[164:167], v[120:123], v[68:71], v[164:167]\n\t" \
  "ds_read_b64_tr_b16 v[100:101], v58 offset:26624\n\t" \
  "v_mfma_f32_16x16x32_bf16 v[164:167], v[76:79], v[104:107], v[164:167]\n\t" \
  "ds_read_b64_tr_b16 v[102:103], v58 offset:27136\n\t" \
  "v_mfma_f32_16x16x32_bf16 v[172:175], v[76:79], v[80:83], v[172:175]\n\t" \
  "ds_read_b64_tr_b16 v[72:73], v58 offset:30720\n\t" \
  "v_mfma_f32_16x16x32_bf16 v[172:175], v[120:123], v[116:119], v[172:175]\n\t" \
  "ds_read_b64_tr_b16 v[74:75], v58 offset:31232\n\t" \
  "v_mfma_f32_16x16x32_bf16 v[180:183], v[120:123], v[88:91], v[180:183]\n\t" \
  "ds_read_b128 v[68:71], v56 offset:0\n\t" \
  "v_mfma_f32_16x16x32_bf16 v[180:183], v[76:79], v[124:127], v[180:183]\n\t" \
  "ds_read_b128 v[104:107], v57 offset:0\n\t" \
  "s_waitcnt lgkmcnt(7)\n\t" \
  "v_mfma_f32_16x16x32_bf16 v[216:219], v[136:139], v[96:99], v[216:219]\n\t" \
  "ds_read_b64_tr_b16 v[80:81], v59 offset:26624\n\t" \
  "v_mfma_f32_16x16x32_bf16 v[216:219], v[108:111], v[64:67], v[216:219]\n\t" \
  "ds_read_b64_tr_b16 v[82:83], v59 offset:27136\n\t" \
  "v_mfma_f32_16x16x32_bf16 v[224:227], v[144:147], v[64:67], v[224:227]\n\t" \
  "ds_read_b64_tr_b16 v[88:89], v59 offset:30720\n\t" \
  "v_mfma_f32_16x16x32_bf16 v[224:227], v[140:143], v[96:99], v[224:227]\n\t" \
  "ds_read_b64_tr_b16 v[90:91], v59 offset:31232\n\t" \
  "v_mfma_f32_16x16x32_bf16 v[232:235], v[112:115], v[96:99], v[232:235]\n\t" \
  "ds_read_b64_tr_b16 v[116:117], v58 offset:18432\n\t" \
  "v_mfma_f32_16x16x32_bf16 v[232:235], v[132:135], v[64:67], v[232:235]\n\t" \
  "ds_read_b64_tr_b16 v[118:119], v58 offset:18944\n\t" \
  "v_mfma_f32_16x16x32_bf16 v[240:243], v[128:131], v[64:67], v[240:243]\n\t" \
  "ds_read_b64_tr_b16 v[120:121], v58 offset:22528\n\t" \
  "s_waitcnt lgkmcnt(7)\n\t" \
  "v_mfma_f32_16x16x32_bf16 v[240:243], v[148:151], v[96:99], v[240:243]\n\t" \
  "ds_read_b64_tr_b16 v[122:123], v58 offset:23040\n\t" \
  "v_mfma_f32_16x16x32_bf16 v[220:223], v[136:139], v[92:95], v[220:223]\n\t" \
  "ds_read_b64_tr_b16 v[124:125], v59 offset:18432\n\t" \
  "v_mfma_f32_16x16x32_bf16 v[220:223], v[108:111], v[84:87], v[220:223]\n\t" \
  "ds_read_b64_tr_b16 v[126:127], v59 offset:18944\n\t" \
  "v_mfma_f32_16x16x32_bf16 v[228:231], v[144:147], v[84:87], v[228:231]\n\t" \
  "ds_read_b64_tr_b16 v[76:77], v59 offset:22528\n\t" \
  "v_mfma_f32_16x16x32_bf16 v[228:231], v[140:143], v[92:95], v[228:231]\n\t" \
  "ds_read_b64_tr_b16 v[78:79], v59 offset:23040\n\t" \
  "v_mfma_f32_16x16x32_bf16 v[236:239], v[112:115], v[92:95], v[236:239]\n\t" \
  "v_mfma_f32_16x16x32_bf16 v[236:239], v[132:135], v[84:87], v[236:239]\n\t" \
  "v_mfma_f32_16x16x32_bf16 v[244:247], v[128:131], v[84:87], v[244:247]\n\t" \
  "v_mfma_f32_16x16x32_bf16 v[244:247], v[148:151], v[92:95], v[244:247]\n\t" \
  "v_mfma_f32_16x16x32_bf16 v[184:187], v[100:103], v[136:139], v[184:187]\n\t" \
  "v_mfma_f32_16x16x32_bf16 v[184:187], v[72:75], v[108:111], v[184:187]\n\t" \
  "v_mfma_f32_16x16x32_bf16 v[192:195], v[72:75], v[144:147], v[192:195]\n\t" \
  "v_and_b32 v14, -16, v32\n\t" \
  "v_mfma_f32_16x16x32_bf16 v[192:195], v[100:103], v[140:143], v[192:195]\n\t" \
  "v_mfma_f32_16x16x32_bf16 v[200:203], v[100:103], v[112:115], v[200:203]\n\t" \
  "ds_read_b128 v[10:13], v14 offset:128\n\t" \
  "v_mfma_f32_16x16x32_bf16 v[200:203], v[72:75], v[132:135], v[200:203]\n\t" \
  "v_mfma_f32_16x16x32_bf16 v[208:211], v[72:75], v[128:131], v[208:211]\n\t" \
  "v_mfma_f32_16x16x32_bf16 v[208:211], v[100:103], v[148:151], v[208:211]\n\t" \
  "v_mfma_f32_16x16x32_bf16 v[248:251], v[100:103], v[68:71], v[248:251]\n\t" \
  "v_mfma_f32_16x16x32_bf16 v[248:251], v[72:75], v[104:107], v[248:251]\n\t" \
  "s_waitcnt lgkmcnt(1)\n\t" \
  "v_mfma_f32_16x16x32_bf16 v[252:255], v[80:83], v[68:71], v[252:255]\n\t" \
  "s_waitcnt lgkmcnt(0)\n\t" \
  "v_and_b32 v10, v10, v11\n\t" \
  "v_and_b32 v12, v12, v13\n\t" \
  "v_mfma_f32_16x16x32_bf16 v[252:255], v[88:91], v[104:107], v[252:255]\n\t" \
  "v_and_b32 v10, v10, v12\n\t" \
  "v_mfma_f32_16x16x32_bf16 v[188:191], v[88:91], v[108:111], v[188:191]\n\t" \
  "v_readfirstlane_b32 vcc_lo, v10\n\t" \
  "v_mfma_f32_16x16x32_bf16 v[188:191], v[80:83], v[136:139], v[188:191]\n\t" \
  "ds_write_b32 v32, v0 offset:144\n\t" \
  "s_cmp_eq_u32 vcc_lo, 0\n\t" \
  "s_cbranch_scc1 15f\n\t" \
  "14:\n\t" \
  "v_mfma_f32_16x16x32_bf16 v[196:199], v[80:83], v[140:143], v[196:199]\n\t" \
  "ds_read_b64_tr_b16 v[64:65], v62 offset:26624\n\t" \
  "v_mfma_f32_16x16x32_bf16 v[196:199], v[88:91], v[144:147], v[196:199]\n\t" \
  "ds_read_b64_tr_b16 v[66:67], v62 offset:27136\n\t" \
  "v_mfma_f32_16x16x32_bf16 v[204:207], v[88:91], v[132:135], v[204:207]\n\t" \
  "ds_read_b128 v[96:99], v60 offset:0\n\t" \
  "v_mfma_f32_16x16x32_bf16 v[204:207], v[80:83], v[112:115], v[204:207]\n\t" \
  "ds_read_b128 v[84:87], v60 offset:2048\n\t" \
  "v_mfma_f32_16x16x32_bf16 v[212:215], v[80:83], v[148:151], v[212:215]\n\t" \
  "ds_read_b128 v[92:95], v60 offset:4096\n\t" \
  "v_mfma_f32_16x16x32_bf16 v[212:215], v[88:91], v[128:131], v[212:215]\n\t" \
  "ds_read_b128 v[100:103], v60 offset:6144\n\t" \
  "v_mfma_f32_16x16x32_bf16 v[152:155], v[116:119], v[136:139], v[152:155]\n\t" \
  "ds_read_b128 v[72:75], v60 offset:8192\n\t" \
  "v_mfma_f32_16x16x32_bf16 v[152:155], v[120:123], v[108:111], v[152:155]\n\t" \
  "ds_read_b64_tr_b16 v[68:69], v63 offset:26624\n\t" \
  "v_mfma_f32_16x16x32_bf16 v[160:163], v[120:123], v[144:147], v[160:163]\n\t" \
  "ds_read_b64_tr_b16 v[70:71], v63 offset:27136\n\t" \
  "v_mfma_f32_16x16x32_bf16 v[160:163], v[116:119], v[140:143], v[160:163]\n\t" \
  "ds_read_b64_tr_b16 v[104:105], v62 offset:34816\n\t" \
  "v_mfma_f32_16x16x32_bf16 v[168:171], v[116:119], v[112:115], v[168:171]\n\t" \
  "ds_read_b64_tr_b16 v[106:107], v62 offset:35328\n\t" \
  "v_mfma_f32_16x16x32_bf16 v[168:171], v[120:123], v[132:135], v[168:171]\n\t" \
  "ds_read_b64_tr_b16 v[80:81], v63 offset:34816\n\t" \
  "v_mfma_f32_16x16x32_bf16 v[176:179], v[120:123], v[128:131], v[176:179]\n\t" \
  "ds_read_b64_tr_b16 v[82:83], v63 offset:35328\n\t" \
  "v_mfma_f32_16x16x32_bf16 v[176:179], v[116:119], v[148:151], v[176:179]\n\t" \
  "ds_read_b64_tr_b16 v[88:89], v62 offset:18432\n\t" \
  "v_mfma_f32_16x16x32_bf16 v[156:159], v[124:127], v[136:139], v[156:159]\n\t" \
  "ds_read_b64_tr_b16 v[90:91], v62 offset:18944\n\t" \
  "v_mfma_f32_16x16x32_bf16 v[156:159], v[76:79], v[108:111], v[156:159]\n\t" \
  "ds_read_b64_tr_b16 v[120:121], v62 offset:30720\n\t" \
  "v_mfma_f32_16x16x32_bf16 v[164:167], v[76:79], v[144:147], v[164:167]\n\t" \
  "ds_read_b64_tr_b16 v[122:123], v62 offset:31232\n\t" \
  "v_mfma_f32_16x16x32_bf16 v[164:167], v[124:127], v[140:143], v[164:167]\n\t" \
  "ds_read_b128 v[116:119], v61 offset:0\n\t" \
  "v_mfma_f32_16x16x32_bf16 v[172:175], v[124:127], v[112:115], v[172:175]\n\t" \
  "ds_read_b128 v[136:139], v61 offset:2048\n\t" \
  "v_mfma_f32_16x16x32_bf16 v[172:175], v[76:79], v[132:135], v[172:175]\n\t" \
  "ds_read_b128 v[108:111], v61 offset:4096\n\t" \
  "v_mfma_f32_16x16x32_bf16 v[180:183], v[76:79], v[128:131], v[180:183]\n\t" \
  "ds_read_b64_tr_b16 v[76:77], v63 offset:18432\n\t" \
  "v_mfma_f32_16x16x32_bf16 v[180:183], v[124:127], v[148:151], v[180:183]\n\t" \
  "ds_read_b64_tr_b16 v[78:79], v63 offset:18944\n\t" \
  "s_waitcnt lgkmcnt(7)\n\t" \
  "v_mfma_f32_16x16x32_bf16 v[248:251], v[64:67], v[96:99], v[248:251]\n\t" \
  "ds_read_b128 v[144:147], v61 offset:6144\n\t" \
  "v_mfma_f32_16x16x32_bf16 v[184:187], v[64:67], v[84:87], v[184:187]\n\t" \
  "ds_read_b128 v[140:143], v61 offset:8192\n\t" \
  "v_mfma_f32_16x16x32_bf16 v[192:195], v[64:67], v[92:95], v[192:195]\n\t" \
  "ds_read_b64_tr_b16 v[112:113], v63 offset:30720\n\t" \
  "v_mfma_f32_16x16x32_bf16 v[200:203], v[64:67], v[100:103], v[200:203]\n\t" \
  "ds_read_b64_tr_b16 v[114:115], v63 offset:31232\n\t" \
  "v_mfma_f32_16x16x32_bf16 v[208:211], v[64:67], v[72:75], v[208:211]\n\t" \
  "ds_read_b64_tr_b16 v[132:133], v62 offset:22528\n\t" \
  "v_mfma_f32_16x16x32_bf16 v[252:255], v[68:71], v[96:99], v[252:255]\n\t" \
  "ds_read_b64_tr_b16 v[134:135], v62 offset:23040\n\t" \
  "v_mfma_f32_16x16x32_bf16 v[188:191], v[68:71], v[84:87], v[188:191]\n\t" \
  "ds_read_b64_tr_b16 v[128:129], v63 offset:22528\n\t" \
  "v_mfma_f32_16x16x32_bf16 v[196:199], v[68:71], v[92:95], v[196:199]\n\t" \
  "ds_read_b64_tr_b16 v[130:131], v63 offset:23040\n\t" \
  "v_mfma_f32_16x16x32_bf16 v[204:207], v[68:71], v[100:103], v[204:207]\n\t" \
  "ds_read_b64_tr_b16 v[124:125], v62 offset:38912\n\t" \
  "v_mfma_f32_16x16x32_bf16 v[212:215], v[68:71], v[72:75], v[212:215]\n\t" \
  "ds_read_b64_tr_b16 v[126:127], v62 offset:39424\n\t" \
  "v_mfma_f32_16x16x32_bf16 v[216:219], v[84:87], v[104:107], v[216:219]\n\t" \
  "ds_read_b64_tr_b16 v[148:149], v63 offset:38912\n\t" \
  "v_mfma_f32_16x16x32_bf16 v[224:227], v[92:95], v[104:107], v[224:227]\n\t" \
  "ds_read_b64_tr_b16 v[150:151], v63 offset:39424\n\t" \
  "v_mfma_f32_16x16x32_bf16 v[232:235], v[100:103], v[104:107], v[232:235]\n\t" \
  "v_mfma_f32_16x16x32_bf16 v[240:243], v[72:75], v[104:107], v[240:243]\n\t" \
  "v_mfma_f32_16x16x32_bf16 v[220:223], v[84:87], v[80:83], v[220:223]\n\t" \
  "v_mfma_f32_16x16x32_bf16 v[228:231], v[92:95], v[80:83], v[228:231]\n\t" \
  "v_mfma_f32_16x16x32_bf16 v[236:239], v[100:103], v[80:83], v[236:239]\n\t" \
  "v_mfma_f32_16x16x32_bf16 v[244:247], v[72:75], v[80:83], v[244:247]\n\t" \
  "v_mfma_f32_16x16x32_bf16 v[152:155], v[88:91], v[84:87], v[152:155]\n\t" \
  "v_mfma_f32_16x16x32_bf16 v[160:163], v[88:91], v[92:95], v[160:163]\n\t" \
  "v_mfma_f32_16x16x32_bf16 v[168:171], v[88:91], v[100:103], v[168:171]\n\t" \
  "v_mfma_f32_16x16x32_bf16 v[176:179], v[88:91], v[72:75], v[176:179]\n\t" \
  "s_waitcnt lgkmcnt(0)\n\t" \
  "v_mfma_f32_16x16x32_bf16 v[156:159], v[76:79], v[84:87], v[156:159]\n\t" \
  "v_mfma_f32_16x16x32_bf16 v[164:167], v[76:79], v[92:95], v[164:167]\n\t" \
  "ds_write_b32 v32, v0\n\t" \
  "v_mfma_f32_16x16x32_bf16 v[172:175], v[76:79], v[100:103], v[172:175]\n\t" \
  "v_mfma_f32_16x16x32_bf16 v[180:183], v[76:79], v[72:75], v[180:183]\n\t" \
  "v_mfma_f32_16x16x32_bf16 v[248:251], v[120:123], v[116:119], v[248:251]\n\t" \
  "v_and_b32 v56, -16, v32\n\t" \
  "v_mfma_f32_16x16x32_bf16 v[184:187], v[120:123], v[136:139], v[184:187]\n\t" \
  "v_mfma_f32_16x16x32_bf16 v[192:195], v[120:123], v[108:111], v[192:195]\n\t" \
  "v_mfma_f32_16x16x32_bf16 v[200:203], v[120:123], v[144:147], v[200:203]\n\t" \
  "v_mfma_f32_16x16x32_bf16 v[208:211], v[120:123], v[140:143], v[208:211]\n\t" \
  "v_mfma_f32_16x16x32_bf16 v[252:255], v[112:115], v[116:119], v[252:255]\n\t" \
  "v_mfma_f32_16x16x32_bf16 v[188:191], v[112:115], v[136:139], v[188:191]\n\t" \
  "v_mfma_f32_16x16x32_bf16 v[196:199], v[112:115], v[108:111], v[196:199]\n\t" \
  "v_cvt_pk_bf16_f32 v10, v248, v249\n\t" \
  "v_cvt_pk_bf16_f32 v11, v250, v251\n\t" \
  "v_mfma_f32_16x16x32_bf16 v[204:207], v[112:115], v[144:147], v[204:207]\n\t" \
  "v_cvt_pk_bf16_f32 v14, v184, v185\n\t" \
  "v_cvt_pk_bf16_f32 v15, v186, v187\n\t" \
  "v_mfma_f32_16x16x32_bf16 v[212:215], v[112:115], v[140:143], v[212:215]\n\t" \
  "v_cvt_pk_bf16_f32 v12, v252, v253\n\t" \
  "v_cvt_pk_bf16_f32 v13, v254, v255\n\t" \
  "v_mfma_f32_16x16x32_bf16 v[152:155], v[132:135], v[136:139], v[152:155]\n\t" \
  "ds_write_b128 v30, v[10:13]\n\t" \
  "ds_write_b32 v32, v0 offset:48\n\t" \
  "v_mfma_f32_16x16x32_bf16 v[160:163], v[132:135], v[108:111], v[160:163]\n\t" \
  "v_cvt_pk_bf16_f32 v16, v188, v189\n\t" \
  "v_cvt_pk_bf16_f32 v17, v190, v191\n\t" \
  "v_mfma_f32_16x16x32_bf16 v[168:171], v[132:135], v[144:147], v[168:171]\n\t" \
  "v_cvt_pk_bf16_f32 v18, v192, v193\n\t" \
  "v_cvt_pk_bf16_f32 v19, v194, v195\n\t" \
  "v_mfma_f32_16x16x32_bf16 v[176:179], v[132:135], v[140:143], v[176:179]\n\t" \
  "v_cvt_pk_bf16_f32 v20, v196, v197\n\t" \
  "v_cvt_pk_bf16_f32 v21, v198, v199\n\t" \
  "v_mfma_f32_16x16x32_bf16 v[156:159], v[128:131], v[136:139], v[156:159]\n\t" \
  "v_cvt_pk_bf16_f32 v22, v200, v201\n\t" \
  "v_cvt_pk_bf16_f32 v23, v202, v203\n\t" \
  "v_mfma_f32_16x16x32_bf16 v[164:167], v[128:131], v[108:111], v[164:167]\n\t" \
  "v_cvt_pk_bf16_f32 v24, v204, v205\n\t" \
  "v_cvt_pk_bf16_f32 v25, v206, v207\n\t" \
  "v_mfma_f32_16x16x32_bf16 v[172:175], v[128:131], v[144:147], v[172:175]\n\t" \
  "v_cvt_pk_bf16_f32 v26, v208, v209\n\t" \
  "v_cvt_pk_bf16_f32 v27, v210, v211\n\t" \
  "v_mfma_f32_16x16x32_bf16 v[180:183], v[128:131], v[140:143], v[180:183]\n\t" \
  "v_cvt_pk_bf16_f32 v28, v212, v213\n\t" \
  "v_cvt_pk_bf16_f32 v29, v214, v215\n\t" \
  "v_mfma_f32_16x16x32_bf16 v[216:219], v[136:139], v[124:127], v[216:219]\n\t" \
  "v_cvt_pk_bf16_f32 v84, v152, v153\n\t" \
  "v_cvt_pk_bf16_f32 v85, v154, v155\n\t" \
  "v_mfma_f32_16x16x32_bf16 v[224:227], v[108:111], v[124:127], v[224:227]\n\t" \
  "v_cvt_pk_bf16_f32 v86, v156, v157\n\t" \
  "v_cvt_pk_bf16_f32 v87, v158, v159\n\t" \
  "v_mfma_f32_16x16x32_bf16 v[232:235], v[144:147], v[124:127], v[232:235]\n\t" \
  "v_cvt_pk_bf16_f32 v88, v160, v161\n\t" \
  "v_cvt_pk_bf16_f32 v89, v162, v163\n\t" \
  "v_mfma_f32_16x16x32_bf16 v[240:243], v[140:143], v[124:127], v[240:243]\n\t" \
  "v_cvt_pk_bf16_f32 v90, v164, v165\n\t" \
  "v_cvt_pk_bf16_f32 v91, v166, v167\n\t" \
  "v_mfma_f32_16x16x32_bf16 v[220:223], v[136:139], v[148:151], v[220:223]\n\t" \
  "v_cvt_pk_bf16_f32 v92, v168, v169\n\t" \
  "v_cvt_pk_bf16_f32 v93, v170, v171\n\t" \
  "v_mfma_f32_16x16x32_bf16 v[228:231], v[108:111], v[148:151], v[228:231]\n\t" \
  "v_cvt_pk_bf16_f32 v94, v172, v173\n\t" \
  "v_cvt_pk_bf16_f32 v95, v174, v175\n\t" \
  "v_mfma_f32_16x16x32_bf16 v[236:239], v[144:147], v[148:151], v[236:239]\n\t" \
  "v_cvt_pk_bf16_f32 v96, v176, v177\n\t" \
  "v_cvt_pk_bf16_f32 v97, v178, v179\n\t" \
  "v_mfma_f32_16x16x32_bf16 v[244:247], v[140:143], v[148:151], v[244:247]\n\t" \
  "v_cvt_pk_bf16_f32 v98, v180, v181\n\t" \
  "v_cvt_pk_bf16_f32 v99, v182, v183\n\t" \
  "v_mfma_f32_16x16x32_bf16 v[200:203], v[22:25], v[92:95], v[34:37]\n\t" \
  "v_cvt_pk_bf16_f32 v66, v232, v233\n\t" \
  "v_cvt_pk_bf16_f32 v67, v234, v235\n\t" \
  "v_mfma_f32_16x16x32_bf16 v[208:211], v[26:29], v[96:99], v[34:37]\n\t" \
  "v_cvt_pk_bf16_f32 v76, v236, v237\n\t" \
  "v_cvt_pk_bf16_f32 v77, v238, v239\n\t" \
  "v_mfma_f32_16x16x32_bf16 v[212:215], v[22:25], v[96:99], v[38:41]\n\t" \
  "v_cvt_pk_bf16_f32 v64, v240, v241\n\t" \
  "v_cvt_pk_bf16_f32 v65, v242, v243\n\t" \
  "v_mfma_f32_16x16x32_bf16 v[204:207], v[18:21], v[92:95], v[38:41]\n\t" \
  "v_cvt_pk_bf16_f32 v74, v244, v245\n\t" \
  "v_cvt_pk_bf16_f32 v75, v246, v247\n\t" \
  "v_mfma_f32_16x16x32_bf16 v[192:195], v[18:21], v[88:91], v[34:37]\n\t" \
  "v_cvt_pk_bf16_f32 v68, v224, v225\n\t" \
  "v_cvt_pk_bf16_f32 v69, v226, v227\n\t" \
  "v_mfma_f32_16x16x32_bf16 v[196:199], v[14:17], v[88:91], v[38:41]\n\t" \
  "v_max_f32 v208, v208, v212\n\t" \
  "v_max_f32 v209, v209, v213\n\t" \
  "v_mfma_f32_16x16x32_bf16 v[184:187], v[14:17], v[84:87], v[34:37]\n\t" \
  "v_max_f32 v210, v210, v214\n\t" \
  "v_max_f32 v200, v200, v204\n\t" \
  "v_mfma_f32_16x16x32_bf16 v[188:191], v[10:13], v[84:87], v[42:45]\n\t" \
  "v_max_f32 v201, v201, v205\n\t" \
  "v_max_f32 v202, v202, v206\n\t" \
  "v_max_f32 v192, v192, v196\n\t" \
  "v_max_f32 v193, v193, v197\n\t" \
  "v_max_f32 v194, v194, v198\n\t" \
  "v_max_f32 v211, v211, v215\n\t" \
  "ds_read_b32 v52, v33 offset:48\n\t" \
  "ds_read_b64 v[72:73], v31\n\t" \
  "ds_read_b64 v[82:83], v31 offset:8\n\t" \
  "v_max3_f32 v106, v208, v209, v210\n\t" \
  "v_max_f32 v184, v184, v188\n\t" \
  "v_max_f32 v185, v185, v189\n\t" \
  "v_max_f32 v186, v186, v190\n\t" \
  "v_max_f32 v203, v203, v207\n\t" \
  "v_max3_f32 v104, v200, v201, v202\n\t" \
  "v_max_f32 v195, v195, v199\n\t" \
  "v_max3_f32 v102, v192, v193, v194\n\t" \
  "v_max_f32 v187, v187, v191\n\t" \
  "v_max3_f32 v100, v184, v185, v186\n\t" \
  "v_max_f32 v106, v106, v211\n\t" \
  "v_max_f32 v104, v104, v203\n\t" \
  "v_max_f32 v102, v102, v195\n\t" \
  "v_max_f32 v100, v100, v187\n\t" \
  "v_permlane16_swap_b32 v104, v106\n\t" \
  "v_cvt_pk_bf16_f32 v78, v228, v229\n\t" \
  "v_permlane16_swap_b32 v100, v102\n\t" \
  "v_max_f32 v104, v104, v106\n\t" \
  "v_max_f32 v100, v100, v102\n\t" \
  "v_cvt_pk_bf16_f32 v79, v230, v231\n\t" \
  "v_cvt_pk_bf16_f32 v70, v216, v217\n\t" \
  "v_permlane32_swap_b32 v100, v104\n\t" \
  "v_cvt_pk_bf16_f32 v71, v218, v219\n\t" \
  "v_max_f32 v100, v100, v104\n\t" \
  "v_mul_f32 v100, 0xbe8293ee, v100\n\t" \
  "v_mov_b32 v104, v100\n\t" \
  "v_cvt_pk_bf16_f32 v80, v220, v221\n\t" \
  "v_cvt_pk_bf16_f32 v81, v222, v223\n\t" \
  "v_permlane32_swap_b32 v100, v104\n\t" \
  "s_waitcnt lgkmcnt(0)\n\t" \
  "v_readfirstlane_b32 vcc_lo, v52\n\t" \
  "s_nop 1\n\t" \
  "s_cmp_eq_u32 vcc_lo, 0\n\t" \
  "s_cbranch_scc1 5f\n\t" \
  "4:\n\t" \
  "v_mov_b32 v106, v104\n\t" \
  "v_mov_b32 v102, v100\n\t" \
  "s_nop 0\n\t" \
  "v_permlane16_swap_b32 v104, v106\n\t" \
  "v_permlane16_swap_b32 v100, v102\n\t" \
  "v_fma_f32 v208, v208, %16, v106\n\t" \
  "v_fma_f32 v209, v209, %16, v106\n\t" \
  "v_fma_f32 v210, v210, %16, v106\n\t" \
  "v_fma_f32 v211, v211, %16, v106\n\t" \
  "v_exp_f32 v208, v208\n\t" \
  "v_exp_f32 v209, v209\n\t" \
  "v_exp_f32 v210, v210\n\t" \
  "v_exp_f32 v211, v211\n\t" \
  "v_cvt_pk_bf16_f32 v212, v208, v209\n\t" \
  "v_cvt_pk_bf16_f32 v213, v210, v211\n\t" \
  "v_bfi_b32 v214, v46, 0, v212\n\t" \
  "v_bfi_b32 v215, v47, 0, v213\n\t" \
  "v_and_b32 v212, v46, v212\n\t" \
  "v_and_b32 v213, v47, v213\n\t" \
  "v_fma_f32 v200, v200, %16, v104\n\t" \
  "v_fma_f32 v201, v201, %16, v104\n\t" \
  "v_mfma_f32_16x16x32_bf16 v[208:211], v[48:51], v[212:215], 0\n\t" \
  "v_fma_f32 v202, v202, %16, v104\n\t" \
  "v_fma_f32 v203, v203, %16, v104\n\t" \
  "v_mfma_f32_16x16x32_bf16 v[176:179], v[64:67], v[212:215], 0\n\t" \
  "v_exp_f32 v200, v200\n\t" \
  "v_mfma_f32_16x16x32_bf16 v[180:183], v[74:77], v[212:215], 0\n\t" \
  "v_exp_f32 v201, v201\n\t" \
  "v_exp_f32 v202, v202\n\t" \
  "v_exp_f32 v203, v203\n\t" \
  "v_cvt_pk_bf16_f32 v204, v200, v201\n\t" \
  "v_cvt_pk_bf16_f32 v205, v202, v203\n\t" \
  "v_bfi_b32 v206, v46, 0, v204\n\t" \
  "v_bfi_b32 v207, v47, 0, v205\n\t" \
  "v_rcp_f32 v106, v208\n\t" \
  "v_and_b32 v204, v46, v204\n\t" \
  "v_and_b32 v205, v47, v205\n\t" \
  "v_pk_mul_f32 v[176:177], v[176:177], v[106:107] op_sel_hi:[1,0]\n\t" \
  "v_pk_mul_f32 v[178:179], v[178:179], v[106:107] op_sel_hi:[1,0]\n\t" \
  "v_mfma_f32_16x16x32_bf16 v[200:203], v[48:51], v[204:207], 0\n\t" \
  "v_pk_mul_f32 v[180:181], v[180:181], v[106:107] op_sel_hi:[1,0]\n\t" \
  "v_pk_mul_f32 v[182:183], v[182:183], v[106:107] op_sel_hi:[1,0]\n\t" \
  "v_mfma_f32_16x16x32_bf16 v[168:171], v[66:69], v[204:207], 0\n\t" \
  "v_cvt_pk_bf16_f32 v240, v176, v177\n\t" \
  "v_cvt_pk_bf16_f32 v241, v178, v179\n\t" \
  "v_mfma_f32_16x16x32_bf16 v[172:175], v[76:79], v[204:207], 0\n\t" \
  "v_cvt_pk_bf16_f32 v242, v180, v181\n\t" \
  "v_cvt_pk_bf16_f32 v243, v182, v183\n\t" \
  "global_store_dwordx4 v1, v[240:243], %14 offset:0 sc1\n\t" \
  "v_fma_f32 v192, v192, %16, v102\n\t" \
  "v_fma_f32 v193, v193, %16, v102\n\t" \
  "v_fma_f32 v194, v194, %16, v102\n\t" \
  "v_fma_f32 v195, v195, %16, v102\n\t" \
  "v_exp_f32 v192, v192\n\t" \
  "v_exp_f32 v193, v193\n\t" \
  "v_exp_f32 v194, v194\n\t" \
  "v_exp_f32 v195, v195\n\t" \
  "v_cvt_pk_bf16_f32 v196, v192, v193\n\t" \
  "v_cvt_pk_bf16_f32 v197, v194, v195\n\t" \
  "v_bfi_b32 v198, v46, 0, v196\n\t" \
  "v_bfi_b32 v199, v47, 0, v197\n\t" \
  "v_rcp_f32 v104, v200\n\t" \
  "v_and_b32 v196, v46, v196\n\t" \
  "v_and_b32 v197, v47, v197\n\t" \
  "v_pk_mul_f32 v[168:169], v[168:169], v[104:105] op_sel_hi:[1,0]\n\t" \
  "v_pk_mul_f32 v[170:171], v[170:171], v[104:105] op_sel_hi:[1,0]\n\t" \
  "v_mfma_f32_16x16x32_bf16 v[192:195], v[48:51], v[196:199], 0\n\t" \
  "v_pk_mul_f32 v[172:173], v[172:173], v[104:105] op_sel_hi:[1,0]\n\t" \
  "v_pk_mul_f32 v[174:175], v[174:175], v[104:105] op_sel_hi:[1,0]\n\t" \
  "v_mfma_f32_16x16x32_bf16 v[160:163], v[68:71], v[196:199], 0\n\t" \
  "v_cvt_pk_bf16_f32 v232, v168, v169\n\t" \
  "v_cvt_pk_bf16_f32 v233, v170, v171\n\t" \
  "v_mfma_f32_16x16x32_bf16 v[164:167], v[78:81], v[196:199], 0\n\t" \
  "v_cvt_pk_bf16_f32 v234, v172, v173\n\t" \
  "v_cvt_pk_bf16_f32 v235, v174, v175\n\t" \
  "global_store_dwordx4 v1, v[232:235], %14 offset:-4096 sc1\n\t" \
  "v_fma_f32 v184, v184, %16, v100\n\t" \
  "v_fma_f32 v185, v185, %16, v100\n\t" \
  "v_fma_f32 v186, v186, %16, v100\n\t" \
  "v_fma_f32 v187, v187, %16, v100\n\t" \
  "v_exp_f32 v184, v184\n\t" \
  "v_exp_f32 v185, v185\n\t" \
  "v_exp_f32 v186, v186\n\t" \
  "v_exp_f32 v187, v187\n\t" \
  "v_cvt_pk_bf16_f32 v188, v184, v185\n\t" \
  "v_cvt_pk_bf16_f32 v189, v186, v187\n\t" \
  "v_bfi_b32 v190, v46, 0, v188\n\t" \
  "v_bfi_b32 v191, v47, 0, v189\n\t" \
  "v_rcp_f32 v102, v192\n\t" \
  "v_and_b32 v188, v46, v188\n\t" \
  "v_and_b32 v189, v47, v189\n\t" \
  "v_pk_mul_f32 v[160:161], v[160:161], v[102:103] op_sel_hi:[1,0]\n\t" \
  "v_pk_mul_f32 v[162:163], v[162:163], v[102:103] op_sel_hi:[1,0]\n\t" \
  "v_mfma_f32_16x16x32_bf16 v[184:187], v[48:51], v[188:191], 0\n\t" \
  "v_pk_mul_f32 v[164:165], v[164:165], v[102:103] op_sel_hi:[1,0]\n\t" \
  "v_pk_mul_f32 v[166:167], v[166:167], v[102:103] op_sel_hi:[1,0]\n\t" \
  "v_mfma_f32_16x16x32_bf16 v[152:155], v[70:73], v[188:191], 0\n\t" \
  "v_cvt_pk_bf16_f32 v224, v160, v161\n\t" \
  "v_cvt_pk_bf16_f32 v225, v162, v163\n\t" \
  "v_mfma_f32_16x16x32_bf16 v[156:159], v[80:83], v[188:191], 0\n\t" \
  "v_cvt_pk_bf16_f32 v226, v164, v165\n\t" \
  "v_cvt_pk_bf16_f32 v227, v166, v167\n\t" \
  "global_store_dwordx4 v4, v[224:227], %14 offset:0 sc1\n\t" \
  "v_rcp_f32 v100, v184\n\t" \
  "ds_read_b128 v[60:63], v56 offset:64\n\t" \
  "ds_read_b64_tr_b16 v[108:109], v5 offset:0\n\t" \
  "ds_read_b64_tr_b16 v[110:111], v5 offset:16\n\t" \
  "ds_read_b128 v[112:115], v6 offset:16384\n\t" \
  "ds_read_b128 v[116:119], v6 offset:15360\n\t" \
  "506:\n\t" \
  "s_cbranch_execz 510f\n\t" \
  "ds_read_b64_tr_b16 v[120:121], v5 offset:512\n\t" \
  "ds_read_b64_tr_b16 v[122:123], v5 offset:528\n\t" \
  "ds_read_b128 v[124:127], v6 offset:14336\n\t" \
  "ds_read_b64_tr_b16 v[128:129], v5 offset:1024\n\t" \
  "ds_read_b64_tr_b16 v[130:131], v5 offset:1040\n\t" \
  "ds_read_b128 v[132:135], v6 offset:13312\n\t" \
  "ds_read_b64_tr_b16 v[136:137], v5 offset:1536\n\t" \
  "ds_read_b64_tr_b16 v[138:139], v5 offset:1552\n\t" \
  "ds_read_b128 v[140:143], v6 offset:12288\n\t" \
  "ds_read_b64_tr_b16 v[144:145], v5 offset:2048\n\t" \
  "v_pk_mul_f32 v[152:153], v[152:153], v[100:101] op_sel_hi:[1,0]\n\t" \
  "v_pk_mul_f32 v[154:155], v[154:155], v[100:101] op_sel_hi:[1,0]\n\t" \
  "v_cvt_pk_bf16_f32 v216, v152, v153\n\t" \
  "v_pk_mul_f32 v[156:157], v[156:157], v[100:101] op_sel_hi:[1,0]\n\t" \
  "v_pk_mul_f32 v[158:159], v[158:159], v[100:101] op_sel_hi:[1,0]\n\t" \
  "v_cvt_pk_bf16_f32 v217, v154, v155\n\t" \
  "v_cvt_pk_bf16_f32 v218, v156, v157\n\t" \
  "v_cvt_pk_bf16_f32 v219, v158, v159\n\t" \
  "global_store_dwordx4 v4, v[216:219], %14 offset:-4096 sc1\n\t" \
  "ds_write_b32 v32, v0 offset:112\n\t" \
  "s_branch 9f\n\t" \
  "5:\n\t" \
  "ds_read_b32 v52, v33 offset:48\n\t" \
  "ds_read_b64 v[72:73], v31\n\t" \
  "ds_read_b64 v[82:83], v31 offset:8\n\t" \
  "s_waitcnt lgkmcnt(0)\n\t" \
  "v_readfirstlane_b32 vcc_lo, v52\n\t" \
  "s_nop 1\n\t" \
  "s_cmp_eq_u32 vcc_lo, 0\n\t" \
  "s_cbranch_scc1 5b\n\t" \
  "s_branch 4b\n\t" \
  "15:\n\t" \
  "ds_read_b128 v[10:13], v14 offset:128\n\t" \
  "s_waitcnt lgkmcnt(0)\n\t" \
  "v_and_b32 v10, v10, v11\n\t" \
  "v_and_b32 v12, v12, v13\n\t" \
  "v_and_b32 v10, v10, v12\n\t" \
  "s_nop 1\n\t" \
  "v_readfirstlane_b32 vcc_lo, v10\n\t" \
  "s_nop 1\n\t" \
  "s_cmp_eq_u32 vcc_lo, 0\n\t" \
  "s_cbranch_scc1 15b\n\t" \
  "s_branch 14b\n\t" \
  "9:\n\t" \
  "507:\n\t" \
  "s_cbranch_execz 26b\n\t" \
  "s_waitcnt lgkmcnt(14)\n\t" \
  "v_and_b32 v60, v60, v61\n\t" \
  "v_and_b32 v62, v62, v63\n\t" \
  "v_mov_b32 v12, %13\n\t" \
  "v_and_b32 v60, v60, v62\n\t" \
  "s_nop 0\n\t" \
  "v_readfirstlane_b32 vcc_lo, v60\n\t" \
  "s_nop 1\n\t" \
  "s_cmp_eq_u32 vcc_lo, 0\n\t" \
  "s_cbranch_scc1 7f\n\t" \
  "6:\n\t" \
  "s_waitcnt lgkmcnt(11)\n\t" \
  "v_mfma_f32_16x16x32_bf16 v[192:195], v[108:111], v[112:115], 0\n\t" \
  "ds_read_b64_tr_b16 v[146:147], v5 offset:2064\n\t" \
  "ds_read_b128 v[148:151], v6 offset:11264\n\t" \
  "s_waitcnt lgkmcnt(12)\n\t" \
  "v_mfma_f32_16x16x32_bf16 v[196:199], v[108:111], v[116:119], 0\n\t" \
  "ds_read_b64_tr_b16 v[64:65], v5 offset:2560\n\t" \
  "ds_read_b64_tr_b16 v[66:67], v5 offset:2576\n\t" \
  "s_waitcnt lgkmcnt(12)\n\t" \
  "v_mfma_f32_16x16x32_bf16 v[196:199], v[120:123], v[112:115], v[196:199]\n\t" \
  "ds_read_b128 v[68:71], v6 offset:10240\n\t" \
  "ds_read_b64_tr_b16 v[72:73], v5 offset:3072\n\t" \
  "s_waitcnt lgkmcnt(13)\n\t" \
  "v_mfma_f32_16x16x32_bf16 v[200:203], v[108:111], v[124:127], 0\n\t" \
  "ds_read_b64_tr_b16 v[74:75], v5 offset:3088\n\t" \
  "ds_read_b128 v[76:79], v6 offset:9216\n\t" \
  "v_mfma_f32_16x16x32_bf16 v[200:203], v[120:123], v[116:119], v[200:203]\n\t" \
  "ds_read_b64_tr_b16 v[80:81], v5 offset:3584\n\t" \
  "ds_read_b64_tr_b16 v[82:83], v5 offset:3600\n\t" \
  "s_waitcnt lgkmcnt(15)\n\t" \
  "v_mfma_f32_16x16x32_bf16 v[200:203], v[128:131], v[112:115], v[200:203]\n\t" \
  "ds_read_b128 v[84:87], v6 offset:0\n\t" \
  "ds_read_b128 v[88:91], v6 offset:1024\n\t" \
  "s_waitcnt lgkmcnt(15)\n\t" \
  "v_mfma_f32_16x16x32_bf16 v[204:207], v[108:111], v[132:135], 0\n\t" \
  "ds_read_b128 v[92:95], v6 offset:2048\n\t" \
  "ds_read_b128 v[96:99], v6 offset:3072\n\t" \
  "v_mfma_f32_16x16x32_bf16 v[204:207], v[120:123], v[124:127], v[204:207]\n\t" \
  "ds_read_b128 v[100:103], v6 offset:4096\n\t" \
  "ds_read_b128 v[104:107], v6 offset:5120\n\t" \
  "v_mfma_f32_16x16x32_bf16 v[204:207], v[128:131], v[116:119], v[204:207]\n\t" \
  "ds_read_b128 v[152:155], v6 offset:6144\n\t" \
  "ds_read_b128 v[156:159], v6 offset:7168\n\t" \
  "s_waitcnt lgkmcnt(14)\n\t" \
  "v_mfma_f32_16x16x32_bf16 v[204:207], v[136:139], v[112:115], v[204:207]\n\t" \
  "v_lshl_add_u32 v12, v12, 12, v4\n\t" \
  "s_lshl_b32 s88, %13, 5\n\t" \
  "v_mfma_f32_16x16x32_bf16 v[208:211], v[108:111], v[140:143], 0\n\t" \
  "v_mfma_f32_16x16x32_bf16 v[208:211], v[120:123], v[132:135], v[208:211]\n\t" \
  "v_mfma_f32_16x16x32_bf16 v[208:211], v[128:131], v[124:127], v[208:211]\n\t" \
  "s_and_b32 s88, s88, 64\n\t" \
  "v_xor_b32 v13, s88, v54\n\t" \
  "v_mfma_f32_16x16x32_bf16 v[208:211], v[136:139], v[116:119], v[208:211]\n\t" \
  "v_mfma_f32_16x16x32_bf16 v[208:211], v[144:147], v[112:115], v[208:211]\n\t" \
  "v_mfma_f32_16x16x32_bf16 v[212:215], v[108:111], v[148:151], 0\n\t" \
  "ds_read_b32 v55, v56 offset:104\n\t" \
  "v_mfma_f32_16x16x32_bf16 v[212:215], v[120:123], v[140:143], v[212:215]\n\t" \
  "v_mfma_f32_16x16x32_bf16 v[212:215], v[128:131], v[132:135], v[212:215]\n\t" \
  "v_bfe_u32 v11, v12, 4, 4\n\t" \
  "v_mfma_f32_16x16x32_bf16 v[212:215], v[136:139], v[124:127], v[212:215]\n\t" \
  "v_lshlrev_b32 v11, 7, v11\n\t" \
  "v_mfma_f32_16x16x32_bf16 v[212:215], v[144:147], v[116:119], v[212:215]\n\t" \
  "v_mov_b32 v10, 1\n\t" \
  "v_mfma_f32_16x16x32_bf16 v[212:215], v[64:67], v[112:115], v[212:215]\n\t" \
  "s_waitcnt lgkmcnt(1)\n\t" \
  "v_mfma_f32_16x16x32_bf16 v[216:219], v[108:111], v[68:71], 0\n\t" \
  "v_mfma_f32_16x16x32_bf16 v[216:219], v[120:123], v[148:151], v[216:219]\n\t" \
  "v_mfma_f32_16x16x32_bf16 v[216:219], v[128:131], v[140:143], v[216:219]\n\t" \
  "v_mfma_f32_16x16x32_bf16 v[216:219], v[136:139], v[132:135], v[216:219]\n\t" \
  "v_mfma_f32_16x16x32_bf16 v[216:219], v[144:147], v[124:127], v[216:219]\n\t" \
  "v_mfma_f32_16x16x32_bf16 v[216:219], v[64:67], v[116:119], v[216:219]\n\t" \
  "v_mfma_f32_16x16x32_bf16 v[216:219], v[72:75], v[112:115], v[216:219]\n\t" \
  "v_mfma_f32_16x16x32_bf16 v[220:223], v[108:111], v[76:79], 0\n\t" \
  "v_mfma_f32_16x16x32_bf16 v[220:223], v[120:123], v[68:71], v[220:223]\n\t" \
  "v_mfma_f32_16x16x32_bf16 v[220:223], v[128:131], v[148:151], v[220:223]\n\t" \
  "v_mfma_f32_16x16x32_bf16 v[220:223], v[136:139], v[140:143], v[220:223]\n\t" \
  "v_mfma_f32_16x16x32_bf16 v[220:223], v[144:147], v[132:135], v[220:223]\n\t" \
  "ds_read_b64_tr_b16 v[160:161], v5 offset:128\n\t" \
  "v_mfma_f32_16x16x32_bf16 v[220:223], v[64:67], v[124:127], v[220:223]\n\t" \
  "ds_read_b64_tr_b16 v[162:163], v5 offset:144\n\t" \
  "v_mfma_f32_16x16x32_bf16 v[220:223], v[72:75], v[116:119], v[220:223]\n\t" \
  "ds_read_b128 v[68:71], v7 offset:16384\n\t" \
  "v_mfma_f32_16x16x32_bf16 v[220:223], v[80:83], v[112:115], v[220:223]\n\t" \
  "ds_read_b128 v[76:79], v7 offset:15360\n\t" \
  "v_mfma_f32_16x16x32_bf16 v[252:255], v[108:111], v[84:87], 0\n\t" \
  "ds_read_b64_tr_b16 v[164:165], v5 offset:640\n\t" \
  "v_mfma_f32_16x16x32_bf16 v[252:255], v[120:123], v[88:91], v[252:255]\n\t" \
  "ds_read_b64_tr_b16 v[166:167], v5 offset:656\n\t" \
  "v_mfma_f32_16x16x32_bf16 v[252:255], v[128:131], v[92:95], v[252:255]\n\t" \
  "ds_read_b128 v[168:171], v7 offset:14336\n\t" \
  "v_mfma_f32_16x16x32_bf16 v[252:255], v[136:139], v[96:99], v[252:255]\n\t" \
  "ds_read_b64_tr_b16 v[84:85], v5 offset:1152\n\t" \
  "v_mfma_f32_16x16x32_bf16 v[252:255], v[144:147], v[100:103], v[252:255]\n\t" \
  "ds_read_b64_tr_b16 v[86:87], v5 offset:1168\n\t" \
  "v_mfma_f32_16x16x32_bf16 v[252:255], v[64:67], v[104:107], v[252:255]\n\t" \
  "ds_read_b128 v[172:175], v7 offset:13312\n\t" \
  "v_mfma_f32_16x16x32_bf16 v[252:255], v[72:75], v[152:155], v[252:255]\n\t" \
  "ds_read_b64_tr_b16 v[176:177], v5 offset:1664\n\t" \
  "v_mfma_f32_16x16x32_bf16 v[252:255], v[80:83], v[156:159], v[252:255]\n\t" \
  "ds_read_b64_tr_b16 v[178:179], v5 offset:1680\n\t" \
  "v_mfma_f32_16x16x32_bf16 v[248:251], v[108:111], v[88:91], 0\n\t" \
  "ds_read_b128 v[180:183], v7 offset:12288\n\t" \
  "v_mfma_f32_16x16x32_bf16 v[248:251], v[120:123], v[92:95], v[248:251]\n\t" \
  "v_mfma_f32_16x16x32_bf16 v[248:251], v[128:131], v[96:99], v[248:251]\n\t" \
  "ds_read_b64_tr_b16 v[80:81], v5 offset:2176\n\t" \
  "v_mfma_f32_16x16x32_bf16 v[248:251], v[136:139], v[100:103], v[248:251]\n\t" \
  "ds_read_b64_tr_b16 v[82:83], v5 offset:2192\n\t" \
  "v_mfma_f32_16x16x32_bf16 v[248:251], v[144:147], v[104:107], v[248:251]\n\t" \
  "ds_read_b128 v[88:91], v7 offset:11264\n\t" \
  "v_mfma_f32_16x16x32_bf16 v[248:251], v[64:67], v[152:155], v[248:251]\n\t" \
  "v_mfma_f32_16x16x32_bf16 v[248:251], v[72:75], v[156:159], v[248:251]\n\t" \
  "v_mfma_f32_16x16x32_bf16 v[244:247], v[108:111], v[92:95], 0\n\t" \
  "v_mfma_f32_16x16x32_bf16 v[244:247], v[120:123], v[96:99], v[244:247]\n\t" \
  "ds_read_b64_tr_b16 v[92:93], v5 offset:2688\n\t" \
  "v_mfma_f32_16x16x32_bf16 v[244:247], v[128:131], v[100:103], v[244:247]\n\t" \
  "ds_read_b64_tr_b16 v[94:95], v5 offset:2704\n\t" \
  "v_mfma_f32_16x16x32_bf16 v[244:247], v[136:139], v[104:107], v[244:247]\n\t" \
  "ds_read_b128 v[72:75], v7 offset:10240\n\t" \
  "v_mfma_f32_16x16x32_bf16 v[244:247], v[144:147], v[152:155], v[244:247]\n\t" \
  "v_mfma_f32_16x16x32_bf16 v[244:247], v[64:67], v[156:159], v[244:247]\n\t" \
  "v_mfma_f32_16x16x32_bf16 v[240:243], v[108:111], v[96:99], 0\n\t" \
  "v_mfma_f32_16x16x32_bf16 v[240:243], v[120:123], v[100:103], v[240:243]\n\t" \
  "v_mfma_f32_16x16x32_bf16 v[240:243], v[128:131], v[104:107], v[240:243]\n\t" \
  "ds_read_b64_tr_b16 v[96:97], v5 offset:3200\n\t" \
  "v_mfma_f32_16x16x32_bf16 v[240:243], v[136:139], v[152:155], v[240:243]\n\t" \
  "ds_read_b64_tr_b16 v[98:99], v5 offset:3216\n\t" \
  "v_mfma_f32_16x16x32_bf16 v[240:243], v[144:147], v[156:159], v[240:243]\n\t" \
  "ds_read_b128 v[64:67], v7 offset:9216\n\t" \
  "v_mfma_f32_16x16x32_bf16 v[236:239], v[108:111], v[100:103], 0\n\t" \
  "v_mfma_f32_16x16x32_bf16 v[236:239], v[120:123], v[104:107], v[236:239]\n\t" \
  "v_mfma_f32_16x16x32_bf16 v[236:239], v[128:131], v[152:155], v[236:239]\n\t" \
  "v_mfma_f32_16x16x32_bf16 v[236:239], v[136:139], v[156:159], v[236:239]\n\t" \
  "v_mfma_f32_16x16x32_bf16 v[232:235], v[108:111], v[104:107], 0\n\t" \
  "s_waitcnt vmcnt(0)\n\t" \
  "s_mov_b64 exec, 1\n\t" \
  "ds_add_rtn_u32 v10, v56, v10 offset:16\n\t" \
  "s_mov_b64 exec, -1\n\t" \
  "v_mfma_f32_16x16x32_bf16 v[232:235], v[120:123], v[152:155], v[232:235]\n\t" \
  "ds_read_b64_tr_b16 v[100:101], v5 offset:3712\n\t" \
  "v_mfma_f32_16x16x32_bf16 v[232:235], v[128:131], v[156:159], v[232:235]\n\t" \
  "ds_read_b64_tr_b16 v[102:103], v5 offset:3728\n\t" \
  "v_mfma_f32_16x16x32_bf16 v[228:231], v[108:111], v[152:155], 0\n\t" \
  "ds_read_b128 v[104:107], v7 offset:0\n\t" \
  "v_mfma_f32_16x16x32_bf16 v[228:231], v[120:123], v[156:159], v[228:231]\n\t" \
  "ds_read_b128 v[184:187], v7 offset:1024\n\t" \
  "v_mfma_f32_16x16x32_bf16 v[224:227], v[108:111], v[156:159], 0\n\t" \
  "ds_read_b128 v[188:191], v7 offset:2048\n\t" \
  "s_waitcnt lgkmcnt(6)\n\t" \
  "v_mfma_f32_16x16x32_bf16 v[192:195], v[160:163], v[68:71], v[192:195]\n\t" \
  "ds_read_b128 v[108:111], v7 offset:3072\n\t" \
  "v_mfma_f32_16x16x32_bf16 v[196:199], v[160:163], v[76:79], v[196:199]\n\t" \
  "s_waitcnt lgkmcnt(6)\n\t" \
  "v_readfirstlane_b32 vcc_lo, v10\n\t" \
  "v_mfma_f32_16x16x32_bf16 v[196:199], v[164:167], v[68:71], v[196:199]\n\t" \
  "s_cmp_eq_u32 vcc_lo, 3\n\t" \
  "s_cbranch_scc1 23f\n\t" \
  "22:\n\t" \
  "v_mfma_f32_16x16x32_bf16 v[200:203], v[160:163], v[168:171], v[200:203]\n\t" \
  "ds_read_b128 v[112:115], v7 offset:4096\n\t" \
  "v_mfma_f32_16x16x32_bf16 v[200:203], v[164:167], v[76:79], v[200:203]\n\t" \
  "ds_read_b128 v[116:119], v7 offset:5120\n\t" \
  "v_mfma_f32_16x16x32_bf16 v[200:203], v[84:87], v[68:71], v[200:203]\n\t" \
  "ds_read_b128 v[120:123], v7 offset:6144\n\t" \
  "v_mfma_f32_16x16x32_bf16 v[204:207], v[160:163], v[172:175], v[204:207]\n\t" \
  "ds_read_b128 v[124:127], v7 offset:7168\n\t" \
  "v_mfma_f32_16x16x32_bf16 v[204:207], v[164:167], v[168:171], v[204:207]\n\t" \
  "v_mfma_f32_16x16x32_bf16 v[204:207], v[84:87], v[76:79], v[204:207]\n\t" \
  "v_mfma_f32_16x16x32_bf16 v[204:207], v[176:179], v[68:71], v[204:207]\n\t" \
  "v_mfma_f32_16x16x32_bf16 v[208:211], v[160:163], v[180:183], v[208:211]\n\t" \
  "v_and_b32 v53, 0x2000, v12\n\t" \
  "v_mfma_f32_16x16x32_bf16 v[208:211], v[164:167], v[172:175], v[208:211]\n\t" \
  "v_mfma_f32_16x16x32_bf16 v[208:211], v[84:87], v[168:171], v[208:211]\n\t" \
  "v_add_u32 v53, v53, v12\n\t" \
  "v_mfma_f32_16x16x32_bf16 v[208:211], v[176:179], v[76:79], v[208:211]\n\t" \
  "v_mfma_f32_16x16x32_bf16 v[208:211], v[80:83], v[68:71], v[208:211]\n\t" \
  "v_lshrrev_b32 v14, 1, v4\n\t" \
  "v_mfma_f32_16x16x32_bf16 v[212:215], v[160:163], v[88:91], v[212:215]\n\t" \
  "v_mfma_f32_16x16x32_bf16 v[212:215], v[164:167], v[180:183], v[212:215]\n\t" \
  "v_lshrrev_b32 v15, 24, v3\n\t" \
  "v_mfma_f32_16x16x32_bf16 v[212:215], v[84:87], v[172:175], v[212:215]\n\t" \
  "v_mfma_f32_16x16x32_bf16 v[212:215], v[176:179], v[168:171], v[212:215]\n\t" \
  "v_add_u32 v14, %20, v14\n\t" \
  "v_mfma_f32_16x16x32_bf16 v[212:215], v[80:83], v[76:79], v[212:215]\n\t" \
  "v_mfma_f32_16x16x32_bf16 v[212:215], v[92:95], v[68:71], v[212:215]\n\t" \
  "v_lshrrev_b32 v18, 10, v12\n\t" \
  "v_mfma_f32_16x16x32_bf16 v[216:219], v[160:163], v[72:75], v[216:219]\n\t" \
  "v_mfma_f32_16x16x32_bf16 v[216:219], v[164:167], v[88:91], v[216:219]\n\t" \
  "v_and_b32 v18, 12, v18\n\t" \
  "v_mfma_f32_16x16x32_bf16 v[216:219], v[84:87], v[180:183], v[216:219]\n\t" \
  "v_mfma_f32_16x16x32_bf16 v[216:219], v[176:179], v[172:175], v[216:219]\n\t" \
  "v_mov_b32 v17, 16\n\t" \
  "v_mfma_f32_16x16x32_bf16 v[216:219], v[80:83], v[168:171], v[216:219]\n\t" \
  "v_mfma_f32_16x16x32_bf16 v[216:219], v[92:95], v[76:79], v[216:219]\n\t" \
  "v_add_u32 v18, v56, v18\n\t" \
  "v_mfma_f32_16x16x32_bf16 v[216:219], v[96:99], v[68:71], v[216:219]\n\t" \
  "ds_read_b32 v16, v56 offset:16\n\t" \
  "v_mfma_f32_16x16x32_bf16 v[220:223], v[160:163], v[64:67], v[220:223]\n\t" \
  "s_mov_b32 s76, 0\n\t" \
  "v_mfma_f32_16x16x32_bf16 v[220:223], v[164:167], v[72:75], v[220:223]\n\t" \
  "v_mfma_f32_16x16x32_bf16 v[220:223], v[84:87], v[88:91], v[220:223]\n\t" \
  "s_brev_b32 s77, 1\n\t" \
  "v_mfma_f32_16x16x32_bf16 v[220:223], v[176:179], v[180:183], v[220:223]\n\t" \
  "v_mfma_f32_16x16x32_bf16 v[220:223], v[80:83], v[172:175], v[220:223]\n\t" \
  "v_add_u32 v28, 0, v13\n\t" \
  "ds_read_b64_tr_b16 v[88:89], v5 offset:256\n\t" \
  "v_mfma_f32_16x16x32_bf16 v[220:223], v[92:95], v[168:171], v[220:223]\n\t" \
  "ds_read_b64_tr_b16 v[90:91], v5 offset:272\n\t" \
  "v_mfma_f32_16x16x32_bf16 v[220:223], v[96:99], v[76:79], v[220:223]\n\t" \
  "v_xor_b32 v29, 32, v28\n\t" \
  "s_waitcnt lgkmcnt(2)\n\t" \
  "v_readfirstlane_b32 vcc_lo, v16\n\t" \
  "v_mfma_f32_16x16x32_bf16 v[220:223], v[100:103], v[68:71], v[220:223]\n\t" \
  "s_cmp_ge_u32 vcc_lo, 4\n\t" \
  "s_cbranch_scc0 30f\n\t" \
  "31:\n\t" \
  "v_mfma_f32_16x16x32_bf16 v[252:255], v[160:163], v[104:107], v[252:255]\n\t" \
  "v_xor_b32 v30, 64, v28\n\t" \
  "s_lshr_b64 s[90:91], %18, 0\n\t" \
  "s_and_b32 s90, s90, 15\n\t" \
  "s_lshl_b32 s90, s90, 15\n\t" \
  "v_mfma_f32_16x16x32_bf16 v[252:255], v[164:167], v[184:187], v[252:255]\n\t" \
  "s_add_u32 s90, s90, s92\n\t" \
  "s_addc_u32 s91, s93, 0\n\t" \
  "v_mfma_f32_16x16x32_bf16 v[252:255], v[84:87], v[188:191], v[252:255]\n\t" \
  "v_xor_b32 v31, 96, v28\n\t" \
  "global_load_dwordx4 v[64:67], v53, s[90:91] offset:0 sc1\n\t" \
  "v_mfma_f32_16x16x32_bf16 v[252:255], v[176:179], v[108:111], v[252:255]\n\t" \
  "ds_read_b128 v[128:131], v8 offset:16384\n\t" \
  "v_mfma_f32_16x16x32_bf16 v[252:255], v[80:83], v[112:115], v[252:255]\n\t" \
  "v_add_u32 v32, 65536, v13\n\t" \
  "ds_read_b128 v[132:135], v8 offset:15360\n\t" \
  "v_mfma_f32_16x16x32_bf16 v[252:255], v[92:95], v[116:119], v[252:255]\n\t" \
  "global_load_dwordx4 v[68:71], v53, s[90:91] offset:1024 sc1\n\t" \
  "ds_read_b64_tr_b16 v[136:137], v5 offset:768\n\t" \
  "v_mfma_f32_16x16x32_bf16 v[252:255], v[96:99], v[120:123], v[252:255]\n\t" \
  "v_xor_b32 v33, 32, v32\n\t" \
  "ds_read_b64_tr_b16 v[138:139], v5 offset:784\n\t" \
  "v_mfma_f32_16x16x32_bf16 v[252:255], v[100:103], v[124:127], v[252:255]\n\t" \
  "ds_read_b128 v[140:143], v8 offset:14336\n\t" \
  "v_mfma_f32_16x16x32_bf16 v[248:251], v[160:163], v[184:187], v[248:251]\n\t" \
  "v_xor_b32 v34, 64, v32\n\t" \
  "global_load_dwordx4 v[72:75], v53, s[90:91] offset:2048 sc1\n\t" \
  "v_mfma_f32_16x16x32_bf16 v[248:251], v[164:167], v[188:191], v[248:251]\n\t" \
  "ds_read_b64_tr_b16 v[104:105], v5 offset:1280\n\t" \
  "v_mfma_f32_16x16x32_bf16 v[248:251], v[84:87], v[108:111], v[248:251]\n\t" \
  "v_xor_b32 v35, 96, v32\n\t" \
  "ds_read_b64_tr_b16 v[106:107], v5 offset:1296\n\t" \
  "v_mfma_f32_16x16x32_bf16 v[248:251], v[176:179], v[112:115], v[248:251]\n\t" \
  "global_load_dwordx4 v[76:79], v53, s[90:91] offset:3072 sc1\n\t" \
  "ds_read_b128 v[144:147], v8 offset:13312\n\t" \
  "v_mfma_f32_16x16x32_bf16 v[248:251], v[80:83], v[116:119], v[248:251]\n\t" \
  "v_add_u32 v36, 131072, v13\n\t" \
  "ds_read_b64_tr_b16 v[148:149], v5 offset:1792\n\t" \
  "v_mfma_f32_16x16x32_bf16 v[248:251], v[92:95], v[120:123], v[248:251]\n\t" \
  "ds_read_b64_tr_b16 v[150:151], v5 offset:1808\n\t" \
  "v_mfma_f32_16x16x32_bf16 v[248:251], v[96:99], v[124:127], v[248:251]\n\t" \
  "v_xor_b32 v37, 32, v36\n\t" \
  "ds_read_b128 v[152:155], v8 offset:12288\n\t" \
  "v_mfma_f32_16x16x32_bf16 v[244:247], v[160:163], v[188:191], v[244:247]\n\t" \
  "ds_read_b64_tr_b16 v[100:101], v5 offset:2304\n\t" \
  "v_mfma_f32_16x16x32_bf16 v[244:247], v[164:167], v[108:111], v[244:247]\n\t" \
  "v_xor_b32 v38, 64, v36\n\t" \
  "ds_read_b64_tr_b16 v[102:103], v5 offset:2320\n\t" \
  "v_mfma_f32_16x16x32_bf16 v[244:247], v[84:87], v[112:115], v[244:247]\n\t" \
  "ds_read_b128 v[156:159], v8 offset:11264\n\t" \
  "v_mfma_f32_16x16x32_bf16 v[244:247], v[176:179], v[116:119], v[244:247]\n\t" \
  "v_xor_b32 v39, 96, v36\n\t" \
  "ds_read_b64_tr_b16 v[96:97], v5 offset:2816\n\t" \
  "v_mfma_f32_16x16x32_bf16 v[244:247], v[80:83], v[120:123], v[244:247]\n\t" \
  "ds_read_b64_tr_b16 v[98:99], v5 offset:2832\n\t" \
  "v_mfma_f32_16x16x32_bf16 v[244:247], v[92:95], v[124:127], v[244:247]\n\t" \
  "v_lshrrev_b32 v57, 5, v4\n\t" \
  "ds_read_b128 v[168:171], v8 offset:10240\n\t" \
  "v_mfma_f32_16x16x32_bf16 v[240:243], v[160:163], v[108:111], v[240:243]\n\t" \
  "v_mfma_f32_16x16x32_bf16 v[240:243], v[164:167], v[112:115], v[240:243]\n\t" \
  "v_and_b32 v57, 0x18, v57\n\t" \
  "v_mfma_f32_16x16x32_bf16 v[240:243], v[84:87], v[116:119], v[240:243]\n\t" \
  "ds_read_b64_tr_b16 v[92:93], v5 offset:3328\n\t" \
  "v_mfma_f32_16x16x32_bf16 v[240:243], v[176:179], v[120:123], v[240:243]\n\t" \
  "v_and_b32 v62, 0x70, v4\n\t" \
  "ds_read_b64_tr_b16 v[94:95], v5 offset:3344\n\t" \
  "v_mfma_f32_16x16x32_bf16 v[240:243], v[80:83], v[124:127], v[240:243]\n\t" \
  "ds_read_b128 v[108:111], v8 offset:9216\n\t" \
  "v_mfma_f32_16x16x32_bf16 v[236:239], v[160:163], v[112:115], v[236:239]\n\t" \
  "v_xor_b32 v57, v57, v62\n\t" \
  "v_mfma_f32_16x16x32_bf16 v[236:239], v[164:167], v[116:119], v[236:239]\n\t" \
  "v_mfma_f32_16x16x32_bf16 v[236:239], v[84:87], v[120:123], v[236:239]\n\t" \
  "v_and_b32 v58, 0xf0, v4\n\t" \
  "v_mfma_f32_16x16x32_bf16 v[236:239], v[176:179], v[124:127], v[236:239]\n\t" \
  "v_mfma_f32_16x16x32_bf16 v[232:235], v[160:163], v[116:119], v[232:235]\n\t" \
  "v_lshl_add_u32 v57, v58, 3, v57\n\t" \
  "v_mfma_f32_16x16x32_bf16 v[232:235], v[164:167], v[120:123], v[232:235]\n\t" \
  "ds_read_b64_tr_b16 v[80:81], v5 offset:3840\n\t" \
  "v_mfma_f32_16x16x32_bf16 v[232:235], v[84:87], v[124:127], v[232:235]\n\t" \
  "v_add_u32 v57, %23, v57\n\t" \
  "ds_read_b64_tr_b16 v[82:83], v5 offset:3856\n\t" \
  "v_mfma_f32_16x16x32_bf16 v[228:231], v[160:163], v[120:123], v[228:231]\n\t" \
  "ds_read_b128 v[84:87], v8 offset:0\n\t" \
  "v_mfma_f32_16x16x32_bf16 v[228:231], v[164:167], v[124:127], v[228:231]\n\t" \
  "v_xor_b32 v58, 32, v57\n\t" \
  "ds_read_b128 v[172:175], v8 offset:1024\n\t" \
  "v_mfma_f32_16x16x32_bf16 v[224:227], v[160:163], v[124:127], v[224:227]\n\t" \
  "ds_read_b128 v[164:167], v8 offset:2048\n\t" \
  "s_waitcnt lgkmcnt(5)\n\t" \
  "v_mfma_f32_16x16x32_bf16 v[192:195], v[88:91], v[128:131], v[192:195]\n\t" \
  "v_xor_b32 v59, 64, v57\n\t" \
  "ds_read_b128 v[160:163], v8 offset:3072\n\t" \
  "v_mfma_f32_16x16x32_bf16 v[196:199], v[88:91], v[132:135], v[196:199]\n\t" \
  "ds_read_b128 v[176:179], v8 offset:4096\n\t" \
  "v_mfma_f32_16x16x32_bf16 v[196:199], v[136:139], v[128:131], v[196:199]\n\t" \
  "v_xor_b32 v60, 96, v57\n\t" \
  "ds_read_b128 v[180:183], v8 offset:5120\n\t" \
  "v_mfma_f32_16x16x32_bf16 v[200:203], v[88:91], v[140:143], v[200:203]\n\t" \
  "ds_read_b128 v[184:187], v8 offset:6144\n\t" \
  "v_mfma_f32_16x16x32_bf16 v[200:203], v[136:139], v[132:135], v[200:203]\n\t" \
  "v_lshrrev_b32 v61, 3, v4\n\t" \
  "ds_read_b128 v[188:191], v8 offset:7168\n\t" \
  "v_mfma_f32_16x16x32_bf16 v[200:203], v[104:107], v[128:131], v[200:203]\n\t" \
  "v_mfma_f32_16x16x32_bf16 v[204:207], v[88:91], v[144:147], v[204:207]\n\t" \
  "v_and_b32 v61, 0x70, v61\n\t" \
  "v_mfma_f32_16x16x32_bf16 v[204:207], v[136:139], v[140:143], v[204:207]\n\t" \
  "v_mfma_f32_16x16x32_bf16 v[204:207], v[104:107], v[132:135], v[204:207]\n\t" \
  "v_xor_b32 v61, v61, v4\n\t" \
  "v_mfma_f32_16x16x32_bf16 v[204:207], v[148:151], v[128:131], v[204:207]\n\t" \
  "v_mfma_f32_16x16x32_bf16 v[208:211], v[88:91], v[152:155], v[208:211]\n\t" \
  "v_add_u32 v61, %23, v61\n\t" \
  "v_mfma_f32_16x16x32_bf16 v[208:211], v[136:139], v[144:147], v[208:211]\n\t" \
  "v_mfma_f32_16x16x32_bf16 v[208:211], v[104:107], v[140:143], v[208:211]\n\t" \
  "v_and_b32 v63, 0x380, v4\n\t" \
  "v_mfma_f32_16x16x32_bf16 v[208:211], v[148:151], v[132:135], v[208:211]\n\t" \
  "v_mfma_f32_16x16x32_bf16 v[208:211], v[100:103], v[128:131], v[208:211]\n\t" \
  "v_lshl_add_u32 v62, v63, 6, v62\n\t" \
  "v_mfma_f32_16x16x32_bf16 v[212:215], v[88:91], v[156:159], v[212:215]\n\t" \
  "v_mfma_f32_16x16x32_bf16 v[212:215], v[136:139], v[152:155], v[212:215]\n\t" \
  "v_add_u32 v63, 0x10000, v62\n\t" \
  "v_mfma_f32_16x16x32_bf16 v[212:215], v[104:107], v[144:147], v[212:215]\n\t" \
  "v_mfma_f32_16x16x32_bf16 v[212:215], v[148:151], v[140:143], v[212:215]\n\t" \
  "v_mfma_f32_16x16x32_bf16 v[212:215], v[100:103], v[132:135], v[212:215]\n\t" \
  "v_mfma_f32_16x16x32_bf16 v[212:215], v[96:99], v[128:131], v[212:215]\n\t" \
  "v_mfma_f32_16x16x32_bf16 v[216:219], v[88:91], v[168:171], v[216:219]\n\t" \
  "v_mfma_f32_16x16x32_bf16 v[216:219], v[136:139], v[156:159], v[216:219]\n\t" \
  "v_mfma_f32_16x16x32_bf16 v[216:219], v[104:107], v[152:155], v[216:219]\n\t" \
  "v_mfma_f32_16x16x32_bf16 v[216:219], v[148:151], v[144:147], v[216:219]\n\t" \
  "v_mfma_f32_16x16x32_bf16 v[216:219], v[100:103], v[140:143], v[216:219]\n\t" \
  "v_mfma_f32_16x16x32_bf16 v[216:219], v[96:99], v[132:135], v[216:219]\n\t" \
  "v_mfma_f32_16x16x32_bf16 v[216:219], v[92:95], v[128:131], v[216:219]\n\t" \
  "v_mfma_f32_16x16x32_bf16 v[220:223], v[88:91], v[108:111], v[220:223]\n\t" \
  "v_mfma_f32_16x16x32_bf16 v[220:223], v[136:139], v[168:171], v[220:223]\n\t" \
  "v_mfma_f32_16x16x32_bf16 v[220:223], v[104:107], v[156:159], v[220:223]\n\t" \
  "v_mfma_f32_16x16x32_bf16 v[220:223], v[148:151], v[152:155], v[220:223]\n\t" \
  "v_mfma_f32_16x16x32_bf16 v[220:223], v[100:103], v[144:147], v[220:223]\n\t" \
  "ds_read_b64_tr_b16 v[112:113], v5 offset:384\n\t" \
  "v_mfma_f32_16x16x32_bf16 v[220:223], v[96:99], v[140:143], v[220:223]\n\t" \
  "ds_read_b64_tr_b16 v[114:115], v5 offset:400\n\t" \
  "v_mfma_f32_16x16x32_bf16 v[220:223], v[92:95], v[132:135], v[220:223]\n\t" \
  "ds_read_b128 v[108:111], v9 offset:16384\n\t" \
  "s_waitcnt lgkmcnt(3)\n\t" \
  "v_mfma_f32_16x16x32_bf16 v[220:223], v[80:83], v[128:131], v[220:223]\n\t" \
  "ds_read_b128 v[116:119], v9 offset:15360\n\t" \
  "v_mfma_f32_16x16x32_bf16 v[252:255], v[88:91], v[84:87], v[252:255]\n\t" \
  "ds_read_b64_tr_b16 v[120:121], v5 offset:896\n\t" \
  "v_mfma_f32_16x16x32_bf16 v[252:255], v[136:139], v[172:175], v[252:255]\n\t" \
  "ds_read_b64_tr_b16 v[122:123], v5 offset:912\n\t" \
  "v_mfma_f32_16x16x32_bf16 v[252:255], v[104:107], v[164:167], v[252:255]\n\t" \
  "ds_read_b128 v[124:127], v9 offset:14336\n\t" \
  "v_mfma_f32_16x16x32_bf16 v[252:255], v[148:151], v[160:163], v[252:255]\n\t" \
  "ds_read_b64_tr_b16 v[128:129], v5 offset:1408\n\t" \
  "v_mfma_f32_16x16x32_bf16 v[252:255], v[100:103], v[176:179], v[252:255]\n\t" \
  "ds_read_b64_tr_b16 v[130:131], v5 offset:1424\n\t" \
  "v_mfma_f32_16x16x32_bf16 v[252:255], v[96:99], v[180:183], v[252:255]\n\t" \
  "ds_read_b128 v[84:87], v9 offset:13312\n\t" \
  "v_mfma_f32_16x16x32_bf16 v[252:255], v[92:95], v[184:187], v[252:255]\n\t" \
  "ds_read_b64_tr_b16 v[132:133], v5 offset:1920\n\t" \
  "v_mfma_f32_16x16x32_bf16 v[252:255], v[80:83], v[188:191], v[252:255]\n\t" \
  "ds_read_b64_tr_b16 v[134:135], v5 offset:1936\n\t" \
  "v_mfma_f32_16x16x32_bf16 v[248:251], v[88:91], v[172:175], v[248:251]\n\t" \
  "ds_read_b128 v[140:143], v9 offset:12288\n\t" \
  "v_mfma_f32_16x16x32_bf16 v[248:251], v[136:139], v[164:167], v[248:251]\n\t" \
  "v_mfma_f32_16x16x32_bf16 v[248:251], v[104:107], v[160:163], v[248:251]\n\t" \
  "ds_read_b64_tr_b16 v[144:145], v5 offset:2432\n\t" \
  "v_mfma_f32_16x16x32_bf16 v[248:251], v[148:151], v[176:179], v[248:251]\n\t" \
  "ds_read_b64_tr_b16 v[146:147], v5 offset:2448\n\t" \
  "v_mfma_f32_16x16x32_bf16 v[248:251], v[100:103], v[180:183], v[248:251]\n\t" \
  "ds_read_b128 v[80:83], v9 offset:11264\n\t" \
  "v_mfma_f32_16x16x32_bf16 v[248:251], v[96:99], v[184:187], v[248:251]\n\t" \
  "v_mfma_f32_16x16x32_bf16 v[248:251], v[92:95], v[188:191], v[248:251]\n\t" \
  "v_mfma_f32_16x16x32_bf16 v[244:247], v[88:91], v[164:167], v[244:247]\n\t" \
  "v_mfma_f32_16x16x32_bf16 v[244:247], v[136:139], v[160:163], v[244:247]\n\t" \
  "ds_read_b64_tr_b16 v[152:153], v5 offset:2944\n\t" \
  "v_mfma_f32_16x16x32_bf16 v[244:247], v[104:107], v[176:179], v[244:247]\n\t" \
  "ds_read_b64_tr_b16 v[154:155], v5 offset:2960\n\t" \
  "v_mfma_f32_16x16x32_bf16 v[244:247], v[148:151], v[180:183], v[244:247]\n\t" \
  "ds_read_b128 v[92:95], v9 offset:10240\n\t" \
  "v_mfma_f32_16x16x32_bf16 v[244:247], v[100:103], v[184:187], v[244:247]\n\t" \
  "v_mfma_f32_16x16x32_bf16 v[244:247], v[96:99], v[188:191], v[244:247]\n\t" \
  "v_mfma_f32_16x16x32_bf16 v[240:243], v[88:91], v[160:163], v[240:243]\n\t" \
  "v_mfma_f32_16x16x32_bf16 v[240:243], v[136:139], v[176:179], v[240:243]\n\t" \
  "v_mfma_f32_16x16x32_bf16 v[240:243], v[104:107], v[180:183], v[240:243]\n\t" \
  "ds_read_b64_tr_b16 v[156:157], v5 offset:3456\n\t" \
  "v_mfma_f32_16x16x32_bf16 v[240:243], v[148:151], v[184:187], v[240:243]\n\t" \
  "ds_read_b64_tr_b16 v[158:159], v5 offset:3472\n\t" \
  "v_mfma_f32_16x16x32_bf16 v[240:243], v[100:103], v[188:191], v[240:243]\n\t" \
  "ds_read_b128 v[96:99], v9 offset:9216\n\t" \
  "v_mfma_f32_16x16x32_bf16 v[236:239], v[88:91], v[176:179], v[236:239]\n\t" \
  "v_mfma_f32_16x16x32_bf16 v[236:239], v[136:139], v[180:183], v[236:239]\n\t" \
  "v_mfma_f32_16x16x32_bf16 v[236:239], v[104:107], v[184:187], v[236:239]\n\t" \
  "v_mfma_f32_16x16x32_bf16 v[236:239], v[148:151], v[188:191], v[236:239]\n\t" \
  "v_mfma_f32_16x16x32_bf16 v[232:235], v[88:91], v[180:183], v[232:235]\n\t" \
  "v_mfma_f32_16x16x32_bf16 v[232:235], v[136:139], v[184:187], v[232:235]\n\t" \
  "ds_read_b64_tr_b16 v[148:149], v5 offset:3968\n\t" \
  "v_mfma_f32_16x16x32_bf16 v[232:235], v[104:107], v[188:191], v[232:235]\n\t" \
  "ds_read_b64_tr_b16 v[150:151], v5 offset:3984\n\t" \
  "ds_write_b32 v18, v17 offset:176\n\t" \
  "v_mfma_f32_16x16x32_bf16 v[228:231], v[88:91], v[184:187], v[228:231]\n\t" \
  "ds_read_b128 v[168:171], v9 offset:7168\n\t" \
  "v_mfma_f32_16x16x32_bf16 v[228:231], v[136:139], v[188:191], v[228:231]\n\t" \
  "ds_read_b128 v[172:175], v9 offset:6144\n\t" \
  "v_mfma_f32_16x16x32_bf16 v[224:227], v[88:91], v[188:191], v[224:227]\n\t" \
  "ds_read_b128 v[136:139], v9 offset:5120\n\t" \
  "s_waitcnt lgkmcnt(6)\n\t" \
  "v_mfma_f32_16x16x32_bf16 v[192:195], v[112:115], v[108:111], v[192:195]\n\t" \
  "v_mfma_f32_16x16x32_bf16 v[196:199], v[112:115], v[116:119], v[196:199]\n\t" \
  "v_mfma_f32_16x16x32_bf16 v[196:199], v[120:123], v[108:111], v[196:199]\n\t" \
  "ds_read_b128 v[164:167], v9 offset:4096\n\t" \
  "v_mfma_f32_16x16x32_bf16 v[200:203], v[112:115], v[124:127], v[200:203]\n\t" \
  "v_mfma_f32_16x16x32_bf16 v[200:203], v[120:123], v[116:119], v[200:203]\n\t" \
  "v_mfma_f32_16x16x32_bf16 v[200:203], v[128:131], v[108:111], v[200:203]\n\t" \
  "v_mfma_f32_16x16x32_bf16 v[204:207], v[112:115], v[84:87], v[204:207]\n\t" \
  "ds_read_b128 v[160:163], v9 offset:3072\n\t" \
  "v_cndmask_b32 v20, v195, 0, s[76:77]\n\t" \
  "v_mfma_f32_16x16x32_bf16 v[204:207], v[120:123], v[124:127], v[204:207]\n\t" \
  "ds_bpermute_b32 v20, v15, v20\n\t" \
  "v_add_f32 v225, v225, v192\n\t" \
  "v_mfma_f32_16x16x32_bf16 v[204:207], v[128:131], v[116:119], v[204:207]\n\t" \
  "v_add_f32 v226, v226, v193\n\t" \
  "v_add_f32 v227, v227, v194\n\t" \
  "v_mfma_f32_16x16x32_bf16 v[204:207], v[132:135], v[108:111], v[204:207]\n\t" \
  "ds_read_b128 v[40:43], v56 offset:80\n\t" \
  "v_cndmask_b32 v21, v199, v195, s[76:77]\n\t" \
  "v_mfma_f32_16x16x32_bf16 v[208:211], v[112:115], v[140:143], v[208:211]\n\t" \
  "ds_bpermute_b32 v21, v15, v21\n\t" \
  "v_add_f32 v229, v229, v196\n\t" \
  "v_mfma_f32_16x16x32_bf16 v[208:211], v[120:123], v[84:87], v[208:211]\n\t" \
  "ds_read_b128 v[176:179], v9 offset:2048\n\t" \
  "v_add_f32 v230, v230, v197\n\t" \
  "v_mfma_f32_16x16x32_bf16 v[208:211], v[128:131], v[124:127], v[208:211]\n\t" \
  "v_add_f32 v231, v231, v198\n\t" \
  "v_cndmask_b32 v22, v203, v199, s[76:77]\n\t" \
  "v_mfma_f32_16x16x32_bf16 v[208:211], v[132:135], v[116:119], v[208:211]\n\t" \
  "ds_bpermute_b32 v22, v15, v22\n\t" \
  "v_add_f32 v233, v233, v200\n\t" \
  "v_mfma_f32_16x16x32_bf16 v[208:211], v[144:147], v[108:111], v[208:211]\n\t" \
  "v_add_f32 v234, v234, v201\n\t" \
  "v_add_f32 v235, v235, v202\n\t" \
  "v_mfma_f32_16x16x32_bf16 v[212:215], v[112:115], v[80:83], v[212:215]\n\t" \
  "s_waitcnt lgkmcnt(4)\n\t" \
  "v_add_f32 v224, v224, v20\n\t" \
  "v_cndmask_b32 v23, v207, v203, s[76:77]\n\t" \
  "v_mfma_f32_16x16x32_bf16 v[212:215], v[120:123], v[140:143], v[212:215]\n\t" \
  "s_waitcnt lgkmcnt(3)\n\t" \
  "v_min_u32 v40, v40, v41\n\t" \
  "v_min3_u32 v40, v40, v42, v43\n\t" \
  "v_mfma_f32_16x16x32_bf16 v[212:215], v[128:131], v[84:87], v[212:215]\n\t" \
  "v_readfirstlane_b32 vcc_lo, v40\n\t" \
  "ds_read_b128 v[180:183], v9 offset:1024\n\t" \
  "v_mfma_f32_16x16x32_bf16 v[212:215], v[132:135], v[124:127], v[212:215]\n\t" \
  "s_cmp_ge_u32 vcc_lo, 7\n\t" \
  "s_cbranch_scc0 32f\n\t" \
  "33:\n\t" \
  "v_mfma_f32_16x16x32_bf16 v[212:215], v[144:147], v[116:119], v[212:215]\n\t" \
  "ds_bpermute_b32 v23, v15, v23\n\t" \
  "v_add_f32 v237, v237, v204\n\t" \
  "v_mfma_f32_16x16x32_bf16 v[212:215], v[152:155], v[108:111], v[212:215]\n\t" \
  "v_add_f32 v238, v238, v205\n\t" \
  "v_add_f32 v239, v239, v206\n\t" \
  "v_mfma_f32_16x16x32_bf16 v[216:219], v[112:115], v[92:95], v[216:219]\n\t" \
  "s_waitcnt lgkmcnt(4)\n\t" \
  "v_add_f32 v228, v228, v21\n\t" \
  "s_waitcnt lgkmcnt(2)\n\t" \
  "v_add_f32 v232, v232, v22\n\t" \
  "v_mfma_f32_16x16x32_bf16 v[216:219], v[120:123], v[80:83], v[216:219]\n\t" \
  "v_cndmask_b32 v24, v211, v207, s[76:77]\n\t" \
  "ds_bpermute_b32 v24, v15, v24\n\t" \
  "v_mfma_f32_16x16x32_bf16 v[216:219], v[128:131], v[140:143], v[216:219]\n\t" \
  "v_add_f32 v241, v241, v208\n\t" \
  "v_add_f32 v242, v242, v209\n\t" \
  "v_mfma_f32_16x16x32_bf16 v[216:219], v[132:135], v[84:87], v[216:219]\n\t" \
  "ds_read_b128 v[184:187], v9 offset:0\n\t" \
  "ds_write_b32 v18, v17 offset:192\n\t" \
  "v_mfma_f32_16x16x32_bf16 v[216:219], v[144:147], v[124:127], v[216:219]\n\t" \
  "v_add_f32 v243, v243, v210\n\t" \
  "ds_read_b64_tr_b16 v[192:193], v28 offset:22528\n\t" \
  "v_mfma_f32_16x16x32_bf16 v[216:219], v[152:155], v[116:119], v[216:219]\n\t" \
  "v_cndmask_b32 v25, v215, v211, s[76:77]\n\t" \
  "ds_bpermute_b32 v25, v15, v25\n\t" \
  "v_mfma_f32_16x16x32_bf16 v[216:219], v[156:159], v[108:111], v[216:219]\n\t" \
  "s_waitcnt lgkmcnt(5)\n\t" \
  "v_add_f32 v236, v236, v23\n\t" \
  "v_add_f32 v245, v245, v212\n\t" \
  "v_mfma_f32_16x16x32_bf16 v[220:223], v[112:115], v[96:99], v[220:223]\n\t" \
  "v_add_f32 v246, v246, v213\n\t" \
  "v_add_f32 v247, v247, v214\n\t" \
  "v_mfma_f32_16x16x32_bf16 v[220:223], v[120:123], v[92:95], v[220:223]\n\t" \
  "ds_read_b64_tr_b16 v[194:195], v28 offset:23040\n\t" \
  "v_mfma_f32_16x16x32_bf16 v[220:223], v[128:131], v[80:83], v[220:223]\n\t" \
  "s_waitcnt lgkmcnt(5)\n\t" \
  "v_add_f32 v240, v240, v24\n\t" \
  "v_mfma_f32_16x16x32_bf16 v[220:223], v[132:135], v[140:143], v[220:223]\n\t" \
  "ds_read_b64_tr_b16 v[196:197], v28 offset:26624\n\t" \
  "v_mfma_f32_16x16x32_bf16 v[220:223], v[144:147], v[84:87], v[220:223]\n\t" \
  "ds_read_b64_tr_b16 v[198:199], v28 offset:27136\n\t" \
  "v_mfma_f32_16x16x32_bf16 v[220:223], v[152:155], v[124:127], v[220:223]\n\t" \
  "ds_read_b32 v16, v56 offset:96\n\t" \
  "v_cndmask_b32 v26, v219, v215, s[76:77]\n\t" \
  "v_mfma_f32_16x16x32_bf16 v[220:223], v[156:159], v[116:119], v[220:223]\n\t" \
  "ds_bpermute_b32 v26, v15, v26\n\t" \
  "v_add_f32 v249, v249, v216\n\t" \
  "v_mfma_f32_16x16x32_bf16 v[220:223], v[148:151], v[108:111], v[220:223]\n\t" \
  "v_add_f32 v250, v250, v217\n\t" \
  "v_add_f32 v251, v251, v218\n\t" \
  "v_mfma_f32_16x16x32_bf16 v[224:227], v[112:115], v[168:171], v[224:227]\n\t" \
  "s_waitcnt lgkmcnt(5)\n\t" \
  "v_add_f32 v244, v244, v25\n\t" \
  "v_mfma_f32_16x16x32_bf16 v[228:231], v[112:115], v[172:175], v[228:231]\n\t" \
  "ds_read_b64_tr_b16 v[200:201], v29 offset:22528\n\t" \
  "v_mfma_f32_16x16x32_bf16 v[228:231], v[120:123], v[168:171], v[228:231]\n\t" \
  "ds_read_b64_tr_b16 v[202:203], v29 offset:23040\n\t" \
  "v_mfma_f32_16x16x32_bf16 v[232:235], v[112:115], v[136:139], v[232:235]\n\t" \
  "ds_read_b64_tr_b16 v[204:205], v29 offset:26624\n\t" \
  "v_mfma_f32_16x16x32_bf16 v[232:235], v[120:123], v[172:175], v[232:235]\n\t" \
  "s_waitcnt lgkmcnt(4)\n\t" \
  "v_readfirstlane_b32 s75, v16\n\t" \
  "v_mfma_f32_16x16x32_bf16 v[232:235], v[128:131], v[168:171], v[232:235]\n\t" \
  "s_cmp_lg_u32 s75, 0\n\t" \
  "s_cselect_b32 s69, -1, 0\n\t" \
  "v_mfma_f32_16x16x32_bf16 v[236:239], v[112:115], v[164:167], v[236:239]\n\t" \
  "s_lshr_b64 s[90:91], %18, 4\n\t" \
  "508:\n\t" \
  "s_cbranch_execz 26b\n\t" \
  "s_and_b32 s90, s90, 15\n\t" \
  "s_lshl_b32 s90, s90, 15\n\t" \
  "v_and_b32 v52, s69, v53\n\t" \
  "v_mfma_f32_16x16x32_bf16 v[236:239], v[120:123], v[136:139], v[236:239]\n\t" \
  "s_add_u32 s90, s90, s92\n\t" \
  "s_addc_u32 s91, s93, 0\n\t" \
  "s_cmp_lg_u32 s75, 0\n\t" \
  "v_mfma_f32_16x16x32_bf16 v[236:239], v[128:131], v[172:175], v[236:239]\n\t" \
  "s_cselect_b64 s[90:91], s[90:91], %11\n\t" \
  "s_waitcnt lgkmcnt(3)\n\t" \
  "v_add_f32 v248, v248, v26\n\t" \
  "v_mfma_f32_16x16x32_bf16 v[236:239], v[132:135], v[168:171], v[236:239]\n\t" \
  "global_load_dwordx4 v[80:83], v52, s[90:91] offset:0 sc1\n\t" \
  "v_cndmask_b32 v27, v223, v219, s[76:77]\n\t" \
  "v_mfma_f32_16x16x32_bf16 v[240:243], v[112:115], v[160:163], v[240:243]\n\t" \
  "ds_bpermute_b32 v27, v15, v27\n\t" \
  "v_add_f32 v253, v253, v220\n\t" \
  "v_mfma_f32_16x16x32_bf16 v[240:243], v[120:123], v[164:167], v[240:243]\n\t" \
  "v_add_f32 v254, v254, v221\n\t" \
  "v_add_f32 v255, v255, v222\n\t" \
  "v_mfma_f32_16x16x32_bf16 v[240:243], v[128:131], v[136:139], v[240:243]\n\t" \
  "global_load_dwordx4 v[84:87], v52, s[90:91] offset:1024 sc1\n\t" \
  "v_cvt_pk_bf16_f32 v45, v226, v227\n\t" \
  "v_mfma_f32_16x16x32_bf16 v[240:243], v[132:135], v[172:175], v[240:243]\n\t" \
  "v_cvt_pk_bf16_f32 v44, v224, v225\n\t" \
  "v_cvt_pk_bf16_f32 v47, v230, v231\n\t" \
  "v_mfma_f32_16x16x32_bf16 v[240:243], v[144:147], v[168:171], v[240:243]\n\t" \
  "ds_write_b64 v14, v[44:45] offset:0\n\t" \
  "v_cvt_pk_bf16_f32 v46, v228, v229\n\t" \
  "v_mfma_f32_16x16x32_bf16 v[244:247], v[112:115], v[176:179], v[244:247]\n\t" \
  "global_load_dwordx4 v[88:91], v52, s[90:91] offset:2048 sc1\n\t" \
  "ds_write_b64 v14, v[46:47] offset:512\n\t" \
  "v_mfma_f32_16x16x32_bf16 v[244:247], v[120:123], v[160:163], v[244:247]\n\t" \
  "v_cvt_pk_bf16_f32 v49, v234, v235\n\t" \
  "v_cvt_pk_bf16_f32 v48, v232, v233\n\t" \
  "v_mfma_f32_16x16x32_bf16 v[244:247], v[128:131], v[164:167], v[244:247]\n\t" \
  "ds_write_b64 v14, v[48:49] offset:1024\n\t" \
  "v_cvt_pk_bf16_f32 v51, v238, v239\n\t" \
  "v_mfma_f32_16x16x32_bf16 v[244:247], v[132:135], v[136:139], v[244:247]\n\t" \
  "global_load_dwordx4 v[92:95], v52, s[90:91] offset:3072 sc1\n\t" \
  "s_waitcnt lgkmcnt(3)\n\t" \
  "v_add_f32 v252, v252, v27\n\t" \
  "v_mfma_f32_16x16x32_bf16 v[244:247], v[144:147], v[172:175], v[244:247]\n\t" \
  "v_cvt_pk_bf16_f32 v50, v236, v237\n\t" \
  "ds_read_b64_tr_b16 v[206:207], v29 offset:27136\n\t" \
  "v_mfma_f32_16x16x32_bf16 v[244:247], v[152:155], v[168:171], v[244:247]\n\t" \
  "s_lshr_b64 s[90:91], %18, 8\n\t" \
  "s_and_b32 s90, s90, 15\n\t" \
  "s_lshl_b32 s90, s90, 15\n\t" \
  "v_mfma_f32_16x16x32_bf16 v[248:251], v[112:115], v[180:183], v[248:251]\n\t" \
  "s_add_u32 s90, s90, s92\n\t" \
  "s_addc_u32 s91, s93, 0\n\t" \
  "s_cmp_lg_u32 s75, 0\n\t" \
  "v_mfma_f32_16x16x32_bf16 v[248:251], v[120:123], v[176:179], v[248:251]\n\t" \
  "s_cselect_b64 s[90:91], s[90:91], %11\n\t" \
  "ds_write_b64 v14, v[50:51] offset:1536\n\t" \
  "v_mfma_f32_16x16x32_bf16 v[248:251], v[128:131], v[160:163], v[248:251]\n\t" \
  "global_load_dwordx4 v[96:99], v52, s[90:91] offset:0 sc1\n\t" \
  "v_cvt_pk_bf16_f32 v45, v242, v243\n\t" \
  "v_mfma_f32_16x16x32_bf16 v[248:251], v[132:135], v[164:167], v[248:251]\n\t" \
  "v_cvt_pk_bf16_f32 v44, v240, v241\n\t" \
  "ds_read_b64_tr_b16 v[208:209], v30 offset:22528\n\t" \
  "v_mfma_f32_16x16x32_bf16 v[248:251], v[144:147], v[136:139], v[248:251]\n\t" \
  "ds_write_b64 v14, v[44:45] offset:2048\n\t" \
  "ds_read_b64_tr_b16 v[210:211], v30 offset:23040\n\t" \
  "v_mfma_f32_16x16x32_bf16 v[248:251], v[152:155], v[172:175], v[248:251]\n\t" \
  "global_load_dwordx4 v[100:103], v52, s[90:91] offset:1024 sc1\n\t" \
  "v_cvt_pk_bf16_f32 v47, v246, v247\n\t" \
  "v_mfma_f32_16x16x32_bf16 v[248:251], v[156:159], v[168:171], v[248:251]\n\t" \
  "v_cvt_pk_bf16_f32 v46, v244, v245\n\t" \
  "ds_read_b64_tr_b16 v[212:213], v30 offset:26624\n\t" \
  "v_mfma_f32_16x16x32_bf16 v[252:255], v[112:115], v[184:187], v[252:255]\n\t" \
  "ds_write_b64 v14, v[46:47] offset:2560\n\t" \
  "ds_read_b64_tr_b16 v[214:215], v30 offset:27136\n\t" \
  "v_mfma_f32_16x16x32_bf16 v[252:255], v[120:123], v[180:183], v[252:255]\n\t" \
  "global_load_dwordx4 v[104:107], v52, s[90:91] offset:2048 sc1\n\t" \
  "ds_read_b64_tr_b16 v[216:217], v31 offset:22528\n\t" \
  "v_mfma_f32_16x16x32_bf16 v[252:255], v[128:131], v[176:179], v[252:255]\n\t" \
  "ds_read_b64_tr_b16 v[218:219], v31 offset:23040\n\t" \
  "v_mfma_f32_16x16x32_bf16 v[252:255], v[132:135], v[160:163], v[252:255]\n\t" \
  "ds_read_b64_tr_b16 v[220:221], v31 offset:26624\n\t" \
  "v_mfma_f32_16x16x32_bf16 v[252:255], v[144:147], v[164:167], v[252:255]\n\t" \
  "global_load_dwordx4 v[108:111], v52, s[90:91] offset:3072 sc1\n\t" \
  "v_mov_b32 v19, 15\n\t" \
  "v_mfma_f32_16x16x32_bf16 v[252:255], v[152:155], v[136:139], v[252:255]\n\t" \
  "v_cvt_pk_bf16_f32 v49, v250, v251\n\t" \
  "v_cvt_pk_bf16_f32 v48, v248, v249\n\t" \
  "v_mfma_f32_16x16x32_bf16 v[252:255], v[156:159], v[172:175], v[252:255]\n\t" \
  "ds_write_b64 v14, v[48:49] offset:3072\n\t" \
  "ds_read_b64_tr_b16 v[222:223], v31 offset:27136\n\t" \
  "v_mfma_f32_16x16x32_bf16 v[252:255], v[148:151], v[168:171], v[252:255]\n\t" \
  "ds_read_b32 v16, v56 offset:96\n\t" \
  "s_nop 6\n\t" \
  "v_cvt_pk_bf16_f32 v51, v254, v255\n\t" \
  "v_cvt_pk_bf16_f32 v50, v252, v253\n\t" \
  "ds_write_b64 v14, v[50:51] offset:3584\n\t" \
  "ds_write_b32 v18, v19 offset:32\n\t" \
  "s_cmp_eq_u32 s75, 0\n\t" \
  "s_cbranch_scc1 40f\n\t" \
  "41:\n\t" \
  "s_waitcnt vmcnt(10)\n\t" \
  "v_mfma_f32_16x16x32_bf16 v[224:227], v[192:195], v[64:67], 0\n\t" \
  "v_mfma_f32_16x16x32_bf16 v[224:227], v[196:199], v[68:71], v[224:227]\n\t" \
  "s_lshr_b64 s[90:91], %18, 12\n\t" \
  "s_and_b32 s90, s90, 15\n\t" \
  "s_lshl_b32 s90, s90, 15\n\t" \
  "s_add_u32 s90, s90, s92\n\t" \
  "s_addc_u32 s91, s93, 0\n\t" \
  "global_load_dwordx4 v[112:115], v53, s[90:91] offset:0 sc1\n\t" \
  "s_waitcnt vmcnt(9)\n\t" \
  "v_mfma_f32_16x16x32_bf16 v[228:231], v[196:199], v[76:79], 0\n\t" \
  "v_mfma_f32_16x16x32_bf16 v[228:231], v[192:195], v[72:75], v[228:231]\n\t" \
  "global_load_dwordx4 v[116:119], v53, s[90:91] offset:1024 sc1\n\t" \
  "v_mfma_f32_16x16x32_bf16 v[232:235], v[200:203], v[64:67], 0\n\t" \
  "ds_read_b64_tr_b16 v[192:193], v28 offset:30720\n\t" \
  "s_waitcnt lgkmcnt(4)\n\t" \
  "v_mfma_f32_16x16x32_bf16 v[232:235], v[204:207], v[68:71], v[232:235]\n\t" \
  "ds_read_b64_tr_b16 v[194:195], v28 offset:31232\n\t" \
  "global_load_dwordx4 v[120:123], v53, s[90:91] offset:2048 sc1\n\t" \
  "v_mfma_f32_16x16x32_bf16 v[236:239], v[204:207], v[76:79], 0\n\t" \
  "ds_read_b64_tr_b16 v[196:197], v28 offset:34816\n\t" \
  "v_mfma_f32_16x16x32_bf16 v[236:239], v[200:203], v[72:75], v[236:239]\n\t" \
  "ds_read_b64_tr_b16 v[198:199], v28 offset:35328\n\t" \
  "global_load_dwordx4 v[124:127], v53, s[90:91] offset:3072 sc1\n\t" \
  "v_mfma_f32_16x16x32_bf16 v[240:243], v[208:211], v[64:67], 0\n\t" \
  "ds_read_b64_tr_b16 v[200:201], v29 offset:30720\n\t" \
  "v_mfma_f32_16x16x32_bf16 v[240:243], v[212:215], v[68:71], v[240:243]\n\t" \
  "ds_read_b64_tr_b16 v[202:203], v29 offset:31232\n\t" \
  "s_lshr_b64 s[90:91], %18, 16\n\t" \
  "s_and_b32 s90, s90, 15\n\t" \
  "s_lshl_b32 s90, s90, 15\n\t" \
  "s_add_u32 s90, s90, s92\n\t" \
  "s_addc_u32 s91, s93, 0\n\t" \
  "global_load_dwordx4 v[128:131], v53, s[90:91] offset:0 sc1\n\t" \
  "v_mfma_f32_16x16x32_bf16 v[244:247], v[212:215], v[76:79], 0\n\t" \
  "ds_read_b64_tr_b16 v[204:205], v29 offset:34816\n\t" \
  "v_mfma_f32_16x16x32_bf16 v[244:247], v[208:211], v[72:75], v[244:247]\n\t" \
  "ds_read_b64_tr_b16 v[206:207], v29 offset:35328\n\t" \
  "global_load_dwordx4 v[132:135], v53, s[90:91] offset:1024 sc1\n\t" \
  "v_mfma_f32_16x16x32_bf16 v[248:251], v[216:219], v[64:67], 0\n\t" \
  "ds_read_b64_tr_b16 v[208:209], v30 offset:30720\n\t" \
  "v_mfma_f32_16x16x32_bf16 v[248:251], v[220:223], v[68:71], v[248:251]\n\t" \
  "ds_read_b64_tr_b16 v[210:211], v30 offset:31232\n\t" \
  "global_load_dwordx4 v[136:139], v53, s[90:91] offset:2048 sc1\n\t" \
  "v_mfma_f32_16x16x32_bf16 v[252:255], v[220:223], v[76:79], 0\n\t" \
  "ds_read_b64_tr_b16 v[212:213], v30 offset:34816\n\t" \
  "v_mfma_f32_16x16x32_bf16 v[252:255], v[216:219], v[72:75], v[252:255]\n\t" \
  "ds_read_b64_tr_b16 v[214:215], v30 offset:35328\n\t" \
  "global_load_dwordx4 v[140:143], v53, s[90:91] offset:3072 sc1\n\t" \
  "s_waitcnt lgkmcnt(6)\n\t" \
  "s_waitcnt vmcnt(14)\n\t" \
  "v_mfma_f32_16x16x32_bf16 v[224:227], v[192:195], v[80:83], v[224:227]\n\t" \
  "ds_read_b64_tr_b16 v[216:217], v31 offset:30720\n\t" \
  "s_lshr_b64 s[90:91], %18, 20\n\t" \
  "s_and_b32 s90, s90, 15\n\t" \
  "s_lshl_b32 s90, s90, 15\n\t" \
  "v_mfma_f32_16x16x32_bf16 v[224:227], v[196:199], v[84:87], v[224:227]\n\t" \
  "ds_read_b64_tr_b16 v[218:219], v31 offset:31232\n\t" \
  "s_add_u32 s90, s90, s92\n\t" \
  "s_addc_u32 s91, s93, 0\n\t" \
  "s_waitcnt vmcnt(12)\n\t" \
  "v_mfma_f32_16x16x32_bf16 v[228:231], v[196:199], v[92:95], v[228:231]\n\t" \
  "ds_read_b64_tr_b16 v[220:221], v31 offset:34816\n\t" \
  "global_load_dwordx4 v[144:147], v53, s[90:91] offset:0 sc1\n\t" \
  "v_mfma_f32_16x16x32_bf16 v[228:231], v[192:195], v[88:91], v[228:231]\n\t" \
  "ds_read_b64_tr_b16 v[222:223], v31 offset:35328\n\t" \
  "v_mfma_f32_16x16x32_bf16 v[232:235], v[200:203], v[80:83], v[232:235]\n\t" \
  "ds_read_b64_tr_b16 v[192:193], v28 offset:38912\n\t" \
  "s_waitcnt lgkmcnt(6)\n\t" \
  "v_mfma_f32_16x16x32_bf16 v[232:235], v[204:207], v[84:87], v[232:235]\n\t" \
  "ds_read_b64_tr_b16 v[194:195], v28 offset:39424\n\t" \
  "ds_write_b32 v18, v17 offset:160\n\t" \
  "v_mfma_f32_16x16x32_bf16 v[236:239], v[204:207], v[92:95], v[236:239]\n\t" \
  "ds_read_b64_tr_b16 v[196:197], v28 offset:43008\n\t" \
  "global_load_dwordx4 v[148:151], v53, s[90:91] offset:1024 sc1\n\t" \
  "v_mfma_f32_16x16x32_bf16 v[236:239], v[200:203], v[88:91], v[236:239]\n\t" \
  "ds_read_b64_tr_b16 v[198:199], v28 offset:43520\n\t" \
  "v_mfma_f32_16x16x32_bf16 v[240:243], v[208:211], v[80:83], v[240:243]\n\t" \
  "ds_read_b64_tr_b16 v[200:201], v29 offset:38912\n\t" \
  "s_waitcnt lgkmcnt(7)\n\t" \
  "v_mfma_f32_16x16x32_bf16 v[240:243], v[212:215], v[84:87], v[240:243]\n\t" \
  "ds_read_b64_tr_b16 v[202:203], v29 offset:39424\n\t" \
  "v_mfma_f32_16x16x32_bf16 v[244:247], v[212:215], v[92:95], v[244:247]\n\t" \
  "ds_read_b64_tr_b16 v[204:205], v29 offset:43008\n\t" \
  "global_load_dwordx4 v[152:155], v53, s[90:91] offset:2048 sc1\n\t" \
  "v_mfma_f32_16x16x32_bf16 v[244:247], v[208:211], v[88:91], v[244:247]\n\t" \
  "ds_read_b64_tr_b16 v[206:207], v29 offset:43520\n\t" \
  "v_mfma_f32_16x16x32_bf16 v[248:251], v[216:219], v[80:83], v[248:251]\n\t" \
  "ds_read_b64_tr_b16 v[208:209], v30 offset:38912\n\t" \
  "s_waitcnt lgkmcnt(6)\n\t" \
  "v_mfma_f32_16x16x32_bf16 v[248:251], v[220:223], v[84:87], v[248:251]\n\t" \
  "ds_read_b64_tr_b16 v[210:211], v30 offset:39424\n\t" \
  "v_mfma_f32_16x16x32_bf16 v[252:255], v[220:223], v[92:95], v[252:255]\n\t" \
  "ds_read_b64_tr_b16 v[212:213], v30 offset:43008\n\t" \
  "global_load_dwordx4 v[156:159], v53, s[90:91] offset:3072 sc1\n\t" \
  "v_mfma_f32_16x16x32_bf16 v[252:255], v[216:219], v[88:91], v[252:255]\n\t" \
  "ds_read_b64_tr_b16 v[214:215], v30 offset:43520\n\t" \
  "s_waitcnt vmcnt(14)\n\t" \
  "v_mfma_f32_16x16x32_bf16 v[224:227], v[192:195], v[96:99], v[224:227]\n\t" \
  "ds_read_b64_tr_b16 v[216:217], v31 offset:38912\n\t" \
  "s_lshr_b64 s[90:91], %18, 24\n\t" \
  "s_and_b32 s90, s90, 15\n\t" \
  "s_lshl_b32 s90, s90, 15\n\t" \
  "s_waitcnt lgkmcnt(6)\n\t" \
  "v_mfma_f32_16x16x32_bf16 v[224:227], v[196:199], v[100:103], v[224:227]\n\t" \
  "ds_read_b64_tr_b16 v[218:219], v31 offset:39424\n\t" \
  "s_add_u32 s90, s90, s92\n\t" \
  "s_addc_u32 s91, s93, 0\n\t" \
  "s_waitcnt vmcnt(12)\n\t" \
  "v_mfma_f32_16x16x32_bf16 v[228:231], v[196:199], v[108:111], v[228:231]\n\t" \
  "ds_read_b64_tr_b16 v[220:221], v31 offset:43008\n\t" \
  "global_load_dwordx4 v[160:163], v53, s[90:91] offset:0 sc1\n\t" \
  "v_mfma_f32_16x16x32_bf16 v[228:231], v[192:195], v[104:107], v[228:231]\n\t" \
  "ds_read_b64_tr_b16 v[222:223], v31 offset:43520\n\t" \
  "v_mfma_f32_16x16x32_bf16 v[232:235], v[200:203], v[96:99], v[232:235]\n\t" \
  "ds_read_b64_tr_b16 v[192:193], v28 offset:47104\n\t" \
  "s_waitcnt lgkmcnt(6)\n\t" \
  "v_mfma_f32_16x16x32_bf16 v[232:235], v[204:207], v[100:103], v[232:235]\n\t" \
  "ds_read_b64_tr_b16 v[194:195], v28 offset:47616\n\t" \
  "v_mfma_f32_16x16x32_bf16 v[236:239], v[204:207], v[108:111], v[236:239]\n\t" \
  "ds_read_b64_tr_b16 v[196:197], v28 offset:51200\n\t" \
  "global_load_dwordx4 v[164:167], v53, s[90:91] offset:1024 sc1\n\t" \
  "ds_read_b32 v16, v56 offset:96\n\t" \
  "v_mfma_f32_16x16x32_bf16 v[236:239], v[200:203], v[104:107], v[236:239]\n\t" \
  "ds_read_b64_tr_b16 v[198:199], v28 offset:51712\n\t" \
  "v_mfma_f32_16x16x32_bf16 v[240:243], v[208:211], v[96:99], v[240:243]\n\t" \
  "ds_read_b64_tr_b16 v[200:201], v29 offset:47104\n\t" \
  "s_waitcnt lgkmcnt(7)\n\t" \
  "v_mfma_f32_16x16x32_bf16 v[240:243], v[212:215], v[100:103], v[240:243]\n\t" \
  "ds_read_b64_tr_b16 v[202:203], v29 offset:47616\n\t" \
  "v_mfma_f32_16x16x32_bf16 v[244:247], v[212:215], v[108:111], v[244:247]\n\t" \
  "ds_read_b64_tr_b16 v[204:205], v29 offset:51200\n\t" \
  "global_load_dwordx4 v[168:171], v53, s[90:91] offset:2048 sc1\n\t" \
  "v_mfma_f32_16x16x32_bf16 v[244:247], v[208:211], v[104:107], v[244:247]\n\t" \
  "ds_read_b64_tr_b16 v[206:207], v29 offset:51712\n\t" \
  "v_mfma_f32_16x16x32_bf16 v[248:251], v[216:219], v[96:99], v[248:251]\n\t" \
  "ds_read_b64_tr_b16 v[208:209], v30 offset:47104\n\t" \
  "s_waitcnt lgkmcnt(6)\n\t" \
  "v_mfma_f32_16x16x32_bf16 v[248:251], v[220:223], v[100:103], v[248:251]\n\t" \
  "ds_read_b64_tr_b16 v[210:211], v30 offset:47616\n\t" \
  "v_mfma_f32_16x16x32_bf16 v[252:255], v[220:223], v[108:111], v[252:255]\n\t" \
  "ds_read_b64_tr_b16 v[212:213], v30 offset:51200\n\t" \
  "global_load_dwordx4 v[172:175], v53, s[90:91] offset:3072 sc1\n\t" \
  "v_readfirstlane_b32 vcc_lo, v16\n\t" \
  "v_mfma_f32_16x16x32_bf16 v[252:255], v[216:219], v[104:107], v[252:255]\n\t" \
  "ds_read_b64_tr_b16 v[214:215], v30 offset:51712\n\t" \
  "s_cmp_ge_u32 vcc_lo, 2\n\t" \
  "s_cbranch_scc0 45f\n\t" \
  "46:\n\t" \
  "s_waitcnt vmcnt(14)\n\t" \
  "v_mfma_f32_16x16x32_bf16 v[224:227], v[192:195], v[112:115], v[224:227]\n\t" \
  "ds_read_b64_tr_b16 v[216:217], v31 offset:47104\n\t" \
  "s_lshr_b64 s[90:91], %18, 28\n\t" \
  "s_and_b32 s90, s90, 15\n\t" \
  "s_lshl_b32 s90, s90, 15\n\t" \
  "s_waitcnt lgkmcnt(6)\n\t" \
  "v_mfma_f32_16x16x32_bf16 v[224:227], v[196:199], v[116:119], v[224:227]\n\t" \
  "ds_read_b64_tr_b16 v[218:219], v31 offset:47616\n\t" \
  "s_add_u32 s90, s90, s92\n\t" \
  "s_addc_u32 s91, s93, 0\n\t" \
  "s_waitcnt vmcnt(12)\n\t" \
  "v_mfma_f32_16x16x32_bf16 v[228:231], v[196:199], v[124:127], v[228:231]\n\t" \
  "ds_read_b64_tr_b16 v[220:221], v31 offset:51200\n\t" \
  "global_load_dwordx4 v[176:179], v53, s[90:91] offset:0 sc1\n\t" \
  "v_mfma_f32_16x16x32_bf16 v[228:231], v[192:195], v[120:123], v[228:231]\n\t" \
  "ds_read_b64_tr_b16 v[222:223], v31 offset:51712\n\t" \
  "v_mfma_f32_16x16x32_bf16 v[232:235], v[200:203], v[112:115], v[232:235]\n\t" \
  "ds_read_b64_tr_b16 v[192:193], v28 offset:55296\n\t" \
  "s_waitcnt lgkmcnt(6)\n\t" \
  "v_mfma_f32_16x16x32_bf16 v[232:235], v[204:207], v[116:119], v[232:235]\n\t" \
  "ds_read_b64_tr_b16 v[194:195], v28 offset:55808\n\t" \
  "v_mfma_f32_16x16x32_bf16 v[236:239], v[204:207], v[124:127], v[236:239]\n\t" \
  "ds_read_b64_tr_b16 v[196:197], v28 offset:59392\n\t" \
  "global_load_dwordx4 v[180:183], v53, s[90:91] offset:1024 sc1\n\t" \
  "v_mfma_f32_16x16x32_bf16 v[236:239], v[200:203], v[120:123], v[236:239]\n\t" \
  "ds_read_b64_tr_b16 v[198:199], v28 offset:59904\n\t" \
  "v_mfma_f32_16x16x32_bf16 v[240:243], v[208:211], v[112:115], v[240:243]\n\t" \
  "ds_read_b64_tr_b16 v[200:201], v29 offset:55296\n\t" \
  "s_waitcnt lgkmcnt(6)\n\t" \
  "v_mfma_f32_16x16x32_bf16 v[240:243], v[212:215], v[116:119], v[240:243]\n\t" \
  "ds_read_b64_tr_b16 v[202:203], v29 offset:55808\n\t" \
  "v_mfma_f32_16x16x32_bf16 v[244:247], v[212:215], v[124:127], v[244:247]\n\t" \
  "ds_read_b64_tr_b16 v[204:205], v29 offset:59392\n\t" \
  "global_load_dwordx4 v[184:187], v53, s[90:91] offset:2048 sc1\n\t" \
  "v_mfma_f32_16x16x32_bf16 v[244:247], v[208:211], v[120:123], v[244:247]\n\t" \
  "ds_read_b64_tr_b16 v[206:207], v29 offset:59904\n\t" \
  "v_mfma_f32_16x16x32_bf16 v[248:251], v[216:219], v[112:115], v[248:251]\n\t" \
  "ds_read_b64_tr_b16 v[208:209], v30 offset:55296\n\t" \
  "s_waitcnt lgkmcnt(6)\n\t" \
  "v_mfma_f32_16x16x32_bf16 v[248:251], v[220:223], v[116:119], v[248:251]\n\t" \
  "ds_read_b64_tr_b16 v[210:211], v30 offset:55808\n\t" \
  "v_mfma_f32_16x16x32_bf16 v[252:255], v[220:223], v[124:127], v[252:255]\n\t" \
  "ds_read_b64_tr_b16 v[212:213], v30 offset:59392\n\t" \
  "global_load_dwordx4 v[188:191], v53, s[90:91] offset:3072 sc1\n\t" \
  "v_mfma_f32_16x16x32_bf16 v[252:255], v[216:219], v[120:123], v[252:255]\n\t" \
  "ds_read_b64_tr_b16 v[214:215], v30 offset:59904\n\t" \
  "s_waitcnt vmcnt(14)\n\t" \
  "v_mfma_f32_16x16x32_bf16 v[224:227], v[192:195], v[128:131], v[224:227]\n\t" \
  "ds_read_b64_tr_b16 v[216:217], v31 offset:55296\n\t" \
  "s_lshr_b64 s[90:91], %18, 32\n\t" \
  "s_and_b32 s90, s90, 15\n\t" \
  "s_lshl_b32 s90, s90, 15\n\t" \
  "s_waitcnt lgkmcnt(6)\n\t" \
  "v_mfma_f32_16x16x32_bf16 v[224:227], v[196:199], v[132:135], v[224:227]\n\t" \
  "ds_read_b64_tr_b16 v[218:219], v31 offset:55808\n\t" \
  "s_add_u32 s90, s90, s92\n\t" \
  "s_addc_u32 s91, s93, 0\n\t" \
  "s_waitcnt vmcnt(12)\n\t" \
  "v_mfma_f32_16x16x32_bf16 v[228:231], v[196:199], v[140:143], v[228:231]\n\t" \
  "ds_read_b64_tr_b16 v[220:221], v31 offset:59392\n\t" \
  "global_load_dwordx4 v[64:67], v53, s[90:91] offset:0 sc1\n\t" \
  "v_mfma_f32_16x16x32_bf16 v[228:231], v[192:195], v[136:139], v[228:231]\n\t" \
  "ds_read_b64_tr_b16 v[222:223], v31 offset:59904\n\t" \
  "v_mfma_f32_16x16x32_bf16 v[232:235], v[200:203], v[128:131], v[232:235]\n\t" \
  "ds_read_b64_tr_b16 v[192:193], v28 offset:63488\n\t" \
  "s_waitcnt lgkmcnt(6)\n\t" \
  "v_mfma_f32_16x16x32_bf16 v[232:235], v[204:207], v[132:135], v[232:235]\n\t" \
  "ds_read_b64_tr_b16 v[194:195], v28 offset:64000\n\t" \
  "v_mfma_f32_16x16x32_bf16 v[236:239], v[204:207], v[140:143], v[236:239]\n\t" \
  "ds_read_b64_tr_b16 v[196:197], v32 offset:2048\n\t" \
  "global_load_dwordx4 v[68:71], v53, s[90:91] offset:1024 sc1\n\t" \
  "v_mfma_f32_16x16x32_bf16 v[236:239], v[200:203], v[136:139], v[236:239]\n\t" \
  "ds_read_b64_tr_b16 v[198:199], v32 offset:2560\n\t" \
  "v_mfma_f32_16x16x32_bf16 v[240:243], v[208:211], v[128:131], v[240:243]\n\t" \
  "ds_read_b64_tr_b16 v[200:201], v29 offset:63488\n\t" \
  "s_waitcnt lgkmcnt(6)\n\t" \
  "v_mfma_f32_16x16x32_bf16 v[240:243], v[212:215], v[132:135], v[240:243]\n\t" \
  "ds_read_b64_tr_b16 v[202:203], v29 offset:64000\n\t" \
  "v_mfma_f32_16x16x32_bf16 v[244:247], v[212:215], v[140:143], v[244:247]\n\t" \
  "ds_read_b64_tr_b16 v[204:205], v33 offset:2048\n\t" \
  "global_load_dwordx4 v[72:75], v53, s[90:91] offset:2048 sc1\n\t" \
  "v_mfma_f32_16x16x32_bf16 v[244:247], v[208:211], v[136:139], v[244:247]\n\t" \
  "ds_read_b64_tr_b16 v[206:207], v33 offset:2560\n\t" \
  "v_mfma_f32_16x16x32_bf16 v[248:251], v[216:219], v[128:131], v[248:251]\n\t" \
  "ds_read_b64_tr_b16 v[208:209], v30 offset:63488\n\t" \
  "s_waitcnt lgkmcnt(6)\n\t" \
  "v_mfma_f32_16x16x32_bf16 v[248:251], v[220:223], v[132:135], v[248:251]\n\t" \
  "ds_read_b64_tr_b16 v[210:211], v30 offset:64000\n\t" \
  "v_mfma_f32_16x16x32_bf16 v[252:255], v[220:223], v[140:143], v[252:255]\n\t" \
  "ds_read_b64_tr_b16 v[212:213], v34 offset:2048\n\t" \
  "global_load_dwordx4 v[76:79], v53, s[90:91] offset:3072 sc1\n\t" \
  "v_mfma_f32_16x16x32_bf16 v[252:255], v[216:219], v[136:139], v[252:255]\n\t" \
  "ds_read_b64_tr_b16 v[214:215], v34 offset:2560\n\t" \
  "s_waitcnt vmcnt(14)\n\t" \
  "v_mfma_f32_16x16x32_bf16 v[224:227], v[192:195], v[144:147], v[224:227]\n\t" \
  "ds_read_b64_tr_b16 v[216:217], v31 offset:63488\n\t" \
  "s_lshr_b64 s[90:91], %18, 36\n\t" \
  "s_and_b32 s90, s90, 15\n\t" \
  "s_lshl_b32 s90, s90, 15\n\t" \
  "s_waitcnt lgkmcnt(6)\n\t" \
  "v_mfma_f32_16x16x32_bf16 v[224:227], v[196:199], v[148:151], v[224:227]\n\t" \
  "ds_read_b64_tr_b16 v[218:219], v31 offset:64000\n\t" \
  "s_add_u32 s90, s90, s92\n\t" \
  "s_addc_u32 s91, s93, 0\n\t" \
  "s_waitcnt vmcnt(12)\n\t" \
  "v_mfma_f32_16x16x32_bf16 v[228:231], v[196:199], v[156:159], v[228:231]\n\t" \
  "ds_read_b64_tr_b16 v[220:221], v35 offset:2048\n\t" \
  "global_load_dwordx4 v[80:83], v53, s[90:91] offset:0 sc1\n\t" \
  "v_mfma_f32_16x16x32_bf16 v[228:231], v[192:195], v[152:155], v[228:231]\n\t" \
  "ds_read_b64_tr_b16 v[222:223], v35 offset:2560\n\t" \
  "v_mfma_f32_16x16x32_bf16 v[232:235], v[200:203], v[144:147], v[232:235]\n\t" \
  "ds_read_b64_tr_b16 v[192:193], v32 offset:6144\n\t" \
  "s_waitcnt lgkmcnt(6)\n\t" \
  "v_mfma_f32_16x16x32_bf16 v[232:235], v[204:207], v[148:151], v[232:235]\n\t" \
  "ds_read_b64_tr_b16 v[194:195], v32 offset:6656\n\t" \
  "v_mfma_f32_16x16x32_bf16 v[236:239], v[204:207], v[156:159], v[236:239]\n\t" \
  "ds_read_b64_tr_b16 v[196:197], v32 offset:10240\n\t" \
  "global_load_dwordx4 v[84:87], v53, s[90:91] offset:1024 sc1\n\t" \
  "v_mfma_f32_16x16x32_bf16 v[236:239], v[200:203], v[152:155], v[236:239]\n\t" \
  "ds_read_b64_tr_b16 v[198:199], v32 offset:10752\n\t" \
  "v_mfma_f32_16x16x32_bf16 v[240:243], v[208:211], v[144:147], v[240:243]\n\t" \
  "ds_read_b64_tr_b16 v[200:201], v33 offset:6144\n\t" \
  "s_waitcnt lgkmcnt(6)\n\t" \
  "v_mfma_f32_16x16x32_bf16 v[240:243], v[212:215], v[148:151], v[240:243]\n\t" \
  "ds_read_b128 v[40:43], v56 offset:80\n\t" \
  "ds_read_b64_tr_b16 v[202:203], v33 offset:6656\n\t" \
  "v_mfma_f32_16x16x32_bf16 v[244:247], v[212:215], v[156:159], v[244:247]\n\t" \
  "ds_read_b64_tr_b16 v[204:205], v33 offset:10240\n\t" \
  "global_load_dwordx4 v[88:91], v53, s[90:91] offset:2048 sc1\n\t" \
  "v_mfma_f32_16x16x32_bf16 v[244:247], v[208:211], v[152:155], v[244:247]\n\t" \
  "ds_read_b64_tr_b16 v[206:207], v33 offset:10752\n\t" \
  "v_mfma_f32_16x16x32_bf16 v[248:251], v[216:219], v[144:147], v[248:251]\n\t" \
  "ds_read_b64_tr_b16 v[208:209], v34 offset:6144\n\t" \
  "s_waitcnt lgkmcnt(7)\n\t" \
  "v_mfma_f32_16x16x32_bf16 v[248:251], v[220:223], v[148:151], v[248:251]\n\t" \
  "ds_read_b64_tr_b16 v[210:211], v34 offset:6656\n\t" \
  "v_mfma_f32_16x16x32_bf16 v[252:255], v[220:223], v[156:159], v[252:255]\n\t" \
  "ds_read_b64_tr_b16 v[212:213], v34 offset:10240\n\t" \
  "global_load_dwordx4 v[92:95], v53, s[90:91] offset:3072 sc1\n\t" \
  "v_mfma_f32_16x16x32_bf16 v[252:255], v[216:219], v[152:155], v[252:255]\n\t" \
  "ds_read_b64_tr_b16 v[214:215], v34 offset:10752\n\t" \
  "s_waitcnt vmcnt(14)\n\t" \
  "v_mfma_f32_16x16x32_bf16 v[224:227], v[192:195], v[160:163], v[224:227]\n\t" \
  "s_waitcnt lgkmcnt(7)\n\t" \
  "v_min_u32 v40, v40, v41\n\t" \
  "v_min3_u32 v40, v40, v42, v43\n\t" \
  "ds_read_b64_tr_b16 v[216:217], v35 offset:6144\n\t" \
  "s_lshr_b64 s[90:91], %18, 40\n\t" \
  "s_and_b32 s90, s90, 15\n\t" \
  "s_lshl_b32 s90, s90, 15\n\t" \
  "v_mfma_f32_16x16x32_bf16 v[224:227], v[196:199], v[164:167], v[224:227]\n\t" \
  "v_readfirstlane_b32 vcc_lo, v40\n\t" \
  "ds_read_b64_tr_b16 v[218:219], v35 offset:6656\n\t" \
  "s_add_u32 s90, s90, s92\n\t" \
  "s_addc_u32 s91, s93, 0\n\t" \
  "s_waitcnt vmcnt(12)\n\t" \
  "v_mfma_f32_16x16x32_bf16 v[228:231], v[196:199], v[172:175], v[228:231]\n\t" \
  "s_cmp_ge_u32 vcc_lo, 13\n\t" \
  "s_cbranch_scc0 34f\n\t" \
  "35:\n\t" \
  "ds_read_b64_tr_b16 v[220:221], v35 offset:10240\n\t" \
  "global_load_dwordx4 v[96:99], v53, s[90:91] offset:0 sc1\n\t" \
  "v_mfma_f32_16x16x32_bf16 v[228:231], v[192:195], v[168:171], v[228:231]\n\t" \
  "ds_read_b64_tr_b16 v[222:223], v35 offset:10752\n\t" \
  "s_waitcnt lgkmcnt(6)\n\t" \
  "v_mfma_f32_16x16x32_bf16 v[232:235], v[200:203], v[160:163], v[232:235]\n\t" \
  "ds_read_b64_tr_b16 v[192:193], v32 offset:14336\n\t" \
  "v_mfma_f32_16x16x32_bf16 v[232:235], v[204:207], v[164:167], v[232:235]\n\t" \
  "ds_read_b64_tr_b16 v[194:195], v32 offset:14848\n\t" \
  "v_mfma_f32_16x16x32_bf16 v[236:239], v[204:207], v[172:175], v[236:239]\n\t" \
  "ds_read_b64_tr_b16 v[196:197], v32 offset:18432\n\t" \
  "global_load_dwordx4 v[100:103], v53, s[90:91] offset:1024 sc1\n\t" \
  "v_mfma_f32_16x16x32_bf16 v[236:239], v[200:203], v[168:171], v[236:239]\n\t" \
  "ds_read_b64_tr_b16 v[198:199], v32 offset:18944\n\t" \
  "v_mfma_f32_16x16x32_bf16 v[240:243], v[208:211], v[160:163], v[240:243]\n\t" \
  "ds_read_b64_tr_b16 v[200:201], v33 offset:14336\n\t" \
  "s_waitcnt lgkmcnt(6)\n\t" \
  "v_mfma_f32_16x16x32_bf16 v[240:243], v[212:215], v[164:167], v[240:243]\n\t" \
  "ds_read_b64_tr_b16 v[202:203], v33 offset:14848\n\t" \
  "v_mfma_f32_16x16x32_bf16 v[244:247], v[212:215], v[172:175], v[244:247]\n\t" \
  "ds_read_b64_tr_b16 v[204:205], v33 offset:18432\n\t" \
  "global_load_dwordx4 v[104:107], v53, s[90:91] offset:2048 sc1\n\t" \
  "v_mfma_f32_16x16x32_bf16 v[244:247], v[208:211], v[168:171], v[244:247]\n\t" \
  "ds_read_b64_tr_b16 v[206:207], v33 offset:18944\n\t" \
  "v_mfma_f32_16x16x32_bf16 v[248:251], v[216:219], v[160:163], v[248:251]\n\t" \
  "ds_read_b64_tr_b16 v[208:209], v34 offset:14336\n\t" \
  "s_waitcnt lgkmcnt(6)\n\t" \
  "v_mfma_f32_16x16x32_bf16 v[248:251], v[220:223], v[164:167], v[248:251]\n\t" \
  "ds_read_b64_tr_b16 v[210:211], v34 offset:14848\n\t" \
  "v_mfma_f32_16x16x32_bf16 v[252:255], v[220:223], v[172:175], v[252:255]\n\t" \
  "ds_read_b64_tr_b16 v[212:213], v34 offset:18432\n\t" \
  "global_load_dwordx4 v[108:111], v53, s[90:91] offset:3072 sc1\n\t" \
  "v_mfma_f32_16x16x32_bf16 v[252:255], v[216:219], v[168:171], v[252:255]\n\t" \
  "ds_read_b64_tr_b16 v[214:215], v34 offset:18944\n\t" \
  "s_waitcnt vmcnt(14)\n\t" \
  "v_mfma_f32_16x16x32_bf16 v[224:227], v[192:195], v[176:179], v[224:227]\n\t" \
  "ds_read_b64_tr_b16 v[216:217], v35 offset:14336\n\t" \
  "s_lshr_b64 s[90:91], %18, 44\n\t" \
  "s_and_b32 s90, s90, 15\n\t" \
  "s_lshl_b32 s90, s90, 15\n\t" \
  "s_waitcnt lgkmcnt(6)\n\t" \
  "v_mfma_f32_16x16x32_bf16 v[224:227], v[196:199], v[180:183], v[224:227]\n\t" \
  "ds_read_b64_tr_b16 v[218:219], v35 offset:14848\n\t" \
  "s_add_u32 s90, s90, s92\n\t" \
  "s_addc_u32 s91, s93, 0\n\t" \
  "s_waitcnt vmcnt(12)\n\t" \
  "v_mfma_f32_16x16x32_bf16 v[228:231], v[196:199], v[188:191], v[228:231]\n\t" \
  "ds_read_b64_tr_b16 v[220:221], v35 offset:18432\n\t" \
  "global_load_dwordx4 v[112:115], v53, s[90:91] offset:0 sc1\n\t" \
  "v_mfma_f32_16x16x32_bf16 v[228:231], v[192:195], v[184:187], v[228:231]\n\t" \
  "ds_read_b64_tr_b16 v[222:223], v35 offset:18944\n\t" \
  "v_mfma_f32_16x16x32_bf16 v[232:235], v[200:203], v[176:179], v[232:235]\n\t" \
  "ds_read_b64_tr_b16 v[192:193], v32 offset:22528\n\t" \
  "s_waitcnt lgkmcnt(6)\n\t" \
  "v_mfma_f32_16x16x32_bf16 v[232:235], v[204:207], v[180:183], v[232:235]\n\t" \
  "ds_read_b64_tr_b16 v[194:195], v32 offset:23040\n\t" \
  "v_mfma_f32_16x16x32_bf16 v[236:239], v[204:207], v[188:191], v[236:239]\n\t" \
  "ds_read_b64_tr_b16 v[196:197], v32 offset:26624\n\t" \
  "global_load_dwordx4 v[116:119], v53, s[90:91] offset:1024 sc1\n\t" \
  "v_mfma_f32_16x16x32_bf16 v[236:239], v[200:203], v[184:187], v[236:239]\n\t" \
  "ds_read_b64_tr_b16 v[198:199], v32 offset:27136\n\t" \
  "v_mfma_f32_16x16x32_bf16 v[240:243], v[208:211], v[176:179], v[240:243]\n\t" \
  "ds_read_b64_tr_b16 v[200:201], v33 offset:22528\n\t" \
  "s_waitcnt lgkmcnt(6)\n\t" \
  "v_mfma_f32_16x16x32_bf16 v[240:243], v[212:215], v[180:183], v[240:243]\n\t" \
  "ds_read_b64_tr_b16 v[202:203], v33 offset:23040\n\t" \
  "v_mfma_f32_16x16x32_bf16 v[244:247], v[212:215], v[188:191], v[244:247]\n\t" \
  "ds_read_b64_tr_b16 v[204:205], v33 offset:26624\n\t" \
  "global_load_dwordx4 v[120:123], v53, s[90:91] offset:2048 sc1\n\t" \
  "v_mfma_f32_16x16x32_bf16 v[244:247], v[208:211], v[184:187], v[244:247]\n\t" \
  "ds_read_b64_tr_b16 v[206:207], v33 offset:27136\n\t" \
  "v_mfma_f32_16x16x32_bf16 v[248:251], v[216:219], v[176:179], v[248:251]\n\t" \
  "ds_read_b64_tr_b16 v[208:209], v34 offset:22528\n\t" \
  "s_waitcnt lgkmcnt(6)\n\t" \
  "v_mfma_f32_16x16x32_bf16 v[248:251], v[220:223], v[180:183], v[248:251]\n\t" \
  "ds_read_b64_tr_b16 v[210:211], v34 offset:23040\n\t" \
  "v_mfma_f32_16x16x32_bf16 v[252:255], v[220:223], v[188:191], v[252:255]\n\t" \
  "ds_read_b64_tr_b16 v[212:213], v34 offset:26624\n\t" \
  "global_load_dwordx4 v[124:127], v53, s[90:91] offset:3072 sc1\n\t" \
  "ds_write_b32 v56, v56 offset:108\n\t" \
  "v_mfma_f32_16x16x32_bf16 v[252:255], v[216:219], v[184:187], v[252:255]\n\t" \
  "ds_read_b64_tr_b16 v[214:215], v34 offset:27136\n\t" \
  "s_waitcnt vmcnt(14)\n\t" \
  "v_mfma_f32_16x16x32_bf16 v[224:227], v[192:195], v[64:67], v[224:227]\n\t" \
  "ds_read_b64_tr_b16 v[216:217], v35 offset:22528\n\t" \
  "s_lshr_b64 s[90:91], %18, 48\n\t" \
  "s_and_b32 s90, s90, 15\n\t" \
  "s_lshl_b32 s90, s90, 15\n\t" \
  "s_waitcnt lgkmcnt(7)\n\t" \
  "v_mfma_f32_16x16x32_bf16 v[224:227], v[196:199], v[68:71], v[224:227]\n\t" \
  "ds_read_b64_tr_b16 v[218:219], v35 offset:23040\n\t" \
  "s_add_u32 s90, s90, s92\n\t" \
  "s_addc_u32 s91, s93, 0\n\t" \
  "s_waitcnt vmcnt(12)\n\t" \
  "v_mfma_f32_16x16x32_bf16 v[228:231], v[196:199], v[76:79], v[228:231]\n\t" \
  "ds_read_b64_tr_b16 v[220:221], v35 offset:26624\n\t" \
  "global_load_dwordx4 v[128:131], v53, s[90:91] offset:0 sc1\n\t" \
  "v_mfma_f32_16x16x32_bf16 v[228:231], v[192:195], v[72:75], v[228:231]\n\t" \
  "ds_read_b64_tr_b16 v[222:223], v35 offset:27136\n\t" \
  "v_mfma_f32_16x16x32_bf16 v[232:235], v[200:203], v[64:67], v[232:235]\n\t" \
  "ds_read_b64_tr_b16 v[192:193], v32 offset:30720\n\t" \
  "s_waitcnt lgkmcnt(6)\n\t" \
  "v_mfma_f32_16x16x32_bf16 v[232:235], v[204:207], v[68:71], v[232:235]\n\t" \
  "ds_read_b64_tr_b16 v[194:195], v32 offset:31232\n\t" \
  "v_mfma_f32_16x16x32_bf16 v[236:239], v[204:207], v[76:79], v[236:239]\n\t" \
  "ds_read_b64_tr_b16 v[196:197], v32 offset:34816\n\t" \
  "global_load_dwordx4 v[132:135], v53, s[90:91] offset:1024 sc1\n\t" \
  "v_mfma_f32_16x16x32_bf16 v[236:239], v[200:203], v[72:75], v[236:239]\n\t" \
  "ds_read_b64_tr_b16 v[198:199], v32 offset:35328\n\t" \
  "v_mfma_f32_16x16x32_bf16 v[240:243], v[208:211], v[64:67], v[240:243]\n\t" \
  "ds_read_b64_tr_b16 v[200:201], v33 offset:30720\n\t" \
  "s_waitcnt lgkmcnt(6)\n\t" \
  "v_mfma_f32_16x16x32_bf16 v[240:243], v[212:215], v[68:71], v[240:243]\n\t" \
  "ds_read_b64_tr_b16 v[202:203], v33 offset:31232\n\t" \
  "v_mfma_f32_16x16x32_bf16 v[244:247], v[212:215], v[76:79], v[244:247]\n\t" \
  "ds_read_b64_tr_b16 v[204:205], v33 offset:34816\n\t" \
  "global_load_dwordx4 v[136:139], v53, s[90:91] offset:2048 sc1\n\t" \
  "v_mfma_f32_16x16x32_bf16 v[244:247], v[208:211], v[72:75], v[244:247]\n\t" \
  "ds_read_b64_tr_b16 v[206:207], v33 offset:35328\n\t" \
  "v_mfma_f32_16x16x32_bf16 v[248:251], v[216:219], v[64:67], v[248:251]\n\t" \
  "ds_read_b64_tr_b16 v[208:209], v34 offset:30720\n\t" \
  "s_waitcnt lgkmcnt(6)\n\t" \
  "v_mfma_f32_16x16x32_bf16 v[248:251], v[220:223], v[68:71], v[248:251]\n\t" \
  "ds_read_b64_tr_b16 v[210:211], v34 offset:31232\n\t" \
  "v_mfma_f32_16x16x32_bf16 v[252:255], v[220:223], v[76:79], v[252:255]\n\t" \
  "ds_read_b64_tr_b16 v[212:213], v34 offset:34816\n\t" \
  "global_load_dwordx4 v[140:143], v53, s[90:91] offset:3072 sc1\n\t" \
  "v_mfma_f32_16x16x32_bf16 v[252:255], v[216:219], v[72:75], v[252:255]\n\t" \
  "ds_read_b64_tr_b16 v[214:215], v34 offset:35328\n\t" \
  "s_waitcnt vmcnt(14)\n\t" \
  "v_mfma_f32_16x16x32_bf16 v[224:227], v[192:195], v[80:83], v[224:227]\n\t" \
  "ds_read_b64_tr_b16 v[216:217], v35 offset:30720\n\t" \
  "s_lshr_b64 s[90:91], %18, 52\n\t" \
  "s_and_b32 s90, s90, 15\n\t" \
  "s_lshl_b32 s90, s90, 15\n\t" \
  "s_waitcnt lgkmcnt(6)\n\t" \
  "v_mfma_f32_16x16x32_bf16 v[224:227], v[196:199], v[84:87], v[224:227]\n\t" \
  "ds_read_b64_tr_b16 v[218:219], v35 offset:31232\n\t" \
  "s_add_u32 s90, s90, s92\n\t" \
  "s_addc_u32 s91, s93, 0\n\t" \
  "s_waitcnt vmcnt(12)\n\t" \
  "v_mfma_f32_16x16x32_bf16 v[228:231], v[196:199], v[92:95], v[228:231]\n\t" \
  "ds_read_b64_tr_b16 v[220:221], v35 offset:34816\n\t" \
  "global_load_dwordx4 v[144:147], v53, s[90:91] offset:0 sc1\n\t" \
  "v_mfma_f32_16x16x32_bf16 v[228:231], v[192:195], v[88:91], v[228:231]\n\t" \
  "ds_read_b64_tr_b16 v[222:223], v35 offset:35328\n\t" \
  "v_mfma_f32_16x16x32_bf16 v[232:235], v[200:203], v[80:83], v[232:235]\n\t" \
  "ds_read_b64_tr_b16 v[192:193], v32 offset:38912\n\t" \
  "s_waitcnt lgkmcnt(6)\n\t" \
  "v_mfma_f32_16x16x32_bf16 v[232:235], v[204:207], v[84:87], v[232:235]\n\t" \
  "ds_read_b64_tr_b16 v[194:195], v32 offset:39424\n\t" \
  "v_mfma_f32_16x16x32_bf16 v[236:239], v[204:207], v[92:95], v[236:239]\n\t" \
  "ds_read_b64_tr_b16 v[196:197], v32 offset:43008\n\t" \
  "global_load_dwordx4 v[148:151], v53, s[90:91] offset:1024 sc1\n\t" \
  "v_mfma_f32_16x16x32_bf16 v[236:239], v[200:203], v[88:91], v[236:239]\n\t" \
  "ds_read_b64_tr_b16 v[198:199], v32 offset:43520\n\t" \
  "v_mfma_f32_16x16x32_bf16 v[240:243], v[208:211], v[80:83], v[240:243]\n\t" \
  "ds_read_b64_tr_b16 v[200:201], v33 offset:38912\n\t" \
  "s_waitcnt lgkmcnt(6)\n\t" \
  "v_mfma_f32_16x16x32_bf16 v[240:243], v[212:215], v[84:87], v[240:243]\n\t" \
  "ds_read_b64_tr_b16 v[202:203], v33 offset:39424\n\t" \
  "v_mfma_f32_16x16x32_bf16 v[244:247], v[212:215], v[92:95], v[244:247]\n\t" \
  "ds_read_b64_tr_b16 v[204:205], v33 offset:43008\n\t" \
  "global_load_dwordx4 v[152:155], v53, s[90:91] offset:2048 sc1\n\t" \
  "v_mfma_f32_16x16x32_bf16 v[244:247], v[208:211], v[88:91], v[244:247]\n\t" \
  "ds_read_b64_tr_b16 v[206:207], v33 offset:43520\n\t" \
  "v_mfma_f32_16x16x32_bf16 v[248:251], v[216:219], v[80:83], v[248:251]\n\t" \
  "ds_read_b64_tr_b16 v[208:209], v34 offset:38912\n\t" \
  "s_waitcnt lgkmcnt(6)\n\t" \
  "v_mfma_f32_16x16x32_bf16 v[248:251], v[220:223], v[84:87], v[248:251]\n\t" \
  "ds_read_b64_tr_b16 v[210:211], v34 offset:39424\n\t" \
  "v_mfma_f32_16x16x32_bf16 v[252:255], v[220:223], v[92:95], v[252:255]\n\t" \
  "ds_read_b64_tr_b16 v[212:213], v34 offset:43008\n\t" \
  "global_load_dwordx4 v[156:159], v53, s[90:91] offset:3072 sc1\n\t" \
  "v_mfma_f32_16x16x32_bf16 v[252:255], v[216:219], v[88:91], v[252:255]\n\t" \
  "ds_read_b64_tr_b16 v[214:215], v34 offset:43520\n\t" \
  "s_waitcnt vmcnt(14)\n\t" \
  "509:\n\t" \
  "s_cbranch_execz 26b\n\t" \
  "v_mfma_f32_16x16x32_bf16 v[224:227], v[192:195], v[96:99], v[224:227]\n\t" \
  "ds_read_b64_tr_b16 v[216:217], v35 offset:38912\n\t" \
  "s_lshr_b64 s[90:91], %18, 56\n\t" \
  "s_and_b32 s90, s90, 15\n\t" \
  "s_lshl_b32 s90, s90, 15\n\t" \
  "s_waitcnt lgkmcnt(6)\n\t" \
  "v_mfma_f32_16x16x32_bf16 v[224:227], v[196:199], v[100:103], v[224:227]\n\t" \
  "ds_read_b64_tr_b16 v[218:219], v35 offset:39424\n\t" \
  "s_add_u32 s90, s90, s92\n\t" \
  "s_addc_u32 s91, s93, 0\n\t" \
  "s_waitcnt vmcnt(12)\n\t" \
  "v_mfma_f32_16x16x32_bf16 v[228:231], v[196:199], v[108:111], v[228:231]\n\t" \
  "ds_read_b64_tr_b16 v[220:221], v35 offset:43008\n\t" \
  "global_load_dwordx4 v[160:163], v53, s[90:91] offset:0 sc1\n\t" \
  "v_mfma_f32_16x16x32_bf16 v[228:231], v[192:195], v[104:107], v[228:231]\n\t" \
  "ds_read_b64_tr_b16 v[222:223], v35 offset:43520\n\t" \
  "v_mfma_f32_16x16x32_bf16 v[232:235], v[200:203], v[96:99], v[232:235]\n\t" \
  "ds_read_b64_tr_b16 v[192:193], v32 offset:47104\n\t" \
  "s_waitcnt lgkmcnt(6)\n\t" \
  "v_mfma_f32_16x16x32_bf16 v[232:235], v[204:207], v[100:103], v[232:235]\n\t" \
  "ds_read_b64_tr_b16 v[194:195], v32 offset:47616\n\t" \
  "v_mfma_f32_16x16x32_bf16 v[236:239], v[204:207], v[108:111], v[236:239]\n\t" \
  "ds_read_b64_tr_b16 v[196:197], v32 offset:51200\n\t" \
  "global_load_dwordx4 v[164:167], v53, s[90:91] offset:1024 sc1\n\t" \
  "v_mfma_f32_16x16x32_bf16 v[236:239], v[200:203], v[104:107], v[236:239]\n\t" \
  "ds_read_b64_tr_b16 v[198:199], v32 offset:51712\n\t" \
  "v_mfma_f32_16x16x32_bf16 v[240:243], v[208:211], v[96:99], v[240:243]\n\t" \
  "ds_read_b64_tr_b16 v[200:201], v33 offset:47104\n\t" \
  "s_waitcnt lgkmcnt(6)\n\t" \
  "v_mfma_f32_16x16x32_bf16 v[240:243], v[212:215], v[100:103], v[240:243]\n\t" \
  "ds_read_b64_tr_b16 v[202:203], v33 offset:47616\n\t" \
  "v_mfma_f32_16x16x32_bf16 v[244:247], v[212:215], v[108:111], v[244:247]\n\t" \
  "ds_read_b64_tr_b16 v[204:205], v33 offset:51200\n\t" \
  "global_load_dwordx4 v[168:171], v53, s[90:91] offset:2048 sc1\n\t" \
  "v_mfma_f32_16x16x32_bf16 v[244:247], v[208:211], v[104:107], v[244:247]\n\t" \
  "ds_read_b64_tr_b16 v[206:207], v33 offset:51712\n\t" \
  "v_mfma_f32_16x16x32_bf16 v[248:251], v[216:219], v[96:99], v[248:251]\n\t" \
  "ds_read_b64_tr_b16 v[208:209], v34 offset:47104\n\t" \
  "s_waitcnt lgkmcnt(6)\n\t" \
  "v_mfma_f32_16x16x32_bf16 v[248:251], v[220:223], v[100:103], v[248:251]\n\t" \
  "ds_read_b64_tr_b16 v[210:211], v34 offset:47616\n\t" \
  "v_mfma_f32_16x16x32_bf16 v[252:255], v[220:223], v[108:111], v[252:255]\n\t" \
  "ds_read_b64_tr_b16 v[212:213], v34 offset:51200\n\t" \
  "global_load_dwordx4 v[172:175], v53, s[90:91] offset:3072 sc1\n\t" \
  "v_mfma_f32_16x16x32_bf16 v[252:255], v[216:219], v[104:107], v[252:255]\n\t" \
  "ds_read_b64_tr_b16 v[214:215], v34 offset:51712\n\t" \
  "s_waitcnt vmcnt(14)\n\t" \
  "v_mfma_f32_16x16x32_bf16 v[224:227], v[192:195], v[112:115], v[224:227]\n\t" \
  "ds_read_b64_tr_b16 v[216:217], v35 offset:47104\n\t" \
  "s_lshr_b64 s[90:91], %18, 60\n\t" \
  "s_and_b32 s90, s90, 15\n\t" \
  "s_lshl_b32 s90, s90, 15\n\t" \
  "s_waitcnt lgkmcnt(6)\n\t" \
  "v_mfma_f32_16x16x32_bf16 v[224:227], v[196:199], v[116:119], v[224:227]\n\t" \
  "ds_read_b64_tr_b16 v[218:219], v35 offset:47616\n\t" \
  "s_add_u32 s90, s90, s92\n\t" \
  "s_addc_u32 s91, s93, 0\n\t" \
  "s_waitcnt vmcnt(12)\n\t" \
  "v_mfma_f32_16x16x32_bf16 v[228:231], v[196:199], v[124:127], v[228:231]\n\t" \
  "ds_read_b64_tr_b16 v[220:221], v35 offset:51200\n\t" \
  "global_load_dwordx4 v[176:179], v53, s[90:91] offset:0 sc1\n\t" \
  "v_mfma_f32_16x16x32_bf16 v[228:231], v[192:195], v[120:123], v[228:231]\n\t" \
  "ds_read_b64_tr_b16 v[222:223], v35 offset:51712\n\t" \
  "v_mfma_f32_16x16x32_bf16 v[232:235], v[200:203], v[112:115], v[232:235]\n\t" \
  "ds_read_b64_tr_b16 v[192:193], v32 offset:55296\n\t" \
  "s_waitcnt lgkmcnt(6)\n\t" \
  "v_mfma_f32_16x16x32_bf16 v[232:235], v[204:207], v[116:119], v[232:235]\n\t" \
  "ds_read_b64_tr_b16 v[194:195], v32 offset:55808\n\t" \
  "v_mfma_f32_16x16x32_bf16 v[236:239], v[204:207], v[124:127], v[236:239]\n\t" \
  "ds_read_b64_tr_b16 v[196:197], v32 offset:59392\n\t" \
  "global_load_dwordx4 v[180:183], v53, s[90:91] offset:1024 sc1\n\t" \
  "v_mfma_f32_16x16x32_bf16 v[236:239], v[200:203], v[120:123], v[236:239]\n\t" \
  "ds_read_b64_tr_b16 v[198:199], v32 offset:59904\n\t" \
  "v_mfma_f32_16x16x32_bf16 v[240:243], v[208:211], v[112:115], v[240:243]\n\t" \
  "ds_read_b64_tr_b16 v[200:201], v33 offset:55296\n\t" \
  "s_waitcnt lgkmcnt(6)\n\t" \
  "v_mfma_f32_16x16x32_bf16 v[240:243], v[212:215], v[116:119], v[240:243]\n\t" \
  "ds_read_b128 v[40:43], v56 offset:80\n\t" \
  "ds_read_b64_tr_b16 v[202:203], v33 offset:55808\n\t" \
  "v_mfma_f32_16x16x32_bf16 v[244:247], v[212:215], v[124:127], v[244:247]\n\t" \
  "ds_read_b64_tr_b16 v[204:205], v33 offset:59392\n\t" \
  "global_load_dwordx4 v[184:187], v53, s[90:91] offset:2048 sc1\n\t" \
  "v_mfma_f32_16x16x32_bf16 v[244:247], v[208:211], v[120:123], v[244:247]\n\t" \
  "ds_read_b64_tr_b16 v[206:207], v33 offset:59904\n\t" \
  "v_mfma_f32_16x16x32_bf16 v[248:251], v[216:219], v[112:115], v[248:251]\n\t" \
  "ds_read_b64_tr_b16 v[208:209], v34 offset:55296\n\t" \
  "s_waitcnt lgkmcnt(7)\n\t" \
  "v_mfma_f32_16x16x32_bf16 v[248:251], v[220:223], v[116:119], v[248:251]\n\t" \
  "ds_read_b64_tr_b16 v[210:211], v34 offset:55808\n\t" \
  "v_mfma_f32_16x16x32_bf16 v[252:255], v[220:223], v[124:127], v[252:255]\n\t" \
  "ds_read_b64_tr_b16 v[212:213], v34 offset:59392\n\t" \
  "global_load_dwordx4 v[188:191], v53, s[90:91] offset:3072 sc1\n\t" \
  "v_mfma_f32_16x16x32_bf16 v[252:255], v[216:219], v[120:123], v[252:255]\n\t" \
  "ds_read_b64_tr_b16 v[214:215], v34 offset:59904\n\t" \
  "s_waitcnt vmcnt(14)\n\t" \
  "v_mfma_f32_16x16x32_bf16 v[224:227], v[192:195], v[128:131], v[224:227]\n\t" \
  "s_waitcnt lgkmcnt(7)\n\t" \
  "v_min_u32 v40, v40, v41\n\t" \
  "v_min3_u32 v40, v40, v42, v43\n\t" \
  "ds_read_b64_tr_b16 v[216:217], v35 offset:55296\n\t" \
  "v_mfma_f32_16x16x32_bf16 v[224:227], v[196:199], v[132:135], v[224:227]\n\t" \
  "v_readfirstlane_b32 vcc_lo, v40\n\t" \
  "ds_read_b64_tr_b16 v[218:219], v35 offset:55808\n\t" \
  "s_waitcnt vmcnt(12)\n\t" \
  "v_mfma_f32_16x16x32_bf16 v[228:231], v[196:199], v[140:143], v[228:231]\n\t" \
  "s_cmp_ge_u32 vcc_lo, 16\n\t" \
  "s_cbranch_scc0 36f\n\t" \
  "37:\n\t" \
  "ds_read_b64_tr_b16 v[220:221], v35 offset:59392\n\t" \
  "v_mfma_f32_16x16x32_bf16 v[228:231], v[192:195], v[136:139], v[228:231]\n\t" \
  "ds_read_b64_tr_b16 v[222:223], v35 offset:59904\n\t" \
  "s_waitcnt lgkmcnt(6)\n\t" \
  "v_mfma_f32_16x16x32_bf16 v[232:235], v[200:203], v[128:131], v[232:235]\n\t" \
  "ds_read_b64_tr_b16 v[192:193], v36 offset:2304\n\t" \
  "v_mfma_f32_16x16x32_bf16 v[232:235], v[204:207], v[132:135], v[232:235]\n\t" \
  "ds_read_b64_tr_b16 v[194:195], v36 offset:2816\n\t" \
  "v_mfma_f32_16x16x32_bf16 v[236:239], v[204:207], v[140:143], v[236:239]\n\t" \
  "ds_read_b64_tr_b16 v[196:197], v36 offset:10752\n\t" \
  "v_mfma_f32_16x16x32_bf16 v[236:239], v[200:203], v[136:139], v[236:239]\n\t" \
  "ds_read_b64_tr_b16 v[198:199], v36 offset:11264\n\t" \
  "v_mfma_f32_16x16x32_bf16 v[240:243], v[208:211], v[128:131], v[240:243]\n\t" \
  "ds_read_b64_tr_b16 v[200:201], v37 offset:2304\n\t" \
  "s_waitcnt lgkmcnt(6)\n\t" \
  "v_mfma_f32_16x16x32_bf16 v[240:243], v[212:215], v[132:135], v[240:243]\n\t" \
  "ds_read_b64_tr_b16 v[202:203], v37 offset:2816\n\t" \
  "v_mfma_f32_16x16x32_bf16 v[244:247], v[212:215], v[140:143], v[244:247]\n\t" \
  "ds_read_b64_tr_b16 v[204:205], v37 offset:10752\n\t" \
  "v_mfma_f32_16x16x32_bf16 v[244:247], v[208:211], v[136:139], v[244:247]\n\t" \
  "ds_read_b64_tr_b16 v[206:207], v37 offset:11264\n\t" \
  "v_mfma_f32_16x16x32_bf16 v[248:251], v[216:219], v[128:131], v[248:251]\n\t" \
  "ds_read_b64_tr_b16 v[208:209], v38 offset:2304\n\t" \
  "s_waitcnt lgkmcnt(6)\n\t" \
  "v_mfma_f32_16x16x32_bf16 v[248:251], v[220:223], v[132:135], v[248:251]\n\t" \
  "ds_read_b64_tr_b16 v[210:211], v38 offset:2816\n\t" \
  "v_mfma_f32_16x16x32_bf16 v[252:255], v[220:223], v[140:143], v[252:255]\n\t" \
  "ds_read_b64_tr_b16 v[212:213], v38 offset:10752\n\t" \
  "v_mfma_f32_16x16x32_bf16 v[252:255], v[216:219], v[136:139], v[252:255]\n\t" \
  "ds_read_b64_tr_b16 v[214:215], v38 offset:11264\n\t" \
  "s_waitcnt vmcnt(10)\n\t" \
  "v_mfma_f32_16x16x32_bf16 v[224:227], v[192:195], v[144:147], v[224:227]\n\t" \
  "ds_read_b64_tr_b16 v[216:217], v39 offset:2304\n\t" \
  "s_waitcnt lgkmcnt(6)\n\t" \
  "v_mfma_f32_16x16x32_bf16 v[224:227], v[196:199], v[148:151], v[224:227]\n\t" \
  "ds_read_b64_tr_b16 v[218:219], v39 offset:2816\n\t" \
  "s_waitcnt vmcnt(8)\n\t" \
  "v_mfma_f32_16x16x32_bf16 v[228:231], v[196:199], v[156:159], v[228:231]\n\t" \
  "ds_read_b64_tr_b16 v[220:221], v39 offset:10752\n\t" \
  "v_mfma_f32_16x16x32_bf16 v[228:231], v[192:195], v[152:155], v[228:231]\n\t" \
  "ds_read_b64_tr_b16 v[222:223], v39 offset:11264\n\t" \
  "v_mfma_f32_16x16x32_bf16 v[232:235], v[200:203], v[144:147], v[232:235]\n\t" \
  "ds_read_b64_tr_b16 v[192:193], v36 offset:19200\n\t" \
  "s_waitcnt lgkmcnt(6)\n\t" \
  "v_mfma_f32_16x16x32_bf16 v[232:235], v[204:207], v[148:151], v[232:235]\n\t" \
  "ds_read_b64_tr_b16 v[194:195], v36 offset:19712\n\t" \
  "v_mfma_f32_16x16x32_bf16 v[236:239], v[204:207], v[156:159], v[236:239]\n\t" \
  "ds_read_b64_tr_b16 v[196:197], v36 offset:27648\n\t" \
  "v_mfma_f32_16x16x32_bf16 v[236:239], v[200:203], v[152:155], v[236:239]\n\t" \
  "ds_read_b64_tr_b16 v[198:199], v36 offset:28160\n\t" \
  "v_mfma_f32_16x16x32_bf16 v[240:243], v[208:211], v[144:147], v[240:243]\n\t" \
  "ds_read_b64_tr_b16 v[200:201], v37 offset:19200\n\t" \
  "s_waitcnt lgkmcnt(6)\n\t" \
  "v_mfma_f32_16x16x32_bf16 v[240:243], v[212:215], v[148:151], v[240:243]\n\t" \
  "ds_read_b128 v[40:43], v56 offset:160\n\t" \
  "ds_read_b64_tr_b16 v[202:203], v37 offset:19712\n\t" \
  "v_mfma_f32_16x16x32_bf16 v[244:247], v[212:215], v[156:159], v[244:247]\n\t" \
  "ds_read_b64_tr_b16 v[204:205], v37 offset:27648\n\t" \
  "v_mfma_f32_16x16x32_bf16 v[244:247], v[208:211], v[152:155], v[244:247]\n\t" \
  "ds_read_b64_tr_b16 v[206:207], v37 offset:28160\n\t" \
  "v_mfma_f32_16x16x32_bf16 v[248:251], v[216:219], v[144:147], v[248:251]\n\t" \
  "ds_read_b64_tr_b16 v[208:209], v38 offset:19200\n\t" \
  "s_waitcnt lgkmcnt(7)\n\t" \
  "v_mfma_f32_16x16x32_bf16 v[248:251], v[220:223], v[148:151], v[248:251]\n\t" \
  "ds_read_b64_tr_b16 v[210:211], v38 offset:19712\n\t" \
  "v_mfma_f32_16x16x32_bf16 v[252:255], v[220:223], v[156:159], v[252:255]\n\t" \
  "ds_read_b64_tr_b16 v[212:213], v38 offset:27648\n\t" \
  "v_mfma_f32_16x16x32_bf16 v[252:255], v[216:219], v[152:155], v[252:255]\n\t" \
  "ds_read_b64_tr_b16 v[214:215], v38 offset:28160\n\t" \
  "s_waitcnt vmcnt(6)\n\t" \
  "v_mfma_f32_16x16x32_bf16 v[224:227], v[192:195], v[160:163], v[224:227]\n\t" \
  "s_waitcnt lgkmcnt(7)\n\t" \
  "v_min_u32 v40, v40, v41\n\t" \
  "v_min3_u32 v40, v40, v42, v43\n\t" \
  "ds_read_b64_tr_b16 v[216:217], v39 offset:19200\n\t" \
  "v_mfma_f32_16x16x32_bf16 v[224:227], v[196:199], v[164:167], v[224:227]\n\t" \
  "v_readfirstlane_b32 vcc_lo, v40\n\t" \
  "ds_read_b64_tr_b16 v[218:219], v39 offset:19712\n\t" \
  "s_waitcnt vmcnt(4)\n\t" \
  "v_mfma_f32_16x16x32_bf16 v[228:231], v[196:199], v[172:175], v[228:231]\n\t" \
  "s_cmp_ge_u32 vcc_lo, 16\n\t" \
  "s_cbranch_scc0 38f\n\t" \
  "39:\n\t" \
  "ds_read_b64_tr_b16 v[220:221], v39 offset:27648\n\t" \
  "v_mfma_f32_16x16x32_bf16 v[228:231], v[192:195], v[168:171], v[228:231]\n\t" \
  "ds_read_b64_tr_b16 v[222:223], v39 offset:28160\n\t" \
  "s_waitcnt lgkmcnt(6)\n\t" \
  "v_mfma_f32_16x16x32_bf16 v[232:235], v[200:203], v[160:163], v[232:235]\n\t" \
  "ds_read_b64_tr_b16 v[192:193], v28 offset:0\n\t" \
  "v_mfma_f32_16x16x32_bf16 v[232:235], v[204:207], v[164:167], v[232:235]\n\t" \
  "ds_read_b64_tr_b16 v[194:195], v28 offset:512\n\t" \
  "v_mfma_f32_16x16x32_bf16 v[236:239], v[204:207], v[172:175], v[236:239]\n\t" \
  "ds_read_b64_tr_b16 v[196:197], v28 offset:4096\n\t" \
  "v_mfma_f32_16x16x32_bf16 v[236:239], v[200:203], v[168:171], v[236:239]\n\t" \
  "ds_read_b64_tr_b16 v[198:199], v28 offset:4608\n\t" \
  "v_mfma_f32_16x16x32_bf16 v[240:243], v[208:211], v[160:163], v[240:243]\n\t" \
  "ds_read_b64_tr_b16 v[200:201], v29 offset:0\n\t" \
  "s_waitcnt lgkmcnt(6)\n\t" \
  "v_mfma_f32_16x16x32_bf16 v[240:243], v[212:215], v[164:167], v[240:243]\n\t" \
  "ds_read_b64_tr_b16 v[202:203], v29 offset:512\n\t" \
  "v_mfma_f32_16x16x32_bf16 v[244:247], v[212:215], v[172:175], v[244:247]\n\t" \
  "ds_read_b64_tr_b16 v[204:205], v29 offset:4096\n\t" \
  "v_mfma_f32_16x16x32_bf16 v[244:247], v[208:211], v[168:171], v[244:247]\n\t" \
  "ds_read_b64_tr_b16 v[206:207], v29 offset:4608\n\t" \
  "v_mfma_f32_16x16x32_bf16 v[248:251], v[216:219], v[160:163], v[248:251]\n\t" \
  "ds_read_b64_tr_b16 v[208:209], v30 offset:0\n\t" \
  "s_waitcnt lgkmcnt(6)\n\t" \
  "v_mfma_f32_16x16x32_bf16 v[248:251], v[220:223], v[164:167], v[248:251]\n\t" \
  "ds_read_b64_tr_b16 v[210:211], v30 offset:512\n\t" \
  "v_mfma_f32_16x16x32_bf16 v[252:255], v[220:223], v[172:175], v[252:255]\n\t" \
  "ds_read_b64_tr_b16 v[212:213], v30 offset:4096\n\t" \
  "v_mfma_f32_16x16x32_bf16 v[252:255], v[216:219], v[168:171], v[252:255]\n\t" \
  "ds_read_b64_tr_b16 v[214:215], v30 offset:4608\n\t" \
  "s_waitcnt vmcnt(2)\n\t" \
  "v_mfma_f32_16x16x32_bf16 v[224:227], v[192:195], v[176:179], v[224:227]\n\t" \
  "ds_read_b64_tr_b16 v[216:217], v31 offset:0\n\t" \
  "s_waitcnt lgkmcnt(6)\n\t" \
  "v_mfma_f32_16x16x32_bf16 v[224:227], v[196:199], v[180:183], v[224:227]\n\t" \
  "ds_read_b64_tr_b16 v[218:219], v31 offset:512\n\t" \
  "s_waitcnt vmcnt(0)\n\t" \
  "v_mfma_f32_16x16x32_bf16 v[228:231], v[196:199], v[188:191], v[228:231]\n\t" \
  "ds_read_b64_tr_b16 v[220:221], v31 offset:4096\n\t" \
  "v_mfma_f32_16x16x32_bf16 v[228:231], v[192:195], v[184:187], v[228:231]\n\t" \
  "ds_read_b64_tr_b16 v[222:223], v31 offset:4608\n\t" \
  "v_mfma_f32_16x16x32_bf16 v[232:235], v[200:203], v[176:179], v[232:235]\n\t" \
  "s_nop 0\n\t" \
  "v_cvt_pk_bf16_f32 v224, v224, v225\n\t" \
  "v_cvt_pk_bf16_f32 v225, v226, v227\n\t" \
  "s_waitcnt lgkmcnt(5)\n\t" \
  "v_mfma_f32_16x16x32_bf16 v[232:235], v[204:207], v[180:183], v[232:235]\n\t" \
  "ds_write_b64 v57, v[224:225] offset:0\n\t" \
  "v_mfma_f32_16x16x32_bf16 v[236:239], v[204:207], v[188:191], v[236:239]\n\t" \
  "v_cvt_pk_bf16_f32 v228, v228, v229\n\t" \
  "v_cvt_pk_bf16_f32 v229, v230, v231\n\t" \
  "v_mfma_f32_16x16x32_bf16 v[236:239], v[200:203], v[184:187], v[236:239]\n\t" \
  "ds_write_b64 v57, v[228:229] offset:2048\n\t" \
  "v_mfma_f32_16x16x32_bf16 v[240:243], v[208:211], v[176:179], v[240:243]\n\t" \
  "s_nop 0\n\t" \
  "v_cvt_pk_bf16_f32 v232, v232, v233\n\t" \
  "v_cvt_pk_bf16_f32 v233, v234, v235\n\t" \
  "s_waitcnt lgkmcnt(3)\n\t" \
  "v_mfma_f32_16x16x32_bf16 v[240:243], v[212:215], v[180:183], v[240:243]\n\t" \
  "ds_write_b64 v58, v[232:233] offset:0\n\t" \
  "s_waitcnt lgkmcnt(3)\n\t" \
  "v_mfma_f32_16x16x32_bf16 v[248:251], v[220:223], v[180:183], v[248:251]\n\t" \
  "v_cvt_pk_bf16_f32 v236, v236, v237\n\t" \
  "v_cvt_pk_bf16_f32 v237, v238, v239\n\t" \
  "v_mfma_f32_16x16x32_bf16 v[248:251], v[216:219], v[176:179], v[248:251]\n\t" \
  "ds_write_b64 v58, v[236:237] offset:2048\n\t" \
  "v_mfma_f32_16x16x32_bf16 v[244:247], v[208:211], v[184:187], v[244:247]\n\t" \
  "v_cvt_pk_bf16_f32 v240, v240, v241\n\t" \
  "v_cvt_pk_bf16_f32 v241, v242, v243\n\t" \
  "v_mfma_f32_16x16x32_bf16 v[244:247], v[212:215], v[188:191], v[244:247]\n\t" \
  "ds_write_b64 v59, v[240:241] offset:0\n\t" \
  "v_mfma_f32_16x16x32_bf16 v[252:255], v[220:223], v[188:191], v[252:255]\n\t" \
  "s_nop 0\n\t" \
  "v_cvt_pk_bf16_f32 v248, v248, v249\n\t" \
  "v_cvt_pk_bf16_f32 v249, v250, v251\n\t" \
  "v_mfma_f32_16x16x32_bf16 v[252:255], v[216:219], v[184:187], v[252:255]\n\t" \
  "ds_write_b64 v60, v[248:249] offset:0\n\t" \
  "ds_read_b128 v[64:67], v61 offset:0\n\t" \
  "ds_read_b128 v[68:71], v61 offset:1024\n\t" \
  "v_cvt_pk_bf16_f32 v244, v244, v245\n\t" \
  "v_cvt_pk_bf16_f32 v245, v246, v247\n\t" \
  "s_nop 2\n\t" \
  "v_cvt_pk_bf16_f32 v252, v252, v253\n\t" \
  "v_cvt_pk_bf16_f32 v253, v254, v255\n\t" \
  "ds_write_b64 v59, v[244:245] offset:2048\n\t" \
  "ds_write_b64 v60, v[252:253] offset:2048\n\t" \
  "ds_read_b128 v[72:75], v61 offset:2048\n\t" \
  "ds_read_b128 v[76:79], v61 offset:3072\n\t" \
  "s_waitcnt lgkmcnt(5)\n\t" \
  "global_store_dwordx4 v62, v[64:67], %22 offset:-4096" OUTPOL "\n\t" \
  "s_waitcnt lgkmcnt(4)\n\t" \
  "global_store_dwordx4 v63, v[68:71], %22 offset:-4096" OUTPOL "\n\t" \
  "s_waitcnt lgkmcnt(1)\n\t" \
  "global_store_dwordx4 v62, v[72:75], %22 offset:0" OUTPOL "\n\t" \
  "s_waitcnt lgkmcnt(0)\n\t" \
  "global_store_dwordx4 v63, v[76:79], %22 offset:0" OUTPOL "\n\t" \
  "s_branch 19f\n\t" \
  "23:\n\t" \
  "510:\n\t" \
  "s_cbranch_execz 26b\n\t" \
  "s_mov_b64 exec, 0xffff\n\t" \
  "global_store_dword v11, v55, %19 sc1\n\t" \
  "s_mov_b64 exec, -1\n\t" \
  "s_branch 22b\n\t" \
  "7:\n\t" \
  "s_sleep 1\n\t" \
  "ds_read_b128 v[60:63], v56 offset:64\n\t" \
  "ds_read_b64_tr_b16 v[108:109], v5 offset:0\n\t" \
  "ds_read_b64_tr_b16 v[110:111], v5 offset:16\n\t" \
  "ds_read_b128 v[112:115], v6 offset:16384\n\t" \
  "ds_read_b128 v[116:119], v6 offset:15360\n\t" \
  "ds_read_b64_tr_b16 v[120:121], v5 offset:512\n\t" \
  "ds_read_b64_tr_b16 v[122:123], v5 offset:528\n\t" \
  "ds_read_b128 v[124:127], v6 offset:14336\n\t" \
  "ds_read_b64_tr_b16 v[128:129], v5 offset:1024\n\t" \
  "ds_read_b64_tr_b16 v[130:131], v5 offset:1040\n\t" \
  "ds_read_b128 v[132:135], v6 offset:13312\n\t" \
  "ds_read_b64_tr_b16 v[136:137], v5 offset:1536\n\t" \
  "ds_read_b64_tr_b16 v[138:139], v5 offset:1552\n\t" \
  "ds_read_b128 v[140:143], v6 offset:12288\n\t" \
  "ds_read_b64_tr_b16 v[144:145], v5 offset:2048\n\t" \
  "s_waitcnt lgkmcnt(0)\n\t" \
  "v_and_b32 v60, v60, v61\n\t" \
  "v_and_b32 v62, v62, v63\n\t" \
  "v_mov_b32 v12, %13\n\t" \
  "v_and_b32 v60, v60, v62\n\t" \
  "s_nop 0\n\t" \
  "v_readfirstlane_b32 vcc_lo, v60\n\t" \
  "s_nop 1\n\t" \
  "s_cmp_eq_u32 vcc_lo, 0\n\t" \
  "s_cbranch_scc1 7b\n\t" \
  "s_branch 6b\n\t" \
  "30:\n\t" \
  "ds_read_b32 v16, v56 offset:16\n\t" \
  "s_waitcnt lgkmcnt(0)\n\t" \
  "v_readfirstlane_b32 vcc_lo, v16\n\t" \
  "s_nop 1\n\t" \
  "s_cmp_ge_u32 vcc_lo, 4\n\t" \
  "s_cbranch_scc0 30b\n\t" \
  "s_branch 31b\n\t" \
  "32:\n\t" \
  "ds_read_b128 v[40:43], v56 offset:80\n\t" \
  "s_waitcnt lgkmcnt(0)\n\t" \
  "v_min_u32 v40, v40, v41\n\t" \
  "v_min3_u32 v40, v40, v42, v43\n\t" \
  "s_nop 1\n\t" \
  "v_readfirstlane_b32 vcc_lo, v40\n\t" \
  "s_nop 1\n\t" \
  "s_cmp_ge_u32 vcc_lo, 7\n\t" \
  "s_cbranch_scc0 32b\n\t" \
  "s_branch 33b\n\t" \
  "34:\n\t" \
  "ds_read_b128 v[40:43], v56 offset:80\n\t" \
  "s_waitcnt lgkmcnt(0)\n\t" \
  "v_min_u32 v40, v40, v41\n\t" \
  "v_min3_u32 v40, v40, v42, v43\n\t" \
  "s_nop 1\n\t" \
  "v_readfirstlane_b32 vcc_lo, v40\n\t" \
  "s_nop 1\n\t" \
  "s_cmp_ge_u32 vcc_lo, 13\n\t" \
  "s_cbranch_scc0 34b\n\t" \
  "s_branch 35b\n\t" \
  "36:\n\t" \
  "ds_read_b128 v[40:43], v56 offset:80\n\t" \
  "s_waitcnt lgkmcnt(0)\n\t" \
  "v_min_u32 v40, v40, v41\n\t" \
  "v_min3_u32 v40, v40, v42, v43\n\t" \
  "s_nop 1\n\t" \
  "v_readfirstlane_b32 vcc_lo, v40\n\t" \
  "s_nop 1\n\t" \
  "s_cmp_ge_u32 vcc_lo, 16\n\t" \
  "s_cbranch_scc0 36b\n\t" \
  "s_branch 37b\n\t" \
  "38:\n\t" \
  "ds_read_b128 v[40:43], v56 offset:160\n\t" \
  "s_waitcnt lgkmcnt(0)\n\t" \
  "v_min_u32 v40, v40, v41\n\t" \
  "v_min3_u32 v40, v40, v42, v43\n\t" \
  "s_nop 1\n\t" \
  "v_readfirstlane_b32 vcc_lo, v40\n\t" \
  "s_nop 1\n\t" \
  "s_cmp_ge_u32 vcc_lo, 16\n\t" \
  "s_cbranch_scc0 38b\n\t" \
  "s_branch 39b\n\t" \
  "40:\n\t" \
  "s_waitcnt vmcnt(0) lgkmcnt(0)\n\t" \
  "v_readfirstlane_b32 vcc_lo, v16\n\t" \
  "s_nop 1\n\t" \
  "s_cmp_ge_u32 vcc_lo, 1\n\t" \
  "s_cbranch_scc0 42f\n\t" \
  "43:\n\t" \
  "s_lshr_b64 s[90:91], %18, 4\n\t" \
  "s_and_b32 s90, s90, 15\n\t" \
  "s_lshl_b32 s90, s90, 15\n\t" \
  "s_add_u32 s90, s90, s92\n\t" \
  "s_addc_u32 s91, s93, 0\n\t" \
  "global_load_dwordx4 v[80:83], v53, s[90:91] offset:0 sc1\n\t" \
  "global_load_dwordx4 v[84:87], v53, s[90:91] offset:1024 sc1\n\t" \
  "global_load_dwordx4 v[88:91], v53, s[90:91] offset:2048 sc1\n\t" \
  "global_load_dwordx4 v[92:95], v53, s[90:91] offset:3072 sc1\n\t" \
  "s_lshr_b64 s[90:91], %18, 8\n\t" \
  "s_and_b32 s90, s90, 15\n\t" \
  "s_lshl_b32 s90, s90, 15\n\t" \
  "s_add_u32 s90, s90, s92\n\t" \
  "s_addc_u32 s91, s93, 0\n\t" \
  "global_load_dwordx4 v[96:99], v53, s[90:91] offset:0 sc1\n\t" \
  "global_load_dwordx4 v[100:103], v53, s[90:91] offset:1024 sc1\n\t" \
  "global_load_dwordx4 v[104:107], v53, s[90:91] offset:2048 sc1\n\t" \
  "global_load_dwordx4 v[108:111], v53, s[90:91] offset:3072 sc1\n\t" \
  "s_branch 41b\n\t" \
  "42:\n\t" \
  "s_cmp_eq_u32 %13, 0\n\t" \
  "s_cbranch_scc1 44f\n\t" \
  "s_sleep 1\n\t" \
  "ds_read_b32 v16, v56 offset:96\n\t" \
  "s_waitcnt lgkmcnt(0)\n\t" \
  "v_readfirstlane_b32 vcc_lo, v16\n\t" \
  "s_nop 1\n\t" \
  "s_cmp_ge_u32 vcc_lo, 1\n\t" \
  "s_cbranch_scc0 42b\n\t" \
  "s_branch 43b\n\t" \
  "44:\n\t" \
  "v_bfe_u32 v19, v12, 4, 4\n\t" \
  "v_lshlrev_b32 v19, 2, v19\n\t" \
  "s_mov_b32 s90, 0\n\t" \
  "144:\n\t" \
  "global_load_dword v16, v19, %21 sc1\n\t" \
  "s_add_u32 s90, s90, 1\n\t" \
  "s_waitcnt vmcnt(0)\n\t" \
  "v_cmp_ne_u32 vcc, v55, v16\n\t" \
  "s_mov_b32 s88, %26\n\t" \
  "s_mov_b32 s89, s88\n\t" \
  "s_and_b64 vcc, vcc, s[88:89]\n\t" \
  "s_cbranch_scc0 244f\n\t" \
  "s_sleep 2\n\t" \
  "s_cmp_lt_u32 s90, 0x400000\n\t" \
  "s_cbranch_scc1 144b\n\t" \
  "244:\n\t" \
  "v_mov_b32 v16, 1\n\t" \
  "ds_max_u32 v56, v16 offset:96\n\t" \
  "s_branch 43b\n\t" \
  "45:\n\t" \
  "s_cmp_eq_u32 %13, 0\n\t" \
  "s_cbranch_scc1 47f\n\t" \
  "s_sleep 1\n\t" \
  "ds_read_b32 v16, v56 offset:96\n\t" \
  "s_waitcnt lgkmcnt(0)\n\t" \
  "v_readfirstlane_b32 vcc_lo, v16\n\t" \
  "s_nop 1\n\t" \
  "s_cmp_ge_u32 vcc_lo, 2\n\t" \
  "s_cbranch_scc0 45b\n\t" \
  "s_branch 46b\n\t" \
  "47:\n\t" \
  "v_bfe_u32 v19, v12, 4, 4\n\t" \
  "v_lshlrev_b32 v19, 2, v19\n\t" \
  "s_mov_b32 s90, 0\n\t" \
  "147:\n\t" \
  "global_load_dword v16, v19, %21 sc1\n\t" \
  "s_add_u32 s90, s90, 1\n\t" \
  "s_waitcnt vmcnt(0)\n\t" \
  "v_cmp_ne_u32 vcc, v55, v16\n\t" \
  "s_mov_b32 s88, %26\n\t" \
  "s_mov_b32 s89, s88\n\t" \
  "s_and_b64 vcc, vcc, -1\n\t" \
  "s_cbranch_scc0 247f\n\t" \
  "s_sleep 2\n\t" \
  "s_cmp_lt_u32 s90, 0x400000\n\t" \
  "s_cbranch_scc1 147b\n\t" \
  "247:\n\t" \
  "v_mov_b32 v16, 2\n\t" \
  "ds_max_u32 v56, v16 offset:96\n\t" \
  "s_branch 46b\n\t" \
  "19:\n\t"
#define C_CLOB "v10","v11","v12","v13","v18","v19","v20","v21","v22","v23","v24","v25","v26","v27","v28","v29","v30","v31","v32","v33","v34","v35","v36","v37","v38","v39","v40","v41","v42","v43","v44","v45","v46","v47","v48","v49","v50","v53","v55","v56","v57","v58","v59","v60","v61","v62","v63","v64","v65","v66","v67","v68","v69","v70","v71","v72","v73","v74","v75","v76","v77","v78","v79","v80","v81","v82","v83","v84","v85","v86","v87","v88","v89","v90","v91","v92","v93","v94","v95","v96","v97","v98","v99","v100","v101","v102","v103","v104","v105","v106","v107","v108","v109","v110","v111","v112","v113","v114","v115","v116","v117","v118","v119","v120","v121","v122","v123","v124","v125","v126","v127","v128","v129","v130","v131","v132","v133","v134","v135","v136","v137","v138","v139","v140","v141","v142","v143","v144","v145","v146","v147","v148","v149","v150","v151","v152","v153","v154","v155","v156","v157","v158","v159","v160","v161","v162","v163","v164","v165","v166","v167","v168","v169","v170","v171","v172","v173","v174","v175","v176","v177","v178","v179","v180","v181","v182","v183","v184","v185","v186","v187","v188","v189","v190","v191","v192","v193","v194","v195","v196","v197","v198","v199","v200","v201","v202","v203","v204","v205","v206","v207","v208","v209","v210","v211","v212","v213","v214","v215","v216","v217","v218","v219","v220","v221","v222","v223","v224","v225","v226","v227","v228","v229","v230","v231","v232","v233","v234","v235","v236","v237","v238","v239","v240","v241","v242","v243","v244","v245","v246","v247","v248","v249","v250","v251","v252","v253","v254","v255"

static_assert(KVP<13 && KVP>TGS+1, "");

typedef unsigned long long u64;
typedef u64 u64x8 __attribute__((ext_vector_type(8)));
typedef u64 u64x4 __attribute__((ext_vector_type(4)));
typedef unsigned u32x8 __attribute__((ext_vector_type(8)));
typedef unsigned u32x16 __attribute__((ext_vector_type(16)));
template<class T> __device__ __forceinline__ T sg(T v){
  if constexpr(sizeof(T)==4) return (T)__builtin_amdgcn_readfirstlane((unsigned)v); else return v; }
__device__ __forceinline__ const char* at(const void* b, unsigned o){ asm volatile("" : "+s"(o)); return (const char*)b+o; }
#define GP(T,v) ((T*)(__attribute__((address_space(1))) T*)(v))
__global__ __launch_bounds__(512,2) __attribute__((aligned(4096))) void fused_kernel(const u16* x_, const u16* wqkv_, const u16* kin_, const u16* vin_, const u16* shk_, const u16* hk_,
    const u16* wo_, const u16* qin_, const u16* sD_, u16* out_, u16* ws_, u16* unused_)
{
#define ARGS0() u64x8 ka; u64x4 kb; asm volatile("s_load_dwordx16 %0, %2, 0\n\ts_load_dwordx8 %1, %2, 64" : "=&s"(ka), "=&s"(kb) : "s"(__builtin_amdgcn_kernarg_segment_ptr()) : "memory")
#define ARGS() asm volatile("s_waitcnt lgkmcnt(0)" : "+s"(ka), "+s"(kb) :: "memory"); \
  const u16 *x=GP(const u16,ka[0]), *wqkv=GP(const u16,ka[1]), *kin=GP(const u16,ka[2]), *vin=GP(const u16,ka[3]), *shk=GP(const u16,ka[4]), *hk=GP(const u16,ka[5]), *wo=GP(const u16,ka[6]), *qin=GP(const u16,ka[7]), *sD=GP(const u16,kb[0]); \
  u16 *out=GP(u16,kb[1]), *o=GP(u16,kb[2]); char* ws=(char*)o; unsigned cpar=xcd>>2; \
  auto loc=[&](unsigned off)->unsigned{ return off|(((cpar^(off>>21))&1)<<13); }; \
  (void)x; (void)wqkv; (void)kin; (void)vin; (void)shk; (void)hk; (void)wo; (void)qin; (void)sD; (void)out; (void)loc
  __shared__ __attribute__((aligned(1024))) char smem[ALL_LDS];
  const int tid=threadIdx.x;
  const int w8=__builtin_amdgcn_readfirstlane(tid>>6);
  asm volatile("s_sub_u32 vcc_lo, %0, 4\n\ts_cbranch_scc1 1f\n\ts_cmp_lg_u32 %0, 7\n\ts_cbranch_scc1 2f\n\t"
      "s_lshr_b32 vcc_lo, %2, 3\n\ts_mul_i32 vcc_lo, vcc_lo, %5\n\tv_and_b32 v254, 15, %1\n\tv_lshl_add_u32 v254, v254, 7, vcc_lo\n\ts_getpc_b64 vcc\n\tglobal_load_dword v255, v254, vcc\n\ts_branch 1f\n"
      "2:\n\ts_and_b32 vcc_hi, %2, 7\n\ts_cbranch_scc1 1f\n\ts_cmp_gt_u32 %2, 32\n\ts_cbranch_scc1 1f\n\ts_mul_i32 vcc_hi, %2, 3\n\ts_lshr_b32 vcc_hi, vcc_hi, 3\n\ts_add_u32 vcc_lo, vcc_lo, vcc_hi\n\t"
      "v_and_b32 v253, 3, %1\n\tv_and_b32 v254, 4, %1\n\tv_lshlrev_b32 v253, 7, v253\n\tv_lshl_add_u32 v253, v254, 11, v253\n\tv_lshl_add_u32 v253, vcc_lo, 9, v253\n\tv_add_u32 v253, %4, v253\n\t"
      "s_load_dwordx2 vcc, %3, 80\n\ts_mov_b64 exec, 255\n\ts_waitcnt lgkmcnt(0)\n\tglobal_store_dword v253, v253, vcc sc1\n\ts_mov_b64 exec, -1\n1:"
      :: "s"(w8), "v"(tid), "s"(blockIdx.x), "s"(__builtin_amdgcn_kernarg_segment_ptr()), "n"(WS_GEN+16384), "n"(CODE_ST) : "memory", "scc", "vcc", "v253", "v254", "v255");
  LDS_AS unsigned* const ct=(LDS_AS unsigned*)(LDS_AS char*)(smem+CT_OFF);
  volatile LDS_AS unsigned* const vct=ct;
  auto all4=[&](int i)->unsigned{ u32x4 v=*(volatile LDS_AS u32x4*)(ct+i); return __builtin_amdgcn_readfirstlane(v[0]&v[1]&v[2]&v[3]); };
#define ROTF(b) ((4+4*xr+4*xc+8*(b))&15)
#define BLOCK() int bid=blockIdx.x, w=w8&3; asm volatile("" : "+s"(bid), "+s"(w)); __builtin_assume(bid>=0 && bid<256 && w>=0 && w<4); DERIVE()
#define DERIVE() const int xcd=bid&7, idx=bid>>3, ra=idx&3, rbb=idx>>2, xr=xcd>>1, xc=xcd&1, rb=xr*4+ra, hp=xc*8+rbb, s0=rb*128, tm=rb, tn=hp^TNX, cb=(xcd>>2)*128+idx*4+(xcd&3), gi=cb^16; (void)s0; (void)gi; \
  auto korder=[&]()->unsigned long long{ const unsigned N=0x7654210u, sh=4*(KR), q=(((N>>sh)|(N<<(28-sh)))&0xfffffffu)|0x30000000u; \
    const int p=4*(rbb==3? 7 : (rbb-(rbb>3)+7-(KR))%7); const unsigned q0=(q&~((16u<<p)-1))|((q&((1u<<p)-1))<<4)|rbb; \
    return ((unsigned long long)q<<32|q0)^(xc? 0x88888888ull : 0x8888888800000000ull); }; \
  const unsigned l1m=0xf7u<<(8*xc); (void)l1m; (void)korder

  if(__builtin_expect(w8>=4,0)){
  BLOCK();
  const int hd=w>>1, par=w&1;
  {
    ARGS0(); LANES();
    if(w==0){ if(l<52) ct[l]=0u; if(idx==0) *(u32x4*)(smem+AUX+l*16)=u32x4{0,0,0,0}; }
    unsigned a0b=fr*128+((g^(fr&7))*16);
    const int r0=8*g+tq;
    unsigned bbb=r0*128+(((2*hd)^wswz(r0))*32)+8*tp;
    unsigned kA=AUX+(fr^3)*KVP_+w*64+(g&1)*16-(g>>1)*256, kB=AUX+(20+4*g-tq)*KVP_+w*64+8*tp;
    const int cch=127-4*idx-g;
    unsigned kvo=(8*w+tq)*32768+(cch>>6)*16384+((cch>>4)&3)*2048+(cch&15)*64+((tp^((cch>>2)&3))*16);
    unsigned sho=((xcd^KX)*128+32*w+fr)*8; const unsigned ctl=CT_OFF+4*w;
    u32x4 sel;
    { const unsigned b2=(g>>1)? 0x0302u : 0x0100u, sv=(fr&1)? (b2<<16|0x0c0cu) : (0x0c0c0000u|b2);
      #pragma unroll
      for(int i=0;i<4;i++) sel[i]= (fr>>1)==(g&1)*4+i ? sv : 0x0c0c0c0cu; }
    unsigned msk, lx=w*1024+l*16;
    { const unsigned t=4*g+(fr^15), n1=(0x00ffffffu>>t)&15, n3=(0xffffu>>t)&15, n2= par? n1 : n3|((l&32)? 15u : 0u);
      msk=((0xffff00ffu>>t)&15)|n1<<4|n2<<8|n3<<12; }
    msk|=(unsigned)(g? l-16 : 48+((l-1)&15))<<26;
    const unsigned hb=__builtin_amdgcn_readfirstlane(rb==0 ? 0xcfa8965fu : 0u);
    unsigned ap=AUX+w*CV_W+(tq&1)*(CV_CP-4)+8+32*g+4*tq+8*tp;
    u32x4 bp;
    #pragma unroll
    for(int b=0;b<4;b++){ const int c=15+4*b-fr+g; bp[b]=c*64+((w^((c>>2)&3))*16); }
    asm volatile("" : "+v"(a0b), "+v"(bbb), "+v"(kA), "+v"(kB), "+v"(kvo), "+v"(sho), "+v"(msk), "+v"(lx), "+v"(ap), "+v"(bp), "+v"(sel));
    ARGS();
    const char* ow=at(ws,loc((rb*16+hp)*32768)+(par*2+hd)*1024+4096);
    asm volatile("" : "+s"(shk));
    __builtin_amdgcn_s_setprio(CPRIO);
    asm volatile(C_ASM : "+{v52}"(a0b), "+{v54}"(bbb), "+{v0}"(kA), "+{v1}"(kB), "+{v2}"(kvo), "+{v51}"(sho), "+{v3}"(msk), "+{v4}"(lx), "+{v5}"(ap), "+{v[6:9]}"(bp), "+{v[14:17]}"(sel)
       : "s"(shk), "s"(ctl), "s"(w), "s"(ow), "s"(hb), "s"(0.17677669529663687f*1.44269504088896341f), "{s[92:93]}"(ws+loc(tm*16*32768)), "s"(korder()),
         "s"(at(ws,loc(flo(1,rb*16))+hp*4)), "s"(AUX+w*(CV_W+64)), "s"(at(ws,loc(flo(1,rb*16))+tn*128)),
         "s"(at(out,(tm*128+w*32+1)*4096+2048+tn*128)), "s"(8192+w*4096), "s"(ws+loc(WS_KV+(xcd^KX)*32*32768)), "s"(at(ws,loc(flo(0,(xcd^KX)*32))+idx*4)), "s"(l1m), "s"(xcd==0 && idx<5 && w<3? (u64)at(ws,WS_GEN+idx*3+w) : 0ull) : "memory", "scc", "vcc", "s68", "s69", "s70", "s71", "s74", "s75", "s76", "s77", "s88", "s89", "s90", "s91",
       C_CLOB);

  }
  return;
  }
  {
    ARGS0(); BLOCK(); LANES();
    const unsigned vx=(l>>3)*4096+(((l&7)^(l>>3))*16), vxs=vx+(s0+32*w)*2048, vw=(l>>3)*6144+((((l&7)>>1)^((l>>4)&1))*32)+(l&1)*16+hp*128+w*98304, vw1=(vw^64)+49152-1024;
    u32x8 va; u32x2 vb; unsigned vsp;
    #pragma unroll
    for(int i=0;i<6;i++) va[i]=((i&1)? vw1 : vw)+(i>>1)*2048;
    va[6]=vxs; va[7]=vxs+32768-1024; vb[0]=vxs+2048; vb[1]=vxs+2048+32768-1024;
    vsp=vx+(rb? s0-16+w : 0)*2048;
    const unsigned sw1=w*1024, sw2=w*2048;
    unsigned rot=ROTF(rbb);
    asm volatile("" : "+v"(va), "+v"(vb), "+v"(vsp));
    asm volatile("" : "+s"(rot));
    unsigned vt=(w&1)? (xr*512+idx*16+(l>>1))*2048+(l&1)*1024 : (2*idx+(l&1))*6144+((l>>1)&3)*2048+xc*1024;
    const int kt0=idx*64;
    unsigned kvl=(kt0+16*w+g)*2048+(xcd^KX)*256+fr*16, l16=l*16;
    asm volatile("" : "+v"(vt), "+v"(kvl), "+v"(l16));
    ARGS();
    asm volatile(H0_ASM : "+{s[40:41]}"(x), "+{s[42:43]}"(wqkv) : "{v[0:7]}"(va), "{v[8:9]}"(vb), "{v10}"(vsp), "{s52}"(rot), "{s53}"(sw2), "{s59}"(w)
      : "memory", "scc", "s76", "s77", "s78", "s79", "s82", "s83");
    asm volatile("" : "+s"(w)); __builtin_assume(w>=0 && w<4);
    int kvs=KS0+KVSEL;
    const bool hl= w==0 && kt0;
    unsigned c46=sg(w<2? 128u : w==2? 393216u : 0u), c49=sg(w<2? sw1 : (unsigned)ALL_LDS-96), c64=sg(hl? 8192u : 0u), c68=sg(hl? AUX : ALL_LDS-96);
    unsigned long long c50= w<2? ~0ull : w==2? 63ull : 1ull, c66= hl? ~0ull : 1ull;
    asm volatile("" : "+s"(c46), "+s"(c49), "+s"(c64), "+s"(c68), "+s"(c50), "+s"(c66), "+s"(kvs));
    const u16* spb= w==2? wqkv : x;
    const int hl3=(3-(int)((size_t)spb>>7))&7, roth=ROTF(hl3), xs=((hl3-ROTF(0))&7)-LEAD;
    unsigned i11=sg((w==2? roth+LEAD+1 : rot+1)&15), sbo, sst;
    if(w>=2){
      vsp=vt+hl3*128;
      touch(spb, vsp+((roth+(l>>3))&15)*c46, w==2? 0x3f3f3f3f3f3f3f3full>>(56-8*LEAD) : xs<=1? 0xffffffffull : 1ull);
    }
    unsigned gof=(((gi&15)*128+64*(w&1)+4*(w>>1)+16*((l>>5)&1)+((l>>3)&3))*DM+(gi>>4)*64)*2+(l16&127);
    unsigned wv[2];
    wv[0]=(((l>>5)*16+w*4+((l>>3)&3))*DM+tn*64)*2+((l16&127)^((l&16)*2)^((w&1)*64)); wv[1]=wv[0]+32*DM*2;
    unsigned zpa=(w>>1)*9216+(w&1)*512+l16, hda=AUX+w*CV_W+(l&1)*CV_CP+(l>>1)*16, yra=AUX+tq*(CV_W+64)+(g>>1)*128+(g&1)*32+tp*8+w*1024;
    const unsigned f0=loc(flo(0,cb)), f1=loc(flo(1,tm*16+tn)), f2=loc(flo(2,gi));
    unsigned vc=(w&1)? (g==0? WS_GEN : (g==1? f0 : g==2? f1 : f2)^((l&1)<<13)) : idx*CODE_ST+fr*128u;
    unsigned kfo=(f0^((l&32)<<8))+(l&31)*4, ofo= w==0? f1+fr*4 : WS_GEN;
    asm volatile("" : "+v"(kfo), "+v"(ofo), "+v"(gof), "+v"(wv[0]), "+v"(wv[1]), "+v"(hda), "+v"(yra), "+v"(zpa), "+v"(vc));
    unsigned long long kt=korder();
    asm volatile("" : "+s"(kt));
#define H_IN "{v[0:7]}"(va), "{v[8:9]}"(vb), "{v10}"(vsp), "{v11}"(kvl), "{s[40:41]}"(x), "{s[42:43]}"(wqkv), "{s[44:45]}"(spb), "{s46}"(c46), "{s49}"(c49), "{s[50:51]}"(c50), "{s53}"(sw2), \
      "{s58}"(kvs), "{s59}"(w), "{s[60:61]}"(kin), "{s[62:63]}"(vin), "{s64}"(c64), "{s[66:67]}"(c66), "{s68}"(c68)
#define H_CLOB "memory", "scc", "vcc", "s76", "s77", "s78", "s79", "s80", "s81", "s82", "s83", "s84", "s85", "v20", "v21", "v22", "v253", "v254", "v255"
    asm volatile(H1_ASM : "+{s47}"(i11), "+{s52}"(rot), "={s54}"(sbo), "={s55}"(sst) : H_IN : H_CLOB);
    const u16* hrow=hk+(size_t)(cb*4+w)*S;
    u32x4 h0; unsigned dvu;
    asm volatile("s_load_dwordx4 %0, %2, 0\n\ts_load_dword %1, %3, %4" : "=&s"(h0), "=&s"(dvu) : "s"(hrow), "s"(sD), "s"(cb*8+(w&2)*2) : "memory");
    unsigned long long pc; asm volatile("s_getpc_b64 %0" : "=s"(pc)); pc&=~4095ull;
    asm volatile("" : "+s"(wo), "+s"(qin), "+s"(out), "+s"(ws));
    __builtin_amdgcn_s_setprio(HP1);
    asm volatile(H_ASM : "+{s47}"(i11), "+{s52}"(rot), "+{s54}"(sbo), "+{s55}"(sst)
      : H_IN, "{v14}"(vc), "{v13}"(WS_GEN+256+(l&31)*4), "{s56}"(sg(3u<<(kvs-1) | 1u<<15)),
        "{s57}"(sg(((w&1)? (1u<<TGS | 3u<<PLS | (xs>1? 8u : 0u)) : 1u<<CPS)&~(unsigned)(c50>>32))), "{s[70:71]}"(ws), "{s[72:73]}"(pc) : H_CLOB);
    asm volatile("" : "+s"(h0), "+s"(dvu));
    asm volatile("" : "+s"(bid), "+s"(w)); __builtin_assume(bid>=0 && bid<256 && w>=0 && w<4);
    { DERIVE(); const unsigned sw1=w*1024, sw2=w*2048;
    u32x4 qv[4];
    __builtin_amdgcn_s_setprio(HP2);
    unsigned kf_;
    asm volatile("global_load_dword %0, %1, %2 sc1" : "=v"(kf_) : "v"(kfo), "s"(ws) : "memory");
    { const unsigned fa=AUX+w*CV_W+32;
      asm volatile("s_mov_b64 exec, -2\n\ts_add_u32 m0, %3, %4\n\ts_nop 0\n\tglobal_load_lds_dwordx4 %0, %1" LDPOL "\n\ts_mov_b32 m0, %3\n\ts_nop 0\n\tglobal_load_lds_dwordx4 %0, %2" LDPOL "\n\ts_mov_b64 exec, -1"
        :: "v"(l16), "s"(hrow), "s"(hrow-2), "s"(fa), "n"(CV_CP) : "memory", "scc");
      #pragma unroll
      for(int i=1;i<4;i++){ gldsn(hrow+i*512, l16, fa, CV_CP+i*1024); gldsn(hrow-2+i*512, l16, fa, i*1024); } }
    WAITV(9);
    vct[32+w]=1u;
    const unsigned tag=__builtin_amdgcn_readfirstlane(vct[26]);
    const unsigned pl=__builtin_amdgcn_readfirstlane(vct[6]); cpar=(xcd>>2)^(pl>>13); if(w==0) ofo^=pl;
    u32x4 hv;
    { const unsigned d0=(h0[0]&0xffff0000u)|(pack2(bflo(h0[0])+((w&1)? bfhi(dvu) : bflo(dvu)),0.f)&0xffffu);
      hv= l==4? u32x4{0,d0,h0[1],h0[2]} : l==5? u32x4{d0,h0[1],h0[2],h0[3]} : u32x4{0,0,0,0}; }
    asm volatile("s_mov_b64 exec, 63\n\tds_write_b128 %0, %1\n\ts_mov_b64 exec, %3\n\tds_write_b128 %2, %1 offset:7680\n\ts_mov_b64 exec, -1"
      :: "v"(hda), "v"(hv), "v"(zpa), "s"(0xffffffff00000000ull) : "memory");
#define QLD() asm volatile("global_load_dwordx4 %0, %4, %5" LDPOL "\n\tglobal_load_dwordx4 %1, %4, %6" LDPOL "\n\tglobal_load_dwordx4 %2, %4, %7" LDPOL "\n\tglobal_load_dwordx4 %3, %4, %8" LDPOL \
        : "=&v"(qv[0]), "=&v"(qv[1]), "=&v"(qv[2]), "=&v"(qv[3]) : "v"(gof), "s"(qin), "s"(qin+8*DM), "s"(qin+32*DM), "s"(qin+40*DM) : "memory")
    auto kvt=[&](){
      #pragma unroll
      for(int i=0;i<4;i++) gldsc(ws+loc(WS_KV+cb*32768)+(w>>1)*16384+(w&1)*4096+i*1024, l16, sw2, (i&1)*9216+(i>>1)*1024); };
    asm volatile("s_waitcnt vmcnt(8)" : "+v"(kf_) :: "memory");
    if((unsigned)(__ballot(kf_!=tag)>>(pl>>8))) wait_flag(ws+loc(flo(0,cb)), (l&31)*4, tag);
    kvt();
    auto wos=[&](int st){ const u16* b=wo+(size_t)((kt>>(4*st))&15)*64*DM; glds(b,wv[0],sw1,WOB+st*8192); glds(b,wv[1],sw1,WOB+st*8192+4096); };
    wos(0); wos(1);
    while(!all4(36)) __builtin_amdgcn_s_sleep(1);
    #pragma unroll
    for(int st=2;st<NE;st++) wos(st);
    WAITV(2*NE); WAITL();
    vct[16+w]=1u;
    __builtin_amdgcn_s_setprio(HP4);
    QLD();
    #pragma unroll
    for(int st=NE;st<13;st++){ if(st==7) while(!all4(0)) __builtin_amdgcn_s_sleep(1);
      WAITV(W1);
      wos(st); if(st==7+(W1+1)/2) vct[20+w]=7u; }
    unsigned op;
    asm volatile("global_load_dword %0, %1, %2 sc1" : "=v"(op) : "v"(ofo), "s"(ws) : "memory");
    WAITV(1); vct[20+w]=13u;
    asm volatile("s_waitcnt vmcnt(0)" : "+v"(qv[0]), "+v"(qv[1]), "+v"(qv[2]), "+v"(qv[3]), "+v"(op) :: "memory");
    unsigned oseen=0;
    auto olevel=[&](unsigned v){
      const unsigned ne=(unsigned)__ballot(v!=tag), lv= ne==0 ? 2u : (ne&l1m)==0 ? 1u : 0u;
      if(lv>oseen){ oseen=lv; if(l==0) __hip_atomic_fetch_max(ct+24, lv, __ATOMIC_RELAXED, __HIP_MEMORY_SCOPE_WORKGROUP); } };
    auto opoll=[&](){ olevel(ld4c(ws,ofo)); };
    if(w==0) olevel(op);
    float qf[32];
    auto qun=[&](){
    #pragma unroll
    for(int i=0;i<16;i++){ qf[2*i]=bflo(qv[i>>2][i&3]); qf[2*i+1]=bfhi(qv[i>>2][i&3]); }
    #pragma unroll
    for(int i=0;i<32;i+=8) asm volatile("" : "+v"(qf[i]), "+v"(qf[i+1]), "+v"(qf[i+2]), "+v"(qf[i+3]), "+v"(qf[i+4]), "+v"(qf[i+5]), "+v"(qf[i+6]), "+v"(qf[i+7]));
    };
    qun();
    auto wol=[&](int i){ glds(wo+(size_t)((kt>>(52+4*(i>>1)))&15)*64*DM, wv[i&1], sw1, i<4? AUX+4352+i*CV_W : (i&1)*4096); };
    while(!all4(44)){ if(w==0 && oseen<1) opoll(); else __builtin_amdgcn_s_sleep(SPN); }
    wol(0); wol(1); wol(2); wol(3);
    while(!all4(48)){ if(w==0 && oseen<1) opoll(); else __builtin_amdgcn_s_sleep(SPN); }
    wol(4); wol(5);
    __builtin_amdgcn_s_setprio(HP3);
    bool gen= !(bid==0 && w==1);
    auto trygen=[&](){
      u32x4 v; asm volatile("global_load_dwordx4 %0, %1, %2 sc1\n\ts_waitcnt vmcnt(0)" : "=v"(v) : "v"((loc(flo(0,(l>>3)*32))^((l&32)<<8))+(l16&127)), "s"(ws) : "memory");
      if(__all(v[0]==tag && v[1]==tag && v[2]==tag && v[3]==tag)){ if(l==0) st4c(ws+WS_GEN, tag); gen=true; } };
    if(!gen) trygen();
    while(!((all4(8)>>w)&1)){
      if(w==0 && oseen<2) opoll();
      __builtin_amdgcn_s_sleep(OPOLLS);
    }
    __builtin_amdgcn_s_setprio(HP5);
    {
      const char* yb=smem+yra;
      s16x4 ya[4], yc[4];
      #pragma unroll
      for(int i=0;i<4;i++){ ya[i]=trread(yb+(i>>1)*512+(i&1)*64); yc[i]=trread(yb+(i>>1)*512+(i&1)*64+256); }
      #pragma unroll
      for(int i=0;i<4;i++) st16c(ws+loc(WS_Y+(cb&~15)*32768)+(w*4+i)*32768+((cb>>3)&1)*16384+(cb&7)*1024, l16, __builtin_bit_cast(u32x4,__builtin_shufflevector(ya[i],yc[i],0,1,2,3,4,5,6,7)));
    }
    unsigned po=gof+(gof&~2047u);
    const char* p0=ws+loc(WS_Y+gi*32768);
    unsigned y0=g*1024+w*256+((fr^(g&2))*16);
    asm volatile("" : "+v"(y0), "+v"(po));
    asm volatile("" : "+s"(p0));
    asm volatile("s_waitcnt vmcnt(0)\n\ts_mov_b64 exec, 15\n\tglobal_store_dword %0, %1, %2 sc1\n\ts_mov_b64 exec, -1" :: "v"(l16*8), "v"(tag), "s"(ws+loc(flo(2,cb&~15))+w*512+(cb&15)*4) : "memory");
    vct[20+w]=16u;
    if(w==0){ int sp_=0; while(oseen<2 && vct[24]<2u && ++sp_<(1<<22)){ opoll(); __builtin_amdgcn_s_sleep(8); } }
    if(w==0){
      const char* fp=ws+loc(flo(2,gi)); unsigned fo=fr*4; int spins=0; asm volatile("" : "+s"(fp)); asm volatile("" : "+v"(fo));
      while(vct[27]==0u) __builtin_amdgcn_s_sleep(2);
      while(true){
        unsigned v=ld4c(fp,fo);
        if(__all(v==tag) || ++spins>(1<<20)) break;
        __builtin_amdgcn_s_sleep(POLLS);
      }
      { u32x4 v; do{ v=*(volatile LDS_AS u32x4*)(ct+20); }while(__builtin_amdgcn_readfirstlane(v[0]&v[1]&v[2]&v[3]&16u)==0); }
      vct[25]=1u;
    } else { while(vct[25]==0u) __builtin_amdgcn_s_sleep(2); }
    {
      u32x4 yv[4];
      #pragma unroll
      for(int i=0;i<4;i++) gldsc(p0+(i>>1)*16384+(i&1)*4096, y0^(i*64), w*CV_W, AUX+i*1024);
      const char* ys=smem+AUX+w*CV_W+(l&7)*512;
      const int yi=((l>>3)^((l&7)*2))*16;
      WAITV(0);
      yv[0]=*(const u32x4*)(ys+yi); yv[1]=*(const u32x4*)(ys+yi+256); yv[2]=*(const u32x4*)(ys+(yi^128)); yv[3]=*(const u32x4*)(ys+(yi^128)+256);
      #pragma unroll
      for(int r=0;r<4;r++){
        u32x4 ov;
        #pragma unroll
        for(int e=0;e<4;e++){ const unsigned y2=yv[(r>>1)*2+(e>>1)][(r&1)*2+(e&1)]; ov[e]=pack2(qf[r*8+2*e]*bflo(y2), qf[r*8+2*e+1]*bfhi(y2)); }
        stos(out+((r&1)*8+(r>>1)*32)*2048, po, ov);
      }
    }
    for(int sp_=0; !gen && sp_<(1<<22); sp_++) trygen();
    }
  }

}

extern "C" void kernel_launch(void* const* d_in, const int* in_sizes, int n_in, void* d_out, int out_size, void* d_ws, size_t ws_size, hipStream_t stream){
  const u16* q=(const u16*)d_in[0]; const u16* k=(const u16*)d_in[1]; const u16* v=(const u16*)d_in[2];
  const u16* shk=(const u16*)d_in[3]; const u16* hk=(const u16*)d_in[4]; const u16* sD=(const u16*)d_in[5];
  const u16* x=(const u16*)d_in[6]; const u16* wqkv=(const u16*)d_in[7]; const u16* wo=(const u16*)d_in[8];
  u16* ws=(u16*)(((size_t)d_ws+16383)&~(size_t)16383);
  fused_kernel<<<256,512,0,stream>>>(x,wqkv,k,v,shk,hk,wo,q,sD,(u16*)d_out,ws,ws);
}
```
